```python
import torch

def _softmax(x, axis=-1):
    return torch.softmax(x, dim=axis)
def _gelu(x):
    return torch.nn.functional.gelu(x, approximate="tanh")
def _silu(x):
    return torch.nn.functional.silu(x)
def _sigmoid(x):
    return torch.sigmoid(x)
def _layernorm(x, axis=-1, eps=1e-5):
    m = x.mean(dim=axis, keepdim=True)
    v = x.var(dim=axis, keepdim=True, unbiased=False)
    return (x - m) / torch.sqrt(v + eps)
def _scatter_add(tgt, idx, src, axis=0):
    return tgt.clone().index_add_(axis, idx, src)
def _segment_sum(src, seg, n):
    out = torch.zeros((n,) + src.shape[1:], dtype=src.dtype)
    return out.index_add_(0, seg, src)
def _pad(x, pw):
    flat = tuple(v for pair in reversed(pw) for v in pair)
    return torch.nn.functional.pad(x, flat)

kernel_name = 'standalone__deformable_dcnv1__vision'

def setup_inputs():

    _w = (lambda a: (lambda t: t.to(torch.bfloat16) if t.is_floating_point() else t)(torch.as_tensor(a)))
    H = W = 224
    C = 96
    K = 3
    return {
        "x": _w((lambda *s: torch.randn(*s, dtype=torch.bfloat16) * 0.05)(1, H, W, C)),
        "offset": _w((lambda *s: torch.randn(*s, dtype=torch.bfloat16) * 0.05)(1, H, W, 2 * K * K) * 0.5),
        "w": _w((lambda *s: torch.randn(*s, dtype=torch.bfloat16) * 0.05)(K, K, C, C) * (K * K * C) ** -0.5),
    }

def reference(x, offset, w):
    B, H, W, C = x.shape
    K = 3
    hh = (lambda x, dt: x.to(dt))(torch.arange(H), x.dtype).reshape(1, H, 1)
    ww = (lambda x, dt: x.to(dt))(torch.arange(W), x.dtype).reshape(1, 1, W)
    xf = x.reshape(B, H * W, C)
    def _gather(yi, xi):
        lin = (yi * W + xi).reshape(B, H * W)
        idx = (lambda x, n, axis: torch.repeat_interleave(x, n, dim=axis))(lin[..., None], C, axis=-1)
        return (lambda x, i, axis: torch.take_along_dim(x, i, dim=axis))(xf, idx, axis=1).reshape(B, H, W, C)
    taps = []
    for ki in range(K):
        for kj in range(K):
            t = ki * K + kj
            dy = offset[..., 2 * t]
            dx = offset[..., 2 * t + 1]
            py = torch.clip(hh + (ki - 0.5 * (K - 1)) + dy, 0.0, H - 1.0)
            px = torch.clip(ww + (kj - 0.5 * (K - 1)) + dx, 0.0, W - 1.0)
            y0 = torch.floor(py)
            x0 = torch.floor(px)
            y1 = torch.clip(y0 + 1.0, 0.0, H - 1.0)
            x1 = torch.clip(x0 + 1.0, 0.0, W - 1.0)
            wy = (py - y0)[..., None]
            wx = (px - x0)[..., None]
            y0i = (lambda x, dt: x.to(dt))(y0, torch.int64)
            y1i = (lambda x, dt: x.to(dt))(y1, torch.int64)
            x0i = (lambda x, dt: x.to(dt))(x0, torch.int64)
            x1i = (lambda x, dt: x.to(dt))(x1, torch.int64)
            v = (1 - wy) * (1 - wx) * _gather(y0i, x0i) + (1 - wy) * wx * _gather(y0i, x1i) + wy * (1 - wx) * _gather(y1i, x0i) + wy * wx * _gather(y1i, x1i)
            taps.append(v)
    samp = torch.stack(taps, 3)
    return torch.einsum("bhwkc,kco->bhwo", samp, w.reshape(K * K, C, C))
```

```cpp
#include <hip/hip_runtime.h>
#include <stdint.h>
typedef __attribute__((ext_vector_type(8))) __bf16 bf16x8;
typedef __attribute__((ext_vector_type(2))) __bf16 bf16x2;
typedef __attribute__((ext_vector_type(4))) float f32x4;
typedef __attribute__((ext_vector_type(4))) unsigned u32x4;
typedef __attribute__((ext_vector_type(2))) unsigned u32x2;
struct Tiles { unsigned w[256]; };
struct Args { const unsigned short *x, *off, *w; unsigned short* out; void* ws; Tiles t; };
static __device__ const unsigned recs[256 * 64] = {
1419264,1419264,1419264,1462272,1462272,1462272,1505280,1505280,1505280,1548288,1548288,1548288,32768,35840,38912,43008,46080,49152,53248,56320,59392,63488,66560,69632,4294967294,14,30,22,22,19,192,1040137727,16780300,50465804,8064,36,320,1107820544,1107820544,1107820544,0,1098907648,1107296256,274176,274752,275328,32768,35840,38912,1462272,1465344,1468416,16900,1111507472,4294957772,4294967294,16900,1111507472,4294957772,4294967294,16900,1111507472,4294957772,4294967294,
3311616,3311616,3311616,3354624,3354624,3354624,3397632,3397632,3397632,3440640,3440640,3440640,32768,35840,38912,43008,46080,49152,53248,56320,59392,63488,66560,69632,4294967294,14,30,22,22,19,192,1040137855,16780300,50465804,8064,36,320,1117519872,1117519872,1117519872,0,1098907648,1107296256,628992,629568,630144,32768,35840,38912,3354624,3357696,3360768,17050,1111507616,4294943692,4294967294,17050,1111507616,4294943692,4294967294,17050,1111507616,4294943692,4294967294,
5289984,5289984,5289984,5332992,5332992,5332992,5376000,5376000,5376000,5419008,5419008,5419008,32768,35840,38912,43008,46080,49152,53248,56320,59392,63488,66560,69632,4294967294,14,30,22,22,19,192,1048526591,16780300,50465804,8064,36,320,1123549184,1123549184,1123549184,0,1098907648,1107296256,999936,1000512,1001088,32768,35840,38912,5332992,5336064,5339136,17142,1111507708,4294928972,4294967294,17142,1111507708,4294928972,4294967294,17142,1111507708,4294928972,4294967294,
7182336,7182336,7182336,7225344,7225344,7225344,7268352,7268352,7268352,7311360,7311360,7311360,32768,35840,38912,43008,46080,49152,53248,56320,59392,63488,66560,69632,4294967294,14,30,22,22,19,192,1048526463,16780300,50465804,8064,36,320,1126694912,1126694912,1126694912,0,1098907648,1107296256,1354752,1355328,1355904,32768,35840,38912,7225344,7228416,7231488,17191,1111507754,4294914892,4294967294,17191,1111507754,4294914892,4294967294,17191,1111507754,4294914892,4294967294,
9074688,9074688,9074688,9117696,9117696,9117696,9160704,9160704,9160704,9203712,9203712,9203712,32768,35840,38912,43008,46080,49152,53248,56320,59392,63488,66560,69632,4294967294,14,30,22,22,19,192,1048526591,16780300,50465804,8064,36,320,1129578496,1129578496,1129578496,0,1098907648,1107296256,1709568,1710144,1710720,32768,35840,38912,9117696,9120768,9123840,17235,1111507798,4294900812,4294967294,17235,1111507798,4294900812,4294967294,17235,1111507798,4294900812,4294967294,
16320,16320,16320,16512,16512,16512,16704,16704,16704,16896,16896,16896,32768,35840,38912,43008,46080,49152,53248,56320,59392,63488,66560,69632,4294967294,14,30,22,22,19,43008,1056915199,50465804,16780300,36,8064,320,1118568448,1118568448,1118568448,0,1098907648,1107296256,3096,132120,261144,32768,35840,38912,16512,704640,1392768,17066,1111507632,4294941132,4294967294,17066,1111507632,4294941132,4294967294,17066,1111507632,4294941132,4294967294,
25536,25536,25536,25728,25728,25728,25920,25920,25920,26112,26112,26112,32768,35840,38912,43008,46080,49152,53248,56320,59392,63488,66560,69632,4294967294,14,30,22,22,19,43008,1056915071,50465804,16780300,36,8064,320,1124466688,1124466688,1124466688,0,1098907648,1107296256,4824,133848,262872,32768,35840,38912,25728,713856,1401984,17157,1111507720,4294925772,4294967294,17157,1111507720,4294925772,4294967294,17157,1111507720,4294925772,4294967294,
33984,33984,33984,34176,34176,34176,34368,34368,34368,34560,34560,34560,32768,35840,38912,43008,46080,49152,53248,56320,59392,63488,66560,69632,4294967294,14,30,22,22,19,43008,1056915199,50465804,16780300,36,8064,320,1127350272,1127350272,1127350272,0,1098907648,1107296256,6408,135432,264456,32768,35840,38912,34176,722304,1410432,17201,1111507764,4294911692,4294967294,17201,1111507764,4294911692,4294967294,17201,1111507764,4294911692,4294967294,
1505280,1505280,1505280,1548288,1548288,1548288,1591296,1591296,1591296,1634304,1634304,1634304,32768,35840,38912,43008,46080,49152,53248,56320,59392,63488,66560,69632,4294967294,14,30,22,22,19,192,1040137727,16780300,50465804,8064,36,320,1108344832,1108344832,1108344832,0,1098907648,1107296256,290304,290880,291456,32768,35840,38912,1548288,1551360,1554432,16908,1111507480,4294957132,4294967294,16908,1111507480,4294957132,4294967294,16908,1111507480,4294957132,4294967294,
3397632,3397632,3397632,3440640,3440640,3440640,3483648,3483648,3483648,3526656,3526656,3526656,32768,35840,38912,43008,46080,49152,53248,56320,59392,63488,66560,69632,4294967294,14,30,22,22,19,192,1040137855,16780300,50465804,8064,36,320,1117782016,1117782016,1117782016,0,1098907648,1107296256,645120,645696,646272,32768,35840,38912,3440640,3443712,3446784,17054,1111507620,4294943052,4294967294,17054,1111507620,4294943052,4294967294,17054,1111507620,4294943052,4294967294,
5376000,5376000,5376000,5419008,5419008,5419008,5462016,5462016,5462016,5505024,5505024,5505024,32768,35840,38912,43008,46080,49152,53248,56320,59392,63488,66560,69632,4294967294,14,30,22,22,19,192,1048526591,16780300,50465804,8064,36,320,1123811328,1123811328,1123811328,0,1098907648,1107296256,1016064,1016640,1017216,32768,35840,38912,5419008,5422080,5425152,17146,1111507712,4294928332,4294967294,17146,1111507712,4294928332,4294967294,17146,1111507712,4294928332,4294967294,
7268352,7268352,7268352,7311360,7311360,7311360,7354368,7354368,7354368,7397376,7397376,7397376,32768,35840,38912,43008,46080,49152,53248,56320,59392,63488,66560,69632,4294967294,14,30,22,22,19,192,1048526463,16780300,50465804,8064,36,320,1126825984,1126825984,1126825984,0,1098907648,1107296256,1370880,1371456,1372032,32768,35840,38912,7311360,7314432,7317504,17193,1111507756,4294914252,4294967294,17193,1111507756,4294914252,4294967294,17193,1111507756,4294914252,4294967294,
9160704,9160704,9160704,9203712,9203712,9203712,9246720,9246720,9246720,9289728,9289728,9289728,32768,35840,38912,43008,46080,49152,53248,56320,59392,63488,66560,69632,4294967294,14,30,22,22,19,192,1048526591,16780300,50465804,8064,36,320,1129709568,1129709568,1129709568,0,1098907648,1107296256,1725696,1726272,1726848,32768,35840,38912,9203712,9206784,9209856,17237,1111507800,4294900172,4294967294,17237,1111507800,4294900172,4294967294,17237,1111507800,4294900172,4294967294,
16704,16704,16704,16896,16896,16896,17088,17088,17088,17280,17280,17280,32768,35840,38912,43008,46080,49152,53248,56320,59392,63488,66560,69632,4294967294,14,30,22,22,19,43008,1056914943,50465804,16780300,36,8064,320,1118830592,1118830592,1118830592,0,1098907648,1107296256,3168,132192,261216,32768,35840,38912,16896,705024,1393152,17070,1111507636,4294940492,4294967294,17070,1111507636,4294940492,4294967294,17070,1111507636,4294940492,4294967294,
0,0,0,0,0,0,0,0,0,0,0,0,0,0,0,0,0,0,0,0,0,0,0,0,0,0,0,0,0,0,0,1056915071,0,0,0,0,0,0,0,0,0,0,0,0,0,0,0,0,0,0,0,0,0,0,0,0,0,0,0,0,0,0,0,0,
34368,34368,34368,34560,34560,34560,34752,34752,34752,34944,34944,34944,32768,35840,38912,43008,46080,49152,53248,56320,59392,63488,66560,69632,4294967294,14,30,22,22,19,43008,1056915199,50465804,16780300,36,8064,320,1127481344,1127481344,1127481344,0,1098907648,1107296256,6480,135504,264528,32768,35840,38912,34560,722688,1410816,17203,1111507766,4294911052,4294967294,17203,1111507766,4294911052,4294967294,17203,1111507766,4294911052,4294967294,
0,0,0,0,0,0,0,0,0,0,0,0,0,0,0,0,0,0,0,0,0,0,0,0,0,0,0,0,0,0,0,1040137727,0,0,0,0,0,0,0,0,0,0,0,0,0,0,0,0,0,0,0,0,0,0,0,0,0,0,0,0,0,0,0,0,
258048,301056,5978112,344064,387072,6021120,430080,473088,6021120,473088,473088,6021120,40960,45056,32768,49152,53248,36864,57344,61440,36864,61440,61440,36864,4294967294,4294967294,62,21,21,19,192,1040137983,16780300,50465804,8064,36,128,1124597760,1090519040,1124728832,1115684864,0,1115684864,1099008,64512,1115136,16384,45056,24576,1073741824,344064,5947392,1115439875,1117799180,4294950156,62,16576,1099972912,524,4294967294,1115439875,1117799180,4294950156,62,
5462016,5462016,5462016,5505024,5505024,5505024,5548032,5548032,5548032,5591040,5591040,5591040,32768,35840,38912,43008,46080,49152,53248,56320,59392,63488,66560,69632,4294967294,14,30,22,22,19,192,1048526591,16780300,50465804,8064,36,320,1124073472,1124073472,1124073472,0,1098907648,1107296256,1032192,1032768,1033344,32768,35840,38912,5505024,5508096,5511168,17150,1111507714,4294927692,4294967294,17150,1111507714,4294927692,4294967294,17150,1111507714,4294927692,4294967294,
7354368,7354368,7354368,7397376,7397376,7397376,7440384,7440384,7440384,7483392,7483392,7483392,32768,35840,38912,43008,46080,49152,53248,56320,59392,63488,66560,69632,4294967294,14,30,22,22,19,192,1048526463,16780300,50465804,8064,36,320,1126957056,1126957056,1126957056,0,1098907648,1107296256,1387008,1387584,1388160,32768,35840,38912,7397376,7400448,7403520,17195,1111507758,4294913612,4294967294,17195,1111507758,4294913612,4294967294,17195,1111507758,4294913612,4294967294,
9246720,9246720,9246720,9289728,9289728,9289728,9332736,9332736,9332736,9375744,9375744,9375744,32768,35840,38912,43008,46080,49152,53248,56320,59392,63488,66560,69632,4294967294,14,30,22,22,19,192,1048526591,16780300,50465804,8064,36,320,1129840640,1129840640,1129840640,0,1098907648,1107296256,1741824,1742400,1742976,32768,35840,38912,9289728,9292800,9295872,17239,1111507802,4294899532,4294967294,17239,1111507802,4294899532,4294967294,17239,1111507802,4294899532,4294967294,
17088,17088,17088,17280,17280,17280,17472,17472,17472,17664,17664,17664,32768,35840,38912,43008,46080,49152,53248,56320,59392,63488,66560,69632,4294967294,14,30,22,22,19,43008,1056914943,50465804,16780300,36,8064,320,1119092736,1119092736,1119092736,0,1098907648,1107296256,3240,132264,261288,32768,35840,38912,17280,705408,1393536,17074,1111507640,4294939852,4294967294,17074,1111507640,4294939852,4294967294,17074,1111507640,4294939852,4294967294,
0,0,5117952,0,43008,5160960,86016,129024,5203968,129024,129024,5246976,49152,53248,32768,57344,61440,36864,65536,69632,40960,69632,69632,45056,30,30,174,21,21,19,192,1056915071,16780300,50465804,8064,36,128,0,1122762752,1123024896,1107296256,1127219200,1127219200,1152,957888,974016,53248,24576,32768,6144,5108736,5194752,1106247680,1112031296,1612,30,1127170782,1128284916,4294952044,174,1127170782,1128284916,4294952044,174,
34752,34752,34752,34944,34944,34944,35136,35136,35136,35328,35328,35328,32768,35840,38912,43008,46080,49152,53248,56320,59392,63488,66560,69632,4294967294,14,30,22,22,19,43008,1056915199,50465804,16780300,36,8064,320,1127612416,1127612416,1127612416,0,1098907648,1107296256,6552,135576,264600,32768,35840,38912,34944,723072,1411200,17205,1111507768,4294910412,4294967294,17205,1111507768,4294910412,4294967294,17205,1111507768,4294910412,4294967294,
1591296,1591296,1591296,1634304,1634304,1634304,1677312,1677312,1677312,1720320,1720320,1720320,32768,35840,38912,43008,46080,49152,53248,56320,59392,63488,66560,69632,4294967294,14,30,22,22,19,192,1040137855,16780300,50465804,8064,36,320,1108869120,1108869120,1108869120,0,1098907648,1107296256,306432,307008,307584,32768,35840,38912,1634304,1637376,1640448,16916,1111507488,4294956492,4294967294,16916,1111507488,4294956492,4294967294,16916,1111507488,4294956492,4294967294,
3483648,3483648,3483648,3526656,3526656,3526656,3569664,3569664,3569664,3612672,3612672,3612672,32768,35840,38912,43008,46080,49152,53248,56320,59392,63488,66560,69632,4294967294,14,30,22,22,19,192,1040137983,16780300,50465804,8064,36,320,1118044160,1118044160,1118044160,0,1098907648,1107296256,661248,661824,662400,32768,35840,38912,3526656,3529728,3532800,17058,1111507624,4294942412,4294967294,17058,1111507624,4294942412,4294967294,17058,1111507624,4294942412,4294967294,
688128,731136,8988672,774144,817152,9031680,860160,903168,9074688,903168,903168,9117696,49152,53248,32768,57344,61440,36864,65536,69632,40960,69632,69632,45056,4294967294,4294967294,78,21,21,19,192,1048526591,16780300,50465804,8064,36,128,1099956224,1129316352,1129447424,0,1117782016,1117782016,145152,1680192,1696320,53248,24576,32768,774144,8961024,9047040,16768,1099973032,4294966796,4294967294,1117668169,1119896404,4294941100,78,1117668169,1119896404,4294941100,78,
7440384,7440384,7440384,7483392,7483392,7483392,7526400,7526400,7526400,7569408,7569408,7569408,32768,35840,38912,43008,46080,49152,53248,56320,59392,63488,66560,69632,4294967294,14,30,22,22,19,192,1048526463,16780300,50465804,8064,36,320,1127088128,1127088128,1127088128,0,1098907648,1107296256,1403136,1403712,1404288,32768,35840,38912,7483392,7486464,7489536,17197,1111507760,4294912972,4294967294,17197,1111507760,4294912972,4294967294,17197,1111507760,4294912972,4294967294,
0,43008,8128512,86016,129024,8171520,172032,215040,8214528,215040,215040,8257536,49152,53248,32768,57344,61440,36864,65536,69632,40960,69632,69632,45056,14,14,126,21,21,19,192,1048526591,16780300,50465804,8064,36,128,1073741824,1128005632,1128136704,1098907648,1124073472,1124073472,16704,1520640,1536768,53248,24576,32768,89088,8110080,8196096,1096810496,1107837088,1452,14,1123959605,1125139264,4294943372,126,1123959605,1125139264,4294943372,126,
17472,17472,17472,17664,17664,17664,17856,17856,17856,18048,18048,18048,32768,35840,38912,43008,46080,49152,53248,56320,59392,63488,66560,69632,4294967294,14,30,22,22,19,43008,1056914943,50465804,16780300,36,8064,320,1119354880,1119354880,1119354880,0,1098907648,1107296256,3312,132336,261360,32768,35840,38912,17664,705792,1393920,17078,1111507644,4294939212,4294967294,17078,1111507644,4294939212,4294967294,17078,1111507644,4294939212,4294967294,
25920,25920,25920,26112,26112,26112,26304,26304,26304,26496,26496,26496,32768,35840,38912,43008,46080,49152,53248,56320,59392,63488,66560,69632,4294967294,14,30,22,22,19,43008,1056915071,50465804,16780300,36,8064,320,1124597760,1124597760,1124597760,0,1098907648,1107296256,4896,133920,262944,32768,35840,38912,26112,714240,1402368,17159,1111507722,4294925132,4294967294,17159,1111507722,4294925132,4294967294,17159,1111507722,4294925132,4294967294,
0,0,0,0,0,0,0,0,0,0,0,0,0,0,0,0,0,0,0,0,0,0,0,0,0,0,0,0,0,0,0,1056915199,0,0,0,0,0,0,0,0,0,0,0,0,0,0,0,0,0,0,0,0,0,0,0,0,0,0,0,0,0,0,0,0,
0,0,3741696,0,43008,3784704,86016,129024,3784704,129024,129024,3784704,40960,45056,32768,49152,53248,36864,57344,61440,36864,61440,61440,36864,4294967294,4294967294,46,21,21,19,192,1040137855,16780300,50465804,8064,36,128,1118306304,0,1118568448,1111490560,0,1111490560,679104,0,695232,16384,45056,24576,1073741824,0,3707904,1111245470,1115701936,4294956908,46,0,1099972672,1548,4294967294,1111245470,1115701936,4294956908,46,
3569664,3569664,3569664,3612672,3612672,3612672,3655680,3655680,3655680,3698688,3698688,3698688,32768,35840,38912,43008,46080,49152,53248,56320,59392,63488,66560,69632,4294967294,14,30,22,22,19,192,1040137983,16780300,50465804,8064,36,320,1118306304,1118306304,1118306304,0,1098907648,1107296256,677376,677952,678528,32768,35840,38912,3612672,3615744,3618816,17062,1111507628,4294941772,4294967294,17062,1111507628,4294941772,4294967294,17062,1111507628,4294941772,4294967294,
5548032,5548032,5548032,5591040,5591040,5591040,5634048,5634048,5634048,5677056,5677056,5677056,32768,35840,38912,43008,46080,49152,53248,56320,59392,63488,66560,69632,4294967294,14,30,22,22,19,192,1048526591,16780300,50465804,8064,36,320,1124204544,1124204544,1124204544,0,1098907648,1107296256,1048320,1048896,1049472,32768,35840,38912,5591040,5594112,5597184,17153,1111507716,4294927052,4294967294,17153,1111507716,4294927052,4294967294,17153,1111507716,4294927052,4294967294,
0,0,0,0,0,0,0,0,0,0,0,0,0,0,0,0,0,0,0,0,0,0,0,0,0,0,0,0,0,0,0,1048526463,0,0,0,0,0,0,0,0,0,0,0,0,0,0,0,0,0,0,0,0,0,0,0,0,0,0,0,0,0,0,0,0,
9332736,9332736,9332736,9375744,9375744,9375744,9418752,9418752,9418752,9461760,9461760,9461760,32768,35840,38912,43008,46080,49152,53248,56320,59392,63488,66560,69632,4294967294,14,30,22,22,19,192,1048526591,16780300,50465804,8064,36,320,1129971712,1129971712,1129971712,0,1098907648,1107296256,1757952,1758528,1759104,32768,35840,38912,9375744,9378816,9381888,17241,1111507804,4294898892,4294967294,17241,1111507804,4294898892,4294967294,17241,1111507804,4294898892,4294967294,
0,0,0,0,0,0,0,0,0,0,0,0,0,0,0,0,0,0,0,0,0,0,0,0,0,0,0,0,0,0,0,1056915071,0,0,0,0,0,0,0,0,0,0,0,0,0,0,0,0,0,0,0,0,0,0,0,0,0,0,0,0,0,0,0,0,
26304,26304,26304,26496,26496,26496,26688,26688,26688,26880,26880,26880,32768,35840,38912,43008,46080,49152,53248,56320,59392,63488,66560,69632,4294967294,14,30,22,22,19,43008,1056915071,50465804,16780300,36,8064,320,1124728832,1124728832,1124728832,0,1098907648,1107296256,4968,133992,263016,32768,35840,38912,26496,714624,1402752,17161,1111507724,4294924492,4294967294,17161,1111507724,4294924492,4294967294,17161,1111507724,4294924492,4294967294,
430080,473088,8816640,516096,559104,8859648,602112,645120,8859648,688128,731136,8859648,40960,45056,32768,49152,53248,36864,57344,61440,36864,65536,69632,36864,30,30,190,21,21,19,192,1056915199,16780300,50465804,8064,36,128,1094713344,1096810496,1129054208,1107296256,1107296256,1128267776,97920,114048,1651968,45056,53248,24576,522240,608256,8810496,1106264352,1112031624,4294967116,30,1106264352,1112031624,4294967116,30,1128219461,1129333582,4294940940,190,
1677312,1677312,1677312,1720320,1720320,1720320,1763328,1763328,1763328,1806336,1806336,1806336,32768,35840,38912,43008,46080,49152,53248,56320,59392,63488,66560,69632,4294967294,14,30,22,22,19,192,1040137855,16780300,50465804,8064,36,320,1109393408,1109393408,1109393408,0,1098907648,1107296256,322560,323136,323712,32768,35840,38912,1720320,1723392,1726464,16924,1111507496,4294955852,4294967294,16924,1111507496,4294955852,4294967294,16924,1111507496,4294955852,4294967294,
0,0,0,0,0,0,0,0,0,0,0,0,0,0,0,0,0,0,0,0,0,0,0,0,0,0,0,0,0,0,0,1040137983,0,0,0,0,0,0,0,0,0,0,0,0,0,0,0,0,0,0,0,0,0,0,0,0,0,0,0,0,0,0,0,0,
5634048,5634048,5634048,5677056,5677056,5677056,5720064,5720064,5720064,5763072,5763072,5763072,32768,35840,38912,43008,46080,49152,53248,56320,59392,63488,66560,69632,4294967294,14,30,22,22,19,192,1048526591,16780300,50465804,8064,36,320,1124335616,1124335616,1124335616,0,1098907648,1107296256,1064448,1065024,1065600,32768,35840,38912,5677056,5680128,5683200,17155,1111507718,4294926412,4294967294,17155,1111507718,4294926412,4294967294,17155,1111507718,4294926412,4294967294,
7526400,7526400,7526400,7569408,7569408,7569408,7612416,7612416,7612416,7655424,7655424,7655424,32768,35840,38912,43008,46080,49152,53248,56320,59392,63488,66560,69632,4294967294,14,30,22,22,19,192,1048526463,16780300,50465804,8064,36,320,1127219200,1127219200,1127219200,0,1098907648,1107296256,1419264,1419840,1420416,32768,35840,38912,7569408,7572480,7575552,17199,1111507762,4294912332,4294967294,17199,1111507762,4294912332,4294967294,17199,1111507762,4294912332,4294967294,
0,0,0,0,0,0,0,0,0,0,0,0,0,0,0,0,0,0,0,0,0,0,0,0,0,0,0,0,0,0,0,1048526591,0,0,0,0,0,0,0,0,0,0,0,0,0,0,0,0,0,0,0,0,0,0,0,0,0,0,0,0,0,0,0,0,
430080,473088,3741696,516096,559104,3784704,602112,645120,3784704,688128,731136,3784704,40960,45056,32768,49152,53248,36864,57344,61440,36864,65536,69632,36864,14,14,158,21,21,19,192,1056915071,16780300,50465804,8064,36,128,1094713344,1096810496,1118568448,1098907648,1098907648,1126170624,97344,113472,699264,45056,53248,24576,519168,605184,3729408,1096827168,1107837320,4294967212,14,1096827168,1107837320,4294967212,14,1126122142,1127236272,4294956236,158,
26688,26688,26688,26880,26880,26880,27072,27072,27072,27264,27264,27264,32768,35840,38912,43008,46080,49152,53248,56320,59392,63488,66560,69632,4294967294,14,30,22,22,19,43008,1056915199,50465804,16780300,36,8064,320,1124859904,1124859904,1124859904,0,1098907648,1107296256,5040,134064,263088,32768,35840,38912,26880,715008,1403136,17163,1111507726,4294923852,4294967294,17163,1111507726,4294923852,4294967294,17163,1111507726,4294923852,4294967294,
35136,35136,35136,35328,35328,35328,35520,35520,35520,35712,35712,35712,32768,35840,38912,43008,46080,49152,53248,56320,59392,63488,66560,69632,4294967294,14,30,22,22,19,43008,1056915199,50465804,16780300,36,8064,320,1127743488,1127743488,1127743488,0,1098907648,1107296256,6624,135648,264672,32768,35840,38912,35328,723456,1411584,17207,1111507770,4294909772,4294967294,17207,1111507770,4294909772,4294967294,17207,1111507770,4294909772,4294967294,
1763328,1763328,1763328,1806336,1806336,1806336,1849344,1849344,1849344,1892352,1892352,1892352,32768,35840,38912,43008,46080,49152,53248,56320,59392,63488,66560,69632,4294967294,14,30,22,22,19,192,1040137855,16780300,50465804,8064,36,320,1109917696,1109917696,1109917696,0,1098907648,1107296256,338688,339264,339840,32768,35840,38912,1806336,1809408,1812480,16932,1111507504,4294955212,4294967294,16932,1111507504,4294955212,4294967294,16932,1111507504,4294955212,4294967294,
3655680,3655680,3655680,3698688,3698688,3698688,3741696,3741696,3741696,3784704,3784704,3784704,32768,35840,38912,43008,46080,49152,53248,56320,59392,63488,66560,69632,4294967294,14,30,22,22,19,192,1040137983,16780300,50465804,8064,36,320,1118568448,1118568448,1118568448,0,1098907648,1107296256,693504,694080,694656,32768,35840,38912,3698688,3701760,3704832,17066,1111507632,4294941132,4294967294,17066,1111507632,4294941132,4294967294,17066,1111507632,4294941132,4294967294,
5720064,5720064,5720064,5763072,5763072,5763072,5806080,5806080,5806080,5849088,5849088,5849088,32768,35840,38912,43008,46080,49152,53248,56320,59392,63488,66560,69632,4294967294,14,30,22,22,19,192,1048526335,16780300,50465804,8064,36,320,1124466688,1124466688,1124466688,0,1098907648,1107296256,1080576,1081152,1081728,32768,35840,38912,5763072,5766144,5769216,17157,1111507720,4294925772,4294967294,17157,1111507720,4294925772,4294967294,17157,1111507720,4294925772,4294967294,
1118208,1161216,5806080,1204224,1247232,5849088,1290240,1333248,5892096,1333248,1333248,5935104,49152,53248,32768,57344,61440,36864,65536,69632,40960,69632,69632,45056,4294967294,4294967294,110,21,21,19,192,1048526463,16780300,50465804,8064,36,128,1105199104,1124466688,1124597760,0,1121976320,1121976320,225792,1084608,1100736,53248,24576,32768,1204224,5784576,5870592,16848,1099973112,4294965516,4294967294,1121862398,1124090634,4294950380,110,1121862398,1124090634,4294950380,110,
9418752,9418752,9418752,9461760,9461760,9461760,9504768,9504768,9504768,9547776,9547776,9547776,32768,35840,38912,43008,46080,49152,53248,56320,59392,63488,66560,69632,4294967294,14,30,22,22,19,192,1048526591,16780300,50465804,8064,36,320,1130102784,1130102784,1130102784,0,1098907648,1107296256,1774080,1774656,1775232,32768,35840,38912,9461760,9464832,9467904,17243,1111507806,4294898252,4294967294,17243,1111507806,4294898252,4294967294,17243,1111507806,4294898252,4294967294,
17856,17856,17856,18048,18048,18048,18240,18240,18240,18432,18432,18432,32768,35840,38912,43008,46080,49152,53248,56320,59392,63488,66560,69632,4294967294,14,30,22,22,19,43008,1056915071,50465804,16780300,36,8064,320,1119617024,1119617024,1119617024,0,1098907648,1107296256,3384,132408,261432,32768,35840,38912,18048,706176,1394304,17082,1111507648,4294938572,4294967294,17082,1111507648,4294938572,4294967294,17082,1111507648,4294938572,4294967294,
27072,27072,27072,27264,27264,27264,27456,27456,27456,27648,27648,27648,32768,35840,38912,43008,46080,49152,53248,56320,59392,63488,66560,69632,4294967294,14,30,22,22,19,43008,1056915199,50465804,16780300,36,8064,320,1124990976,1124990976,1124990976,0,1098907648,1107296256,5112,134136,263160,32768,35840,38912,27264,715392,1403520,17165,1111507728,4294923212,4294967294,17165,1111507728,4294923212,4294967294,17165,1111507728,4294923212,4294967294,
35520,35520,35520,35712,35712,35712,35904,35904,35904,36096,36096,36096,32768,35840,38912,43008,46080,49152,53248,56320,59392,63488,66560,69632,4294967294,14,30,22,22,19,43008,1056915199,50465804,16780300,36,8064,320,1127874560,1127874560,1127874560,0,1098907648,1107296256,6696,135720,264744,32768,35840,38912,35712,723840,1411968,17209,1111507772,4294909132,4294967294,17209,1111507772,4294909132,4294967294,17209,1111507772,4294909132,4294967294,
1849344,1849344,1849344,1892352,1892352,1892352,1935360,1935360,1935360,1978368,1978368,1978368,32768,35840,38912,43008,46080,49152,53248,56320,59392,63488,66560,69632,4294967294,14,30,22,22,19,192,1040137855,16780300,50465804,8064,36,320,1110441984,1110441984,1110441984,0,1098907648,1107296256,354816,355392,355968,32768,35840,38912,1892352,1895424,1898496,16940,1111507512,4294954572,4294967294,16940,1111507512,4294954572,4294967294,16940,1111507512,4294954572,4294967294,
3741696,3741696,3741696,3784704,3784704,3784704,3827712,3827712,3827712,3870720,3870720,3870720,32768,35840,38912,43008,46080,49152,53248,56320,59392,63488,66560,69632,4294967294,14,30,22,22,19,192,1040137983,16780300,50465804,8064,36,320,1118830592,1118830592,1118830592,0,1098907648,1107296256,709632,710208,710784,32768,35840,38912,3784704,3787776,3790848,17070,1111507636,4294940492,4294967294,17070,1111507636,4294940492,4294967294,17070,1111507636,4294940492,4294967294,
5806080,5806080,5806080,5849088,5849088,5849088,5892096,5892096,5892096,5935104,5935104,5935104,32768,35840,38912,43008,46080,49152,53248,56320,59392,63488,66560,69632,4294967294,14,30,22,22,19,192,1048526335,16780300,50465804,8064,36,320,1124597760,1124597760,1124597760,0,1098907648,1107296256,1096704,1097280,1097856,32768,35840,38912,5849088,5852160,5855232,17159,1111507722,4294925132,4294967294,17159,1111507722,4294925132,4294967294,17159,1111507722,4294925132,4294967294,
7612416,7612416,7612416,7655424,7655424,7655424,7698432,7698432,7698432,7741440,7741440,7741440,32768,35840,38912,43008,46080,49152,53248,56320,59392,63488,66560,69632,4294967294,14,30,22,22,19,192,1048526591,16780300,50465804,8064,36,320,1127350272,1127350272,1127350272,0,1098907648,1107296256,1435392,1435968,1436544,32768,35840,38912,7655424,7658496,7661568,17201,1111507764,4294911692,4294967294,17201,1111507764,4294911692,4294967294,17201,1111507764,4294911692,4294967294,
9504768,9504768,9504768,9547776,9547776,9547776,9590784,9590784,9590784,9590784,9590784,9590784,32768,35840,38912,43008,46080,49152,53248,56320,59392,63488,66560,69632,4294967294,14,30,22,22,19,192,1048526591,16780300,50465804,8064,36,320,1130233856,1130233856,1130233856,0,1098907648,1107296256,1790208,1790784,1791360,32768,35840,38912,9547776,9550848,9553920,17245,1111507807,4294897612,4294967294,17245,1111507807,4294897612,4294967294,17245,1111507807,4294897612,4294967294,
18240,18240,18240,18432,18432,18432,18624,18624,18624,18816,18816,18816,32768,35840,38912,43008,46080,49152,53248,56320,59392,63488,66560,69632,4294967294,14,30,22,22,19,43008,1056915071,50465804,16780300,36,8064,320,1119879168,1119879168,1119879168,0,1098907648,1107296256,3456,132480,261504,32768,35840,38912,18432,706560,1394688,17086,1111507652,4294937932,4294967294,17086,1111507652,4294937932,4294967294,17086,1111507652,4294937932,4294967294,
0,0,0,0,0,0,0,0,0,0,0,0,0,0,0,0,0,0,0,0,0,0,0,0,0,0,0,0,0,0,0,1056915199,0,0,0,0,0,0,0,0,0,0,0,0,0,0,0,0,0,0,0,0,0,0,0,0,0,0,0,0,0,0,0,0,
35904,35904,35904,36096,36096,36096,36288,36288,36288,36480,36480,36480,32768,35840,38912,43008,46080,49152,53248,56320,59392,63488,66560,69632,4294967294,14,30,22,22,19,43008,1056915199,50465804,16780300,36,8064,320,1128005632,1128005632,1128005632,0,1098907648,1107296256,6768,135792,264816,32768,35840,38912,36096,724224,1412352,17211,1111507774,4294908492,4294967294,17211,1111507774,4294908492,4294967294,17211,1111507774,4294908492,4294967294,
0,0,0,0,0,0,0,0,0,0,0,0,0,0,0,0,0,0,0,0,0,0,0,0,0,0,0,0,0,0,0,1040137855,0,0,0,0,0,0,0,0,0,0,0,0,0,0,0,0,0,0,0,0,0,0,0,0,0,0,0,0,0,0,0,0,
3827712,3827712,3827712,3870720,3870720,3870720,3913728,3913728,3913728,3956736,3956736,3956736,32768,35840,38912,43008,46080,49152,53248,56320,59392,63488,66560,69632,4294967294,14,30,22,22,19,192,1040137983,16780300,50465804,8064,36,320,1119092736,1119092736,1119092736,0,1098907648,1107296256,725760,726336,726912,32768,35840,38912,3870720,3873792,3876864,17074,1111507640,4294939852,4294967294,17074,1111507640,4294939852,4294967294,17074,1111507640,4294939852,4294967294,
0,0,0,0,0,0,0,0,0,0,0,0,0,0,0,0,0,0,0,0,0,0,0,0,0,0,0,0,0,0,0,1048526335,0,0,0,0,0,0,0,0,0,0,0,0,0,0,0,0,0,0,0,0,0,0,0,0,0,0,0,0,0,0,0,0,
7698432,7698432,7698432,7741440,7741440,7741440,7784448,7784448,7784448,7827456,7827456,7827456,32768,35840,38912,43008,46080,49152,53248,56320,59392,63488,66560,69632,4294967294,14,30,22,22,19,192,1048526591,16780300,50465804,8064,36,320,1127481344,1127481344,1127481344,0,1098907648,1107296256,1451520,1452096,1452672,32768,35840,38912,7741440,7744512,7747584,17203,1111507766,4294911052,4294967294,17203,1111507766,4294911052,4294967294,17203,1111507766,4294911052,4294967294,
9024,9024,9024,9216,9216,9216,9408,9408,9408,9600,9600,9600,32768,35840,38912,43008,46080,49152,53248,56320,59392,63488,66560,69632,4294967294,14,30,22,22,19,43008,1056914943,50465804,16780300,36,8064,320,1111490560,1111490560,1111490560,0,1098907648,1107296256,1728,130752,259776,32768,35840,38912,9216,697344,1385472,16956,1111507528,4294953292,4294967294,16956,1111507528,4294953292,4294967294,16956,1111507528,4294953292,4294967294,
18624,18624,18624,18816,18816,18816,19008,19008,19008,19200,19200,19200,32768,35840,38912,43008,46080,49152,53248,56320,59392,63488,66560,69632,4294967294,14,30,22,22,19,43008,1056915071,50465804,16780300,36,8064,320,1120141312,1120141312,1120141312,0,1098907648,1107296256,3528,132552,261576,32768,35840,38912,18816,706944,1395072,17090,1111507656,4294937292,4294967294,17090,1111507656,4294937292,4294967294,17090,1111507656,4294937292,4294967294,
0,43008,7182336,86016,129024,7225344,172032,215040,7268352,215040,215040,7311360,49152,53248,32768,57344,61440,36864,65536,69632,40960,69632,69632,45056,30,30,174,21,21,19,192,1056915199,16780300,50465804,8064,36,128,1073741824,1126563840,1126694912,1107296256,1127219200,1127219200,17280,1344960,1361088,53248,24576,32768,92160,7173120,7259136,1106247680,1112031392,1356,30,1127170847,1128284970,4294945900,174,1127170847,1128284970,4294945900,174,
36288,36288,36288,36480,36480,36480,36672,36672,36672,36864,36864,36864,32768,35840,38912,43008,46080,49152,53248,56320,59392,63488,66560,69632,4294967294,14,30,22,22,19,43008,1056914943,50465804,16780300,36,8064,320,1128136704,1128136704,1128136704,0,1098907648,1107296256,6840,135864,264888,32768,35840,38912,36480,724608,1412736,17213,1111507776,4294907852,4294967294,17213,1111507776,4294907852,4294967294,17213,1111507776,4294907852,4294967294,
1935360,1935360,1935360,1978368,1978368,1978368,2021376,2021376,2021376,2064384,2064384,2064384,32768,35840,38912,43008,46080,49152,53248,56320,59392,63488,66560,69632,4294967294,14,30,22,22,19,192,1040137855,16780300,50465804,8064,36,320,1110966272,1110966272,1110966272,0,1098907648,1107296256,370944,371520,372096,32768,35840,38912,1978368,1981440,1984512,16948,1111507520,4294953932,4294967294,16948,1111507520,4294953932,4294967294,16948,1111507520,4294953932,4294967294,
344064,387072,8214528,430080,473088,8257536,516096,559104,8257536,559104,559104,8257536,40960,45056,32768,49152,53248,36864,57344,61440,36864,61440,61440,36864,4294967294,4294967294,62,21,21,19,192,1040137983,16780300,50465804,8064,36,128,1128005632,1092616192,1128136704,1115684864,0,1115684864,1518336,80640,1534464,16384,45056,24576,1073741824,430080,8183808,1115439927,1117799232,4294943500,62,16640,1099972944,268,4294967294,1115439927,1117799232,4294943500,62,
774144,817152,3483648,860160,903168,3526656,946176,989184,3569664,989184,989184,3612672,49152,53248,32768,57344,61440,36864,65536,69632,40960,69632,69632,45056,4294967294,4294967294,94,21,21,19,192,1048526463,16780300,50465804,8064,36,128,1101004800,1117782016,1118044160,0,1119879168,1119879168,161280,648576,664704,53248,24576,32768,860160,3459072,3545088,16784,1099973048,4294966540,4294967294,1119765138,1121993384,4294957388,94,1119765138,1121993384,4294957388,94,
0,0,0,0,0,0,0,0,0,0,0,0,0,0,0,0,0,0,0,0,0,0,0,0,0,0,0,0,0,0,0,1048526591,0,0,0,0,0,0,0,0,0,0,0,0,0,0,0,0,0,0,0,0,0,0,0,0,0,0,0,0,0,0,0,0,
9408,9408,9408,9600,9600,9600,9792,9792,9792,9984,9984,9984,32768,35840,38912,43008,46080,49152,53248,56320,59392,63488,66560,69632,4294967294,14,30,22,22,19,43008,1056914943,50465804,16780300,36,8064,320,1112014848,1112014848,1112014848,0,1098907648,1107296256,1800,130824,259848,32768,35840,38912,9600,697728,1385856,16964,1111507536,4294952652,4294967294,16964,1111507536,4294952652,4294967294,16964,1111507536,4294952652,4294967294,
19008,19008,19008,19200,19200,19200,19392,19392,19392,19584,19584,19584,32768,35840,38912,43008,46080,49152,53248,56320,59392,63488,66560,69632,4294967294,14,30,22,22,19,43008,1056915071,50465804,16780300,36,8064,320,1120403456,1120403456,1120403456,0,1098907648,1107296256,3600,132624,261648,32768,35840,38912,19200,707328,1395456,17094,1111507660,4294936652,4294967294,17094,1111507660,4294936652,4294967294,17094,1111507660,4294936652,4294967294,
27456,27456,27456,27648,27648,27648,27840,27840,27840,28032,28032,28032,32768,35840,38912,43008,46080,49152,53248,56320,59392,63488,66560,69632,4294967294,14,30,22,22,19,43008,1056915199,50465804,16780300,36,8064,320,1125122048,1125122048,1125122048,0,1098907648,1107296256,5184,134208,263232,32768,35840,38912,27648,715776,1403904,17167,1111507730,4294922572,4294967294,17167,1111507730,4294922572,4294967294,17167,1111507730,4294922572,4294967294,
36672,36672,36672,36864,36864,36864,37056,37056,37056,37248,37248,37248,32768,35840,38912,43008,46080,49152,53248,56320,59392,63488,66560,69632,4294967294,14,30,22,22,19,43008,1056914943,50465804,16780300,36,8064,320,1128267776,1128267776,1128267776,0,1098907648,1107296256,6912,135936,264960,32768,35840,38912,36864,724992,1413120,17215,1111507778,4294907212,4294967294,17215,1111507778,4294907212,4294967294,17215,1111507778,4294907212,4294967294,
2021376,2021376,2021376,2064384,2064384,2064384,2107392,2107392,2107392,2150400,2150400,2150400,32768,35840,38912,43008,46080,49152,53248,56320,59392,63488,66560,69632,4294967294,14,30,22,22,19,192,1040137855,16780300,50465804,8064,36,320,1111490560,1111490560,1111490560,0,1098907648,1107296256,387072,387648,388224,32768,35840,38912,2064384,2067456,2070528,16956,1111507528,4294953292,4294967294,16956,1111507528,4294953292,4294967294,16956,1111507528,4294953292,4294967294,
3913728,3913728,3913728,3956736,3956736,3956736,3999744,3999744,3999744,4042752,4042752,4042752,32768,35840,38912,43008,46080,49152,53248,56320,59392,63488,66560,69632,4294967294,14,30,22,22,19,192,1040137983,16780300,50465804,8064,36,320,1119354880,1119354880,1119354880,0,1098907648,1107296256,741888,742464,743040,32768,35840,38912,3956736,3959808,3962880,17078,1111507644,4294939212,4294967294,17078,1111507644,4294939212,4294967294,17078,1111507644,4294939212,4294967294,
5892096,5892096,5892096,5935104,5935104,5935104,5978112,5978112,5978112,6021120,6021120,6021120,32768,35840,38912,43008,46080,49152,53248,56320,59392,63488,66560,69632,4294967294,14,30,22,22,19,192,1048526463,16780300,50465804,8064,36,320,1124728832,1124728832,1124728832,0,1098907648,1107296256,1112832,1113408,1113984,32768,35840,38912,5935104,5938176,5941248,17161,1111507724,4294924492,4294967294,17161,1111507724,4294924492,4294967294,17161,1111507724,4294924492,4294967294,
7784448,7784448,7784448,7827456,7827456,7827456,7870464,7870464,7870464,7913472,7913472,7913472,32768,35840,38912,43008,46080,49152,53248,56320,59392,63488,66560,69632,4294967294,14,30,22,22,19,192,1048526591,16780300,50465804,8064,36,320,1127612416,1127612416,1127612416,0,1098907648,1107296256,1467648,1468224,1468800,32768,35840,38912,7827456,7830528,7833600,17205,1111507768,4294910412,4294967294,17205,1111507768,4294910412,4294967294,17205,1111507768,4294910412,4294967294,
9792,9792,9792,9984,9984,9984,10176,10176,10176,10368,10368,10368,32768,35840,38912,43008,46080,49152,53248,56320,59392,63488,66560,69632,4294967294,14,30,22,22,19,43008,1056914943,50465804,16780300,36,8064,320,1112539136,1112539136,1112539136,0,1098907648,1107296256,1872,130896,259920,32768,35840,38912,9984,698112,1386240,16972,1111507544,4294952012,4294967294,16972,1111507544,4294952012,4294967294,16972,1111507544,4294952012,4294967294,
19392,19392,19392,19584,19584,19584,19776,19776,19776,19968,19968,19968,32768,35840,38912,43008,46080,49152,53248,56320,59392,63488,66560,69632,4294967294,14,30,22,22,19,43008,1056915071,50465804,16780300,36,8064,320,1120665600,1120665600,1120665600,0,1098907648,1107296256,3672,132696,261720,32768,35840,38912,19584,707712,1395840,17098,1111507664,4294936012,4294967294,17098,1111507664,4294936012,4294967294,17098,1111507664,4294936012,4294967294,
27840,27840,27840,28032,28032,28032,28224,28224,28224,28416,28416,28416,32768,35840,38912,43008,46080,49152,53248,56320,59392,63488,66560,69632,4294967294,14,30,22,22,19,43008,1056915199,50465804,16780300,36,8064,320,1125253120,1125253120,1125253120,0,1098907648,1107296256,5256,134280,263304,32768,35840,38912,28032,716160,1404288,17169,1111507732,4294921932,4294967294,17169,1111507732,4294921932,4294967294,17169,1111507732,4294921932,4294967294,
0,0,0,0,0,0,0,0,0,0,0,0,0,0,0,0,0,0,0,0,0,0,0,0,0,0,0,0,0,0,0,1056914943,0,0,0,0,0,0,0,0,0,0,0,0,0,0,0,0,0,0,0,0,0,0,0,0,0,0,0,0,0,0,0,0,
0,43008,5978112,86016,129024,6021120,172032,215040,6021120,215040,215040,6021120,40960,45056,32768,49152,53248,36864,57344,61440,36864,61440,61440,36864,4294967294,4294967294,46,21,21,19,192,1040137983,16780300,50465804,8064,36,128,1124597760,1073741824,1124728832,1111490560,0,1111490560,1098432,16128,1114560,16384,45056,24576,1073741824,86016,5944320,1111245571,1115702028,4294950252,46,0,1099972768,1292,4294967294,1111245571,1115702028,4294950252,46,
0,0,0,0,0,0,0,0,0,0,0,0,0,0,0,0,0,0,0,0,0,0,0,0,0,0,0,0,0,0,0,1040137983,0,0,0,0,0,0,0,0,0,0,0,0,0,0,0,0,0,0,0,0,0,0,0,0,0,0,0,0,0,0,0,0,
5978112,5978112,5978112,6021120,6021120,6021120,6064128,6064128,6064128,6107136,6107136,6107136,32768,35840,38912,43008,46080,49152,53248,56320,59392,63488,66560,69632,4294967294,14,30,22,22,19,192,1048526463,16780300,50465804,8064,36,320,1124859904,1124859904,1124859904,0,1098907648,1107296256,1128960,1129536,1130112,32768,35840,38912,6021120,6024192,6027264,17163,1111507726,4294923852,4294967294,17163,1111507726,4294923852,4294967294,17163,1111507726,4294923852,4294967294,
7870464,7870464,7870464,7913472,7913472,7913472,7956480,7956480,7956480,7999488,7999488,7999488,32768,35840,38912,43008,46080,49152,53248,56320,59392,63488,66560,69632,4294967294,14,30,22,22,19,192,1048526591,16780300,50465804,8064,36,320,1127743488,1127743488,1127743488,0,1098907648,1107296256,1483776,1484352,1484928,32768,35840,38912,7913472,7916544,7919616,17207,1111507770,4294909772,4294967294,17207,1111507770,4294909772,4294967294,17207,1111507770,4294909772,4294967294,
86016,129024,3139584,172032,215040,3182592,258048,301056,3225600,301056,301056,3268608,49152,53248,32768,57344,61440,36864,65536,69632,40960,69632,69632,45056,14,14,142,21,21,19,192,1056915071,16780300,50465804,8064,36,128,1082130432,1116733440,1116995584,1098907648,1125122048,1125122048,32832,585792,601920,53248,24576,32768,175104,3124224,3210240,1096826880,1107837152,1196,14,1125073538,1126187672,4294958124,142,1125073538,1126187672,4294958124,142,
19776,19776,19776,19968,19968,19968,20160,20160,20160,20352,20352,20352,32768,35840,38912,43008,46080,49152,53248,56320,59392,63488,66560,69632,4294967294,14,30,22,22,19,43008,1056915071,50465804,16780300,36,8064,320,1120927744,1120927744,1120927744,0,1098907648,1107296256,3744,132768,261792,32768,35840,38912,19968,708096,1396224,17102,1111507668,4294935372,4294967294,17102,1111507668,4294935372,4294967294,17102,1111507668,4294935372,4294967294,
28224,28224,28224,28416,28416,28416,28608,28608,28608,28800,28800,28800,32768,35840,38912,43008,46080,49152,53248,56320,59392,63488,66560,69632,4294967294,14,30,22,22,19,43008,1056915199,50465804,16780300,36,8064,320,1125384192,1125384192,1125384192,0,1098907648,1107296256,5328,134352,263376,32768,35840,38912,28416,716544,1404672,17171,1111507734,4294921292,4294967294,17171,1111507734,4294921292,4294967294,17171,1111507734,4294921292,4294967294,
37056,37056,37056,37248,37248,37248,37440,37440,37440,37632,37632,37632,32768,35840,38912,43008,46080,49152,53248,56320,59392,63488,66560,69632,4294967294,14,30,22,22,19,43008,1056915071,50465804,16780300,36,8064,320,1128398848,1128398848,1128398848,0,1098907648,1107296256,6984,136008,265032,32768,35840,38912,37248,725376,1413504,17217,1111507780,4294906572,4294967294,17217,1111507780,4294906572,4294967294,17217,1111507780,4294906572,4294967294,
2107392,2107392,2107392,2150400,2150400,2150400,2193408,2193408,2193408,2236416,2236416,2236416,32768,35840,38912,43008,46080,49152,53248,56320,59392,63488,66560,69632,4294967294,14,30,22,22,19,192,1040137983,16780300,50465804,8064,36,320,1112014848,1112014848,1112014848,0,1098907648,1107296256,403200,403776,404352,32768,35840,38912,2150400,2153472,2156544,16964,1111507536,4294952652,4294967294,16964,1111507536,4294952652,4294967294,16964,1111507536,4294952652,4294967294,
3999744,3999744,3999744,4042752,4042752,4042752,4085760,4085760,4085760,4128768,4128768,4128768,32768,35840,38912,43008,46080,49152,53248,56320,59392,63488,66560,69632,4294967294,14,30,22,22,19,192,1040137983,16780300,50465804,8064,36,320,1119617024,1119617024,1119617024,0,1098907648,1107296256,758016,758592,759168,32768,35840,38912,4042752,4045824,4048896,17082,1111507648,4294938572,4294967294,17082,1111507648,4294938572,4294967294,17082,1111507648,4294938572,4294967294,
6064128,6064128,6064128,6107136,6107136,6107136,6150144,6150144,6150144,6193152,6193152,6193152,32768,35840,38912,43008,46080,49152,53248,56320,59392,63488,66560,69632,4294967294,14,30,22,22,19,192,1048526463,16780300,50465804,8064,36,320,1124990976,1124990976,1124990976,0,1098907648,1107296256,1145088,1145664,1146240,32768,35840,38912,6107136,6110208,6113280,17165,1111507728,4294923212,4294967294,17165,1111507728,4294923212,4294967294,17165,1111507728,4294923212,4294967294,
7956480,7956480,7956480,7999488,7999488,7999488,8042496,8042496,8042496,8085504,8085504,8085504,32768,35840,38912,43008,46080,49152,53248,56320,59392,63488,66560,69632,4294967294,14,30,22,22,19,192,1048526591,16780300,50465804,8064,36,320,1127874560,1127874560,1127874560,0,1098907648,1107296256,1499904,1500480,1501056,32768,35840,38912,7999488,8002560,8005632,17209,1111507772,4294909132,4294967294,17209,1111507772,4294909132,4294967294,17209,1111507772,4294909132,4294967294,
10176,10176,10176,10368,10368,10368,10560,10560,10560,10752,10752,10752,32768,35840,38912,43008,46080,49152,53248,56320,59392,63488,66560,69632,4294967294,14,30,22,22,19,43008,1056915071,50465804,16780300,36,8064,320,1113063424,1113063424,1113063424,0,1098907648,1107296256,1944,130968,259992,32768,35840,38912,10368,698496,1386624,16980,1111507552,4294951372,4294967294,16980,1111507552,4294951372,4294967294,16980,1111507552,4294951372,4294967294,
0,0,0,0,0,0,0,0,0,0,0,0,0,0,0,0,0,0,0,0,0,0,0,0,0,0,0,0,0,0,0,1056915071,0,0,0,0,0,0,0,0,0,0,0,0,0,0,0,0,0,0,0,0,0,0,0,0,0,0,0,0,0,0,0,0,
0,0,0,0,0,0,0,0,0,0,0,0,0,0,0,0,0,0,0,0,0,0,0,0,0,0,0,0,0,0,0,1056915199,0,0,0,0,0,0,0,0,0,0,0,0,0,0,0,0,0,0,0,0,0,0,0,0,0,0,0,0,0,0,0,0,
602112,645120,688128,731136,774144,817152,860160,903168,946176,989184,989184,989184,32768,36864,40960,45056,49152,53248,57344,61440,65536,69632,69632,69632,30,30,30,21,21,21,192,1056915071,16780300,50465804,8064,36,128,1098907648,1099956224,1101004800,1107296256,1107296256,1107296256,130176,146304,162432,36864,45056,53248,694272,780288,866304,1106264416,1112031672,4294966348,30,1106264416,1112031672,4294966348,30,1106264416,1112031672,4294966348,30,
2193408,2193408,2193408,2236416,2236416,2236416,2279424,2279424,2279424,2322432,2322432,2322432,32768,35840,38912,43008,46080,49152,53248,56320,59392,63488,66560,69632,4294967294,14,30,22,22,19,192,1040137983,16780300,50465804,8064,36,320,1112539136,1112539136,1112539136,0,1098907648,1107296256,419328,419904,420480,32768,35840,38912,2236416,2239488,2242560,16972,1111507544,4294952012,4294967294,16972,1111507544,4294952012,4294967294,16972,1111507544,4294952012,4294967294,
4085760,4085760,4085760,4128768,4128768,4128768,4171776,4171776,4171776,4214784,4214784,4214784,32768,35840,38912,43008,46080,49152,53248,56320,59392,63488,66560,69632,4294967294,14,30,22,22,19,192,1040137983,16780300,50465804,8064,36,320,1119879168,1119879168,1119879168,0,1098907648,1107296256,774144,774720,775296,32768,35840,38912,4128768,4131840,4134912,17086,1111507652,4294937932,4294967294,17086,1111507652,4294937932,4294967294,17086,1111507652,4294937932,4294967294,
6150144,6150144,6150144,6193152,6193152,6193152,6236160,6236160,6236160,6279168,6279168,6279168,32768,35840,38912,43008,46080,49152,53248,56320,59392,63488,66560,69632,4294967294,14,30,22,22,19,192,1048526463,16780300,50465804,8064,36,320,1125122048,1125122048,1125122048,0,1098907648,1107296256,1161216,1161792,1162368,32768,35840,38912,6193152,6196224,6199296,17167,1111507730,4294922572,4294967294,17167,1111507730,4294922572,4294967294,17167,1111507730,4294922572,4294967294,
1204224,1247232,8128512,1290240,1333248,8171520,1376256,1419264,8214528,1419264,1419264,8257536,49152,53248,32768,57344,61440,36864,65536,69632,40960,69632,69632,45056,4294967294,4294967294,110,21,21,19,192,1048526591,16780300,50465804,8064,36,128,1106247680,1128005632,1128136704,0,1121976320,1121976320,241920,1520064,1536192,53248,24576,32768,1290240,8107008,8193024,16864,1099973124,4294965260,4294967294,1121862453,1124090688,4294943468,110,1121862453,1124090688,4294943468,110,
10560,10560,10560,10752,10752,10752,10944,10944,10944,11136,11136,11136,32768,35840,38912,43008,46080,49152,53248,56320,59392,63488,66560,69632,4294967294,14,30,22,22,19,43008,1056915071,50465804,16780300,36,8064,320,1113587712,1113587712,1113587712,0,1098907648,1107296256,2016,131040,260064,32768,35840,38912,10752,698880,1387008,16988,1111507560,4294950732,4294967294,16988,1111507560,4294950732,4294967294,16988,1111507560,4294950732,4294967294,
602112,645120,688128,731136,774144,817152,860160,903168,946176,989184,989184,989184,32768,36864,40960,45056,49152,53248,57344,61440,65536,69632,69632,69632,14,14,14,21,21,21,192,1056915199,16780300,50465804,8064,36,128,1098907648,1099956224,1101004800,1098907648,1098907648,1098907648,129600,145728,161856,36864,45056,53248,691200,777216,863232,1096827232,1107837368,4294966444,14,1096827232,1107837368,4294966444,14,1096827232,1107837368,4294966444,14,
86016,129024,8988672,172032,215040,9031680,258048,301056,9074688,301056,301056,9117696,49152,53248,32768,57344,61440,36864,65536,69632,40960,69632,69632,45056,30,30,174,21,21,19,192,1056915199,16780300,50465804,8064,36,128,1082130432,1129316352,1129447424,1107296256,1127219200,1127219200,33408,1683648,1699776,53248,24576,32768,178176,8979456,9065472,1106264064,1112031456,1100,30,1127170889,1128285012,4294940524,174,1127170889,1128285012,4294940524,174,
37440,37440,37440,37632,37632,37632,37824,37824,37824,38016,38016,38016,32768,35840,38912,43008,46080,49152,53248,56320,59392,63488,66560,69632,4294967294,14,30,22,22,19,43008,1056915071,50465804,16780300,36,8064,320,1128529920,1128529920,1128529920,0,1098907648,1107296256,7056,136080,265104,32768,35840,38912,37632,725760,1413888,17219,1111507782,4294905932,4294967294,17219,1111507782,4294905932,4294967294,17219,1111507782,4294905932,4294967294,
0,0,0,0,0,0,0,0,0,0,0,0,0,0,0,0,0,0,0,0,0,0,0,0,0,0,0,0,0,0,0,1040137983,0,0,0,0,0,0,0,0,0,0,0,0,0,0,0,0,0,0,0,0,0,0,0,0,0,0,0,0,0,0,0,0,
4171776,4171776,4171776,4214784,4214784,4214784,4257792,4257792,4257792,4300800,4300800,4300800,32768,35840,38912,43008,46080,49152,53248,56320,59392,63488,66560,69632,4294967294,14,30,22,22,19,192,1048526335,16780300,50465804,8064,36,320,1120141312,1120141312,1120141312,0,1098907648,1107296256,790272,790848,791424,32768,35840,38912,4214784,4217856,4220928,17090,1111507656,4294937292,4294967294,17090,1111507656,4294937292,4294967294,17090,1111507656,4294937292,4294967294,
0,0,0,0,0,0,0,0,0,0,0,0,0,0,0,0,0,0,0,0,0,0,0,0,0,0,0,0,0,0,0,1048526463,0,0,0,0,0,0,0,0,0,0,0,0,0,0,0,0,0,0,0,0,0,0,0,0,0,0,0,0,0,0,0,0,
8042496,8042496,8042496,8085504,8085504,8085504,8128512,8128512,8128512,8171520,8171520,8171520,32768,35840,38912,43008,46080,49152,53248,56320,59392,63488,66560,69632,4294967294,14,30,22,22,19,192,1048526591,16780300,50465804,8064,36,320,1128005632,1128005632,1128005632,0,1098907648,1107296256,1516032,1516608,1517184,32768,35840,38912,8085504,8088576,8091648,17211,1111507774,4294908492,4294967294,17211,1111507774,4294908492,4294967294,17211,1111507774,4294908492,4294967294,
0,0,0,0,0,0,0,0,0,0,0,0,0,0,0,0,0,0,0,0,0,0,0,0,0,0,0,0,0,0,0,1056915071,0,0,0,0,0,0,0,0,0,0,0,0,0,0,0,0,0,0,0,0,0,0,0,0,0,0,0,0,0,0,0,0,
20160,20160,20160,20352,20352,20352,20544,20544,20544,20736,20736,20736,32768,35840,38912,43008,46080,49152,53248,56320,59392,63488,66560,69632,4294967294,14,30,22,22,19,43008,1056915199,50465804,16780300,36,8064,320,1121189888,1121189888,1121189888,0,1098907648,1107296256,3816,132840,261864,32768,35840,38912,20352,708480,1396608,17106,1111507672,4294934732,4294967294,17106,1111507672,4294934732,4294967294,17106,1111507672,4294934732,4294967294,
28608,28608,28608,28800,28800,28800,28992,28992,28992,29184,29184,29184,32768,35840,38912,43008,46080,49152,53248,56320,59392,63488,66560,69632,4294967294,14,30,22,22,19,43008,1056915199,50465804,16780300,36,8064,320,1125515264,1125515264,1125515264,0,1098907648,1107296256,5400,134424,263448,32768,35840,38912,28800,716928,1405056,17173,1111507736,4294920652,4294967294,17173,1111507736,4294920652,4294967294,17173,1111507736,4294920652,4294967294,
37824,37824,37824,38016,38016,38016,38208,38208,38208,38400,38400,38400,32768,35840,38912,43008,46080,49152,53248,56320,59392,63488,66560,69632,4294967294,14,30,22,22,19,43008,1056915071,50465804,16780300,36,8064,320,1128660992,1128660992,1128660992,0,1098907648,1107296256,7128,136152,265176,32768,35840,38912,38016,726144,1414272,17221,1111507784,4294905292,4294967294,17221,1111507784,4294905292,4294967294,17221,1111507784,4294905292,4294967294,
2279424,2279424,2279424,2322432,2322432,2322432,2365440,2365440,2365440,2408448,2408448,2408448,32768,35840,38912,43008,46080,49152,53248,56320,59392,63488,66560,69632,4294967294,14,30,22,22,19,192,1040137983,16780300,50465804,8064,36,320,1113063424,1113063424,1113063424,0,1098907648,1107296256,435456,436032,436608,32768,35840,38912,2322432,2325504,2328576,16980,1111507552,4294951372,4294967294,16980,1111507552,4294951372,4294967294,16980,1111507552,4294951372,4294967294,
4257792,4257792,4257792,4300800,4300800,4300800,4343808,4343808,4343808,4386816,4386816,4386816,32768,35840,38912,43008,46080,49152,53248,56320,59392,63488,66560,69632,4294967294,14,30,22,22,19,192,1048526335,16780300,50465804,8064,36,320,1120403456,1120403456,1120403456,0,1098907648,1107296256,806400,806976,807552,32768,35840,38912,4300800,4303872,4306944,17094,1111507660,4294936652,4294967294,17094,1111507660,4294936652,4294967294,17094,1111507660,4294936652,4294967294,
6236160,6236160,6236160,6279168,6279168,6279168,6322176,6322176,6322176,6365184,6365184,6365184,32768,35840,38912,43008,46080,49152,53248,56320,59392,63488,66560,69632,4294967294,14,30,22,22,19,192,1048526463,16780300,50465804,8064,36,320,1125253120,1125253120,1125253120,0,1098907648,1107296256,1177344,1177920,1178496,32768,35840,38912,6279168,6282240,6285312,17169,1111507732,4294921932,4294967294,17169,1111507732,4294921932,4294967294,17169,1111507732,4294921932,4294967294,
0,0,0,0,0,0,0,0,0,0,0,0,0,0,0,0,0,0,0,0,0,0,0,0,0,0,0,0,0,0,0,1048526591,0,0,0,0,0,0,0,0,0,0,0,0,0,0,0,0,0,0,0,0,0,0,0,0,0,0,0,0,0,0,0,0,
10944,10944,10944,11136,11136,11136,11328,11328,11328,11520,11520,11520,32768,35840,38912,43008,46080,49152,53248,56320,59392,63488,66560,69632,4294967294,14,30,22,22,19,43008,1056915071,50465804,16780300,36,8064,320,1114112000,1114112000,1114112000,0,1098907648,1107296256,2088,131112,260136,32768,35840,38912,11136,699264,1387392,16996,1111507568,4294950092,4294967294,16996,1111507568,4294950092,4294967294,16996,1111507568,4294950092,4294967294,
20544,20544,20544,20736,20736,20736,20928,20928,20928,21120,21120,21120,32768,35840,38912,43008,46080,49152,53248,56320,59392,63488,66560,69632,4294967294,14,30,22,22,19,43008,1056915199,50465804,16780300,36,8064,320,1121452032,1121452032,1121452032,0,1098907648,1107296256,3888,132912,261936,32768,35840,38912,20736,708864,1396992,17110,1111507676,4294934092,4294967294,17110,1111507676,4294934092,4294967294,17110,1111507676,4294934092,4294967294,
28992,28992,28992,29184,29184,29184,29376,29376,29376,29568,29568,29568,32768,35840,38912,43008,46080,49152,53248,56320,59392,63488,66560,69632,4294967294,14,30,22,22,19,43008,1056915199,50465804,16780300,36,8064,320,1125646336,1125646336,1125646336,0,1098907648,1107296256,5472,134496,263520,32768,35840,38912,29184,717312,1405440,17175,1111507738,4294920012,4294967294,17175,1111507738,4294920012,4294967294,17175,1111507738,4294920012,4294967294,
38208,38208,38208,38400,38400,38400,38592,38592,38592,38784,38784,38784,32768,35840,38912,43008,46080,49152,53248,56320,59392,63488,66560,69632,4294967294,14,30,22,22,19,43008,1056915071,50465804,16780300,36,8064,320,1128792064,1128792064,1128792064,0,1098907648,1107296256,7200,136224,265248,32768,35840,38912,38400,726528,1414656,17223,1111507786,4294904652,4294967294,17223,1111507786,4294904652,4294967294,17223,1111507786,4294904652,4294967294,
2365440,2365440,2365440,2408448,2408448,2408448,2451456,2451456,2451456,2494464,2494464,2494464,32768,35840,38912,43008,46080,49152,53248,56320,59392,63488,66560,69632,4294967294,14,30,22,22,19,192,1040137983,16780300,50465804,8064,36,320,1113587712,1113587712,1113587712,0,1098907648,1107296256,451584,452160,452736,32768,35840,38912,2408448,2411520,2414592,16988,1111507560,4294950732,4294967294,16988,1111507560,4294950732,4294967294,16988,1111507560,4294950732,4294967294,
430080,473088,2881536,516096,559104,2924544,602112,645120,2967552,645120,645120,3010560,49152,53248,32768,57344,61440,36864,65536,69632,40960,69632,69632,45056,4294967294,4294967294,78,21,21,19,192,1048526335,16780300,50465804,8064,36,128,1094713344,1115947008,1116209152,0,1117782016,1117782016,96768,535104,551232,53248,24576,32768,516096,2853888,2939904,16672,1099972976,268,4294967294,1117667948,1119896204,4294959276,78,1117667948,1119896204,4294959276,78,
860160,903168,5806080,946176,989184,5849088,1032192,1075200,5892096,1075200,1075200,5935104,49152,53248,32768,57344,61440,36864,65536,69632,40960,69632,69632,45056,4294967294,4294967294,94,21,21,19,192,1048526463,16780300,50465804,8064,36,128,1102053376,1124466688,1124597760,0,1119879168,1119879168,177408,1084032,1100160,53248,24576,32768,946176,5781504,5867520,16800,1099973064,4294966284,4294967294,1119765246,1121993482,4294950476,94,1119765246,1121993482,4294950476,94,
8128512,8128512,8128512,8171520,8171520,8171520,8214528,8214528,8214528,8257536,8257536,8257536,32768,35840,38912,43008,46080,49152,53248,56320,59392,63488,66560,69632,4294967294,14,30,22,22,19,192,1048526591,16780300,50465804,8064,36,320,1128136704,1128136704,1128136704,0,1098907648,1107296256,1532160,1532736,1533312,32768,35840,38912,8171520,8174592,8177664,17213,1111507776,4294907852,4294967294,17213,1111507776,4294907852,4294967294,17213,1111507776,4294907852,4294967294,
11328,11328,11328,11520,11520,11520,11712,11712,11712,11904,11904,11904,32768,35840,38912,43008,46080,49152,53248,56320,59392,63488,66560,69632,4294967294,14,30,22,22,19,43008,1056915071,50465804,16780300,36,8064,320,1114636288,1114636288,1114636288,0,1098907648,1107296256,2160,131184,260208,32768,35840,38912,11520,699648,1387776,17004,1111507576,4294949452,4294967294,17004,1111507576,4294949452,4294967294,17004,1111507576,4294949452,4294967294,
20928,20928,20928,21120,21120,21120,21312,21312,21312,21504,21504,21504,32768,35840,38912,43008,46080,49152,53248,56320,59392,63488,66560,69632,4294967294,14,30,22,22,19,43008,1056915199,50465804,16780300,36,8064,320,1121714176,1121714176,1121714176,0,1098907648,1107296256,3960,132984,262008,32768,35840,38912,21120,709248,1397376,17114,1111507680,4294933452,4294967294,17114,1111507680,4294933452,4294967294,17114,1111507680,4294933452,4294967294,
29376,29376,29376,29568,29568,29568,29760,29760,29760,29952,29952,29952,32768,35840,38912,43008,46080,49152,53248,56320,59392,63488,66560,69632,4294967294,14,30,22,22,19,43008,1056914943,50465804,16780300,36,8064,320,1125777408,1125777408,1125777408,0,1098907648,1107296256,5544,134568,263592,32768,35840,38912,29568,717696,1405824,17177,1111507740,4294919372,4294967294,17177,1111507740,4294919372,4294967294,17177,1111507740,4294919372,4294967294,
0,0,0,0,0,0,0,0,0,0,0,0,0,0,0,0,0,0,0,0,0,0,0,0,0,0,0,0,0,0,0,1056915071,0,0,0,0,0,0,0,0,0,0,0,0,0,0,0,0,0,0,0,0,0,0,0,0,0,0,0,0,0,0,0,0,
2451456,2451456,2451456,2494464,2494464,2494464,2537472,2537472,2537472,2580480,2580480,2580480,32768,35840,38912,43008,46080,49152,53248,56320,59392,63488,66560,69632,4294967294,14,30,22,22,19,192,1040137983,16780300,50465804,8064,36,320,1114112000,1114112000,1114112000,0,1098907648,1107296256,467712,468288,468864,32768,35840,38912,2494464,2497536,2500608,16996,1111507568,4294950092,4294967294,16996,1111507568,4294950092,4294967294,16996,1111507568,4294950092,4294967294,
4343808,4343808,4343808,4386816,4386816,4386816,4429824,4429824,4429824,4472832,4472832,4472832,32768,35840,38912,43008,46080,49152,53248,56320,59392,63488,66560,69632,4294967294,14,30,22,22,19,192,1048526463,16780300,50465804,8064,36,320,1120665600,1120665600,1120665600,0,1098907648,1107296256,822528,823104,823680,32768,35840,38912,4386816,4389888,4392960,17098,1111507664,4294936012,4294967294,17098,1111507664,4294936012,4294967294,17098,1111507664,4294936012,4294967294,
6322176,6322176,6322176,6365184,6365184,6365184,6408192,6408192,6408192,6451200,6451200,6451200,32768,35840,38912,43008,46080,49152,53248,56320,59392,63488,66560,69632,4294967294,14,30,22,22,19,192,1048526591,16780300,50465804,8064,36,320,1125384192,1125384192,1125384192,0,1098907648,1107296256,1193472,1194048,1194624,32768,35840,38912,6365184,6368256,6371328,17171,1111507734,4294921292,4294967294,17171,1111507734,4294921292,4294967294,17171,1111507734,4294921292,4294967294,
8214528,8214528,8214528,8257536,8257536,8257536,8300544,8300544,8300544,8343552,8343552,8343552,32768,35840,38912,43008,46080,49152,53248,56320,59392,63488,66560,69632,4294967294,14,30,22,22,19,192,1048526591,16780300,50465804,8064,36,320,1128267776,1128267776,1128267776,0,1098907648,1107296256,1548288,1548864,1549440,32768,35840,38912,8257536,8260608,8263680,17215,1111507778,4294907212,4294967294,17215,1111507778,4294907212,4294967294,17215,1111507778,4294907212,4294967294,
11712,11712,11712,11904,11904,11904,12096,12096,12096,12288,12288,12288,32768,35840,38912,43008,46080,49152,53248,56320,59392,63488,66560,69632,4294967294,14,30,22,22,19,43008,1056915071,50465804,16780300,36,8064,320,1115160576,1115160576,1115160576,0,1098907648,1107296256,2232,131256,260280,32768,35840,38912,11904,700032,1388160,17012,1111507584,4294948812,4294967294,17012,1111507584,4294948812,4294967294,17012,1111507584,4294948812,4294967294,
21312,21312,21312,21504,21504,21504,21696,21696,21696,21888,21888,21888,32768,35840,38912,43008,46080,49152,53248,56320,59392,63488,66560,69632,4294967294,14,30,22,22,19,43008,1056915199,50465804,16780300,36,8064,320,1121976320,1121976320,1121976320,0,1098907648,1107296256,4032,133056,262080,32768,35840,38912,21504,709632,1397760,17118,1111507684,4294932812,4294967294,17118,1111507684,4294932812,4294967294,17118,1111507684,4294932812,4294967294,
29760,29760,29760,29952,29952,29952,30144,30144,30144,30336,30336,30336,32768,35840,38912,43008,46080,49152,53248,56320,59392,63488,66560,69632,4294967294,14,30,22,22,19,43008,1056914943,50465804,16780300,36,8064,320,1125908480,1125908480,1125908480,0,1098907648,1107296256,5616,134640,263664,32768,35840,38912,29952,718080,1406208,17179,1111507742,4294918732,4294967294,17179,1111507742,4294918732,4294967294,17179,1111507742,4294918732,4294967294,
38592,38592,38592,38784,38784,38784,38976,38976,38976,39168,39168,39168,32768,35840,38912,43008,46080,49152,53248,56320,59392,63488,66560,69632,4294967294,14,30,22,22,19,43008,1056915071,50465804,16780300,36,8064,320,1128923136,1128923136,1128923136,0,1098907648,1107296256,7272,136296,265320,32768,35840,38912,38784,726912,1415040,17225,1111507788,4294904012,4294967294,17225,1111507788,4294904012,4294967294,17225,1111507788,4294904012,4294967294,
86016,129024,8214528,172032,215040,8257536,258048,301056,8257536,301056,301056,8257536,40960,45056,32768,49152,53248,36864,57344,61440,36864,61440,61440,36864,4294967294,4294967294,46,21,21,19,192,1040137983,16780300,50465804,8064,36,128,1128005632,1082130432,1128136704,1111490560,0,1111490560,1517760,32256,1533888,16384,45056,24576,1073741824,172032,8180736,1111245623,1115702080,4294943596,46,16384,1099972832,1036,4294967294,1111245623,1115702080,4294943596,46,
4429824,4429824,4429824,4472832,4472832,4472832,4515840,4515840,4515840,4558848,4558848,4558848,32768,35840,38912,43008,46080,49152,53248,56320,59392,63488,66560,69632,4294967294,14,30,22,22,19,192,1048526463,16780300,50465804,8064,36,320,1120927744,1120927744,1120927744,0,1098907648,1107296256,838656,839232,839808,32768,35840,38912,4472832,4475904,4478976,17102,1111507668,4294935372,4294967294,17102,1111507668,4294935372,4294967294,17102,1111507668,4294935372,4294967294,
6408192,6408192,6408192,6451200,6451200,6451200,6494208,6494208,6494208,6537216,6537216,6537216,32768,35840,38912,43008,46080,49152,53248,56320,59392,63488,66560,69632,4294967294,14,30,22,22,19,192,1048526591,16780300,50465804,8064,36,320,1125515264,1125515264,1125515264,0,1098907648,1107296256,1209600,1210176,1210752,32768,35840,38912,6451200,6454272,6457344,17173,1111507736,4294920652,4294967294,17173,1111507736,4294920652,4294967294,17173,1111507736,4294920652,4294967294,
8300544,8300544,8300544,8343552,8343552,8343552,8386560,8386560,8386560,8429568,8429568,8429568,32768,35840,38912,43008,46080,49152,53248,56320,59392,63488,66560,69632,4294967294,14,30,22,22,19,192,1048526335,16780300,50465804,8064,36,320,1128398848,1128398848,1128398848,0,1098907648,1107296256,1564416,1564992,1565568,32768,35840,38912,8343552,8346624,8349696,17217,1111507780,4294906572,4294967294,17217,1111507780,4294906572,4294967294,17217,1111507780,4294906572,4294967294,
12096,12096,12096,12288,12288,12288,12480,12480,12480,12672,12672,12672,32768,35840,38912,43008,46080,49152,53248,56320,59392,63488,66560,69632,4294967294,14,30,22,22,19,43008,1056915071,50465804,16780300,36,8064,320,1115684864,1115684864,1115684864,0,1098907648,1107296256,2304,131328,260352,32768,35840,38912,12288,700416,1388544,17020,1111507588,4294948172,4294967294,17020,1111507588,4294948172,4294967294,17020,1111507588,4294948172,4294967294,
21696,21696,21696,21888,21888,21888,22080,22080,22080,22272,22272,22272,32768,35840,38912,43008,46080,49152,53248,56320,59392,63488,66560,69632,4294967294,14,30,22,22,19,43008,1056915199,50465804,16780300,36,8064,320,1122238464,1122238464,1122238464,0,1098907648,1107296256,4104,133128,262152,32768,35840,38912,21888,710016,1398144,17122,1111507688,4294932172,4294967294,17122,1111507688,4294932172,4294967294,17122,1111507688,4294932172,4294967294,
30144,30144,30144,30336,30336,30336,30528,30528,30528,30720,30720,30720,32768,35840,38912,43008,46080,49152,53248,56320,59392,63488,66560,69632,4294967294,14,30,22,22,19,43008,1056914943,50465804,16780300,36,8064,320,1126039552,1126039552,1126039552,0,1098907648,1107296256,5688,134712,263736,32768,35840,38912,30336,718464,1406592,17181,1111507744,4294918092,4294967294,17181,1111507744,4294918092,4294967294,17181,1111507744,4294918092,4294967294,
38976,38976,38976,39168,39168,39168,39360,39360,39360,39552,39552,39552,32768,35840,38912,43008,46080,49152,53248,56320,59392,63488,66560,69632,4294967294,14,30,22,22,19,43008,1056915071,50465804,16780300,36,8064,320,1129054208,1129054208,1129054208,0,1098907648,1107296256,7344,136368,265392,32768,35840,38912,39168,727296,1415424,17227,1111507790,4294903372,4294967294,17227,1111507790,4294903372,4294967294,17227,1111507790,4294903372,4294967294,
2537472,2537472,2537472,2580480,2580480,2580480,2623488,2623488,2623488,2666496,2666496,2666496,32768,35840,38912,43008,46080,49152,53248,56320,59392,63488,66560,69632,4294967294,14,30,22,22,19,192,1040137983,16780300,50465804,8064,36,320,1114636288,1114636288,1114636288,0,1098907648,1107296256,483840,484416,484992,32768,35840,38912,2580480,2583552,2586624,17004,1111507576,4294949452,4294967294,17004,1111507576,4294949452,4294967294,17004,1111507576,4294949452,4294967294,
0,0,0,0,0,0,0,0,0,0,0,0,0,0,0,0,0,0,0,0,0,0,0,0,0,0,0,0,0,0,0,1048526463,0,0,0,0,0,0,0,0,0,0,0,0,0,0,0,0,0,0,0,0,0,0,0,0,0,0,0,0,0,0,0,0,
0,0,0,0,0,0,0,0,0,0,0,0,0,0,0,0,0,0,0,0,0,0,0,0,0,0,0,0,0,0,0,1048526591,0,0,0,0,0,0,0,0,0,0,0,0,0,0,0,0,0,0,0,0,0,0,0,0,0,0,0,0,0,0,0,0,
8386560,8386560,8386560,8429568,8429568,8429568,8472576,8472576,8472576,8515584,8515584,8515584,32768,35840,38912,43008,46080,49152,53248,56320,59392,63488,66560,69632,4294967294,14,30,22,22,19,192,1048526335,16780300,50465804,8064,36,320,1128529920,1128529920,1128529920,0,1098907648,1107296256,1580544,1581120,1581696,32768,35840,38912,8429568,8432640,8435712,17219,1111507782,4294905932,4294967294,17219,1111507782,4294905932,4294967294,17219,1111507782,4294905932,4294967294,
12480,12480,12480,12672,12672,12672,12864,12864,12864,13056,13056,13056,32768,35840,38912,43008,46080,49152,53248,56320,59392,63488,66560,69632,4294967294,14,30,22,22,19,43008,1056915071,50465804,16780300,36,8064,320,1115947008,1115947008,1115947008,0,1098907648,1107296256,2376,131400,260424,32768,35840,38912,12672,700800,1388928,17026,1111507592,4294947532,4294967294,17026,1111507592,4294947532,4294967294,17026,1111507592,4294947532,4294967294,
22080,22080,22080,22272,22272,22272,22464,22464,22464,22656,22656,22656,32768,35840,38912,43008,46080,49152,53248,56320,59392,63488,66560,69632,4294967294,14,30,22,22,19,43008,1056915199,50465804,16780300,36,8064,320,1122500608,1122500608,1122500608,0,1098907648,1107296256,4176,133200,262224,32768,35840,38912,22272,710400,1398528,17126,1111507692,4294931532,4294967294,17126,1111507692,4294931532,4294967294,17126,1111507692,4294931532,4294967294,
0,0,0,0,0,0,0,0,0,0,0,0,0,0,0,0,0,0,0,0,0,0,0,0,0,0,0,0,0,0,0,1056915071,0,0,0,0,0,0,0,0,0,0,0,0,0,0,0,0,0,0,0,0,0,0,0,0,0,0,0,0,0,0,0,0,
860160,903168,946176,989184,1032192,1075200,1118208,1161216,1204224,1247232,1247232,1247232,32768,36864,40960,45056,49152,53248,57344,61440,65536,69632,69632,69632,30,30,30,21,21,21,192,1056915199,16780300,50465804,8064,36,128,1102053376,1103101952,1104150528,1107296256,1107296256,1107296256,178560,194688,210816,36864,45056,53248,952320,1038336,1124352,1106264480,1112031720,4294965580,30,1106264480,1112031720,4294965580,30,1106264480,1112031720,4294965580,30,
0,0,0,0,0,0,0,0,0,0,0,0,0,0,0,0,0,0,0,0,0,0,0,0,0,0,0,0,0,0,0,1040137983,0,0,0,0,0,0,0,0,0,0,0,0,0,0,0,0,0,0,0,0,0,0,0,0,0,0,0,0,0,0,0,0,
4515840,4515840,4515840,4558848,4558848,4558848,4601856,4601856,4601856,4644864,4644864,4644864,32768,35840,38912,43008,46080,49152,53248,56320,59392,63488,66560,69632,4294967294,14,30,22,22,19,192,1048526463,16780300,50465804,8064,36,320,1121189888,1121189888,1121189888,0,1098907648,1107296256,854784,855360,855936,32768,35840,38912,4558848,4561920,4564992,17106,1111507672,4294934732,4294967294,17106,1111507672,4294934732,4294967294,17106,1111507672,4294934732,4294967294,
6494208,6494208,6494208,6537216,6537216,6537216,6580224,6580224,6580224,6623232,6623232,6623232,32768,35840,38912,43008,46080,49152,53248,56320,59392,63488,66560,69632,4294967294,14,30,22,22,19,192,1048526591,16780300,50465804,8064,36,320,1125646336,1125646336,1125646336,0,1098907648,1107296256,1225728,1226304,1226880,32768,35840,38912,6537216,6540288,6543360,17175,1111507738,4294920012,4294967294,17175,1111507738,4294920012,4294967294,17175,1111507738,4294920012,4294967294,
0,0,0,0,0,0,0,0,0,0,0,0,0,0,0,0,0,0,0,0,0,0,0,0,0,0,0,0,0,0,0,1048526335,0,0,0,0,0,0,0,0,0,0,0,0,0,0,0,0,0,0,0,0,0,0,0,0,0,0,0,0,0,0,0,0,
172032,215040,5978112,258048,301056,6021120,344064,387072,6064128,387072,387072,6107136,49152,53248,32768,57344,61440,36864,65536,69632,40960,69632,69632,45056,14,14,142,21,21,19,192,1056915199,16780300,50465804,8064,36,128,1086324736,1124728832,1124859904,1098907648,1125122048,1125122048,48960,1118016,1134144,53248,24576,32768,261120,5962752,6048768,1096827008,1107837200,940,14,1125073667,1126187790,4294949676,142,1125073667,1126187790,4294949676,142,
22464,22464,22464,22656,22656,22656,22848,22848,22848,23040,23040,23040,32768,35840,38912,43008,46080,49152,53248,56320,59392,63488,66560,69632,4294967294,14,30,22,22,19,43008,1056915199,50465804,16780300,36,8064,320,1122762752,1122762752,1122762752,0,1098907648,1107296256,4248,133272,262296,32768,35840,38912,22656,710784,1398912,17130,1111507696,4294930892,4294967294,17130,1111507696,4294930892,4294967294,17130,1111507696,4294930892,4294967294,
172032,215040,3741696,258048,301056,3784704,344064,387072,3827712,387072,387072,3870720,49152,53248,32768,57344,61440,36864,65536,69632,40960,69632,69632,45056,30,30,190,21,21,19,192,1056915071,16780300,50465804,8064,36,128,1086324736,1118568448,1118830592,1107296256,1128267776,1128267776,49536,700416,716544,53248,24576,32768,264192,3735552,3821568,1106264192,1112031504,844,30,1128219294,1129333428,4294956044,190,1128219294,1129333428,4294956044,190,
39360,39360,39360,39552,39552,39552,39744,39744,39744,39936,39936,39936,32768,35840,38912,43008,46080,49152,53248,56320,59392,63488,66560,69632,4294967294,14,30,22,22,19,43008,1056915199,50465804,16780300,36,8064,320,1129185280,1129185280,1129185280,0,1098907648,1107296256,7416,136440,265464,32768,35840,38912,39552,727680,1415808,17229,1111507792,4294902732,4294967294,17229,1111507792,4294902732,4294967294,17229,1111507792,4294902732,4294967294,
2623488,2623488,2623488,2666496,2666496,2666496,2709504,2709504,2709504,2752512,2752512,2752512,32768,35840,38912,43008,46080,49152,53248,56320,59392,63488,66560,69632,4294967294,14,30,22,22,19,192,1040137983,16780300,50465804,8064,36,320,1115160576,1115160576,1115160576,0,1098907648,1107296256,499968,500544,501120,32768,35840,38912,2666496,2669568,2672640,17012,1111507584,4294948812,4294967294,17012,1111507584,4294948812,4294967294,17012,1111507584,4294948812,4294967294,
4601856,4601856,4601856,4644864,4644864,4644864,4687872,4687872,4687872,4730880,4730880,4730880,32768,35840,38912,43008,46080,49152,53248,56320,59392,63488,66560,69632,4294967294,14,30,22,22,19,192,1048526463,16780300,50465804,8064,36,320,1121452032,1121452032,1121452032,0,1098907648,1107296256,870912,871488,872064,32768,35840,38912,4644864,4647936,4651008,17110,1111507676,4294934092,4294967294,17110,1111507676,4294934092,4294967294,17110,1111507676,4294934092,4294967294,
6580224,6580224,6580224,6623232,6623232,6623232,6666240,6666240,6666240,6709248,6709248,6709248,32768,35840,38912,43008,46080,49152,53248,56320,59392,63488,66560,69632,4294967294,14,30,22,22,19,192,1048526591,16780300,50465804,8064,36,320,1125777408,1125777408,1125777408,0,1098907648,1107296256,1241856,1242432,1243008,32768,35840,38912,6623232,6626304,6629376,17177,1111507740,4294919372,4294967294,17177,1111507740,4294919372,4294967294,17177,1111507740,4294919372,4294967294,
1290240,1333248,3483648,1376256,1419264,3526656,1462272,1505280,3569664,1505280,1505280,3612672,49152,53248,32768,57344,61440,36864,65536,69632,40960,69632,69632,45056,4294967294,4294967294,126,21,21,19,192,1048526463,16780300,50465804,8064,36,128,1107296256,1117782016,1118044160,0,1124073472,1124073472,258048,649728,665856,53248,24576,32768,1376256,3465216,3551232,16880,1099973132,4294965004,4294967294,1123959442,1125139112,4294957196,126,1123959442,1125139112,4294957196,126,
12864,12864,12864,13056,13056,13056,13248,13248,13248,13440,13440,13440,32768,35840,38912,43008,46080,49152,53248,56320,59392,63488,66560,69632,4294967294,14,30,22,22,19,43008,1056915199,50465804,16780300,36,8064,320,1116209152,1116209152,1116209152,0,1098907648,1107296256,2448,131472,260496,32768,35840,38912,13056,701184,1389312,17030,1111507596,4294946892,4294967294,17030,1111507596,4294946892,4294967294,17030,1111507596,4294946892,4294967294,
0,0,0,0,0,0,0,0,0,0,0,0,0,0,0,0,0,0,0,0,0,0,0,0,0,0,0,0,0,0,0,1056915199,0,0,0,0,0,0,0,0,0,0,0,0,0,0,0,0,0,0,0,0,0,0,0,0,0,0,0,0,0,0,0,0,
30528,30528,30528,30720,30720,30720,30912,30912,30912,31104,31104,31104,32768,35840,38912,43008,46080,49152,53248,56320,59392,63488,66560,69632,4294967294,14,30,22,22,19,43008,1056915071,50465804,16780300,36,8064,320,1126170624,1126170624,1126170624,0,1098907648,1107296256,5760,134784,263808,32768,35840,38912,30720,718848,1406976,17183,1111507746,4294917452,4294967294,17183,1111507746,4294917452,4294967294,17183,1111507746,4294917452,4294967294,
39744,39744,39744,39936,39936,39936,40128,40128,40128,40320,40320,40320,32768,35840,38912,43008,46080,49152,53248,56320,59392,63488,66560,69632,4294967294,14,30,22,22,19,43008,1056915199,50465804,16780300,36,8064,320,1129316352,1129316352,1129316352,0,1098907648,1107296256,7488,136512,265536,32768,35840,38912,39936,728064,1416192,17231,1111507794,4294902092,4294967294,17231,1111507794,4294902092,4294967294,17231,1111507794,4294902092,4294967294,
2709504,2709504,2709504,2752512,2752512,2752512,2795520,2795520,2795520,2838528,2838528,2838528,32768,35840,38912,43008,46080,49152,53248,56320,59392,63488,66560,69632,4294967294,14,30,22,22,19,192,1040137983,16780300,50465804,8064,36,320,1115684864,1115684864,1115684864,0,1098907648,1107296256,516096,516672,517248,32768,35840,38912,2752512,2755584,2758656,17020,1111507588,4294948172,4294967294,17020,1111507588,4294948172,4294967294,17020,1111507588,4294948172,4294967294,
4687872,4687872,4687872,4730880,4730880,4730880,4773888,4773888,4773888,4816896,4816896,4816896,32768,35840,38912,43008,46080,49152,53248,56320,59392,63488,66560,69632,4294967294,14,30,22,22,19,192,1048526463,16780300,50465804,8064,36,320,1121714176,1121714176,1121714176,0,1098907648,1107296256,887040,887616,888192,32768,35840,38912,4730880,4733952,4737024,17114,1111507680,4294933452,4294967294,17114,1111507680,4294933452,4294967294,17114,1111507680,4294933452,4294967294,
6666240,6666240,6666240,6709248,6709248,6709248,6752256,6752256,6752256,6795264,6795264,6795264,32768,35840,38912,43008,46080,49152,53248,56320,59392,63488,66560,69632,4294967294,14,30,22,22,19,192,1048526591,16780300,50465804,8064,36,320,1125908480,1125908480,1125908480,0,1098907648,1107296256,1257984,1258560,1259136,32768,35840,38912,6709248,6712320,6715392,17179,1111507742,4294918732,4294967294,17179,1111507742,4294918732,4294967294,17179,1111507742,4294918732,4294967294,
8472576,8472576,8472576,8515584,8515584,8515584,8558592,8558592,8558592,8601600,8601600,8601600,32768,35840,38912,43008,46080,49152,53248,56320,59392,63488,66560,69632,4294967294,14,30,22,22,19,192,1048526463,16780300,50465804,8064,36,320,1128660992,1128660992,1128660992,0,1098907648,1107296256,1596672,1597248,1597824,32768,35840,38912,8515584,8518656,8521728,17221,1111507784,4294905292,4294967294,17221,1111507784,4294905292,4294967294,17221,1111507784,4294905292,4294967294,
13248,13248,13248,13440,13440,13440,13632,13632,13632,13824,13824,13824,32768,35840,38912,43008,46080,49152,53248,56320,59392,63488,66560,69632,4294967294,14,30,22,22,19,43008,1056915199,50465804,16780300,36,8064,320,1116471296,1116471296,1116471296,0,1098907648,1107296256,2520,131544,260568,32768,35840,38912,13440,701568,1389696,17034,1111507600,4294946252,4294967294,17034,1111507600,4294946252,4294967294,17034,1111507600,4294946252,4294967294,
860160,903168,946176,989184,1032192,1075200,1118208,1161216,1204224,1247232,1247232,1247232,32768,36864,40960,45056,49152,53248,57344,61440,65536,69632,69632,69632,14,14,14,21,21,21,192,1056915199,16780300,50465804,8064,36,128,1102053376,1103101952,1104150528,1098907648,1098907648,1098907648,177984,194112,210240,36864,45056,53248,949248,1035264,1121280,1096827296,1107837416,4294965676,14,1096827296,1107837416,4294965676,14,1096827296,1107837416,4294965676,14,
30912,30912,30912,31104,31104,31104,31296,31296,31296,31488,31488,31488,32768,35840,38912,43008,46080,49152,53248,56320,59392,63488,66560,69632,4294967294,14,30,22,22,19,43008,1056915071,50465804,16780300,36,8064,320,1126301696,1126301696,1126301696,0,1098907648,1107296256,5832,134856,263880,32768,35840,38912,31104,719232,1407360,17185,1111507748,4294916812,4294967294,17185,1111507748,4294916812,4294967294,17185,1111507748,4294916812,4294967294,
40128,40128,40128,40320,40320,40320,40512,40512,40512,40704,40704,40704,32768,35840,38912,43008,46080,49152,53248,56320,59392,63488,66560,69632,4294967294,14,30,22,22,19,43008,1056915199,50465804,16780300,36,8064,320,1129447424,1129447424,1129447424,0,1098907648,1107296256,7560,136584,265608,32768,35840,38912,40320,728448,1416576,17233,1111507796,4294901452,4294967294,17233,1111507796,4294901452,4294967294,17233,1111507796,4294901452,4294967294,
2795520,2795520,2795520,2838528,2838528,2838528,2881536,2881536,2881536,2924544,2924544,2924544,32768,35840,38912,43008,46080,49152,53248,56320,59392,63488,66560,69632,4294967294,14,30,22,22,19,192,1040137727,16780300,50465804,8064,36,320,1115947008,1115947008,1115947008,0,1098907648,1107296256,532224,532800,533376,32768,35840,38912,2838528,2841600,2844672,17026,1111507592,4294947532,4294967294,17026,1111507592,4294947532,4294967294,17026,1111507592,4294947532,4294967294,
516096,559104,5203968,602112,645120,5246976,688128,731136,5289984,731136,731136,5332992,49152,53248,32768,57344,61440,36864,65536,69632,40960,69632,69632,45056,4294967294,4294967294,78,21,21,19,192,1048526463,16780300,50465804,8064,36,128,1096810496,1123024896,1123287040,0,1117782016,1117782016,112896,970560,986688,53248,24576,32768,602112,5176320,5262336,16704,1099973000,12,4294967294,1117668066,1119896312,4294952364,78,1117668066,1119896312,4294952364,78,
946176,989184,8128512,1032192,1075200,8171520,1118208,1161216,8214528,1161216,1161216,8257536,49152,53248,32768,57344,61440,36864,65536,69632,40960,69632,69632,45056,4294967294,4294967294,94,21,21,19,192,1048526591,16780300,50465804,8064,36,128,1103101952,1128005632,1128136704,0,1119879168,1119879168,193536,1519488,1535616,53248,24576,32768,1032192,8103936,8189952,16816,1099973080,4294966028,4294967294,1119765301,1121993536,4294943564,94,1119765301,1121993536,4294943564,94,
8558592,8558592,8558592,8601600,8601600,8601600,8644608,8644608,8644608,8687616,8687616,8687616,32768,35840,38912,43008,46080,49152,53248,56320,59392,63488,66560,69632,4294967294,14,30,22,22,19,192,1048526463,16780300,50465804,8064,36,320,1128792064,1128792064,1128792064,0,1098907648,1107296256,1612800,1613376,1613952,32768,35840,38912,8601600,8604672,8607744,17223,1111507786,4294904652,4294967294,17223,1111507786,4294904652,4294967294,17223,1111507786,4294904652,4294967294,
13632,13632,13632,13824,13824,13824,14016,14016,14016,14208,14208,14208,32768,35840,38912,43008,46080,49152,53248,56320,59392,63488,66560,69632,4294967294,14,30,22,22,19,43008,1056915199,50465804,16780300,36,8064,320,1116733440,1116733440,1116733440,0,1098907648,1107296256,2592,131616,260640,32768,35840,38912,13824,701952,1390080,17038,1111507604,4294945612,4294967294,17038,1111507604,4294945612,4294967294,17038,1111507604,4294945612,4294967294,
22848,22848,22848,23040,23040,23040,23232,23232,23232,23424,23424,23424,32768,35840,38912,43008,46080,49152,53248,56320,59392,63488,66560,69632,4294967294,14,30,22,22,19,43008,1056915199,50465804,16780300,36,8064,320,1123024896,1123024896,1123024896,0,1098907648,1107296256,4320,133344,262368,32768,35840,38912,23040,711168,1399296,17134,1111507700,4294930252,4294967294,17134,1111507700,4294930252,4294967294,17134,1111507700,4294930252,4294967294,
31296,31296,31296,31488,31488,31488,31680,31680,31680,31872,31872,31872,32768,35840,38912,43008,46080,49152,53248,56320,59392,63488,66560,69632,4294967294,14,30,22,22,19,43008,1056915071,50465804,16780300,36,8064,320,1126432768,1126432768,1126432768,0,1098907648,1107296256,5904,134928,263952,32768,35840,38912,31488,719616,1407744,17187,1111507750,4294916172,4294967294,17187,1111507750,4294916172,4294967294,17187,1111507750,4294916172,4294967294,
0,0,0,0,0,0,0,0,0,0,0,0,0,0,0,0,0,0,0,0,0,0,0,0,0,0,0,0,0,0,0,1056915199,0,0,0,0,0,0,0,0,0,0,0,0,0,0,0,0,0,0,0,0,0,0,0,0,0,0,0,0,0,0,0,0,
2881536,2881536,2881536,2924544,2924544,2924544,2967552,2967552,2967552,3010560,3010560,3010560,32768,35840,38912,43008,46080,49152,53248,56320,59392,63488,66560,69632,4294967294,14,30,22,22,19,192,1040137727,16780300,50465804,8064,36,320,1116209152,1116209152,1116209152,0,1098907648,1107296256,548352,548928,549504,32768,35840,38912,2924544,2927616,2930688,17030,1111507596,4294946892,4294967294,17030,1111507596,4294946892,4294967294,17030,1111507596,4294946892,4294967294,
4773888,4773888,4773888,4816896,4816896,4816896,4859904,4859904,4859904,4902912,4902912,4902912,32768,35840,38912,43008,46080,49152,53248,56320,59392,63488,66560,69632,4294967294,14,30,22,22,19,192,1048526463,16780300,50465804,8064,36,320,1121976320,1121976320,1121976320,0,1098907648,1107296256,903168,903744,904320,32768,35840,38912,4816896,4819968,4823040,17118,1111507684,4294932812,4294967294,17118,1111507684,4294932812,4294967294,17118,1111507684,4294932812,4294967294,
6752256,6752256,6752256,6795264,6795264,6795264,6838272,6838272,6838272,6881280,6881280,6881280,32768,35840,38912,43008,46080,49152,53248,56320,59392,63488,66560,69632,4294967294,14,30,22,22,19,192,1048526591,16780300,50465804,8064,36,320,1126039552,1126039552,1126039552,0,1098907648,1107296256,1274112,1274688,1275264,32768,35840,38912,6795264,6798336,6801408,17181,1111507744,4294918092,4294967294,17181,1111507744,4294918092,4294967294,17181,1111507744,4294918092,4294967294,
8644608,8644608,8644608,8687616,8687616,8687616,8730624,8730624,8730624,8773632,8773632,8773632,32768,35840,38912,43008,46080,49152,53248,56320,59392,63488,66560,69632,4294967294,14,30,22,22,19,192,1048526463,16780300,50465804,8064,36,320,1128923136,1128923136,1128923136,0,1098907648,1107296256,1628928,1629504,1630080,32768,35840,38912,8687616,8690688,8693760,17225,1111507788,4294904012,4294967294,17225,1111507788,4294904012,4294967294,17225,1111507788,4294904012,4294967294,
14016,14016,14016,14208,14208,14208,14400,14400,14400,14592,14592,14592,32768,35840,38912,43008,46080,49152,53248,56320,59392,63488,66560,69632,4294967294,14,30,22,22,19,43008,1056915199,50465804,16780300,36,8064,320,1116995584,1116995584,1116995584,0,1098907648,1107296256,2664,131688,260712,32768,35840,38912,14208,702336,1390464,17042,1111507608,4294944972,4294967294,17042,1111507608,4294944972,4294967294,17042,1111507608,4294944972,4294967294,
23232,23232,23232,23424,23424,23424,23616,23616,23616,23808,23808,23808,32768,35840,38912,43008,46080,49152,53248,56320,59392,63488,66560,69632,4294967294,14,30,22,22,19,43008,1056915199,50465804,16780300,36,8064,320,1123287040,1123287040,1123287040,0,1098907648,1107296256,4392,133416,262440,32768,35840,38912,23424,711552,1399680,17138,1111507704,4294929612,4294967294,17138,1111507704,4294929612,4294967294,17138,1111507704,4294929612,4294967294,
31680,31680,31680,31872,31872,31872,32064,32064,32064,32256,32256,32256,32768,35840,38912,43008,46080,49152,53248,56320,59392,63488,66560,69632,4294967294,14,30,22,22,19,43008,1056915071,50465804,16780300,36,8064,320,1126563840,1126563840,1126563840,0,1098907648,1107296256,5976,135000,264024,32768,35840,38912,31872,720000,1408128,17189,1111507752,4294915532,4294967294,17189,1111507752,4294915532,4294967294,17189,1111507752,4294915532,4294967294,
40512,40512,40512,40704,40704,40704,40896,40896,40896,41088,41088,41088,32768,35840,38912,43008,46080,49152,53248,56320,59392,63488,66560,69632,4294967294,14,30,22,22,19,43008,1056915199,50465804,16780300,36,8064,320,1129578496,1129578496,1129578496,0,1098907648,1107296256,7632,136656,265680,32768,35840,38912,40704,728832,1416960,17235,1111507798,4294900812,4294967294,17235,1111507798,4294900812,4294967294,17235,1111507798,4294900812,4294967294,
0,0,0,0,0,0,0,0,0,0,0,0,0,0,0,0,0,0,0,0,0,0,0,0,0,0,0,0,0,0,0,1040137727,0,0,0,0,0,0,0,0,0,0,0,0,0,0,0,0,0,0,0,0,0,0,0,0,0,0,0,0,0,0,0,0,
4859904,4859904,4859904,4902912,4902912,4902912,4945920,4945920,4945920,4988928,4988928,4988928,32768,35840,38912,43008,46080,49152,53248,56320,59392,63488,66560,69632,4294967294,14,30,22,22,19,192,1048526591,16780300,50465804,8064,36,320,1122238464,1122238464,1122238464,0,1098907648,1107296256,919296,919872,920448,32768,35840,38912,4902912,4905984,4909056,17122,1111507688,4294932172,4294967294,17122,1111507688,4294932172,4294967294,17122,1111507688,4294932172,4294967294,
0,0,0,0,0,0,0,0,0,0,0,0,0,0,0,0,0,0,0,0,0,0,0,0,0,0,0,0,0,0,0,1048526591,0,0,0,0,0,0,0,0,0,0,0,0,0,0,0,0,0,0,0,0,0,0,0,0,0,0,0,0,0,0,0,0,
8730624,8730624,8730624,8773632,8773632,8773632,8816640,8816640,8816640,8859648,8859648,8859648,32768,35840,38912,43008,46080,49152,53248,56320,59392,63488,66560,69632,4294967294,14,30,22,22,19,192,1048526463,16780300,50465804,8064,36,320,1129054208,1129054208,1129054208,0,1098907648,1107296256,1645056,1645632,1646208,32768,35840,38912,8773632,8776704,8779776,17227,1111507790,4294903372,4294967294,17227,1111507790,4294903372,4294967294,17227,1111507790,4294903372,4294967294,
14400,14400,14400,14592,14592,14592,14784,14784,14784,14976,14976,14976,32768,35840,38912,43008,46080,49152,53248,56320,59392,63488,66560,69632,4294967294,14,30,22,22,19,43008,1056915199,50465804,16780300,36,8064,320,1117257728,1117257728,1117257728,0,1098907648,1107296256,2736,131760,260784,32768,35840,38912,14592,702720,1390848,17046,1111507612,4294944332,4294967294,17046,1111507612,4294944332,4294967294,17046,1111507612,4294944332,4294967294,
23616,23616,23616,23808,23808,23808,24000,24000,24000,24192,24192,24192,32768,35840,38912,43008,46080,49152,53248,56320,59392,63488,66560,69632,4294967294,14,30,22,22,19,43008,1056915199,50465804,16780300,36,8064,320,1123549184,1123549184,1123549184,0,1098907648,1107296256,4464,133488,262512,32768,35840,38912,23808,711936,1400064,17142,1111507708,4294928972,4294967294,17142,1111507708,4294928972,4294967294,17142,1111507708,4294928972,4294967294,
32064,32064,32064,32256,32256,32256,32448,32448,32448,32640,32640,32640,32768,35840,38912,43008,46080,49152,53248,56320,59392,63488,66560,69632,4294967294,14,30,22,22,19,43008,1056915071,50465804,16780300,36,8064,320,1126694912,1126694912,1126694912,0,1098907648,1107296256,6048,135072,264096,32768,35840,38912,32256,720384,1408512,17191,1111507754,4294914892,4294967294,17191,1111507754,4294914892,4294967294,17191,1111507754,4294914892,4294967294,
40896,40896,40896,41088,41088,41088,41280,41280,41280,41472,41472,41472,32768,35840,38912,43008,46080,49152,53248,56320,59392,63488,66560,69632,4294967294,14,30,22,22,19,43008,1056915199,50465804,16780300,36,8064,320,1129709568,1129709568,1129709568,0,1098907648,1107296256,7704,136728,265752,32768,35840,38912,41088,729216,1417344,17237,1111507800,4294900172,4294967294,17237,1111507800,4294900172,4294967294,17237,1111507800,4294900172,4294967294,
2967552,2967552,2967552,3010560,3010560,3010560,3053568,3053568,3053568,3096576,3096576,3096576,32768,35840,38912,43008,46080,49152,53248,56320,59392,63488,66560,69632,4294967294,14,30,22,22,19,192,1040137855,16780300,50465804,8064,36,320,1116471296,1116471296,1116471296,0,1098907648,1107296256,564480,565056,565632,32768,35840,38912,3010560,3013632,3016704,17034,1111507600,4294946252,4294967294,17034,1111507600,4294946252,4294967294,17034,1111507600,4294946252,4294967294,
4945920,4945920,4945920,4988928,4988928,4988928,5031936,5031936,5031936,5074944,5074944,5074944,32768,35840,38912,43008,46080,49152,53248,56320,59392,63488,66560,69632,4294967294,14,30,22,22,19,192,1048526591,16780300,50465804,8064,36,320,1122500608,1122500608,1122500608,0,1098907648,1107296256,935424,936000,936576,32768,35840,38912,4988928,4992000,4995072,17126,1111507692,4294931532,4294967294,17126,1111507692,4294931532,4294967294,17126,1111507692,4294931532,4294967294,
6838272,6838272,6838272,6881280,6881280,6881280,6924288,6924288,6924288,6967296,6967296,6967296,32768,35840,38912,43008,46080,49152,53248,56320,59392,63488,66560,69632,4294967294,14,30,22,22,19,192,1048526591,16780300,50465804,8064,36,320,1126170624,1126170624,1126170624,0,1098907648,1107296256,1290240,1290816,1291392,32768,35840,38912,6881280,6884352,6887424,17183,1111507746,4294917452,4294967294,17183,1111507746,4294917452,4294967294,17183,1111507746,4294917452,4294967294,
0,0,0,0,0,0,0,0,0,0,0,0,0,0,0,0,0,0,0,0,0,0,0,0,0,0,0,0,0,0,0,1048526463,0,0,0,0,0,0,0,0,0,0,0,0,0,0,0,0,0,0,0,0,0,0,0,0,0,0,0,0,0,0,0,0,
0,0,0,0,0,0,0,0,0,0,0,0,0,0,0,0,0,0,0,0,0,0,0,0,0,0,0,0,0,0,0,1056915199,0,0,0,0,0,0,0,0,0,0,0,0,0,0,0,0,0,0,0,0,0,0,0,0,0,0,0,0,0,0,0,0,
24000,24000,24000,24192,24192,24192,24384,24384,24384,24576,24576,24576,32768,35840,38912,43008,46080,49152,53248,56320,59392,63488,66560,69632,4294967294,14,30,22,22,19,43008,1056914943,50465804,16780300,36,8064,320,1123811328,1123811328,1123811328,0,1098907648,1107296256,4536,133560,262584,32768,35840,38912,24192,712320,1400448,17146,1111507712,4294928332,4294967294,17146,1111507712,4294928332,4294967294,17146,1111507712,4294928332,4294967294,
32448,32448,32448,32640,32640,32640,32832,32832,32832,33024,33024,33024,32768,35840,38912,43008,46080,49152,53248,56320,59392,63488,66560,69632,4294967294,14,30,22,22,19,43008,1056915071,50465804,16780300,36,8064,320,1126825984,1126825984,1126825984,0,1098907648,1107296256,6120,135144,264168,32768,35840,38912,32640,720768,1408896,17193,1111507756,4294914252,4294967294,17193,1111507756,4294914252,4294967294,17193,1111507756,4294914252,4294967294,
41280,41280,41280,41472,41472,41472,41664,41664,41664,41856,41856,41856,32768,35840,38912,43008,46080,49152,53248,56320,59392,63488,66560,69632,4294967294,14,30,22,22,19,43008,1056915199,50465804,16780300,36,8064,320,1129840640,1129840640,1129840640,0,1098907648,1107296256,7776,136800,265824,32768,35840,38912,41472,729600,1417728,17239,1111507802,4294899532,4294967294,17239,1111507802,4294899532,4294967294,17239,1111507802,4294899532,4294967294,
172032,215040,3741696,258048,301056,3784704,344064,387072,3784704,387072,387072,3784704,40960,45056,32768,49152,53248,36864,57344,61440,36864,61440,61440,36864,4294967294,4294967294,62,21,21,19,192,1040137855,16780300,50465804,8064,36,128,1118306304,1086324736,1118568448,1115684864,0,1115684864,679680,48384,695808,16384,45056,24576,1073741824,258048,3710976,1115439774,1117799088,4294956812,62,16512,1099972880,780,4294967294,1115439774,1117799088,4294956812,62,
5031936,5031936,5031936,5074944,5074944,5074944,5117952,5117952,5117952,5160960,5160960,5160960,32768,35840,38912,43008,46080,49152,53248,56320,59392,63488,66560,69632,4294967294,14,30,22,22,19,192,1048526591,16780300,50465804,8064,36,320,1122762752,1122762752,1122762752,0,1098907648,1107296256,951552,952128,952704,32768,35840,38912,5074944,5078016,5081088,17130,1111507696,4294930892,4294967294,17130,1111507696,4294930892,4294967294,17130,1111507696,4294930892,4294967294,
6924288,6924288,6924288,6967296,6967296,6967296,7010304,7010304,7010304,7053312,7053312,7053312,32768,35840,38912,43008,46080,49152,53248,56320,59392,63488,66560,69632,4294967294,14,30,22,22,19,192,1048526591,16780300,50465804,8064,36,320,1126301696,1126301696,1126301696,0,1098907648,1107296256,1306368,1306944,1307520,32768,35840,38912,6967296,6970368,6973440,17185,1111507748,4294916812,4294967294,17185,1111507748,4294916812,4294967294,17185,1111507748,4294916812,4294967294,
8816640,8816640,8816640,8859648,8859648,8859648,8902656,8902656,8902656,8945664,8945664,8945664,32768,35840,38912,43008,46080,49152,53248,56320,59392,63488,66560,69632,4294967294,14,30,22,22,19,192,1048526463,16780300,50465804,8064,36,320,1129185280,1129185280,1129185280,0,1098907648,1107296256,1661184,1661760,1662336,32768,35840,38912,8859648,8862720,8865792,17229,1111507792,4294902732,4294967294,17229,1111507792,4294902732,4294967294,17229,1111507792,4294902732,4294967294,
14784,14784,14784,14976,14976,14976,15168,15168,15168,15360,15360,15360,32768,35840,38912,43008,46080,49152,53248,56320,59392,63488,66560,69632,4294967294,14,30,22,22,19,43008,1056915199,50465804,16780300,36,8064,320,1117519872,1117519872,1117519872,0,1098907648,1107296256,2808,131832,260856,32768,35840,38912,14976,703104,1391232,17050,1111507616,4294943692,4294967294,17050,1111507616,4294943692,4294967294,17050,1111507616,4294943692,4294967294,
24384,24384,24384,24576,24576,24576,24768,24768,24768,24960,24960,24960,32768,35840,38912,43008,46080,49152,53248,56320,59392,63488,66560,69632,4294967294,14,30,22,22,19,43008,1056914943,50465804,16780300,36,8064,320,1124073472,1124073472,1124073472,0,1098907648,1107296256,4608,133632,262656,32768,35840,38912,24576,712704,1400832,17150,1111507714,4294927692,4294967294,17150,1111507714,4294927692,4294967294,17150,1111507714,4294927692,4294967294,
0,0,0,0,0,0,0,0,0,0,0,0,0,0,0,0,0,0,0,0,0,0,0,0,0,0,0,0,0,0,0,1056915071,0,0,0,0,0,0,0,0,0,0,0,0,0,0,0,0,0,0,0,0,0,0,0,0,0,0,0,0,0,0,0,0,
1118208,1161216,1204224,1247232,1290240,1333248,1376256,1419264,1462272,1505280,1505280,1505280,32768,36864,40960,45056,49152,53248,57344,61440,65536,69632,69632,69632,30,30,30,21,21,21,192,1056915199,16780300,50465804,8064,36,128,1105199104,1106247680,1107296256,1107296256,1107296256,1107296256,226944,243072,259200,36864,45056,53248,1210368,1296384,1382400,1106264528,1112031756,4294964812,30,1106264528,1112031756,4294964812,30,1106264528,1112031756,4294964812,30,
3053568,3053568,3053568,3096576,3096576,3096576,3139584,3139584,3139584,3182592,3182592,3182592,32768,35840,38912,43008,46080,49152,53248,56320,59392,63488,66560,69632,4294967294,14,30,22,22,19,192,1040137855,16780300,50465804,8064,36,320,1116733440,1116733440,1116733440,0,1098907648,1107296256,580608,581184,581760,32768,35840,38912,3096576,3099648,3102720,17038,1111507604,4294945612,4294967294,17038,1111507604,4294945612,4294967294,17038,1111507604,4294945612,4294967294,
602112,645120,6666240,688128,731136,6709248,774144,817152,6752256,817152,817152,6795264,49152,53248,32768,57344,61440,36864,65536,69632,40960,69632,69632,45056,4294967294,4294967294,78,21,21,19,192,1048526591,16780300,50465804,8064,36,128,1098907648,1125777408,1125908480,0,1117782016,1117782016,129024,1244736,1260864,53248,24576,32768,688128,6638592,6724608,16736,1099973016,4294967052,4294967294,1117668115,1119896350,4294948012,78,1117668115,1119896350,4294948012,78,
7010304,7010304,7010304,7053312,7053312,7053312,7096320,7096320,7096320,7139328,7139328,7139328,32768,35840,38912,43008,46080,49152,53248,56320,59392,63488,66560,69632,4294967294,14,30,22,22,19,192,1048526335,16780300,50465804,8064,36,320,1126432768,1126432768,1126432768,0,1098907648,1107296256,1322496,1323072,1323648,32768,35840,38912,7053312,7056384,7059456,17187,1111507750,4294916172,4294967294,17187,1111507750,4294916172,4294967294,17187,1111507750,4294916172,4294967294,
0,0,5806080,0,43008,5849088,86016,129024,5892096,129024,129024,5935104,49152,53248,32768,57344,61440,36864,65536,69632,40960,69632,69632,45056,14,14,126,21,21,19,192,1048526463,16780300,50465804,8064,36,128,0,1124466688,1124597760,1098907648,1124073472,1124073472,576,1085184,1101312,53248,24576,32768,3072,5787648,5873664,1096810496,1107836992,1708,14,1123959550,1125139210,4294950284,126,1123959550,1125139210,4294950284,126,
15168,15168,15168,15360,15360,15360,15552,15552,15552,15744,15744,15744,32768,35840,38912,43008,46080,49152,53248,56320,59392,63488,66560,69632,4294967294,14,30,22,22,19,43008,1056915199,50465804,16780300,36,8064,320,1117782016,1117782016,1117782016,0,1098907648,1107296256,2880,131904,260928,32768,35840,38912,15360,703488,1391616,17054,1111507620,4294943052,4294967294,17054,1111507620,4294943052,4294967294,17054,1111507620,4294943052,4294967294,
0,0,0,0,0,0,0,0,0,0,0,0,0,0,0,0,0,0,0,0,0,0,0,0,0,0,0,0,0,0,0,1056914943,0,0,0,0,0,0,0,0,0,0,0,0,0,0,0,0,0,0,0,0,0,0,0,0,0,0,0,0,0,0,0,0,
258048,301056,6322176,344064,387072,6365184,430080,473088,6365184,516096,559104,6365184,40960,45056,32768,49152,53248,36864,57344,61440,36864,65536,69632,36864,30,30,190,21,21,19,192,1056915199,16780300,50465804,8064,36,128,1090519040,1092616192,1125253120,1107296256,1107296256,1128267776,65664,81792,1184256,45056,53248,24576,350208,436224,6316032,1106264256,1112031568,332,30,1106264256,1112031568,332,30,1128219403,1129333524,4294948364,190,
41664,41664,41664,41856,41856,41856,42048,42048,42048,42240,42240,42240,32768,35840,38912,43008,46080,49152,53248,56320,59392,63488,66560,69632,4294967294,14,30,22,22,19,43008,1056915199,50465804,16780300,36,8064,320,1129971712,1129971712,1129971712,0,1098907648,1107296256,7848,136872,265896,32768,35840,38912,41856,729984,1418112,17241,1111507804,4294898892,4294967294,17241,1111507804,4294898892,4294967294,17241,1111507804,4294898892,4294967294,
3139584,3139584,3139584,3182592,3182592,3182592,3225600,3225600,3225600,3268608,3268608,3268608,32768,35840,38912,43008,46080,49152,53248,56320,59392,63488,66560,69632,4294967294,14,30,22,22,19,192,1040137855,16780300,50465804,8064,36,320,1116995584,1116995584,1116995584,0,1098907648,1107296256,596736,597312,597888,32768,35840,38912,3182592,3185664,3188736,17042,1111507608,4294944972,4294967294,17042,1111507608,4294944972,4294967294,17042,1111507608,4294944972,4294967294,
5117952,5117952,5117952,5160960,5160960,5160960,5203968,5203968,5203968,5246976,5246976,5246976,32768,35840,38912,43008,46080,49152,53248,56320,59392,63488,66560,69632,4294967294,14,30,22,22,19,192,1048526591,16780300,50465804,8064,36,320,1123024896,1123024896,1123024896,0,1098907648,1107296256,967680,968256,968832,32768,35840,38912,5160960,5164032,5167104,17134,1111507700,4294930252,4294967294,17134,1111507700,4294930252,4294967294,17134,1111507700,4294930252,4294967294,
7096320,7096320,7096320,7139328,7139328,7139328,7182336,7182336,7182336,7225344,7225344,7225344,32768,35840,38912,43008,46080,49152,53248,56320,59392,63488,66560,69632,4294967294,14,30,22,22,19,192,1048526335,16780300,50465804,8064,36,320,1126563840,1126563840,1126563840,0,1098907648,1107296256,1338624,1339200,1339776,32768,35840,38912,7139328,7142400,7145472,17189,1111507752,4294915532,4294967294,17189,1111507752,4294915532,4294967294,17189,1111507752,4294915532,4294967294,
8902656,8902656,8902656,8945664,8945664,8945664,8988672,8988672,8988672,9031680,9031680,9031680,32768,35840,38912,43008,46080,49152,53248,56320,59392,63488,66560,69632,4294967294,14,30,22,22,19,192,1048526591,16780300,50465804,8064,36,320,1129316352,1129316352,1129316352,0,1098907648,1107296256,1677312,1677888,1678464,32768,35840,38912,8945664,8948736,8951808,17231,1111507794,4294902092,4294967294,17231,1111507794,4294902092,4294967294,17231,1111507794,4294902092,4294967294,
258048,301056,8816640,344064,387072,8859648,430080,473088,8859648,516096,559104,8859648,40960,45056,32768,49152,53248,36864,57344,61440,36864,65536,69632,36864,14,14,142,21,21,19,192,1056915199,16780300,50465804,8064,36,128,1090519040,1092616192,1129054208,1098907648,1098907648,1125122048,65088,81216,1650240,45056,53248,24576,347136,433152,8801280,1096827072,1107837264,428,14,1096827072,1107837264,428,14,1125073733,1126187854,4294941228,142,
1118208,1161216,1204224,1247232,1290240,1333248,1376256,1419264,1462272,1505280,1505280,1505280,32768,36864,40960,45056,49152,53248,57344,61440,65536,69632,69632,69632,14,14,14,21,21,21,192,1056915071,16780300,50465804,8064,36,128,1105199104,1106247680,1107296256,1098907648,1098907648,1098907648,226368,242496,258624,36864,45056,53248,1207296,1293312,1379328,1096827344,1107837452,4294964908,14,1096827344,1107837452,4294964908,14,1096827344,1107837452,4294964908,14,
32832,32832,32832,33024,33024,33024,33216,33216,33216,33408,33408,33408,32768,35840,38912,43008,46080,49152,53248,56320,59392,63488,66560,69632,4294967294,14,30,22,22,19,43008,1056915199,50465804,16780300,36,8064,320,1126957056,1126957056,1126957056,0,1098907648,1107296256,6192,135216,264240,32768,35840,38912,33024,721152,1409280,17195,1111507758,4294913612,4294967294,17195,1111507758,4294913612,4294967294,17195,1111507758,4294913612,4294967294,
0,0,0,0,0,0,0,0,0,0,0,0,0,0,0,0,0,0,0,0,0,0,0,0,0,0,0,0,0,0,0,1056915199,0,0,0,0,0,0,0,0,0,0,0,0,0,0,0,0,0,0,0,0,0,0,0,0,0,0,0,0,0,0,0,0,
3225600,3225600,3225600,3268608,3268608,3268608,3311616,3311616,3311616,3354624,3354624,3354624,32768,35840,38912,43008,46080,49152,53248,56320,59392,63488,66560,69632,4294967294,14,30,22,22,19,192,1040137855,16780300,50465804,8064,36,320,1117257728,1117257728,1117257728,0,1098907648,1107296256,612864,613440,614016,32768,35840,38912,3268608,3271680,3274752,17046,1111507612,4294944332,4294967294,17046,1111507612,4294944332,4294967294,17046,1111507612,4294944332,4294967294,
5203968,5203968,5203968,5246976,5246976,5246976,5289984,5289984,5289984,5332992,5332992,5332992,32768,35840,38912,43008,46080,49152,53248,56320,59392,63488,66560,69632,4294967294,14,30,22,22,19,192,1048526591,16780300,50465804,8064,36,320,1123287040,1123287040,1123287040,0,1098907648,1107296256,983808,984384,984960,32768,35840,38912,5246976,5250048,5253120,17138,1111507704,4294929612,4294967294,17138,1111507704,4294929612,4294967294,17138,1111507704,4294929612,4294967294,
0,0,0,0,0,0,0,0,0,0,0,0,0,0,0,0,0,0,0,0,0,0,0,0,0,0,0,0,0,0,0,1048526335,0,0,0,0,0,0,0,0,0,0,0,0,0,0,0,0,0,0,0,0,0,0,0,0,0,0,0,0,0,0,0,0,
8988672,8988672,8988672,9031680,9031680,9031680,9074688,9074688,9074688,9117696,9117696,9117696,32768,35840,38912,43008,46080,49152,53248,56320,59392,63488,66560,69632,4294967294,14,30,22,22,19,192,1048526591,16780300,50465804,8064,36,320,1129447424,1129447424,1129447424,0,1098907648,1107296256,1693440,1694016,1694592,32768,35840,38912,9031680,9034752,9037824,17233,1111507796,4294901452,4294967294,17233,1111507796,4294901452,4294967294,17233,1111507796,4294901452,4294967294,
15552,15552,15552,15744,15744,15744,15936,15936,15936,16128,16128,16128,32768,35840,38912,43008,46080,49152,53248,56320,59392,63488,66560,69632,4294967294,14,30,22,22,19,43008,1056915199,50465804,16780300,36,8064,320,1118044160,1118044160,1118044160,0,1098907648,1107296256,2952,131976,261000,32768,35840,38912,15744,703872,1392000,17058,1111507624,4294942412,4294967294,17058,1111507624,4294942412,4294967294,17058,1111507624,4294942412,4294967294,
24768,24768,24768,24960,24960,24960,25152,25152,25152,25344,25344,25344,32768,35840,38912,43008,46080,49152,53248,56320,59392,63488,66560,69632,4294967294,14,30,22,22,19,43008,1056915071,50465804,16780300,36,8064,320,1124204544,1124204544,1124204544,0,1098907648,1107296256,4680,133704,262728,32768,35840,38912,24960,713088,1401216,17153,1111507716,4294927052,4294967294,17153,1111507716,4294927052,4294967294,17153,1111507716,4294927052,4294967294,
33216,33216,33216,33408,33408,33408,33600,33600,33600,33792,33792,33792,32768,35840,38912,43008,46080,49152,53248,56320,59392,63488,66560,69632,4294967294,14,30,22,22,19,43008,1056915199,50465804,16780300,36,8064,320,1127088128,1127088128,1127088128,0,1098907648,1107296256,6264,135288,264312,32768,35840,38912,33408,721536,1409664,17197,1111507760,4294912972,4294967294,17197,1111507760,4294912972,4294967294,17197,1111507760,4294912972,4294967294,
42048,42048,42048,42240,42240,42240,42432,42432,42432,42624,42624,42624,32768,35840,38912,43008,46080,49152,53248,56320,59392,63488,66560,69632,4294967294,14,30,22,22,19,43008,1056915199,50465804,16780300,36,8064,320,1130102784,1130102784,1130102784,0,1098907648,1107296256,7920,136944,265968,32768,35840,38912,42240,730368,1418496,17243,1111507806,4294898252,4294967294,17243,1111507806,4294898252,4294967294,17243,1111507806,4294898252,4294967294,
0,0,0,0,0,0,0,0,0,0,0,0,0,0,0,0,0,0,0,0,0,0,0,0,0,0,0,0,0,0,0,1040137855,0,0,0,0,0,0,0,0,0,0,0,0,0,0,0,0,0,0,0,0,0,0,0,0,0,0,0,0,0,0,0,0,
0,0,0,0,0,0,0,0,0,0,0,0,0,0,0,0,0,0,0,0,0,0,0,0,0,0,0,0,0,0,0,1048526591,0,0,0,0,0,0,0,0,0,0,0,0,0,0,0,0,0,0,0,0,0,0,0,0,0,0,0,0,0,0,0,0,
1032192,1075200,3483648,1118208,1161216,3526656,1204224,1247232,3569664,1247232,1247232,3612672,49152,53248,32768,57344,61440,36864,65536,69632,40960,69632,69632,45056,4294967294,4294967294,110,21,21,19,192,1048526463,16780300,50465804,8064,36,128,1104150528,1117782016,1118044160,0,1121976320,1121976320,209664,649152,665280,53248,24576,32768,1118208,3462144,3548160,16832,1099973096,4294965772,4294967294,1121862290,1124090536,4294957292,110,1121862290,1124090536,4294957292,110,
0,0,0,0,0,0,0,0,0,0,0,0,0,0,0,0,0,0,0,0,0,0,0,0,0,0,0,0,0,0,0,1048526591,0,0,0,0,0,0,0,0,0,0,0,0,0,0,0,0,0,0,0,0,0,0,0,0,0,0,0,0,0,0,0,0,
15936,15936,15936,16128,16128,16128,16320,16320,16320,16512,16512,16512,32768,35840,38912,43008,46080,49152,53248,56320,59392,63488,66560,69632,4294967294,14,30,22,22,19,43008,1056915199,50465804,16780300,36,8064,320,1118306304,1118306304,1118306304,0,1098907648,1107296256,3024,132048,261072,32768,35840,38912,16128,704256,1392384,17062,1111507628,4294941772,4294967294,17062,1111507628,4294941772,4294967294,17062,1111507628,4294941772,4294967294,
25152,25152,25152,25344,25344,25344,25536,25536,25536,25728,25728,25728,32768,35840,38912,43008,46080,49152,53248,56320,59392,63488,66560,69632,4294967294,14,30,22,22,19,43008,1056915071,50465804,16780300,36,8064,320,1124335616,1124335616,1124335616,0,1098907648,1107296256,4752,133776,262800,32768,35840,38912,25344,713472,1401600,17155,1111507718,4294926412,4294967294,17155,1111507718,4294926412,4294967294,17155,1111507718,4294926412,4294967294,
33600,33600,33600,33792,33792,33792,33984,33984,33984,34176,34176,34176,32768,35840,38912,43008,46080,49152,53248,56320,59392,63488,66560,69632,4294967294,14,30,22,22,19,43008,1056915199,50465804,16780300,36,8064,320,1127219200,1127219200,1127219200,0,1098907648,1107296256,6336,135360,264384,32768,35840,38912,33792,721920,1410048,17199,1111507762,4294912332,4294967294,17199,1111507762,4294912332,4294967294,17199,1111507762,4294912332,4294967294,
42432,42432,42432,42624,42624,42624,42816,42816,42816,42816,42816,42816,32768,35840,38912,43008,46080,49152,53248,56320,59392,63488,66560,69632,4294967294,14,30,22,22,19,43008,1056915199,50465804,16780300,36,8064,320,1130233856,1130233856,1130233856,0,1098907648,1107296256,7992,137016,266040,32768,35840,38912,42624,730752,1418880,17245,1111507807,4294897612,4294967294,17245,1111507807,4294897612,4294967294,17245,1111507807,4294897612,4294967294};
__device__ __forceinline__ float bf_lo(unsigned u) { return __uint_as_float(u << 16); }
__device__ __forceinline__ float bf_hi(unsigned u) { return __uint_as_float(u & 0xffff0000u); }
__device__ __forceinline__ unsigned cvt_pk(float lo, float hi) { bf16x2 r = {(__bf16)lo, (__bf16)hi}; return __builtin_bit_cast(unsigned, r); }
__device__ __forceinline__ float rb(float v) { return bf_lo(cvt_pk(v, 0.f)); }
__global__ __launch_bounds__(256) void dcn_kernel(const unsigned short* x_, const unsigned short* off_, const unsigned short* w_, unsigned short* out_, void* ws_, Tiles tl_) {
    __shared__ __attribute__((aligned(128))) unsigned char lds[163840];
    asm volatile("" ::"v"((__attribute__((address_space(3))) unsigned char*)lds));
    unsigned tid = threadIdx.x, bid = blockIdx.x;
    const __attribute__((address_space(4))) Args* ka = (const __attribute__((address_space(4))) Args*)__builtin_amdgcn_kernarg_segment_ptr();
    asm volatile(
        "s_load_dwordx2 s[24:25], %1, 16\n\t"
        "s_load_dwordx2 s[16:17], %1, 0\n\t"
        "s_lshl_b32 s8, %2, 2\n\t"
        "s_add_u32 s8, s8, 40\n\t"
        "s_load_dword s7, %1, s8\n\t"
        "s_load_dwordx2 s[20:21], %1, 8\n\t"
        "s_load_dwordx2 s[28:29], %1, 24\n\t"
        "v_mov_b32 v156, %0\n\t"
        "s_mov_b32 s42, %2\n\t"
        "s_getpc_b64 s[44:45]\n\t"
        "s_and_b32 s44, s44, 0xffffff00\n\t"
        "9:\n\t"
        "s_mov_b32 s46, 99f-9b+3700\n\t"
        "s_mov_b32 s47, 0x20000\n\t"
        "v_lshlrev_b32 v160, 10, v156\n\t"
        "s_lshl_b32 s43, s42, 4\n\t"
        "s_and_b32 s43, s43, 0x380\n\t"
        "s_mov_b32 m0, 161856\n\t"
        "v_cmp_gt_u32 vcc, 64, v156\n\t"
        "s_nop 1\n\t"
        "s_cbranch_vccz 10f\n\t"
        "buffer_load_dword v160, s[44:47], s43 offen lds\n\t"
        "10:\n\t"
        "v_and_b32 v157, 63, v156\n\t"
        "s_mov_b64 s[8:9], %3\n\t"
        "s_mov_b32 s10, 65536\n\t"
        "s_mov_b32 s11, 0x20000\n\t"
        "v_lshlrev_b32 v160, 2, v157\n\t"
        "s_lshl_b32 s12, s42, 8\n\t"
        "buffer_load_dword v170, v160, s[8:11], s12 offen\n\t"
        "v_lshrrev_b32 v160, 6, v156\n\t"
        "v_and_b32 v158, 15, v156\n\t"
        "v_readfirstlane_b32 s4, v160\n\t"
        "v_bfe_u32 v159, v156, 4, 2\n\t"
        "s_mov_b32 s18, 9633792\n\t"
        "s_mov_b32 s19, 0x20000\n\t"
        "s_mov_b32 s26, 165888\n\t"
        "s_mov_b32 s27, 0x20000\n\t"
        "v_lshlrev_b32 v140, 4, v157\n\t"
        "v_xor_b32 v142, 32, v140\n\t"
        "v_and_b32 v163, 32, v157\n\t"
        "v_xor_b32 v141, v163, v140\n\t"
        "s_lshr_b32 s8, s4, 1\n\t"
        "s_and_b32 s49, s4, 1\n\t"
        "s_mul_i32 s11, s49, 3072\n\t"
        "s_mul_i32 s36, s8, 12288\n\t"
        "s_add_u32 s36, s36, s11\n\t"
        "s_add_u32 s36, s36, 73728\n\t"
        "s_mul_i32 s37, s4, 3072\n\t"
        "s_add_u32 s37, s37, 73728\n\t"
        "s_lshl_b32 s8, s49, 5\n\t"
        "v_xor_b32 v152, s8, v140\n\t"
        "s_movk_i32 s39, 6144\n\t"
        "s_movk_i32 s38, 6144\n\t"
        "s_cmp_lt_u32 s4, 2\n\t"
        "s_cmovk_i32 s39, -18432\n\t"
        "s_cmovk_i32 s38, -30720\n\t"
        "v_mul_u32_u24 v168, 171, v156\n\t"
        "v_lshrrev_b32 v168, 11, v168\n\t"
        "v_mul_u32_u24 v162, 12, v168\n\t"
        "v_sub_u32 v162, v156, v162\n\t"
        "v_lshrrev_b32 v163, 1, v168\n\t"
        "v_and_b32 v163, 2, v163\n\t"
        "v_xor_b32 v162, v162, v163\n\t"
        "v_lshlrev_b32 v169, 4, v162\n\t"
        "v_add_u32 v161, -1, v168\n\t"
        "v_cmp_lt_u32 vcc, 17, v161\n\t"
        "v_mov_b32 v163, 0x7ff00000\n\t"
        "s_nop 0\n\t"
        "v_cndmask_b32 v162, v169, v163, vcc\n\t"
        "v_add_u32 v161, -2, v168\n\t"
        "s_movk_i32 s9, 192\n\t"
        "s_lshl_b32 s35, s4, 10\n\t"
        "s_add_u32 m0, s37, 0\n\t"
        "s_waitcnt lgkmcnt(0)\n\t"
        "buffer_load_dwordx4 v140, s[24:27], s36 offen offset:0 lds\n\t"
        "buffer_load_dwordx4 v141, s[24:27], s36 offen offset:1024 lds\n\t"
        "buffer_load_dwordx4 v142, s[24:27], s36 offen offset:2048 lds\n\t"
        "s_bfe_u32 s6, s7, 0x80008\n\t"
        "s_and_b32 s5, s7, 255\n\t"
        "v_add_u32 v161, s6, v161\n\t"
        "v_min_u32 v161, 223, v161\n\t"
        "s_mul_i32 s34, s5, 43008\n\t"
        "v_mad_u32_u24 v143, v161, s9, v162\n\t"
        "s_sub_u32 s34, s34, 43008\n\t"
        "s_add_u32 m0, s35, 4096\n\t"
        "s_add_u32 s43, s34, 43008\n\t"
        "buffer_load_dwordx4 v143, s[16:19], s43 offen  lds\n\t"
        "s_add_u32 m0, s35, 8192\n\t"
        "s_add_u32 s43, s34, 86016\n\t"
        "buffer_load_dwordx4 v143, s[16:19], s43 offen  lds\n\t"
        "s_mul_i32 s43, s4, 1536\n\t"
        "s_add_u32 m0, s43, 86016\n\t"
        "s_add_u32 s43, s43, 79872\n\t"
        "buffer_load_dwordx4 v152, s[24:27], s43 offen lds\n\t"
        "buffer_load_dwordx4 v152, s[24:27], s43 offen offset:512 lds\n\t"
        "s_add_u32 m0, s35, 12288\n\t"
        "s_add_u32 s43, s34, 129024\n\t"
        "buffer_load_dwordx4 v143, s[16:19], s43 offen  lds\n\t"
        "s_add_u32 m0, s35, 16384\n\t"
        "s_add_u32 s43, s34, 172032\n\t"
        "buffer_load_dwordx4 v143, s[16:19], s43 offen  lds\n\t"
        "s_add_u32 m0, s37, 24576\n\t"
        "s_add_u32 s36, s36, s39\n\t"
        "buffer_load_dwordx4 v140, s[24:27], s36 offen offset:0 lds\n\t"
        "buffer_load_dwordx4 v141, s[24:27], s36 offen offset:1024 lds\n\t"
        "s_lshr_b32 s8, s4, 1\n\t"
        "s_lshl_b32 s8, s8, 12\n\t"
        "v_add_u32 v161, 1, v158\n\t"
        "v_lshrrev_b32 v162, 1, v161\n\t"
        "v_and_b32 v162, 2, v162\n\t"
        "v_xor_b32 v162, v162, v159\n\t"
        "v_mul_u32_u24 v161, 192, v161\n\t"
        "v_lshl_add_u32 v161, v162, 4, v161\n\t"
        "v_add_u32 v128, s8, v161\n\t"
        "v_add_u32 v161, 2, v158\n\t"
        "v_lshrrev_b32 v162, 1, v161\n\t"
        "v_and_b32 v162, 2, v162\n\t"
        "v_xor_b32 v162, v162, v159\n\t"
        "v_mul_u32_u24 v161, 192, v161\n\t"
        "v_lshl_add_u32 v161, v162, 4, v161\n\t"
        "v_add_u32 v129, s8, v161\n\t"
        "v_add_u32 v161, 3, v158\n\t"
        "v_lshrrev_b32 v162, 1, v161\n\t"
        "v_and_b32 v162, 2, v162\n\t"
        "v_xor_b32 v162, v162, v159\n\t"
        "v_mul_u32_u24 v161, 192, v161\n\t"
        "v_lshl_add_u32 v161, v162, 4, v161\n\t"
        "v_add_u32 v130, s8, v161\n\t"
        "v_lshrrev_b32 v161, 2, v158\n\t"
        "v_lshl_add_u32 v161, v159, 3, v161\n\t"
        "v_mul_u32_u24 v161, 192, v161\n\t"
        "v_add_u32 v161, 73728, v161\n\t"
        "v_and_b32 v162, 3, v158\n\t"
        "v_lshlrev_b32 v162, 3, v162\n\t"
        "v_and_b32 v163, 1, v159\n\t"
        "v_lshl_or_b32 v162, v163, 5, v162\n\t"
        "s_lshl_b32 s11, s49, 7\n\t"
        "v_add3_u32 v137, v161, v162, s11\n\t"
        "v_xor_b32 v138, 32, v137\n\t"
        "s_lshl_b32 s12, s49, 5\n\t"
        "v_xor_b32 v162, s12, v162\n\t"
        "v_add3_u32 v139, v161, v162, 64\n\t"
        "s_bfe_u32 s8, s7, 0x20010\n\t"
        "s_cmp_eq_u32 s8, 0\n\t"
        "s_cbranch_scc1 20f\n\t"
        "s_mov_b32 s22, 1806336\n\t"
        "s_mov_b32 s23, 0x20000\n\t"
        "v_lshrrev_b32 v160, 4, v156\n\t"
        "v_min_u32 v160, 5, v160\n\t"
        "v_mul_u32_u24 v160, 8064, v160\n\t"
        "v_mad_u32_u24 v55, v158, 36, v160\n\t"
        "s_mul_i32 s8, s5, 224\n\t"
        "s_add_u32 s8, s8, s6\n\t"
        "s_mul_i32 s48, s8, 36\n\t"
        "s_mul_i32 s41, s8, 192\n\t"
        "s_mov_b32 s54, 0x7fff7fff\n\t"
        "s_lshl_b32 s8, s4, 2\n\t"
        "s_add_u32 s8, s8, 161792\n\t"
        "v_mov_b32 v153, s8\n\t"
        "v_mov_b32 v154, 161792\n\t"
        "v_add_u32 v181, 61440, v137\n\t"
        "v_add_u32 v182, 61440, v138\n\t"
        "v_add_u32 v183, 61440, v139\n\t"
        "v_mov_b32 v64, 0\n\t"
        "v_mov_b32 v65, 0\n\t"
        "v_mov_b32 v66, 0\n\t"
        "v_mov_b32 v67, 0\n\t"
        "v_min_u32 v61, 95, v156\n\t"
        "v_lshlrev_b32 v61, 4, v61\n\t"
        "v_add_u32 v61, 147456, v61\n\t"
        "ds_write_b128 v61, v[64:67] offset:0\n\t"
        "ds_write_b128 v61, v[64:67] offset:2048\n\t"
        "ds_write_b128 v61, v[64:67] offset:4096\n\t"
        "v_and_b32 v61, 31, v156\n\t"
        "v_lshrrev_b32 v62, 5, v156\n\t"
        "v_mul_u32_u24 v63, 48, v61\n\t"
        "v_lshl_add_u32 v63, v62, 2, v63\n\t"
        "v_add_u32 v53, 147456, v63\n\t"
        "v_mul_u32_u24 v49, 11, v62\n\t"
        "v_lshrrev_b32 v49, 5, v49\n\t"
        "v_mul_u32_u24 v63, 3, v49\n\t"
        "v_sub_u32 v50, v62, v63\n\t"
        "v_mov_b32 v54, 1\n\t"
        "s_lshl_b32 s60, s4, 10\n\t"
        "s_add_u32 s60, s60, 153600\n\t"
        "s_mov_b64 s[56:57], 0\n\t"
        "s_mov_b32 s63, 0x435f0000\n\t"
        "buffer_load_dwordx4 v142, s[24:27], s36 offen offset:2048 lds\n\t"
        "s_add_u32 m0, s35, 20480\n\t"
        "s_add_u32 s43, s34, 215040\n\t"
        "buffer_load_dwordx4 v143, s[16:19], s43 offen  lds\n\t"
        "s_add_u32 m0, s35, 24576\n\t"
        "s_add_u32 s43, s34, 258048\n\t"
        "buffer_load_dwordx4 v143, s[16:19], s43 offen  lds\n\t"
        "s_add_u32 m0, s37, 36864\n\t"
        "s_add_u32 s36, s36, 6144\n\t"
        "buffer_load_dwordx4 v140, s[24:27], s36 offen offset:0 lds\n\t"
        "s_waitcnt vmcnt(10)\n\t"
        "s_barrier\n\t"
        "buffer_load_dwordx4 v141, s[24:27], s36 offen offset:1024 lds\n\t"
        "ds_read_b64_tr_b16 a[0:1], v137 offset:0\n\t"
        "ds_read_b64_tr_b16 a[2:3], v137 offset:768\n\t"
        "ds_read_b64_tr_b16 a[4:5], v138 offset:0\n\t"
        "ds_read_b64_tr_b16 a[6:7], v138 offset:768\n\t"
        "ds_read_b64_tr_b16 a[8:9], v139 offset:0\n\t"
        "ds_read_b64_tr_b16 a[10:11], v139 offset:768\n\t"
        "ds_read_b128 v[84:87], v129 offset:4096\n\t"
        "ds_read_b64_tr_b16 a[12:13], v137 offset:6144\n\t"
        "ds_read_b64_tr_b16 a[14:15], v137 offset:6912\n\t"
        "ds_read_b64_tr_b16 a[16:17], v138 offset:6144\n\t"
        "ds_read_b64_tr_b16 a[18:19], v138 offset:6912\n\t"
        "ds_read_b64_tr_b16 a[20:21], v139 offset:6144\n\t"
        "ds_read_b64_tr_b16 a[22:23], v139 offset:6912\n\t"
        "ds_read_b128 v[88:91], v129 offset:4224\n\t"
        "s_waitcnt lgkmcnt(7)\n\t"
        "v_mfma_f32_16x16x32_bf16 v[0:3], a[0:3], v[84:87], 0\n\t"
        "buffer_load_dwordx4 v142, s[24:27], s36 offen offset:2048 lds\n\t"
        "s_waitcnt vmcnt(10)\n\t"
        "s_barrier\n\t"
        "ds_read_b64_tr_b16 a[24:25], v137 offset:12288\n\t"
        "ds_read_b64_tr_b16 a[26:27], v137 offset:13056\n\t"
        "ds_read_b64_tr_b16 a[28:29], v138 offset:12288\n\t"
        "ds_read_b64_tr_b16 a[30:31], v138 offset:13056\n\t"
        "ds_read_b64_tr_b16 a[32:33], v139 offset:12288\n\t"
        "ds_read_b64_tr_b16 a[34:35], v139 offset:13056\n\t"
        "ds_read_b128 v[92:95], v129 offset:4160\n\t"
        "s_add_u32 m0, s35, 0\n\t"
        "s_add_u32 s43, s34, 0\n\t"
        "v_mfma_f32_16x16x32_bf16 v[4:7], a[4:7], v[84:87], 0\n\t"
        "buffer_load_dwordx4 v143, s[16:19], s43 offen  lds\n\t"
        "s_add_u32 m0, s35, 28672\n\t"
        "s_add_u32 s43, s34, 301056\n\t"
        "s_min_u32 s43, s43, 9590784\n\t"
        "v_mfma_f32_16x16x32_bf16 v[8:11], a[8:11], v[84:87], 0\n\t"
        "buffer_load_dwordx4 v143, s[16:19], s43 offen  lds\n\t"
        "s_waitcnt vmcnt(10)\n\t"
        "s_barrier\n\t"
        "ds_read_b128 v[96:99], v129 offset:12288\n\t"
        "s_add_u32 m0, s37, 49152\n\t"
        "s_add_u32 s36, s36, 6144\n\t"
        "s_waitcnt lgkmcnt(8)\n\t"
        "v_mfma_f32_16x16x32_bf16 v[0:3], a[12:15], v[88:91], v[0:3]\n\t"
        "buffer_load_dwordx4 v140, s[24:27], s36 offen offset:0 lds\n\t"
        "v_readlane_b32 s8, v170, 34\n\t"
        "v_readlane_b32 s9, v170, 35\n\t"
        "v_mfma_f32_16x16x32_bf16 v[4:7], a[16:19], v[88:91], v[4:7]\n\t"
        "ds_read_b128 v[100:103], v129 offset:12416\n\t"
        "v_readlane_b32 s44, v170, 32\n\t"
        "v_readlane_b32 s45, v170, 33\n\t"
        "v_mfma_f32_16x16x32_bf16 v[8:11], a[20:23], v[88:91], v[8:11]\n\t"
        "ds_read_b128 v[104:107], v129 offset:12352\n\t"
        "v_lshrrev_b32 v62, 3, v156\n\t"
        "v_and_b32 v62, 28, v62\n\t"
        "s_waitcnt lgkmcnt(3)\n\t"
        "v_mfma_f32_16x16x32_bf16 v[0:3], a[24:27], v[92:95], v[0:3]\n\t"
        "buffer_load_dwordx4 v141, s[24:27], s36 offen offset:1024 lds\n\t"
        "v_bfe_u32 v61, v156, 4, 1\n\t"
        "v_mad_u32_u24 v62, v61, s8, v62\n\t"
        "v_mfma_f32_16x16x32_bf16 v[4:7], a[28:31], v[92:95], v[4:7]\n\t"
        "v_mad_u32_u24 v51, v158, s9, v62\n\t"
        "s_cmp_eq_u32 s8, 36\n\t"
        "s_cselect_b64 exec, -1, 0\n\t"
        "v_swap_b32 v49, v50\n\t"
        "s_mov_b64 exec, -1\n\t"
        "v_mfma_f32_16x16x32_bf16 v[8:11], a[32:35], v[92:95], v[8:11]\n\t"
        "v_add3_u32 v49, v49, v61, -1\n\t"
        "v_add3_u32 v50, v50, v158, -1\n\t"
        "v_cvt_f32_i32 v49, v49\n\t"
        "s_waitcnt lgkmcnt(2)\n\t"
        "v_mfma_f32_16x16x32_bf16 v[12:15], a[0:3], v[96:99], 0\n\t"
        "buffer_load_dwordx4 v142, s[24:27], s36 offen offset:2048 lds\n\t"
        "v_readlane_b32 s43, v170, 43\n\t"
        "v_cvt_f32_i32 v50, v50\n\t"
        "v_mfma_f32_16x16x32_bf16 v[16:19], a[4:7], v[96:99], 0\n\t"
        "v_readlane_b32 s9, v170, 30\n\t"
        "s_movk_i32 s10, 223\n\t"
        "v_mov_b32 v63, 0x7ff00000\n\t"
        "v_mfma_f32_16x16x32_bf16 v[20:23], a[8:11], v[96:99], 0\n\t"
        "s_waitcnt vmcnt(10)\n\t"
        "s_barrier\n\t"
        "buffer_load_dword v45, v51, s[20:23], s43 offen\n\t"
        "ds_read_b64_tr_b16 a[36:37], v137 offset:24576\n\t"
        "ds_read_b64_tr_b16 a[38:39], v137 offset:25344\n\t"
        "ds_read_b64_tr_b16 a[40:41], v138 offset:24576\n\t"
        "ds_read_b64_tr_b16 a[42:43], v138 offset:25344\n\t"
        "ds_read_b64_tr_b16 a[44:45], v139 offset:24576\n\t"
        "ds_read_b64_tr_b16 a[46:47], v139 offset:25344\n\t"
        "ds_read_b128 v[108:111], v128 offset:4096\n\t"
        "v_readlane_b32 s43, v170, 44\n\t"
        "s_waitcnt lgkmcnt(7)\n\t"
        "v_mfma_f32_16x16x32_bf16 v[12:15], a[12:15], v[100:103], v[12:15]\n\t"
        "s_nop 2\n\t"
        "buffer_load_dword v46, v51, s[20:23], s43 offen\n\t"
        "ds_read_b128 v[112:115], v128 offset:12288\n\t"
        "v_readlane_b32 s43, v170, 45\n\t"
        "v_mfma_f32_16x16x32_bf16 v[16:19], a[16:19], v[100:103], v[16:19]\n\t"
        "ds_read_b64_tr_b16 a[48:49], v137 offset:30720\n\t"
        "ds_read_b64_tr_b16 a[50:51], v137 offset:31488\n\t"
        "v_readlane_b32 s8, v170, 24\n\t"
        "v_mfma_f32_16x16x32_bf16 v[20:23], a[20:23], v[100:103], v[20:23]\n\t"
        "ds_read_b64_tr_b16 a[52:53], v138 offset:30720\n\t"
        "ds_read_b64_tr_b16 a[54:55], v138 offset:31488\n\t"
        "v_readlane_b32 s11, v170, 27\n\t"
        "v_mfma_f32_16x16x32_bf16 v[12:15], a[24:27], v[104:107], v[12:15]\n\t"
        "s_waitcnt vmcnt(10)\n\t"
        "s_barrier\n\t"
        "buffer_load_dword v47, v51, s[20:23], s43 offen\n\t"
        "ds_read_b128 v[84:87], v129 offset:20480\n\t"
        "s_add_u32 s43, s4, 43\n\t"
        "v_readlane_b32 s43, v170, s43\n\t"
        "v_lshrrev_b32 v48, 3, v156\n\t"
        "v_and_b32 v48, 28, v48\n\t"
        "v_sub_u32 v51, v51, v48\n\t"
        "v_mfma_f32_16x16x32_bf16 v[16:19], a[28:31], v[104:107], v[16:19]\n\t"
        "ds_read_b128 v[88:91], v129 offset:20608\n\t"
        "ds_read_b128 v[92:95], v129 offset:20544\n\t"
        "v_add_u32 v61, s8, v168\n\t"
        "v_mfma_f32_16x16x32_bf16 v[20:23], a[32:35], v[104:107], v[20:23]\n\t"
        "buffer_load_dword v48, v51, s[20:23], s43 offen offset:32\n\t"
        "ds_read_b128 v[96:99], v128 offset:20480\n\t"
        "s_add_u32 m0, s37, 61440\n\t"
        "s_add_u32 s36, s36, s38\n\t"
        "s_waitcnt lgkmcnt(8)\n\t"
        "v_mfma_f32_16x16x32_bf16 v[0:3], a[36:39], v[108:111], v[0:3]\n\t"
        "ds_read_b64_tr_b16 a[56:57], v139 offset:30720\n\t"
        "ds_read_b64_tr_b16 a[58:59], v139 offset:31488\n\t"
        "v_med3_i32 v61, v61, 0, s10\n\t"
        "v_mfma_f32_16x16x32_bf16 v[4:7], a[40:43], v[108:111], v[4:7]\n\t"
        "ds_read_b128 v[100:103], v130 offset:4096\n\t"
        "v_cmp_gt_u32 vcc, s11, v168\n\t"
        "v_mad_u32_u24 v61, v61, s9, v169\n\t"
        "v_mfma_f32_16x16x32_bf16 v[8:11], a[44:47], v[108:111], v[8:11]\n\t"
        "buffer_load_dwordx4 v140, s[24:27], s36 offen offset:0 lds\n\t"
        "v_cndmask_b32 v131, v63, v61, vcc\n\t"
        "v_readlane_b32 s8, v170, 25\n\t"
        "v_mfma_f32_16x16x32_bf16 v[12:15], a[36:39], v[112:115], v[12:15]\n\t"
        "ds_read_b128 v[104:107], v130 offset:12288\n\t"
        "v_readlane_b32 s11, v170, 28\n\t"
        "v_mfma_f32_16x16x32_bf16 v[16:19], a[40:43], v[112:115], v[16:19]\n\t"
        "ds_read_b128 v[108:111], v130 offset:20480\n\t"
        "v_add_u32 v61, s8, v168\n\t"
        "v_med3_i32 v61, v61, 0, s10\n\t"
        "v_mfma_f32_16x16x32_bf16 v[20:23], a[44:47], v[112:115], v[20:23]\n\t"
        "v_cmp_gt_u32 vcc, s11, v168\n\t"
        "v_mad_u32_u24 v61, v61, s9, v169\n\t"
        "s_waitcnt lgkmcnt(7)\n\t"
        "v_mfma_f32_16x16x32_bf16 v[24:27], a[0:3], v[84:87], 0\n\t"
        "v_cndmask_b32 v132, v63, v61, vcc\n\t"
        "v_readlane_b32 s8, v170, 26\n\t"
        "v_readlane_b32 s11, v170, 29\n\t"
        "v_mfma_f32_16x16x32_bf16 v[28:31], a[4:7], v[84:87], 0\n\t"
        "buffer_load_dwordx4 v141, s[24:27], s36 offen offset:1024 lds\n\t"
        "v_add_u32 v61, s8, v168\n\t"
        "v_med3_i32 v61, v61, 0, s10\n\t"
        "v_mfma_f32_16x16x32_bf16 v[32:35], a[8:11], v[84:87], 0\n\t"
        "v_cmp_gt_u32 vcc, s11, v168\n\t"
        "v_mad_u32_u24 v61, v61, s9, v169\n\t"
        "v_mfma_f32_16x16x32_bf16 v[24:27], a[12:15], v[88:91], v[24:27]\n\t"
        "v_cndmask_b32 v133, v63, v61, vcc\n\t"
        "v_max_u32 v61, 51, v157\n\t"
        "v_lshrrev_b32 v62, 2, v61\n\t"
        "v_mfma_f32_16x16x32_bf16 v[28:31], a[16:19], v[88:91], v[28:31]\n\t"
        "v_and_b32 v61, 3, v61\n\t"
        "v_lshlrev_b32 v62, 11, v62\n\t"
        "v_lshl_add_u32 v61, v61, 2, v62\n\t"
        "v_mfma_f32_16x16x32_bf16 v[32:35], a[20:23], v[88:91], v[32:35]\n\t"
        "v_max_u32 v61, 25132, v61\n\t"
        "v_add_u32 v61, 120832, v61\n\t"
        "s_waitcnt lgkmcnt(1)\n\t"
        "v_mfma_f32_16x16x32_bf16 v[24:27], a[24:27], v[92:95], v[24:27]\n\t"
        "buffer_load_dwordx4 v142, s[24:27], s36 offen offset:2048 lds\n\t"
        "s_add_u32 m0, s37, 0\n\t"
        "s_add_u32 s36, s36, 6144\n\t"
        "v_mfma_f32_16x16x32_bf16 v[28:31], a[28:31], v[92:95], v[28:31]\n\t"
        "ds_write_b32 v61, v170 offset:1536\n\t"
        "v_readlane_b32 s8, v170, 31\n\t"
        "v_mfma_f32_16x16x32_bf16 v[32:35], a[32:35], v[92:95], v[32:35]\n\t"
        "s_nop 0\n\t"
        "v_mov_b32 v155, s8\n\t"
        "v_mfma_f32_16x16x32_bf16 v[24:27], a[36:39], v[96:99], v[24:27]\n\t"
        "v_mfma_f32_16x16x32_bf16 v[28:31], a[40:43], v[96:99], v[28:31]\n\t"
        "s_waitcnt vmcnt(10)\n\t"
        "s_barrier\n\t"
        "buffer_load_dwordx4 v140, s[24:27], s36 offen offset:0 lds\n\t"
        "ds_read_b64_tr_b16 a[60:61], v137 offset:36864\n\t"
        "v_mfma_f32_16x16x32_bf16 v[32:35], a[44:47], v[96:99], v[32:35]\n\t"
        "ds_read_b64_tr_b16 a[62:63], v137 offset:37632\n\t"
        "ds_read_b64_tr_b16 a[64:65], v138 offset:36864\n\t"
        "v_and_b32 v116, 48, v140\n\t"
        "v_mfma_f32_16x16x32_bf16 v[0:3], a[48:51], v[100:103], v[0:3]\n\t"
        "buffer_load_dwordx4 v141, s[24:27], s36 offen offset:1024 lds\n\t"
        "ds_read_b64_tr_b16 a[66:67], v138 offset:37632\n\t"
        "ds_read_b64_tr_b16 a[68:69], v139 offset:36864\n\t"
        "v_mfma_f32_16x16x32_bf16 v[4:7], a[52:55], v[100:103], v[4:7]\n\t"
        "ds_read_b64_tr_b16 a[70:71], v139 offset:37632\n\t"
        "ds_read_b128 v[112:115], v128 offset:4160\n\t"
        "v_lshrrev_b32 v160, 1, v158\n\t"
        "v_mfma_f32_16x16x32_bf16 v[8:11], a[56:59], v[100:103], v[8:11]\n\t"
        "ds_read_b128 v[84:87], v128 offset:12352\n\t"
        "ds_read_b128 v[88:91], v128 offset:20544\n\t"
        "v_and_b32 v160, 2, v160\n\t"
        "v_mfma_f32_16x16x32_bf16 v[12:15], a[48:51], v[104:107], v[12:15]\n\t"
        "buffer_load_dwordx4 v142, s[24:27], s36 offen offset:2048 lds\n\t"
        "ds_read_b64_tr_b16 a[72:73], v137 offset:43008\n\t"
        "v_readlane_b32 s40, v170, 12\n\t"
        "v_readlane_b32 s43, v170, 0\n\t"
        "s_nop 0\n\t"
        "s_add_u32 m0, s40, s35\n\t"
        "v_mfma_f32_16x16x32_bf16 v[16:19], a[52:55], v[104:107], v[16:19]\n\t"
        "ds_read_b64_tr_b16 a[74:75], v137 offset:43776\n\t"
        "ds_read_b64_tr_b16 a[76:77], v138 offset:43008\n\t"
        "v_add_u32 v160, v160, v159\n\t"
        "v_mfma_f32_16x16x32_bf16 v[20:23], a[56:59], v[104:107], v[20:23]\n\t"
        "ds_read_b64_tr_b16 a[78:79], v138 offset:43776\n\t"
        "ds_read_b64_tr_b16 a[80:81], v139 offset:43008\n\t"
        "v_and_b32 v160, 3, v160\n\t"
        "s_waitcnt lgkmcnt(11)\n\t"
        "v_mfma_f32_16x16x32_bf16 v[24:27], a[48:51], v[108:111], v[24:27]\n\t"
        "buffer_load_dwordx4 v131, s[16:19], s43 offen  lds\n\t"
        "ds_read_b64_tr_b16 a[82:83], v139 offset:43776\n\t"
        "v_readlane_b32 s40, v170, 13\n\t"
        "v_mfma_f32_16x16x32_bf16 v[28:31], a[52:55], v[108:111], v[28:31]\n\t"
        "ds_read_b128 v[92:95], v130 offset:4160\n\t"
        "v_readlane_b32 s43, v170, 1\n\t"
        "s_add_u32 m0, s40, s35\n\t"
        "v_mfma_f32_16x16x32_bf16 v[32:35], a[56:59], v[108:111], v[32:35]\n\t"
        "ds_read_b128 v[96:99], v130 offset:12352\n\t"
        "v_and_or_b32 v160, v158, 12, v160\n\t"
        "s_add_u32 s9, s35, 157696\n\t"
        "s_waitcnt lgkmcnt(9)\n\t"
        "v_mfma_f32_16x16x32_bf16 v[0:3], a[60:63], v[112:115], v[0:3]\n\t"
        "buffer_load_dwordx4 v132, s[16:19], s43 offen  lds\n\t"
        "v_readlane_b32 s40, v170, 14\n\t"
        "v_readlane_b32 s43, v170, 2\n\t"
        "v_mfma_f32_16x16x32_bf16 v[4:7], a[64:67], v[112:115], v[4:7]\n\t"
        "ds_read_b128 v[100:103], v130 offset:20544\n\t"
        "s_add_u32 m0, s40, s35\n\t"
        "v_lshl_add_u32 v121, v160, 4, s9\n\t"
        "v_mfma_f32_16x16x32_bf16 v[8:11], a[68:71], v[112:115], v[8:11]\n\t"
        "v_lshlrev_b32 v160, 2, v158\n\t"
        "v_and_b32 v160, 16, v160\n\t"
        "s_mov_b32 s9, 0x0c0c0100\n\t"
        "v_mfma_f32_16x16x32_bf16 v[12:15], a[60:63], v[84:87], v[12:15]\n\t"
        "buffer_load_dwordx4 v133, s[16:19], s43 offen  lds\n\t"
        "s_add_u32 m0, s37, 12288\n\t"
        "s_add_u32 s36, s36, 6144\n\t"
        "v_mfma_f32_16x16x32_bf16 v[16:19], a[64:67], v[84:87], v[16:19]\n\t"
        "v_alignbit_b32 v118, s9, s9, v160\n\t"
        "s_mov_b32 s9, 0x0c0c0302\n\t"
        "v_alignbit_b32 v120, s9, s9, v160\n\t"
        "v_mfma_f32_16x16x32_bf16 v[20:23], a[68:71], v[84:87], v[20:23]\n\t"
        "v_mov_b32 v161, 0x0c0c0c0c\n\t"
        "s_mov_b32 s10, 8519745\n\t"
        "s_mov_b32 s11, 2621460\n\t"
        "s_waitcnt lgkmcnt(3)\n\t"
        "v_mfma_f32_16x16x32_bf16 v[24:27], a[60:63], v[88:91], v[24:27]\n\t"
        "buffer_load_dwordx4 v140, s[24:27], s36 offen offset:0 lds\n\t"
        "v_cndmask_b32 v117, v161, v118, s[10:11]\n\t"
        "v_cndmask_b32 v119, v161, v120, s[10:11]\n\t"
        "v_mfma_f32_16x16x32_bf16 v[28:31], a[64:67], v[88:91], v[28:31]\n\t"
        "s_mov_b32 s10, 2181054720\n\t"
        "s_mov_b32 s11, 671093760\n\t"
        "v_cndmask_b32 v118, v161, v118, s[10:11]\n\t"
        "v_mfma_f32_16x16x32_bf16 v[32:35], a[68:71], v[88:91], v[32:35]\n\t"
        "v_cndmask_b32 v120, v161, v120, s[10:11]\n\t"
        "s_waitcnt lgkmcnt(1)\n\t"
        "v_mfma_f32_16x16x32_bf16 v[0:3], a[72:75], v[92:95], v[0:3]\n\t"
        "buffer_load_dwordx4 v141, s[24:27], s36 offen offset:1024 lds\n\t"
        "v_mfma_f32_16x16x32_bf16 v[4:7], a[76:79], v[92:95], v[4:7]\n\t"
        "v_mfma_f32_16x16x32_bf16 v[8:11], a[80:83], v[92:95], v[8:11]\n\t"
        "buffer_load_dwordx4 v142, s[24:27], s36 offen offset:2048 lds\n\t"
        "buffer_load_dwordx4 v[36:39], v55, s[20:23], s48 offen\n\t"
        "s_waitcnt vmcnt(10)\n\t"
        "s_barrier\n\t"
        "ds_read_b64_tr_b16 a[84:85], v137 offset:49152\n\t"
        "ds_read_b64_tr_b16 a[86:87], v137 offset:49920\n\t"
        "ds_read_b64_tr_b16 a[88:89], v138 offset:49152\n\t"
        "ds_read_b64_tr_b16 a[90:91], v138 offset:49920\n\t"
        "ds_read_b64_tr_b16 a[92:93], v139 offset:49152\n\t"
        "ds_read_b64_tr_b16 a[94:95], v139 offset:49920\n\t"
        "ds_read_b128 v[104:107], v128 offset:4224\n\t"
        "v_mfma_f32_16x16x32_bf16 v[12:15], a[72:75], v[96:99], v[12:15]\n\t"
        "ds_read_b128 v[108:111], v128 offset:12416\n\t"
        "ds_read_b128 v[112:115], v128 offset:20608\n\t"
        "v_readlane_b32 s46, v170, 37\n\t"
        "v_mfma_f32_16x16x32_bf16 v[16:19], a[76:79], v[96:99], v[16:19]\n\t"
        "ds_read_b64_tr_b16 a[96:97], v137 offset:55296\n\t"
        "ds_read_b64_tr_b16 a[98:99], v137 offset:56064\n\t"
        "v_readlane_b32 s47, v170, 40\n\t"
        "v_mfma_f32_16x16x32_bf16 v[20:23], a[80:83], v[96:99], v[20:23]\n\t"
        "buffer_load_dwordx4 v[40:43], v55, s[20:23], s48 offen offset:16\n\t"
        "ds_read_b64_tr_b16 a[100:101], v138 offset:55296\n\t"
        "v_perm_b32 v56, v45, v45, s44\n\t"
        "s_waitcnt lgkmcnt(12)\n\t"
        "v_mfma_f32_16x16x32_bf16 v[24:27], a[72:75], v[100:103], v[24:27]\n\t"
        "ds_read_b64_tr_b16 a[102:103], v138 offset:56064\n\t"
        "ds_read_b64_tr_b16 a[104:105], v139 offset:55296\n\t"
        "v_perm_b32 v57, v45, v45, s45\n\t"
        "v_mfma_f32_16x16x32_bf16 v[28:31], a[76:79], v[100:103], v[28:31]\n\t"
        "ds_read_b64_tr_b16 a[106:107], v139 offset:56064\n\t"
        "ds_read_b128 v[84:87], v130 offset:4224\n\t"
        "v_add_f32 v58, s46, v49\n\t"
        "v_mfma_f32_16x16x32_bf16 v[32:35], a[80:83], v[100:103], v[32:35]\n\t"
        "buffer_load_dword v44, v55, s[20:23], s48 offen offset:32\n\t"
        "ds_read_b128 v[88:91], v130 offset:12416\n\t"
        "s_add_u32 m0, s37, 24576\n\t"
        "s_waitcnt lgkmcnt(10)\n\t"
        "v_mfma_f32_16x16x32_bf16 v[0:3], a[84:87], v[104:107], v[0:3]\n\t"
        "ds_read_b128 v[92:95], v130 offset:20608\n\t"
        "ds_read_b64_tr_b16 a[108:109], v181 offset:0\n\t"
        "s_add_u32 s36, s36, s38\n\t"
        "v_mfma_f32_16x16x32_bf16 v[4:7], a[88:91], v[104:107], v[4:7]\n\t"
        "ds_read_b64_tr_b16 a[110:111], v181 offset:768\n\t"
        "ds_read_b64_tr_b16 a[112:113], v182 offset:0\n\t"
        "v_add_f32 v59, s47, v50\n\t"
        "v_mfma_f32_16x16x32_bf16 v[8:11], a[92:95], v[104:107], v[8:11]\n\t"
        "buffer_load_dwordx4 v140, s[24:27], s36 offen offset:0 lds\n\t"
        "ds_read_b64_tr_b16 a[114:115], v182 offset:768\n\t"
        "v_add_f32 v56, v56, v58\n\t"
        "s_waitcnt lgkmcnt(11)\n\t"
        "v_mfma_f32_16x16x32_bf16 v[12:15], a[84:87], v[108:111], v[12:15]\n\t"
        "ds_read_b64_tr_b16 a[116:117], v183 offset:0\n\t"
        "ds_read_b64_tr_b16 a[118:119], v183 offset:768\n\t"
        "v_add_f32 v57, v57, v59\n\t"
        "v_mfma_f32_16x16x32_bf16 v[16:19], a[88:91], v[108:111], v[16:19]\n\t"
        "ds_read_b128 v[96:99], v130 offset:0\n\t"
        "ds_read_b128 v[100:103], v130 offset:8192\n\t"
        "v_cvt_pk_bf16_f32 v52, v56, v57\n\t"
        "v_mfma_f32_16x16x32_bf16 v[20:23], a[92:95], v[108:111], v[20:23]\n\t"
        "buffer_load_dwordx4 v141, s[24:27], s36 offen offset:1024 lds\n\t"
        "ds_read_b128 v[104:107], v130 offset:16384\n\t"
        "v_cvt_pk_bf16_f32 v58, v58, v59\n\t"
        "v_mfma_f32_16x16x32_bf16 v[24:27], a[84:87], v[112:115], v[24:27]\n\t"
        "ds_read_b64_tr_b16 a[120:121], v181 offset:6144\n\t"
        "ds_read_b64_tr_b16 a[122:123], v181 offset:6912\n\t"
        "v_cmp_ne_u32 s[64:65], v52, v58\n\t"
        "v_mfma_f32_16x16x32_bf16 v[28:31], a[88:91], v[112:115], v[28:31]\n\t"
        "ds_read_b64_tr_b16 a[124:125], v182 offset:6144\n\t"
        "ds_read_b64_tr_b16 a[126:127], v182 offset:6912\n\t"
        "v_mbcnt_lo_u32_b32 v56, s64, v54\n\t"
        "v_mfma_f32_16x16x32_bf16 v[32:35], a[92:95], v[112:115], v[32:35]\n\t"
        "buffer_load_dwordx4 v142, s[24:27], s36 offen offset:2048 lds\n\t"
        "ds_read_b64_tr_b16 a[128:129], v183 offset:6144\n\t"
        "v_readlane_b32 s40, v170, 15\n\t"
        "s_waitcnt lgkmcnt(11)\n\t"
        "v_mfma_f32_16x16x32_bf16 v[0:3], a[96:99], v[84:87], v[0:3]\n\t"
        "ds_read_b64_tr_b16 a[130:131], v183 offset:6912\n\t"
        "v_readlane_b32 s43, v170, 3\n\t"
        "s_add_u32 m0, s40, s35\n\t"
        "v_mfma_f32_16x16x32_bf16 v[4:7], a[100:103], v[84:87], v[4:7]\n\t"
        "ds_read_b128 v[108:111], v128 offset:8192\n\t"
        "v_mbcnt_hi_u32_b32 v56, s65, v56\n\t"
        "s_bcnt1_i32_b64 s46, s[64:65]\n\t"
        "v_mfma_f32_16x16x32_bf16 v[8:11], a[104:107], v[84:87], v[8:11]\n\t"
        "buffer_load_dwordx4 v131, s[16:19], s43 offen  lds\n\t"
        "v_readlane_b32 s40, v170, 16\n\t"
        "v_readlane_b32 s43, v170, 4\n\t"
        "v_mfma_f32_16x16x32_bf16 v[12:15], a[96:99], v[88:91], v[12:15]\n\t"
        "ds_read_b128 v[112:115], v128 offset:16384\n\t"
        "s_add_u32 m0, s40, s35\n\t"
        "v_add_u32 v54, s46, v54\n\t"
        "v_mfma_f32_16x16x32_bf16 v[16:19], a[100:103], v[88:91], v[16:19]\n\t"
        "ds_read_b128 v[84:87], v128 offset:24576\n\t"
        "v_min_u32 v56, 97, v56\n\t"
        "v_cndmask_b32 v56, 0, v56, s[64:65]\n\t"
        "v_mfma_f32_16x16x32_bf16 v[20:23], a[104:107], v[88:91], v[20:23]\n\t"
        "buffer_load_dwordx4 v132, s[16:19], s43 offen  lds\n\t"
        "v_readlane_b32 s40, v170, 17\n\t"
        "v_readlane_b32 s43, v170, 5\n\t"
        "v_mfma_f32_16x16x32_bf16 v[24:27], a[96:99], v[92:95], v[24:27]\n\t"
        "s_add_u32 m0, s40, s35\n\t"
        "v_lshl_add_u32 v56, v56, 3, s60\n\t"
        "v_mfma_f32_16x16x32_bf16 v[28:31], a[100:103], v[92:95], v[28:31]\n\t"
        "ds_write_b64 v56, v[52:53]\n\t"
        "v_mfma_f32_16x16x32_bf16 v[32:35], a[104:107], v[92:95], v[32:35]\n\t"
        "buffer_load_dwordx4 v133, s[16:19], s43 offen  lds\n\t"
        "s_add_u32 m0, s37, 36864\n\t"
        "s_add_u32 s36, s36, 6144\n\t"
        "s_waitcnt lgkmcnt(4)\n\t"
        "v_mfma_f32_16x16x32_bf16 v[0:3], a[108:111], v[96:99], v[0:3]\n\t"
        "s_waitcnt vmcnt(9)\n\t"
        "s_barrier\n\t"
        "ds_read_b64_tr_b16 a[132:133], v137 offset:0\n\t"
        "ds_read_b64_tr_b16 a[134:135], v137 offset:768\n\t"
        "v_mfma_f32_16x16x32_bf16 v[4:7], a[112:115], v[96:99], v[4:7]\n\t"
        "ds_read_b64_tr_b16 a[136:137], v138 offset:0\n\t"
        "ds_read_b64_tr_b16 a[138:139], v138 offset:768\n\t"
        "v_add_u32 v53, 2048, v53\n\t"
        "v_mfma_f32_16x16x32_bf16 v[8:11], a[116:119], v[96:99], v[8:11]\n\t"
        "buffer_load_dwordx4 v140, s[24:27], s36 offen offset:0 lds\n\t"
        "ds_read_b64_tr_b16 a[140:141], v139 offset:0\n\t"
        "v_readlane_b32 s46, v170, 38\n\t"
        "v_mfma_f32_16x16x32_bf16 v[12:15], a[108:111], v[100:103], v[12:15]\n\t"
        "ds_read_b64_tr_b16 a[142:143], v139 offset:768\n\t"
        "ds_read_b128 v[88:91], v130 offset:64\n\t"
        "v_readlane_b32 s47, v170, 41\n\t"
        "v_mfma_f32_16x16x32_bf16 v[16:19], a[112:115], v[100:103], v[16:19]\n\t"
        "ds_read_b128 v[92:95], v130 offset:8256\n\t"
        "ds_read_b128 v[96:99], v130 offset:16448\n\t"
        "v_perm_b32 v56, v46, v46, s44\n\t"
        "v_mfma_f32_16x16x32_bf16 v[20:23], a[116:119], v[100:103], v[20:23]\n\t"
        "buffer_load_dwordx4 v141, s[24:27], s36 offen offset:1024 lds\n\t"
        "ds_read_b64_tr_b16 a[144:145], v137 offset:6144\n\t"
        "v_perm_b32 v57, v46, v46, s45\n\t"
        "v_mfma_f32_16x16x32_bf16 v[24:27], a[108:111], v[104:107], v[24:27]\n\t"
        "ds_read_b64_tr_b16 a[146:147], v137 offset:6912\n\t"
        "ds_read_b64_tr_b16 a[148:149], v138 offset:6144\n\t"
        "v_add_f32 v58, s46, v49\n\t"
        "v_mfma_f32_16x16x32_bf16 v[28:31], a[112:115], v[104:107], v[28:31]\n\t"
        "ds_read_b64_tr_b16 a[150:151], v138 offset:6912\n\t"
        "ds_read_b64_tr_b16 a[152:153], v139 offset:6144\n\t"
        "v_add_f32 v59, s47, v50\n\t"
        "v_mfma_f32_16x16x32_bf16 v[32:35], a[116:119], v[104:107], v[32:35]\n\t"
        "buffer_load_dwordx4 v142, s[24:27], s36 offen offset:2048 lds\n\t"
        "ds_read_b64_tr_b16 a[154:155], v139 offset:6912\n\t"
        "s_add_u32 m0, s37, 49152\n\t"
        "s_waitcnt lgkmcnt(11)\n\t"
        "v_mfma_f32_16x16x32_bf16 v[0:3], a[120:123], v[108:111], v[0:3]\n\t"
        "ds_read_b128 v[100:103], v128 offset:8256\n\t"
        "ds_read_b128 v[104:107], v128 offset:16448\n\t"
        "s_add_u32 s36, s36, 6144\n\t"
        "v_mfma_f32_16x16x32_bf16 v[4:7], a[124:127], v[108:111], v[4:7]\n\t"
        "ds_read_b64_tr_b16 a[156:157], v137 offset:12288\n\t"
        "ds_read_b64_tr_b16 a[158:159], v137 offset:13056\n\t"
        "v_add_f32 v56, v56, v58\n\t"
        "v_mfma_f32_16x16x32_bf16 v[8:11], a[128:131], v[108:111], v[8:11]\n\t"
        "ds_read_b64_tr_b16 a[160:161], v138 offset:12288\n\t"
        "ds_read_b64_tr_b16 a[162:163], v138 offset:13056\n\t"
        "v_add_f32 v57, v57, v59\n\t"
        "v_mfma_f32_16x16x32_bf16 v[12:15], a[120:123], v[112:115], v[12:15]\n\t"
        "buffer_load_dwordx4 v140, s[24:27], s36 offen offset:0 lds\n\t"
        "ds_read_b128 v[108:111], v128 offset:24640\n\t"
        "v_cvt_pk_bf16_f32 v52, v56, v57\n\t"
        "v_mfma_f32_16x16x32_bf16 v[16:19], a[124:127], v[112:115], v[16:19]\n\t"
        "ds_read_b64_tr_b16 a[164:165], v139 offset:12288\n\t"
        "ds_read_b64_tr_b16 a[166:167], v139 offset:13056\n\t"
        "v_cvt_pk_bf16_f32 v58, v58, v59\n\t"
        "v_mfma_f32_16x16x32_bf16 v[20:23], a[128:131], v[112:115], v[20:23]\n\t"
        "ds_read_b64_tr_b16 a[168:169], v137 offset:18432\n\t"
        "ds_read_b64_tr_b16 a[170:171], v137 offset:19200\n\t"
        "v_cmp_ne_u32 s[64:65], v52, v58\n\t"
        "v_mfma_f32_16x16x32_bf16 v[24:27], a[120:123], v[84:87], v[24:27]\n\t"
        "buffer_load_dwordx4 v141, s[24:27], s36 offen offset:1024 lds\n\t"
        "ds_read_b128 v[112:115], v130 offset:128\n\t"
        "v_mbcnt_lo_u32_b32 v56, s64, v54\n\t"
        "v_mfma_f32_16x16x32_bf16 v[28:31], a[124:127], v[84:87], v[28:31]\n\t"
        "ds_read_b64_tr_b16 a[172:173], v138 offset:18432\n\t"
        "ds_read_b64_tr_b16 a[174:175], v138 offset:19200\n\t"
        "v_mbcnt_hi_u32_b32 v56, s65, v56\n\t"
        "v_mfma_f32_16x16x32_bf16 v[32:35], a[128:131], v[84:87], v[32:35]\n\t"
        "ds_read_b64_tr_b16 a[176:177], v139 offset:18432\n\t"
        "ds_read_b64_tr_b16 a[178:179], v139 offset:19200\n\t"
        "s_bcnt1_i32_b64 s46, s[64:65]\n\t"
        "s_waitcnt lgkmcnt(11)\n\t"
        "v_mfma_f32_16x16x32_bf16 v[0:3], a[132:135], v[88:91], v[0:3]\n\t"
        "buffer_load_dwordx4 v142, s[24:27], s36 offen offset:2048 lds\n\t"
        "v_readlane_b32 s40, v170, 18\n\t"
        "v_readlane_b32 s43, v170, 6\n\t"
        "v_mfma_f32_16x16x32_bf16 v[4:7], a[136:139], v[88:91], v[4:7]\n\t"
        "ds_read_b128 v[84:87], v128 offset:8320\n\t"
        "s_add_u32 m0, s40, s35\n\t"
        "v_add_u32 v54, s46, v54\n\t"
        "v_mfma_f32_16x16x32_bf16 v[8:11], a[140:143], v[88:91], v[8:11]\n\t"
        "v_min_u32 v56, 97, v56\n\t"
        "v_cndmask_b32 v56, 0, v56, s[64:65]\n\t"
        "v_lshl_add_u32 v56, v56, 3, s60\n\t"
        "v_mfma_f32_16x16x32_bf16 v[12:15], a[132:135], v[92:95], v[12:15]\n\t"
        "buffer_load_dwordx4 v131, s[16:19], s43 offen  lds\n\t"
        "v_readlane_b32 s40, v170, 19\n\t"
        "v_readlane_b32 s43, v170, 7\n\t"
        "v_mfma_f32_16x16x32_bf16 v[16:19], a[136:139], v[92:95], v[16:19]\n\t"
        "ds_read_b128 v[88:91], v130 offset:8320\n\t"
        "s_add_u32 m0, s40, s35\n\t"
        "v_mfma_f32_16x16x32_bf16 v[20:23], a[140:143], v[92:95], v[20:23]\n\t"
        "ds_write_b64 v56, v[52:53]\n\t"
        "v_mfma_f32_16x16x32_bf16 v[24:27], a[132:135], v[96:99], v[24:27]\n\t"
        "ds_read_b128 v[92:95], v128 offset:16512\n\t"
        "v_add_u32 v53, 2048, v53\n\t"
        "v_readlane_b32 s46, v170, 39\n\t"
        "v_mfma_f32_16x16x32_bf16 v[28:31], a[136:139], v[96:99], v[28:31]\n\t"
        "buffer_load_dwordx4 v132, s[16:19], s43 offen  lds\n\t"
        "v_readlane_b32 s40, v170, 20\n\t"
        "v_readlane_b32 s43, v170, 8\n\t"
        "v_mfma_f32_16x16x32_bf16 v[32:35], a[140:143], v[96:99], v[32:35]\n\t"
        "s_add_u32 m0, s40, s35\n\t"
        "v_readlane_b32 s47, v170, 42\n\t"
        "v_perm_b32 v56, v47, v47, s44\n\t"
        "v_mfma_f32_16x16x32_bf16 v[0:3], a[144:147], v[100:103], v[0:3]\n\t"
        "ds_read_b128 v[96:99], v130 offset:16512\n\t"
        "v_perm_b32 v57, v47, v47, s45\n\t"
        "v_add_f32 v58, s46, v49\n\t"
        "v_mfma_f32_16x16x32_bf16 v[4:7], a[148:151], v[100:103], v[4:7]\n\t"
        "buffer_load_dwordx4 v133, s[16:19], s43 offen  lds\n\t"
        "s_add_u32 m0, s37, 61440\n\t"
        "s_add_u32 s36, s36, s38\n\t"
        "v_mfma_f32_16x16x32_bf16 v[8:11], a[152:155], v[100:103], v[8:11]\n\t"
        "v_add_f32 v59, s47, v50\n\t"
        "v_add_f32 v56, v56, v58\n\t"
        "v_add_f32 v57, v57, v59\n\t"
        "v_mfma_f32_16x16x32_bf16 v[12:15], a[144:147], v[104:107], v[12:15]\n\t"
        "ds_read_b128 v[100:103], v128 offset:24704\n\t"
        "v_cvt_pk_bf16_f32 v52, v56, v57\n\t"
        "v_cvt_pk_bf16_f32 v58, v58, v59\n\t"
        "v_mfma_f32_16x16x32_bf16 v[16:19], a[148:151], v[104:107], v[16:19]\n\t"
        "buffer_load_dwordx4 v140, s[24:27], s36 offen offset:0 lds\n\t"
        "v_cmp_ne_u32 s[64:65], v52, v58\n\t"
        "v_mfma_f32_16x16x32_bf16 v[20:23], a[152:155], v[104:107], v[20:23]\n\t"
        "s_nop 0\n\t"
        "v_mbcnt_lo_u32_b32 v56, s64, v54\n\t"
        "v_mbcnt_hi_u32_b32 v56, s65, v56\n\t"
        "s_bcnt1_i32_b64 s46, s[64:65]\n\t"
        "s_waitcnt lgkmcnt(2)\n\t"
        "v_mfma_f32_16x16x32_bf16 v[24:27], a[144:147], v[108:111], v[24:27]\n\t"
        "v_add_u32 v54, s46, v54\n\t"
        "v_min_u32 v56, 97, v56\n\t"
        "v_cndmask_b32 v56, 0, v56, s[64:65]\n\t"
        "v_mfma_f32_16x16x32_bf16 v[28:31], a[148:151], v[108:111], v[28:31]\n\t"
        "v_lshl_add_u32 v56, v56, 3, s60\n\t"
        "ds_write_b64 v56, v[52:53]\n\t"
        "v_mfma_f32_16x16x32_bf16 v[32:35], a[152:155], v[108:111], v[32:35]\n\t"
        "buffer_load_dwordx4 v141, s[24:27], s36 offen offset:1024 lds\n\t"
        "v_and_b32 v56, 31, v157\n\t"
        "v_lshrrev_b32 v57, 4, v56\n\t"
        "v_mfma_f32_16x16x32_bf16 v[0:3], a[156:159], v[112:115], v[0:3]\n\t"
        "v_add_u32 v57, 1, v57\n\t"
        "v_cvt_f32_u32 v49, v57\n\t"
        "v_add_u32 v57, 1, v158\n\t"
        "v_mfma_f32_16x16x32_bf16 v[4:7], a[160:163], v[112:115], v[4:7]\n\t"
        "v_cvt_f32_u32 v50, v57\n\t"
        "s_min_u32 s46, s4, 2\n\t"
        "s_lshl_b32 s46, s46, 11\n\t"
        "v_mfma_f32_16x16x32_bf16 v[8:11], a[164:167], v[112:115], v[8:11]\n\t"
        "buffer_load_dwordx4 v142, s[24:27], s36 offen offset:2048 lds\n\t"
        "s_add_u32 m0, s37, 0\n\t"
        "s_add_u32 s36, s36, 6144\n\t"
        "v_mfma_f32_16x16x32_bf16 v[0:3], a[168:171], v[84:87], v[0:3]\n\t"
        "s_add_u32 s46, s46, 147488\n\t"
        "v_mad_u32_u24 v53, v56, 48, s46\n\t"
        "s_add_u32 s46, s4, 37\n\t"
        "v_mfma_f32_16x16x32_bf16 v[4:7], a[172:175], v[84:87], v[4:7]\n\t"
        "s_add_u32 s47, s4, 40\n\t"
        "v_readlane_b32 s46, v170, s46\n\t"
        "v_readlane_b32 s47, v170, s47\n\t"
        "v_mfma_f32_16x16x32_bf16 v[8:11], a[176:179], v[84:87], v[8:11]\n\t"
        "v_perm_b32 v56, v48, v48, s44\n\t"
        "v_perm_b32 v57, v48, v48, s45\n\t"
        "v_add_f32 v58, s46, v49\n\t"
        "v_mfma_f32_16x16x32_bf16 v[12:15], a[156:159], v[88:91], v[12:15]\n\t"
        "v_add_f32 v59, s47, v50\n\t"
        "v_add_f32 v56, v56, v58\n\t"
        "v_add_f32 v57, v57, v59\n\t"
        "v_mfma_f32_16x16x32_bf16 v[16:19], a[160:163], v[88:91], v[16:19]\n\t"
        "v_cvt_pk_bf16_f32 v52, v56, v57\n\t"
        "v_cvt_pk_bf16_f32 v58, v58, v59\n\t"
        "v_cmp_ne_u32 s[64:65], v52, v58\n\t"
        "v_mfma_f32_16x16x32_bf16 v[20:23], a[164:167], v[88:91], v[20:23]\n\t"
        "s_cmp_lt_u32 s4, 3\n\t"
        "s_cselect_b32 s64, s64, 0\n\t"
        "s_mov_b32 s65, 0\n\t"
        "v_mfma_f32_16x16x32_bf16 v[12:15], a[168:171], v[92:95], v[12:15]\n\t"
        "s_waitcnt vmcnt(9)\n\t"
        "s_barrier\n\t"
        "buffer_load_dwordx4 v140, s[24:27], s36 offen offset:0 lds\n\t"
        "ds_read_b64_tr_b16 a[180:181], v137 offset:24576\n\t"
        "ds_read_b64_tr_b16 a[182:183], v137 offset:25344\n\t"
        "v_mfma_f32_16x16x32_bf16 v[16:19], a[172:175], v[92:95], v[16:19]\n\t"
        "ds_read_b64_tr_b16 a[184:185], v138 offset:24576\n\t"
        "ds_read_b64_tr_b16 a[186:187], v138 offset:25344\n\t"
        "ds_read_b64_tr_b16 a[188:189], v139 offset:24576\n\t"
        "v_mfma_f32_16x16x32_bf16 v[20:23], a[176:179], v[92:95], v[20:23]\n\t"
        "ds_read_b64_tr_b16 a[190:191], v139 offset:25344\n\t"
        "ds_read_b128 v[104:107], v129 offset:0\n\t"
        "v_mbcnt_lo_u32_b32 v56, s64, v54\n\t"
        "s_waitcnt lgkmcnt(8)\n\t"
        "v_mfma_f32_16x16x32_bf16 v[24:27], a[156:159], v[96:99], v[24:27]\n\t"
        "buffer_load_dwordx4 v141, s[24:27], s36 offen offset:1024 lds\n\t"
        "ds_read_b128 v[108:111], v129 offset:8192\n\t"
        "v_mbcnt_hi_u32_b32 v56, s65, v56\n\t"
        "v_mfma_f32_16x16x32_bf16 v[28:31], a[160:163], v[96:99], v[28:31]\n\t"
        "ds_read_b128 v[112:115], v129 offset:16384\n\t"
        "ds_read_b64_tr_b16 a[192:193], v137 offset:30720\n\t"
        "s_bcnt1_i32_b64 s46, s[64:65]\n\t"
        "v_mfma_f32_16x16x32_bf16 v[32:35], a[164:167], v[96:99], v[32:35]\n\t"
        "ds_read_b64_tr_b16 a[194:195], v137 offset:31488\n\t"
        "ds_read_b64_tr_b16 a[196:197], v138 offset:30720\n\t"
        "v_add_u32 v54, s46, v54\n\t"
        "v_mfma_f32_16x16x32_bf16 v[24:27], a[168:171], v[100:103], v[24:27]\n\t"
        "buffer_load_dwordx4 v142, s[24:27], s36 offen offset:2048 lds\n\t"
        "ds_read_b64_tr_b16 a[198:199], v138 offset:31488\n\t"
        "v_readlane_b32 s40, v170, 21\n\t"
        "v_mfma_f32_16x16x32_bf16 v[28:31], a[172:175], v[100:103], v[28:31]\n\t"
        "ds_read_b64_tr_b16 a[200:201], v139 offset:30720\n\t"
        "ds_read_b64_tr_b16 a[202:203], v139 offset:31488\n\t"
        "v_readlane_b32 s43, v170, 9\n\t"
        "v_mfma_f32_16x16x32_bf16 v[32:35], a[176:179], v[100:103], v[32:35]\n\t"
        "ds_read_b128 v[84:87], v129 offset:8192\n\t"
        "ds_read_b128 v[88:91], v129 offset:16384\n\t"
        "s_add_u32 m0, s40, s35\n\t"
        "s_waitcnt lgkmcnt(10)\n\t"
        "v_mfma_f32_16x16x32_bf16 v[0:3], a[180:183], v[104:107], v[0:3]\n\t"
        "buffer_load_dwordx4 v131, s[16:19], s43 offen  lds\n\t"
        "ds_read_b128 v[92:95], v129 offset:24576\n\t"
        "v_readlane_b32 s40, v170, 22\n\t"
        "v_mfma_f32_16x16x32_bf16 v[4:7], a[184:187], v[104:107], v[4:7]\n\t"
        "ds_read_b64_tr_b16 a[204:205], v137 offset:36864\n\t"
        "ds_read_b64_tr_b16 a[206:207], v137 offset:37632\n\t"
        "v_readlane_b32 s43, v170, 10\n\t"
        "v_mfma_f32_16x16x32_bf16 v[8:11], a[188:191], v[104:107], v[8:11]\n\t"
        "ds_read_b64_tr_b16 a[208:209], v138 offset:36864\n\t"
        "ds_read_b64_tr_b16 a[210:211], v138 offset:37632\n\t"
        "s_add_u32 m0, s40, s35\n\t"
        "s_waitcnt lgkmcnt(11)\n\t"
        "v_mfma_f32_16x16x32_bf16 v[12:15], a[180:183], v[108:111], v[12:15]\n\t"
        "buffer_load_dwordx4 v132, s[16:19], s43 offen  lds\n\t"
        "ds_read_b64_tr_b16 a[212:213], v139 offset:36864\n\t"
        "v_readlane_b32 s40, v170, 23\n\t"
        "v_mfma_f32_16x16x32_bf16 v[16:19], a[184:187], v[108:111], v[16:19]\n\t"
        "ds_read_b64_tr_b16 a[214:215], v139 offset:37632\n\t"
        "ds_read_b128 v[96:99], v129 offset:64\n\t"
        "v_readlane_b32 s43, v170, 11\n\t"
        "v_mfma_f32_16x16x32_bf16 v[20:23], a[188:191], v[108:111], v[20:23]\n\t"
        "ds_read_b128 v[100:103], v129 offset:8256\n\t"
        "ds_read_b128 v[104:107], v129 offset:16448\n\t"
        "s_add_u32 m0, s40, s35\n\t"
        "v_mfma_f32_16x16x32_bf16 v[24:27], a[180:183], v[112:115], v[24:27]\n\t"
        "buffer_load_dwordx4 v133, s[16:19], s43 offen  lds\n\t"
        "ds_read_b64_tr_b16 a[216:217], v137 offset:43008\n\t"
        "s_add_u32 m0, s37, 12288\n\t"
        "v_mfma_f32_16x16x32_bf16 v[28:31], a[184:187], v[112:115], v[28:31]\n\t"
        "ds_read_b64_tr_b16 a[218:219], v137 offset:43776\n\t"
        "ds_read_b64_tr_b16 a[220:221], v138 offset:43008\n\t"
        "s_add_u32 s36, s36, 6144\n\t"
        "v_mfma_f32_16x16x32_bf16 v[32:35], a[188:191], v[112:115], v[32:35]\n\t"
        "ds_read_b64_tr_b16 a[222:223], v138 offset:43776\n\t"
        "ds_read_b64_tr_b16 a[224:225], v139 offset:43008\n\t"
        "v_min_u32 v56, 97, v56\n\t"
        "s_waitcnt lgkmcnt(11)\n\t"
        "v_mfma_f32_16x16x32_bf16 v[0:3], a[192:195], v[84:87], v[0:3]\n\t"
        "buffer_load_dwordx4 v140, s[24:27], s36 offen offset:0 lds\n\t"
        "ds_read_b64_tr_b16 a[226:227], v139 offset:43776\n\t"
        "v_cndmask_b32 v56, 0, v56, s[64:65]\n\t"
        "v_mfma_f32_16x16x32_bf16 v[4:7], a[196:199], v[84:87], v[4:7]\n\t"
        "ds_read_b128 v[108:111], v129 offset:8256\n\t"
        "v_lshl_add_u32 v56, v56, 3, s60\n\t"
        "v_mfma_f32_16x16x32_bf16 v[8:11], a[200:203], v[84:87], v[8:11]\n\t"
        "ds_read_b128 v[112:115], v129 offset:16448\n\t"
        "ds_write_b64 v56, v[52:53]\n\t"
        "v_mfma_f32_16x16x32_bf16 v[12:15], a[192:195], v[88:91], v[12:15]\n\t"
        "buffer_load_dwordx4 v141, s[24:27], s36 offen offset:1024 lds\n\t"
        "v_readlane_b32 s62, v170, 36\n\t"
        "v_add_u32 v123, 1, v157\n\t"
        "v_mfma_f32_16x16x32_bf16 v[16:19], a[196:199], v[88:91], v[16:19]\n\t"
        "ds_read_b128 v[84:87], v129 offset:24640\n\t"
        "s_mul_i32 s8, s4, 18432\n\t"
        "s_add_u32 s8, s8, 73728\n\t"
        "v_mfma_f32_16x16x32_bf16 v[20:23], a[200:203], v[88:91], v[20:23]\n\t"
        "v_mov_b32 v126, s62\n\t"
        "v_mul_u32_u24 v61, 192, v157\n\t"
        "v_lshlrev_b32 v62, 3, v157\n\t"
        "v_mfma_f32_16x16x32_bf16 v[24:27], a[192:195], v[92:95], v[24:27]\n\t"
        "buffer_load_dwordx4 v142, s[24:27], s36 offen offset:2048 lds\n\t"
        "v_and_b32 v62, 32, v62\n\t"
        "v_add3_u32 v124, v61, v62, s8\n\t"
        "v_mfma_f32_16x16x32_bf16 v[28:31], a[196:199], v[92:95], v[28:31]\n\t"
        "v_lshlrev_b32 v186, 4, v159\n\t"
        "v_mul_u32_u24 v61, 192, v158\n\t"
        "v_xor_b32 v62, v186, v62\n\t"
        "v_mfma_f32_16x16x32_bf16 v[32:35], a[200:203], v[92:95], v[32:35]\n\t"
        "v_add3_u32 v122, v61, v62, s8\n\t"
        "s_add_u32 s8, s35, 157696\n\t"
        "v_add_u32 v125, s8, v140\n\t"
        "s_waitcnt lgkmcnt(3)\n\t"
        "v_mfma_f32_16x16x32_bf16 v[0:3], a[204:207], v[96:99], v[0:3]\n\t"
        "s_waitcnt vmcnt(9)\n\t"
        "s_barrier\n\t"
        "ds_read_b64_tr_b16 a[228:229], v137 offset:49152\n\t"
        "ds_read_b64_tr_b16 a[230:231], v137 offset:49920\n\t"
        "v_mfma_f32_16x16x32_bf16 v[4:7], a[208:211], v[96:99], v[4:7]\n\t"
        "ds_read_b64_tr_b16 a[232:233], v138 offset:49152\n\t"
        "ds_read_b64_tr_b16 a[234:235], v138 offset:49920\n\t"
        "v_readfirstlane_b32 s61, v54\n\t"
        "v_mfma_f32_16x16x32_bf16 v[8:11], a[212:215], v[96:99], v[8:11]\n\t"
        "ds_read_b64_tr_b16 a[236:237], v139 offset:49152\n\t"
        "ds_read_b64_tr_b16 a[238:239], v139 offset:49920\n\t"
        "s_sub_u32 s61, s61, 1\n\t"
        "v_mfma_f32_16x16x32_bf16 v[12:15], a[204:207], v[100:103], v[12:15]\n\t"
        "ds_read_b128 v[88:91], v129 offset:128\n\t"
        "ds_read_b128 v[92:95], v129 offset:8320\n\t"
        "v_mov_b32 v60, v155\n\t"
        "v_mfma_f32_16x16x32_bf16 v[16:19], a[208:211], v[100:103], v[16:19]\n\t"
        "ds_read_b128 v[96:99], v129 offset:16512\n\t"
        "ds_read_b64_tr_b16 a[240:241], v137 offset:55296\n\t"
        "v_and_b32 v36, s54, v36\n\t"
        "v_mfma_f32_16x16x32_bf16 v[20:23], a[212:215], v[100:103], v[20:23]\n\t"
        "ds_read_b64_tr_b16 a[242:243], v137 offset:56064\n\t"
        "ds_read_b64_tr_b16 a[244:245], v138 offset:55296\n\t"
        "v_pk_max_u16 v60, v60, v36\n\t"
        "v_mfma_f32_16x16x32_bf16 v[24:27], a[204:207], v[104:107], v[24:27]\n\t"
        "ds_read_b64_tr_b16 a[246:247], v138 offset:56064\n\t"
        "ds_read_b64_tr_b16 a[248:249], v139 offset:55296\n\t"
        "v_and_b32 v37, s54, v37\n\t"
        "v_mfma_f32_16x16x32_bf16 v[28:31], a[208:211], v[104:107], v[28:31]\n\t"
        "ds_read_b64_tr_b16 a[250:251], v139 offset:56064\n\t"
        "ds_read_b128 v[100:103], v129 offset:8320\n\t"
        "v_pk_max_u16 v60, v60, v37\n\t"
        "v_mfma_f32_16x16x32_bf16 v[32:35], a[212:215], v[104:107], v[32:35]\n\t"
        "ds_read_b64_tr_b16 a[252:253], v181 offset:0\n\t"
        "ds_read_b64_tr_b16 a[254:255], v181 offset:768\n\t"
        "v_and_b32 v38, s54, v38\n\t"
        "v_mfma_f32_16x16x32_bf16 v[0:3], a[216:219], v[108:111], v[0:3]\n\t"
        "ds_read_b128 v[104:107], v129 offset:16512\n\t"
        "ds_read_b64_tr_b16 v[188:189], v182 offset:0\n\t"
        "v_pk_max_u16 v60, v60, v38\n\t"
        "v_mfma_f32_16x16x32_bf16 v[4:7], a[220:223], v[108:111], v[4:7]\n\t"
        "ds_read_b64_tr_b16 v[190:191], v182 offset:768\n\t"
        "ds_read_b64_tr_b16 v[192:193], v183 offset:0\n\t"
        "v_and_b32 v39, s54, v39\n\t"
        "v_mfma_f32_16x16x32_bf16 v[8:11], a[224:227], v[108:111], v[8:11]\n\t"
        "ds_read_b64_tr_b16 v[194:195], v183 offset:768\n\t"
        "ds_read_b64_tr_b16 v[196:197], v181 offset:6144\n\t"
        "v_pk_max_u16 v60, v60, v39\n\t"
        "s_waitcnt lgkmcnt(13)\n\t"
        "v_mfma_f32_16x16x32_bf16 v[12:15], a[216:219], v[112:115], v[12:15]\n\t"
        "ds_read_b128 v[108:111], v128 offset:0\n\t"
        "ds_read_b64_tr_b16 v[198:199], v181 offset:6912\n\t"
        "v_and_b32 v40, s54, v40\n\t"
        "v_mfma_f32_16x16x32_bf16 v[16:19], a[220:223], v[112:115], v[16:19]\n\t"
        "ds_read_b64_tr_b16 v[200:201], v182 offset:6144\n\t"
        "ds_read_b64_tr_b16 v[202:203], v182 offset:6912\n\t"
        "v_pk_max_u16 v60, v60, v40\n\t"
        "v_mfma_f32_16x16x32_bf16 v[20:23], a[224:227], v[112:115], v[20:23]\n\t"
        "ds_read_b64_tr_b16 v[204:205], v183 offset:6144\n\t"
        "ds_read_b64_tr_b16 v[206:207], v183 offset:6912\n\t"
        "v_and_b32 v41, s54, v41\n\t"
        "v_mfma_f32_16x16x32_bf16 v[24:27], a[216:219], v[84:87], v[24:27]\n\t"
        "ds_read_b128 v[112:115], v130 offset:8192\n\t"
        "s_cmp_gt_u32 s61, 96\n\t"
        "s_cselect_b32 s8, 1, 0\n\t"
        "v_mfma_f32_16x16x32_bf16 v[28:31], a[220:223], v[84:87], v[28:31]\n\t"
        "s_or_b32 s56, s56, s8\n\t"
        "s_min_u32 s61, s61, 96\n\t"
        "v_min_u32 v78, s61, v123\n\t"
        "v_mfma_f32_16x16x32_bf16 v[32:35], a[224:227], v[84:87], v[32:35]\n\t"
        "v_lshl_add_u32 v78, v78, 3, s60\n\t"
        "ds_read_b64 v[62:63], v78\n\t"
        "v_cmp_ge_u32 s[66:67], s61, v123\n\t"
        "v_mfma_f32_16x16x32_bf16 v[0:3], a[228:231], v[88:91], v[0:3]\n\t"
        "ds_read_b128 v[84:87], v128 offset:8192\n\t"
        "s_mov_b32 s46, 0x07060302\n\t"
        "v_pk_max_u16 v60, v60, v41\n\t"
        "v_mfma_f32_16x16x32_bf16 v[4:7], a[232:235], v[88:91], v[4:7]\n\t"
        "v_and_b32 v42, s54, v42\n\t"
        "v_pk_max_u16 v60, v60, v42\n\t"
        "v_and_b32 v43, s54, v43\n\t"
        "v_mfma_f32_16x16x32_bf16 v[8:11], a[236:239], v[88:91], v[8:11]\n\t"
        "v_pk_max_u16 v60, v60, v43\n\t"
        "v_and_b32 v44, s54, v44\n\t"
        "v_pk_max_u16 v60, v60, v44\n\t"
        "v_mfma_f32_16x16x32_bf16 v[12:15], a[228:231], v[92:95], v[12:15]\n\t"
        "ds_read_b128 v[88:91], v130 offset:16384\n\t"
        "v_cmp_ne_u32 s[12:13], v60, v155\n\t"
        "s_lshl_b32 s9, s62, 5\n\t"
        "v_mfma_f32_16x16x32_bf16 v[16:19], a[232:235], v[92:95], v[16:19]\n\t"
        "s_or_b64 s[56:57], s[56:57], s[12:13]\n\t"
        "s_lshr_b32 s10, s4, 1\n\t"
        "s_sub_u32 s11, s9, 4096\n\t"
        "v_mfma_f32_16x16x32_bf16 v[20:23], a[236:239], v[92:95], v[20:23]\n\t"
        "s_mul_i32 s11, s11, s10\n\t"
        "v_add_u32 v172, s11, v128\n\t"
        "v_add_u32 v173, s11, v129\n\t"
        "v_mfma_f32_16x16x32_bf16 v[24:27], a[228:231], v[96:99], v[24:27]\n\t"
        "ds_read_b128 v[92:95], v129 offset:24704\n\t"
        "v_add_u32 v174, s11, v130\n\t"
        "s_add_u32 s11, s11, s9\n\t"
        "v_mfma_f32_16x16x32_bf16 v[28:31], a[232:235], v[96:99], v[28:31]\n\t"
        "s_waitcnt lgkmcnt(3)\n\t"
        "v_and_b32 v78, 0xfffff800, v63\n\t"
        "ds_read_b128 v[64:67], v78 offset:1536\n\t"
        "v_lshlrev_b32 v72, 16, v62\n\t"
        "v_mfma_f32_16x16x32_bf16 v[32:35], a[236:239], v[96:99], v[32:35]\n\t"
        "v_and_b32 v73, 0xffff0000, v62\n\t"
        "v_med3_f32 v72, v72, 0, s63\n\t"
        "v_med3_f32 v73, v73, 0, s63\n\t"
        "v_mfma_f32_16x16x32_bf16 v[0:3], a[240:243], v[100:103], v[0:3]\n\t"
        "ds_read_b128 v[96:99], v128 offset:16384\n\t"
        "s_mov_b64 exec, s[66:67]\n\t"
        "ds_write_b32 v63, v124\n\t"
        "s_mov_b64 exec, -1\n\t"
        "v_mfma_f32_16x16x32_bf16 v[4:7], a[244:247], v[100:103], v[4:7]\n\t"
        "v_perm_b32 v62, v73, v72, s46\n\t"
        "v_fract_f32 v74, v72\n\t"
        "v_fract_f32 v75, v73\n\t"
        "v_mfma_f32_16x16x32_bf16 v[8:11], a[248:251], v[100:103], v[8:11]\n\t"
        "v_cvt_u32_f32 v76, v72\n\t"
        "v_cvt_u32_f32 v77, v73\n\t"
        "v_cmp_neq_f32 s[64:65], 0, v74\n\t"
        "v_mfma_f32_16x16x32_bf16 v[12:15], a[240:243], v[104:107], v[12:15]\n\t"
        "ds_read_b128 v[100:103], v130 offset:24576\n\t"
        "v_cmp_neq_f32 s[44:45], 0, v75\n\t"
        "v_sub_f32 v78, 1.0, v74\n\t"
        "v_mfma_f32_16x16x32_bf16 v[16:19], a[244:247], v[104:107], v[16:19]\n\t"
        "v_sub_f32 v79, 1.0, v75\n\t"
        "v_cvt_pk_bf16_f32 v79, v78, v79\n\t"
        "v_lshlrev_b32 v78, 16, v79\n\t"
        "v_mfma_f32_16x16x32_bf16 v[20:23], a[248:251], v[104:107], v[20:23]\n\t"
        "s_waitcnt vmcnt(0)\n\t"
        "s_barrier\n\t"
        "ds_read_b64_tr_b16 v[208:209], v137 offset:0\n\t"
        "ds_read_b64_tr_b16 v[210:211], v137 offset:768\n\t"
        "v_mfma_f32_16x16x32_bf16 v[0:3], a[252:255], v[108:111], v[0:3]\n\t"
        "ds_read_b64_tr_b16 v[212:213], v138 offset:0\n\t"
        "ds_read_b64_tr_b16 v[214:215], v138 offset:768\n\t"
        "v_and_b32 v79, 0xffff0000, v79\n\t"
        "v_mfma_f32_16x16x32_bf16 v[4:7], v[188:191], v[108:111], v[4:7]\n\t"
        "ds_read_b64_tr_b16 v[216:217], v139 offset:0\n\t"
        "ds_read_b64_tr_b16 v[218:219], v139 offset:768\n\t"
        "v_mul_f32 v80, v78, v79\n\t"
        "v_mfma_f32_16x16x32_bf16 v[8:11], v[192:195], v[108:111], v[8:11]\n\t"
        "ds_read_b128 v[104:107], v128 offset:64\n\t"
        "ds_read_b64_tr_b16 v[220:221], v137 offset:6144\n\t"
        "v_mul_f32 v78, v78, v75\n\t"
        "v_mfma_f32_16x16x32_bf16 v[0:3], v[196:199], v[112:115], v[0:3]\n\t"
        "ds_read_b128 v[108:111], v128 offset:8256\n\t"
        "ds_read_b64_tr_b16 v[222:223], v137 offset:6912\n\t"
        "v_mul_f32 v79, v74, v79\n\t"
        "v_mfma_f32_16x16x32_bf16 v[4:7], v[200:203], v[112:115], v[4:7]\n\t"
        "ds_read_b64_tr_b16 v[224:225], v138 offset:6144\n\t"
        "ds_read_b64_tr_b16 v[226:227], v138 offset:6912\n\t"
        "v_mul_f32 v81, v74, v75\n\t"
        "v_mfma_f32_16x16x32_bf16 v[8:11], v[204:207], v[112:115], v[8:11]\n\t"
        "ds_read_b64_tr_b16 v[228:229], v139 offset:6144\n\t"
        "ds_read_b64_tr_b16 v[230:231], v139 offset:6912\n\t"
        "v_cndmask_b32 v78, v79, v78, s[44:45]\n\t"
        "s_waitcnt lgkmcnt(13)\n\t"
        "v_mfma_f32_16x16x32_bf16 v[12:15], a[252:255], v[84:87], v[12:15]\n\t"
        "ds_read_b128 v[112:115], v130 offset:8256\n\t"
        "ds_read_b64_tr_b16 v[232:233], v137 offset:12288\n\t"
        "v_cndmask_b32 v79, 0, v79, s[44:45]\n\t"
        "v_mfma_f32_16x16x32_bf16 v[16:19], v[188:191], v[84:87], v[16:19]\n\t"
        "ds_read_b64_tr_b16 v[234:235], v137 offset:13056\n\t"
        "ds_read_b64_tr_b16 v[236:237], v138 offset:12288\n\t"
        "v_cvt_pk_bf16_f32 v70, v80, v78\n\t"
        "v_mfma_f32_16x16x32_bf16 v[20:23], v[192:195], v[84:87], v[20:23]\n\t"
        "ds_read_b64_tr_b16 v[238:239], v138 offset:13056\n\t"
        "ds_read_b64_tr_b16 v[240:241], v139 offset:12288\n\t"
        "v_cvt_pk_bf16_f32 v71, v79, v81\n\t"
        "v_mfma_f32_16x16x32_bf16 v[12:15], v[196:199], v[88:91], v[12:15]\n\t"
        "ds_read_b128 v[84:87], v130 offset:16448\n\t"
        "ds_read_b64_tr_b16 v[242:243], v139 offset:13056\n\t"
        "v_pk_max_u16 v78, v62, v64\n\t"
        "v_mfma_f32_16x16x32_bf16 v[16:19], v[200:203], v[88:91], v[16:19]\n\t"
        "ds_read_b64_tr_b16 v[244:245], v137 offset:18432\n\t"
        "ds_read_b64_tr_b16 v[246:247], v137 offset:19200\n\t"
        "v_pk_min_u16 v78, v78, v65\n\t"
        "v_mfma_f32_16x16x32_bf16 v[20:23], v[204:207], v[88:91], v[20:23]\n\t"
        "ds_read_b64_tr_b16 v[248:249], v138 offset:18432\n\t"
        "ds_read_b64_tr_b16 v[250:251], v138 offset:19200\n\t"
        "v_cmp_ne_u32 vcc, v78, v62\n\t"
        "v_mfma_f32_16x16x32_bf16 v[24:27], a[240:243], v[92:95], v[24:27]\n\t"
        "ds_read_b128 v[88:91], v128 offset:128\n\t"
        "ds_read_b64_tr_b16 v[252:253], v139 offset:18432\n\t"
        "v_sub_u32 v78, v77, v67\n\t"
        "v_mfma_f32_16x16x32_bf16 v[28:31], a[244:247], v[92:95], v[28:31]\n\t"
        "ds_read_b64_tr_b16 v[254:255], v139 offset:19200\n\t"
        "v_mad_u32_u24 v79, v77, 6, v66\n\t"
        "v_mad_u32_u24 v79, v76, s62, v79\n\t"
        "v_mfma_f32_16x16x32_bf16 v[32:35], a[248:251], v[92:95], v[32:35]\n\t"
        "s_and_b64 vcc, vcc, s[66:67]\n\t"
        "s_or_b64 s[56:57], s[56:57], vcc\n\t"
        "v_bfe_u32 v80, v78, 2, 1\n\t"
        "v_mfma_f32_16x16x32_bf16 v[24:27], a[252:255], v[96:99], v[24:27]\n\t"
        "ds_read_b128 v[92:95], v130 offset:8320\n\t"
        "v_or_b32 v80, v80, v79\n\t"
        "v_add_u32 v78, 1, v78\n\t"
        "v_mfma_f32_16x16x32_bf16 v[28:31], v[188:191], v[96:99], v[28:31]\n\t"
        "v_bfe_u32 v78, v78, 2, 1\n\t"
        "v_add3_u32 v78, v79, v78, 6\n\t"
        "v_cndmask_b32 v78, v80, v78, s[44:45]\n\t"
        "v_mfma_f32_16x16x32_bf16 v[32:35], v[192:195], v[96:99], v[32:35]\n\t"
        "v_cndmask_b32 v79, 0, v126, s[64:65]\n\t"
        "v_add_u32 v81, v80, v79\n\t"
        "v_add_u32 v82, v78, v79\n\t"
        "v_mfma_f32_16x16x32_bf16 v[24:27], v[196:199], v[100:103], v[24:27]\n\t"
        "ds_read_b128 v[96:99], v128 offset:8320\n\t"
        "v_cndmask_b32 v78, v81, v78, s[44:45]\n\t"
        "v_lshl_or_b32 v68, v78, 16, v80\n\t"
        "v_mfma_f32_16x16x32_bf16 v[28:31], v[200:203], v[100:103], v[28:31]\n\t"
        "v_lshl_or_b32 v69, v82, 16, v81\n\t"
        "ds_write_b128 v125, v[68:71]\n\t"
        "v_mfma_f32_16x16x32_bf16 v[32:35], v[204:207], v[100:103], v[32:35]\n\t"
        "v_add_u32 v175, s11, v128\n\t"
        "v_add_u32 v176, s11, v129\n\t"
        "v_add_u32 v177, s11, v130\n\t"
        "s_waitcnt lgkmcnt(3)\n\t"
        "v_mfma_f32_16x16x32_bf16 v[0:3], v[208:211], v[104:107], v[0:3]\n\t"
        "ds_read_b128 v[100:103], v130 offset:16512\n\t"
        "s_add_u32 s11, s11, s9\n\t"
        "v_add_u32 v178, s11, v128\n\t"
        "v_mfma_f32_16x16x32_bf16 v[4:7], v[212:215], v[104:107], v[4:7]\n\t"
        "v_add_u32 v179, s11, v129\n\t"
        "v_add_u32 v180, s11, v130\n\t"
        "s_bfe_u32 s9, s7, 0x20010\n\t"
        "v_mfma_f32_16x16x32_bf16 v[8:11], v[216:219], v[104:107], v[8:11]\n\t"
        "s_cmp_eq_u32 s9, 2\n\t"
        "s_cselect_b64 exec, -1, 0\n\t"
        "v_swap_b32 v173, v175\n\t"
        "v_swap_b32 v174, v178\n\t"
        "v_swap_b32 v177, v179\n\t"
        "s_mov_b64 exec, -1\n\t"
        "v_mfma_f32_16x16x32_bf16 v[12:15], v[208:211], v[108:111], v[12:15]\n\t"
        "ds_read_b128 v[104:107], v128 offset:16448\n\t"
        "s_mul_i32 s10, s10, 768\n\t"
        "s_add_u32 s10, s10, 147456\n\t"
        "v_mfma_f32_16x16x32_bf16 v[16:19], v[212:215], v[108:111], v[16:19]\n\t"
        "v_mad_u32_u24 v134, v158, 48, s10\n\t"
        "s_lshl_b32 s10, s49, 11\n\t"
        "v_add_u32 v135, s10, v134\n\t"
        "v_mfma_f32_16x16x32_bf16 v[20:23], v[216:219], v[108:111], v[20:23]\n\t"
        "v_subrev_u32 v136, s10, v134\n\t"
        "v_readlane_b32 s5, v170, 46\n\t"
        "s_add_u32 s9, s49, 47\n\t"
        "v_mfma_f32_16x16x32_bf16 v[0:3], v[220:223], v[112:115], v[0:3]\n\t"
        "ds_read_b128 v[108:111], v130 offset:24640\n\t"
        "s_sub_u32 s10, 48, s49\n\t"
        "v_readlane_b32 s6, v170, s9\n\t"
        "v_mfma_f32_16x16x32_bf16 v[4:7], v[224:227], v[112:115], v[4:7]\n\t"
        "v_readlane_b32 s3, v170, s10\n\t"
        "v_mfma_f32_16x16x32_bf16 v[8:11], v[228:231], v[112:115], v[8:11]\n\t"
        "s_barrier\n\t"
        "s_cmp_eq_u32 s61, 0\n\t"
        "s_cbranch_scc1 32f\n\t"
        "s_mov_b32 s47, s61\n\t"
        "s_mov_b32 s40, 4\n\t"
        "s_waitcnt lgkmcnt(0)\n\t"
        "s_nop 7\n\t"
        "30:\n\t"
        "ds_read_b128 v[160:163], v121\n\t"
        "s_waitcnt lgkmcnt(0)\n\t"
        "v_lshlrev_b32_sdwa v151, 5, v160 dst_sel:DWORD dst_unused:UNUSED_PAD src0_sel:DWORD src1_sel:WORD_0\n\t"
        "v_lshlrev_b32_sdwa v187, 5, v160 dst_sel:DWORD dst_unused:UNUSED_PAD src0_sel:DWORD src1_sel:WORD_1\n\t"
        "v_xor_b32 v151, v116, v151\n\t"
        "v_xor_b32 v187, v116, v187\n\t"
        "ds_read_b64_tr_b16 v[36:37], v151 offset:0\n\t"
        "v_perm_b32 v164, v162, v162, v117\n\t"
        "ds_read_b64_tr_b16 v[38:39], v187 offset:0\n\t"
        "v_perm_b32 v165, v162, v162, v118\n\t"
        "ds_read_b64_tr_b16 v[40:41], v151 offset:8\n\t"
        "v_perm_b32 v166, v162, v162, v119\n\t"
        "ds_read_b64_tr_b16 v[42:43], v187 offset:8\n\t"
        "v_perm_b32 v167, v162, v162, v120\n\t"
        "ds_read_b64_tr_b16 v[44:45], v151 offset:64\n\t"
        "v_cmp_ne_u32 vcc, 0, v163\n\t"
        "ds_read_b64_tr_b16 v[46:47], v187 offset:64\n\t"
        "ds_read_b64_tr_b16 v[48:49], v151 offset:72\n\t"
        "ds_read_b64_tr_b16 v[50:51], v187 offset:72\n\t"
        "ds_read_b64_tr_b16 v[52:53], v151 offset:128\n\t"
        "ds_read_b64_tr_b16 v[54:55], v187 offset:128\n\t"
        "ds_read_b64_tr_b16 v[56:57], v151 offset:136\n\t"
        "ds_read_b64_tr_b16 v[58:59], v187 offset:136\n\t"
        "s_waitcnt lgkmcnt(10)\n\t"
        "v_mfma_f32_16x16x32_bf16 v[60:63], v[36:39], v[164:167], 0\n\t"
        "s_waitcnt lgkmcnt(8)\n\t"
        "v_mfma_f32_16x16x32_bf16 v[64:67], v[40:43], v[164:167], 0\n\t"
        "s_waitcnt lgkmcnt(6)\n\t"
        "v_mfma_f32_16x16x32_bf16 v[68:71], v[44:47], v[164:167], 0\n\t"
        "s_waitcnt lgkmcnt(4)\n\t"
        "v_mfma_f32_16x16x32_bf16 v[72:75], v[48:51], v[164:167], 0\n\t"
        "s_waitcnt lgkmcnt(2)\n\t"
        "v_mfma_f32_16x16x32_bf16 v[76:79], v[52:55], v[164:167], 0\n\t"
        "s_waitcnt lgkmcnt(0)\n\t"
        "v_mfma_f32_16x16x32_bf16 v[80:83], v[56:59], v[164:167], 0\n\t"
        "s_cbranch_vccz 31f\n\t"
        "v_lshlrev_b32_sdwa v151, 5, v161 dst_sel:DWORD dst_unused:UNUSED_PAD src0_sel:DWORD src1_sel:WORD_0\n\t"
        "v_lshlrev_b32_sdwa v187, 5, v161 dst_sel:DWORD dst_unused:UNUSED_PAD src0_sel:DWORD src1_sel:WORD_1\n\t"
        "v_xor_b32 v151, v116, v151\n\t"
        "v_xor_b32 v187, v116, v187\n\t"
        "ds_read_b64_tr_b16 v[36:37], v151 offset:0\n\t"
        "v_perm_b32 v164, v163, v163, v117\n\t"
        "ds_read_b64_tr_b16 v[38:39], v187 offset:0\n\t"
        "v_perm_b32 v165, v163, v163, v118\n\t"
        "ds_read_b64_tr_b16 v[40:41], v151 offset:8\n\t"
        "v_perm_b32 v166, v163, v163, v119\n\t"
        "ds_read_b64_tr_b16 v[42:43], v187 offset:8\n\t"
        "v_perm_b32 v167, v163, v163, v120\n\t"
        "ds_read_b64_tr_b16 v[44:45], v151 offset:64\n\t"
        "ds_read_b64_tr_b16 v[46:47], v187 offset:64\n\t"
        "ds_read_b64_tr_b16 v[48:49], v151 offset:72\n\t"
        "ds_read_b64_tr_b16 v[50:51], v187 offset:72\n\t"
        "ds_read_b64_tr_b16 v[52:53], v151 offset:128\n\t"
        "ds_read_b64_tr_b16 v[54:55], v187 offset:128\n\t"
        "ds_read_b64_tr_b16 v[56:57], v151 offset:136\n\t"
        "ds_read_b64_tr_b16 v[58:59], v187 offset:136\n\t"
        "s_waitcnt lgkmcnt(10)\n\t"
        "v_mfma_f32_16x16x32_bf16 v[60:63], v[36:39], v[164:167], v[60:63]\n\t"
        "s_waitcnt lgkmcnt(8)\n\t"
        "v_mfma_f32_16x16x32_bf16 v[64:67], v[40:43], v[164:167], v[64:67]\n\t"
        "s_waitcnt lgkmcnt(6)\n\t"
        "v_mfma_f32_16x16x32_bf16 v[68:71], v[44:47], v[164:167], v[68:71]\n\t"
        "s_waitcnt lgkmcnt(4)\n\t"
        "v_mfma_f32_16x16x32_bf16 v[72:75], v[48:51], v[164:167], v[72:75]\n\t"
        "s_waitcnt lgkmcnt(2)\n\t"
        "v_mfma_f32_16x16x32_bf16 v[76:79], v[52:55], v[164:167], v[76:79]\n\t"
        "s_waitcnt lgkmcnt(0)\n\t"
        "v_mfma_f32_16x16x32_bf16 v[80:83], v[56:59], v[164:167], v[80:83]\n\t"
        "31:\n\t"
        "v_cvt_pk_bf16_f32 v36, v60, v61\n\t"
        "v_cvt_pk_bf16_f32 v37, v62, v63\n\t"
        "v_cvt_pk_bf16_f32 v38, v64, v65\n\t"
        "v_cvt_pk_bf16_f32 v39, v66, v67\n\t"
        "ds_write_b128 v122, v[36:39] offset:0\n\t"
        "v_cvt_pk_bf16_f32 v40, v68, v69\n\t"
        "v_cvt_pk_bf16_f32 v41, v70, v71\n\t"
        "v_cvt_pk_bf16_f32 v42, v72, v73\n\t"
        "v_cvt_pk_bf16_f32 v43, v74, v75\n\t"
        "ds_write_b128 v122, v[40:43] offset:64\n\t"
        "v_cvt_pk_bf16_f32 v44, v76, v77\n\t"
        "v_cvt_pk_bf16_f32 v45, v78, v79\n\t"
        "v_cvt_pk_bf16_f32 v46, v80, v81\n\t"
        "v_cvt_pk_bf16_f32 v47, v82, v83\n\t"
        "ds_write_b128 v122, v[44:47] offset:128\n\t"
        "v_add_u32 v121, 256, v121\n\t"
        "v_add_u32 v122, 3072, v122\n\t"
        "s_sub_i32 s47, s47, 16\n\t"
        "s_cmp_le_i32 s47, 0\n\t"
        "s_cbranch_scc1 32f\n\t"
        "s_sub_u32 s40, s40, 1\n\t"
        "s_cmp_lg_u32 s40, 0\n\t"
        "s_cbranch_scc1 30b\n\t"
        "v_add_u32 v123, 64, v123\n\t"
        "v_add_u32 v124, 12288, v124\n\t"
        "v_subrev_u32 v121, 1024, v121\n\t"
        "v_min_u32 v78, s61, v123\n\t"
        "v_lshl_add_u32 v78, v78, 3, s60\n\t"
        "ds_read_b64 v[62:63], v78\n\t"
        "v_cmp_ge_u32 s[66:67], s61, v123\n\t"
        "s_mov_b32 s46, 0x07060302\n\t"
        "s_waitcnt lgkmcnt(0)\n\t"
        "v_and_b32 v78, 0xfffff800, v63\n\t"
        "ds_read_b128 v[64:67], v78 offset:1536\n\t"
        "v_lshlrev_b32 v72, 16, v62\n\t"
        "v_and_b32 v73, 0xffff0000, v62\n\t"
        "v_med3_f32 v72, v72, 0, s63\n\t"
        "v_med3_f32 v73, v73, 0, s63\n\t"
        "s_mov_b64 exec, s[66:67]\n\t"
        "ds_write_b32 v63, v124\n\t"
        "s_mov_b64 exec, -1\n\t"
        "v_perm_b32 v62, v73, v72, s46\n\t"
        "v_fract_f32 v74, v72\n\t"
        "v_fract_f32 v75, v73\n\t"
        "v_cvt_u32_f32 v76, v72\n\t"
        "v_cvt_u32_f32 v77, v73\n\t"
        "v_cmp_neq_f32 s[64:65], 0, v74\n\t"
        "v_cmp_neq_f32 s[44:45], 0, v75\n\t"
        "v_sub_f32 v78, 1.0, v74\n\t"
        "v_sub_f32 v79, 1.0, v75\n\t"
        "v_cvt_pk_bf16_f32 v79, v78, v79\n\t"
        "v_lshlrev_b32 v78, 16, v79\n\t"
        "v_and_b32 v79, 0xffff0000, v79\n\t"
        "v_mul_f32 v80, v78, v79\n\t"
        "v_mul_f32 v78, v78, v75\n\t"
        "v_mul_f32 v79, v74, v79\n\t"
        "v_mul_f32 v81, v74, v75\n\t"
        "v_cndmask_b32 v78, v79, v78, s[44:45]\n\t"
        "v_cndmask_b32 v79, 0, v79, s[44:45]\n\t"
        "v_cvt_pk_bf16_f32 v70, v80, v78\n\t"
        "v_cvt_pk_bf16_f32 v71, v79, v81\n\t"
        "s_waitcnt lgkmcnt(1)\n\t"
        "v_pk_max_u16 v78, v62, v64\n\t"
        "v_pk_min_u16 v78, v78, v65\n\t"
        "v_cmp_ne_u32 vcc, v78, v62\n\t"
        "v_sub_u32 v78, v77, v67\n\t"
        "v_mad_u32_u24 v79, v77, 6, v66\n\t"
        "v_mad_u32_u24 v79, v76, s62, v79\n\t"
        "s_and_b64 vcc, vcc, s[66:67]\n\t"
        "s_or_b64 s[56:57], s[56:57], vcc\n\t"
        "v_bfe_u32 v80, v78, 2, 1\n\t"
        "v_or_b32 v80, v80, v79\n\t"
        "v_add_u32 v78, 1, v78\n\t"
        "v_bfe_u32 v78, v78, 2, 1\n\t"
        "v_add3_u32 v78, v79, v78, 6\n\t"
        "v_cndmask_b32 v78, v80, v78, s[44:45]\n\t"
        "v_cndmask_b32 v79, 0, v126, s[64:65]\n\t"
        "v_add_u32 v81, v80, v79\n\t"
        "v_add_u32 v82, v78, v79\n\t"
        "v_cndmask_b32 v78, v81, v78, s[44:45]\n\t"
        "v_lshl_or_b32 v68, v78, 16, v80\n\t"
        "v_lshl_or_b32 v69, v82, 16, v81\n\t"
        "ds_write_b128 v125, v[68:71]\n\t"
        "s_mov_b32 s40, 4\n\t"
        "s_waitcnt lgkmcnt(0)\n\t"
        "s_nop 1\n\t"
        "s_branch 30b\n\t"
        "32:\n\t"
        "s_or_b32 s8, s56, s57\n\t"
        "v_mov_b32 v56, s8\n\t"
        "ds_write_b32 v153, v56\n\t"
        "s_waitcnt lgkmcnt(0)\n\t"
        "s_barrier\n\t"
        "v_mfma_f32_16x16x32_bf16 v[12:15], v[220:223], v[84:87], v[12:15]\n\t"
        "ds_read_b128 v[72:75], v134 offset:0\n\t"
        "v_mfma_f32_16x16x32_bf16 v[16:19], v[224:227], v[84:87], v[16:19]\n\t"
        "ds_read_b128 v[76:79], v134 offset:16\n\t"
        "v_mfma_f32_16x16x32_bf16 v[20:23], v[228:231], v[84:87], v[20:23]\n\t"
        "ds_read_b32 v80, v134 offset:32\n\t"
        "v_mfma_f32_16x16x32_bf16 v[0:3], v[232:235], v[88:91], v[0:3]\n\t"
        "ds_read_b128 v[112:115], v128 offset:16512\n\t"
        "v_mfma_f32_16x16x32_bf16 v[4:7], v[236:239], v[88:91], v[4:7]\n\t"
        "ds_read_b128 v[84:87], v130 offset:24704\n\t"
        "v_mfma_f32_16x16x32_bf16 v[8:11], v[240:243], v[88:91], v[8:11]\n\t"
        "ds_read_b128 v[164:167], v154\n\t"
        "v_mfma_f32_16x16x32_bf16 v[0:3], v[244:247], v[92:95], v[0:3]\n\t"
        "v_mfma_f32_16x16x32_bf16 v[4:7], v[248:251], v[92:95], v[4:7]\n\t"
        "v_mfma_f32_16x16x32_bf16 v[8:11], v[252:255], v[92:95], v[8:11]\n\t"
        "s_waitcnt lgkmcnt(5)\n\t"
        "v_xor_b32 v72, v186, v72\n\t"
        "v_add_u32 v151, s5, v172\n\t"
        "v_mfma_f32_16x16x32_bf16 v[12:15], v[232:235], v[96:99], v[12:15]\n\t"
        "v_max_u32 v151, v151, v72\n\t"
        "v_xor_b32 v73, v186, v73\n\t"
        "v_mfma_f32_16x16x32_bf16 v[16:19], v[236:239], v[96:99], v[16:19]\n\t"
        "v_add_u32 v187, s5, v173\n\t"
        "v_max_u32 v187, v187, v73\n\t"
        "v_mfma_f32_16x16x32_bf16 v[20:23], v[240:243], v[96:99], v[20:23]\n\t"
        "v_xor_b32 v74, v186, v74\n\t"
        "ds_read_b128 v[88:91], v151 offset:0\n\t"
        "v_mfma_f32_16x16x32_bf16 v[12:15], v[244:247], v[100:103], v[12:15]\n\t"
        "ds_read_b128 v[92:95], v151 offset:64\n\t"
        "v_mfma_f32_16x16x32_bf16 v[16:19], v[248:251], v[100:103], v[16:19]\n\t"
        "ds_read_b128 v[96:99], v151 offset:128\n\t"
        "v_add_u32 v151, s5, v174\n\t"
        "v_mfma_f32_16x16x32_bf16 v[20:23], v[252:255], v[100:103], v[20:23]\n\t"
        "v_max_u32 v151, v151, v74\n\t"
        "v_xor_b32 v75, v186, v75\n\t"
        "v_mfma_f32_16x16x32_bf16 v[24:27], v[208:211], v[104:107], v[24:27]\n\t"
        "ds_read_b128 v[100:103], v187 offset:0\n\t"
        "s_waitcnt lgkmcnt(4)\n\t"
        "v_or3_b32 v164, v164, v165, v166\n\t"
        "v_mfma_f32_16x16x32_bf16 v[28:31], v[212:215], v[104:107], v[28:31]\n\t"
        "v_or_b32 v164, v164, v167\n\t"
        "v_mfma_f32_16x16x32_bf16 v[32:35], v[216:219], v[104:107], v[32:35]\n\t"
        "v_readfirstlane_b32 s8, v164\n\t"
        "v_mfma_f32_16x16x32_bf16 v[24:27], v[220:223], v[108:111], v[24:27]\n\t"
        "ds_read_b128 v[104:107], v187 offset:64\n\t"
        "v_mfma_f32_16x16x32_bf16 v[28:31], v[224:227], v[108:111], v[28:31]\n\t"
        "s_cmp_lg_u32 s8, 0\n\t"
        "s_cbranch_scc1 99f\n\t"
        "v_mfma_f32_16x16x32_bf16 v[32:35], v[228:231], v[108:111], v[32:35]\n\t"
        "s_mov_b32 s30, 9633792\n\t"
        "s_mov_b32 s31, 0x20000\n\t"
        "v_mfma_f32_16x16x32_bf16 v[24:27], v[232:235], v[112:115], v[24:27]\n\t"
        "ds_read_b128 v[108:111], v187 offset:128\n\t"
        "v_add_u32 v187, s5, v175\n\t"
        "v_mfma_f32_16x16x32_bf16 v[28:31], v[236:239], v[112:115], v[28:31]\n\t"
        "v_max_u32 v187, v187, v75\n\t"
        "ds_read_b128 v[72:75], v135 offset:2048\n\t"
        "v_mfma_f32_16x16x32_bf16 v[32:35], v[240:243], v[112:115], v[32:35]\n\t"
        "v_xor_b32 v76, v186, v76\n\t"
        "s_lshl_b32 s11, s49, 7\n\t"
        "v_mfma_f32_16x16x32_bf16 v[24:27], v[244:247], v[84:87], v[24:27]\n\t"
        "ds_read_b128 v[112:115], v151 offset:0\n\t"
        "s_lshl_b32 s9, s49, 5\n\t"
        "v_mfma_f32_16x16x32_bf16 v[28:31], v[248:251], v[84:87], v[28:31]\n\t"
        "v_lshrrev_b32 v161, 1, v159\n\t"
        "v_lshlrev_b32 v161, 4, v161\n\t"
        "v_mfma_f32_16x16x32_bf16 v[32:35], v[252:255], v[84:87], v[32:35]\n\t"
        "v_and_b32 v162, 1, v159\n\t"
        "v_lshl_or_b32 v161, v162, 5, v161\n\t"
        "s_waitcnt lgkmcnt(4)\n\t"
        "v_mfma_f32_16x16x32_bf16 v[36:39], a[252:255], v[88:91], 0\n\t"
        "ds_read_b128 v[84:87], v151 offset:64\n\t"
        "v_mul_u32_u24 v160, 192, v158\n\t"
        "v_mfma_f32_16x16x32_bf16 v[40:43], v[188:191], v[88:91], 0\n\t"
        "v_add3_u32 v144, v160, v161, s11\n\t"
        "v_readlane_b32 s10, v170, 30\n\t"
        "v_mfma_f32_16x16x32_bf16 v[44:47], v[192:195], v[88:91], 0\n\t"
        "s_lshr_b32 s8, s4, 1\n\t"
        "s_sub_u32 s13, 43200, s10\n\t"
        "v_mfma_f32_16x16x32_bf16 v[36:39], v[208:211], v[92:95], v[36:39]\n\t"
        "ds_read_b128 v[88:91], v151 offset:128\n\t"
        "v_add_u32 v151, s5, v176\n\t"
        "v_mfma_f32_16x16x32_bf16 v[40:43], v[212:215], v[92:95], v[40:43]\n\t"
        "v_max_u32 v151, v151, v76\n\t"
        "v_xor_b32 v77, v186, v77\n\t"
        "v_mfma_f32_16x16x32_bf16 v[44:47], v[216:219], v[92:95], v[44:47]\n\t"
        "s_mul_i32 s13, s13, s8\n\t"
        "v_mul_u32_u24 v160, s10, v158\n\t"
        "v_mfma_f32_16x16x32_bf16 v[36:39], v[232:235], v[96:99], v[36:39]\n\t"
        "ds_read_b128 v[92:95], v187 offset:0\n\t"
        "v_add_u32 v160, s13, v160\n\t"
        "v_mfma_f32_16x16x32_bf16 v[40:43], v[236:239], v[96:99], v[40:43]\n\t"
        "v_add3_u32 v184, v160, v161, s11\n\t"
        "v_xor_b32 v161, s9, v161\n\t"
        "v_mfma_f32_16x16x32_bf16 v[44:47], v[240:243], v[96:99], v[44:47]\n\t"
        "v_add3_u32 v146, v160, v161, 64\n\t"
        "v_lshrrev_b32 v160, 2, v157\n\t"
        "v_mfma_f32_16x16x32_bf16 v[36:39], a[180:183], v[100:103], v[36:39]\n\t"
        "ds_read_b128 v[96:99], v187 offset:64\n\t"
        "v_and_b32 v161, 3, v157\n\t"
        "v_mfma_f32_16x16x32_bf16 v[40:43], a[184:187], v[100:103], v[40:43]\n\t"
        "v_lshl_add_u32 v161, v161, 4, 64\n\t"
        "s_movk_i32 s11, 192\n\t"
        "v_mfma_f32_16x16x32_bf16 v[44:47], a[188:191], v[100:103], v[44:47]\n\t"
        "v_mad_u32_u24 v145, v160, s11, v161\n\t"
        "s_waitcnt lgkmcnt(3)\n\t"
        "v_mfma_f32_16x16x32_bf16 v[36:39], a[204:207], v[104:107], v[36:39]\n\t"
        "ds_read_b128 v[100:103], v187 offset:128\n\t"
        "v_add_u32 v187, s5, v177\n\t"
        "v_mfma_f32_16x16x32_bf16 v[40:43], a[208:211], v[104:107], v[40:43]\n\t"
        "v_max_u32 v187, v187, v77\n\t"
        "v_xor_b32 v78, v186, v78\n\t"
        "v_mfma_f32_16x16x32_bf16 v[44:47], a[212:215], v[104:107], v[44:47]\n\t"
        "s_cmp_eq_u32 s49, 0\n\t"
        "s_cselect_b32 s10, s11, s10\n\t"
        "v_mfma_f32_16x16x32_bf16 v[36:39], a[228:231], v[108:111], v[36:39]\n\t"
        "ds_read_b128 v[104:107], v151 offset:0\n\t"
        "v_mad_u32_u24 v185, v160, s10, v161\n\t"
        "v_mfma_f32_16x16x32_bf16 v[40:43], a[232:235], v[108:111], v[40:43]\n\t"
        "s_lshr_b32 s8, s4, 1\n\t"
        "s_lshl_b32 s10, s8, 10\n\t"
        "v_mfma_f32_16x16x32_bf16 v[44:47], a[236:239], v[108:111], v[44:47]\n\t"
        "s_add_u32 s10, s10, 153600\n\t"
        "s_add_u32 s11, s10, s9\n\t"
        "v_mfma_f32_16x16x32_bf16 v[36:39], a[108:111], v[112:115], v[36:39]\n\t"
        "ds_read_b128 v[108:111], v151 offset:64\n\t"
        "v_lshlrev_b32 v160, 6, v158\n\t"
        "v_mfma_f32_16x16x32_bf16 v[40:43], a[112:115], v[112:115], v[40:43]\n\t"
        "v_lshl_add_u32 v160, v159, 3, v160\n\t"
        "v_add_u32 v147, s11, v160\n\t"
        "v_mfma_f32_16x16x32_bf16 v[44:47], a[116:119], v[112:115], v[44:47]\n\t"
        "s_lshl_b32 s11, s49, 11\n\t"
        "s_add_u32 s11, s11, s10\n\t"
        "v_mfma_f32_16x16x32_bf16 v[36:39], a[132:135], v[84:87], v[36:39]\n\t"
        "ds_read_b128 v[112:115], v151 offset:128\n\t"
        "v_add_u32 v151, s5, v178\n\t"
        "v_mfma_f32_16x16x32_bf16 v[40:43], a[136:139], v[84:87], v[40:43]\n\t"
        "v_max_u32 v151, v151, v78\n\t"
        "v_xor_b32 v79, v186, v79\n\t"
        "v_mfma_f32_16x16x32_bf16 v[44:47], a[140:143], v[84:87], v[44:47]\n\t"
        "v_add_u32 v148, s11, v140\n\t"
        "s_lshl_b32 s11, s4, 9\n\t"
        "s_waitcnt lgkmcnt(3)\n\t"
        "v_mfma_f32_16x16x32_bf16 v[36:39], a[156:159], v[88:91], v[36:39]\n\t"
        "ds_read_b128 v[84:87], v187 offset:0\n\t"
        "s_add_u32 s11, s11, 0\n\t"
        "v_mfma_f32_16x16x32_bf16 v[40:43], a[160:163], v[88:91], v[40:43]\n\t"
        "v_lshl_add_u32 v149, v157, 3, s11\n\t"
        "v_xor_b32 v150, 512, v149\n\t"
        "v_mfma_f32_16x16x32_bf16 v[44:47], a[164:167], v[88:91], v[44:47]\n\t"
        "s_mul_i32 s8, s8, 43008\n\t"
        "s_add_u32 s41, s41, s8\n\t"
        "v_mfma_f32_16x16x32_bf16 v[36:39], a[36:39], v[92:95], v[36:39]\n\t"
        "ds_read_b128 v[88:91], v187 offset:64\n\t"
        "s_mul_i32 s8, s49, 86016\n\t"
        "v_mfma_f32_16x16x32_bf16 v[40:43], a[40:43], v[92:95], v[40:43]\n\t"
        "s_add_u32 s50, s41, s8\n\t"
        "v_readlane_b32 s53, v170, 49\n\t"
        "v_mfma_f32_16x16x32_bf16 v[44:47], a[44:47], v[92:95], v[44:47]\n\t"
        "s_add_u32 s8, s49, 50\n\t"
        "s_sub_u32 s9, 51, s49\n\t"
        "v_mfma_f32_16x16x32_bf16 v[36:39], a[60:63], v[96:99], v[36:39]\n\t"
        "ds_read_b128 v[92:95], v187 offset:128\n\t"
        "v_add_u32 v187, s5, v179\n\t"
        "v_mfma_f32_16x16x32_bf16 v[40:43], a[64:67], v[96:99], v[40:43]\n\t"
        "v_max_u32 v187, v187, v79\n\t"
        "ds_read_b128 v[76:79], v135 offset:2064\n\t"
        "v_mfma_f32_16x16x32_bf16 v[44:47], a[68:71], v[96:99], v[44:47]\n\t"
        "v_xor_b32 v80, v186, v80\n\t"
        "v_readlane_b32 s51, v170, s8\n\t"
        "v_mfma_f32_16x16x32_bf16 v[36:39], a[84:87], v[100:103], v[36:39]\n\t"
        "ds_read_b128 v[96:99], v151 offset:0\n\t"
        "v_readlane_b32 s52, v170, s9\n\t"
        "v_mfma_f32_16x16x32_bf16 v[40:43], a[88:91], v[100:103], v[40:43]\n\t"
        "s_add_u32 s39, s41, 172032\n\t"
        "s_add_u32 s8, s53, s13\n\t"
        "v_mfma_f32_16x16x32_bf16 v[44:47], a[92:95], v[100:103], v[44:47]\n\t"
        "s_cmp_eq_u32 s49, 0\n\t"
        "s_cselect_b32 s39, s39, s8\n\t"
        "s_waitcnt lgkmcnt(4)\n\t"
        "v_mfma_f32_16x16x32_bf16 v[36:39], a[0:3], v[104:107], v[36:39]\n\t"
        "ds_read_b128 v[100:103], v151 offset:64\n\t"
        "v_cvt_pk_bf16_f32 v116, v0, v1\n\t"
        "v_mfma_f32_16x16x32_bf16 v[40:43], a[4:7], v[104:107], v[40:43]\n\t"
        "v_cvt_pk_bf16_f32 v117, v2, v3\n\t"
        "v_cvt_pk_bf16_f32 v118, v4, v5\n\t"
        "v_mfma_f32_16x16x32_bf16 v[44:47], a[8:11], v[104:107], v[44:47]\n\t"
        "v_cvt_pk_bf16_f32 v119, v6, v7\n\t"
        "v_cvt_pk_bf16_f32 v120, v8, v9\n\t"
        "v_mfma_f32_16x16x32_bf16 v[36:39], a[24:27], v[108:111], v[36:39]\n\t"
        "ds_read_b128 v[104:107], v151 offset:128\n\t"
        "v_add_u32 v151, s5, v180\n\t"
        "v_mfma_f32_16x16x32_bf16 v[40:43], a[28:31], v[108:111], v[40:43]\n\t"
        "v_max_u32 v151, v151, v80\n\t"
        "ds_read_b32 v80, v135 offset:2080\n\t"
        "v_mfma_f32_16x16x32_bf16 v[44:47], a[32:35], v[108:111], v[44:47]\n\t"
        "v_xor_b32 v72, v186, v72\n\t"
        "v_cvt_pk_bf16_f32 v121, v10, v11\n\t"
        "v_mfma_f32_16x16x32_bf16 v[36:39], a[12:15], v[112:115], v[36:39]\n\t"
        "ds_read_b128 v[108:111], v187 offset:0\n\t"
        "v_mfma_f32_16x16x32_bf16 v[40:43], a[16:19], v[112:115], v[40:43]\n\t"
        "v_permlane16_swap_b32 v116, v118\n\t"
        "v_mfma_f32_16x16x32_bf16 v[44:47], a[20:23], v[112:115], v[44:47]\n\t"
        "v_permlane16_swap_b32 v117, v119\n\t"
        "v_mfma_f32_16x16x32_bf16 v[36:39], a[48:51], v[84:87], v[36:39]\n\t"
        "ds_read_b128 v[112:115], v187 offset:64\n\t"
        "s_add_u32 s55, s41, 0\n\t"
        "v_mfma_f32_16x16x32_bf16 v[40:43], a[52:55], v[84:87], v[40:43]\n\t"
        "buffer_store_dwordx4 v[116:119], v144, s[28:31], s55 offen sc1\n\t"
        "v_mfma_f32_16x16x32_bf16 v[44:47], a[56:59], v[84:87], v[44:47]\n\t"
        "ds_write_b64 v147, v[120:121] offset:0\n\t"
        "s_waitcnt lgkmcnt(5)\n\t"
        "v_mfma_f32_16x16x32_bf16 v[36:39], a[72:75], v[88:91], v[36:39]\n\t"
        "ds_read_b128 v[84:87], v187 offset:128\n\t"
        "v_add_u32 v187, s6, v172\n\t"
        "v_mfma_f32_16x16x32_bf16 v[40:43], a[76:79], v[88:91], v[40:43]\n\t"
        "v_max_u32 v187, v187, v72\n\t"
        "v_xor_b32 v73, v186, v73\n\t"
        "v_mfma_f32_16x16x32_bf16 v[44:47], a[80:83], v[88:91], v[44:47]\n\t"
        "v_cvt_pk_bf16_f32 v116, v12, v13\n\t"
        "v_cvt_pk_bf16_f32 v117, v14, v15\n\t"
        "v_mfma_f32_16x16x32_bf16 v[36:39], a[96:99], v[92:95], v[36:39]\n\t"
        "ds_read_b128 v[88:91], v151 offset:0\n\t"
        "v_cvt_pk_bf16_f32 v118, v16, v17\n\t"
        "v_mfma_f32_16x16x32_bf16 v[40:43], a[100:103], v[92:95], v[40:43]\n\t"
        "v_cvt_pk_bf16_f32 v119, v18, v19\n\t"
        "v_cvt_pk_bf16_f32 v120, v20, v21\n\t"
        "v_mfma_f32_16x16x32_bf16 v[44:47], a[104:107], v[92:95], v[44:47]\n\t"
        "v_cvt_pk_bf16_f32 v121, v22, v23\n\t"
        "v_mfma_f32_16x16x32_bf16 v[36:39], a[120:123], v[96:99], v[36:39]\n\t"
        "ds_read_b128 v[92:95], v151 offset:64\n\t"
        "v_mfma_f32_16x16x32_bf16 v[40:43], a[124:127], v[96:99], v[40:43]\n\t"
        "v_permlane16_swap_b32 v116, v118\n\t"
        "v_mfma_f32_16x16x32_bf16 v[44:47], a[128:131], v[96:99], v[44:47]\n\t"
        "v_permlane16_swap_b32 v117, v119\n\t"
        "v_mfma_f32_16x16x32_bf16 v[36:39], a[144:147], v[100:103], v[36:39]\n\t"
        "ds_read_b128 v[96:99], v151 offset:128\n\t"
        "v_add_u32 v151, s6, v173\n\t"
        "v_mfma_f32_16x16x32_bf16 v[40:43], a[148:151], v[100:103], v[40:43]\n\t"
        "v_max_u32 v151, v151, v73\n\t"
        "v_xor_b32 v74, v186, v74\n\t"
        "v_mfma_f32_16x16x32_bf16 v[44:47], a[152:155], v[100:103], v[44:47]\n\t"
        "s_add_u32 s55, s41, 86016\n\t"
        "s_waitcnt lgkmcnt(3)\n\t"
        "v_mfma_f32_16x16x32_bf16 v[36:39], a[168:171], v[104:107], v[36:39]\n\t"
        "ds_read_b128 v[100:103], v187 offset:0\n\t"
        "v_mfma_f32_16x16x32_bf16 v[40:43], a[172:175], v[104:107], v[40:43]\n\t"
        "buffer_store_dwordx4 v[116:119], v144, s[28:31], s55 offen sc1\n\t"
        "v_mfma_f32_16x16x32_bf16 v[44:47], a[176:179], v[104:107], v[44:47]\n\t"
        "ds_write_b64 v147, v[120:121] offset:2048\n\t"
        "v_mfma_f32_16x16x32_bf16 v[36:39], a[192:195], v[108:111], v[36:39]\n\t"
        "ds_read_b128 v[104:107], v187 offset:64\n\t"
        "v_cvt_pk_bf16_f32 v116, v24, v25\n\t"
        "v_mfma_f32_16x16x32_bf16 v[40:43], a[196:199], v[108:111], v[40:43]\n\t"
        "v_cvt_pk_bf16_f32 v117, v26, v27\n\t"
        "v_cvt_pk_bf16_f32 v118, v28, v29\n\t"
        "v_mfma_f32_16x16x32_bf16 v[44:47], a[200:203], v[108:111], v[44:47]\n\t"
        "v_cvt_pk_bf16_f32 v119, v30, v31\n\t"
        "v_cvt_pk_bf16_f32 v120, v32, v33\n\t"
        "v_mfma_f32_16x16x32_bf16 v[36:39], a[216:219], v[112:115], v[36:39]\n\t"
        "ds_read_b128 v[108:111], v187 offset:128\n\t"
        "v_add_u32 v187, s6, v174\n\t"
        "v_mfma_f32_16x16x32_bf16 v[40:43], a[220:223], v[112:115], v[40:43]\n\t"
        "v_max_u32 v187, v187, v74\n\t"
        "v_xor_b32 v75, v186, v75\n\t"
        "v_mfma_f32_16x16x32_bf16 v[44:47], a[224:227], v[112:115], v[44:47]\n\t"
        "v_cvt_pk_bf16_f32 v121, v34, v35\n\t"
        "v_mfma_f32_16x16x32_bf16 v[36:39], a[240:243], v[84:87], v[36:39]\n\t"
        "ds_read_b128 v[112:115], v151 offset:0\n\t"
        "v_mfma_f32_16x16x32_bf16 v[40:43], a[244:247], v[84:87], v[40:43]\n\t"
        "v_permlane16_swap_b32 v116, v118\n\t"
        "v_mfma_f32_16x16x32_bf16 v[44:47], a[248:251], v[84:87], v[44:47]\n\t"
        "v_permlane16_swap_b32 v117, v119\n\t"
        "s_waitcnt lgkmcnt(4)\n\t"
        "v_mfma_f32_16x16x32_bf16 v[36:39], v[196:199], v[88:91], v[36:39]\n\t"
        "ds_read_b128 v[84:87], v151 offset:64\n\t"
        "s_add_u32 s55, s41, 172032\n\t"
        "v_mfma_f32_16x16x32_bf16 v[40:43], v[200:203], v[88:91], v[40:43]\n\t"
        "v_mfma_f32_16x16x32_bf16 v[44:47], v[204:207], v[88:91], v[44:47]\n\t"
        "v_mfma_f32_16x16x32_bf16 v[36:39], v[220:223], v[92:95], v[36:39]\n\t"
        "ds_read_b128 v[88:91], v151 offset:128\n\t"
        "v_add_u32 v151, s6, v175\n\t"
        "v_mfma_f32_16x16x32_bf16 v[40:43], v[224:227], v[92:95], v[40:43]\n\t"
        "v_max_u32 v151, v151, v75\n\t"
        "ds_read_b128 v[72:75], v136 offset:4096\n\t"
        "v_mfma_f32_16x16x32_bf16 v[44:47], v[228:231], v[92:95], v[44:47]\n\t"
        "v_xor_b32 v76, v186, v76\n\t"
        "v_mfma_f32_16x16x32_bf16 v[36:39], v[244:247], v[96:99], v[36:39]\n\t"
        "ds_read_b128 v[92:95], v187 offset:0\n\t"
        "v_mfma_f32_16x16x32_bf16 v[40:43], v[248:251], v[96:99], v[40:43]\n\t"
        "buffer_store_dwordx4 v[116:119], v144, s[28:31], s55 offen sc1\n\t"
        "v_mfma_f32_16x16x32_bf16 v[44:47], v[252:255], v[96:99], v[44:47]\n\t"
        "ds_write_b64 v147, v[120:121] offset:4096\n\t"
        "v_mfma_f32_16x16x32_bf16 v[48:51], a[252:255], v[100:103], 0\n\t"
        "ds_read_b128 v[96:99], v187 offset:64\n\t"
        "v_mfma_f32_16x16x32_bf16 v[52:55], v[188:191], v[100:103], 0\n\t"
        "v_mfma_f32_16x16x32_bf16 v[56:59], v[192:195], v[100:103], 0\n\t"
        "s_waitcnt lgkmcnt(5)\n\t"
        "v_mfma_f32_16x16x32_bf16 v[48:51], v[208:211], v[104:107], v[48:51]\n\t"
        "ds_read_b128 v[100:103], v187 offset:128\n\t"
        "v_add_u32 v187, s6, v176\n\t"
        "v_mfma_f32_16x16x32_bf16 v[52:55], v[212:215], v[104:107], v[52:55]\n\t"
        "v_max_u32 v187, v187, v76\n\t"
        "v_xor_b32 v77, v186, v77\n\t"
        "v_mfma_f32_16x16x32_bf16 v[56:59], v[216:219], v[104:107], v[56:59]\n\t"
        "s_waitcnt lgkmcnt(2)\n\t"
        "s_barrier\n\t"
        "v_mfma_f32_16x16x32_bf16 v[48:51], v[232:235], v[108:111], v[48:51]\n\t"
        "ds_read_b128 v[104:107], v151 offset:0\n\t"
        "v_mfma_f32_16x16x32_bf16 v[52:55], v[236:239], v[108:111], v[52:55]\n\t"
        "ds_read_b128 v[124:127], v148 offset:0\n\t"
        "v_mfma_f32_16x16x32_bf16 v[56:59], v[240:243], v[108:111], v[56:59]\n\t"
        "v_mfma_f32_16x16x32_bf16 v[48:51], a[180:183], v[112:115], v[48:51]\n\t"
        "ds_read_b128 v[108:111], v151 offset:64\n\t"
        "v_mfma_f32_16x16x32_bf16 v[52:55], a[184:187], v[112:115], v[52:55]\n\t"
        "v_mfma_f32_16x16x32_bf16 v[56:59], a[188:191], v[112:115], v[56:59]\n\t"
        "v_mfma_f32_16x16x32_bf16 v[48:51], a[204:207], v[84:87], v[48:51]\n\t"
        "ds_read_b128 v[112:115], v151 offset:128\n\t"
        "v_add_u32 v151, s6, v177\n\t"
        "v_mfma_f32_16x16x32_bf16 v[52:55], a[208:211], v[84:87], v[52:55]\n\t"
        "v_max_u32 v151, v151, v77\n\t"
        "v_xor_b32 v78, v186, v78\n\t"
        "v_mfma_f32_16x16x32_bf16 v[56:59], a[212:215], v[84:87], v[56:59]\n\t"
        "v_mfma_f32_16x16x32_bf16 v[48:51], a[228:231], v[88:91], v[48:51]\n\t"
        "ds_read_b128 v[84:87], v187 offset:0\n\t"
        "v_mfma_f32_16x16x32_bf16 v[52:55], a[232:235], v[88:91], v[52:55]\n\t"
        "s_waitcnt lgkmcnt(3)\n\t"
        "buffer_store_dwordx4 v[124:127], v145, s[28:31], s50 offen sc1\n\t"
        "v_mfma_f32_16x16x32_bf16 v[56:59], a[236:239], v[88:91], v[56:59]\n\t"
        "v_cvt_pk_bf16_f32 v116, v36, v37\n\t"
        "v_cvt_pk_bf16_f32 v117, v38, v39\n\t"
        "v_mfma_f32_16x16x32_bf16 v[48:51], a[108:111], v[92:95], v[48:51]\n\t"
        "ds_read_b128 v[88:91], v187 offset:64\n\t"
        "v_cvt_pk_bf16_f32 v118, v40, v41\n\t"
        "v_mfma_f32_16x16x32_bf16 v[52:55], a[112:115], v[92:95], v[52:55]\n\t"
        "v_cvt_pk_bf16_f32 v119, v42, v43\n\t"
        "v_cvt_pk_bf16_f32 v120, v44, v45\n\t"
        "v_mfma_f32_16x16x32_bf16 v[56:59], a[116:119], v[92:95], v[56:59]\n\t"
        "v_cvt_pk_bf16_f32 v121, v46, v47\n\t"
        "v_mfma_f32_16x16x32_bf16 v[48:51], a[132:135], v[96:99], v[48:51]\n\t"
        "ds_read_b128 v[92:95], v187 offset:128\n\t"
        "v_add_u32 v187, s6, v178\n\t"
        "v_mfma_f32_16x16x32_bf16 v[52:55], a[136:139], v[96:99], v[52:55]\n\t"
        "v_max_u32 v187, v187, v78\n\t"
        "v_xor_b32 v79, v186, v79\n\t"
        "v_mfma_f32_16x16x32_bf16 v[56:59], a[140:143], v[96:99], v[56:59]\n\t"
        "v_permlane16_swap_b32 v116, v118\n\t"
        "v_mfma_f32_16x16x32_bf16 v[48:51], a[156:159], v[100:103], v[48:51]\n\t"
        "ds_read_b128 v[96:99], v151 offset:0\n\t"
        "v_mfma_f32_16x16x32_bf16 v[52:55], a[160:163], v[100:103], v[52:55]\n\t"
        "v_permlane16_swap_b32 v117, v119\n\t"
        "v_mfma_f32_16x16x32_bf16 v[56:59], a[164:167], v[100:103], v[56:59]\n\t"
        "v_mfma_f32_16x16x32_bf16 v[48:51], a[36:39], v[104:107], v[48:51]\n\t"
        "ds_read_b128 v[100:103], v151 offset:64\n\t"
        "v_mfma_f32_16x16x32_bf16 v[52:55], a[40:43], v[104:107], v[52:55]\n\t"
        "v_mfma_f32_16x16x32_bf16 v[56:59], a[44:47], v[104:107], v[56:59]\n\t"
        "s_waitcnt lgkmcnt(3)\n\t"
        "v_mfma_f32_16x16x32_bf16 v[48:51], a[60:63], v[108:111], v[48:51]\n\t"
        "ds_read_b128 v[104:107], v151 offset:128\n\t"
        "v_add_u32 v151, s6, v179\n\t"
        "v_mfma_f32_16x16x32_bf16 v[52:55], a[64:67], v[108:111], v[52:55]\n\t"
        "v_max_u32 v151, v151, v79\n\t"
        "ds_read_b128 v[76:79], v136 offset:4112\n\t"
        "v_mfma_f32_16x16x32_bf16 v[56:59], a[68:71], v[108:111], v[56:59]\n\t"
        "v_xor_b32 v80, v186, v80\n\t"
        "v_mfma_f32_16x16x32_bf16 v[48:51], a[84:87], v[112:115], v[48:51]\n\t"
        "ds_read_b128 v[108:111], v187 offset:0\n\t"
        "v_mfma_f32_16x16x32_bf16 v[52:55], a[88:91], v[112:115], v[52:55]\n\t"
        "buffer_store_dwordx4 v[116:119], v184, s[28:31], s53 offen sc1\n\t"
        "v_mfma_f32_16x16x32_bf16 v[56:59], a[92:95], v[112:115], v[56:59]\n\t"
        "ds_write_b64 v147, v[120:121] offset:6144\n\t"
        "v_mfma_f32_16x16x32_bf16 v[48:51], a[0:3], v[84:87], v[48:51]\n\t"
        "ds_read_b128 v[112:115], v187 offset:64\n\t"
        "v_mfma_f32_16x16x32_bf16 v[52:55], a[4:7], v[84:87], v[52:55]\n\t"
        "v_mfma_f32_16x16x32_bf16 v[56:59], a[8:11], v[84:87], v[56:59]\n\t"
        "v_mfma_f32_16x16x32_bf16 v[48:51], a[24:27], v[88:91], v[48:51]\n\t"
        "ds_read_b128 v[84:87], v187 offset:128\n\t"
        "v_add_u32 v187, s6, v180\n\t"
        "v_mfma_f32_16x16x32_bf16 v[52:55], a[28:31], v[88:91], v[52:55]\n\t"
        "v_max_u32 v187, v187, v80\n\t"
        "ds_read_b32 v80, v136 offset:4128\n\t"
        "v_mfma_f32_16x16x32_bf16 v[56:59], a[32:35], v[88:91], v[56:59]\n\t"
        "v_xor_b32 v72, v186, v72\n\t"
        "s_waitcnt lgkmcnt(5)\n\t"
        "v_mfma_f32_16x16x32_bf16 v[48:51], a[12:15], v[92:95], v[48:51]\n\t"
        "ds_read_b128 v[88:91], v151 offset:0\n\t"
        "v_mfma_f32_16x16x32_bf16 v[52:55], a[16:19], v[92:95], v[52:55]\n\t"
        "v_mfma_f32_16x16x32_bf16 v[56:59], a[20:23], v[92:95], v[56:59]\n\t"
        "v_mfma_f32_16x16x32_bf16 v[48:51], a[48:51], v[96:99], v[48:51]\n\t"
        "ds_read_b128 v[92:95], v151 offset:64\n\t"
        "v_mfma_f32_16x16x32_bf16 v[52:55], a[52:55], v[96:99], v[52:55]\n\t"
        "v_mfma_f32_16x16x32_bf16 v[56:59], a[56:59], v[96:99], v[56:59]\n\t"
        "v_mfma_f32_16x16x32_bf16 v[48:51], a[72:75], v[100:103], v[48:51]\n\t"
        "ds_read_b128 v[96:99], v151 offset:128\n\t"
        "v_add_u32 v151, s3, v172\n\t"
        "v_mfma_f32_16x16x32_bf16 v[52:55], a[76:79], v[100:103], v[52:55]\n\t"
        "v_max_u32 v151, v151, v72\n\t"
        "v_xor_b32 v73, v186, v73\n\t"
        "v_mfma_f32_16x16x32_bf16 v[56:59], a[80:83], v[100:103], v[56:59]\n\t"
        "v_mfma_f32_16x16x32_bf16 v[48:51], a[96:99], v[104:107], v[48:51]\n\t"
        "ds_read_b128 v[100:103], v187 offset:0\n\t"
        "v_mfma_f32_16x16x32_bf16 v[52:55], a[100:103], v[104:107], v[52:55]\n\t"
        "v_mfma_f32_16x16x32_bf16 v[56:59], a[104:107], v[104:107], v[56:59]\n\t"
        "s_waitcnt lgkmcnt(3)\n\t"
        "v_mfma_f32_16x16x32_bf16 v[48:51], a[120:123], v[108:111], v[48:51]\n\t"
        "ds_read_b128 v[104:107], v187 offset:64\n\t"
        "v_mfma_f32_16x16x32_bf16 v[52:55], a[124:127], v[108:111], v[52:55]\n\t"
        "v_mfma_f32_16x16x32_bf16 v[56:59], a[128:131], v[108:111], v[56:59]\n\t"
        "v_mfma_f32_16x16x32_bf16 v[48:51], a[144:147], v[112:115], v[48:51]\n\t"
        "ds_read_b128 v[108:111], v187 offset:128\n\t"
        "v_add_u32 v187, s3, v173\n\t"
        "v_mfma_f32_16x16x32_bf16 v[52:55], a[148:151], v[112:115], v[52:55]\n\t"
        "v_max_u32 v187, v187, v73\n\t"
        "v_xor_b32 v74, v186, v74\n\t"
        "v_mfma_f32_16x16x32_bf16 v[56:59], a[152:155], v[112:115], v[56:59]\n\t"
        "v_mfma_f32_16x16x32_bf16 v[48:51], a[168:171], v[84:87], v[48:51]\n\t"
        "ds_read_b128 v[112:115], v151 offset:0\n\t"
        "v_mfma_f32_16x16x32_bf16 v[52:55], a[172:175], v[84:87], v[52:55]\n\t"
        "v_mfma_f32_16x16x32_bf16 v[56:59], a[176:179], v[84:87], v[56:59]\n\t"
        "v_mfma_f32_16x16x32_bf16 v[48:51], a[192:195], v[88:91], v[48:51]\n\t"
        "ds_read_b128 v[84:87], v151 offset:64\n\t"
        "v_mfma_f32_16x16x32_bf16 v[52:55], a[196:199], v[88:91], v[52:55]\n\t"
        "v_mfma_f32_16x16x32_bf16 v[56:59], a[200:203], v[88:91], v[56:59]\n\t"
        "s_waitcnt lgkmcnt(3)\n\t"
        "v_mfma_f32_16x16x32_bf16 v[48:51], a[216:219], v[92:95], v[48:51]\n\t"
        "ds_read_b128 v[88:91], v151 offset:128\n\t"
        "v_add_u32 v151, s3, v174\n\t"
        "v_mfma_f32_16x16x32_bf16 v[52:55], a[220:223], v[92:95], v[52:55]\n\t"
        "v_max_u32 v151, v151, v74\n\t"
        "v_xor_b32 v75, v186, v75\n\t"
        "v_mfma_f32_16x16x32_bf16 v[56:59], a[224:227], v[92:95], v[56:59]\n\t"
        "v_mfma_f32_16x16x32_bf16 v[48:51], a[240:243], v[96:99], v[48:51]\n\t"
        "ds_read_b128 v[92:95], v187 offset:0\n\t"
        "v_mfma_f32_16x16x32_bf16 v[52:55], a[244:247], v[96:99], v[52:55]\n\t"
        "v_mfma_f32_16x16x32_bf16 v[56:59], a[248:251], v[96:99], v[56:59]\n\t"
        "v_mfma_f32_16x16x32_bf16 v[48:51], v[196:199], v[100:103], v[48:51]\n\t"
        "ds_read_b128 v[96:99], v187 offset:64\n\t"
        "v_mfma_f32_16x16x32_bf16 v[52:55], v[200:203], v[100:103], v[52:55]\n\t"
        "v_mfma_f32_16x16x32_bf16 v[56:59], v[204:207], v[100:103], v[56:59]\n\t"
        "v_mfma_f32_16x16x32_bf16 v[48:51], v[220:223], v[104:107], v[48:51]\n\t"
        "ds_read_b128 v[100:103], v187 offset:128\n\t"
        "v_add_u32 v187, s3, v175\n\t"
        "v_mfma_f32_16x16x32_bf16 v[52:55], v[224:227], v[104:107], v[52:55]\n\t"
        "v_max_u32 v187, v187, v75\n\t"
        "v_xor_b32 v76, v186, v76\n\t"
        "v_mfma_f32_16x16x32_bf16 v[56:59], v[228:231], v[104:107], v[56:59]\n\t"
        "s_waitcnt lgkmcnt(3)\n\t"
        "v_mfma_f32_16x16x32_bf16 v[48:51], v[244:247], v[108:111], v[48:51]\n\t"
        "ds_read_b128 v[104:107], v151 offset:0\n\t"
        "v_mfma_f32_16x16x32_bf16 v[52:55], v[248:251], v[108:111], v[52:55]\n\t"
        "v_mfma_f32_16x16x32_bf16 v[56:59], v[252:255], v[108:111], v[56:59]\n\t"
        "v_mfma_f32_16x16x32_bf16 v[60:63], a[252:255], v[112:115], 0\n\t"
        "ds_read_b128 v[108:111], v151 offset:64\n\t"
        "v_mfma_f32_16x16x32_bf16 v[64:67], v[188:191], v[112:115], 0\n\t"
        "v_mfma_f32_16x16x32_bf16 v[68:71], v[192:195], v[112:115], 0\n\t"
        "v_mfma_f32_16x16x32_bf16 v[60:63], v[208:211], v[84:87], v[60:63]\n\t"
        "ds_read_b128 v[112:115], v151 offset:128\n\t"
        "v_add_u32 v151, s3, v176\n\t"
        "v_mfma_f32_16x16x32_bf16 v[64:67], v[212:215], v[84:87], v[64:67]\n\t"
        "v_max_u32 v151, v151, v76\n\t"
        "v_xor_b32 v77, v186, v77\n\t"
        "v_mfma_f32_16x16x32_bf16 v[68:71], v[216:219], v[84:87], v[68:71]\n\t"
        "v_cvt_pk_bf16_f32 v116, v48, v49\n\t"
        "v_cvt_pk_bf16_f32 v117, v50, v51\n\t"
        "v_mfma_f32_16x16x32_bf16 v[60:63], v[232:235], v[88:91], v[60:63]\n\t"
        "ds_read_b128 v[84:87], v187 offset:0\n\t"
        "v_cvt_pk_bf16_f32 v118, v52, v53\n\t"
        "v_mfma_f32_16x16x32_bf16 v[64:67], v[236:239], v[88:91], v[64:67]\n\t"
        "v_cvt_pk_bf16_f32 v119, v54, v55\n\t"
        "v_cvt_pk_bf16_f32 v120, v56, v57\n\t"
        "v_mfma_f32_16x16x32_bf16 v[68:71], v[240:243], v[88:91], v[68:71]\n\t"
        "v_cvt_pk_bf16_f32 v121, v58, v59\n\t"
        "s_waitcnt lgkmcnt(3)\n\t"
        "v_mfma_f32_16x16x32_bf16 v[60:63], a[180:183], v[92:95], v[60:63]\n\t"
        "ds_read_b128 v[88:91], v187 offset:64\n\t"
        "v_mfma_f32_16x16x32_bf16 v[64:67], a[184:187], v[92:95], v[64:67]\n\t"
        "v_permlane16_swap_b32 v116, v118\n\t"
        "v_mfma_f32_16x16x32_bf16 v[68:71], a[188:191], v[92:95], v[68:71]\n\t"
        "v_permlane16_swap_b32 v117, v119\n\t"
        "v_mfma_f32_16x16x32_bf16 v[60:63], a[204:207], v[96:99], v[60:63]\n\t"
        "ds_read_b128 v[92:95], v187 offset:128\n\t"
        "v_add_u32 v187, s3, v177\n\t"
        "v_mfma_f32_16x16x32_bf16 v[64:67], a[208:211], v[96:99], v[64:67]\n\t"
        "v_max_u32 v187, v187, v77\n\t"
        "v_xor_b32 v78, v186, v78\n\t"
        "v_mfma_f32_16x16x32_bf16 v[68:71], a[212:215], v[96:99], v[68:71]\n\t"
        "buffer_store_dwordx4 v[116:119], v184, s[28:31], s51 offen sc1\n\t"
        "v_mfma_f32_16x16x32_bf16 v[60:63], a[228:231], v[100:103], v[60:63]\n\t"
        "ds_read_b128 v[96:99], v151 offset:0\n\t"
        "v_mfma_f32_16x16x32_bf16 v[64:67], a[232:235], v[100:103], v[64:67]\n\t"
        "ds_write_b64 v149, v[120:121]\n\t"
        "v_mfma_f32_16x16x32_bf16 v[68:71], a[236:239], v[100:103], v[68:71]\n\t"
        "v_mfma_f32_16x16x32_bf16 v[60:63], a[108:111], v[104:107], v[60:63]\n\t"
        "ds_read_b128 v[100:103], v151 offset:64\n\t"
        "v_mfma_f32_16x16x32_bf16 v[64:67], a[112:115], v[104:107], v[64:67]\n\t"
        "v_mfma_f32_16x16x32_bf16 v[68:71], a[116:119], v[104:107], v[68:71]\n\t"
        "s_waitcnt lgkmcnt(4)\n\t"
        "v_mfma_f32_16x16x32_bf16 v[60:63], a[132:135], v[108:111], v[60:63]\n\t"
        "ds_read_b128 v[104:107], v151 offset:128\n\t"
        "v_add_u32 v151, s3, v178\n\t"
        "v_mfma_f32_16x16x32_bf16 v[64:67], a[136:139], v[108:111], v[64:67]\n\t"
        "v_max_u32 v151, v151, v78\n\t"
        "v_xor_b32 v79, v186, v79\n\t"
        "v_mfma_f32_16x16x32_bf16 v[68:71], a[140:143], v[108:111], v[68:71]\n\t"
        "s_waitcnt lgkmcnt(2)\n\t"
        "s_barrier\n\t"
        "v_mfma_f32_16x16x32_bf16 v[60:63], a[156:159], v[112:115], v[60:63]\n\t"
        "ds_read_b128 v[108:111], v187 offset:0\n\t"
        "v_mfma_f32_16x16x32_bf16 v[64:67], a[160:163], v[112:115], v[64:67]\n\t"
        "ds_read_b128 v[124:127], v148 offset:4096\n\t"
        "v_mfma_f32_16x16x32_bf16 v[68:71], a[164:167], v[112:115], v[68:71]\n\t"
        "v_mfma_f32_16x16x32_bf16 v[60:63], a[36:39], v[84:87], v[60:63]\n\t"
        "ds_read_b128 v[112:115], v187 offset:64\n\t"
        "v_mfma_f32_16x16x32_bf16 v[64:67], a[40:43], v[84:87], v[64:67]\n\t"
        "v_mfma_f32_16x16x32_bf16 v[68:71], a[44:47], v[84:87], v[68:71]\n\t"
        "v_mfma_f32_16x16x32_bf16 v[60:63], a[60:63], v[88:91], v[60:63]\n\t"
        "ds_read_b128 v[84:87], v187 offset:128\n\t"
        "v_add_u32 v187, s3, v179\n\t"
        "v_mfma_f32_16x16x32_bf16 v[64:67], a[64:67], v[88:91], v[64:67]\n\t"
        "v_max_u32 v187, v187, v79\n\t"
        "v_xor_b32 v80, v186, v80\n\t"
        "v_mfma_f32_16x16x32_bf16 v[68:71], a[68:71], v[88:91], v[68:71]\n\t"
        "v_mfma_f32_16x16x32_bf16 v[60:63], a[84:87], v[92:95], v[60:63]\n\t"
        "ds_read_b128 v[88:91], v151 offset:0\n\t"
        "v_mfma_f32_16x16x32_bf16 v[64:67], a[88:91], v[92:95], v[64:67]\n\t"
        "s_waitcnt lgkmcnt(3)\n\t"
        "buffer_store_dwordx4 v[124:127], v185, s[28:31], s39 offen sc1\n\t"
        "v_mfma_f32_16x16x32_bf16 v[68:71], a[92:95], v[92:95], v[68:71]\n\t"
        "ds_read_b64 v[122:123], v150\n\t"
        "v_mfma_f32_16x16x32_bf16 v[60:63], a[0:3], v[96:99], v[60:63]\n\t"
        "ds_read_b128 v[92:95], v151 offset:64\n\t"
        "v_mfma_f32_16x16x32_bf16 v[64:67], a[4:7], v[96:99], v[64:67]\n\t"
        "v_mfma_f32_16x16x32_bf16 v[68:71], a[8:11], v[96:99], v[68:71]\n\t"
        "v_mfma_f32_16x16x32_bf16 v[60:63], a[24:27], v[100:103], v[60:63]\n\t"
        "ds_read_b128 v[96:99], v151 offset:128\n\t"
        "v_add_u32 v151, s3, v180\n\t"
        "v_mfma_f32_16x16x32_bf16 v[64:67], a[28:31], v[100:103], v[64:67]\n\t"
        "v_max_u32 v151, v151, v80\n\t"
        "v_mfma_f32_16x16x32_bf16 v[68:71], a[32:35], v[100:103], v[68:71]\n\t"
        "v_mfma_f32_16x16x32_bf16 v[60:63], a[12:15], v[104:107], v[60:63]\n\t"
        "ds_read_b128 v[100:103], v187 offset:0\n\t"
        "v_mfma_f32_16x16x32_bf16 v[64:67], a[16:19], v[104:107], v[64:67]\n\t"
        "v_mfma_f32_16x16x32_bf16 v[68:71], a[20:23], v[104:107], v[68:71]\n\t"
        "v_mfma_f32_16x16x32_bf16 v[60:63], a[48:51], v[108:111], v[60:63]\n\t"
        "ds_read_b128 v[104:107], v187 offset:64\n\t"
        "v_mfma_f32_16x16x32_bf16 v[64:67], a[52:55], v[108:111], v[64:67]\n\t"
        "v_mfma_f32_16x16x32_bf16 v[68:71], a[56:59], v[108:111], v[68:71]\n\t"
        "s_waitcnt lgkmcnt(3)\n\t"
        "v_mfma_f32_16x16x32_bf16 v[60:63], a[72:75], v[112:115], v[60:63]\n\t"
        "ds_read_b128 v[108:111], v187 offset:128\n\t"
        "v_mfma_f32_16x16x32_bf16 v[64:67], a[76:79], v[112:115], v[64:67]\n\t"
        "v_mfma_f32_16x16x32_bf16 v[68:71], a[80:83], v[112:115], v[68:71]\n\t"
        "v_mfma_f32_16x16x32_bf16 v[60:63], a[96:99], v[84:87], v[60:63]\n\t"
        "ds_read_b128 v[112:115], v151 offset:0\n\t"
        "v_mfma_f32_16x16x32_bf16 v[64:67], a[100:103], v[84:87], v[64:67]\n\t"
        "v_mfma_f32_16x16x32_bf16 v[68:71], a[104:107], v[84:87], v[68:71]\n\t"
        "v_mfma_f32_16x16x32_bf16 v[60:63], a[120:123], v[88:91], v[60:63]\n\t"
        "ds_read_b128 v[84:87], v151 offset:64\n\t"
        "v_mfma_f32_16x16x32_bf16 v[64:67], a[124:127], v[88:91], v[64:67]\n\t"
        "v_mfma_f32_16x16x32_bf16 v[68:71], a[128:131], v[88:91], v[68:71]\n\t"
        "v_mfma_f32_16x16x32_bf16 v[60:63], a[144:147], v[92:95], v[60:63]\n\t"
        "ds_read_b128 v[88:91], v151 offset:128\n\t"
        "v_mfma_f32_16x16x32_bf16 v[64:67], a[148:151], v[92:95], v[64:67]\n\t"
        "v_mfma_f32_16x16x32_bf16 v[68:71], a[152:155], v[92:95], v[68:71]\n\t"
        "s_waitcnt lgkmcnt(3)\n\t"
        "v_mfma_f32_16x16x32_bf16 v[60:63], a[168:171], v[96:99], v[60:63]\n\t"
        "v_mfma_f32_16x16x32_bf16 v[64:67], a[172:175], v[96:99], v[64:67]\n\t"
        "v_mfma_f32_16x16x32_bf16 v[68:71], a[176:179], v[96:99], v[68:71]\n\t"
        "v_mfma_f32_16x16x32_bf16 v[60:63], a[192:195], v[100:103], v[60:63]\n\t"
        "v_mfma_f32_16x16x32_bf16 v[64:67], a[196:199], v[100:103], v[64:67]\n\t"
        "v_mfma_f32_16x16x32_bf16 v[68:71], a[200:203], v[100:103], v[68:71]\n\t"
        "v_mfma_f32_16x16x32_bf16 v[60:63], a[216:219], v[104:107], v[60:63]\n\t"
        "v_mfma_f32_16x16x32_bf16 v[64:67], a[220:223], v[104:107], v[64:67]\n\t"
        "v_mfma_f32_16x16x32_bf16 v[68:71], a[224:227], v[104:107], v[68:71]\n\t"
        "v_mfma_f32_16x16x32_bf16 v[60:63], a[240:243], v[108:111], v[60:63]\n\t"
        "v_mfma_f32_16x16x32_bf16 v[64:67], a[244:247], v[108:111], v[64:67]\n\t"
        "v_mfma_f32_16x16x32_bf16 v[68:71], a[248:251], v[108:111], v[68:71]\n\t"
        "s_waitcnt lgkmcnt(0)\n\t"
        "v_mfma_f32_16x16x32_bf16 v[60:63], v[196:199], v[112:115], v[60:63]\n\t"
        "v_mfma_f32_16x16x32_bf16 v[64:67], v[200:203], v[112:115], v[64:67]\n\t"
        "v_mfma_f32_16x16x32_bf16 v[68:71], v[204:207], v[112:115], v[68:71]\n\t"
        "v_mfma_f32_16x16x32_bf16 v[60:63], v[220:223], v[84:87], v[60:63]\n\t"
        "v_mfma_f32_16x16x32_bf16 v[64:67], v[224:227], v[84:87], v[64:67]\n\t"
        "v_mfma_f32_16x16x32_bf16 v[68:71], v[228:231], v[84:87], v[68:71]\n\t"
        "v_mfma_f32_16x16x32_bf16 v[60:63], v[244:247], v[88:91], v[60:63]\n\t"
        "v_mfma_f32_16x16x32_bf16 v[64:67], v[248:251], v[88:91], v[64:67]\n\t"
        "v_mfma_f32_16x16x32_bf16 v[68:71], v[252:255], v[88:91], v[68:71]\n\t"
        "s_nop 5\n\t"
        "v_cvt_pk_bf16_f32 v116, v60, v61\n\t"
        "v_cvt_pk_bf16_f32 v117, v62, v63\n\t"
        "v_cvt_pk_bf16_f32 v118, v64, v65\n\t"
        "v_cvt_pk_bf16_f32 v119, v66, v67\n\t"
        "v_cvt_pk_bf16_f32 v120, v68, v69\n\t"
        "v_cvt_pk_bf16_f32 v121, v70, v71\n\t"
        "v_permlane16_swap_b32 v116, v118\n\t"
        "v_permlane16_swap_b32 v117, v119\n\t"
        "buffer_store_dwordx4 v[116:119], v184, s[28:31], s52 offen sc1\n\t"
        "v_permlane16_swap_b32 v120, v122\n\t"
        "v_permlane16_swap_b32 v121, v123\n\t"
        "buffer_store_dwordx4 v[120:123], v146, s[28:31], s52 offen sc1\n\t"
        "s_endpgm\n\t"
        "20:\n\t"
        "s_mov_b32 s22, 1806336\n\t"
        "s_mov_b32 s23, 0x20000\n\t"
        "v_lshrrev_b32 v160, 4, v156\n\t"
        "v_min_u32 v160, 13, v160\n\t"
        "v_mul_u32_u24 v160, 8064, v160\n\t"
        "v_mad_u32_u24 v151, v158, 36, v160\n\t"
        "s_mul_i32 s8, s5, 224\n\t"
        "s_add_u32 s8, s8, s6\n\t"
        "s_mul_i32 s48, s8, 36\n\t"
        "s_mul_i32 s41, s8, 192\n\t"
        "s_mov_b32 s54, 0x7fff7fff\n\t"
        "s_lshl_b32 s8, s4, 2\n\t"
        "s_add_u32 s8, s8, 161792\n\t"
        "v_mov_b32 v153, s8\n\t"
        "v_mov_b32 v154, 161792\n\t"
        "v_add_u32 v181, 61440, v137\n\t"
        "v_add_u32 v182, 61440, v138\n\t"
        "v_add_u32 v183, 61440, v139\n\t"
        "s_lshl_b32 s9, s49, 13\n\t"
        "v_add_u32 v131, s9, v128\n\t"
        "v_add_u32 v132, s9, v129\n\t"
        "v_add_u32 v133, s9, v130\n\t"
        "v_subrev_u32 v134, s9, v128\n\t"
        "v_subrev_u32 v135, s9, v129\n\t"
        "v_subrev_u32 v136, s9, v130\n\t"
        "buffer_load_dwordx4 v142, s[24:27], s36 offen offset:2048 lds\n\t"
        "s_add_u32 m0, s35, 20480\n\t"
        "s_add_u32 s43, s34, 215040\n\t"
        "buffer_load_dwordx4 v143, s[16:19], s43 offen  lds\n\t"
        "s_add_u32 m0, s35, 24576\n\t"
        "s_add_u32 s43, s34, 258048\n\t"
        "buffer_load_dwordx4 v143, s[16:19], s43 offen  lds\n\t"
        "s_add_u32 m0, s37, 36864\n\t"
        "s_add_u32 s36, s36, 6144\n\t"
        "buffer_load_dwordx4 v140, s[24:27], s36 offen offset:0 lds\n\t"
        "s_waitcnt vmcnt(10)\n\t"
        "s_barrier\n\t"
        "buffer_load_dwordx4 v141, s[24:27], s36 offen offset:1024 lds\n\t"
        "ds_read_b64_tr_b16 a[0:1], v137 offset:0\n\t"
        "ds_read_b64_tr_b16 a[2:3], v137 offset:768\n\t"
        "ds_read_b64_tr_b16 a[4:5], v138 offset:0\n\t"
        "ds_read_b64_tr_b16 a[6:7], v138 offset:768\n\t"
        "ds_read_b64_tr_b16 a[8:9], v139 offset:0\n\t"
        "ds_read_b64_tr_b16 a[10:11], v139 offset:768\n\t"
        "ds_read_b128 v[84:87], v129 offset:4096\n\t"
        "ds_read_b64_tr_b16 a[12:13], v137 offset:6144\n\t"
        "ds_read_b64_tr_b16 a[14:15], v137 offset:6912\n\t"
        "ds_read_b64_tr_b16 a[16:17], v138 offset:6144\n\t"
        "ds_read_b64_tr_b16 a[18:19], v138 offset:6912\n\t"
        "ds_read_b64_tr_b16 a[20:21], v139 offset:6144\n\t"
        "ds_read_b64_tr_b16 a[22:23], v139 offset:6912\n\t"
        "ds_read_b128 v[88:91], v129 offset:4224\n\t"
        "s_waitcnt lgkmcnt(7)\n\t"
        "v_mfma_f32_16x16x32_bf16 v[0:3], a[0:3], v[84:87], 0\n\t"
        "buffer_load_dwordx4 v142, s[24:27], s36 offen offset:2048 lds\n\t"
        "s_waitcnt vmcnt(10)\n\t"
        "s_barrier\n\t"
        "ds_read_b64_tr_b16 a[24:25], v137 offset:12288\n\t"
        "ds_read_b64_tr_b16 a[26:27], v137 offset:13056\n\t"
        "ds_read_b64_tr_b16 a[28:29], v138 offset:12288\n\t"
        "ds_read_b64_tr_b16 a[30:31], v138 offset:13056\n\t"
        "ds_read_b64_tr_b16 a[32:33], v139 offset:12288\n\t"
        "ds_read_b64_tr_b16 a[34:35], v139 offset:13056\n\t"
        "ds_read_b128 v[92:95], v129 offset:4160\n\t"
        "s_add_u32 m0, s35, 28672\n\t"
        "s_add_u32 s43, s34, 301056\n\t"
        "v_mfma_f32_16x16x32_bf16 v[4:7], a[4:7], v[84:87], 0\n\t"
        "buffer_load_dwordx4 v143, s[16:19], s43 offen  lds\n\t"
        "s_add_u32 m0, s35, 32768\n\t"
        "s_add_u32 s43, s34, 344064\n\t"
        "v_mfma_f32_16x16x32_bf16 v[8:11], a[8:11], v[84:87], 0\n\t"
        "buffer_load_dwordx4 v143, s[16:19], s43 offen  lds\n\t"
        "s_waitcnt vmcnt(10)\n\t"
        "s_barrier\n\t"
        "ds_read_b128 v[96:99], v129 offset:12288\n\t"
        "s_add_u32 m0, s37, 49152\n\t"
        "s_add_u32 s36, s36, 6144\n\t"
        "s_waitcnt lgkmcnt(8)\n\t"
        "v_mfma_f32_16x16x32_bf16 v[0:3], a[12:15], v[88:91], v[0:3]\n\t"
        "buffer_load_dwordx4 v140, s[24:27], s36 offen offset:0 lds\n\t"
        "v_mfma_f32_16x16x32_bf16 v[4:7], a[16:19], v[88:91], v[4:7]\n\t"
        "ds_read_b128 v[100:103], v129 offset:12416\n\t"
        "v_mfma_f32_16x16x32_bf16 v[8:11], a[20:23], v[88:91], v[8:11]\n\t"
        "ds_read_b128 v[104:107], v129 offset:12352\n\t"
        "s_waitcnt lgkmcnt(3)\n\t"
        "v_mfma_f32_16x16x32_bf16 v[0:3], a[24:27], v[92:95], v[0:3]\n\t"
        "buffer_load_dwordx4 v141, s[24:27], s36 offen offset:1024 lds\n\t"
        "v_mfma_f32_16x16x32_bf16 v[4:7], a[28:31], v[92:95], v[4:7]\n\t"
        "v_mfma_f32_16x16x32_bf16 v[8:11], a[32:35], v[92:95], v[8:11]\n\t"
        "s_waitcnt lgkmcnt(2)\n\t"
        "v_mfma_f32_16x16x32_bf16 v[12:15], a[0:3], v[96:99], 0\n\t"
        "v_mfma_f32_16x16x32_bf16 v[16:19], a[4:7], v[96:99], 0\n\t"
        "v_mfma_f32_16x16x32_bf16 v[20:23], a[8:11], v[96:99], 0\n\t"
        "buffer_load_dwordx4 v142, s[24:27], s36 offen offset:2048 lds\n\t"
        "s_waitcnt vmcnt(10)\n\t"
        "s_barrier\n\t"
        "ds_read_b64_tr_b16 a[36:37], v137 offset:24576\n\t"
        "ds_read_b64_tr_b16 a[38:39], v137 offset:25344\n\t"
        "ds_read_b64_tr_b16 a[40:41], v138 offset:24576\n\t"
        "ds_read_b64_tr_b16 a[42:43], v138 offset:25344\n\t"
        "ds_read_b64_tr_b16 a[44:45], v139 offset:24576\n\t"
        "ds_read_b64_tr_b16 a[46:47], v139 offset:25344\n\t"
        "ds_read_b128 v[108:111], v128 offset:4096\n\t"
        "s_add_u32 m0, s35, 0\n\t"
        "s_add_u32 s43, s34, 0\n\t"
        "s_waitcnt lgkmcnt(7)\n\t"
        "v_mfma_f32_16x16x32_bf16 v[12:15], a[12:15], v[100:103], v[12:15]\n\t"
        "buffer_load_dwordx4 v143, s[16:19], s43 offen  lds\n\t"
        "ds_read_b128 v[112:115], v128 offset:12288\n\t"
        "s_add_u32 m0, s35, 36864\n\t"
        "s_add_u32 s43, s34, 387072\n\t"
        "v_mfma_f32_16x16x32_bf16 v[16:19], a[16:19], v[100:103], v[16:19]\n\t"
        "buffer_load_dwordx4 v143, s[16:19], s43 offen  lds\n\t"
        "ds_read_b64_tr_b16 a[48:49], v137 offset:30720\n\t"
        "s_add_u32 m0, s35, 40960\n\t"
        "s_add_u32 s43, s34, 430080\n\t"
        "v_mfma_f32_16x16x32_bf16 v[20:23], a[20:23], v[100:103], v[20:23]\n\t"
        "ds_read_b64_tr_b16 a[50:51], v137 offset:31488\n\t"
        "ds_read_b64_tr_b16 a[52:53], v138 offset:30720\n\t"
        "v_mfma_f32_16x16x32_bf16 v[12:15], a[24:27], v[104:107], v[12:15]\n\t"
        "s_waitcnt vmcnt(10)\n\t"
        "s_barrier\n\t"
        "buffer_load_dwordx4 v143, s[16:19], s43 offen  lds\n\t"
        "ds_read_b128 v[84:87], v129 offset:20480\n\t"
        "s_add_u32 m0, s37, 61440\n\t"
        "s_add_u32 s36, s36, s38\n\t"
        "v_mfma_f32_16x16x32_bf16 v[16:19], a[28:31], v[104:107], v[16:19]\n\t"
        "ds_read_b128 v[88:91], v129 offset:20608\n\t"
        "ds_read_b128 v[92:95], v129 offset:20544\n\t"
        "v_mfma_f32_16x16x32_bf16 v[20:23], a[32:35], v[104:107], v[20:23]\n\t"
        "ds_read_b128 v[96:99], v128 offset:20480\n\t"
        "ds_read_b64_tr_b16 a[54:55], v138 offset:31488\n\t"
        "s_waitcnt lgkmcnt(9)\n\t"
        "v_mfma_f32_16x16x32_bf16 v[0:3], a[36:39], v[108:111], v[0:3]\n\t"
        "buffer_load_dwordx4 v140, s[24:27], s36 offen offset:0 lds\n\t"
        "ds_read_b64_tr_b16 a[56:57], v139 offset:30720\n\t"
        "v_mfma_f32_16x16x32_bf16 v[4:7], a[40:43], v[108:111], v[4:7]\n\t"
        "ds_read_b64_tr_b16 a[58:59], v139 offset:31488\n\t"
        "v_mfma_f32_16x16x32_bf16 v[8:11], a[44:47], v[108:111], v[8:11]\n\t"
        "ds_read_b128 v[100:103], v130 offset:4096\n\t"
        "s_waitcnt lgkmcnt(10)\n\t"
        "v_mfma_f32_16x16x32_bf16 v[12:15], a[36:39], v[112:115], v[12:15]\n\t"
        "ds_read_b128 v[104:107], v130 offset:12288\n\t"
        "v_mfma_f32_16x16x32_bf16 v[16:19], a[40:43], v[112:115], v[16:19]\n\t"
        "buffer_load_dwordx4 v141, s[24:27], s36 offen offset:1024 lds\n\t"
        "v_mfma_f32_16x16x32_bf16 v[20:23], a[44:47], v[112:115], v[20:23]\n\t"
        "ds_read_b128 v[108:111], v130 offset:20480\n\t"
        "s_waitcnt lgkmcnt(9)\n\t"
        "v_mfma_f32_16x16x32_bf16 v[24:27], a[0:3], v[84:87], 0\n\t"
        "v_mfma_f32_16x16x32_bf16 v[28:31], a[4:7], v[84:87], 0\n\t"
        "v_mfma_f32_16x16x32_bf16 v[32:35], a[8:11], v[84:87], 0\n\t"
        "s_waitcnt lgkmcnt(7)\n\t"
        "v_mfma_f32_16x16x32_bf16 v[24:27], a[12:15], v[88:91], v[24:27]\n\t"
        "v_mfma_f32_16x16x32_bf16 v[28:31], a[16:19], v[88:91], v[28:31]\n\t"
        "buffer_load_dwordx4 v142, s[24:27], s36 offen offset:2048 lds\n\t"
        "s_add_u32 m0, s35, 45056\n\t"
        "s_add_u32 s43, s34, 473088\n\t"
        "v_mfma_f32_16x16x32_bf16 v[32:35], a[20:23], v[88:91], v[32:35]\n\t"
        "v_mfma_f32_16x16x32_bf16 v[24:27], a[24:27], v[92:95], v[24:27]\n\t"
        "v_mfma_f32_16x16x32_bf16 v[28:31], a[28:31], v[92:95], v[28:31]\n\t"
        "v_mfma_f32_16x16x32_bf16 v[32:35], a[32:35], v[92:95], v[32:35]\n\t"
        "s_waitcnt lgkmcnt(1)\n\t"
        "v_mfma_f32_16x16x32_bf16 v[24:27], a[36:39], v[96:99], v[24:27]\n\t"
        "v_mfma_f32_16x16x32_bf16 v[28:31], a[40:43], v[96:99], v[28:31]\n\t"
        "buffer_load_dwordx4 v143, s[16:19], s43 offen  lds\n\t"
        "s_add_u32 m0, s35, 49152\n\t"
        "s_add_u32 s43, s34, 516096\n\t"
        "v_mfma_f32_16x16x32_bf16 v[32:35], a[44:47], v[96:99], v[32:35]\n\t"
        "v_mfma_f32_16x16x32_bf16 v[0:3], a[48:51], v[100:103], v[0:3]\n\t"
        "v_mfma_f32_16x16x32_bf16 v[4:7], a[52:55], v[100:103], v[4:7]\n\t"
        "s_waitcnt vmcnt(10)\n\t"
        "s_barrier\n\t"
        "buffer_load_dwordx4 v143, s[16:19], s43 offen  lds\n\t"
        "ds_read_b128 v[112:115], v129 offset:28672\n\t"
        "s_add_u32 m0, s37, 73728\n\t"
        "s_add_u32 s36, s36, 6144\n\t"
        "v_mfma_f32_16x16x32_bf16 v[8:11], a[56:59], v[100:103], v[8:11]\n\t"
        "ds_read_b128 v[84:87], v129 offset:28800\n\t"
        "ds_read_b128 v[88:91], v129 offset:28736\n\t"
        "v_mfma_f32_16x16x32_bf16 v[12:15], a[48:51], v[104:107], v[12:15]\n\t"
        "ds_read_b128 v[92:95], v128 offset:28672\n\t"
        "ds_read_b128 v[96:99], v130 offset:28672\n\t"
        "v_mfma_f32_16x16x32_bf16 v[16:19], a[52:55], v[104:107], v[16:19]\n\t"
        "ds_read_b64_tr_b16 a[60:61], v137 offset:36864\n\t"
        "ds_read_b64_tr_b16 a[62:63], v137 offset:37632\n\t"
        "v_mfma_f32_16x16x32_bf16 v[20:23], a[56:59], v[104:107], v[20:23]\n\t"
        "ds_read_b64_tr_b16 a[64:65], v138 offset:36864\n\t"
        "ds_read_b64_tr_b16 a[66:67], v138 offset:37632\n\t"
        "s_waitcnt lgkmcnt(9)\n\t"
        "v_mfma_f32_16x16x32_bf16 v[24:27], a[48:51], v[108:111], v[24:27]\n\t"
        "ds_read_b64_tr_b16 a[68:69], v139 offset:36864\n\t"
        "ds_read_b64_tr_b16 a[70:71], v139 offset:37632\n\t"
        "v_mfma_f32_16x16x32_bf16 v[28:31], a[52:55], v[108:111], v[28:31]\n\t"
        "ds_read_b128 v[100:103], v128 offset:4160\n\t"
        "s_mov_b32 s30, 9633792\n\t"
        "v_mfma_f32_16x16x32_bf16 v[32:35], a[56:59], v[108:111], v[32:35]\n\t"
        "ds_read_b128 v[104:107], v128 offset:12352\n\t"
        "s_mov_b32 s31, 0x20000\n\t"
        "s_waitcnt lgkmcnt(12)\n\t"
        "v_mfma_f32_16x16x32_bf16 v[36:39], a[0:3], v[112:115], 0\n\t"
        "ds_read_b128 v[108:111], v128 offset:20544\n\t"
        "s_lshl_b32 s11, s49, 7\n\t"
        "v_mfma_f32_16x16x32_bf16 v[40:43], a[4:7], v[112:115], 0\n\t"
        "buffer_load_dwordx4 v140, s[24:27], s36 offen offset:0 lds\n\t"
        "s_lshl_b32 s9, s49, 5\n\t"
        "v_mfma_f32_16x16x32_bf16 v[44:47], a[8:11], v[112:115], 0\n\t"
        "ds_read_b64_tr_b16 a[72:73], v137 offset:43008\n\t"
        "v_lshrrev_b32 v161, 1, v159\n\t"
        "s_waitcnt lgkmcnt(12)\n\t"
        "v_mfma_f32_16x16x32_bf16 v[36:39], a[12:15], v[84:87], v[36:39]\n\t"
        "ds_read_b128 v[112:115], v128 offset:28736\n\t"
        "ds_read_b64_tr_b16 a[74:75], v137 offset:43776\n\t"
        "v_mfma_f32_16x16x32_bf16 v[40:43], a[16:19], v[84:87], v[40:43]\n\t"
        "ds_read_b64_tr_b16 a[76:77], v138 offset:43008\n\t"
        "ds_read_b64_tr_b16 a[78:79], v138 offset:43776\n\t"
        "v_mfma_f32_16x16x32_bf16 v[44:47], a[20:23], v[84:87], v[44:47]\n\t"
        "ds_read_b64_tr_b16 a[80:81], v139 offset:43008\n\t"
        "ds_read_b64_tr_b16 a[82:83], v139 offset:43776\n\t"
        "v_mfma_f32_16x16x32_bf16 v[36:39], a[24:27], v[88:91], v[36:39]\n\t"
        "ds_read_b128 v[84:87], v130 offset:4160\n\t"
        "v_lshlrev_b32 v161, 4, v161\n\t"
        "v_mfma_f32_16x16x32_bf16 v[40:43], a[28:31], v[88:91], v[40:43]\n\t"
        "buffer_load_dwordx4 v141, s[24:27], s36 offen offset:1024 lds\n\t"
        "v_and_b32 v162, 1, v159\n\t"
        "v_mfma_f32_16x16x32_bf16 v[44:47], a[32:35], v[88:91], v[44:47]\n\t"
        "v_lshl_or_b32 v161, v162, 5, v161\n\t"
        "v_mul_u32_u24 v160, 192, v158\n\t"
        "s_waitcnt lgkmcnt(9)\n\t"
        "v_mfma_f32_16x16x32_bf16 v[36:39], a[36:39], v[92:95], v[36:39]\n\t"
        "ds_read_b128 v[88:91], v130 offset:12352\n\t"
        "v_add3_u32 v144, v160, v161, s11\n\t"
        "v_mfma_f32_16x16x32_bf16 v[40:43], a[40:43], v[92:95], v[40:43]\n\t"
        "v_xor_b32 v161, s9, v161\n\t"
        "v_add3_u32 v146, v160, v161, 64\n\t"
        "v_mfma_f32_16x16x32_bf16 v[44:47], a[44:47], v[92:95], v[44:47]\n\t"
        "buffer_load_dwordx4 v142, s[24:27], s36 offen offset:2048 lds\n\t"
        "s_add_u32 m0, s35, 53248\n\t"
        "v_mfma_f32_16x16x32_bf16 v[36:39], a[48:51], v[96:99], v[36:39]\n\t"
        "ds_read_b128 v[92:95], v130 offset:20544\n\t"
        "s_add_u32 s43, s34, 559104\n\t"
        "v_mfma_f32_16x16x32_bf16 v[40:43], a[52:55], v[96:99], v[40:43]\n\t"
        "v_lshrrev_b32 v160, 2, v157\n\t"
        "v_and_b32 v161, 3, v157\n\t"
        "v_mfma_f32_16x16x32_bf16 v[44:47], a[56:59], v[96:99], v[44:47]\n\t"
        "v_lshl_add_u32 v161, v161, 4, 64\n\t"
        "s_movk_i32 s11, 192\n\t"
        "v_mfma_f32_16x16x32_bf16 v[0:3], a[60:63], v[100:103], v[0:3]\n\t"
        "ds_read_b128 v[96:99], v130 offset:28736\n\t"
        "v_mad_u32_u24 v145, v160, s11, v161\n\t"
        "v_mfma_f32_16x16x32_bf16 v[4:7], a[64:67], v[100:103], v[4:7]\n\t"
        "s_waitcnt vmcnt(8) lgkmcnt(3)\n\t"
        "s_barrier\n\t"
        "v_mfma_f32_16x16x32_bf16 v[8:11], a[68:71], v[100:103], v[8:11]\n\t"
        "buffer_load_dwordx4 v143, s[16:19], s43 offen  lds\n\t"
        "s_add_u32 m0, s35, 57344\n\t"
        "v_mfma_f32_16x16x32_bf16 v[12:15], a[60:63], v[104:107], v[12:15]\n\t"
        "ds_read_b128 v[100:103], v129 offset:36864\n\t"
        "s_add_u32 s43, s34, 602112\n\t"
        "v_mfma_f32_16x16x32_bf16 v[16:19], a[64:67], v[104:107], v[16:19]\n\t"
        "ds_read_b64_tr_b16 a[84:85], v137 offset:49152\n\t"
        "s_lshr_b32 s8, s4, 1\n\t"
        "v_mfma_f32_16x16x32_bf16 v[20:23], a[68:71], v[104:107], v[20:23]\n\t"
        "ds_read_b64_tr_b16 a[86:87], v137 offset:49920\n\t"
        "s_lshl_b32 s10, s8, 10\n\t"
        "v_mfma_f32_16x16x32_bf16 v[24:27], a[60:63], v[108:111], v[24:27]\n\t"
        "ds_read_b128 v[104:107], v129 offset:36992\n\t"
        "s_add_u32 s10, s10, 73728\n\t"
        "v_mfma_f32_16x16x32_bf16 v[28:31], a[64:67], v[108:111], v[28:31]\n\t"
        "buffer_load_dwordx4 v143, s[16:19], s43 offen  lds\n\t"
        "s_add_u32 m0, s37, 0\n\t"
        "v_mfma_f32_16x16x32_bf16 v[32:35], a[68:71], v[108:111], v[32:35]\n\t"
        "ds_read_b64_tr_b16 a[88:89], v138 offset:49152\n\t"
        "s_add_u32 s36, s36, 6144\n\t"
        "v_mfma_f32_16x16x32_bf16 v[36:39], a[60:63], v[112:115], v[36:39]\n\t"
        "ds_read_b128 v[108:111], v129 offset:36928\n\t"
        "s_add_u32 s11, s10, s9\n\t"
        "v_mfma_f32_16x16x32_bf16 v[40:43], a[64:67], v[112:115], v[40:43]\n\t"
        "ds_read_b64_tr_b16 a[90:91], v138 offset:49920\n\t"
        "v_lshlrev_b32 v160, 6, v158\n\t"
        "v_mfma_f32_16x16x32_bf16 v[44:47], a[68:71], v[112:115], v[44:47]\n\t"
        "buffer_load_dwordx4 v140, s[24:27], s36 offen offset:0 lds\n\t"
        "v_lshl_add_u32 v160, v159, 3, v160\n\t"
        "v_mfma_f32_16x16x32_bf16 v[0:3], a[72:75], v[84:87], v[0:3]\n\t"
        "ds_read_b128 v[112:115], v128 offset:36864\n\t"
        "v_add_u32 v147, s11, v160\n\t"
        "v_mfma_f32_16x16x32_bf16 v[4:7], a[76:79], v[84:87], v[4:7]\n\t"
        "ds_read_b64_tr_b16 a[92:93], v139 offset:49152\n\t"
        "s_lshl_b32 s11, s49, 11\n\t"
        "v_mfma_f32_16x16x32_bf16 v[8:11], a[80:83], v[84:87], v[8:11]\n\t"
        "ds_read_b64_tr_b16 a[94:95], v139 offset:49920\n\t"
        "s_add_u32 s11, s11, s10\n\t"
        "s_waitcnt lgkmcnt(8)\n\t"
        "v_mfma_f32_16x16x32_bf16 v[12:15], a[72:75], v[88:91], v[12:15]\n\t"
        "ds_read_b128 v[84:87], v128 offset:4224\n\t"
        "v_add_u32 v148, s11, v140\n\t"
        "v_mfma_f32_16x16x32_bf16 v[16:19], a[76:79], v[88:91], v[16:19]\n\t"
        "buffer_load_dwordx4 v141, s[24:27], s36 offen offset:1024 lds\n\t"
        "s_lshl_b32 s11, s4, 9\n\t"
        "v_mfma_f32_16x16x32_bf16 v[20:23], a[80:83], v[88:91], v[20:23]\n\t"
        "ds_read_b64_tr_b16 a[96:97], v137 offset:55296\n\t"
        "s_add_u32 s11, s11, 83968\n\t"
        "v_mfma_f32_16x16x32_bf16 v[24:27], a[72:75], v[92:95], v[24:27]\n\t"
        "ds_read_b128 v[88:91], v128 offset:12416\n\t"
        "v_lshl_add_u32 v149, v157, 3, s11\n\t"
        "v_mfma_f32_16x16x32_bf16 v[28:31], a[76:79], v[92:95], v[28:31]\n\t"
        "ds_read_b64_tr_b16 a[98:99], v137 offset:56064\n\t"
        "v_xor_b32 v150, 512, v149\n\t"
        "v_mfma_f32_16x16x32_bf16 v[32:35], a[80:83], v[92:95], v[32:35]\n\t"
        "buffer_load_dwordx4 v142, s[24:27], s36 offen offset:2048 lds\n\t"
        "s_add_u32 m0, s35, 61440\n\t"
        "v_mfma_f32_16x16x32_bf16 v[36:39], a[72:75], v[96:99], v[36:39]\n\t"
        "ds_read_b128 v[92:95], v128 offset:20608\n\t"
        "s_add_u32 s43, s34, 645120\n\t"
        "v_mfma_f32_16x16x32_bf16 v[40:43], a[76:79], v[96:99], v[40:43]\n\t"
        "ds_read_b64_tr_b16 a[100:101], v138 offset:55296\n\t"
        "s_min_u32 s43, s43, 9590784\n\t"
        "v_mfma_f32_16x16x32_bf16 v[44:47], a[80:83], v[96:99], v[44:47]\n\t"
        "ds_read_b64_tr_b16 a[102:103], v138 offset:56064\n\t"
        "s_mul_i32 s8, s8, 43008\n\t"
        "v_mfma_f32_16x16x32_bf16 v[48:51], a[0:3], v[100:103], 0\n\t"
        "ds_read_b128 v[96:99], v128 offset:28800\n\t"
        "s_add_u32 s41, s41, s8\n\t"
        "v_mfma_f32_16x16x32_bf16 v[52:55], a[4:7], v[100:103], 0\n\t"
        "buffer_load_dwordx4 v143, s[16:19], s43 offen  lds\n\t"
        "s_add_u32 m0, s37, 12288\n\t"
        "v_mfma_f32_16x16x32_bf16 v[56:59], a[8:11], v[100:103], 0\n\t"
        "ds_read_b64_tr_b16 a[104:105], v139 offset:55296\n\t"
        "s_add_u32 s36, s36, s38\n\t"
        "s_waitcnt lgkmcnt(8)\n\t"
        "v_mfma_f32_16x16x32_bf16 v[48:51], a[12:15], v[104:107], v[48:51]\n\t"
        "ds_read_b128 v[100:103], v130 offset:36864\n\t"
        "s_mul_i32 s8, s49, 86016\n\t"
        "v_mfma_f32_16x16x32_bf16 v[52:55], a[16:19], v[104:107], v[52:55]\n\t"
        "ds_read_b64_tr_b16 a[106:107], v139 offset:56064\n\t"
        "s_add_u32 s50, s41, s8\n\t"
        "v_mfma_f32_16x16x32_bf16 v[56:59], a[20:23], v[104:107], v[56:59]\n\t"
        "buffer_load_dwordx4 v140, s[24:27], s36 offen offset:0 lds\n\t"
        "s_add_u32 s51, s50, 430080\n\t"
        "v_mfma_f32_16x16x32_bf16 v[48:51], a[24:27], v[108:111], v[48:51]\n\t"
        "ds_read_b128 v[104:107], v130 offset:4224\n\t"
        "s_sub_u32 s52, s41, s8\n\t"
        "v_mfma_f32_16x16x32_bf16 v[52:55], a[28:31], v[108:111], v[52:55]\n\t"
        "s_add_u32 s52, s52, 516096\n\t"
        "s_lshl_b32 s8, s49, 30\n\t"
        "v_mfma_f32_16x16x32_bf16 v[56:59], a[32:35], v[108:111], v[56:59]\n\t"
        "s_add_u32 s53, s41, s8\n\t"
        "v_readlane_b32 s8, v170, 31\n\t"
        "v_mfma_f32_16x16x32_bf16 v[48:51], a[36:39], v[112:115], v[48:51]\n\t"
        "ds_read_b128 v[108:111], v130 offset:12416\n\t"
        "v_mov_b32 v155, s8\n\t"
        "v_mfma_f32_16x16x32_bf16 v[52:55], a[40:43], v[112:115], v[52:55]\n\t"
        "buffer_load_dwordx4 v141, s[24:27], s36 offen offset:1024 lds\n\t"
        "v_mfma_f32_16x16x32_bf16 v[56:59], a[44:47], v[112:115], v[56:59]\n\t"
        "v_mfma_f32_16x16x32_bf16 v[0:3], a[84:87], v[84:87], v[0:3]\n\t"
        "ds_read_b128 v[112:115], v130 offset:20608\n\t"
        "v_mfma_f32_16x16x32_bf16 v[4:7], a[88:91], v[84:87], v[4:7]\n\t"
        "v_mfma_f32_16x16x32_bf16 v[8:11], a[92:95], v[84:87], v[8:11]\n\t"
        "buffer_load_dwordx4 v142, s[24:27], s36 offen offset:2048 lds\n\t"
        "s_waitcnt lgkmcnt(3)\n\t"
        "v_mfma_f32_16x16x32_bf16 v[12:15], a[84:87], v[88:91], v[12:15]\n\t"
        "ds_read_b128 v[84:87], v130 offset:28800\n\t"
        "v_mfma_f32_16x16x32_bf16 v[16:19], a[88:91], v[88:91], v[16:19]\n\t"
        "v_mfma_f32_16x16x32_bf16 v[20:23], a[92:95], v[88:91], v[20:23]\n\t"
        "v_mfma_f32_16x16x32_bf16 v[24:27], a[84:87], v[92:95], v[24:27]\n\t"
        "ds_read_b128 v[88:91], v128 offset:36928\n\t"
        "v_mfma_f32_16x16x32_bf16 v[28:31], a[88:91], v[92:95], v[28:31]\n\t"
        "buffer_load_dwordx4 v[172:175], v151, s[20:23], s48 offen\n\t"
        "v_mfma_f32_16x16x32_bf16 v[32:35], a[92:95], v[92:95], v[32:35]\n\t"
        "s_waitcnt vmcnt(10)\n\t"
        "s_barrier\n\t"
        "v_mfma_f32_16x16x32_bf16 v[36:39], a[84:87], v[96:99], v[36:39]\n\t"
        "ds_read_b128 v[92:95], v130 offset:36928\n\t"
        "v_mfma_f32_16x16x32_bf16 v[40:43], a[88:91], v[96:99], v[40:43]\n\t"
        "ds_read_b64_tr_b16 a[108:109], v181 offset:0\n\t"
        "v_mfma_f32_16x16x32_bf16 v[44:47], a[92:95], v[96:99], v[44:47]\n\t"
        "buffer_load_dwordx4 v[176:179], v151, s[20:23], s48 offen offset:16\n\t"
        "v_mfma_f32_16x16x32_bf16 v[48:51], a[48:51], v[100:103], v[48:51]\n\t"
        "ds_read_b128 v[96:99], v128 offset:36992\n\t"
        "v_mfma_f32_16x16x32_bf16 v[52:55], a[52:55], v[100:103], v[52:55]\n\t"
        "ds_read_b64_tr_b16 a[110:111], v181 offset:768\n\t"
        "v_mfma_f32_16x16x32_bf16 v[56:59], a[56:59], v[100:103], v[56:59]\n\t"
        "ds_read_b64_tr_b16 a[112:113], v182 offset:0\n\t"
        "s_waitcnt lgkmcnt(6)\n\t"
        "v_mfma_f32_16x16x32_bf16 v[0:3], a[96:99], v[104:107], v[0:3]\n\t"
        "ds_read_b128 v[100:103], v130 offset:36992\n\t"
        "ds_read_b64_tr_b16 a[114:115], v182 offset:768\n\t"
        "v_mfma_f32_16x16x32_bf16 v[4:7], a[100:103], v[104:107], v[4:7]\n\t"
        "ds_read_b64_tr_b16 a[116:117], v183 offset:0\n\t"
        "ds_read_b64_tr_b16 a[118:119], v183 offset:768\n\t"
        "v_mfma_f32_16x16x32_bf16 v[8:11], a[104:107], v[104:107], v[8:11]\n\t"
        "buffer_load_dword v180, v151, s[20:23], s48 offen offset:32\n\t"
        "s_add_u32 m0, s37, 24576\n\t"
        "v_mfma_f32_16x16x32_bf16 v[12:15], a[96:99], v[108:111], v[12:15]\n\t"
        "ds_read_b128 v[104:107], v130 offset:0\n\t"
        "s_add_u32 s36, s36, 6144\n\t"
        "v_mfma_f32_16x16x32_bf16 v[16:19], a[100:103], v[108:111], v[16:19]\n\t"
        "ds_read_b64_tr_b16 a[120:121], v181 offset:6144\n\t"
        "v_mfma_f32_16x16x32_bf16 v[20:23], a[104:107], v[108:111], v[20:23]\n\t"
        "ds_read_b64_tr_b16 a[122:123], v181 offset:6912\n\t"
        "v_mfma_f32_16x16x32_bf16 v[24:27], a[96:99], v[112:115], v[24:27]\n\t"
        "ds_read_b128 v[108:111], v130 offset:8192\n\t"
        "v_mfma_f32_16x16x32_bf16 v[28:31], a[100:103], v[112:115], v[28:31]\n\t"
        "buffer_load_dwordx4 v140, s[24:27], s36 offen offset:0 lds\n\t"
        "v_mfma_f32_16x16x32_bf16 v[32:35], a[104:107], v[112:115], v[32:35]\n\t"
        "ds_read_b64_tr_b16 a[124:125], v182 offset:6144\n\t"
        "v_mfma_f32_16x16x32_bf16 v[36:39], a[96:99], v[84:87], v[36:39]\n\t"
        "ds_read_b128 v[112:115], v130 offset:16384\n\t"
        "v_mfma_f32_16x16x32_bf16 v[40:43], a[100:103], v[84:87], v[40:43]\n\t"
        "ds_read_b64_tr_b16 a[126:127], v182 offset:6912\n\t"
        "v_mfma_f32_16x16x32_bf16 v[44:47], a[104:107], v[84:87], v[44:47]\n\t"
        "buffer_load_dwordx4 v141, s[24:27], s36 offen offset:1024 lds\n\t"
        "s_waitcnt lgkmcnt(7)\n\t"
        "v_mfma_f32_16x16x32_bf16 v[48:51], a[60:63], v[88:91], v[48:51]\n\t"
        "ds_read_b128 v[84:87], v130 offset:24576\n\t"
        "v_mfma_f32_16x16x32_bf16 v[52:55], a[64:67], v[88:91], v[52:55]\n\t"
        "ds_read_b64_tr_b16 a[128:129], v183 offset:6144\n\t"
        "v_mfma_f32_16x16x32_bf16 v[56:59], a[68:71], v[88:91], v[56:59]\n\t"
        "ds_read_b64_tr_b16 a[130:131], v183 offset:6912\n\t"
        "v_mfma_f32_16x16x32_bf16 v[48:51], a[72:75], v[92:95], v[48:51]\n\t"
        "ds_read_b128 v[88:91], v128 offset:8192\n\t"
        "v_mfma_f32_16x16x32_bf16 v[52:55], a[76:79], v[92:95], v[52:55]\n\t"
        "buffer_load_dwordx4 v142, s[24:27], s36 offen offset:2048 lds\n\t"
        "s_add_u32 m0, s37, 36864\n\t"
        "v_mfma_f32_16x16x32_bf16 v[56:59], a[80:83], v[92:95], v[56:59]\n\t"
        "ds_read_b64_tr_b16 a[132:133], v181 offset:12288\n\t"
        "s_add_u32 s36, s36, 6144\n\t"
        "v_mfma_f32_16x16x32_bf16 v[48:51], a[84:87], v[96:99], v[48:51]\n\t"
        "ds_read_b128 v[92:95], v128 offset:16384\n\t"
        "v_mfma_f32_16x16x32_bf16 v[52:55], a[88:91], v[96:99], v[52:55]\n\t"
        "ds_read_b64_tr_b16 a[134:135], v181 offset:13056\n\t"
        "v_mfma_f32_16x16x32_bf16 v[56:59], a[92:95], v[96:99], v[56:59]\n\t"
        "buffer_load_dwordx4 v140, s[24:27], s36 offen offset:0 lds\n\t"
        "v_mfma_f32_16x16x32_bf16 v[48:51], a[96:99], v[100:103], v[48:51]\n\t"
        "ds_read_b128 v[96:99], v128 offset:24576\n\t"
        "v_mfma_f32_16x16x32_bf16 v[52:55], a[100:103], v[100:103], v[52:55]\n\t"
        "ds_read_b64_tr_b16 a[136:137], v182 offset:12288\n\t"
        "v_mfma_f32_16x16x32_bf16 v[56:59], a[104:107], v[100:103], v[56:59]\n\t"
        "ds_read_b64_tr_b16 a[138:139], v182 offset:13056\n\t"
        "s_waitcnt lgkmcnt(8)\n\t"
        "v_mfma_f32_16x16x32_bf16 v[0:3], a[108:111], v[104:107], v[0:3]\n\t"
        "ds_read_b128 v[100:103], v128 offset:32768\n\t"
        "v_mfma_f32_16x16x32_bf16 v[4:7], a[112:115], v[104:107], v[4:7]\n\t"
        "buffer_load_dwordx4 v141, s[24:27], s36 offen offset:1024 lds\n\t"
        "v_mfma_f32_16x16x32_bf16 v[8:11], a[116:119], v[104:107], v[8:11]\n\t"
        "ds_read_b64_tr_b16 a[140:141], v183 offset:12288\n\t"
        "v_mfma_f32_16x16x32_bf16 v[12:15], a[108:111], v[108:111], v[12:15]\n\t"
        "ds_read_b128 v[104:107], v130 offset:32768\n\t"
        "v_mfma_f32_16x16x32_bf16 v[16:19], a[112:115], v[108:111], v[16:19]\n\t"
        "ds_read_b64_tr_b16 a[142:143], v183 offset:13056\n\t"
        "v_mfma_f32_16x16x32_bf16 v[20:23], a[116:119], v[108:111], v[20:23]\n\t"
        "buffer_load_dwordx4 v142, s[24:27], s36 offen offset:2048 lds\n\t"
        "s_add_u32 m0, s37, 49152\n\t"
        "v_mfma_f32_16x16x32_bf16 v[24:27], a[108:111], v[112:115], v[24:27]\n\t"
        "ds_read_b128 v[108:111], v130 offset:64\n\t"
        "s_add_u32 s36, s36, s38\n\t"
        "v_mfma_f32_16x16x32_bf16 v[28:31], a[112:115], v[112:115], v[28:31]\n\t"
        "ds_read_b64_tr_b16 a[144:145], v181 offset:18432\n\t"
        "v_mfma_f32_16x16x32_bf16 v[32:35], a[116:119], v[112:115], v[32:35]\n\t"
        "ds_read_b64_tr_b16 a[146:147], v181 offset:19200\n\t"
        "v_mfma_f32_16x16x32_bf16 v[36:39], a[108:111], v[84:87], v[36:39]\n\t"
        "ds_read_b128 v[112:115], v130 offset:8256\n\t"
        "v_mfma_f32_16x16x32_bf16 v[40:43], a[112:115], v[84:87], v[40:43]\n\t"
        "buffer_load_dwordx4 v140, s[24:27], s36 offen offset:0 lds\n\t"
        "v_mfma_f32_16x16x32_bf16 v[44:47], a[116:119], v[84:87], v[44:47]\n\t"
        "s_waitcnt vmcnt(10) lgkmcnt(7)\n\t"
        "s_barrier\n\t"
        "v_mfma_f32_16x16x32_bf16 v[0:3], a[120:123], v[88:91], v[0:3]\n\t"
        "ds_read_b128 v[84:87], v130 offset:16448\n\t"
        "v_mfma_f32_16x16x32_bf16 v[4:7], a[124:127], v[88:91], v[4:7]\n\t"
        "ds_read_b64_tr_b16 a[148:149], v182 offset:18432\n\t"
        "v_mfma_f32_16x16x32_bf16 v[8:11], a[128:131], v[88:91], v[8:11]\n\t"
        "buffer_load_dwordx4 v141, s[24:27], s36 offen offset:1024 lds\n\t"
        "v_mfma_f32_16x16x32_bf16 v[12:15], a[120:123], v[92:95], v[12:15]\n\t"
        "ds_read_b128 v[88:91], v130 offset:24640\n\t"
        "v_mfma_f32_16x16x32_bf16 v[16:19], a[124:127], v[92:95], v[16:19]\n\t"
        "ds_read_b64_tr_b16 a[150:151], v182 offset:19200\n\t"
        "v_mfma_f32_16x16x32_bf16 v[20:23], a[128:131], v[92:95], v[20:23]\n\t"
        "ds_read_b64_tr_b16 a[152:153], v183 offset:18432\n\t"
        "v_mfma_f32_16x16x32_bf16 v[24:27], a[120:123], v[96:99], v[24:27]\n\t"
        "ds_read_b128 v[92:95], v128 offset:40960\n\t"
        "v_mfma_f32_16x16x32_bf16 v[28:31], a[124:127], v[96:99], v[28:31]\n\t"
        "buffer_load_dwordx4 v142, s[24:27], s36 offen offset:2048 lds\n\t"
        "s_add_u32 m0, s37, 61440\n\t"
        "v_mfma_f32_16x16x32_bf16 v[32:35], a[128:131], v[96:99], v[32:35]\n\t"
        "ds_read_b64_tr_b16 a[154:155], v183 offset:19200\n\t"
        "s_add_u32 s36, s36, 6144\n\t"
        "v_mfma_f32_16x16x32_bf16 v[36:39], a[120:123], v[100:103], v[36:39]\n\t"
        "ds_read_b128 v[96:99], v128 offset:8256\n\t"
        "v_mfma_f32_16x16x32_bf16 v[40:43], a[124:127], v[100:103], v[40:43]\n\t"
        "ds_read_b64_tr_b16 a[156:157], v137 offset:0\n\t"
        "v_mfma_f32_16x16x32_bf16 v[44:47], a[128:131], v[100:103], v[44:47]\n\t"
        "buffer_load_dwordx4 v140, s[24:27], s36 offen offset:0 lds\n\t"
        "s_waitcnt lgkmcnt(7)\n\t"
        "v_mfma_f32_16x16x32_bf16 v[48:51], a[108:111], v[104:107], v[48:51]\n\t"
        "ds_read_b128 v[100:103], v128 offset:16448\n\t"
        "v_mfma_f32_16x16x32_bf16 v[52:55], a[112:115], v[104:107], v[52:55]\n\t"
        "ds_read_b64_tr_b16 a[158:159], v137 offset:768\n\t"
        "v_mfma_f32_16x16x32_bf16 v[56:59], a[116:119], v[104:107], v[56:59]\n\t"
        "ds_read_b64_tr_b16 a[160:161], v138 offset:0\n\t"
        "v_mfma_f32_16x16x32_bf16 v[0:3], a[132:135], v[108:111], v[0:3]\n\t"
        "ds_read_b128 v[104:107], v128 offset:24640\n\t"
        "v_mfma_f32_16x16x32_bf16 v[4:7], a[136:139], v[108:111], v[4:7]\n\t"
        "buffer_load_dwordx4 v141, s[24:27], s36 offen offset:1024 lds\n\t"
        "v_mfma_f32_16x16x32_bf16 v[8:11], a[140:143], v[108:111], v[8:11]\n\t"
        "ds_read_b64_tr_b16 a[162:163], v138 offset:768\n\t"
        "v_mfma_f32_16x16x32_bf16 v[12:15], a[132:135], v[112:115], v[12:15]\n\t"
        "ds_read_b128 v[108:111], v128 offset:32832\n\t"
        "v_mfma_f32_16x16x32_bf16 v[16:19], a[136:139], v[112:115], v[16:19]\n\t"
        "ds_read_b64_tr_b16 a[164:165], v139 offset:0\n\t"
        "v_mfma_f32_16x16x32_bf16 v[20:23], a[140:143], v[112:115], v[20:23]\n\t"
        "buffer_load_dwordx4 v142, s[24:27], s36 offen offset:2048 lds\n\t"
        "s_add_u32 m0, s37, 73728\n\t"
        "v_mfma_f32_16x16x32_bf16 v[24:27], a[132:135], v[84:87], v[24:27]\n\t"
        "ds_read_b128 v[112:115], v130 offset:32832\n\t"
        "s_add_u32 s36, s36, 6144\n\t"
        "v_mfma_f32_16x16x32_bf16 v[28:31], a[136:139], v[84:87], v[28:31]\n\t"
        "ds_read_b64_tr_b16 a[166:167], v139 offset:768\n\t"
        "v_mfma_f32_16x16x32_bf16 v[32:35], a[140:143], v[84:87], v[32:35]\n\t"
        "ds_read_b64_tr_b16 a[168:169], v137 offset:6144\n\t"
        "s_waitcnt lgkmcnt(8)\n\t"
        "v_mfma_f32_16x16x32_bf16 v[36:39], a[132:135], v[88:91], v[36:39]\n\t"
        "ds_read_b128 v[84:87], v130 offset:128\n\t"
        "v_mfma_f32_16x16x32_bf16 v[40:43], a[136:139], v[88:91], v[40:43]\n\t"
        "ds_read_b64_tr_b16 a[170:171], v137 offset:6912\n\t"
        "v_mfma_f32_16x16x32_bf16 v[44:47], a[140:143], v[88:91], v[44:47]\n\t"
        "ds_read_b64_tr_b16 a[172:173], v138 offset:6144\n\t"
        "v_mfma_f32_16x16x32_bf16 v[48:51], a[120:123], v[92:95], v[48:51]\n\t"
        "ds_read_b128 v[88:91], v130 offset:8320\n\t"
        "v_mfma_f32_16x16x32_bf16 v[52:55], a[124:127], v[92:95], v[52:55]\n\t"
        "ds_read_b64_tr_b16 a[174:175], v138 offset:6912\n\t"
        "v_mfma_f32_16x16x32_bf16 v[56:59], a[128:131], v[92:95], v[56:59]\n\t"
        "ds_read_b64_tr_b16 a[176:177], v139 offset:6144\n\t"
        "v_mfma_f32_16x16x32_bf16 v[0:3], a[144:147], v[96:99], v[0:3]\n\t"
        "ds_read_b128 v[92:95], v130 offset:16512\n\t"
        "v_mfma_f32_16x16x32_bf16 v[4:7], a[148:151], v[96:99], v[4:7]\n\t"
        "ds_read_b64_tr_b16 a[178:179], v139 offset:6912\n\t"
        "v_mfma_f32_16x16x32_bf16 v[8:11], a[152:155], v[96:99], v[8:11]\n\t"
        "ds_read_b64_tr_b16 a[180:181], v137 offset:12288\n\t"
        "v_mfma_f32_16x16x32_bf16 v[12:15], a[144:147], v[100:103], v[12:15]\n\t"
        "ds_read_b128 v[96:99], v128 offset:8320\n\t"
        "v_mfma_f32_16x16x32_bf16 v[16:19], a[148:151], v[100:103], v[16:19]\n\t"
        "ds_read_b64_tr_b16 a[182:183], v137 offset:13056\n\t"
        "v_mfma_f32_16x16x32_bf16 v[20:23], a[152:155], v[100:103], v[20:23]\n\t"
        "ds_read_b64_tr_b16 a[184:185], v138 offset:12288\n\t"
        "s_waitcnt lgkmcnt(10)\n\t"
        "v_mfma_f32_16x16x32_bf16 v[24:27], a[144:147], v[104:107], v[24:27]\n\t"
        "ds_read_b128 v[100:103], v128 offset:16512\n\t"
        "v_mfma_f32_16x16x32_bf16 v[28:31], a[148:151], v[104:107], v[28:31]\n\t"
        "ds_read_b64_tr_b16 a[186:187], v138 offset:13056\n\t"
        "v_mfma_f32_16x16x32_bf16 v[32:35], a[152:155], v[104:107], v[32:35]\n\t"
        "ds_read_b64_tr_b16 a[188:189], v139 offset:12288\n\t"
        "v_mfma_f32_16x16x32_bf16 v[36:39], a[144:147], v[108:111], v[36:39]\n\t"
        "ds_read_b128 v[104:107], v128 offset:24704\n\t"
        "v_mfma_f32_16x16x32_bf16 v[40:43], a[148:151], v[108:111], v[40:43]\n\t"
        "ds_read_b64_tr_b16 a[190:191], v139 offset:13056\n\t"
        "v_mfma_f32_16x16x32_bf16 v[44:47], a[152:155], v[108:111], v[44:47]\n\t"
        "ds_read_b64_tr_b16 a[192:193], v137 offset:18432\n\t"
        "v_mfma_f32_16x16x32_bf16 v[48:51], a[132:135], v[112:115], v[48:51]\n\t"
        "ds_read_b128 v[108:111], v129 offset:0\n\t"
        "v_mfma_f32_16x16x32_bf16 v[52:55], a[136:139], v[112:115], v[52:55]\n\t"
        "ds_read_b64_tr_b16 a[194:195], v137 offset:19200\n\t"
        "v_mov_b32 v161, v155\n\t"
        "v_mfma_f32_16x16x32_bf16 v[56:59], a[140:143], v[112:115], v[56:59]\n\t"
        "ds_read_b64_tr_b16 a[196:197], v138 offset:18432\n\t"
        "s_waitcnt vmcnt(14)\n\t"
        "v_and_b32 v172, s54, v172\n\t"
        "v_mfma_f32_16x16x32_bf16 v[0:3], a[156:159], v[84:87], v[0:3]\n\t"
        "ds_read_b128 v[112:115], v129 offset:8192\n\t"
        "v_pk_max_u16 v161, v161, v172\n\t"
        "v_mfma_f32_16x16x32_bf16 v[4:7], a[160:163], v[84:87], v[4:7]\n\t"
        "ds_read_b64_tr_b16 a[198:199], v138 offset:19200\n\t"
        "v_and_b32 v173, s54, v173\n\t"
        "v_mfma_f32_16x16x32_bf16 v[8:11], a[164:167], v[84:87], v[8:11]\n\t"
        "ds_read_b64_tr_b16 a[200:201], v139 offset:18432\n\t"
        "v_pk_max_u16 v161, v161, v173\n\t"
        "s_waitcnt lgkmcnt(10)\n\t"
        "v_mfma_f32_16x16x32_bf16 v[12:15], a[156:159], v[88:91], v[12:15]\n\t"
        "ds_read_b128 v[84:87], v129 offset:16384\n\t"
        "v_and_b32 v174, s54, v174\n\t"
        "v_mfma_f32_16x16x32_bf16 v[16:19], a[160:163], v[88:91], v[16:19]\n\t"
        "ds_read_b64_tr_b16 a[202:203], v139 offset:19200\n\t"
        "v_pk_max_u16 v161, v161, v174\n\t"
        "v_mfma_f32_16x16x32_bf16 v[20:23], a[164:167], v[88:91], v[20:23]\n\t"
        "v_and_b32 v175, s54, v175\n\t"
        "v_pk_max_u16 v161, v161, v175\n\t"
        "v_mfma_f32_16x16x32_bf16 v[24:27], a[156:159], v[92:95], v[24:27]\n\t"
        "ds_read_b128 v[88:91], v129 offset:8192\n\t"
        "s_waitcnt vmcnt(13)\n\t"
        "v_and_b32 v176, s54, v176\n\t"
        "v_mfma_f32_16x16x32_bf16 v[28:31], a[160:163], v[92:95], v[28:31]\n\t"
        "v_pk_max_u16 v161, v161, v176\n\t"
        "v_and_b32 v177, s54, v177\n\t"
        "v_mfma_f32_16x16x32_bf16 v[32:35], a[164:167], v[92:95], v[32:35]\n\t"
        "v_pk_max_u16 v161, v161, v177\n\t"
        "v_and_b32 v178, s54, v178\n\t"
        "v_mfma_f32_16x16x32_bf16 v[0:3], a[168:171], v[96:99], v[0:3]\n\t"
        "ds_read_b128 v[92:95], v129 offset:16384\n\t"
        "v_pk_max_u16 v161, v161, v178\n\t"
        "v_mfma_f32_16x16x32_bf16 v[4:7], a[172:175], v[96:99], v[4:7]\n\t"
        "v_and_b32 v179, s54, v179\n\t"
        "v_pk_max_u16 v161, v161, v179\n\t"
        "v_mfma_f32_16x16x32_bf16 v[8:11], a[176:179], v[96:99], v[8:11]\n\t"
        "s_waitcnt vmcnt(12)\n\t"
        "v_and_b32 v180, s54, v180\n\t"
        "v_pk_max_u16 v161, v161, v180\n\t"
        "v_mfma_f32_16x16x32_bf16 v[12:15], a[168:171], v[100:103], v[12:15]\n\t"
        "ds_read_b128 v[96:99], v129 offset:24576\n\t"
        "v_cmp_ne_u32 vcc, v161, v155\n\t"
        "v_mfma_f32_16x16x32_bf16 v[16:19], a[172:175], v[100:103], v[16:19]\n\t"
        "s_nop 0\n\t"
        "s_or_b32 s8, vcc_lo, vcc_hi\n\t"
        "v_mov_b32 v161, s8\n\t"
        "v_mfma_f32_16x16x32_bf16 v[20:23], a[176:179], v[100:103], v[20:23]\n\t"
        "ds_write_b32 v153, v161\n\t"
        "s_waitcnt lgkmcnt(4)\n\t"
        "v_mfma_f32_16x16x32_bf16 v[24:27], a[168:171], v[104:107], v[24:27]\n\t"
        "ds_read_b128 v[100:103], v130 offset:24704\n\t"
        "v_mfma_f32_16x16x32_bf16 v[28:31], a[172:175], v[104:107], v[28:31]\n\t"
        "v_mfma_f32_16x16x32_bf16 v[32:35], a[176:179], v[104:107], v[32:35]\n\t"
        "v_mfma_f32_16x16x32_bf16 v[0:3], a[180:183], v[108:111], v[0:3]\n\t"
        "ds_read_b128 v[104:107], v128 offset:32896\n\t"
        "v_mfma_f32_16x16x32_bf16 v[4:7], a[184:187], v[108:111], v[4:7]\n\t"
        "s_waitcnt lgkmcnt(2)\n\t"
        "s_barrier\n\t"
        "v_mfma_f32_16x16x32_bf16 v[8:11], a[188:191], v[108:111], v[8:11]\n\t"
        "ds_read_b128 v[164:167], v154\n\t"
        "v_mfma_f32_16x16x32_bf16 v[12:15], a[180:183], v[112:115], v[12:15]\n\t"
        "ds_read_b128 v[108:111], v129 offset:24576\n\t"
        "v_mfma_f32_16x16x32_bf16 v[16:19], a[184:187], v[112:115], v[16:19]\n\t"
        "v_mfma_f32_16x16x32_bf16 v[20:23], a[188:191], v[112:115], v[20:23]\n\t"
        "v_mfma_f32_16x16x32_bf16 v[24:27], a[180:183], v[84:87], v[24:27]\n\t"
        "ds_read_b128 v[112:115], v129 offset:32768\n\t"
        "v_mfma_f32_16x16x32_bf16 v[28:31], a[184:187], v[84:87], v[28:31]\n\t"
        "s_waitcnt vmcnt(6)\n\t"
        "s_barrier\n\t"
        "v_mfma_f32_16x16x32_bf16 v[32:35], a[188:191], v[84:87], v[32:35]\n\t"
        "buffer_load_dwordx4 v140, s[24:27], s36 offen offset:0 lds\n\t"
        "v_mfma_f32_16x16x32_bf16 v[0:3], a[192:195], v[88:91], v[0:3]\n\t"
        "ds_read_b128 v[84:87], v128 offset:41024\n\t"
        "v_mfma_f32_16x16x32_bf16 v[4:7], a[196:199], v[88:91], v[4:7]\n\t"
        "ds_read_b64_tr_b16 a[204:205], v137 offset:24576\n\t"
        "s_waitcnt lgkmcnt(4)\n\t"
        "v_or3_b32 v164, v164, v165, v166\n\t"
        "v_mfma_f32_16x16x32_bf16 v[8:11], a[200:203], v[88:91], v[8:11]\n\t"
        "ds_read_b64_tr_b16 a[206:207], v137 offset:25344\n\t"
        "v_or_b32 v164, v164, v167\n\t"
        "v_mfma_f32_16x16x32_bf16 v[12:15], a[192:195], v[92:95], v[12:15]\n\t"
        "ds_read_b128 v[88:91], v130 offset:32896\n\t"
        "v_readfirstlane_b32 s8, v164\n\t"
        "v_mfma_f32_16x16x32_bf16 v[16:19], a[196:199], v[92:95], v[16:19]\n\t"
        "buffer_load_dwordx4 v141, s[24:27], s36 offen offset:1024 lds\n\t"
        "v_mfma_f32_16x16x32_bf16 v[20:23], a[200:203], v[92:95], v[20:23]\n\t"
        "ds_read_b64_tr_b16 a[208:209], v138 offset:24576\n\t"
        "v_mfma_f32_16x16x32_bf16 v[24:27], a[192:195], v[96:99], v[24:27]\n\t"
        "ds_read_b128 v[92:95], v128 offset:41088\n\t"
        "v_mfma_f32_16x16x32_bf16 v[28:31], a[196:199], v[96:99], v[28:31]\n\t"
        "ds_read_b64_tr_b16 a[210:211], v138 offset:25344\n\t"
        "v_mfma_f32_16x16x32_bf16 v[32:35], a[200:203], v[96:99], v[32:35]\n\t"
        "buffer_load_dwordx4 v142, s[24:27], s36 offen offset:2048 lds\n\t"
        "v_mfma_f32_16x16x32_bf16 v[36:39], a[156:159], v[100:103], v[36:39]\n\t"
        "ds_read_b128 v[96:99], v129 offset:32768\n\t"
        "v_mfma_f32_16x16x32_bf16 v[40:43], a[160:163], v[100:103], v[40:43]\n\t"
        "ds_read_b64_tr_b16 a[212:213], v139 offset:24576\n\t"
        "v_mfma_f32_16x16x32_bf16 v[44:47], a[164:167], v[100:103], v[44:47]\n\t"
        "ds_read_b64_tr_b16 a[214:215], v139 offset:25344\n\t"
        "v_mfma_f32_16x16x32_bf16 v[36:39], a[168:171], v[104:107], v[36:39]\n\t"
        "ds_read_b128 v[100:103], v129 offset:64\n\t"
        "v_mfma_f32_16x16x32_bf16 v[40:43], a[172:175], v[104:107], v[40:43]\n\t"
        "ds_read_b64_tr_b16 a[216:217], v137 offset:30720\n\t"
        "v_mfma_f32_16x16x32_bf16 v[44:47], a[176:179], v[104:107], v[44:47]\n\t"
        "ds_read_b64_tr_b16 a[218:219], v137 offset:31488\n\t"
        "s_waitcnt lgkmcnt(9)\n\t"
        "v_mfma_f32_16x16x32_bf16 v[36:39], a[180:183], v[108:111], v[36:39]\n\t"
        "ds_read_b128 v[104:107], v129 offset:8256\n\t"
        "v_mfma_f32_16x16x32_bf16 v[40:43], a[184:187], v[108:111], v[40:43]\n\t"
        "ds_read_b64_tr_b16 a[220:221], v138 offset:30720\n\t"
        "v_mfma_f32_16x16x32_bf16 v[44:47], a[188:191], v[108:111], v[44:47]\n\t"
        "ds_read_b64_tr_b16 a[222:223], v138 offset:31488\n\t"
        "v_mfma_f32_16x16x32_bf16 v[36:39], a[192:195], v[112:115], v[36:39]\n\t"
        "ds_read_b128 v[108:111], v129 offset:16448\n\t"
        "v_mfma_f32_16x16x32_bf16 v[40:43], a[196:199], v[112:115], v[40:43]\n\t"
        "ds_read_b64_tr_b16 a[224:225], v139 offset:30720\n\t"
        "v_mfma_f32_16x16x32_bf16 v[44:47], a[200:203], v[112:115], v[44:47]\n\t"
        "ds_read_b64_tr_b16 a[226:227], v139 offset:31488\n\t"
        "v_mfma_f32_16x16x32_bf16 v[48:51], a[144:147], v[84:87], v[48:51]\n\t"
        "ds_read_b128 v[112:115], v129 offset:8256\n\t"
        "v_mfma_f32_16x16x32_bf16 v[52:55], a[148:151], v[84:87], v[52:55]\n\t"
        "ds_read_b64_tr_b16 a[228:229], v137 offset:36864\n\t"
        "v_mfma_f32_16x16x32_bf16 v[56:59], a[152:155], v[84:87], v[56:59]\n\t"
        "ds_read_b64_tr_b16 a[230:231], v137 offset:37632\n\t"
        "v_mfma_f32_16x16x32_bf16 v[48:51], a[156:159], v[88:91], v[48:51]\n\t"
        "ds_read_b128 v[84:87], v129 offset:16448\n\t"
        "v_mfma_f32_16x16x32_bf16 v[52:55], a[160:163], v[88:91], v[52:55]\n\t"
        "s_waitcnt vmcnt(3)\n\t"
        "s_barrier\n\t"
        "v_mfma_f32_16x16x32_bf16 v[56:59], a[164:167], v[88:91], v[56:59]\n\t"
        "ds_read_b64_tr_b16 a[232:233], v138 offset:36864\n\t"
        "s_waitcnt lgkmcnt(9)\n\t"
        "v_mfma_f32_16x16x32_bf16 v[48:51], a[168:171], v[92:95], v[48:51]\n\t"
        "ds_read_b128 v[88:91], v129 offset:24640\n\t"
        "v_mfma_f32_16x16x32_bf16 v[52:55], a[172:175], v[92:95], v[52:55]\n\t"
        "ds_read_b64_tr_b16 a[234:235], v138 offset:37632\n\t"
        "v_mfma_f32_16x16x32_bf16 v[56:59], a[176:179], v[92:95], v[56:59]\n\t"
        "ds_read_b64_tr_b16 a[236:237], v139 offset:36864\n\t"
        "v_mfma_f32_16x16x32_bf16 v[48:51], a[180:183], v[96:99], v[48:51]\n\t"
        "ds_read_b128 v[92:95], v129 offset:24640\n\t"
        "v_mfma_f32_16x16x32_bf16 v[52:55], a[184:187], v[96:99], v[52:55]\n\t"
        "ds_read_b64_tr_b16 a[238:239], v139 offset:37632\n\t"
        "v_mfma_f32_16x16x32_bf16 v[56:59], a[188:191], v[96:99], v[56:59]\n\t"
        "ds_read_b64_tr_b16 a[240:241], v137 offset:43008\n\t"
        "v_mfma_f32_16x16x32_bf16 v[0:3], a[204:207], v[100:103], v[0:3]\n\t"
        "ds_read_b128 v[96:99], v129 offset:128\n\t"
        "v_mfma_f32_16x16x32_bf16 v[4:7], a[208:211], v[100:103], v[4:7]\n\t"
        "ds_read_b64_tr_b16 a[242:243], v137 offset:43776\n\t"
        "v_mfma_f32_16x16x32_bf16 v[8:11], a[212:215], v[100:103], v[8:11]\n\t"
        "ds_read_b64_tr_b16 a[244:245], v138 offset:43008\n\t"
        "v_mfma_f32_16x16x32_bf16 v[12:15], a[204:207], v[104:107], v[12:15]\n\t"
        "ds_read_b128 v[100:103], v129 offset:8320\n\t"
        "v_mfma_f32_16x16x32_bf16 v[16:19], a[208:211], v[104:107], v[16:19]\n\t"
        "ds_read_b64_tr_b16 a[246:247], v138 offset:43776\n\t"
        "v_mfma_f32_16x16x32_bf16 v[20:23], a[212:215], v[104:107], v[20:23]\n\t"
        "ds_read_b64_tr_b16 a[248:249], v139 offset:43008\n\t"
        "s_waitcnt lgkmcnt(10)\n\t"
        "v_mfma_f32_16x16x32_bf16 v[24:27], a[204:207], v[108:111], v[24:27]\n\t"
        "ds_read_b128 v[104:107], v129 offset:16512\n\t"
        "v_mfma_f32_16x16x32_bf16 v[28:31], a[208:211], v[108:111], v[28:31]\n\t"
        "ds_read_b64_tr_b16 a[250:251], v139 offset:43776\n\t"
        "v_mfma_f32_16x16x32_bf16 v[32:35], a[212:215], v[108:111], v[32:35]\n\t"
        "ds_read_b64_tr_b16 a[252:253], v137 offset:49152\n\t"
        "v_mfma_f32_16x16x32_bf16 v[0:3], a[216:219], v[112:115], v[0:3]\n\t"
        "ds_read_b128 v[108:111], v129 offset:8320\n\t"
        "v_mfma_f32_16x16x32_bf16 v[4:7], a[220:223], v[112:115], v[4:7]\n\t"
        "ds_read_b64_tr_b16 a[254:255], v137 offset:49920\n\t"
        "v_mfma_f32_16x16x32_bf16 v[8:11], a[224:227], v[112:115], v[8:11]\n\t"
        "ds_read_b64_tr_b16 v[188:189], v138 offset:49152\n\t"
        "v_mfma_f32_16x16x32_bf16 v[12:15], a[216:219], v[84:87], v[12:15]\n\t"
        "ds_read_b128 v[112:115], v129 offset:16512\n\t"
        "v_mfma_f32_16x16x32_bf16 v[16:19], a[220:223], v[84:87], v[16:19]\n\t"
        "ds_read_b64_tr_b16 v[190:191], v138 offset:49920\n\t"
        "v_mfma_f32_16x16x32_bf16 v[20:23], a[224:227], v[84:87], v[20:23]\n\t"
        "ds_read_b64_tr_b16 v[192:193], v139 offset:49152\n\t"
        "v_mfma_f32_16x16x32_bf16 v[24:27], a[216:219], v[88:91], v[24:27]\n\t"
        "ds_read_b128 v[84:87], v129 offset:24704\n\t"
        "v_mfma_f32_16x16x32_bf16 v[28:31], a[220:223], v[88:91], v[28:31]\n\t"
        "ds_read_b64_tr_b16 v[194:195], v139 offset:49920\n\t"
        "v_mfma_f32_16x16x32_bf16 v[32:35], a[224:227], v[88:91], v[32:35]\n\t"
        "ds_read_b64_tr_b16 v[196:197], v137 offset:55296\n\t"
        "s_waitcnt lgkmcnt(10)\n\t"
        "v_mfma_f32_16x16x32_bf16 v[36:39], a[204:207], v[92:95], v[36:39]\n\t"
        "ds_read_b128 v[88:91], v128 offset:0\n\t"
        "v_mfma_f32_16x16x32_bf16 v[40:43], a[208:211], v[92:95], v[40:43]\n\t"
        "ds_read_b64_tr_b16 v[198:199], v137 offset:56064\n\t"
        "v_mfma_f32_16x16x32_bf16 v[44:47], a[212:215], v[92:95], v[44:47]\n\t"
        "ds_read_b64_tr_b16 v[200:201], v138 offset:55296\n\t"
        "v_mfma_f32_16x16x32_bf16 v[0:3], a[228:231], v[96:99], v[0:3]\n\t"
        "ds_read_b128 v[92:95], v128 offset:8192\n\t"
        "v_mfma_f32_16x16x32_bf16 v[4:7], a[232:235], v[96:99], v[4:7]\n\t"
        "ds_read_b64_tr_b16 v[202:203], v138 offset:56064\n\t"
        "v_mfma_f32_16x16x32_bf16 v[8:11], a[236:239], v[96:99], v[8:11]\n\t"
        "ds_read_b64_tr_b16 v[204:205], v139 offset:55296\n\t"
        "v_mfma_f32_16x16x32_bf16 v[12:15], a[228:231], v[100:103], v[12:15]\n\t"
        "ds_read_b128 v[96:99], v128 offset:16384\n\t"
        "v_mfma_f32_16x16x32_bf16 v[16:19], a[232:235], v[100:103], v[16:19]\n\t"
        "ds_read_b64_tr_b16 v[206:207], v139 offset:56064\n\t"
        "v_mfma_f32_16x16x32_bf16 v[20:23], a[236:239], v[100:103], v[20:23]\n\t"
        "ds_read_b64_tr_b16 v[208:209], v181 offset:0\n\t"
        "v_mfma_f32_16x16x32_bf16 v[24:27], a[228:231], v[104:107], v[24:27]\n\t"
        "ds_read_b128 v[100:103], v130 offset:8192\n\t"
        "v_mfma_f32_16x16x32_bf16 v[28:31], a[232:235], v[104:107], v[28:31]\n\t"
        "ds_read_b64_tr_b16 v[210:211], v181 offset:768\n\t"
        "v_mfma_f32_16x16x32_bf16 v[32:35], a[236:239], v[104:107], v[32:35]\n\t"
        "ds_read_b64_tr_b16 v[212:213], v182 offset:0\n\t"
        "s_waitcnt lgkmcnt(10)\n\t"
        "v_mfma_f32_16x16x32_bf16 v[0:3], a[240:243], v[108:111], v[0:3]\n\t"
        "ds_read_b128 v[104:107], v130 offset:16384\n\t"
        "v_mfma_f32_16x16x32_bf16 v[4:7], a[244:247], v[108:111], v[4:7]\n\t"
        "ds_read_b64_tr_b16 v[214:215], v182 offset:768\n\t"
        "v_mfma_f32_16x16x32_bf16 v[8:11], a[248:251], v[108:111], v[8:11]\n\t"
        "ds_read_b64_tr_b16 v[216:217], v183 offset:0\n\t"
        "v_mfma_f32_16x16x32_bf16 v[12:15], a[240:243], v[112:115], v[12:15]\n\t"
        "ds_read_b128 v[108:111], v130 offset:24576\n\t"
        "v_mfma_f32_16x16x32_bf16 v[16:19], a[244:247], v[112:115], v[16:19]\n\t"
        "ds_read_b64_tr_b16 v[218:219], v183 offset:768\n\t"
        "v_mfma_f32_16x16x32_bf16 v[20:23], a[248:251], v[112:115], v[20:23]\n\t"
        "ds_read_b64_tr_b16 v[220:221], v181 offset:6144\n\t"
        "v_mfma_f32_16x16x32_bf16 v[24:27], a[240:243], v[84:87], v[24:27]\n\t"
        "ds_read_b128 v[112:115], v128 offset:64\n\t"
        "v_mfma_f32_16x16x32_bf16 v[28:31], a[244:247], v[84:87], v[28:31]\n\t"
        "ds_read_b64_tr_b16 v[222:223], v181 offset:6912\n\t"
        "v_mfma_f32_16x16x32_bf16 v[32:35], a[248:251], v[84:87], v[32:35]\n\t"
        "ds_read_b64_tr_b16 v[224:225], v182 offset:6144\n\t"
        "v_mfma_f32_16x16x32_bf16 v[0:3], a[252:255], v[88:91], v[0:3]\n\t"
        "ds_read_b128 v[84:87], v128 offset:8256\n\t"
        "v_mfma_f32_16x16x32_bf16 v[4:7], v[188:191], v[88:91], v[4:7]\n\t"
        "ds_read_b64_tr_b16 v[226:227], v182 offset:6912\n\t"
        "v_mfma_f32_16x16x32_bf16 v[8:11], v[192:195], v[88:91], v[8:11]\n\t"
        "ds_read_b64_tr_b16 v[228:229], v183 offset:6144\n\t"
        "s_waitcnt lgkmcnt(10)\n\t"
        "v_mfma_f32_16x16x32_bf16 v[12:15], a[252:255], v[92:95], v[12:15]\n\t"
        "ds_read_b128 v[88:91], v128 offset:16448\n\t"
        "v_mfma_f32_16x16x32_bf16 v[16:19], v[188:191], v[92:95], v[16:19]\n\t"
        "ds_read_b64_tr_b16 v[230:231], v183 offset:6912\n\t"
        "v_mfma_f32_16x16x32_bf16 v[20:23], v[192:195], v[92:95], v[20:23]\n\t"
        "s_cmp_lg_u32 s8, 0\n\t"
        "s_cbranch_scc1 99f\n\t"
        "v_mfma_f32_16x16x32_bf16 v[24:27], a[252:255], v[96:99], v[24:27]\n\t"
        "ds_read_b128 v[92:95], v130 offset:8256\n\t"
        "v_mfma_f32_16x16x32_bf16 v[28:31], v[188:191], v[96:99], v[28:31]\n\t"
        "v_mfma_f32_16x16x32_bf16 v[32:35], v[192:195], v[96:99], v[32:35]\n\t"
        "v_mfma_f32_16x16x32_bf16 v[0:3], v[196:199], v[100:103], v[0:3]\n\t"
        "ds_read_b128 v[96:99], v130 offset:16448\n\t"
        "v_mfma_f32_16x16x32_bf16 v[4:7], v[200:203], v[100:103], v[4:7]\n\t"
        "v_mfma_f32_16x16x32_bf16 v[8:11], v[204:207], v[100:103], v[8:11]\n\t"
        "v_mfma_f32_16x16x32_bf16 v[12:15], v[196:199], v[104:107], v[12:15]\n\t"
        "ds_read_b128 v[100:103], v130 offset:24640\n\t"
        "v_mfma_f32_16x16x32_bf16 v[16:19], v[200:203], v[104:107], v[16:19]\n\t"
        "s_waitcnt vmcnt(0) lgkmcnt(3)\n\t"
        "s_barrier\n\t"
        "v_mfma_f32_16x16x32_bf16 v[20:23], v[204:207], v[104:107], v[20:23]\n\t"
        "ds_read_b64_tr_b16 v[232:233], v181 offset:12288\n\t"
        "v_mfma_f32_16x16x32_bf16 v[24:27], v[196:199], v[108:111], v[24:27]\n\t"
        "ds_read_b128 v[104:107], v129 offset:32832\n\t"
        "v_mfma_f32_16x16x32_bf16 v[28:31], v[200:203], v[108:111], v[28:31]\n\t"
        "ds_read_b64_tr_b16 v[234:235], v181 offset:13056\n\t"
        "v_mfma_f32_16x16x32_bf16 v[32:35], v[204:207], v[108:111], v[32:35]\n\t"
        "ds_read_b64_tr_b16 v[236:237], v182 offset:12288\n\t"
        "v_mfma_f32_16x16x32_bf16 v[0:3], v[208:211], v[112:115], v[0:3]\n\t"
        "ds_read_b128 v[108:111], v129 offset:24704\n\t"
        "v_mfma_f32_16x16x32_bf16 v[4:7], v[212:215], v[112:115], v[4:7]\n\t"
        "ds_read_b64_tr_b16 v[238:239], v182 offset:13056\n\t"
        "v_mfma_f32_16x16x32_bf16 v[8:11], v[216:219], v[112:115], v[8:11]\n\t"
        "ds_read_b64_tr_b16 v[240:241], v183 offset:12288\n\t"
        "v_mfma_f32_16x16x32_bf16 v[12:15], v[208:211], v[84:87], v[12:15]\n\t"
        "ds_read_b128 v[112:115], v129 offset:32896\n\t"
        "v_mfma_f32_16x16x32_bf16 v[16:19], v[212:215], v[84:87], v[16:19]\n\t"
        "ds_read_b64_tr_b16 v[242:243], v183 offset:13056\n\t"
        "v_mfma_f32_16x16x32_bf16 v[20:23], v[216:219], v[84:87], v[20:23]\n\t"
        "ds_read_b64_tr_b16 v[244:245], v181 offset:18432\n\t"
        "v_mfma_f32_16x16x32_bf16 v[24:27], v[208:211], v[88:91], v[24:27]\n\t"
        "ds_read_b128 v[84:87], v128 offset:128\n\t"
        "v_mfma_f32_16x16x32_bf16 v[28:31], v[212:215], v[88:91], v[28:31]\n\t"
        "ds_read_b64_tr_b16 v[246:247], v181 offset:19200\n\t"
        "v_mfma_f32_16x16x32_bf16 v[32:35], v[216:219], v[88:91], v[32:35]\n\t"
        "ds_read_b64_tr_b16 v[248:249], v182 offset:18432\n\t"
        "s_waitcnt lgkmcnt(10)\n\t"
        "v_mfma_f32_16x16x32_bf16 v[0:3], v[220:223], v[92:95], v[0:3]\n\t"
        "ds_read_b128 v[88:91], v128 offset:8320\n\t"
        "v_mfma_f32_16x16x32_bf16 v[4:7], v[224:227], v[92:95], v[4:7]\n\t"
        "ds_read_b64_tr_b16 v[250:251], v182 offset:19200\n\t"
        "v_mfma_f32_16x16x32_bf16 v[8:11], v[228:231], v[92:95], v[8:11]\n\t"
        "ds_read_b64_tr_b16 v[252:253], v183 offset:18432\n\t"
        "v_mfma_f32_16x16x32_bf16 v[12:15], v[220:223], v[96:99], v[12:15]\n\t"
        "ds_read_b128 v[92:95], v128 offset:16512\n\t"
        "v_mfma_f32_16x16x32_bf16 v[16:19], v[224:227], v[96:99], v[16:19]\n\t"
        "ds_read_b64_tr_b16 v[254:255], v183 offset:19200\n\t"
        "v_mfma_f32_16x16x32_bf16 v[20:23], v[228:231], v[96:99], v[20:23]\n\t"
        "v_mfma_f32_16x16x32_bf16 v[24:27], v[220:223], v[100:103], v[24:27]\n\t"
        "ds_read_b128 v[96:99], v130 offset:8320\n\t"
        "v_mfma_f32_16x16x32_bf16 v[28:31], v[224:227], v[100:103], v[28:31]\n\t"
        "v_mfma_f32_16x16x32_bf16 v[32:35], v[228:231], v[100:103], v[32:35]\n\t"
        "v_mfma_f32_16x16x32_bf16 v[36:39], a[216:219], v[104:107], v[36:39]\n\t"
        "ds_read_b128 v[100:103], v130 offset:16512\n\t"
        "v_mfma_f32_16x16x32_bf16 v[40:43], a[220:223], v[104:107], v[40:43]\n\t"
        "v_mfma_f32_16x16x32_bf16 v[44:47], a[224:227], v[104:107], v[44:47]\n\t"
        "s_waitcnt lgkmcnt(5)\n\t"
        "v_mfma_f32_16x16x32_bf16 v[36:39], a[228:231], v[108:111], v[36:39]\n\t"
        "ds_read_b128 v[104:107], v130 offset:24704\n\t"
        "v_mfma_f32_16x16x32_bf16 v[40:43], a[232:235], v[108:111], v[40:43]\n\t"
        "v_mfma_f32_16x16x32_bf16 v[44:47], a[236:239], v[108:111], v[44:47]\n\t"
        "v_mfma_f32_16x16x32_bf16 v[36:39], a[240:243], v[112:115], v[36:39]\n\t"
        "ds_read_b128 v[108:111], v128 offset:24576\n\t"
        "v_mfma_f32_16x16x32_bf16 v[40:43], a[244:247], v[112:115], v[40:43]\n\t"
        "v_mfma_f32_16x16x32_bf16 v[44:47], a[248:251], v[112:115], v[44:47]\n\t"
        "v_mfma_f32_16x16x32_bf16 v[0:3], v[232:235], v[84:87], v[0:3]\n\t"
        "ds_read_b128 v[112:115], v130 offset:32768\n\t"
        "v_mfma_f32_16x16x32_bf16 v[4:7], v[236:239], v[84:87], v[4:7]\n\t"
        "v_mfma_f32_16x16x32_bf16 v[8:11], v[240:243], v[84:87], v[8:11]\n\t"
        "v_mfma_f32_16x16x32_bf16 v[12:15], v[232:235], v[88:91], v[12:15]\n\t"
        "ds_read_b128 v[84:87], v128 offset:24640\n\t"
        "v_mfma_f32_16x16x32_bf16 v[16:19], v[236:239], v[88:91], v[16:19]\n\t"
        "v_mfma_f32_16x16x32_bf16 v[20:23], v[240:243], v[88:91], v[20:23]\n\t"
        "s_waitcnt lgkmcnt(3)\n\t"
        "v_mfma_f32_16x16x32_bf16 v[24:27], v[232:235], v[92:95], v[24:27]\n\t"
        "ds_read_b128 v[88:91], v130 offset:32832\n\t"
        "v_mfma_f32_16x16x32_bf16 v[28:31], v[236:239], v[92:95], v[28:31]\n\t"
        "v_mfma_f32_16x16x32_bf16 v[32:35], v[240:243], v[92:95], v[32:35]\n\t"
        "v_mfma_f32_16x16x32_bf16 v[0:3], v[244:247], v[96:99], v[0:3]\n\t"
        "ds_read_b128 v[92:95], v128 offset:24704\n\t"
        "v_mfma_f32_16x16x32_bf16 v[4:7], v[248:251], v[96:99], v[4:7]\n\t"
        "v_mfma_f32_16x16x32_bf16 v[8:11], v[252:255], v[96:99], v[8:11]\n\t"
        "v_mfma_f32_16x16x32_bf16 v[12:15], v[244:247], v[100:103], v[12:15]\n\t"
        "ds_read_b128 v[96:99], v130 offset:32896\n\t"
        "v_mfma_f32_16x16x32_bf16 v[16:19], v[248:251], v[100:103], v[16:19]\n\t"
        "v_mfma_f32_16x16x32_bf16 v[20:23], v[252:255], v[100:103], v[20:23]\n\t"
        "v_mfma_f32_16x16x32_bf16 v[24:27], v[244:247], v[104:107], v[24:27]\n\t"
        "ds_read_b128 v[100:103], v129 offset:40960\n\t"
        "v_cvt_pk_bf16_f32 v116, v0, v1\n\t"
        "v_mfma_f32_16x16x32_bf16 v[28:31], v[248:251], v[104:107], v[28:31]\n\t"
        "v_cvt_pk_bf16_f32 v117, v2, v3\n\t"
        "v_cvt_pk_bf16_f32 v118, v4, v5\n\t"
        "v_mfma_f32_16x16x32_bf16 v[32:35], v[252:255], v[104:107], v[32:35]\n\t"
        "v_cvt_pk_bf16_f32 v119, v6, v7\n\t"
        "v_cvt_pk_bf16_f32 v120, v8, v9\n\t"
        "s_waitcnt lgkmcnt(3)\n\t"
        "v_mfma_f32_16x16x32_bf16 v[36:39], a[252:255], v[108:111], v[36:39]\n\t"
        "ds_read_b128 v[104:107], v129 offset:32832\n\t"
        "v_cvt_pk_bf16_f32 v121, v10, v11\n\t"
        "v_mfma_f32_16x16x32_bf16 v[40:43], v[188:191], v[108:111], v[40:43]\n\t"
        "v_permlane16_swap_b32 v116, v118\n\t"
        "v_mfma_f32_16x16x32_bf16 v[44:47], v[192:195], v[108:111], v[44:47]\n\t"
        "v_permlane16_swap_b32 v117, v119\n\t"
        "v_mfma_f32_16x16x32_bf16 v[36:39], v[196:199], v[112:115], v[36:39]\n\t"
        "ds_read_b128 v[108:111], v129 offset:41024\n\t"
        "s_add_u32 s55, s41, 0\n\t"
        "v_mfma_f32_16x16x32_bf16 v[40:43], v[200:203], v[112:115], v[40:43]\n\t"
        "buffer_store_dwordx4 v[116:119], v144, s[28:31], s55 offen sc1\n\t"
        "v_mfma_f32_16x16x32_bf16 v[44:47], v[204:207], v[112:115], v[44:47]\n\t"
        "ds_write_b64 v147, v[120:121] offset:0\n\t"
        "v_mfma_f32_16x16x32_bf16 v[36:39], v[208:211], v[84:87], v[36:39]\n\t"
        "ds_read_b128 v[112:115], v129 offset:32896\n\t"
        "v_cvt_pk_bf16_f32 v116, v12, v13\n\t"
        "v_mfma_f32_16x16x32_bf16 v[40:43], v[212:215], v[84:87], v[40:43]\n\t"
        "v_cvt_pk_bf16_f32 v117, v14, v15\n\t"
        "v_cvt_pk_bf16_f32 v118, v16, v17\n\t"
        "v_mfma_f32_16x16x32_bf16 v[44:47], v[216:219], v[84:87], v[44:47]\n\t"
        "v_cvt_pk_bf16_f32 v119, v18, v19\n\t"
        "v_cvt_pk_bf16_f32 v120, v20, v21\n\t"
        "v_mfma_f32_16x16x32_bf16 v[36:39], v[220:223], v[88:91], v[36:39]\n\t"
        "ds_read_b128 v[84:87], v129 offset:41088\n\t"
        "v_cvt_pk_bf16_f32 v121, v22, v23\n\t"
        "v_mfma_f32_16x16x32_bf16 v[40:43], v[224:227], v[88:91], v[40:43]\n\t"
        "v_permlane16_swap_b32 v116, v118\n\t"
        "v_mfma_f32_16x16x32_bf16 v[44:47], v[228:231], v[88:91], v[44:47]\n\t"
        "v_permlane16_swap_b32 v117, v119\n\t"
        "s_waitcnt lgkmcnt(4)\n\t"
        "v_mfma_f32_16x16x32_bf16 v[36:39], v[232:235], v[92:95], v[36:39]\n\t"
        "ds_read_b128 v[88:91], v128 offset:32768\n\t"
        "s_add_u32 s55, s41, 86016\n\t"
        "v_mfma_f32_16x16x32_bf16 v[40:43], v[236:239], v[92:95], v[40:43]\n\t"
        "v_mfma_f32_16x16x32_bf16 v[44:47], v[240:243], v[92:95], v[44:47]\n\t"
        "v_mfma_f32_16x16x32_bf16 v[36:39], v[244:247], v[96:99], v[36:39]\n\t"
        "ds_read_b128 v[92:95], v130 offset:40960\n\t"
        "v_mfma_f32_16x16x32_bf16 v[40:43], v[248:251], v[96:99], v[40:43]\n\t"
        "v_mfma_f32_16x16x32_bf16 v[44:47], v[252:255], v[96:99], v[44:47]\n\t"
        "v_mfma_f32_16x16x32_bf16 v[48:51], a[192:195], v[100:103], v[48:51]\n\t"
        "ds_read_b128 v[96:99], v128 offset:32832\n\t"
        "v_mfma_f32_16x16x32_bf16 v[52:55], a[196:199], v[100:103], v[52:55]\n\t"
        "buffer_store_dwordx4 v[116:119], v144, s[28:31], s55 offen sc1\n\t"
        "v_mfma_f32_16x16x32_bf16 v[56:59], a[200:203], v[100:103], v[56:59]\n\t"
        "ds_write_b64 v147, v[120:121] offset:2048\n\t"
        "v_mfma_f32_16x16x32_bf16 v[48:51], a[204:207], v[104:107], v[48:51]\n\t"
        "ds_read_b128 v[100:103], v130 offset:41024\n\t"
        "v_cvt_pk_bf16_f32 v116, v24, v25\n\t"
        "v_mfma_f32_16x16x32_bf16 v[52:55], a[208:211], v[104:107], v[52:55]\n\t"
        "v_cvt_pk_bf16_f32 v117, v26, v27\n\t"
        "v_cvt_pk_bf16_f32 v118, v28, v29\n\t"
        "v_mfma_f32_16x16x32_bf16 v[56:59], a[212:215], v[104:107], v[56:59]\n\t"
        "v_cvt_pk_bf16_f32 v119, v30, v31\n\t"
        "v_cvt_pk_bf16_f32 v120, v32, v33\n\t"
        "s_waitcnt lgkmcnt(4)\n\t"
        "v_mfma_f32_16x16x32_bf16 v[48:51], a[216:219], v[108:111], v[48:51]\n\t"
        "ds_read_b128 v[104:107], v128 offset:32896\n\t"
        "v_cvt_pk_bf16_f32 v121, v34, v35\n\t"
        "v_mfma_f32_16x16x32_bf16 v[52:55], a[220:223], v[108:111], v[52:55]\n\t"
        "v_permlane16_swap_b32 v116, v118\n\t"
        "v_mfma_f32_16x16x32_bf16 v[56:59], a[224:227], v[108:111], v[56:59]\n\t"
        "v_permlane16_swap_b32 v117, v119\n\t"
        "v_mfma_f32_16x16x32_bf16 v[48:51], a[228:231], v[112:115], v[48:51]\n\t"
        "ds_read_b128 v[108:111], v130 offset:41088\n\t"
        "s_add_u32 s55, s41, 172032\n\t"
        "v_mfma_f32_16x16x32_bf16 v[52:55], a[232:235], v[112:115], v[52:55]\n\t"
        "v_mfma_f32_16x16x32_bf16 v[56:59], a[236:239], v[112:115], v[56:59]\n\t"
        "v_mfma_f32_16x16x32_bf16 v[48:51], a[240:243], v[84:87], v[48:51]\n\t"
        "ds_read_b128 v[112:115], v132 offset:45056\n\t"
        "v_mfma_f32_16x16x32_bf16 v[52:55], a[244:247], v[84:87], v[52:55]\n\t"
        "v_mfma_f32_16x16x32_bf16 v[56:59], a[248:251], v[84:87], v[56:59]\n\t"
        "v_mfma_f32_16x16x32_bf16 v[48:51], a[252:255], v[88:91], v[48:51]\n\t"
        "ds_read_b128 v[84:87], v132 offset:45184\n\t"
        "v_mfma_f32_16x16x32_bf16 v[52:55], v[188:191], v[88:91], v[52:55]\n\t"
        "buffer_store_dwordx4 v[116:119], v144, s[28:31], s55 offen sc1\n\t"
        "v_mfma_f32_16x16x32_bf16 v[56:59], v[192:195], v[88:91], v[56:59]\n\t"
        "ds_write_b64 v147, v[120:121] offset:4096\n\t"
        "s_waitcnt lgkmcnt(4)\n\t"
        "v_mfma_f32_16x16x32_bf16 v[48:51], v[196:199], v[92:95], v[48:51]\n\t"
        "ds_read_b128 v[88:91], v132 offset:45120\n\t"
        "v_mfma_f32_16x16x32_bf16 v[52:55], v[200:203], v[92:95], v[52:55]\n\t"
        "v_mfma_f32_16x16x32_bf16 v[56:59], v[204:207], v[92:95], v[56:59]\n\t"
        "v_mfma_f32_16x16x32_bf16 v[48:51], v[208:211], v[96:99], v[48:51]\n\t"
        "ds_read_b128 v[92:95], v131 offset:45056\n\t"
        "v_mfma_f32_16x16x32_bf16 v[52:55], v[212:215], v[96:99], v[52:55]\n\t"
        "s_waitcnt lgkmcnt(2)\n\t"
        "s_barrier\n\t"
        "v_mfma_f32_16x16x32_bf16 v[56:59], v[216:219], v[96:99], v[56:59]\n\t"
        "ds_read_b128 v[124:127], v148 offset:0\n\t"
        "v_mfma_f32_16x16x32_bf16 v[48:51], v[220:223], v[100:103], v[48:51]\n\t"
        "ds_read_b128 v[96:99], v133 offset:45056\n\t"
        "v_mfma_f32_16x16x32_bf16 v[52:55], v[224:227], v[100:103], v[52:55]\n\t"
        "v_mfma_f32_16x16x32_bf16 v[56:59], v[228:231], v[100:103], v[56:59]\n\t"
        "v_mfma_f32_16x16x32_bf16 v[48:51], v[232:235], v[104:107], v[48:51]\n\t"
        "ds_read_b128 v[100:103], v131 offset:45120\n\t"
        "v_mfma_f32_16x16x32_bf16 v[52:55], v[236:239], v[104:107], v[52:55]\n\t"
        "v_mfma_f32_16x16x32_bf16 v[56:59], v[240:243], v[104:107], v[56:59]\n\t"
        "v_mfma_f32_16x16x32_bf16 v[48:51], v[244:247], v[108:111], v[48:51]\n\t"
        "ds_read_b128 v[104:107], v133 offset:45120\n\t"
        "v_mfma_f32_16x16x32_bf16 v[52:55], v[248:251], v[108:111], v[52:55]\n\t"
        "s_waitcnt lgkmcnt(3)\n\t"
        "buffer_store_dwordx4 v[124:127], v145, s[28:31], s53 offen sc1\n\t"
        "v_mfma_f32_16x16x32_bf16 v[56:59], v[252:255], v[108:111], v[56:59]\n\t"
        "ds_read_b128 v[124:127], v148 offset:2048\n\t"
        "v_mfma_f32_16x16x32_bf16 v[60:63], a[0:3], v[112:115], 0\n\t"
        "ds_read_b128 v[108:111], v131 offset:45184\n\t"
        "v_mfma_f32_16x16x32_bf16 v[64:67], a[4:7], v[112:115], 0\n\t"
        "v_mfma_f32_16x16x32_bf16 v[68:71], a[8:11], v[112:115], 0\n\t"
        "v_mfma_f32_16x16x32_bf16 v[60:63], a[12:15], v[84:87], v[60:63]\n\t"
        "ds_read_b128 v[112:115], v133 offset:45184\n\t"
        "v_mfma_f32_16x16x32_bf16 v[64:67], a[16:19], v[84:87], v[64:67]\n\t"
        "v_mfma_f32_16x16x32_bf16 v[68:71], a[20:23], v[84:87], v[68:71]\n\t"
        "v_mfma_f32_16x16x32_bf16 v[60:63], a[24:27], v[88:91], v[60:63]\n\t"
        "ds_read_b128 v[84:87], v133 offset:40960\n\t"
        "v_mfma_f32_16x16x32_bf16 v[64:67], a[28:31], v[88:91], v[64:67]\n\t"
        "s_add_u32 s55, s50, 86016\n\t"
        "v_mfma_f32_16x16x32_bf16 v[68:71], a[32:35], v[88:91], v[68:71]\n\t"
        "v_mfma_f32_16x16x32_bf16 v[60:63], a[36:39], v[92:95], v[60:63]\n\t"
        "ds_read_b128 v[88:91], v131 offset:49152\n\t"
        "v_mfma_f32_16x16x32_bf16 v[64:67], a[40:43], v[92:95], v[64:67]\n\t"
        "v_mfma_f32_16x16x32_bf16 v[68:71], a[44:47], v[92:95], v[68:71]\n\t"
        "s_waitcnt lgkmcnt(3)\n\t"
        "v_mfma_f32_16x16x32_bf16 v[60:63], a[48:51], v[96:99], v[60:63]\n\t"
        "ds_read_b128 v[92:95], v133 offset:41024\n\t"
        "v_mfma_f32_16x16x32_bf16 v[64:67], a[52:55], v[96:99], v[64:67]\n\t"
        "buffer_store_dwordx4 v[124:127], v145, s[28:31], s55 offen sc1\n\t"
        "v_mfma_f32_16x16x32_bf16 v[68:71], a[56:59], v[96:99], v[68:71]\n\t"
        "v_cvt_pk_bf16_f32 v116, v36, v37\n\t"
        "v_cvt_pk_bf16_f32 v117, v38, v39\n\t"
        "v_mfma_f32_16x16x32_bf16 v[60:63], a[60:63], v[100:103], v[60:63]\n\t"
        "ds_read_b128 v[96:99], v131 offset:49216\n\t"
        "v_cvt_pk_bf16_f32 v118, v40, v41\n\t"
        "v_mfma_f32_16x16x32_bf16 v[64:67], a[64:67], v[100:103], v[64:67]\n\t"
        "v_cvt_pk_bf16_f32 v119, v42, v43\n\t"
        "v_cvt_pk_bf16_f32 v120, v44, v45\n\t"
        "v_mfma_f32_16x16x32_bf16 v[68:71], a[68:71], v[100:103], v[68:71]\n\t"
        "v_cvt_pk_bf16_f32 v121, v46, v47\n\t"
        "v_mfma_f32_16x16x32_bf16 v[60:63], a[72:75], v[104:107], v[60:63]\n\t"
        "ds_read_b128 v[100:103], v133 offset:41088\n\t"
        "v_mfma_f32_16x16x32_bf16 v[64:67], a[76:79], v[104:107], v[64:67]\n\t"
        "v_permlane16_swap_b32 v116, v118\n\t"
        "v_mfma_f32_16x16x32_bf16 v[68:71], a[80:83], v[104:107], v[68:71]\n\t"
        "v_permlane16_swap_b32 v117, v119\n\t"
        "v_mfma_f32_16x16x32_bf16 v[60:63], a[84:87], v[108:111], v[60:63]\n\t"
        "ds_read_b128 v[104:107], v131 offset:49280\n\t"
        "s_add_u32 s55, s41, 258048\n\t"
        "v_mfma_f32_16x16x32_bf16 v[64:67], a[88:91], v[108:111], v[64:67]\n\t"
        "v_mfma_f32_16x16x32_bf16 v[68:71], a[92:95], v[108:111], v[68:71]\n\t"
        "s_waitcnt lgkmcnt(3)\n\t"
        "v_mfma_f32_16x16x32_bf16 v[60:63], a[96:99], v[112:115], v[60:63]\n\t"
        "ds_read_b128 v[108:111], v132 offset:40960\n\t"
        "v_mfma_f32_16x16x32_bf16 v[64:67], a[100:103], v[112:115], v[64:67]\n\t"
        "v_mfma_f32_16x16x32_bf16 v[68:71], a[104:107], v[112:115], v[68:71]\n\t"
        "v_mfma_f32_16x16x32_bf16 v[60:63], a[108:111], v[84:87], v[60:63]\n\t"
        "ds_read_b128 v[112:115], v132 offset:49152\n\t"
        "v_mfma_f32_16x16x32_bf16 v[64:67], a[112:115], v[84:87], v[64:67]\n\t"
        "buffer_store_dwordx4 v[116:119], v144, s[28:31], s55 offen sc1\n\t"
        "v_mfma_f32_16x16x32_bf16 v[68:71], a[116:119], v[84:87], v[68:71]\n\t"
        "ds_write_b64 v147, v[120:121] offset:6144\n\t"
        "v_mfma_f32_16x16x32_bf16 v[60:63], a[120:123], v[88:91], v[60:63]\n\t"
        "ds_read_b128 v[84:87], v132 offset:41024\n\t"
        "v_cvt_pk_bf16_f32 v116, v48, v49\n\t"
        "v_mfma_f32_16x16x32_bf16 v[64:67], a[124:127], v[88:91], v[64:67]\n\t"
        "v_cvt_pk_bf16_f32 v117, v50, v51\n\t"
        "v_cvt_pk_bf16_f32 v118, v52, v53\n\t"
        "v_mfma_f32_16x16x32_bf16 v[68:71], a[128:131], v[88:91], v[68:71]\n\t"
        "v_cvt_pk_bf16_f32 v119, v54, v55\n\t"
        "v_cvt_pk_bf16_f32 v120, v56, v57\n\t"
        "v_mfma_f32_16x16x32_bf16 v[60:63], a[132:135], v[92:95], v[60:63]\n\t"
        "ds_read_b128 v[88:91], v132 offset:49216\n\t"
        "v_cvt_pk_bf16_f32 v121, v58, v59\n\t"
        "v_mfma_f32_16x16x32_bf16 v[64:67], a[136:139], v[92:95], v[64:67]\n\t"
        "v_permlane16_swap_b32 v116, v118\n\t"
        "v_mfma_f32_16x16x32_bf16 v[68:71], a[140:143], v[92:95], v[68:71]\n\t"
        "v_permlane16_swap_b32 v117, v119\n\t"
        "s_waitcnt lgkmcnt(4)\n\t"
        "v_mfma_f32_16x16x32_bf16 v[60:63], a[144:147], v[96:99], v[60:63]\n\t"
        "ds_read_b128 v[92:95], v132 offset:41088\n\t"
        "s_add_u32 s55, s41, 344064\n\t"
        "v_mfma_f32_16x16x32_bf16 v[64:67], a[148:151], v[96:99], v[64:67]\n\t"
        "v_mfma_f32_16x16x32_bf16 v[68:71], a[152:155], v[96:99], v[68:71]\n\t"
        "v_mfma_f32_16x16x32_bf16 v[60:63], a[156:159], v[100:103], v[60:63]\n\t"
        "ds_read_b128 v[96:99], v132 offset:49280\n\t"
        "v_mfma_f32_16x16x32_bf16 v[64:67], a[160:163], v[100:103], v[64:67]\n\t"
        "v_mfma_f32_16x16x32_bf16 v[68:71], a[164:167], v[100:103], v[68:71]\n\t"
        "v_mfma_f32_16x16x32_bf16 v[60:63], a[168:171], v[104:107], v[60:63]\n\t"
        "ds_read_b128 v[100:103], v131 offset:40960\n\t"
        "v_mfma_f32_16x16x32_bf16 v[64:67], a[172:175], v[104:107], v[64:67]\n\t"
        "buffer_store_dwordx4 v[116:119], v144, s[28:31], s55 offen sc1\n\t"
        "v_mfma_f32_16x16x32_bf16 v[68:71], a[176:179], v[104:107], v[68:71]\n\t"
        "ds_write_b64 v147, v[120:121] offset:8192\n\t"
        "v_mfma_f32_16x16x32_bf16 v[60:63], a[180:183], v[108:111], v[60:63]\n\t"
        "ds_read_b128 v[104:107], v133 offset:49152\n\t"
        "v_mfma_f32_16x16x32_bf16 v[64:67], a[184:187], v[108:111], v[64:67]\n\t"
        "v_mfma_f32_16x16x32_bf16 v[68:71], a[188:191], v[108:111], v[68:71]\n\t"
        "s_waitcnt lgkmcnt(4)\n\t"
        "v_mfma_f32_16x16x32_bf16 v[60:63], a[192:195], v[112:115], v[60:63]\n\t"
        "ds_read_b128 v[108:111], v131 offset:41024\n\t"
        "v_mfma_f32_16x16x32_bf16 v[64:67], a[196:199], v[112:115], v[64:67]\n\t"
        "v_mfma_f32_16x16x32_bf16 v[68:71], a[200:203], v[112:115], v[68:71]\n\t"
        "v_mfma_f32_16x16x32_bf16 v[60:63], a[204:207], v[84:87], v[60:63]\n\t"
        "ds_read_b128 v[112:115], v133 offset:49216\n\t"
        "v_mfma_f32_16x16x32_bf16 v[64:67], a[208:211], v[84:87], v[64:67]\n\t"
        "v_mfma_f32_16x16x32_bf16 v[68:71], a[212:215], v[84:87], v[68:71]\n\t"
        "v_mfma_f32_16x16x32_bf16 v[60:63], a[216:219], v[88:91], v[60:63]\n\t"
        "ds_read_b128 v[84:87], v131 offset:41088\n\t"
        "v_mfma_f32_16x16x32_bf16 v[64:67], a[220:223], v[88:91], v[64:67]\n\t"
        "v_mfma_f32_16x16x32_bf16 v[68:71], a[224:227], v[88:91], v[68:71]\n\t"
        "v_mfma_f32_16x16x32_bf16 v[60:63], a[228:231], v[92:95], v[60:63]\n\t"
        "ds_read_b128 v[88:91], v133 offset:49280\n\t"
        "v_mfma_f32_16x16x32_bf16 v[64:67], a[232:235], v[92:95], v[64:67]\n\t"
        "v_mfma_f32_16x16x32_bf16 v[68:71], a[236:239], v[92:95], v[68:71]\n\t"
        "s_waitcnt lgkmcnt(3)\n\t"
        "v_mfma_f32_16x16x32_bf16 v[60:63], a[240:243], v[96:99], v[60:63]\n\t"
        "ds_read_b128 v[92:95], v135 offset:53248\n\t"
        "v_mfma_f32_16x16x32_bf16 v[64:67], a[244:247], v[96:99], v[64:67]\n\t"
        "v_mfma_f32_16x16x32_bf16 v[68:71], a[248:251], v[96:99], v[68:71]\n\t"
        "v_mfma_f32_16x16x32_bf16 v[60:63], a[252:255], v[100:103], v[60:63]\n\t"
        "ds_read_b128 v[96:99], v135 offset:53376\n\t"
        "v_mfma_f32_16x16x32_bf16 v[64:67], v[188:191], v[100:103], v[64:67]\n\t"
        "v_mfma_f32_16x16x32_bf16 v[68:71], v[192:195], v[100:103], v[68:71]\n\t"
        "v_mfma_f32_16x16x32_bf16 v[60:63], v[196:199], v[104:107], v[60:63]\n\t"
        "ds_read_b128 v[100:103], v135 offset:53312\n\t"
        "v_mfma_f32_16x16x32_bf16 v[64:67], v[200:203], v[104:107], v[64:67]\n\t"
        "v_mfma_f32_16x16x32_bf16 v[68:71], v[204:207], v[104:107], v[68:71]\n\t"
        "v_mfma_f32_16x16x32_bf16 v[60:63], v[208:211], v[108:111], v[60:63]\n\t"
        "ds_read_b128 v[104:107], v134 offset:53248\n\t"
        "v_mfma_f32_16x16x32_bf16 v[64:67], v[212:215], v[108:111], v[64:67]\n\t"
        "v_mfma_f32_16x16x32_bf16 v[68:71], v[216:219], v[108:111], v[68:71]\n\t"
        "s_waitcnt lgkmcnt(3)\n\t"
        "v_mfma_f32_16x16x32_bf16 v[60:63], v[220:223], v[112:115], v[60:63]\n\t"
        "ds_read_b128 v[108:111], v136 offset:53248\n\t"
        "v_mfma_f32_16x16x32_bf16 v[64:67], v[224:227], v[112:115], v[64:67]\n\t"
        "v_mfma_f32_16x16x32_bf16 v[68:71], v[228:231], v[112:115], v[68:71]\n\t"
        "v_mfma_f32_16x16x32_bf16 v[60:63], v[232:235], v[84:87], v[60:63]\n\t"
        "ds_read_b128 v[112:115], v134 offset:53312\n\t"
        "v_mfma_f32_16x16x32_bf16 v[64:67], v[236:239], v[84:87], v[64:67]\n\t"
        "v_mfma_f32_16x16x32_bf16 v[68:71], v[240:243], v[84:87], v[68:71]\n\t"
        "v_mfma_f32_16x16x32_bf16 v[60:63], v[244:247], v[88:91], v[60:63]\n\t"
        "ds_read_b128 v[84:87], v136 offset:53312\n\t"
        "v_mfma_f32_16x16x32_bf16 v[64:67], v[248:251], v[88:91], v[64:67]\n\t"
        "v_mfma_f32_16x16x32_bf16 v[68:71], v[252:255], v[88:91], v[68:71]\n\t"
        "v_mfma_f32_16x16x32_bf16 v[72:75], a[0:3], v[92:95], 0\n\t"
        "ds_read_b128 v[88:91], v134 offset:53376\n\t"
        "v_mfma_f32_16x16x32_bf16 v[76:79], a[4:7], v[92:95], 0\n\t"
        "v_mfma_f32_16x16x32_bf16 v[80:83], a[8:11], v[92:95], 0\n\t"
        "s_waitcnt lgkmcnt(3)\n\t"
        "v_mfma_f32_16x16x32_bf16 v[72:75], a[12:15], v[96:99], v[72:75]\n\t"
        "ds_read_b128 v[92:95], v136 offset:53376\n\t"
        "v_cvt_pk_bf16_f32 v116, v60, v61\n\t"
        "v_mfma_f32_16x16x32_bf16 v[76:79], a[16:19], v[96:99], v[76:79]\n\t"
        "v_cvt_pk_bf16_f32 v117, v62, v63\n\t"
        "v_cvt_pk_bf16_f32 v118, v64, v65\n\t"
        "v_mfma_f32_16x16x32_bf16 v[80:83], a[20:23], v[96:99], v[80:83]\n\t"
        "v_cvt_pk_bf16_f32 v119, v66, v67\n\t"
        "v_cvt_pk_bf16_f32 v120, v68, v69\n\t"
        "v_mfma_f32_16x16x32_bf16 v[72:75], a[24:27], v[100:103], v[72:75]\n\t"
        "ds_read_b128 v[96:99], v136 offset:49152\n\t"
        "v_cvt_pk_bf16_f32 v121, v70, v71\n\t"
        "v_mfma_f32_16x16x32_bf16 v[76:79], a[28:31], v[100:103], v[76:79]\n\t"
        "v_permlane16_swap_b32 v116, v118\n\t"
        "v_mfma_f32_16x16x32_bf16 v[80:83], a[32:35], v[100:103], v[80:83]\n\t"
        "v_permlane16_swap_b32 v117, v119\n\t"
        "v_mfma_f32_16x16x32_bf16 v[72:75], a[36:39], v[104:107], v[72:75]\n\t"
        "ds_read_b128 v[100:103], v134 offset:57344\n\t"
        "v_mfma_f32_16x16x32_bf16 v[76:79], a[40:43], v[104:107], v[76:79]\n\t"
        "buffer_store_dwordx4 v[116:119], v144, s[28:31], s51 offen sc1\n\t"
        "v_mfma_f32_16x16x32_bf16 v[80:83], a[44:47], v[104:107], v[80:83]\n\t"
        "ds_write_b64 v149, v[120:121]\n\t"
        "v_mfma_f32_16x16x32_bf16 v[72:75], a[48:51], v[108:111], v[72:75]\n\t"
        "ds_read_b128 v[104:107], v136 offset:49216\n\t"
        "v_mfma_f32_16x16x32_bf16 v[76:79], a[52:55], v[108:111], v[76:79]\n\t"
        "v_mfma_f32_16x16x32_bf16 v[80:83], a[56:59], v[108:111], v[80:83]\n\t"
        "s_waitcnt lgkmcnt(4)\n\t"
        "v_mfma_f32_16x16x32_bf16 v[72:75], a[60:63], v[112:115], v[72:75]\n\t"
        "ds_read_b128 v[108:111], v134 offset:57408\n\t"
        "v_mfma_f32_16x16x32_bf16 v[76:79], a[64:67], v[112:115], v[76:79]\n\t"
        "s_waitcnt lgkmcnt(2)\n\t"
        "s_barrier\n\t"
        "v_mfma_f32_16x16x32_bf16 v[80:83], a[68:71], v[112:115], v[80:83]\n\t"
        "ds_read_b128 v[124:127], v148 offset:6144\n\t"
        "v_mfma_f32_16x16x32_bf16 v[72:75], a[72:75], v[84:87], v[72:75]\n\t"
        "ds_read_b128 v[112:115], v136 offset:49280\n\t"
        "v_mfma_f32_16x16x32_bf16 v[76:79], a[76:79], v[84:87], v[76:79]\n\t"
        "v_mfma_f32_16x16x32_bf16 v[80:83], a[80:83], v[84:87], v[80:83]\n\t"
        "v_mfma_f32_16x16x32_bf16 v[72:75], a[84:87], v[88:91], v[72:75]\n\t"
        "ds_read_b128 v[84:87], v134 offset:57472\n\t"
        "v_mfma_f32_16x16x32_bf16 v[76:79], a[88:91], v[88:91], v[76:79]\n\t"
        "v_mfma_f32_16x16x32_bf16 v[80:83], a[92:95], v[88:91], v[80:83]\n\t"
        "v_mfma_f32_16x16x32_bf16 v[72:75], a[96:99], v[92:95], v[72:75]\n\t"
        "ds_read_b128 v[88:91], v135 offset:49152\n\t"
        "v_mfma_f32_16x16x32_bf16 v[76:79], a[100:103], v[92:95], v[76:79]\n\t"
        "s_add_u32 s55, s50, 258048\n\t"
        "v_mfma_f32_16x16x32_bf16 v[80:83], a[104:107], v[92:95], v[80:83]\n\t"
        "s_waitcnt lgkmcnt(3)\n\t"
        "buffer_store_dwordx4 v[124:127], v145, s[28:31], s55 offen sc1\n\t"
        "v_mfma_f32_16x16x32_bf16 v[72:75], a[108:111], v[96:99], v[72:75]\n\t"
        "ds_read_b128 v[92:95], v135 offset:57344\n\t"
        "v_mfma_f32_16x16x32_bf16 v[76:79], a[112:115], v[96:99], v[76:79]\n\t"
        "ds_read_b64 v[122:123], v150\n\t"
        "v_mfma_f32_16x16x32_bf16 v[80:83], a[116:119], v[96:99], v[80:83]\n\t"
        "v_mfma_f32_16x16x32_bf16 v[72:75], a[120:123], v[100:103], v[72:75]\n\t"
        "ds_read_b128 v[96:99], v135 offset:49216\n\t"
        "v_mfma_f32_16x16x32_bf16 v[76:79], a[124:127], v[100:103], v[76:79]\n\t"
        "v_mfma_f32_16x16x32_bf16 v[80:83], a[128:131], v[100:103], v[80:83]\n\t"
        "v_mfma_f32_16x16x32_bf16 v[72:75], a[132:135], v[104:107], v[72:75]\n\t"
        "ds_read_b128 v[100:103], v135 offset:57408\n\t"
        "v_mfma_f32_16x16x32_bf16 v[76:79], a[136:139], v[104:107], v[76:79]\n\t"
        "v_mfma_f32_16x16x32_bf16 v[80:83], a[140:143], v[104:107], v[80:83]\n\t"
        "v_mfma_f32_16x16x32_bf16 v[72:75], a[144:147], v[108:111], v[72:75]\n\t"
        "ds_read_b128 v[104:107], v135 offset:49280\n\t"
        "v_mfma_f32_16x16x32_bf16 v[76:79], a[148:151], v[108:111], v[76:79]\n\t"
        "v_mfma_f32_16x16x32_bf16 v[80:83], a[152:155], v[108:111], v[80:83]\n\t"
        "s_waitcnt lgkmcnt(3)\n\t"
        "v_mfma_f32_16x16x32_bf16 v[72:75], a[156:159], v[112:115], v[72:75]\n\t"
        "ds_read_b128 v[108:111], v135 offset:57472\n\t"
        "v_mfma_f32_16x16x32_bf16 v[76:79], a[160:163], v[112:115], v[76:79]\n\t"
        "v_mfma_f32_16x16x32_bf16 v[80:83], a[164:167], v[112:115], v[80:83]\n\t"
        "v_mfma_f32_16x16x32_bf16 v[72:75], a[168:171], v[84:87], v[72:75]\n\t"
        "ds_read_b128 v[112:115], v134 offset:49152\n\t"
        "v_mfma_f32_16x16x32_bf16 v[76:79], a[172:175], v[84:87], v[76:79]\n\t"
        "v_mfma_f32_16x16x32_bf16 v[80:83], a[176:179], v[84:87], v[80:83]\n\t"
        "v_mfma_f32_16x16x32_bf16 v[72:75], a[180:183], v[88:91], v[72:75]\n\t"
        "ds_read_b128 v[84:87], v136 offset:57344\n\t"
        "v_mfma_f32_16x16x32_bf16 v[76:79], a[184:187], v[88:91], v[76:79]\n\t"
        "v_mfma_f32_16x16x32_bf16 v[80:83], a[188:191], v[88:91], v[80:83]\n\t"
        "v_mfma_f32_16x16x32_bf16 v[72:75], a[192:195], v[92:95], v[72:75]\n\t"
        "ds_read_b128 v[88:91], v134 offset:49216\n\t"
        "v_mfma_f32_16x16x32_bf16 v[76:79], a[196:199], v[92:95], v[76:79]\n\t"
        "v_mfma_f32_16x16x32_bf16 v[80:83], a[200:203], v[92:95], v[80:83]\n\t"
        "s_waitcnt lgkmcnt(3)\n\t"
        "v_mfma_f32_16x16x32_bf16 v[72:75], a[204:207], v[96:99], v[72:75]\n\t"
        "ds_read_b128 v[92:95], v136 offset:57408\n\t"
        "v_mfma_f32_16x16x32_bf16 v[76:79], a[208:211], v[96:99], v[76:79]\n\t"
        "v_mfma_f32_16x16x32_bf16 v[80:83], a[212:215], v[96:99], v[80:83]\n\t"
        "v_mfma_f32_16x16x32_bf16 v[72:75], a[216:219], v[100:103], v[72:75]\n\t"
        "ds_read_b128 v[96:99], v134 offset:49280\n\t"
        "v_mfma_f32_16x16x32_bf16 v[76:79], a[220:223], v[100:103], v[76:79]\n\t"
        "v_mfma_f32_16x16x32_bf16 v[80:83], a[224:227], v[100:103], v[80:83]\n\t"
        "v_mfma_f32_16x16x32_bf16 v[72:75], a[228:231], v[104:107], v[72:75]\n\t"
        "ds_read_b128 v[100:103], v136 offset:57472\n\t"
        "v_mfma_f32_16x16x32_bf16 v[76:79], a[232:235], v[104:107], v[76:79]\n\t"
        "v_mfma_f32_16x16x32_bf16 v[80:83], a[236:239], v[104:107], v[80:83]\n\t"
        "v_mfma_f32_16x16x32_bf16 v[72:75], a[240:243], v[108:111], v[72:75]\n\t"
        "v_mfma_f32_16x16x32_bf16 v[76:79], a[244:247], v[108:111], v[76:79]\n\t"
        "v_mfma_f32_16x16x32_bf16 v[80:83], a[248:251], v[108:111], v[80:83]\n\t"
        "s_waitcnt lgkmcnt(2)\n\t"
        "v_mfma_f32_16x16x32_bf16 v[72:75], a[252:255], v[112:115], v[72:75]\n\t"
        "v_mfma_f32_16x16x32_bf16 v[76:79], v[188:191], v[112:115], v[76:79]\n\t"
        "v_mfma_f32_16x16x32_bf16 v[80:83], v[192:195], v[112:115], v[80:83]\n\t"
        "v_mfma_f32_16x16x32_bf16 v[72:75], v[196:199], v[84:87], v[72:75]\n\t"
        "v_mfma_f32_16x16x32_bf16 v[76:79], v[200:203], v[84:87], v[76:79]\n\t"
        "v_mfma_f32_16x16x32_bf16 v[80:83], v[204:207], v[84:87], v[80:83]\n\t"
        "v_mfma_f32_16x16x32_bf16 v[72:75], v[208:211], v[88:91], v[72:75]\n\t"
        "v_mfma_f32_16x16x32_bf16 v[76:79], v[212:215], v[88:91], v[76:79]\n\t"
        "v_mfma_f32_16x16x32_bf16 v[80:83], v[216:219], v[88:91], v[80:83]\n\t"
        "v_mfma_f32_16x16x32_bf16 v[72:75], v[220:223], v[92:95], v[72:75]\n\t"
        "v_mfma_f32_16x16x32_bf16 v[76:79], v[224:227], v[92:95], v[76:79]\n\t"
        "v_mfma_f32_16x16x32_bf16 v[80:83], v[228:231], v[92:95], v[80:83]\n\t"
        "s_waitcnt lgkmcnt(0)\n\t"
        "v_mfma_f32_16x16x32_bf16 v[72:75], v[232:235], v[96:99], v[72:75]\n\t"
        "v_mfma_f32_16x16x32_bf16 v[76:79], v[236:239], v[96:99], v[76:79]\n\t"
        "v_mfma_f32_16x16x32_bf16 v[80:83], v[240:243], v[96:99], v[80:83]\n\t"
        "v_mfma_f32_16x16x32_bf16 v[72:75], v[244:247], v[100:103], v[72:75]\n\t"
        "v_mfma_f32_16x16x32_bf16 v[76:79], v[248:251], v[100:103], v[76:79]\n\t"
        "v_mfma_f32_16x16x32_bf16 v[80:83], v[252:255], v[100:103], v[80:83]\n\t"
        "s_nop 5\n\t"
        "v_cvt_pk_bf16_f32 v116, v72, v73\n\t"
        "v_cvt_pk_bf16_f32 v117, v74, v75\n\t"
        "v_cvt_pk_bf16_f32 v118, v76, v77\n\t"
        "v_cvt_pk_bf16_f32 v119, v78, v79\n\t"
        "v_cvt_pk_bf16_f32 v120, v80, v81\n\t"
        "v_cvt_pk_bf16_f32 v121, v82, v83\n\t"
        "v_permlane16_swap_b32 v116, v118\n\t"
        "v_permlane16_swap_b32 v117, v119\n\t"
        "buffer_store_dwordx4 v[116:119], v144, s[28:31], s52 offen sc1\n\t"
        "v_permlane16_swap_b32 v120, v122\n\t"
        "v_permlane16_swap_b32 v121, v123\n\t"
        "buffer_store_dwordx4 v[120:123], v146, s[28:31], s52 offen sc1\n\t"
        "s_endpgm\n\t"
        "99:\n\t"
        "s_nop 7\n\t"
        "s_waitcnt vmcnt(0) lgkmcnt(0)\n\t"
        "s_barrier\n\t"
        "v_mov_b32 %0, v156\n\t"
        : "+v"(tid) : "s"(ka), "s"(bid), "s"(recs)
        : "memory", "scc", "vcc", "s3", "s4", "s5", "s6", "s7", "s8", "s9", "s10", "s11", "s12", "s13", "s16", "s17", "s18", "s19", "s20", "s21", "s22", "s23", "s24", "s25", "s26", "s27", "s28", "s29", "s30", "s31", "s34", "s35", "s36", "s37", "s38", "s39", "s40", "s41", "s42", "s43", "s44", "s45", "s46", "s47", "s48", "s49", "s50", "s51", "s52", "s53", "s54", "s55", "s56", "s57", "s60", "s61", "s62", "s63", "s64", "s65", "s66", "s67", "s68", "s69", "s70", "s71", "s72", "s73", "s74", "s75", "s76", "s77", "s78", "s79", "s80", "s81", "s82", "s83", "s84", "s85", "s86", "s87", "s88", "s89", "s90", "s91", "s92", "s93", "s94", "s95", "s96", "s97", "s98", "s99", "v1", "v2", "v3", "v4", "v5", "v6", "v7", "v8", "v9", "v10", "v11", "v12", "v13", "v14", "v15", "v16", "v17", "v18", "v19", "v20", "v21", "v22", "v23", "v24", "v25", "v26", "v27", "v28", "v29", "v30", "v31", "v32", "v33", "v34", "v35", "v36", "v37", "v38", "v39", "v40", "v41", "v42", "v43", "v44", "v45", "v46", "v47", "v48", "v49", "v50", "v51", "v52", "v53", "v54", "v55", "v56", "v57", "v58", "v59", "v60", "v61", "v62", "v63", "v64", "v65", "v66", "v67", "v68", "v69", "v70", "v71", "v72", "v73", "v74", "v75", "v76", "v77", "v78", "v79", "v80", "v81", "v82", "v83", "v84", "v85", "v86", "v87", "v88", "v89", "v90", "v91", "v92", "v93", "v94", "v95", "v96", "v97", "v98", "v99", "v100", "v101", "v102", "v103", "v104", "v105", "v106", "v107", "v108", "v109", "v110", "v111", "v112", "v113", "v114", "v115", "v116", "v117", "v118", "v119", "v120", "v121", "v122", "v123", "v124", "v125", "v126", "v127", "v128", "v129", "v130", "v131", "v132", "v133", "v134", "v135", "v136", "v137", "v138", "v139", "v140", "v141", "v142", "v143", "v144", "v145", "v146", "v147", "v148", "v149", "v150", "v151", "v152", "v153", "v154", "v155", "v156", "v157", "v158", "v159", "v160", "v161", "v162", "v163", "v164", "v165", "v166", "v167", "v168", "v169", "v170", "v171", "v172", "v173", "v174", "v175", "v176", "v177", "v178", "v179", "v180", "v181", "v182", "v183", "v184", "v185", "v186", "v187", "v188", "v189", "v190", "v191", "v192", "v193", "v194", "v195", "v196", "v197", "v198", "v199", "v200", "v201", "v202", "v203", "v204", "v205", "v206", "v207", "v208", "v209", "v210", "v211", "v212", "v213", "v214", "v215", "v216", "v217", "v218", "v219", "v220", "v221", "v222", "v223", "v224", "v225", "v226", "v227", "v228", "v229", "v230", "v231", "v232", "v233", "v234", "v235", "v236", "v237", "v238", "v239", "v240", "v241", "v242", "v243", "v244", "v245", "v246", "v247", "v248", "v249", "v250", "v251", "v252", "v253", "v254", "v255", "a0", "a1", "a2", "a3", "a4", "a5", "a6", "a7", "a8", "a9", "a10", "a11", "a12", "a13", "a14", "a15", "a16", "a17", "a18", "a19", "a20", "a21", "a22", "a23", "a24", "a25", "a26", "a27", "a28", "a29", "a30", "a31", "a32", "a33", "a34", "a35", "a36", "a37", "a38", "a39", "a40", "a41", "a42", "a43", "a44", "a45", "a46", "a47", "a48", "a49", "a50", "a51", "a52", "a53", "a54", "a55", "a56", "a57", "a58", "a59", "a60", "a61", "a62", "a63", "a64", "a65", "a66", "a67", "a68", "a69", "a70", "a71", "a72", "a73", "a74", "a75", "a76", "a77", "a78", "a79", "a80", "a81", "a82", "a83", "a84", "a85", "a86", "a87", "a88", "a89", "a90", "a91", "a92", "a93", "a94", "a95", "a96", "a97", "a98", "a99", "a100", "a101", "a102", "a103", "a104", "a105", "a106", "a107", "a108", "a109", "a110", "a111", "a112", "a113", "a114", "a115", "a116", "a117", "a118", "a119", "a120", "a121", "a122", "a123", "a124", "a125", "a126", "a127", "a128", "a129", "a130", "a131", "a132", "a133", "a134", "a135", "a136", "a137", "a138", "a139", "a140", "a141", "a142", "a143", "a144", "a145", "a146", "a147", "a148", "a149", "a150", "a151", "a152", "a153", "a154", "a155", "a156", "a157", "a158", "a159", "a160", "a161", "a162", "a163", "a164", "a165", "a166", "a167", "a168", "a169", "a170", "a171", "a172", "a173", "a174", "a175", "a176", "a177", "a178", "a179", "a180", "a181", "a182", "a183", "a184", "a185", "a186", "a187", "a188", "a189", "a190", "a191", "a192", "a193", "a194", "a195", "a196", "a197", "a198", "a199", "a200", "a201", "a202", "a203", "a204", "a205", "a206", "a207", "a208", "a209", "a210", "a211", "a212", "a213", "a214", "a215", "a216", "a217", "a218", "a219", "a220", "a221", "a222", "a223", "a224", "a225", "a226", "a227", "a228", "a229", "a230", "a231", "a232", "a233", "a234", "a235", "a236", "a237", "a238", "a239", "a240", "a241", "a242", "a243", "a244", "a245", "a246", "a247", "a248", "a249", "a250", "a251", "a252", "a253", "a254", "a255");
    asm volatile("" : "+s"(ka), "+s"(bid) :: "memory");
    const unsigned short *x = ka->x, *off = ka->off, *w = ka->w;
    unsigned short* out = ka->out;
    const unsigned tw = ka->t.w[bid];
    const int r0 = tw & 255, c0 = (tw >> 8) & 255, kind = (tw >> 16) & 3, nu = (tw >> 18) & 3, ne = (tw >> 20) & 3, tb = ((tw >> 22) & 3) * 16, ta = tw >> 24;
    const int nmain = kind == 0 ? 14 : 6 + 2 * ne, nseg = nmain + 2 * nu;
    const int lane = tid & 63, li = lane & 15, q = lane >> 4;
    for (int s = tid >> 6; s < nseg; s += 4) {
        const int j = s - nmain;
        int h = r0 + s, c = c0 + li;
        if (j >= 0) {
            if (kind == 1) { h = ta + (j & 1); c = 16 * (j >> 1) + li; }
            else if (kind == 2) { c = ta + (j & 1); h = 16 * (j >> 1) + li; }
            else { h = ta + j; c = tb + li; }
        }
        const int p = h * 224 + c;
        f32x4 acc[6];
        for (int n = 0; n < 6; ++n) acc[n] = (f32x4){0.f, 0.f, 0.f, 0.f};
#pragma unroll 1
        for (int t = 0; t < 9; ++t) {
            const unsigned od = ((const unsigned*)off)[p * 9 + t];
            float py = rb((float)(h + t / 3 - 1) + bf_lo(od)), px = rb((float)(c + t % 3 - 1) + bf_hi(od));
            py = fminf(fmaxf(py, 0.f), 223.f); px = fminf(fmaxf(px, 0.f), 223.f);
            const float y0 = floorf(py), x0 = floorf(px), y1 = fminf(y0 + 1.f, 223.f), x1 = fminf(x0 + 1.f, 223.f);
            const float wy = py - y0, wx = px - x0, a = rb(1.f - wy), b = rb(1.f - wx);
            const float wt[4] = {rb(a * b), rb(a * wx), rb(wy * b), rb(wy * wx)};
            const int ad[4] = {((int)y0 * 224 + (int)x0) * 96, ((int)y0 * 224 + (int)x1) * 96, ((int)y1 * 224 + (int)x0) * 96, ((int)y1 * 224 + (int)x1) * 96};
#pragma unroll 1
            for (int ks = 0; ks < 3; ++ks) {
                float r[8] = {0, 0, 0, 0, 0, 0, 0, 0};
                for (int cn = 0; cn < 4; ++cn) {
                    const u32x4 g = *(const u32x4*)(x + ad[cn] + ks * 32 + q * 8);
                    for (int d = 0; d < 4; ++d) { r[2 * d] += wt[cn] * bf_lo(g[d]); r[2 * d + 1] += wt[cn] * bf_hi(g[d]); }
                }
                u32x4 bfr;
                for (int d = 0; d < 4; ++d) bfr[d] = cvt_pk(r[2 * d], r[2 * d + 1]);
                for (int n = 0; n < 6; ++n) {
                    const unsigned short* wp = w + (t * 96 + ks * 32 + q * 8) * 96 + n * 16 + li;
                    u32x4 av;
                    for (int d = 0; d < 4; ++d) av[d] = wp[(2 * d) * 96] | ((unsigned)wp[(2 * d + 1) * 96] << 16);
                    acc[n] = __builtin_amdgcn_mfma_f32_16x16x32_bf16(__builtin_bit_cast(bf16x8, av), __builtin_bit_cast(bf16x8, bfr), acc[n], 0, 0, 0);
                }
            }
        }
        for (int n = 0; n < 6; ++n) {
            u32x2 o; o[0] = cvt_pk(acc[n][0], acc[n][1]); o[1] = cvt_pk(acc[n][2], acc[n][3]);
            *(u32x2*)(out + p * 96 + n * 16 + q * 4) = o;
        }
    }
}
extern "C" void kernel_launch(void* const* d_in, const int* in_sizes, int n_in, void* d_out, int out_size, void* d_ws, size_t ws_size, hipStream_t stream) {
    const Tiles tl = {{0x220d3030, 0x4e0d4078, 0x7c0d50b8, 0xa80d7054, 0xd40d80a4, 0x560e90da, 0x860eb05c, 0xb20ec0a6, 0x240d3036, 0x500d407e, 0x7e0d50be, 0xaa0d705a, 0xd60d80aa, 0x580ea030, 0xb062, 0xb40ec0ac, 0x303c, 0x8174084, 0x800d50c4, 0xac0d7060, 0xd80d80b0, 0x5a0ea036, 0xa7b070, 0xb60ec0b2, 0x260d304a, 0x520d408c, 0x122750ca, 0xae0d7066, 0x26780b6, 0x5c0ea03c, 0x880eb07a, 0xc0b8, 0x173050, 0x540d4092, 0x820d50d4, 0x706c, 0xda0d80c0, 0xa042, 0x8a0eb080, 0xc9bc0c6, 0x280d3058, 0x4098, 0x840d50da, 0xb00d707a, 0x80c6, 0xc5ba050, 0x8c0eb086, 0xb80ec0ce, 0x2a0d305e, 0x560d40a6, 0x860d6030, 0x1c277080, 0xdc0d80d4, 0x5e0ea058, 0x8e0eb08c, 0xba0ec0d4, 0x2c0d3064, 0x580d40ac, 0x880d6036, 0xb20d708a, 0xde0d80da, 0x600ea05e, 0xb092, 0xbc0ec0da, 0x306a, 0x5a0d40b2, 0x603c, 0xb40d7090, 0x300e9030, 0x620ea064, 0x2a7b0a0, 0xbe0ed030, 0x2e0d3078, 0xa1740b8, 0x1427604a, 0x7096, 0x320e9036, 0x640ea06a, 0x900eb0aa, 0xc00ed036, 0x300d307e, 0x5c0d40c0, 0x8a0d6054, 0xb60d70a4, 0x340e903c, 0x660ea070, 0x920eb0b0, 0xd03c, 0x2173084, 0x40c6, 0x8c0d605a, 0xb80d70aa, 0x4679042, 0x680ea076, 0x940eb0b6, 0xc20ed04a, 0x320d308c, 0x5e0d40d4, 0x8e0d6060, 0xba0d70b0, 0x360e904c, 0xa07c, 0xb0bc, 0x108fd050, 0x340d3092, 0x600d40da, 0x900d6066, 0x1e2770b6, 0x380e9052, 0x104fa08a, 0x4a7b0ca, 0xc40ed056, 0x3098, 0x620d5030, 0x606c, 0xbc0d70c0, 0x9058, 0x6a0ea090, 0x960eb0d4, 0xc60ed05c, 0x360d30a6, 0x640d5036, 0x920d607a, 0x70c6, 0x3a0e9066, 0x6c0ea096, 0x980eb0da, 0xc80ed062, 0x380d30ac, 0xc27503c, 0x16276080, 0xbe0d70d4, 0x3c0e906c, 0x6e0ea09c, 0x9a0ec030, 0xd068, 0x3a0d30b2, 0x660d5046, 0x940d608a, 0xc00d70da, 0x3e0e9072, 0x700ea0a2, 0x9c0ec036, 0xca0ed076, 0x41730b8, 0x680d504c, 0x960d6090, 0xc20d8030, 0x400e9078, 0x720ea0a8, 0x9e0ec03c, 0xcc0ed07c, 0x3c0d30c0, 0x5052, 0x6096, 0xc40d8036, 0x420e907e, 0x740ea0ae, 0xc042, 0x168fd082, 0x30c6, 0x6a0d5060, 0x980d60a4, 0x803c, 0x6679084, 0x760ea0b4, 0x6a7c050, 0xce0ed088, 0x3e0d30d4, 0x6c0d5066, 0x9a0d60aa, 0x2027804a, 0x440e908e, 0xa0ba, 0xa00ec05a, 0xd00ed08e, 0x400d30da, 0x6e0d506c, 0x9c0d60b0, 0xc60d8054, 0x460e9094, 0x164fa0c8, 0xa20ec060, 0xd20ed094, 0x420d4030, 0xe275072, 0x182760b6, 0xc80d805a, 0x480e909a, 0x780ea0ce, 0xa40ec066, 0xd09a, 0x440d4036, 0x700d507c, 0x9e0d60c0, 0xca0d8060, 0x4a0e90a0, 0x7a0ea0d4, 0xa60ec06c, 0xd40ed0a8, 0x403c, 0x720d5082, 0x60c6, 0xcc0d8066, 0x4c0e90a6, 0x7c0ea0da, 0xa80ec072, 0xd60ed0ae, 0x460d404a, 0x740d5088, 0xa00d60d4, 0x806c, 0x90ac, 0x7e0eb030, 0xaa0ec078, 0xd80ed0b4, 0x6174050, 0x760d508e, 0xa20d60da, 0xce0d807a, 0x4e0e90ba, 0x800eb036, 0xc07e, 0x1c8fd0ba, 0x480d4058, 0x10275094, 0xa40d7030, 0x678080, 0x500e90c0, 0xb03c, 0x89bc08c, 0xda0ed0c0, 0x4a0d405e, 0x780d509e, 0xa60d7036, 0xd00d808a, 0x85b90c6, 0x1c4fb04a, 0xac0ec094, 0xd0c6, 0x4c0d4064, 0x7a0d50a4, 0x703c, 0xd20d8090, 0x520e90ce, 0x820eb050, 0xae0ec09a, 0xdc0ed0d4, 0x406a, 0x50aa, 0x1a27704a, 0x8096, 0x540e90d4, 0x840eb056, 0xb00ec0a0, 0xde0ed0da}};
    dcn_kernel<<<256, 256, 0, stream>>>((const unsigned short*)d_in[0], (const unsigned short*)d_in[1], (const unsigned short*)d_in[2], (unsigned short*)d_out, d_ws, tl);
}
```

```cpp
#include <hip/hip_runtime.h>
#include <stdint.h>
typedef __attribute__((ext_vector_type(8))) __bf16 bf16x8;
typedef __attribute__((ext_vector_type(2))) __bf16 bf16x2;
typedef __attribute__((ext_vector_type(4))) float f32x4;
typedef __attribute__((ext_vector_type(4))) unsigned u32x4;
typedef __attribute__((ext_vector_type(2))) unsigned u32x2;
struct Tiles { unsigned w[256]; };
struct Args { const unsigned short *x, *off, *w; unsigned short* out; void* ws; Tiles t; };
static __device__ const unsigned recs[256 * 64 + 1536] = {
1290240,1290240,1333248,1333248,1376256,1376256,1419264,1419264,1462272,1462272,1505280,1505280,40960,44032,48128,51200,55296,58368,62464,65536,69632,72704,76800,79872,268439552,336991254,1,1024,64,0,192,1040137727,16780300,50465804,8064,36,224,1107296256,1107296256,1107296256,1098907648,1098907648,0,258624,1073741824,258048,51200,51200,48128,1379328,1073741824,1376256,16880,1107575308,4294961868,4294967294,16880,1107575308,4294961868,4294967294,16880,1107575308,4294961868,4294967294,
3397632,3397632,3440640,3440640,3483648,3483648,3526656,3526656,3526656,3526656,3526656,3526656,40960,44032,48128,51200,55296,58368,62464,65536,62464,65536,62464,65536,268439552,336991254,1,0,80,0,192,1040137855,16780300,50465804,8064,36,224,1117782016,1117782016,1117782016,1098907648,1098907648,0,645696,1073741824,645120,44032,44032,40960,3443712,1073741824,3440640,17054,1107575460,4294950892,4294967294,17054,1107575460,4294950892,4294967294,17054,1107575460,4294950892,4294967294,
3456,3456,3648,3648,3840,3840,4032,4032,4224,4224,4416,4416,40960,44032,48128,51200,55296,58368,62464,65536,69632,72704,76800,79872,268439552,336991254,1,1024,64,0,43008,1048526591,50465804,16780300,36,8064,224,1101004800,1101004800,1101004800,1098907648,1098907648,0,129744,1073741824,720,51200,51200,48128,691968,1073741824,3840,16784,1107575224,4294964556,4294967294,16784,1107575224,4294964556,4294967294,16784,1107575224,4294964556,4294967294,
13248,13248,13440,13440,13632,13632,13824,13824,13824,13824,13824,13824,40960,44032,48128,51200,55296,58368,62464,65536,62464,65536,62464,65536,268439552,336991254,1,0,80,0,43008,1048526335,50465804,16780300,36,8064,224,1116471296,1116471296,1116471296,1098907648,1098907648,0,131544,1073741824,2520,44032,44032,40960,701568,1073741824,13440,17034,1107575440,4294953132,4294967294,17034,1107575440,4294953132,4294967294,17034,1107575440,4294953132,4294967294,
1290240,1290240,1333248,1333248,1376256,1376256,1419264,1419264,1462272,1462272,1505280,1505280,40960,44032,48128,51200,55296,58368,62464,65536,69632,72704,76800,79872,1076903984,336991254,1,1024,64,0,192,1048526591,16780300,50465804,8064,36,224,1107296256,1107296256,1107296256,1115684864,1115684864,1111490560,260352,1073741824,259776,51200,51200,48128,1388544,1073741824,1385472,1110983152,1117929996,4294961580,46,1110983152,1117929996,4294961580,46,1110983152,1117929996,4294961580,46,
31296,31296,31488,31488,31680,31680,31872,31872,31872,31872,31872,31872,40960,44032,48128,51200,55296,58368,62464,65536,62464,65536,62464,65536,268439552,336991254,1,0,80,0,43008,1056915199,50465804,16780300,36,8064,224,1126432768,1126432768,1126432768,1098907648,1098907648,0,134928,1073741824,5904,44032,44032,40960,719616,1073741824,31488,17187,1107575590,4294932076,4294967294,17187,1107575590,4294932076,4294967294,17187,1107575590,4294932076,4294967294,
40512,40512,40704,40704,40896,40896,41088,41088,41088,41088,41088,41088,40960,44032,48128,51200,55296,58368,62464,65536,62464,65536,62464,65536,268439552,336991254,1,0,80,0,43008,1056915071,50465804,16780300,36,8064,224,1129578496,1129578496,1129578496,1098907648,1098907648,0,136656,1073741824,7632,44032,44032,40960,728832,1073741824,40704,17235,1107575638,4294921324,4294967294,17235,1107575638,4294921324,4294967294,17235,1107575638,4294921324,4294967294,
7182336,7182336,7225344,7225344,7268352,7268352,7311360,7311360,7311360,7311360,7311360,7311360,40960,44032,48128,51200,55296,58368,62464,65536,62464,65536,62464,65536,268439552,336991254,1,0,80,0,192,1056915199,16780300,50465804,8064,36,224,1126694912,1126694912,1126694912,1098907648,1098907648,0,1355328,1073741824,1354752,44032,44032,40960,7228416,1073741824,7225344,17191,1107575594,4294931180,4294967294,17191,1107575594,4294931180,4294967294,17191,1107575594,4294931180,4294967294,
1419264,1419264,1462272,1462272,1505280,1505280,1548288,1548288,1548288,1548288,1548288,1548288,40960,44032,48128,51200,55296,58368,62464,65536,62464,65536,62464,65536,268439552,336991254,1,0,80,0,192,1040137727,16780300,50465804,8064,36,224,1107820544,1107820544,1107820544,1098907648,1098907648,0,274752,1073741824,274176,44032,44032,40960,1465344,1073741824,1462272,16900,1107575312,4294961196,4294967294,16900,1107575312,4294961196,4294967294,16900,1107575312,4294961196,4294967294,
3483648,3483648,3526656,3526656,3569664,3569664,3612672,3612672,3612672,3612672,3612672,3612672,40960,44032,48128,51200,55296,58368,62464,65536,62464,65536,62464,65536,268439552,336991254,1,0,80,0,192,1040137855,16780300,50465804,8064,36,224,1118044160,1118044160,1118044160,1098907648,1098907648,0,661824,1073741824,661248,44032,44032,40960,3529728,1073741824,3526656,17058,1107575464,4294950444,4294967294,17058,1107575464,4294950444,4294967294,17058,1107575464,4294950444,4294967294,
3840,3840,4032,4032,4224,4224,4416,4416,4608,4608,4800,4800,40960,44032,48128,51200,55296,58368,62464,65536,69632,72704,76800,79872,268439552,336991254,1,1024,64,0,43008,1048526591,50465804,16780300,36,8064,224,1102053376,1102053376,1102053376,1098907648,1098907648,0,129816,1073741824,792,51200,51200,48128,692352,1073741824,4224,16800,1107575240,4294964108,4294967294,16800,1107575240,4294964108,4294967294,16800,1107575240,4294964108,4294967294,
13632,13632,13824,13824,14016,14016,14208,14208,14208,14208,14208,14208,40960,44032,48128,51200,55296,58368,62464,65536,62464,65536,62464,65536,268439552,336991254,1,0,80,0,43008,1048526463,50465804,16780300,36,8064,224,1116733440,1116733440,1116733440,1098907648,1098907648,0,131616,1073741824,2592,44032,44032,40960,701952,1073741824,13824,17038,1107575444,4294952684,4294967294,17038,1107575444,4294952684,4294967294,17038,1107575444,4294952684,4294967294,
1290240,1290240,1333248,1333248,1376256,1376256,1419264,1419264,1462272,1462272,1505280,1505280,40960,44032,48128,51200,55296,58368,62464,65536,69632,72704,76800,79872,1615880272,336991254,1,1024,64,0,192,1048526591,16780300,50465804,8064,36,224,1107296256,1107296256,1107296256,1119879168,1119879168,1117782016,261504,1073741824,260928,51200,51200,48128,1394688,1073741824,1391616,1117536752,1122124300,4294961388,78,1117536752,1122124300,4294961388,78,1117536752,1122124300,4294961388,78,
31680,31680,31872,31872,32064,32064,32256,32256,32256,32256,32256,32256,40960,44032,48128,51200,55296,58368,62464,65536,62464,65536,62464,65536,268439552,336991254,1,0,80,0,43008,1056915199,50465804,16780300,36,8064,224,1126563840,1126563840,1126563840,1098907648,1098907648,0,135000,1073741824,5976,44032,44032,40960,720000,1073741824,31872,17189,1107575592,4294931628,4294967294,17189,1107575592,4294931628,4294967294,17189,1107575592,4294931628,4294967294,
40896,40896,41088,41088,41280,41280,41472,41472,41472,41472,41472,41472,40960,44032,48128,51200,55296,58368,62464,65536,62464,65536,62464,65536,268439552,336991254,1,0,80,0,43008,1056915071,50465804,16780300,36,8064,224,1129709568,1129709568,1129709568,1098907648,1098907648,0,136728,1073741824,7704,44032,44032,40960,729216,1073741824,41088,17237,1107575640,4294920876,4294967294,17237,1107575640,4294920876,4294967294,17237,1107575640,4294920876,4294967294,
7268352,7268352,7311360,7311360,7354368,7354368,7397376,7397376,7397376,7397376,7397376,7397376,40960,44032,48128,51200,55296,58368,62464,65536,62464,65536,62464,65536,268439552,336991254,1,0,80,0,192,1056915199,16780300,50465804,8064,36,224,1126825984,1126825984,1126825984,1098907648,1098907648,0,1371456,1073741824,1370880,44032,44032,40960,7314432,1073741824,7311360,17193,1107575596,4294930732,4294967294,17193,1107575596,4294930732,4294967294,17193,1107575596,4294930732,4294967294,
1505280,1505280,1548288,1548288,1591296,1591296,1634304,1634304,1634304,1634304,1634304,1634304,40960,44032,48128,51200,55296,58368,62464,65536,62464,65536,62464,65536,268439552,336991254,1,0,80,0,192,1040137727,16780300,50465804,8064,36,224,1108344832,1108344832,1108344832,1098907648,1098907648,0,290880,1073741824,290304,44032,44032,40960,1551360,1073741824,1548288,16908,1107575320,4294960748,4294967294,16908,1107575320,4294960748,4294967294,16908,1107575320,4294960748,4294967294,
258048,301056,344064,387072,430080,473088,5720064,5763072,5763072,5763072,5763072,5763072,49152,53248,57344,61440,65536,69632,40960,45056,45056,45056,45056,45056,1077936128,320017685,1,512,80,0,192,1040137855,16780300,50465804,8064,36,128,1124335616,1124335616,1090519040,1115684864,1115684864,0,1066752,1073741824,64512,32768,32768,53248,5689344,1073741824,344064,1115439862,1117799174,4294951180,62,1115439862,1117799174,4294951180,62,16576,1099972912,780,4294967294,
4224,4224,4416,4416,4608,4608,4800,4800,4992,4992,5184,5184,40960,44032,48128,51200,55296,58368,62464,65536,69632,72704,76800,79872,268439552,336991254,1,1024,64,0,43008,1048526591,50465804,16780300,36,8064,224,1103101952,1103101952,1103101952,1098907648,1098907648,0,129888,1073741824,864,51200,51200,48128,692736,1073741824,4608,16816,1107575256,4294963660,4294967294,16816,1107575256,4294963660,4294967294,16816,1107575256,4294963660,4294967294,
14016,14016,14208,14208,14400,14400,14592,14592,14592,14592,14592,14592,40960,44032,48128,51200,55296,58368,62464,65536,62464,65536,62464,65536,268439552,336991254,1,0,80,0,43008,1048526463,50465804,16780300,36,8064,224,1116995584,1116995584,1116995584,1098907648,1098907648,0,131688,1073741824,2664,44032,44032,40960,702336,1073741824,14208,17042,1107575448,4294952236,4294967294,17042,1107575448,4294952236,4294967294,17042,1107575448,4294952236,4294967294,
1290240,1290240,1333248,1333248,1376256,1376256,1419264,1419264,1462272,1462272,1505280,1505280,40960,44032,48128,51200,55296,58368,62464,65536,69632,72704,76800,79872,2154856560,336991254,1,1024,64,0,192,1048526591,16780300,50465804,8064,36,224,1107296256,1107296256,1107296256,1124073472,1124073472,1121976320,262656,1073741824,262080,51200,51200,48128,1400832,1073741824,1397760,1121731056,1125204492,4294961196,110,1121731056,1125204492,4294961196,110,1121731056,1125204492,4294961196,110,
32064,32064,32256,32256,32448,32448,32640,32640,32640,32640,32640,32640,40960,44032,48128,51200,55296,58368,62464,65536,62464,65536,62464,65536,268439552,336991254,1,0,80,0,43008,1056915199,50465804,16780300,36,8064,224,1126694912,1126694912,1126694912,1098907648,1098907648,0,135072,1073741824,6048,44032,44032,40960,720384,1073741824,32256,17191,1107575594,4294931180,4294967294,17191,1107575594,4294931180,4294967294,17191,1107575594,4294931180,4294967294,
41280,41280,41472,41472,41664,41664,41856,41856,41856,41856,41856,41856,40960,44032,48128,51200,55296,58368,62464,65536,62464,65536,62464,65536,268439552,336991254,1,0,80,0,43008,1056915071,50465804,16780300,36,8064,224,1129840640,1129840640,1129840640,1098907648,1098907648,0,136800,1073741824,7776,44032,44032,40960,729600,1073741824,41472,17239,1107575642,4294920428,4294967294,17239,1107575642,4294920428,4294967294,17239,1107575642,4294920428,4294967294,
7354368,7354368,7397376,7397376,7440384,7440384,7483392,7483392,7483392,7483392,7483392,7483392,40960,44032,48128,51200,55296,58368,62464,65536,62464,65536,62464,65536,268439552,336991254,1,0,80,0,192,1056915199,16780300,50465804,8064,36,224,1126957056,1126957056,1126957056,1098907648,1098907648,0,1387584,1073741824,1387008,44032,44032,40960,7400448,1073741824,7397376,17195,1107575598,4294930284,4294967294,17195,1107575598,4294930284,4294967294,17195,1107575598,4294930284,4294967294,
0,0,0,43008,86016,129024,2881536,2924544,2924544,2924544,2924544,2924544,49152,53248,57344,61440,65536,69632,40960,45056,45056,45056,45056,45056,808452096,320017685,1,512,80,0,192,1040137727,16780300,50465804,8064,36,128,1115947008,1115947008,0,1111490560,1111490560,0,533952,1073741824,0,32768,32768,53248,2847744,1073741824,0,1111245412,1115701896,4294959724,46,1111245412,1115701896,4294959724,46,0,1099972672,1804,4294967294,
3569664,3569664,3612672,3612672,3655680,3655680,3698688,3698688,3698688,3698688,3698688,3698688,40960,44032,48128,51200,55296,58368,62464,65536,62464,65536,62464,65536,268439552,336991254,1,0,80,0,192,1040137983,16780300,50465804,8064,36,224,1118306304,1118306304,1118306304,1098907648,1098907648,0,677952,1073741824,677376,44032,44032,40960,3615744,1073741824,3612672,17062,1107575468,4294949996,4294967294,17062,1107575468,4294949996,4294967294,17062,1107575468,4294949996,4294967294,
4608,4608,4800,4800,4992,4992,5184,5184,5376,5376,5568,5568,40960,44032,48128,51200,55296,58368,62464,65536,69632,72704,76800,79872,268439552,336991254,1,1024,64,0,43008,1048526591,50465804,16780300,36,8064,224,1104150528,1104150528,1104150528,1098907648,1098907648,0,129960,1073741824,936,51200,51200,48128,693120,1073741824,4992,16832,1107575272,4294963212,4294967294,16832,1107575272,4294963212,4294967294,16832,1107575272,4294963212,4294967294,
14400,14400,14592,14592,14784,14784,14976,14976,14976,14976,14976,14976,40960,44032,48128,51200,55296,58368,62464,65536,62464,65536,62464,65536,268439552,336991254,1,0,80,0,43008,1048526463,50465804,16780300,36,8064,224,1117257728,1117257728,1117257728,1098907648,1098907648,0,131760,1073741824,2736,44032,44032,40960,702720,1073741824,14592,17046,1107575452,4294951788,4294967294,17046,1107575452,4294951788,4294967294,17046,1107575452,4294951788,4294967294,
1290240,1290240,1333248,1333248,1376256,1376256,1419264,1419264,1462272,1462272,1505280,1505280,40960,44032,48128,51200,55296,58368,62464,65536,69632,72704,76800,79872,2693832848,336991254,1,1024,64,0,192,1048526591,16780300,50465804,8064,36,224,1107296256,1107296256,1107296256,1126170624,1126170624,1125122048,263808,1073741824,263232,51200,51200,48128,1406976,1073741824,1403904,1125007856,1127301644,4294961004,142,1125007856,1127301644,4294961004,142,1125007856,1127301644,4294961004,142,
32448,32448,32640,32640,32832,32832,33024,33024,33024,33024,33024,33024,40960,44032,48128,51200,55296,58368,62464,65536,62464,65536,62464,65536,268439552,336991254,1,0,80,0,43008,1056914943,50465804,16780300,36,8064,224,1126825984,1126825984,1126825984,1098907648,1098907648,0,135144,1073741824,6120,44032,44032,40960,720768,1073741824,32640,17193,1107575596,4294930732,4294967294,17193,1107575596,4294930732,4294967294,17193,1107575596,4294930732,4294967294,
41664,41664,41856,41856,42048,42048,42240,42240,42240,42240,42240,42240,40960,44032,48128,51200,55296,58368,62464,65536,62464,65536,62464,65536,268439552,336991254,1,0,80,0,43008,1056915071,50465804,16780300,36,8064,224,1129971712,1129971712,1129971712,1098907648,1098907648,0,136872,1073741824,7848,44032,44032,40960,729984,1073741824,41856,17241,1107575644,4294919980,4294967294,17241,1107575644,4294919980,4294967294,17241,1107575644,4294919980,4294967294,
7440384,7440384,7483392,7483392,7526400,7526400,7569408,7569408,7569408,7569408,7569408,7569408,40960,44032,48128,51200,55296,58368,62464,65536,62464,65536,62464,65536,268439552,336991254,1,0,80,0,192,1056915199,16780300,50465804,8064,36,224,1127088128,1127088128,1127088128,1098907648,1098907648,0,1403712,1073741824,1403136,44032,44032,40960,7486464,1073741824,7483392,17197,1107575600,4294929836,4294967294,17197,1107575600,4294929836,4294967294,17197,1107575600,4294929836,4294967294,
1290240,1333248,1376256,1419264,1462272,1505280,1548288,1591296,1634304,1677312,1677312,1677312,40960,45056,49152,53248,57344,61440,65536,69632,73728,77824,77824,77824,538976288,353703189,0,1280,64,0,192,1040137855,16780300,50465804,8064,36,128,1108344832,1107820544,1107296256,1107296256,1107296256,1107296256,291456,275328,259200,61440,53248,45056,1554432,1468416,1382400,1106264560,1112031772,4294964556,30,1106264560,1112031772,4294964556,30,1106264560,1112031772,4294964556,30,
3655680,3655680,3698688,3698688,3741696,3741696,3784704,3784704,3784704,3784704,3784704,3784704,40960,44032,48128,51200,55296,58368,62464,65536,62464,65536,62464,65536,268439552,336991254,1,0,80,0,192,1040137983,16780300,50465804,8064,36,224,1118568448,1118568448,1118568448,1098907648,1098907648,0,694080,1073741824,693504,44032,44032,40960,3701760,1073741824,3698688,17066,1107575472,4294949548,4294967294,17066,1107575472,4294949548,4294967294,17066,1107575472,4294949548,4294967294,
3397632,3440640,3483648,3526656,3569664,3612672,3655680,3698688,3698688,3698688,3698688,3698688,40960,45056,49152,53248,57344,61440,65536,69632,69632,69632,69632,69632,538976288,353703189,0,256,80,0,192,1048526591,16780300,50465804,8064,36,128,1118306304,1118044160,1117782016,1107296256,1107296256,1107296256,678528,662400,646272,57344,49152,40960,3618816,3532800,3446784,1106264734,1112031916,4294958284,30,1106264734,1112031916,4294958284,30,1106264734,1112031916,4294958284,30,
14784,14784,14976,14976,15168,15168,15360,15360,15360,15360,15360,15360,40960,44032,48128,51200,55296,58368,62464,65536,62464,65536,62464,65536,268439552,336991254,1,0,80,0,43008,1048526463,50465804,16780300,36,8064,224,1117519872,1117519872,1117519872,1098907648,1098907648,0,131832,1073741824,2808,44032,44032,40960,703104,1073741824,14976,17050,1107575456,4294951340,4294967294,17050,1107575456,4294951340,4294967294,17050,1107575456,4294951340,4294967294,
1290240,1290240,1333248,1333248,1376256,1376256,1419264,1419264,1462272,1462272,1505280,1505280,40960,44032,48128,51200,55296,58368,62464,65536,69632,72704,76800,79872,3232809136,336991254,1,1024,64,0,192,1048526591,16780300,50465804,8064,36,224,1107296256,1107296256,1107296256,1128267776,1128267776,1127219200,264960,1073741824,264384,51200,51200,48128,1413120,1073741824,1410048,1127105008,1129398796,4294960812,174,1127105008,1129398796,4294960812,174,1127105008,1129398796,4294960812,174,
6494208,6537216,6580224,6623232,6666240,6709248,6752256,6795264,6795264,6795264,6795264,6795264,40960,45056,49152,53248,57344,61440,65536,69632,69632,69632,69632,69632,538976288,353703189,0,256,80,0,192,1056914943,16780300,50465804,8064,36,128,1125908480,1125777408,1125646336,1107296256,1107296256,1107296256,1259136,1243008,1226880,57344,49152,40960,6715392,6629376,6543360,1106264855,1112032030,4294949068,30,1106264855,1112032030,4294949068,30,1106264855,1112032030,4294949068,30,
42048,42048,42240,42240,42432,42432,42624,42624,42624,42624,42624,42624,40960,44032,48128,51200,55296,58368,62464,65536,62464,65536,62464,65536,268439552,336991254,1,0,80,0,43008,1056915071,50465804,16780300,36,8064,224,1130102784,1130102784,1130102784,1098907648,1098907648,0,136944,1073741824,7920,44032,44032,40960,730368,1073741824,42240,17243,1107575646,4294919532,4294967294,17243,1107575646,4294919532,4294967294,17243,1107575646,4294919532,4294967294,
7526400,7526400,7569408,7569408,7612416,7612416,7655424,7655424,7655424,7655424,7655424,7655424,40960,44032,48128,51200,55296,58368,62464,65536,62464,65536,62464,65536,268439552,336991254,1,0,80,0,192,1056915199,16780300,50465804,8064,36,224,1127219200,1127219200,1127219200,1098907648,1098907648,0,1419840,1073741824,1419264,44032,44032,40960,7572480,1073741824,7569408,17199,1107575602,4294929388,4294967294,17199,1107575602,4294929388,4294967294,17199,1107575602,4294929388,4294967294,
1591296,1591296,1634304,1634304,1677312,1677312,1720320,1720320,1720320,1720320,1720320,1720320,40960,44032,48128,51200,55296,58368,62464,65536,62464,65536,62464,65536,268439552,336991254,1,0,80,0,192,1040137855,16780300,50465804,8064,36,224,1108869120,1108869120,1108869120,1098907648,1098907648,0,307008,1073741824,306432,44032,44032,40960,1637376,1073741824,1634304,16916,1107575328,4294960300,4294967294,16916,1107575328,4294960300,4294967294,16916,1107575328,4294960300,4294967294,
3741696,3741696,3784704,3784704,3827712,3827712,3870720,3870720,3870720,3870720,3870720,3870720,40960,44032,48128,51200,55296,58368,62464,65536,62464,65536,62464,65536,268439552,336991254,1,0,80,0,192,1040137983,16780300,50465804,8064,36,224,1118830592,1118830592,1118830592,1098907648,1098907648,0,710208,1073741824,709632,44032,44032,40960,3787776,1073741824,3784704,17070,1107575476,4294949100,4294967294,17070,1107575476,4294949100,4294967294,17070,1107575476,4294949100,4294967294,
4992,4992,5184,5184,5376,5376,5568,5568,5760,5760,5952,5952,40960,44032,48128,51200,55296,58368,62464,65536,69632,72704,76800,79872,268439552,336991254,1,1024,64,0,43008,1048526591,50465804,16780300,36,8064,224,1105199104,1105199104,1105199104,1098907648,1098907648,0,130032,1073741824,1008,51200,51200,48128,693504,1073741824,5376,16848,1107575288,4294962764,4294967294,16848,1107575288,4294962764,4294967294,16848,1107575288,4294962764,4294967294,
4429824,4472832,4515840,4558848,4601856,4644864,4687872,4730880,4730880,4730880,4730880,4730880,40960,45056,49152,53248,57344,61440,65536,69632,69632,69632,69632,69632,538976288,353703189,0,256,80,0,192,1048526463,16780300,50465804,8064,36,128,1121452032,1121189888,1120927744,1107296256,1107296256,1107296256,872064,855936,839808,57344,49152,40960,4651008,4564992,4478976,1106264782,1112031964,4294955212,30,1106264782,1112031964,4294955212,30,1106264782,1112031964,4294955212,30,
22848,22848,23040,23040,23232,23232,23424,23424,23424,23424,23424,23424,40960,44032,48128,51200,55296,58368,62464,65536,62464,65536,62464,65536,268439552,336991254,1,0,80,0,43008,1048526591,50465804,16780300,36,8064,224,1123024896,1123024896,1123024896,1098907648,1098907648,0,133344,1073741824,4320,44032,44032,40960,711168,1073741824,23040,17134,1107575540,4294941932,4294967294,17134,1107575540,4294941932,4294967294,17134,1107575540,4294941932,4294967294,
32832,32832,33024,33024,33216,33216,33408,33408,33408,33408,33408,33408,40960,44032,48128,51200,55296,58368,62464,65536,62464,65536,62464,65536,268439552,336991254,1,0,80,0,43008,1056914943,50465804,16780300,36,8064,224,1126957056,1126957056,1126957056,1098907648,1098907648,0,135216,1073741824,6192,44032,44032,40960,721152,1073741824,33024,17195,1107575598,4294930284,4294967294,17195,1107575598,4294930284,4294967294,17195,1107575598,4294930284,4294967294,
42432,42432,42624,42624,42816,42816,42816,42816,42816,42816,42816,42816,40960,44032,48128,51200,55296,58368,62464,65536,62464,65536,62464,65536,268439552,336991254,1,0,80,0,43008,1056915071,50465804,16780300,36,8064,224,1130233856,1130233856,1130233856,1098907648,1098907648,0,137016,1073741824,7992,44032,44032,40960,730752,1073741824,42624,17245,1107575647,4294919084,4294967294,17245,1107575647,4294919084,4294967294,17245,1107575647,4294919084,4294967294,
7612416,7612416,7655424,7655424,7698432,7698432,7741440,7741440,7741440,7741440,7741440,7741440,40960,44032,48128,51200,55296,58368,62464,65536,62464,65536,62464,65536,268439552,336991254,1,0,80,0,192,1056915199,16780300,50465804,8064,36,224,1127350272,1127350272,1127350272,1098907648,1098907648,0,1435968,1073741824,1435392,44032,44032,40960,7658496,1073741824,7655424,17201,1107575604,4294928940,4294967294,17201,1107575604,4294928940,4294967294,17201,1107575604,4294928940,4294967294,
1677312,1677312,1720320,1720320,1763328,1763328,1806336,1806336,1806336,1806336,1806336,1806336,40960,44032,48128,51200,55296,58368,62464,65536,62464,65536,62464,65536,268439552,336991254,1,0,80,0,192,1040137855,16780300,50465804,8064,36,224,1109393408,1109393408,1109393408,1098907648,1098907648,0,323136,1073741824,322560,44032,44032,40960,1723392,1073741824,1720320,16924,1107575336,4294959852,4294967294,16924,1107575336,4294959852,4294967294,16924,1107575336,4294959852,4294967294,
2365440,2408448,2451456,2494464,2537472,2580480,2623488,2666496,2666496,2666496,2666496,2666496,40960,45056,49152,53248,57344,61440,65536,69632,69632,69632,69632,69632,538976288,353703189,0,256,80,0,192,1040137983,16780300,50465804,8064,36,128,1114636288,1114112000,1113587712,1107296256,1107296256,1107296256,484992,468864,452736,57344,49152,40960,2586624,2500608,2414592,1106264668,1112031864,4294961356,30,1106264668,1112031864,4294961356,30,1106264668,1112031864,4294961356,30,
5376,5376,5568,5568,5760,5760,5952,5952,6144,6144,6336,6336,40960,44032,48128,51200,55296,58368,62464,65536,69632,72704,76800,79872,268439552,336991254,1,1024,64,0,43008,1048526335,50465804,16780300,36,8064,224,1106247680,1106247680,1106247680,1098907648,1098907648,0,130104,1073741824,1080,51200,51200,48128,693888,1073741824,5760,16864,1107575300,4294962316,4294967294,16864,1107575300,4294962316,4294967294,16864,1107575300,4294962316,4294967294,
15168,15168,15360,15360,15552,15552,15744,15744,15744,15744,15744,15744,40960,44032,48128,51200,55296,58368,62464,65536,62464,65536,62464,65536,268439552,336991254,1,0,80,0,43008,1048526463,50465804,16780300,36,8064,224,1117782016,1117782016,1117782016,1098907648,1098907648,0,131904,1073741824,2880,44032,44032,40960,703488,1073741824,15360,17054,1107575460,4294950892,4294967294,17054,1107575460,4294950892,4294967294,17054,1107575460,4294950892,4294967294,
5462016,5505024,5548032,5591040,5634048,5677056,5720064,5763072,5763072,5763072,5763072,5763072,40960,45056,49152,53248,57344,61440,65536,69632,69632,69632,69632,69632,538976288,353703189,0,256,80,0,192,1048526591,16780300,50465804,8064,36,128,1124335616,1124204544,1124073472,1107296256,1107296256,1107296256,1065600,1049472,1033344,57344,49152,40960,5683200,5597184,5511168,1106264830,1112032006,4294952140,30,1106264830,1112032006,4294952140,30,1106264830,1112032006,4294952140,30,
946176,989184,1032192,1075200,1118208,1161216,2881536,2924544,2924544,2924544,2924544,2924544,49152,53248,57344,61440,65536,69632,40960,45056,45056,45056,45056,45056,2694840320,320017685,1,0,80,0,192,1056914943,16780300,50465804,8064,36,128,1115947008,1115947008,1103101952,1126170624,1126170624,0,537984,1073741824,193536,32768,32768,53248,2869248,1073741824,1032192,1126122084,1127236232,4294959052,158,1126122084,1127236232,4294959052,158,16816,1099973080,4294966028,4294967294,
7526400,7569408,7612416,7655424,7698432,7741440,7784448,7827456,7827456,7827456,7827456,7827456,40960,45056,49152,53248,57344,61440,65536,69632,69632,69632,69632,69632,538976288,353703189,0,256,80,0,192,1056915199,16780300,50465804,8064,36,128,1127481344,1127350272,1127219200,1107296256,1107296256,1107296256,1452672,1436544,1420416,57344,49152,40960,7747584,7661568,7575552,1106264879,1112032054,4294945996,30,1106264879,1112032054,4294945996,30,1106264879,1112032054,4294945996,30,
8558592,8601600,8644608,8687616,8730624,8773632,8816640,8859648,8859648,8859648,8859648,8859648,40960,45056,49152,53248,57344,61440,65536,69632,69632,69632,69632,69632,538976288,353703189,0,256,80,0,192,1056915199,16780300,50465804,8064,36,128,1129054208,1128923136,1128792064,1107296256,1107296256,1107296256,1646208,1630080,1613952,57344,49152,40960,8779776,8693760,8607744,1106264903,1112032078,4294942924,30,1106264903,1112032078,4294942924,30,1106264903,1112032078,4294942924,30,
1763328,1763328,1806336,1806336,1849344,1849344,1892352,1892352,1892352,1892352,1892352,1892352,40960,44032,48128,51200,55296,58368,62464,65536,62464,65536,62464,65536,268439552,336991254,1,0,80,0,192,1040137855,16780300,50465804,8064,36,224,1109917696,1109917696,1109917696,1098907648,1098907648,0,339264,1073741824,338688,44032,44032,40960,1809408,1073741824,1806336,16932,1107575344,4294959404,4294967294,16932,1107575344,4294959404,4294967294,16932,1107575344,4294959404,4294967294,
3827712,3827712,3870720,3870720,3913728,3913728,3956736,3956736,3956736,3956736,3956736,3956736,40960,44032,48128,51200,55296,58368,62464,65536,62464,65536,62464,65536,268439552,336991254,1,0,80,0,192,1040137983,16780300,50465804,8064,36,224,1119092736,1119092736,1119092736,1098907648,1098907648,0,726336,1073741824,725760,44032,44032,40960,3873792,1073741824,3870720,17074,1107575480,4294948652,4294967294,17074,1107575480,4294948652,4294967294,17074,1107575480,4294948652,4294967294,
5760,5760,5952,5952,6144,6144,6336,6336,6528,6528,6720,6720,40960,44032,48128,51200,55296,58368,62464,65536,69632,72704,76800,79872,268439552,336991254,1,1024,64,0,43008,1048526335,50465804,16780300,36,8064,224,1107296256,1107296256,1107296256,1098907648,1098907648,0,130176,1073741824,1152,51200,51200,48128,694272,1073741824,6144,16880,1107575308,4294961868,4294967294,16880,1107575308,4294961868,4294967294,16880,1107575308,4294961868,4294967294,
15552,15552,15744,15744,15936,15936,16128,16128,16128,16128,16128,16128,40960,44032,48128,51200,55296,58368,62464,65536,62464,65536,62464,65536,268439552,336991254,1,0,80,0,43008,1048526463,50465804,16780300,36,8064,224,1118044160,1118044160,1118044160,1098907648,1098907648,0,131976,1073741824,2952,44032,44032,40960,703872,1073741824,15744,17058,1107575464,4294950444,4294967294,17058,1107575464,4294950444,4294967294,17058,1107575464,4294950444,4294967294,
860160,903168,946176,989184,1032192,1075200,8558592,8601600,8601600,8601600,8601600,8601600,49152,53248,57344,61440,65536,69632,40960,45056,45056,45056,45056,45056,2155872256,320017685,1,0,80,0,192,1048526591,16780300,50465804,8064,36,128,1128660992,1128660992,1102053376,1124073472,1124073472,0,1601280,1073741824,177408,32768,32768,53248,8540160,1073741824,946176,1123959613,1125139272,4294942348,126,1123959613,1125139272,4294942348,126,16800,1099973064,4294966284,4294967294,
33216,33216,33408,33408,33600,33600,33792,33792,33792,33792,33792,33792,40960,44032,48128,51200,55296,58368,62464,65536,62464,65536,62464,65536,268439552,336991254,1,0,80,0,43008,1056915071,50465804,16780300,36,8064,224,1127088128,1127088128,1127088128,1098907648,1098907648,0,135288,1073741824,6264,44032,44032,40960,721536,1073741824,33408,17197,1107575600,4294929836,4294967294,17197,1107575600,4294929836,4294967294,17197,1107575600,4294929836,4294967294,
5462016,5462016,5505024,5505024,5548032,5548032,5591040,5591040,5591040,5591040,5591040,5591040,40960,44032,48128,51200,55296,58368,62464,65536,62464,65536,62464,65536,268439552,336991254,1,0,80,0,192,1056915199,16780300,50465804,8064,36,224,1124073472,1124073472,1124073472,1098907648,1098907648,0,1032768,1073741824,1032192,44032,44032,40960,5508096,1073741824,5505024,17150,1107575554,4294940140,4294967294,17150,1107575554,4294940140,4294967294,17150,1107575554,4294940140,4294967294,
7698432,7698432,7741440,7741440,7784448,7784448,7827456,7827456,7827456,7827456,7827456,7827456,40960,44032,48128,51200,55296,58368,62464,65536,62464,65536,62464,65536,268439552,336991254,1,0,80,0,192,1056915199,16780300,50465804,8064,36,224,1127481344,1127481344,1127481344,1098907648,1098907648,0,1452096,1073741824,1451520,44032,44032,40960,7744512,1073741824,7741440,17203,1107575606,4294928492,4294967294,17203,1107575606,4294928492,4294967294,17203,1107575606,4294928492,4294967294,
1849344,1849344,1892352,1892352,1935360,1935360,1978368,1978368,1978368,1978368,1978368,1978368,40960,44032,48128,51200,55296,58368,62464,65536,62464,65536,62464,65536,268439552,336991254,1,0,80,0,192,1040137855,16780300,50465804,8064,36,224,1110441984,1110441984,1110441984,1098907648,1098907648,0,355392,1073741824,354816,44032,44032,40960,1895424,1073741824,1892352,16940,1107575352,4294958956,4294967294,16940,1107575352,4294958956,4294967294,16940,1107575352,4294958956,4294967294,
3913728,3913728,3956736,3956736,3999744,3999744,4042752,4042752,4042752,4042752,4042752,4042752,40960,44032,48128,51200,55296,58368,62464,65536,62464,65536,62464,65536,268439552,336991254,1,0,80,0,192,1040137983,16780300,50465804,8064,36,224,1119354880,1119354880,1119354880,1098907648,1098907648,0,742464,1073741824,741888,44032,44032,40960,3959808,1073741824,3956736,17078,1107575484,4294948204,4294967294,17078,1107575484,4294948204,4294967294,17078,1107575484,4294948204,4294967294,
6336,6336,6528,6528,6720,6720,6912,6912,6912,6912,6912,6912,40960,44032,48128,51200,55296,58368,62464,65536,62464,65536,62464,65536,268439552,336991254,1,0,80,0,43008,1048526335,50465804,16780300,36,8064,224,1107820544,1107820544,1107820544,1098907648,1098907648,0,130248,1073741824,1224,44032,44032,40960,694656,1073741824,6528,16900,1107575312,4294961196,4294967294,16900,1107575312,4294961196,4294967294,16900,1107575312,4294961196,4294967294,
5634048,5677056,5720064,5763072,5806080,5849088,5849088,5849088,5849088,5849088,5849088,5849088,40960,45056,49152,53248,57344,61440,61440,61440,61440,61440,61440,61440,1886417008,320017171,0,256,80,0,192,1048526463,16780300,50465804,8064,36,128,1124204544,1124335616,1124466688,1121976320,1121976320,1121976320,1052352,1068480,1084608,32768,40960,49152,5612544,5698560,5784576,1121862386,1124090632,4294951148,110,1121862386,1124090632,4294951148,110,1121862386,1124090632,4294951148,110,
23232,23232,23424,23424,23616,23616,23808,23808,23808,23808,23808,23808,40960,44032,48128,51200,55296,58368,62464,65536,62464,65536,62464,65536,268439552,336991254,1,0,80,0,43008,1048526591,50465804,16780300,36,8064,224,1123287040,1123287040,1123287040,1098907648,1098907648,0,133416,1073741824,4392,44032,44032,40960,711552,1073741824,23424,17138,1107575544,4294941484,4294967294,17138,1107575544,4294941484,4294967294,17138,1107575544,4294941484,4294967294,
33600,33600,33792,33792,33984,33984,34176,34176,34176,34176,34176,34176,40960,44032,48128,51200,55296,58368,62464,65536,62464,65536,62464,65536,268439552,336991254,1,0,80,0,43008,1056915071,50465804,16780300,36,8064,224,1127219200,1127219200,1127219200,1098907648,1098907648,0,135360,1073741824,6336,44032,44032,40960,721920,1073741824,33792,17199,1107575602,4294929388,4294967294,17199,1107575602,4294929388,4294967294,17199,1107575602,4294929388,4294967294,
5548032,5548032,5591040,5591040,5634048,5634048,5677056,5677056,5677056,5677056,5677056,5677056,40960,44032,48128,51200,55296,58368,62464,65536,62464,65536,62464,65536,268439552,336991254,1,0,80,0,192,1056915199,16780300,50465804,8064,36,224,1124204544,1124204544,1124204544,1098907648,1098907648,0,1048896,1073741824,1048320,44032,44032,40960,5594112,1073741824,5591040,17153,1107575556,4294939692,4294967294,17153,1107575556,4294939692,4294967294,17153,1107575556,4294939692,4294967294,
7784448,7784448,7827456,7827456,7870464,7870464,7913472,7913472,7913472,7913472,7913472,7913472,40960,44032,48128,51200,55296,58368,62464,65536,62464,65536,62464,65536,268439552,336991254,1,0,80,0,192,1056914943,16780300,50465804,8064,36,224,1127612416,1127612416,1127612416,1098907648,1098907648,0,1468224,1073741824,1467648,44032,44032,40960,7830528,1073741824,7827456,17205,1107575608,4294928044,4294967294,17205,1107575608,4294928044,4294967294,17205,1107575608,4294928044,4294967294,
1935360,1935360,1978368,1978368,2021376,2021376,2064384,2064384,2064384,2064384,2064384,2064384,40960,44032,48128,51200,55296,58368,62464,65536,62464,65536,62464,65536,268439552,336991254,1,0,80,0,192,1040137855,16780300,50465804,8064,36,224,1110966272,1110966272,1110966272,1098907648,1098907648,0,371520,1073741824,370944,44032,44032,40960,1981440,1073741824,1978368,16948,1107575360,4294958508,4294967294,16948,1107575360,4294958508,4294967294,16948,1107575360,4294958508,4294967294,
3999744,3999744,4042752,4042752,4085760,4085760,4128768,4128768,4128768,4128768,4128768,4128768,40960,44032,48128,51200,55296,58368,62464,65536,62464,65536,62464,65536,268439552,336991254,1,0,80,0,192,1040137983,16780300,50465804,8064,36,224,1119617024,1119617024,1119617024,1098907648,1098907648,0,758592,1073741824,758016,44032,44032,40960,4045824,1073741824,4042752,17082,1107575488,4294947756,4294967294,17082,1107575488,4294947756,4294967294,17082,1107575488,4294947756,4294967294,
430080,473088,516096,559104,602112,645120,2881536,2924544,2924544,2924544,2924544,2924544,49152,53248,57344,61440,65536,69632,40960,45056,45056,45056,45056,45056,1616904192,320017685,1,512,80,0,192,1048526335,16780300,50465804,8064,36,128,1115947008,1115947008,1094713344,1119879168,1119879168,0,535680,1073741824,96768,32768,32768,53248,2856960,1073741824,516096,1119765092,1121993352,4294959436,94,1119765092,1121993352,4294959436,94,16672,1099972976,268,4294967294,
15936,15936,16128,16128,16320,16320,16512,16512,16512,16512,16512,16512,40960,44032,48128,51200,55296,58368,62464,65536,62464,65536,62464,65536,268439552,336991254,1,0,80,0,43008,1048526591,50465804,16780300,36,8064,224,1118306304,1118306304,1118306304,1098907648,1098907648,0,132048,1073741824,3024,44032,44032,40960,704256,1073741824,16128,17062,1107575468,4294949996,4294967294,17062,1107575468,4294949996,4294967294,17062,1107575468,4294949996,4294967294,
23616,23616,23808,23808,24000,24000,24192,24192,24192,24192,24192,24192,40960,44032,48128,51200,55296,58368,62464,65536,62464,65536,62464,65536,268439552,336991254,1,0,80,0,43008,1048526591,50465804,16780300,36,8064,224,1123549184,1123549184,1123549184,1098907648,1098907648,0,133488,1073741824,4464,44032,44032,40960,711936,1073741824,23808,17142,1107575548,4294941036,4294967294,17142,1107575548,4294941036,4294967294,17142,1107575548,4294941036,4294967294,
33984,33984,34176,34176,34368,34368,34560,34560,34560,34560,34560,34560,40960,44032,48128,51200,55296,58368,62464,65536,62464,65536,62464,65536,268439552,336991254,1,0,80,0,43008,1056915071,50465804,16780300,36,8064,224,1127350272,1127350272,1127350272,1098907648,1098907648,0,135432,1073741824,6408,44032,44032,40960,722304,1073741824,34176,17201,1107575604,4294928940,4294967294,17201,1107575604,4294928940,4294967294,17201,1107575604,4294928940,4294967294,
5634048,5634048,5677056,5677056,5720064,5720064,5763072,5763072,5763072,5763072,5763072,5763072,40960,44032,48128,51200,55296,58368,62464,65536,62464,65536,62464,65536,268439552,336991254,1,0,80,0,192,1056915199,16780300,50465804,8064,36,224,1124335616,1124335616,1124335616,1098907648,1098907648,0,1065024,1073741824,1064448,44032,44032,40960,5680128,1073741824,5677056,17155,1107575558,4294939244,4294967294,17155,1107575558,4294939244,4294967294,17155,1107575558,4294939244,4294967294,
7870464,7870464,7913472,7913472,7956480,7956480,7999488,7999488,7999488,7999488,7999488,7999488,40960,44032,48128,51200,55296,58368,62464,65536,62464,65536,62464,65536,268439552,336991254,1,0,80,0,192,1056914943,16780300,50465804,8064,36,224,1127743488,1127743488,1127743488,1098907648,1098907648,0,1484352,1073741824,1483776,44032,44032,40960,7916544,1073741824,7913472,17207,1107575610,4294927596,4294967294,17207,1107575610,4294927596,4294967294,17207,1107575610,4294927596,4294967294,
2021376,2021376,2064384,2064384,2107392,2107392,2150400,2150400,2150400,2150400,2150400,2150400,40960,44032,48128,51200,55296,58368,62464,65536,62464,65536,62464,65536,268439552,336991254,1,0,80,0,192,1040137855,16780300,50465804,8064,36,224,1111490560,1111490560,1111490560,1098907648,1098907648,0,387648,1073741824,387072,44032,44032,40960,2067456,1073741824,2064384,16956,1107575368,4294958060,4294967294,16956,1107575368,4294958060,4294967294,16956,1107575368,4294958060,4294967294,
344064,387072,430080,473088,516096,559104,8558592,8601600,8601600,8601600,8601600,8601600,49152,53248,57344,61440,65536,69632,40960,45056,45056,45056,45056,45056,1077936128,320017685,1,512,80,0,192,1040137983,16780300,50465804,8064,36,128,1128660992,1128660992,1092616192,1115684864,1115684864,0,1598976,1073741824,80640,32768,32768,53248,8527872,1073741824,430080,1115439933,1117799240,4294942732,62,1115439933,1117799240,4294942732,62,16640,1099972944,524,4294967294,
6720,6720,6912,6912,7104,7104,7296,7296,7296,7296,7296,7296,40960,44032,48128,51200,55296,58368,62464,65536,62464,65536,62464,65536,268439552,336991254,1,0,80,0,43008,1048526463,50465804,16780300,36,8064,224,1108344832,1108344832,1108344832,1098907648,1098907648,0,130320,1073741824,1296,44032,44032,40960,695040,1073741824,6912,16908,1107575320,4294960748,4294967294,16908,1107575320,4294960748,4294967294,16908,1107575320,4294960748,4294967294,
16320,16320,16512,16512,16704,16704,16896,16896,16896,16896,16896,16896,40960,44032,48128,51200,55296,58368,62464,65536,62464,65536,62464,65536,268439552,336991254,1,0,80,0,43008,1048526591,50465804,16780300,36,8064,224,1118568448,1118568448,1118568448,1098907648,1098907648,0,132120,1073741824,3096,44032,44032,40960,704640,1073741824,16512,17066,1107575472,4294949548,4294967294,17066,1107575472,4294949548,4294967294,17066,1107575472,4294949548,4294967294,
24000,24000,24192,24192,24384,24384,24576,24576,24576,24576,24576,24576,40960,44032,48128,51200,55296,58368,62464,65536,62464,65536,62464,65536,268439552,336991254,1,0,80,0,43008,1048526591,50465804,16780300,36,8064,224,1123811328,1123811328,1123811328,1098907648,1098907648,0,133560,1073741824,4536,44032,44032,40960,712320,1073741824,24192,17146,1107575552,4294940588,4294967294,17146,1107575552,4294940588,4294967294,17146,1107575552,4294940588,4294967294,
34368,34368,34560,34560,34752,34752,34944,34944,34944,34944,34944,34944,40960,44032,48128,51200,55296,58368,62464,65536,62464,65536,62464,65536,268439552,336991254,1,0,80,0,43008,1056915071,50465804,16780300,36,8064,224,1127481344,1127481344,1127481344,1098907648,1098907648,0,135504,1073741824,6480,44032,44032,40960,722688,1073741824,34560,17203,1107575606,4294928492,4294967294,17203,1107575606,4294928492,4294967294,17203,1107575606,4294928492,4294967294,
5720064,5720064,5763072,5763072,5806080,5806080,5849088,5849088,5849088,5849088,5849088,5849088,40960,44032,48128,51200,55296,58368,62464,65536,62464,65536,62464,65536,268439552,336991254,1,0,80,0,192,1056915199,16780300,50465804,8064,36,224,1124466688,1124466688,1124466688,1098907648,1098907648,0,1081152,1073741824,1080576,44032,44032,40960,5766144,1073741824,5763072,17157,1107575560,4294938796,4294967294,17157,1107575560,4294938796,4294967294,17157,1107575560,4294938796,4294967294,
7956480,7956480,7999488,7999488,8042496,8042496,8085504,8085504,8085504,8085504,8085504,8085504,40960,44032,48128,51200,55296,58368,62464,65536,62464,65536,62464,65536,268439552,336991254,1,0,80,0,192,1056914943,16780300,50465804,8064,36,224,1127874560,1127874560,1127874560,1098907648,1098907648,0,1500480,1073741824,1499904,44032,44032,40960,8002560,1073741824,7999488,17209,1107575612,4294927148,4294967294,17209,1107575612,4294927148,4294967294,17209,1107575612,4294927148,4294967294,
0,43008,86016,129024,172032,215040,5720064,5763072,5763072,5763072,5763072,5763072,49152,53248,57344,61440,65536,69632,40960,45056,45056,45056,45056,45056,808452096,320017685,1,512,80,0,192,1040137855,16780300,50465804,8064,36,128,1124335616,1124335616,1073741824,1111490560,1111490560,0,1066176,1073741824,16128,32768,32768,53248,5686272,1073741824,86016,1111245558,1115702022,4294951276,46,1111245558,1115702022,4294951276,46,0,1099972768,1548,4294967294,
4085760,4085760,4128768,4128768,4171776,4171776,4214784,4214784,4214784,4214784,4214784,4214784,40960,44032,48128,51200,55296,58368,62464,65536,62464,65536,62464,65536,268439552,336991254,1,0,80,0,192,1040137983,16780300,50465804,8064,36,224,1119879168,1119879168,1119879168,1098907648,1098907648,0,774720,1073741824,774144,44032,44032,40960,4131840,1073741824,4128768,17086,1107575492,4294947308,4294967294,17086,1107575492,4294947308,4294967294,17086,1107575492,4294947308,4294967294,
7104,7104,7296,7296,7488,7488,7680,7680,7680,7680,7680,7680,40960,44032,48128,51200,55296,58368,62464,65536,62464,65536,62464,65536,268439552,336991254,1,0,80,0,43008,1048526463,50465804,16780300,36,8064,224,1108869120,1108869120,1108869120,1098907648,1098907648,0,130392,1073741824,1368,44032,44032,40960,695424,1073741824,7296,16916,1107575328,4294960300,4294967294,16916,1107575328,4294960300,4294967294,16916,1107575328,4294960300,4294967294,
16704,16704,16896,16896,17088,17088,17280,17280,17280,17280,17280,17280,40960,44032,48128,51200,55296,58368,62464,65536,62464,65536,62464,65536,268439552,336991254,1,0,80,0,43008,1048526591,50465804,16780300,36,8064,224,1118830592,1118830592,1118830592,1098907648,1098907648,0,132192,1073741824,3168,44032,44032,40960,705024,1073741824,16896,17070,1107575476,4294949100,4294967294,17070,1107575476,4294949100,4294967294,17070,1107575476,4294949100,4294967294,
24384,24384,24576,24576,24768,24768,24960,24960,24960,24960,24960,24960,40960,44032,48128,51200,55296,58368,62464,65536,62464,65536,62464,65536,268439552,336991254,1,0,80,0,43008,1056914943,50465804,16780300,36,8064,224,1124073472,1124073472,1124073472,1098907648,1098907648,0,133632,1073741824,4608,44032,44032,40960,712704,1073741824,24576,17150,1107575554,4294940140,4294967294,17150,1107575554,4294940140,4294967294,17150,1107575554,4294940140,4294967294,
34752,34752,34944,34944,35136,35136,35328,35328,35328,35328,35328,35328,40960,44032,48128,51200,55296,58368,62464,65536,62464,65536,62464,65536,268439552,336991254,1,0,80,0,43008,1056915071,50465804,16780300,36,8064,224,1127612416,1127612416,1127612416,1098907648,1098907648,0,135576,1073741824,6552,44032,44032,40960,723072,1073741824,34944,17205,1107575608,4294928044,4294967294,17205,1107575608,4294928044,4294967294,17205,1107575608,4294928044,4294967294,
1204224,1247232,1290240,1333248,1376256,1419264,7870464,7913472,7913472,7913472,7913472,7913472,49152,53248,57344,61440,65536,69632,40960,45056,45056,45056,45056,45056,2964324352,320017685,1,0,80,0,192,1056915199,16780300,50465804,8064,36,128,1127612416,1127612416,1106247680,1127219200,1127219200,0,1473984,1073741824,241920,32768,32768,53248,7861248,1073741824,1290240,1127170861,1128284984,4294944108,174,1127170861,1128284984,4294944108,174,16864,1099973124,4294965260,4294967294,
8042496,8042496,8085504,8085504,8128512,8128512,8171520,8171520,8171520,8171520,8171520,8171520,40960,44032,48128,51200,55296,58368,62464,65536,62464,65536,62464,65536,268439552,336991254,1,0,80,0,192,1056914943,16780300,50465804,8064,36,224,1128005632,1128005632,1128005632,1098907648,1098907648,0,1516608,1073741824,1516032,44032,44032,40960,8088576,1073741824,8085504,17211,1107575614,4294926700,4294967294,17211,1107575614,4294926700,4294967294,17211,1107575614,4294926700,4294967294,
2107392,2107392,2150400,2150400,2193408,2193408,2236416,2236416,2236416,2236416,2236416,2236416,40960,44032,48128,51200,55296,58368,62464,65536,62464,65536,62464,65536,268439552,336991254,1,0,80,0,192,1040137983,16780300,50465804,8064,36,224,1112014848,1112014848,1112014848,1098907648,1098907648,0,403776,1073741824,403200,44032,44032,40960,2153472,1073741824,2150400,16964,1107575376,4294957612,4294967294,16964,1107575376,4294957612,4294967294,16964,1107575376,4294957612,4294967294,
4171776,4171776,4214784,4214784,4257792,4257792,4300800,4300800,4300800,4300800,4300800,4300800,40960,44032,48128,51200,55296,58368,62464,65536,62464,65536,62464,65536,268439552,336991254,1,0,80,0,192,1040137983,16780300,50465804,8064,36,224,1120141312,1120141312,1120141312,1098907648,1098907648,0,790848,1073741824,790272,44032,44032,40960,4217856,1073741824,4214784,17090,1107575496,4294946860,4294967294,17090,1107575496,4294946860,4294967294,17090,1107575496,4294946860,4294967294,
7488,7488,7680,7680,7872,7872,8064,8064,8064,8064,8064,8064,40960,44032,48128,51200,55296,58368,62464,65536,62464,65536,62464,65536,268439552,336991254,1,0,80,0,43008,1048526463,50465804,16780300,36,8064,224,1109393408,1109393408,1109393408,1098907648,1098907648,0,130464,1073741824,1440,44032,44032,40960,695808,1073741824,7680,16924,1107575336,4294959852,4294967294,16924,1107575336,4294959852,4294967294,16924,1107575336,4294959852,4294967294,
17088,17088,17280,17280,17472,17472,17664,17664,17664,17664,17664,17664,40960,44032,48128,51200,55296,58368,62464,65536,62464,65536,62464,65536,268439552,336991254,1,0,80,0,43008,1048526591,50465804,16780300,36,8064,224,1119092736,1119092736,1119092736,1098907648,1098907648,0,132264,1073741824,3240,44032,44032,40960,705408,1073741824,17280,17074,1107575480,4294948652,4294967294,17074,1107575480,4294948652,4294967294,17074,1107575480,4294948652,4294967294,
24768,24768,24960,24960,25152,25152,25344,25344,25344,25344,25344,25344,40960,44032,48128,51200,55296,58368,62464,65536,62464,65536,62464,65536,268439552,336991254,1,0,80,0,43008,1056914943,50465804,16780300,36,8064,224,1124204544,1124204544,1124204544,1098907648,1098907648,0,133704,1073741824,4680,44032,44032,40960,713088,1073741824,24960,17153,1107575556,4294939692,4294967294,17153,1107575556,4294939692,4294967294,17153,1107575556,4294939692,4294967294,
35136,35136,35328,35328,35520,35520,35712,35712,35712,35712,35712,35712,40960,44032,48128,51200,55296,58368,62464,65536,62464,65536,62464,65536,268439552,336991254,1,0,80,0,43008,1056915071,50465804,16780300,36,8064,224,1127743488,1127743488,1127743488,1098907648,1098907648,0,135648,1073741824,6624,44032,44032,40960,723456,1073741824,35328,17207,1107575610,4294927596,4294967294,17207,1107575610,4294927596,4294967294,17207,1107575610,4294927596,4294967294,
5806080,5806080,5849088,5849088,5892096,5892096,5935104,5935104,5935104,5935104,5935104,5935104,40960,44032,48128,51200,55296,58368,62464,65536,62464,65536,62464,65536,268439552,336991254,1,0,80,0,192,1056915199,16780300,50465804,8064,36,224,1124597760,1124597760,1124597760,1098907648,1098907648,0,1097280,1073741824,1096704,44032,44032,40960,5852160,1073741824,5849088,17159,1107575562,4294938348,4294967294,17159,1107575562,4294938348,4294967294,17159,1107575562,4294938348,4294967294,
8128512,8128512,8171520,8171520,8214528,8214528,8257536,8257536,8257536,8257536,8257536,8257536,40960,44032,48128,51200,55296,58368,62464,65536,62464,65536,62464,65536,268439552,336991254,1,0,80,0,192,1056915071,16780300,50465804,8064,36,224,1128136704,1128136704,1128136704,1098907648,1098907648,0,1532736,1073741824,1532160,44032,44032,40960,8174592,1073741824,8171520,17213,1107575616,4294926252,4294967294,17213,1107575616,4294926252,4294967294,17213,1107575616,4294926252,4294967294,
1591296,1634304,1677312,1720320,1763328,1806336,1849344,1892352,1892352,1892352,1892352,1892352,40960,45056,49152,53248,57344,61440,65536,69632,69632,69632,69632,69632,538976288,353703189,0,256,80,0,192,1040137983,16780300,50465804,8064,36,128,1109917696,1109393408,1108869120,1107296256,1107296256,1107296256,339840,323712,307584,57344,49152,40960,1812480,1726464,1640448,1106264596,1112031792,4294963660,30,1106264596,1112031792,4294963660,30,1106264596,1112031792,4294963660,30,
4257792,4257792,4300800,4300800,4343808,4343808,4386816,4386816,4386816,4386816,4386816,4386816,40960,44032,48128,51200,55296,58368,62464,65536,62464,65536,62464,65536,268439552,336991254,1,0,80,0,192,1040137983,16780300,50465804,8064,36,224,1120403456,1120403456,1120403456,1098907648,1098907648,0,806976,1073741824,806400,44032,44032,40960,4303872,1073741824,4300800,17094,1107575500,4294946412,4294967294,17094,1107575500,4294946412,4294967294,17094,1107575500,4294946412,4294967294,
3655680,3698688,3741696,3784704,3827712,3870720,3913728,3956736,3956736,3956736,3956736,3956736,40960,45056,49152,53248,57344,61440,65536,69632,69632,69632,69632,69632,538976288,353703189,0,256,80,0,192,1048526463,16780300,50465804,8064,36,128,1119092736,1118830592,1118568448,1107296256,1107296256,1107296256,726912,710784,694656,57344,49152,40960,3876864,3790848,3704832,1106264746,1112031928,4294957516,30,1106264746,1112031928,4294957516,30,1106264746,1112031928,4294957516,30,
4687872,4730880,4773888,4816896,4859904,4902912,4945920,4988928,4988928,4988928,4988928,4988928,40960,45056,49152,53248,57344,61440,65536,69632,69632,69632,69632,69632,538976288,353703189,0,256,80,0,192,1048526591,16780300,50465804,8064,36,128,1122238464,1121976320,1121714176,1107296256,1107296256,1107296256,920448,904320,888192,57344,49152,40960,4909056,4823040,4737024,1106264794,1112031976,4294954444,30,1106264794,1112031976,4294954444,30,1106264794,1112031976,4294954444,30,
25152,25152,25344,25344,25536,25536,25728,25728,25728,25728,25728,25728,40960,44032,48128,51200,55296,58368,62464,65536,62464,65536,62464,65536,268439552,336991254,1,0,80,0,43008,1056914943,50465804,16780300,36,8064,224,1124335616,1124335616,1124335616,1098907648,1098907648,0,133776,1073741824,4752,44032,44032,40960,713472,1073741824,25344,17155,1107575558,4294939244,4294967294,17155,1107575558,4294939244,4294967294,17155,1107575558,4294939244,4294967294,
6752256,6795264,6838272,6881280,6924288,6967296,7010304,7053312,7053312,7053312,7053312,7053312,40960,45056,49152,53248,57344,61440,65536,69632,69632,69632,69632,69632,538976288,353703189,0,256,80,0,192,1056915071,16780300,50465804,8064,36,128,1126301696,1126170624,1126039552,1107296256,1107296256,1107296256,1307520,1291392,1275264,57344,49152,40960,6973440,6887424,6801408,1106264861,1112032036,4294948300,30,1106264861,1112032036,4294948300,30,1106264861,1112032036,4294948300,30,
5892096,5892096,5935104,5935104,5978112,5978112,6021120,6021120,6021120,6021120,6021120,6021120,40960,44032,48128,51200,55296,58368,62464,65536,62464,65536,62464,65536,268439552,336991254,1,0,80,0,192,1056915199,16780300,50465804,8064,36,224,1124728832,1124728832,1124728832,1098907648,1098907648,0,1113408,1073741824,1112832,44032,44032,40960,5938176,1073741824,5935104,17161,1107575564,4294937900,4294967294,17161,1107575564,4294937900,4294967294,17161,1107575564,4294937900,4294967294,
8816640,8859648,8902656,8945664,8988672,9031680,9074688,9117696,9117696,9117696,9117696,9117696,40960,45056,49152,53248,57344,61440,65536,69632,69632,69632,69632,69632,538976288,353703189,0,256,80,0,192,1056915071,16780300,50465804,8064,36,128,1129447424,1129316352,1129185280,1107296256,1107296256,1107296256,1694592,1678464,1662336,57344,49152,40960,9037824,8951808,8865792,1106264909,1112032084,4294942156,30,1106264909,1112032084,4294942156,30,1106264909,1112032084,4294942156,30,
2193408,2193408,2236416,2236416,2279424,2279424,2322432,2322432,2322432,2322432,2322432,2322432,40960,44032,48128,51200,55296,58368,62464,65536,62464,65536,62464,65536,268439552,336991254,1,0,80,0,192,1040137983,16780300,50465804,8064,36,224,1112539136,1112539136,1112539136,1098907648,1098907648,0,419904,1073741824,419328,44032,44032,40960,2239488,1073741824,2236416,16972,1107575384,4294957164,4294967294,16972,1107575384,4294957164,4294967294,16972,1107575384,4294957164,4294967294,
2623488,2666496,2709504,2752512,2795520,2838528,2881536,2924544,2924544,2924544,2924544,2924544,40960,45056,49152,53248,57344,61440,65536,69632,69632,69632,69632,69632,538976288,353703189,0,256,80,0,192,1048526335,16780300,50465804,8064,36,128,1115947008,1115684864,1115160576,1107296256,1107296256,1107296256,533376,517248,501120,57344,49152,40960,2844672,2758656,2672640,1106264692,1112031880,4294960588,30,1106264692,1112031880,4294960588,30,1106264692,1112031880,4294960588,30,
7872,7872,8064,8064,8256,8256,8448,8448,8448,8448,8448,8448,40960,44032,48128,51200,55296,58368,62464,65536,62464,65536,62464,65536,268439552,336991254,1,0,80,0,43008,1048526463,50465804,16780300,36,8064,224,1109917696,1109917696,1109917696,1098907648,1098907648,0,130536,1073741824,1512,44032,44032,40960,696192,1073741824,8064,16932,1107575344,4294959404,4294967294,16932,1107575344,4294959404,4294967294,16932,1107575344,4294959404,4294967294,
17472,17472,17664,17664,17856,17856,18048,18048,18048,18048,18048,18048,40960,44032,48128,51200,55296,58368,62464,65536,62464,65536,62464,65536,268439552,336991254,1,0,80,0,43008,1048526591,50465804,16780300,36,8064,224,1119354880,1119354880,1119354880,1098907648,1098907648,0,132336,1073741824,3312,44032,44032,40960,705792,1073741824,17664,17078,1107575484,4294948204,4294967294,17078,1107575484,4294948204,4294967294,17078,1107575484,4294948204,4294967294,
5720064,5763072,5806080,5849088,5892096,5935104,5978112,6021120,6021120,6021120,6021120,6021120,40960,45056,49152,53248,57344,61440,65536,69632,69632,69632,69632,69632,538976288,353703189,0,256,80,0,192,1056914943,16780300,50465804,8064,36,128,1124728832,1124597760,1124466688,1107296256,1107296256,1107296256,1113984,1097856,1081728,57344,49152,40960,5941248,5855232,5769216,1106264837,1112032012,4294951372,30,1106264837,1112032012,4294951372,30,1106264837,1112032012,4294951372,30,
1032192,1075200,1118208,1161216,1204224,1247232,5720064,5763072,5763072,5763072,5763072,5763072,49152,53248,57344,61440,65536,69632,40960,45056,45056,45056,45056,45056,2694840320,320017685,1,0,80,0,192,1056915071,16780300,50465804,8064,36,128,1124335616,1124335616,1104150528,1126170624,1126170624,0,1070208,1073741824,209664,32768,32768,53248,5707776,1073741824,1118208,1126122230,1127236358,4294950604,158,1126122230,1127236358,4294950604,158,16832,1099973096,4294965772,4294967294,
7784448,7827456,7870464,7913472,7956480,7999488,8042496,8085504,8085504,8085504,8085504,8085504,40960,45056,49152,53248,57344,61440,65536,69632,69632,69632,69632,69632,538976288,353703189,0,256,80,0,192,1056915199,16780300,50465804,8064,36,128,1127874560,1127743488,1127612416,1107296256,1107296256,1107296256,1501056,1484928,1468800,57344,49152,40960,8005632,7919616,7833600,1106264885,1112032060,4294945228,30,1106264885,1112032060,4294945228,30,1106264885,1112032060,4294945228,30,
8214528,8214528,8257536,8257536,8300544,8300544,8343552,8343552,8343552,8343552,8343552,8343552,40960,44032,48128,51200,55296,58368,62464,65536,62464,65536,62464,65536,268439552,336991254,1,0,80,0,192,1056915071,16780300,50465804,8064,36,224,1128267776,1128267776,1128267776,1098907648,1098907648,0,1548864,1073741824,1548288,44032,44032,40960,8260608,1073741824,8257536,17215,1107575618,4294925804,4294967294,17215,1107575618,4294925804,4294967294,17215,1107575618,4294925804,4294967294,
2279424,2279424,2322432,2322432,2365440,2365440,2408448,2408448,2408448,2408448,2408448,2408448,40960,44032,48128,51200,55296,58368,62464,65536,62464,65536,62464,65536,268439552,336991254,1,0,80,0,192,1040137983,16780300,50465804,8064,36,224,1113063424,1113063424,1113063424,1098907648,1098907648,0,436032,1073741824,435456,44032,44032,40960,2325504,1073741824,2322432,16980,1107575392,4294956716,4294967294,16980,1107575392,4294956716,4294967294,16980,1107575392,4294956716,4294967294,
4343808,4343808,4386816,4386816,4429824,4429824,4472832,4472832,4472832,4472832,4472832,4472832,40960,44032,48128,51200,55296,58368,62464,65536,62464,65536,62464,65536,268439552,336991254,1,0,80,0,192,1048526335,16780300,50465804,8064,36,224,1120665600,1120665600,1120665600,1098907648,1098907648,0,823104,1073741824,822528,44032,44032,40960,4389888,1073741824,4386816,17098,1107575504,4294945964,4294967294,17098,1107575504,4294945964,4294967294,17098,1107575504,4294945964,4294967294,
8256,8256,8448,8448,8640,8640,8832,8832,8832,8832,8832,8832,40960,44032,48128,51200,55296,58368,62464,65536,62464,65536,62464,65536,268439552,336991254,1,0,80,0,43008,1048526463,50465804,16780300,36,8064,224,1110441984,1110441984,1110441984,1098907648,1098907648,0,130608,1073741824,1584,44032,44032,40960,696576,1073741824,8448,16940,1107575352,4294958956,4294967294,16940,1107575352,4294958956,4294967294,16940,1107575352,4294958956,4294967294,
17856,17856,18048,18048,18240,18240,18432,18432,18432,18432,18432,18432,40960,44032,48128,51200,55296,58368,62464,65536,62464,65536,62464,65536,268439552,336991254,1,0,80,0,43008,1048526591,50465804,16780300,36,8064,224,1119617024,1119617024,1119617024,1098907648,1098907648,0,132408,1073741824,3384,44032,44032,40960,706176,1073741824,18048,17082,1107575488,4294947756,4294967294,17082,1107575488,4294947756,4294967294,17082,1107575488,4294947756,4294967294,
25536,25536,25728,25728,25920,25920,26112,26112,26112,26112,26112,26112,40960,44032,48128,51200,55296,58368,62464,65536,62464,65536,62464,65536,268439552,336991254,1,0,80,0,43008,1056915071,50465804,16780300,36,8064,224,1124466688,1124466688,1124466688,1098907648,1098907648,0,133848,1073741824,4824,44032,44032,40960,713856,1073741824,25728,17157,1107575560,4294938796,4294967294,17157,1107575560,4294938796,4294967294,17157,1107575560,4294938796,4294967294,
35520,35520,35712,35712,35904,35904,36096,36096,36096,36096,36096,36096,40960,44032,48128,51200,55296,58368,62464,65536,62464,65536,62464,65536,268439552,336991254,1,0,80,0,43008,1056915199,50465804,16780300,36,8064,224,1127874560,1127874560,1127874560,1098907648,1098907648,0,135720,1073741824,6696,44032,44032,40960,723840,1073741824,35712,17209,1107575612,4294927148,4294967294,17209,1107575612,4294927148,4294967294,17209,1107575612,4294927148,4294967294,
5978112,5978112,6021120,6021120,6064128,6064128,6107136,6107136,6107136,6107136,6107136,6107136,40960,44032,48128,51200,55296,58368,62464,65536,62464,65536,62464,65536,268439552,336991254,1,0,80,0,192,1056915199,16780300,50465804,8064,36,224,1124859904,1124859904,1124859904,1098907648,1098907648,0,1129536,1073741824,1128960,44032,44032,40960,6024192,1073741824,6021120,17163,1107575566,4294937452,4294967294,17163,1107575566,4294937452,4294967294,17163,1107575566,4294937452,4294967294,
8300544,8300544,8343552,8343552,8386560,8386560,8429568,8429568,8429568,8429568,8429568,8429568,40960,44032,48128,51200,55296,58368,62464,65536,62464,65536,62464,65536,268439552,336991254,1,0,80,0,192,1056915071,16780300,50465804,8064,36,224,1128398848,1128398848,1128398848,1098907648,1098907648,0,1564992,1073741824,1564416,44032,44032,40960,8346624,1073741824,8343552,17217,1107575620,4294925356,4294967294,17217,1107575620,4294925356,4294967294,17217,1107575620,4294925356,4294967294,
2365440,2365440,2408448,2408448,2451456,2451456,2494464,2494464,2494464,2494464,2494464,2494464,40960,44032,48128,51200,55296,58368,62464,65536,62464,65536,62464,65536,268439552,336991254,1,0,80,0,192,1040137983,16780300,50465804,8064,36,224,1113587712,1113587712,1113587712,1098907648,1098907648,0,452160,1073741824,451584,44032,44032,40960,2411520,1073741824,2408448,16988,1107575400,4294956268,4294967294,16988,1107575400,4294956268,4294967294,16988,1107575400,4294956268,4294967294,
4429824,4429824,4472832,4472832,4515840,4515840,4558848,4558848,4558848,4558848,4558848,4558848,40960,44032,48128,51200,55296,58368,62464,65536,62464,65536,62464,65536,268439552,336991254,1,0,80,0,192,1048526335,16780300,50465804,8064,36,224,1120927744,1120927744,1120927744,1098907648,1098907648,0,839232,1073741824,838656,44032,44032,40960,4475904,1073741824,4472832,17102,1107575508,4294945516,4294967294,17102,1107575508,4294945516,4294967294,17102,1107575508,4294945516,4294967294,
8640,8640,8832,8832,9024,9024,9216,9216,9216,9216,9216,9216,40960,44032,48128,51200,55296,58368,62464,65536,62464,65536,62464,65536,268439552,336991254,1,0,80,0,43008,1048526463,50465804,16780300,36,8064,224,1110966272,1110966272,1110966272,1098907648,1098907648,0,130680,1073741824,1656,44032,44032,40960,696960,1073741824,8832,16948,1107575360,4294958508,4294967294,16948,1107575360,4294958508,4294967294,16948,1107575360,4294958508,4294967294,
18240,18240,18432,18432,18624,18624,18816,18816,18816,18816,18816,18816,40960,44032,48128,51200,55296,58368,62464,65536,62464,65536,62464,65536,268439552,336991254,1,0,80,0,43008,1048526591,50465804,16780300,36,8064,224,1119879168,1119879168,1119879168,1098907648,1098907648,0,132480,1073741824,3456,44032,44032,40960,706560,1073741824,18432,17086,1107575492,4294947308,4294967294,17086,1107575492,4294947308,4294967294,17086,1107575492,4294947308,4294967294,
25920,25920,26112,26112,26304,26304,26496,26496,26496,26496,26496,26496,40960,44032,48128,51200,55296,58368,62464,65536,62464,65536,62464,65536,268439552,336991254,1,0,80,0,43008,1056915071,50465804,16780300,36,8064,224,1124597760,1124597760,1124597760,1098907648,1098907648,0,133920,1073741824,4896,44032,44032,40960,714240,1073741824,26112,17159,1107575562,4294938348,4294967294,17159,1107575562,4294938348,4294967294,17159,1107575562,4294938348,4294967294,
35904,35904,36096,36096,36288,36288,36480,36480,36480,36480,36480,36480,40960,44032,48128,51200,55296,58368,62464,65536,62464,65536,62464,65536,268439552,336991254,1,0,80,0,43008,1056915199,50465804,16780300,36,8064,224,1128005632,1128005632,1128005632,1098907648,1098907648,0,135792,1073741824,6768,44032,44032,40960,724224,1073741824,36096,17211,1107575614,4294926700,4294967294,17211,1107575614,4294926700,4294967294,17211,1107575614,4294926700,4294967294,
6064128,6064128,6107136,6107136,6150144,6150144,6193152,6193152,6193152,6193152,6193152,6193152,40960,44032,48128,51200,55296,58368,62464,65536,62464,65536,62464,65536,268439552,336991254,1,0,80,0,192,1056915199,16780300,50465804,8064,36,224,1124990976,1124990976,1124990976,1098907648,1098907648,0,1145664,1073741824,1145088,44032,44032,40960,6110208,1073741824,6107136,17165,1107575568,4294937004,4294967294,17165,1107575568,4294937004,4294967294,17165,1107575568,4294937004,4294967294,
8386560,8386560,8429568,8429568,8472576,8472576,8515584,8515584,8515584,8515584,8515584,8515584,40960,44032,48128,51200,55296,58368,62464,65536,62464,65536,62464,65536,268439552,336991254,1,0,80,0,192,1056915071,16780300,50465804,8064,36,224,1128529920,1128529920,1128529920,1098907648,1098907648,0,1581120,1073741824,1580544,44032,44032,40960,8432640,1073741824,8429568,17219,1107575622,4294924908,4294967294,17219,1107575622,4294924908,4294967294,17219,1107575622,4294924908,4294967294,
2451456,2451456,2494464,2494464,2537472,2537472,2580480,2580480,2580480,2580480,2580480,2580480,40960,44032,48128,51200,55296,58368,62464,65536,62464,65536,62464,65536,268439552,336991254,1,0,80,0,192,1040137983,16780300,50465804,8064,36,224,1114112000,1114112000,1114112000,1098907648,1098907648,0,468288,1073741824,467712,44032,44032,40960,2497536,1073741824,2494464,16996,1107575408,4294955820,4294967294,16996,1107575408,4294955820,4294967294,16996,1107575408,4294955820,4294967294,
4515840,4515840,4558848,4558848,4601856,4601856,4644864,4644864,4644864,4644864,4644864,4644864,40960,44032,48128,51200,55296,58368,62464,65536,62464,65536,62464,65536,268439552,336991254,1,0,80,0,192,1048526335,16780300,50465804,8064,36,224,1121189888,1121189888,1121189888,1098907648,1098907648,0,855360,1073741824,854784,44032,44032,40960,4561920,1073741824,4558848,17106,1107575512,4294945068,4294967294,17106,1107575512,4294945068,4294967294,17106,1107575512,4294945068,4294967294,
516096,559104,602112,645120,688128,731136,5720064,5763072,5763072,5763072,5763072,5763072,49152,53248,57344,61440,65536,69632,40960,45056,45056,45056,45056,45056,1616904192,320017685,1,512,80,0,192,1048526463,16780300,50465804,8064,36,128,1124335616,1124335616,1096810496,1119879168,1119879168,0,1067904,1073741824,112896,32768,32768,53248,5695488,1073741824,602112,1119765238,1121993478,4294950988,94,1119765238,1121993478,4294950988,94,16704,1099973000,12,4294967294,
18624,18624,18816,18816,19008,19008,19200,19200,19200,19200,19200,19200,40960,44032,48128,51200,55296,58368,62464,65536,62464,65536,62464,65536,268439552,336991254,1,0,80,0,43008,1048526591,50465804,16780300,36,8064,224,1120141312,1120141312,1120141312,1098907648,1098907648,0,132552,1073741824,3528,44032,44032,40960,706944,1073741824,18816,17090,1107575496,4294946860,4294967294,17090,1107575496,4294946860,4294967294,17090,1107575496,4294946860,4294967294,
26304,26304,26496,26496,26688,26688,26880,26880,26880,26880,26880,26880,40960,44032,48128,51200,55296,58368,62464,65536,62464,65536,62464,65536,268439552,336991254,1,0,80,0,43008,1056915071,50465804,16780300,36,8064,224,1124728832,1124728832,1124728832,1098907648,1098907648,0,133992,1073741824,4968,44032,44032,40960,714624,1073741824,26496,17161,1107575564,4294937900,4294967294,17161,1107575564,4294937900,4294967294,17161,1107575564,4294937900,4294967294,
36288,36288,36480,36480,36672,36672,36864,36864,36864,36864,36864,36864,40960,44032,48128,51200,55296,58368,62464,65536,62464,65536,62464,65536,268439552,336991254,1,0,80,0,43008,1056915199,50465804,16780300,36,8064,224,1128136704,1128136704,1128136704,1098907648,1098907648,0,135864,1073741824,6840,44032,44032,40960,724608,1073741824,36480,17213,1107575616,4294926252,4294967294,17213,1107575616,4294926252,4294967294,17213,1107575616,4294926252,4294967294,
6150144,6150144,6193152,6193152,6236160,6236160,6279168,6279168,6279168,6279168,6279168,6279168,40960,44032,48128,51200,55296,58368,62464,65536,62464,65536,62464,65536,268439552,336991254,1,0,80,0,192,1056914943,16780300,50465804,8064,36,224,1125122048,1125122048,1125122048,1098907648,1098907648,0,1161792,1073741824,1161216,44032,44032,40960,6196224,1073741824,6193152,17167,1107575570,4294936556,4294967294,17167,1107575570,4294936556,4294967294,17167,1107575570,4294936556,4294967294,
8472576,8472576,8515584,8515584,8558592,8558592,8601600,8601600,8601600,8601600,8601600,8601600,40960,44032,48128,51200,55296,58368,62464,65536,62464,65536,62464,65536,268439552,336991254,1,0,80,0,192,1056915071,16780300,50465804,8064,36,224,1128660992,1128660992,1128660992,1098907648,1098907648,0,1597248,1073741824,1596672,44032,44032,40960,8518656,1073741824,8515584,17221,1107575624,4294924460,4294967294,17221,1107575624,4294924460,4294967294,17221,1107575624,4294924460,4294967294,
2537472,2537472,2580480,2580480,2623488,2623488,2666496,2666496,2666496,2666496,2666496,2666496,40960,44032,48128,51200,55296,58368,62464,65536,62464,65536,62464,65536,268439552,336991254,1,0,80,0,192,1040137983,16780300,50465804,8064,36,224,1114636288,1114636288,1114636288,1098907648,1098907648,0,484416,1073741824,483840,44032,44032,40960,2583552,1073741824,2580480,17004,1107575416,4294955372,4294967294,17004,1107575416,4294955372,4294967294,17004,1107575416,4294955372,4294967294,
4601856,4601856,4644864,4644864,4687872,4687872,4730880,4730880,4730880,4730880,4730880,4730880,40960,44032,48128,51200,55296,58368,62464,65536,62464,65536,62464,65536,268439552,336991254,1,0,80,0,192,1048526463,16780300,50465804,8064,36,224,1121452032,1121452032,1121452032,1098907648,1098907648,0,871488,1073741824,870912,44032,44032,40960,4647936,1073741824,4644864,17110,1107575516,4294944620,4294967294,17110,1107575516,4294944620,4294967294,17110,1107575516,4294944620,4294967294,
9024,9024,9216,9216,9408,9408,9600,9600,9600,9600,9600,9600,40960,44032,48128,51200,55296,58368,62464,65536,62464,65536,62464,65536,268439552,336991254,1,0,80,0,43008,1048526591,50465804,16780300,36,8064,224,1111490560,1111490560,1111490560,1098907648,1098907648,0,130752,1073741824,1728,44032,44032,40960,697344,1073741824,9216,16956,1107575368,4294958060,4294967294,16956,1107575368,4294958060,4294967294,16956,1107575368,4294958060,4294967294,
19008,19008,19200,19200,19392,19392,19584,19584,19584,19584,19584,19584,40960,44032,48128,51200,55296,58368,62464,65536,62464,65536,62464,65536,268439552,336991254,1,0,80,0,43008,1048526591,50465804,16780300,36,8064,224,1120403456,1120403456,1120403456,1098907648,1098907648,0,132624,1073741824,3600,44032,44032,40960,707328,1073741824,19200,17094,1107575500,4294946412,4294967294,17094,1107575500,4294946412,4294967294,17094,1107575500,4294946412,4294967294,
26688,26688,26880,26880,27072,27072,27264,27264,27264,27264,27264,27264,40960,44032,48128,51200,55296,58368,62464,65536,62464,65536,62464,65536,268439552,336991254,1,0,80,0,43008,1056915071,50465804,16780300,36,8064,224,1124859904,1124859904,1124859904,1098907648,1098907648,0,134064,1073741824,5040,44032,44032,40960,715008,1073741824,26880,17163,1107575566,4294937452,4294967294,17163,1107575566,4294937452,4294967294,17163,1107575566,4294937452,4294967294,
36672,36672,36864,36864,37056,37056,37248,37248,37248,37248,37248,37248,40960,44032,48128,51200,55296,58368,62464,65536,62464,65536,62464,65536,268439552,336991254,1,0,80,0,43008,1056915199,50465804,16780300,36,8064,224,1128267776,1128267776,1128267776,1098907648,1098907648,0,135936,1073741824,6912,44032,44032,40960,724992,1073741824,36864,17215,1107575618,4294925804,4294967294,17215,1107575618,4294925804,4294967294,17215,1107575618,4294925804,4294967294,
6236160,6236160,6279168,6279168,6322176,6322176,6365184,6365184,6365184,6365184,6365184,6365184,40960,44032,48128,51200,55296,58368,62464,65536,62464,65536,62464,65536,268439552,336991254,1,0,80,0,192,1056914943,16780300,50465804,8064,36,224,1125253120,1125253120,1125253120,1098907648,1098907648,0,1177920,1073741824,1177344,44032,44032,40960,6282240,1073741824,6279168,17169,1107575572,4294936108,4294967294,17169,1107575572,4294936108,4294967294,17169,1107575572,4294936108,4294967294,
8558592,8558592,8601600,8601600,8644608,8644608,8687616,8687616,8687616,8687616,8687616,8687616,40960,44032,48128,51200,55296,58368,62464,65536,62464,65536,62464,65536,268439552,336991254,1,0,80,0,192,1056915071,16780300,50465804,8064,36,224,1128792064,1128792064,1128792064,1098907648,1098907648,0,1613376,1073741824,1612800,44032,44032,40960,8604672,1073741824,8601600,17223,1107575626,4294924012,4294967294,17223,1107575626,4294924012,4294967294,17223,1107575626,4294924012,4294967294,
86016,129024,172032,215040,258048,301056,8558592,8601600,8601600,8601600,8601600,8601600,49152,53248,57344,61440,65536,69632,40960,45056,45056,45056,45056,45056,808452096,320017685,1,512,80,0,192,1040137983,16780300,50465804,8064,36,128,1128660992,1128660992,1082130432,1111490560,1111490560,0,1598400,1073741824,32256,32768,32768,53248,8524800,1073741824,172032,1111245629,1115702088,4294942828,46,1111245629,1115702088,4294942828,46,16384,1099972832,1292,4294967294,
4687872,4687872,4730880,4730880,4773888,4773888,4816896,4816896,4816896,4816896,4816896,4816896,40960,44032,48128,51200,55296,58368,62464,65536,62464,65536,62464,65536,268439552,336991254,1,0,80,0,192,1048526463,16780300,50465804,8064,36,224,1121714176,1121714176,1121714176,1098907648,1098907648,0,887616,1073741824,887040,44032,44032,40960,4733952,1073741824,4730880,17114,1107575520,4294944172,4294967294,17114,1107575520,4294944172,4294967294,17114,1107575520,4294944172,4294967294,
9408,9408,9600,9600,9792,9792,9984,9984,9984,9984,9984,9984,40960,44032,48128,51200,55296,58368,62464,65536,62464,65536,62464,65536,268439552,336991254,1,0,80,0,43008,1048526591,50465804,16780300,36,8064,224,1112014848,1112014848,1112014848,1098907648,1098907648,0,130824,1073741824,1800,44032,44032,40960,697728,1073741824,9600,16964,1107575376,4294957612,4294967294,16964,1107575376,4294957612,4294967294,16964,1107575376,4294957612,4294967294,
19392,19392,19584,19584,19776,19776,19968,19968,19968,19968,19968,19968,40960,44032,48128,51200,55296,58368,62464,65536,62464,65536,62464,65536,268439552,336991254,1,0,80,0,43008,1048526591,50465804,16780300,36,8064,224,1120665600,1120665600,1120665600,1098907648,1098907648,0,132696,1073741824,3672,44032,44032,40960,707712,1073741824,19584,17098,1107575504,4294945964,4294967294,17098,1107575504,4294945964,4294967294,17098,1107575504,4294945964,4294967294,
27072,27072,27264,27264,27456,27456,27648,27648,27648,27648,27648,27648,40960,44032,48128,51200,55296,58368,62464,65536,62464,65536,62464,65536,268439552,336991254,1,0,80,0,43008,1056915071,50465804,16780300,36,8064,224,1124990976,1124990976,1124990976,1098907648,1098907648,0,134136,1073741824,5112,44032,44032,40960,715392,1073741824,27264,17165,1107575568,4294937004,4294967294,17165,1107575568,4294937004,4294967294,17165,1107575568,4294937004,4294967294,
37056,37056,37248,37248,37440,37440,37632,37632,37632,37632,37632,37632,40960,44032,48128,51200,55296,58368,62464,65536,62464,65536,62464,65536,268439552,336991254,1,0,80,0,43008,1056915199,50465804,16780300,36,8064,224,1128398848,1128398848,1128398848,1098907648,1098907648,0,136008,1073741824,6984,44032,44032,40960,725376,1073741824,37248,17217,1107575620,4294925356,4294967294,17217,1107575620,4294925356,4294967294,17217,1107575620,4294925356,4294967294,
6322176,6322176,6365184,6365184,6408192,6408192,6451200,6451200,6451200,6451200,6451200,6451200,40960,44032,48128,51200,55296,58368,62464,65536,62464,65536,62464,65536,268439552,336991254,1,0,80,0,192,1056914943,16780300,50465804,8064,36,224,1125384192,1125384192,1125384192,1098907648,1098907648,0,1194048,1073741824,1193472,44032,44032,40960,6368256,1073741824,6365184,17171,1107575574,4294935660,4294967294,17171,1107575574,4294935660,4294967294,17171,1107575574,4294935660,4294967294,
8644608,8644608,8687616,8687616,8730624,8730624,8773632,8773632,8773632,8773632,8773632,8773632,40960,44032,48128,51200,55296,58368,62464,65536,62464,65536,62464,65536,268439552,336991254,1,0,80,0,192,1056915071,16780300,50465804,8064,36,224,1128923136,1128923136,1128923136,1098907648,1098907648,0,1629504,1073741824,1628928,44032,44032,40960,8690688,1073741824,8687616,17225,1107575628,4294923564,4294967294,17225,1107575628,4294923564,4294967294,17225,1107575628,4294923564,4294967294,
2623488,2623488,2666496,2666496,2709504,2709504,2752512,2752512,2752512,2752512,2752512,2752512,40960,44032,48128,51200,55296,58368,62464,65536,62464,65536,62464,65536,268439552,336991254,1,0,80,0,192,1040137983,16780300,50465804,8064,36,224,1115160576,1115160576,1115160576,1098907648,1098907648,0,500544,1073741824,499968,44032,44032,40960,2669568,1073741824,2666496,17012,1107575424,4294954924,4294967294,17012,1107575424,4294954924,4294967294,17012,1107575424,4294954924,4294967294,
4773888,4773888,4816896,4816896,4859904,4859904,4902912,4902912,4902912,4902912,4902912,4902912,40960,44032,48128,51200,55296,58368,62464,65536,62464,65536,62464,65536,268439552,336991254,1,0,80,0,192,1048526463,16780300,50465804,8064,36,224,1121976320,1121976320,1121976320,1098907648,1098907648,0,903744,1073741824,903168,44032,44032,40960,4819968,1073741824,4816896,17118,1107575524,4294943724,4294967294,17118,1107575524,4294943724,4294967294,17118,1107575524,4294943724,4294967294,
9792,9792,9984,9984,10176,10176,10368,10368,10368,10368,10368,10368,40960,44032,48128,51200,55296,58368,62464,65536,62464,65536,62464,65536,268439552,336991254,1,0,80,0,43008,1048526591,50465804,16780300,36,8064,224,1112539136,1112539136,1112539136,1098907648,1098907648,0,130896,1073741824,1872,44032,44032,40960,698112,1073741824,9984,16972,1107575384,4294957164,4294967294,16972,1107575384,4294957164,4294967294,16972,1107575384,4294957164,4294967294,
19776,19776,19968,19968,20160,20160,20352,20352,20352,20352,20352,20352,40960,44032,48128,51200,55296,58368,62464,65536,62464,65536,62464,65536,268439552,336991254,1,0,80,0,43008,1048526335,50465804,16780300,36,8064,224,1120927744,1120927744,1120927744,1098907648,1098907648,0,132768,1073741824,3744,44032,44032,40960,708096,1073741824,19968,17102,1107575508,4294945516,4294967294,17102,1107575508,4294945516,4294967294,17102,1107575508,4294945516,4294967294,
27456,27456,27648,27648,27840,27840,28032,28032,28032,28032,28032,28032,40960,44032,48128,51200,55296,58368,62464,65536,62464,65536,62464,65536,268439552,336991254,1,0,80,0,43008,1056915071,50465804,16780300,36,8064,224,1125122048,1125122048,1125122048,1098907648,1098907648,0,134208,1073741824,5184,44032,44032,40960,715776,1073741824,27648,17167,1107575570,4294936556,4294967294,17167,1107575570,4294936556,4294967294,17167,1107575570,4294936556,4294967294,
37440,37440,37632,37632,37824,37824,38016,38016,38016,38016,38016,38016,40960,44032,48128,51200,55296,58368,62464,65536,62464,65536,62464,65536,268439552,336991254,1,0,80,0,43008,1056915199,50465804,16780300,36,8064,224,1128529920,1128529920,1128529920,1098907648,1098907648,0,136080,1073741824,7056,44032,44032,40960,725760,1073741824,37632,17219,1107575622,4294924908,4294967294,17219,1107575622,4294924908,4294967294,17219,1107575622,4294924908,4294967294,
6408192,6408192,6451200,6451200,6494208,6494208,6537216,6537216,6537216,6537216,6537216,6537216,40960,44032,48128,51200,55296,58368,62464,65536,62464,65536,62464,65536,268439552,336991254,1,0,80,0,192,1056914943,16780300,50465804,8064,36,224,1125515264,1125515264,1125515264,1098907648,1098907648,0,1210176,1073741824,1209600,44032,44032,40960,6454272,1073741824,6451200,17173,1107575576,4294935212,4294967294,17173,1107575576,4294935212,4294967294,17173,1107575576,4294935212,4294967294,
9074688,9117696,9160704,9203712,9246720,9289728,9332736,9375744,9375744,9375744,9375744,9375744,40960,45056,49152,53248,57344,61440,65536,69632,69632,69632,69632,69632,538976288,353703189,0,256,80,0,192,1056915199,16780300,50465804,8064,36,128,1129840640,1129709568,1129578496,1107296256,1107296256,1107296256,1742976,1726848,1710720,57344,49152,40960,9295872,9209856,9123840,1106264915,1112032090,4294941388,30,1106264915,1112032090,4294941388,30,1106264915,1112032090,4294941388,30,
1849344,1892352,1935360,1978368,2021376,2064384,2107392,2150400,2150400,2150400,2150400,2150400,40960,45056,49152,53248,57344,61440,65536,69632,69632,69632,69632,69632,538976288,353703189,0,256,80,0,192,1040137983,16780300,50465804,8064,36,128,1111490560,1110966272,1110441984,1107296256,1107296256,1107296256,388224,372096,355968,57344,49152,40960,2070528,1984512,1898496,1106264620,1112031816,4294962892,30,1106264620,1112031816,4294962892,30,1106264620,1112031816,4294962892,30,
4859904,4859904,4902912,4902912,4945920,4945920,4988928,4988928,4988928,4988928,4988928,4988928,40960,44032,48128,51200,55296,58368,62464,65536,62464,65536,62464,65536,268439552,336991254,1,0,80,0,192,1048526463,16780300,50465804,8064,36,224,1122238464,1122238464,1122238464,1098907648,1098907648,0,919872,1073741824,919296,44032,44032,40960,4905984,1073741824,4902912,17122,1107575528,4294943276,4294967294,17122,1107575528,4294943276,4294967294,17122,1107575528,4294943276,4294967294,
3913728,3956736,3999744,4042752,4085760,4128768,4171776,4214784,4214784,4214784,4214784,4214784,40960,45056,49152,53248,57344,61440,65536,69632,69632,69632,69632,69632,538976288,353703189,0,256,80,0,192,1048526591,16780300,50465804,8064,36,128,1119879168,1119617024,1119354880,1107296256,1107296256,1107296256,775296,759168,743040,57344,49152,40960,4134912,4048896,3962880,1106264758,1112031940,4294956748,30,1106264758,1112031940,4294956748,30,1106264758,1112031940,4294956748,30,
4945920,4988928,5031936,5074944,5117952,5160960,5203968,5246976,5246976,5246976,5246976,5246976,40960,45056,49152,53248,57344,61440,65536,69632,69632,69632,69632,69632,538976288,353703189,0,256,80,0,192,1048526335,16780300,50465804,8064,36,128,1123024896,1122762752,1122500608,1107296256,1107296256,1107296256,968832,952704,936576,57344,49152,40960,5167104,5081088,4995072,1106264806,1112031988,4294953676,30,1106264806,1112031988,4294953676,30,1106264806,1112031988,4294953676,30,
27840,27840,28032,28032,28224,28224,28416,28416,28416,28416,28416,28416,40960,44032,48128,51200,55296,58368,62464,65536,62464,65536,62464,65536,268439552,336991254,1,0,80,0,43008,1056915071,50465804,16780300,36,8064,224,1125253120,1125253120,1125253120,1098907648,1098907648,0,134280,1073741824,5256,44032,44032,40960,716160,1073741824,28032,17169,1107575572,4294936108,4294967294,17169,1107575572,4294936108,4294967294,17169,1107575572,4294936108,4294967294,
7010304,7053312,7096320,7139328,7182336,7225344,7268352,7311360,7311360,7311360,7311360,7311360,40960,45056,49152,53248,57344,61440,65536,69632,69632,69632,69632,69632,538976288,353703189,0,256,80,0,192,1056915199,16780300,50465804,8064,36,128,1126694912,1126563840,1126432768,1107296256,1107296256,1107296256,1355904,1339776,1323648,57344,49152,40960,7231488,7145472,7059456,1106264867,1112032042,4294947532,30,1106264867,1112032042,4294947532,30,1106264867,1112032042,4294947532,30,
6494208,6494208,6537216,6537216,6580224,6580224,6623232,6623232,6623232,6623232,6623232,6623232,40960,44032,48128,51200,55296,58368,62464,65536,62464,65536,62464,65536,268439552,336991254,1,0,80,0,192,1056915071,16780300,50465804,8064,36,224,1125646336,1125646336,1125646336,1098907648,1098907648,0,1226304,1073741824,1225728,44032,44032,40960,6540288,1073741824,6537216,17175,1107575578,4294934764,4294967294,17175,1107575578,4294934764,4294967294,17175,1107575578,4294934764,4294967294,
8730624,8730624,8773632,8773632,8816640,8816640,8859648,8859648,8859648,8859648,8859648,8859648,40960,44032,48128,51200,55296,58368,62464,65536,62464,65536,62464,65536,268439552,336991254,1,0,80,0,192,1056915199,16780300,50465804,8064,36,224,1129054208,1129054208,1129054208,1098907648,1098907648,0,1645632,1073741824,1645056,44032,44032,40960,8776704,1073741824,8773632,17227,1107575630,4294923116,4294967294,17227,1107575630,4294923116,4294967294,17227,1107575630,4294923116,4294967294,
2709504,2709504,2752512,2752512,2795520,2795520,2838528,2838528,2838528,2838528,2838528,2838528,40960,44032,48128,51200,55296,58368,62464,65536,62464,65536,62464,65536,268439552,336991254,1,0,80,0,192,1040137983,16780300,50465804,8064,36,224,1115684864,1115684864,1115684864,1098907648,1098907648,0,516672,1073741824,516096,44032,44032,40960,2755584,1073741824,2752512,17020,1107575428,4294954476,4294967294,17020,1107575428,4294954476,4294967294,17020,1107575428,4294954476,4294967294,
2881536,2924544,2967552,3010560,3053568,3096576,3139584,3182592,3182592,3182592,3182592,3182592,40960,45056,49152,53248,57344,61440,65536,69632,69632,69632,69632,69632,538976288,353703189,0,256,80,0,192,1048526463,16780300,50465804,8064,36,128,1116733440,1116471296,1116209152,1107296256,1107296256,1107296256,581760,565632,549504,57344,49152,40960,3102720,3016704,2930688,1106264710,1112031892,4294959820,30,1106264710,1112031892,4294959820,30,1106264710,1112031892,4294959820,30,
10176,10176,10368,10368,10560,10560,10752,10752,10752,10752,10752,10752,40960,44032,48128,51200,55296,58368,62464,65536,62464,65536,62464,65536,268439552,336991254,1,0,80,0,43008,1048526591,50465804,16780300,36,8064,224,1113063424,1113063424,1113063424,1098907648,1098907648,0,130968,1073741824,1944,44032,44032,40960,698496,1073741824,10368,16980,1107575392,4294956716,4294967294,16980,1107575392,4294956716,4294967294,16980,1107575392,4294956716,4294967294,
20160,20160,20352,20352,20544,20544,20736,20736,20736,20736,20736,20736,40960,44032,48128,51200,55296,58368,62464,65536,62464,65536,62464,65536,268439552,336991254,1,0,80,0,43008,1048526335,50465804,16780300,36,8064,224,1121189888,1121189888,1121189888,1098907648,1098907648,0,132840,1073741824,3816,44032,44032,40960,708480,1073741824,20352,17106,1107575512,4294945068,4294967294,17106,1107575512,4294945068,4294967294,17106,1107575512,4294945068,4294967294,
5978112,6021120,6064128,6107136,6150144,6193152,6236160,6279168,6279168,6279168,6279168,6279168,40960,45056,49152,53248,57344,61440,65536,69632,69632,69632,69632,69632,538976288,353703189,0,256,80,0,192,1056915071,16780300,50465804,8064,36,128,1125122048,1124990976,1124859904,1107296256,1107296256,1107296256,1162368,1146240,1130112,57344,49152,40960,6199296,6113280,6027264,1106264843,1112032018,4294950604,30,1106264843,1112032018,4294950604,30,1106264843,1112032018,4294950604,30,
1118208,1161216,1204224,1247232,1290240,1333248,8558592,8601600,8601600,8601600,8601600,8601600,49152,53248,57344,61440,65536,69632,40960,45056,45056,45056,45056,45056,2694840320,320017685,1,0,80,0,192,1056915199,16780300,50465804,8064,36,128,1128660992,1128660992,1105199104,1126170624,1126170624,0,1602432,1073741824,225792,32768,32768,53248,8546304,1073741824,1204224,1126122301,1127236424,4294942156,158,1126122301,1127236424,4294942156,158,16848,1099973112,4294965516,4294967294,
8042496,8085504,8128512,8171520,8214528,8257536,8300544,8343552,8343552,8343552,8343552,8343552,40960,45056,49152,53248,57344,61440,65536,69632,69632,69632,69632,69632,538976288,353703189,0,256,80,0,192,1056915071,16780300,50465804,8064,36,128,1128267776,1128136704,1128005632,1107296256,1107296256,1107296256,1549440,1533312,1517184,57344,49152,40960,8263680,8177664,8091648,1106264891,1112032066,4294944460,30,1106264891,1112032066,4294944460,30,1106264891,1112032066,4294944460,30,
8816640,8816640,8859648,8859648,8902656,8902656,8945664,8945664,8945664,8945664,8945664,8945664,40960,44032,48128,51200,55296,58368,62464,65536,62464,65536,62464,65536,268439552,336991254,1,0,80,0,192,1056915199,16780300,50465804,8064,36,224,1129185280,1129185280,1129185280,1098907648,1098907648,0,1661760,1073741824,1661184,44032,44032,40960,8862720,1073741824,8859648,17229,1107575632,4294922668,4294967294,17229,1107575632,4294922668,4294967294,17229,1107575632,4294922668,4294967294,
2795520,2795520,2838528,2838528,2881536,2881536,2924544,2924544,2924544,2924544,2924544,2924544,40960,44032,48128,51200,55296,58368,62464,65536,62464,65536,62464,65536,268439552,336991254,1,0,80,0,192,1040137727,16780300,50465804,8064,36,224,1115947008,1115947008,1115947008,1098907648,1098907648,0,532800,1073741824,532224,44032,44032,40960,2841600,1073741824,2838528,17026,1107575432,4294954028,4294967294,17026,1107575432,4294954028,4294967294,17026,1107575432,4294954028,4294967294,
4945920,4945920,4988928,4988928,5031936,5031936,5074944,5074944,5074944,5074944,5074944,5074944,40960,44032,48128,51200,55296,58368,62464,65536,62464,65536,62464,65536,268439552,336991254,1,0,80,0,192,1048526463,16780300,50465804,8064,36,224,1122500608,1122500608,1122500608,1098907648,1098907648,0,936000,1073741824,935424,44032,44032,40960,4992000,1073741824,4988928,17126,1107575532,4294942828,4294967294,17126,1107575532,4294942828,4294967294,17126,1107575532,4294942828,4294967294,
10560,10560,10752,10752,10944,10944,11136,11136,11136,11136,11136,11136,40960,44032,48128,51200,55296,58368,62464,65536,62464,65536,62464,65536,268439552,336991254,1,0,80,0,43008,1048526591,50465804,16780300,36,8064,224,1113587712,1113587712,1113587712,1098907648,1098907648,0,131040,1073741824,2016,44032,44032,40960,698880,1073741824,10752,16988,1107575400,4294956268,4294967294,16988,1107575400,4294956268,4294967294,16988,1107575400,4294956268,4294967294,
688128,731136,774144,817152,860160,903168,2881536,2924544,2924544,2924544,2924544,2924544,49152,53248,57344,61440,65536,69632,40960,45056,45056,45056,45056,45056,2155872256,320017685,1,0,80,0,192,1048526335,16780300,50465804,8064,36,128,1115947008,1115947008,1099956224,1124073472,1124073472,0,536832,1073741824,145152,32768,32768,53248,2863104,1073741824,774144,1123959396,1125139080,4294959244,126,1123959396,1125139080,4294959244,126,16768,1099973032,4294966796,4294967294,
28224,28224,28416,28416,28608,28608,28800,28800,28800,28800,28800,28800,40960,44032,48128,51200,55296,58368,62464,65536,62464,65536,62464,65536,268439552,336991254,1,0,80,0,43008,1056915199,50465804,16780300,36,8064,224,1125384192,1125384192,1125384192,1098907648,1098907648,0,134352,1073741824,5328,44032,44032,40960,716544,1073741824,28416,17171,1107575574,4294935660,4294967294,17171,1107575574,4294935660,4294967294,17171,1107575574,4294935660,4294967294,
37824,37824,38016,38016,38208,38208,38400,38400,38400,38400,38400,38400,40960,44032,48128,51200,55296,58368,62464,65536,62464,65536,62464,65536,268439552,336991254,1,0,80,0,43008,1056915199,50465804,16780300,36,8064,224,1128660992,1128660992,1128660992,1098907648,1098907648,0,136152,1073741824,7128,44032,44032,40960,726144,1073741824,38016,17221,1107575624,4294924460,4294967294,17221,1107575624,4294924460,4294967294,17221,1107575624,4294924460,4294967294,
6580224,6580224,6623232,6623232,6666240,6666240,6709248,6709248,6709248,6709248,6709248,6709248,40960,44032,48128,51200,55296,58368,62464,65536,62464,65536,62464,65536,268439552,336991254,1,0,80,0,192,1056915071,16780300,50465804,8064,36,224,1125777408,1125777408,1125777408,1098907648,1098907648,0,1242432,1073741824,1241856,44032,44032,40960,6626304,1073741824,6623232,17177,1107575580,4294934316,4294967294,17177,1107575580,4294934316,4294967294,17177,1107575580,4294934316,4294967294,
8902656,8902656,8945664,8945664,8988672,8988672,9031680,9031680,9031680,9031680,9031680,9031680,40960,44032,48128,51200,55296,58368,62464,65536,62464,65536,62464,65536,268439552,336991254,1,0,80,0,192,1056915199,16780300,50465804,8064,36,224,1129316352,1129316352,1129316352,1098907648,1098907648,0,1677888,1073741824,1677312,44032,44032,40960,8948736,1073741824,8945664,17231,1107575634,4294922220,4294967294,17231,1107575634,4294922220,4294967294,17231,1107575634,4294922220,4294967294,
2881536,2881536,2924544,2924544,2967552,2967552,3010560,3010560,3010560,3010560,3010560,3010560,40960,44032,48128,51200,55296,58368,62464,65536,62464,65536,62464,65536,268439552,336991254,1,0,80,0,192,1040137727,16780300,50465804,8064,36,224,1116209152,1116209152,1116209152,1098907648,1098907648,0,548928,1073741824,548352,44032,44032,40960,2927616,1073741824,2924544,17030,1107575436,4294953580,4294967294,17030,1107575436,4294953580,4294967294,17030,1107575436,4294953580,4294967294,
5031936,5031936,5074944,5074944,5117952,5117952,5160960,5160960,5160960,5160960,5160960,5160960,40960,44032,48128,51200,55296,58368,62464,65536,62464,65536,62464,65536,268439552,336991254,1,0,80,0,192,1048526463,16780300,50465804,8064,36,224,1122762752,1122762752,1122762752,1098907648,1098907648,0,952128,1073741824,951552,44032,44032,40960,5078016,1073741824,5074944,17130,1107575536,4294942380,4294967294,17130,1107575536,4294942380,4294967294,17130,1107575536,4294942380,4294967294,
10944,10944,11136,11136,11328,11328,11520,11520,11520,11520,11520,11520,40960,44032,48128,51200,55296,58368,62464,65536,62464,65536,62464,65536,268439552,336991254,1,0,80,0,43008,1048526591,50465804,16780300,36,8064,224,1114112000,1114112000,1114112000,1098907648,1098907648,0,131112,1073741824,2088,44032,44032,40960,699264,1073741824,11136,16996,1107575408,4294955820,4294967294,16996,1107575408,4294955820,4294967294,16996,1107575408,4294955820,4294967294,
20544,20544,20736,20736,20928,20928,21120,21120,21120,21120,21120,21120,40960,44032,48128,51200,55296,58368,62464,65536,62464,65536,62464,65536,268439552,336991254,1,0,80,0,43008,1048526463,50465804,16780300,36,8064,224,1121452032,1121452032,1121452032,1098907648,1098907648,0,132912,1073741824,3888,44032,44032,40960,708864,1073741824,20736,17110,1107575516,4294944620,4294967294,17110,1107575516,4294944620,4294967294,17110,1107575516,4294944620,4294967294,
28608,28608,28800,28800,28992,28992,29184,29184,29184,29184,29184,29184,40960,44032,48128,51200,55296,58368,62464,65536,62464,65536,62464,65536,268439552,336991254,1,0,80,0,43008,1056915199,50465804,16780300,36,8064,224,1125515264,1125515264,1125515264,1098907648,1098907648,0,134424,1073741824,5400,44032,44032,40960,716928,1073741824,28800,17173,1107575576,4294935212,4294967294,17173,1107575576,4294935212,4294967294,17173,1107575576,4294935212,4294967294,
38208,38208,38400,38400,38592,38592,38784,38784,38784,38784,38784,38784,40960,44032,48128,51200,55296,58368,62464,65536,62464,65536,62464,65536,268439552,336991254,1,0,80,0,43008,1056915199,50465804,16780300,36,8064,224,1128792064,1128792064,1128792064,1098907648,1098907648,0,136224,1073741824,7200,44032,44032,40960,726528,1073741824,38400,17223,1107575626,4294924012,4294967294,17223,1107575626,4294924012,4294967294,17223,1107575626,4294924012,4294967294,
6666240,6666240,6709248,6709248,6752256,6752256,6795264,6795264,6795264,6795264,6795264,6795264,40960,44032,48128,51200,55296,58368,62464,65536,62464,65536,62464,65536,268439552,336991254,1,0,80,0,192,1056915071,16780300,50465804,8064,36,224,1125908480,1125908480,1125908480,1098907648,1098907648,0,1258560,1073741824,1257984,44032,44032,40960,6712320,1073741824,6709248,17179,1107575582,4294933868,4294967294,17179,1107575582,4294933868,4294967294,17179,1107575582,4294933868,4294967294,
8988672,8988672,9031680,9031680,9074688,9074688,9117696,9117696,9117696,9117696,9117696,9117696,40960,44032,48128,51200,55296,58368,62464,65536,62464,65536,62464,65536,268439552,336991254,1,0,80,0,192,1056915199,16780300,50465804,8064,36,224,1129447424,1129447424,1129447424,1098907648,1098907648,0,1694016,1073741824,1693440,44032,44032,40960,9034752,1073741824,9031680,17233,1107575636,4294921772,4294967294,17233,1107575636,4294921772,4294967294,17233,1107575636,4294921772,4294967294,
2967552,2967552,3010560,3010560,3053568,3053568,3096576,3096576,3096576,3096576,3096576,3096576,40960,44032,48128,51200,55296,58368,62464,65536,62464,65536,62464,65536,268439552,336991254,1,0,80,0,192,1040137727,16780300,50465804,8064,36,224,1116471296,1116471296,1116471296,1098907648,1098907648,0,565056,1073741824,564480,44032,44032,40960,3013632,1073741824,3010560,17034,1107575440,4294953132,4294967294,17034,1107575440,4294953132,4294967294,17034,1107575440,4294953132,4294967294,
5117952,5117952,5160960,5160960,5203968,5203968,5246976,5246976,5246976,5246976,5246976,5246976,40960,44032,48128,51200,55296,58368,62464,65536,62464,65536,62464,65536,268439552,336991254,1,0,80,0,192,1048526463,16780300,50465804,8064,36,224,1123024896,1123024896,1123024896,1098907648,1098907648,0,968256,1073741824,967680,44032,44032,40960,5164032,1073741824,5160960,17134,1107575540,4294941932,4294967294,17134,1107575540,4294941932,4294967294,17134,1107575540,4294941932,4294967294,
602112,645120,688128,731136,774144,817152,8558592,8601600,8601600,8601600,8601600,8601600,49152,53248,57344,61440,65536,69632,40960,45056,45056,45056,45056,45056,1616904192,320017685,1,0,80,0,192,1048526591,16780300,50465804,8064,36,128,1128660992,1128660992,1098907648,1119879168,1119879168,0,1600128,1073741824,129024,32768,32768,53248,8534016,1073741824,688128,1119765309,1121993544,4294942540,94,1119765309,1121993544,4294942540,94,16736,1099973016,4294967052,4294967294,
20928,20928,21120,21120,21312,21312,21504,21504,21504,21504,21504,21504,40960,44032,48128,51200,55296,58368,62464,65536,62464,65536,62464,65536,268439552,336991254,1,0,80,0,43008,1048526463,50465804,16780300,36,8064,224,1121714176,1121714176,1121714176,1098907648,1098907648,0,132984,1073741824,3960,44032,44032,40960,709248,1073741824,21120,17114,1107575520,4294944172,4294967294,17114,1107575520,4294944172,4294967294,17114,1107575520,4294944172,4294967294,
28992,28992,29184,29184,29376,29376,29568,29568,29568,29568,29568,29568,40960,44032,48128,51200,55296,58368,62464,65536,62464,65536,62464,65536,268439552,336991254,1,0,80,0,43008,1056915199,50465804,16780300,36,8064,224,1125646336,1125646336,1125646336,1098907648,1098907648,0,134496,1073741824,5472,44032,44032,40960,717312,1073741824,29184,17175,1107575578,4294934764,4294967294,17175,1107575578,4294934764,4294967294,17175,1107575578,4294934764,4294967294,
38592,38592,38784,38784,38976,38976,39168,39168,39168,39168,39168,39168,40960,44032,48128,51200,55296,58368,62464,65536,62464,65536,62464,65536,268439552,336991254,1,0,80,0,43008,1056915199,50465804,16780300,36,8064,224,1128923136,1128923136,1128923136,1098907648,1098907648,0,136296,1073741824,7272,44032,44032,40960,726912,1073741824,38784,17225,1107575628,4294923564,4294967294,17225,1107575628,4294923564,4294967294,17225,1107575628,4294923564,4294967294,
6752256,6752256,6795264,6795264,6838272,6838272,6881280,6881280,6881280,6881280,6881280,6881280,40960,44032,48128,51200,55296,58368,62464,65536,62464,65536,62464,65536,268439552,336991254,1,0,80,0,192,1056915071,16780300,50465804,8064,36,224,1126039552,1126039552,1126039552,1098907648,1098907648,0,1274688,1073741824,1274112,44032,44032,40960,6798336,1073741824,6795264,17181,1107575584,4294933420,4294967294,17181,1107575584,4294933420,4294967294,17181,1107575584,4294933420,4294967294,
9074688,9074688,9117696,9117696,9160704,9160704,9203712,9203712,9203712,9203712,9203712,9203712,40960,44032,48128,51200,55296,58368,62464,65536,62464,65536,62464,65536,268439552,336991254,1,0,80,0,192,1056915199,16780300,50465804,8064,36,224,1129578496,1129578496,1129578496,1098907648,1098907648,0,1710144,1073741824,1709568,44032,44032,40960,9120768,1073741824,9117696,17235,1107575638,4294921324,4294967294,17235,1107575638,4294921324,4294967294,17235,1107575638,4294921324,4294967294,
172032,215040,258048,301056,344064,387072,2881536,2924544,2924544,2924544,2924544,2924544,49152,53248,57344,61440,65536,69632,40960,45056,45056,45056,45056,45056,1077936128,320017685,1,512,80,0,192,1040137727,16780300,50465804,8064,36,128,1115947008,1115947008,1086324736,1115684864,1115684864,0,534528,1073741824,48384,32768,32768,53248,2850816,1073741824,258048,1115439716,1117799048,4294959628,62,1115439716,1117799048,4294959628,62,16512,1099972880,1036,4294967294,
5203968,5203968,5246976,5246976,5289984,5289984,5332992,5332992,5332992,5332992,5332992,5332992,40960,44032,48128,51200,55296,58368,62464,65536,62464,65536,62464,65536,268439552,336991254,1,0,80,0,192,1048526591,16780300,50465804,8064,36,224,1123287040,1123287040,1123287040,1098907648,1098907648,0,984384,1073741824,983808,44032,44032,40960,5250048,1073741824,5246976,17138,1107575544,4294941484,4294967294,17138,1107575544,4294941484,4294967294,17138,1107575544,4294941484,4294967294,
11328,11328,11520,11520,11712,11712,11904,11904,11904,11904,11904,11904,40960,44032,48128,51200,55296,58368,62464,65536,62464,65536,62464,65536,268439552,336991254,1,0,80,0,43008,1048526591,50465804,16780300,36,8064,224,1114636288,1114636288,1114636288,1098907648,1098907648,0,131184,1073741824,2160,44032,44032,40960,699648,1073741824,11520,17004,1107575416,4294955372,4294967294,17004,1107575416,4294955372,4294967294,17004,1107575416,4294955372,4294967294,
21312,21312,21504,21504,21696,21696,21888,21888,21888,21888,21888,21888,40960,44032,48128,51200,55296,58368,62464,65536,62464,65536,62464,65536,268439552,336991254,1,0,80,0,43008,1048526463,50465804,16780300,36,8064,224,1121976320,1121976320,1121976320,1098907648,1098907648,0,133056,1073741824,4032,44032,44032,40960,709632,1073741824,21504,17118,1107575524,4294943724,4294967294,17118,1107575524,4294943724,4294967294,17118,1107575524,4294943724,4294967294,
29376,29376,29568,29568,29760,29760,29952,29952,29952,29952,29952,29952,40960,44032,48128,51200,55296,58368,62464,65536,62464,65536,62464,65536,268439552,336991254,1,0,80,0,43008,1056915199,50465804,16780300,36,8064,224,1125777408,1125777408,1125777408,1098907648,1098907648,0,134568,1073741824,5544,44032,44032,40960,717696,1073741824,29568,17177,1107575580,4294934316,4294967294,17177,1107575580,4294934316,4294967294,17177,1107575580,4294934316,4294967294,
38976,38976,39168,39168,39360,39360,39552,39552,39552,39552,39552,39552,40960,44032,48128,51200,55296,58368,62464,65536,62464,65536,62464,65536,268439552,336991254,1,0,80,0,43008,1056914943,50465804,16780300,36,8064,224,1129054208,1129054208,1129054208,1098907648,1098907648,0,136368,1073741824,7344,44032,44032,40960,727296,1073741824,39168,17227,1107575630,4294923116,4294967294,17227,1107575630,4294923116,4294967294,17227,1107575630,4294923116,4294967294,
6838272,6838272,6881280,6881280,6924288,6924288,6967296,6967296,6967296,6967296,6967296,6967296,40960,44032,48128,51200,55296,58368,62464,65536,62464,65536,62464,65536,268439552,336991254,1,0,80,0,192,1056915071,16780300,50465804,8064,36,224,1126170624,1126170624,1126170624,1098907648,1098907648,0,1290816,1073741824,1290240,44032,44032,40960,6884352,1073741824,6881280,17183,1107575586,4294932972,4294967294,17183,1107575586,4294932972,4294967294,17183,1107575586,4294932972,4294967294,
9160704,9160704,9203712,9203712,9246720,9246720,9289728,9289728,9289728,9289728,9289728,9289728,40960,44032,48128,51200,55296,58368,62464,65536,62464,65536,62464,65536,268439552,336991254,1,0,80,0,192,1056915199,16780300,50465804,8064,36,224,1129709568,1129709568,1129709568,1098907648,1098907648,0,1726272,1073741824,1725696,44032,44032,40960,9206784,1073741824,9203712,17237,1107575640,4294920876,4294967294,17237,1107575640,4294920876,4294967294,17237,1107575640,4294920876,4294967294,
3053568,3053568,3096576,3096576,3139584,3139584,3182592,3182592,3182592,3182592,3182592,3182592,40960,44032,48128,51200,55296,58368,62464,65536,62464,65536,62464,65536,268439552,336991254,1,0,80,0,192,1040137855,16780300,50465804,8064,36,224,1116733440,1116733440,1116733440,1098907648,1098907648,0,581184,1073741824,580608,44032,44032,40960,3099648,1073741824,3096576,17038,1107575444,4294952684,4294967294,17038,1107575444,4294952684,4294967294,17038,1107575444,4294952684,4294967294,
5289984,5289984,5332992,5332992,5376000,5376000,5419008,5419008,5419008,5419008,5419008,5419008,40960,44032,48128,51200,55296,58368,62464,65536,62464,65536,62464,65536,268439552,336991254,1,0,80,0,192,1048526591,16780300,50465804,8064,36,224,1123549184,1123549184,1123549184,1098907648,1098907648,0,1000512,1073741824,999936,44032,44032,40960,5336064,1073741824,5332992,17142,1107575548,4294941036,4294967294,17142,1107575548,4294941036,4294967294,17142,1107575548,4294941036,4294967294,
11712,11712,11904,11904,12096,12096,12288,12288,12288,12288,12288,12288,40960,44032,48128,51200,55296,58368,62464,65536,62464,65536,62464,65536,268439552,336991254,1,0,80,0,43008,1048526591,50465804,16780300,36,8064,224,1115160576,1115160576,1115160576,1098907648,1098907648,0,131256,1073741824,2232,44032,44032,40960,700032,1073741824,11904,17012,1107575424,4294954924,4294967294,17012,1107575424,4294954924,4294967294,17012,1107575424,4294954924,4294967294,
21696,21696,21888,21888,22080,22080,22272,22272,22272,22272,22272,22272,40960,44032,48128,51200,55296,58368,62464,65536,62464,65536,62464,65536,268439552,336991254,1,0,80,0,43008,1048526463,50465804,16780300,36,8064,224,1122238464,1122238464,1122238464,1098907648,1098907648,0,133128,1073741824,4104,44032,44032,40960,710016,1073741824,21888,17122,1107575528,4294943276,4294967294,17122,1107575528,4294943276,4294967294,17122,1107575528,4294943276,4294967294,
29760,29760,29952,29952,30144,30144,30336,30336,30336,30336,30336,30336,40960,44032,48128,51200,55296,58368,62464,65536,62464,65536,62464,65536,268439552,336991254,1,0,80,0,43008,1056915199,50465804,16780300,36,8064,224,1125908480,1125908480,1125908480,1098907648,1098907648,0,134640,1073741824,5616,44032,44032,40960,718080,1073741824,29952,17179,1107575582,4294933868,4294967294,17179,1107575582,4294933868,4294967294,17179,1107575582,4294933868,4294967294,
39360,39360,39552,39552,39744,39744,39936,39936,39936,39936,39936,39936,40960,44032,48128,51200,55296,58368,62464,65536,62464,65536,62464,65536,268439552,336991254,1,0,80,0,43008,1056914943,50465804,16780300,36,8064,224,1129185280,1129185280,1129185280,1098907648,1098907648,0,136440,1073741824,7416,44032,44032,40960,727680,1073741824,39552,17229,1107575632,4294922668,4294967294,17229,1107575632,4294922668,4294967294,17229,1107575632,4294922668,4294967294,
6924288,6924288,6967296,6967296,7010304,7010304,7053312,7053312,7053312,7053312,7053312,7053312,40960,44032,48128,51200,55296,58368,62464,65536,62464,65536,62464,65536,268439552,336991254,1,0,80,0,192,1056915071,16780300,50465804,8064,36,224,1126301696,1126301696,1126301696,1098907648,1098907648,0,1306944,1073741824,1306368,44032,44032,40960,6970368,1073741824,6967296,17185,1107575588,4294932524,4294967294,17185,1107575588,4294932524,4294967294,17185,1107575588,4294932524,4294967294,
9246720,9246720,9289728,9289728,9332736,9332736,9375744,9375744,9375744,9375744,9375744,9375744,40960,44032,48128,51200,55296,58368,62464,65536,62464,65536,62464,65536,268439552,336991254,1,0,80,0,192,1056915199,16780300,50465804,8064,36,224,1129840640,1129840640,1129840640,1098907648,1098907648,0,1742400,1073741824,1741824,44032,44032,40960,9292800,1073741824,9289728,17239,1107575642,4294920428,4294967294,17239,1107575642,4294920428,4294967294,17239,1107575642,4294920428,4294967294,
3139584,3139584,3182592,3182592,3225600,3225600,3268608,3268608,3268608,3268608,3268608,3268608,40960,44032,48128,51200,55296,58368,62464,65536,62464,65536,62464,65536,268439552,336991254,1,0,80,0,192,1040137855,16780300,50465804,8064,36,224,1116995584,1116995584,1116995584,1098907648,1098907648,0,597312,1073741824,596736,44032,44032,40960,3185664,1073741824,3182592,17042,1107575448,4294952236,4294967294,17042,1107575448,4294952236,4294967294,17042,1107575448,4294952236,4294967294,
5376000,5376000,5419008,5419008,5462016,5462016,5505024,5505024,5505024,5505024,5505024,5505024,40960,44032,48128,51200,55296,58368,62464,65536,62464,65536,62464,65536,268439552,336991254,1,0,80,0,192,1048526591,16780300,50465804,8064,36,224,1123811328,1123811328,1123811328,1098907648,1098907648,0,1016640,1073741824,1016064,44032,44032,40960,5422080,1073741824,5419008,17146,1107575552,4294940588,4294967294,17146,1107575552,4294940588,4294967294,17146,1107575552,4294940588,4294967294,
12096,12096,12288,12288,12480,12480,12672,12672,12672,12672,12672,12672,40960,44032,48128,51200,55296,58368,62464,65536,62464,65536,62464,65536,268439552,336991254,1,0,80,0,43008,1048526591,50465804,16780300,36,8064,224,1115684864,1115684864,1115684864,1098907648,1098907648,0,131328,1073741824,2304,44032,44032,40960,700416,1073741824,12288,17020,1107575428,4294954476,4294967294,17020,1107575428,4294954476,4294967294,17020,1107575428,4294954476,4294967294,
22080,22080,22272,22272,22464,22464,22656,22656,22656,22656,22656,22656,40960,44032,48128,51200,55296,58368,62464,65536,62464,65536,62464,65536,268439552,336991254,1,0,80,0,43008,1048526463,50465804,16780300,36,8064,224,1122500608,1122500608,1122500608,1098907648,1098907648,0,133200,1073741824,4176,44032,44032,40960,710400,1073741824,22272,17126,1107575532,4294942828,4294967294,17126,1107575532,4294942828,4294967294,17126,1107575532,4294942828,4294967294,
30144,30144,30336,30336,30528,30528,30720,30720,30720,30720,30720,30720,40960,44032,48128,51200,55296,58368,62464,65536,62464,65536,62464,65536,268439552,336991254,1,0,80,0,43008,1056915199,50465804,16780300,36,8064,224,1126039552,1126039552,1126039552,1098907648,1098907648,0,134712,1073741824,5688,44032,44032,40960,718464,1073741824,30336,17181,1107575584,4294933420,4294967294,17181,1107575584,4294933420,4294967294,17181,1107575584,4294933420,4294967294,
39744,39744,39936,39936,40128,40128,40320,40320,40320,40320,40320,40320,40960,44032,48128,51200,55296,58368,62464,65536,62464,65536,62464,65536,268439552,336991254,1,0,80,0,43008,1056914943,50465804,16780300,36,8064,224,1129316352,1129316352,1129316352,1098907648,1098907648,0,136512,1073741824,7488,44032,44032,40960,728064,1073741824,39936,17231,1107575634,4294922220,4294967294,17231,1107575634,4294922220,4294967294,17231,1107575634,4294922220,4294967294,
5634048,5677056,5720064,5763072,5806080,5849088,5849088,5849088,5849088,5849088,5849088,5849088,40960,45056,49152,53248,57344,61440,61440,61440,61440,61440,61440,61440,3233857728,320017171,0,256,80,0,192,1056915071,16780300,50465804,8064,36,128,1124204544,1124335616,1124466688,1128267776,1128267776,1128267776,1055232,1071360,1087488,32768,40960,49152,5627904,5713920,5799936,1128219378,1129333512,4294950668,190,1128219378,1129333512,4294950668,190,1128219378,1129333512,4294950668,190,
9332736,9375744,9418752,9461760,9504768,9547776,9590784,9590784,9590784,9590784,9590784,9590784,40960,45056,49152,53248,57344,61440,65536,69632,69632,69632,69632,69632,538976288,353703189,0,256,80,0,192,1056915199,16780300,50465804,8064,36,128,1130233856,1130102784,1129971712,1107296256,1107296256,1107296256,1791360,1775232,1759104,57344,49152,40960,9553920,9467904,9381888,1106264921,1112032095,4294940620,30,1106264921,1112032095,4294940620,30,1106264921,1112032095,4294940620,30,
2107392,2150400,2193408,2236416,2279424,2322432,2365440,2408448,2408448,2408448,2408448,2408448,40960,45056,49152,53248,57344,61440,65536,69632,69632,69632,69632,69632,538976288,353703189,0,256,80,0,192,1040137855,16780300,50465804,8064,36,128,1113063424,1112539136,1112014848,1107296256,1107296256,1107296256,436608,420480,404352,57344,49152,40960,2328576,2242560,2156544,1106264644,1112031840,4294962124,30,1106264644,1112031840,4294962124,30,1106264644,1112031840,4294962124,30,
3139584,3182592,3225600,3268608,3311616,3354624,3397632,3440640,3440640,3440640,3440640,3440640,40960,45056,49152,53248,57344,61440,65536,69632,69632,69632,69632,69632,538976288,353703189,0,256,80,0,192,1048526591,16780300,50465804,8064,36,128,1117519872,1117257728,1116995584,1107296256,1107296256,1107296256,630144,614016,597888,57344,49152,40960,3360768,3274752,3188736,1106264722,1112031904,4294959052,30,1106264722,1112031904,4294959052,30,1106264722,1112031904,4294959052,30,
12480,12480,12672,12672,12864,12864,13056,13056,13056,13056,13056,13056,40960,44032,48128,51200,55296,58368,62464,65536,62464,65536,62464,65536,268439552,336991254,1,0,80,0,43008,1048526335,50465804,16780300,36,8064,224,1115947008,1115947008,1115947008,1098907648,1098907648,0,131400,1073741824,2376,44032,44032,40960,700800,1073741824,12672,17026,1107575432,4294954028,4294967294,17026,1107575432,4294954028,4294967294,17026,1107575432,4294954028,4294967294,
5203968,5246976,5289984,5332992,5376000,5419008,5462016,5505024,5505024,5505024,5505024,5505024,40960,45056,49152,53248,57344,61440,65536,69632,69632,69632,69632,69632,538976288,353703189,0,256,80,0,192,1048526463,16780300,50465804,8064,36,128,1123811328,1123549184,1123287040,1107296256,1107296256,1107296256,1017216,1001088,984960,57344,49152,40960,5425152,5339136,5253120,1106264818,1112032000,4294952908,30,1106264818,1112032000,4294952908,30,1106264818,1112032000,4294952908,30,
6236160,6279168,6322176,6365184,6408192,6451200,6494208,6537216,6537216,6537216,6537216,6537216,40960,45056,49152,53248,57344,61440,65536,69632,69632,69632,69632,69632,538976288,353703189,0,256,80,0,192,1056915199,16780300,50465804,8064,36,128,1125515264,1125384192,1125253120,1107296256,1107296256,1107296256,1210752,1194624,1178496,57344,49152,40960,6457344,6371328,6285312,1106264849,1112032024,4294949836,30,1106264849,1112032024,4294949836,30,1106264849,1112032024,4294949836,30,
40128,40128,40320,40320,40512,40512,40704,40704,40704,40704,40704,40704,40960,44032,48128,51200,55296,58368,62464,65536,62464,65536,62464,65536,268439552,336991254,1,0,80,0,43008,1056914943,50465804,16780300,36,8064,224,1129447424,1129447424,1129447424,1098907648,1098907648,0,136584,1073741824,7560,44032,44032,40960,728448,1073741824,40320,17233,1107575636,4294921772,4294967294,17233,1107575636,4294921772,4294967294,17233,1107575636,4294921772,4294967294,
7010304,7010304,7053312,7053312,7096320,7096320,7139328,7139328,7139328,7139328,7139328,7139328,40960,44032,48128,51200,55296,58368,62464,65536,62464,65536,62464,65536,268439552,336991254,1,0,80,0,192,1056915199,16780300,50465804,8064,36,224,1126432768,1126432768,1126432768,1098907648,1098907648,0,1323072,1073741824,1322496,44032,44032,40960,7056384,1073741824,7053312,17187,1107575590,4294932076,4294967294,17187,1107575590,4294932076,4294967294,17187,1107575590,4294932076,4294967294,
9332736,9332736,9375744,9375744,9418752,9418752,9461760,9461760,9461760,9461760,9461760,9461760,40960,44032,48128,51200,55296,58368,62464,65536,62464,65536,62464,65536,268439552,336991254,1,0,80,0,192,1056915199,16780300,50465804,8064,36,224,1129971712,1129971712,1129971712,1098907648,1098907648,0,1758528,1073741824,1757952,44032,44032,40960,9378816,1073741824,9375744,17241,1107575644,4294919980,4294967294,17241,1107575644,4294919980,4294967294,17241,1107575644,4294919980,4294967294,
3225600,3225600,3268608,3268608,3311616,3311616,3354624,3354624,3354624,3354624,3354624,3354624,40960,44032,48128,51200,55296,58368,62464,65536,62464,65536,62464,65536,268439552,336991254,1,0,80,0,192,1040137855,16780300,50465804,8064,36,224,1117257728,1117257728,1117257728,1098907648,1098907648,0,613440,1073741824,612864,44032,44032,40960,3271680,1073741824,3268608,17046,1107575452,4294951788,4294967294,17046,1107575452,4294951788,4294967294,17046,1107575452,4294951788,4294967294,
2688,2688,2880,2880,3072,3072,3264,3264,3456,3456,3648,3648,40960,44032,48128,51200,55296,58368,62464,65536,69632,72704,76800,79872,268439552,336991254,1,1024,64,0,43008,1048526591,50465804,16780300,36,8064,224,1098907648,1098907648,1098907648,1098907648,1098907648,0,129600,1073741824,576,51200,51200,48128,691200,1073741824,3072,16736,1107575192,4294965452,4294967294,16736,1107575192,4294965452,4294967294,16736,1107575192,4294965452,4294967294,
4171776,4214784,4257792,4300800,4343808,4386816,4429824,4472832,4472832,4472832,4472832,4472832,40960,45056,49152,53248,57344,61440,65536,69632,69632,69632,69632,69632,538976288,353703189,0,256,80,0,192,1048526335,16780300,50465804,8064,36,128,1120665600,1120403456,1120141312,1107296256,1107296256,1107296256,823680,807552,791424,57344,49152,40960,4392960,4306944,4220928,1106264770,1112031952,4294955980,30,1106264770,1112031952,4294955980,30,1106264770,1112031952,4294955980,30,
22464,22464,22656,22656,22848,22848,23040,23040,23040,23040,23040,23040,40960,44032,48128,51200,55296,58368,62464,65536,62464,65536,62464,65536,268439552,336991254,1,0,80,0,43008,1048526463,50465804,16780300,36,8064,224,1122762752,1122762752,1122762752,1098907648,1098907648,0,133272,1073741824,4248,44032,44032,40960,710784,1073741824,22656,17130,1107575536,4294942380,4294967294,17130,1107575536,4294942380,4294967294,17130,1107575536,4294942380,4294967294,
30528,30528,30720,30720,30912,30912,31104,31104,31104,31104,31104,31104,40960,44032,48128,51200,55296,58368,62464,65536,62464,65536,62464,65536,268439552,336991254,1,0,80,0,43008,1056915199,50465804,16780300,36,8064,224,1126170624,1126170624,1126170624,1098907648,1098907648,0,134784,1073741824,5760,44032,44032,40960,718848,1073741824,30720,17183,1107575586,4294932972,4294967294,17183,1107575586,4294932972,4294967294,17183,1107575586,4294932972,4294967294,
7268352,7311360,7354368,7397376,7440384,7483392,7526400,7569408,7569408,7569408,7569408,7569408,40960,45056,49152,53248,57344,61440,65536,69632,69632,69632,69632,69632,538976288,353703189,0,256,80,0,192,1056915071,16780300,50465804,8064,36,128,1127088128,1126957056,1126825984,1107296256,1107296256,1107296256,1404288,1388160,1372032,57344,49152,40960,7489536,7403520,7317504,1106264873,1112032048,4294946764,30,1106264873,1112032048,4294946764,30,1106264873,1112032048,4294946764,30,
8300544,8343552,8386560,8429568,8472576,8515584,8558592,8601600,8601600,8601600,8601600,8601600,40960,45056,49152,53248,57344,61440,65536,69632,69632,69632,69632,69632,538976288,353703189,0,256,80,0,192,1056915199,16780300,50465804,8064,36,128,1128660992,1128529920,1128398848,1107296256,1107296256,1107296256,1597824,1581696,1565568,57344,49152,40960,8521728,8435712,8349696,1106264897,1112032072,4294943692,30,1106264897,1112032072,4294943692,30,1106264897,1112032072,4294943692,30,
9418752,9418752,9461760,9461760,9504768,9504768,9547776,9547776,9547776,9547776,9547776,9547776,40960,44032,48128,51200,55296,58368,62464,65536,62464,65536,62464,65536,268439552,336991254,1,0,80,0,192,1056915199,16780300,50465804,8064,36,224,1130102784,1130102784,1130102784,1098907648,1098907648,0,1774656,1073741824,1774080,44032,44032,40960,9464832,1073741824,9461760,17243,1107575646,4294919532,4294967294,17243,1107575646,4294919532,4294967294,17243,1107575646,4294919532,4294967294,
3311616,3311616,3354624,3354624,3397632,3397632,3440640,3440640,3440640,3440640,3440640,3440640,40960,44032,48128,51200,55296,58368,62464,65536,62464,65536,62464,65536,268439552,336991254,1,0,80,0,192,1040137855,16780300,50465804,8064,36,224,1117519872,1117519872,1117519872,1098907648,1098907648,0,629568,1073741824,628992,44032,44032,40960,3357696,1073741824,3354624,17050,1107575456,4294951340,4294967294,17050,1107575456,4294951340,4294967294,17050,1107575456,4294951340,4294967294,
3072,3072,3264,3264,3456,3456,3648,3648,3840,3840,4032,4032,40960,44032,48128,51200,55296,58368,62464,65536,69632,72704,76800,79872,268439552,336991254,1,1024,64,0,43008,1048526591,50465804,16780300,36,8064,224,1099956224,1099956224,1099956224,1098907648,1098907648,0,129672,1073741824,648,51200,51200,48128,691584,1073741824,3456,16768,1107575208,4294965004,4294967294,16768,1107575208,4294965004,4294967294,16768,1107575208,4294965004,4294967294,
12864,12864,13056,13056,13248,13248,13440,13440,13440,13440,13440,13440,40960,44032,48128,51200,55296,58368,62464,65536,62464,65536,62464,65536,268439552,336991254,1,0,80,0,43008,1048526335,50465804,16780300,36,8064,224,1116209152,1116209152,1116209152,1098907648,1098907648,0,131472,1073741824,2448,44032,44032,40960,701184,1073741824,13056,17030,1107575436,4294953580,4294967294,17030,1107575436,4294953580,4294967294,17030,1107575436,4294953580,4294967294,
774144,817152,860160,903168,946176,989184,5720064,5763072,5763072,5763072,5763072,5763072,49152,53248,57344,61440,65536,69632,40960,45056,45056,45056,45056,45056,2155872256,320017685,1,0,80,0,192,1048526463,16780300,50465804,8064,36,128,1124335616,1124335616,1101004800,1124073472,1124073472,0,1069056,1073741824,161280,32768,32768,53248,5701632,1073741824,860160,1123959542,1125139206,4294950796,126,1123959542,1125139206,4294950796,126,16784,1099973048,4294966540,4294967294,
30912,30912,31104,31104,31296,31296,31488,31488,31488,31488,31488,31488,40960,44032,48128,51200,55296,58368,62464,65536,62464,65536,62464,65536,268439552,336991254,1,0,80,0,43008,1056915199,50465804,16780300,36,8064,224,1126301696,1126301696,1126301696,1098907648,1098907648,0,134856,1073741824,5832,44032,44032,40960,719232,1073741824,31104,17185,1107575588,4294932524,4294967294,17185,1107575588,4294932524,4294967294,17185,1107575588,4294932524,4294967294,
1290240,1333248,1376256,1419264,1462272,1505280,3569664,3612672,3655680,3698688,3698688,3698688,57344,61440,65536,69632,73728,77824,40960,45056,49152,53248,53248,53248,2964377808,320017685,0,1280,64,0,192,1056915071,16780300,50465804,8064,36,128,1118044160,1118306304,1107296256,1127219200,1127219200,1129316352,667584,683712,265536,32768,40960,61440,3560448,3646464,1416192,1127170706,1128284844,4294956908,174,1127170706,1128284844,4294956908,174,1129202160,1130316300,4294964012,206,
7096320,7096320,7139328,7139328,7182336,7182336,7225344,7225344,7225344,7225344,7225344,7225344,40960,44032,48128,51200,55296,58368,62464,65536,62464,65536,62464,65536,268439552,336991254,1,0,80,0,192,1056915199,16780300,50465804,8064,36,224,1126563840,1126563840,1126563840,1098907648,1098907648,0,1339200,1073741824,1338624,44032,44032,40960,7142400,1073741824,7139328,17189,1107575592,4294931628,4294967294,17189,1107575592,4294931628,4294967294,17189,1107575592,4294931628,4294967294,
9504768,9504768,9547776,9547776,9590784,9590784,9590784,9590784,9590784,9590784,9590784,9590784,40960,44032,48128,51200,55296,58368,62464,65536,62464,65536,62464,65536,268439552,336991254,1,0,80,0,192,1056915199,16780300,50465804,8064,36,224,1130233856,1130233856,1130233856,1098907648,1098907648,0,1790784,1073741824,1790208,44032,44032,40960,9550848,1073741824,9547776,17245,1107575647,4294919084,4294967294,17245,1107575647,4294919084,4294967294,17245,1107575647,4294919084,4294967294,
192,384,576,86016,384,576,800,86024,576,800,992,86032,800,992,1184,86040,992,1184,1376,86208,1184,1376,1536,86216,1376,1536,1728,86224,1536,1728,1920,86232,1728,1920,2112,86400,1920,2112,2336,86408,2112,2336,2528,86416,2336,2528,2720,86424,2528,2720,2912,86592,2720,2912,3072,86600,2912,3072,3264,86608,3072,3264,3456,86616,
208,400,592,87584,400,592,816,87592,592,816,1008,87600,816,1008,1200,87608,1008,1200,1392,87776,1200,1392,1552,87784,1392,1552,1744,87792,1552,1744,1936,87800,1744,1936,2128,87968,1936,2128,2352,87976,2128,2352,2544,87984,2352,2544,2736,87992,2544,2736,2928,88160,2736,2928,3088,88168,2928,3088,3280,88176,3088,3280,3472,88184,
224,416,608,89088,416,608,768,89096,608,768,960,89104,768,960,1152,89112,960,1152,1344,89280,1152,1344,1568,89288,1344,1568,1760,89296,1568,1760,1952,89304,1760,1952,2144,89472,1952,2144,2304,89480,2144,2304,2496,89488,2304,2496,2688,89496,2496,2688,2880,89664,2688,2880,3104,89672,2880,3104,3296,89680,3104,3296,3488,89688,
240,432,624,90656,432,624,784,90664,624,784,976,90672,784,976,1168,90680,976,1168,1360,90848,1168,1360,1584,90856,1360,1584,1776,90864,1584,1776,1968,90872,1776,1968,2160,91040,1968,2160,2320,91048,2160,2320,2512,91056,2320,2512,2704,91064,2512,2704,2896,91232,2704,2896,3120,91240,2896,3120,3312,91248,3120,3312,3504,91256,
0,64,0,0,192,80,64,16,384,96,144,32,576,112,208,48,768,256,288,64,960,272,352,80,1152,288,432,96,1344,304,496,112,1536,448,512,144,1728,464,576,128,1920,480,656,176,2112,496,720,160,2304,640,800,208,2496,656,864,192,2688,672,944,240,2880,688,1008,224,
32,832,8,288,224,848,72,304,416,864,152,256,608,880,216,272,800,1024,296,352,992,1040,360,368,1184,1056,440,320,1376,1072,504,336,1568,1216,520,432,1760,1232,584,416,1952,1248,664,400,2144,1264,728,384,2336,1408,808,496,2528,1424,872,480,2720,1440,952,464,2912,1456,1016,448,
16,1600,16,512,208,1616,80,528,400,1632,128,544,592,1648,192,560,784,1792,304,576,976,1808,368,592,1168,1824,416,608,1360,1840,480,624,1552,1984,528,656,1744,2000,592,640,1936,2016,640,688,2128,2032,704,672,2320,2176,816,720,2512,2192,880,704,2704,2208,928,752,2896,2224,992,736,
48,2368,24,800,240,2384,88,816,432,2400,136,768,624,2416,200,784,816,2560,312,864,1008,2576,376,880,1200,2592,424,832,1392,2608,488,848,1584,2752,536,944,1776,2768,600,928,1968,2784,648,912,2160,2800,712,896,2352,2944,824,1008,2544,2960,888,992,2736,2976,936,976,2928,2992,1000,960,
202113280,202116108,202113794,202116108,202116108,202116108,202116108,202116108,202116108,202116108,202116108,202116108,202116108,202116108,202116108,202116108,16780300,202116108,50465804,202116108,202116108,202116108,202116108,202116108,202116108,202116108,202116108,202116108,202116108,202116108,202116108,202116108,202116108,202116108,202116108,202116108,202116108,202116108,202116108,202116108,202116108,202113280,202116108,202113794,202116108,202116108,202116108,202116108,202116108,202116108,202116108,202116108,202116108,202116108,202116108,202116108,202116108,16780300,202116108,50465804,202116108,202116108,202116108,202116108,
202116108,202116108,202116108,202116108,202113280,202116108,202113794,202116108,202116108,202116108,202116108,202116108,202116108,202116108,202116108,202116108,202116108,202116108,202116108,202116108,16780300,202116108,50465804,202116108,202116108,202116108,202116108,202116108,202116108,202116108,202116108,202116108,202116108,202116108,202116108,202116108,202116108,202116108,202116108,202116108,202116108,202116108,202116108,202116108,202116108,202113280,202116108,202113794,202116108,202116108,202116108,202116108,202116108,202116108,202116108,202116108,202116108,202116108,202116108,202116108,202116108,16780300,202116108,50465804,
202116108,202116108,202116108,202116108,202116108,202116108,202116108,202116108,202113280,202116108,202113794,202116108,202116108,202116108,202116108,202116108,202116108,202116108,202116108,202116108,202116108,202116108,202116108,202116108,16780300,202116108,50465804,202116108,202116108,202116108,202116108,202116108,202116108,202113280,202116108,202113794,202116108,202116108,202116108,202116108,202116108,202116108,202116108,202116108,202116108,202116108,202116108,202116108,202116108,16780300,202116108,50465804,202116108,202116108,202116108,202116108,202116108,202116108,202116108,202116108,202116108,202116108,202116108,202116108,
202116108,202116108,202116108,202116108,202116108,202116108,202116108,202116108,202116108,202116108,202116108,202116108,202113280,202116108,202113794,202116108,202116108,202116108,202116108,202116108,202116108,202116108,202116108,202116108,202116108,202116108,202116108,202116108,16780300,202116108,50465804,202116108,202116108,202116108,202116108,202116108,202116108,202113280,202116108,202113794,202116108,202116108,202116108,202116108,202116108,202116108,202116108,202116108,202116108,202116108,202116108,202116108,202116108,16780300,202116108,50465804,202116108,202116108,202116108,202116108,202116108,202116108,202116108,202116108,
0,157696,0,147456,8,157696,192,147504,16,157696,400,147552,24,157696,592,147600,0,157760,800,147648,8,157760,992,147696,16,157760,1200,147744,24,157760,1392,147792,0,157856,1536,147840,8,157856,1728,147888,16,157856,1936,147936,24,157856,2128,147984,0,157920,2336,148032,8,157920,2528,148080,16,157920,2736,148128,24,157920,2928,148176,
0,157712,8,147456,8,157712,200,147504,16,157712,408,147552,24,157712,600,147600,0,157776,808,147648,8,157776,1000,147696,16,157776,1208,147744,24,157776,1400,147792,0,157872,1544,147840,8,157872,1736,147888,16,157872,1944,147936,24,157872,2136,147984,0,157936,2344,148032,8,157936,2536,148080,16,157936,2744,148128,24,157936,2936,148176,
0,157728,16,147456,8,157728,208,147504,16,157728,384,147552,24,157728,576,147600,0,157792,816,147648,8,157792,1008,147696,16,157792,1184,147744,24,157792,1376,147792,0,157824,1552,147840,8,157824,1744,147888,16,157824,1920,147936,24,157824,2112,147984,0,157888,2352,148032,8,157888,2544,148080,16,157888,2720,148128,24,157888,2912,148176,
0,157744,24,147456,8,157744,216,147504,16,157744,392,147552,24,157744,584,147600,0,157808,824,147648,8,157808,1016,147696,16,157808,1192,147744,24,157808,1384,147792,0,157840,1560,147840,8,157840,1752,147888,16,157840,1928,147936,24,157840,2120,147984,0,157904,2360,148032,8,157904,2552,148080,16,157904,2728,148128,24,157904,2920,148176,
1,0,157696,122368,2,192,157712,122372,3,400,157728,122376,4,592,157744,122380,5,800,157760,124416,6,992,157776,124420,7,1200,157792,124424,8,1392,157808,124428,9,1536,157824,126464,10,1728,157840,126468,11,1936,157856,126472,12,2128,157872,126476,13,2336,157888,128512,14,2528,157904,128516,15,2736,157920,128520,16,2928,157936,128524,
17,12288,157952,130560,18,12480,157968,130564,19,12688,157984,130568,20,12880,158000,130572,21,13088,158016,132608,22,13280,158032,132612,23,13488,158048,132616,24,13680,158064,132620,25,13824,158080,134656,26,14016,158096,134660,27,14224,158112,134664,28,14416,158128,134668,29,14624,158144,136704,30,14816,158160,136708,31,15024,158176,136712,32,15216,158192,136716,
33,24576,158208,138752,34,24768,158224,138756,35,24976,158240,138760,36,25168,158256,138764,37,25376,158272,140800,38,25568,158288,140804,39,25776,158304,140808,40,25968,158320,140812,41,26112,158336,142848,42,26304,158352,142852,43,26512,158368,142856,44,26704,158384,142860,45,26912,158400,144896,46,27104,158416,144900,47,27312,158432,144904,48,27504,158448,144908,
49,36864,158464,146944,50,37056,158480,146948,51,37264,158496,146952,52,37456,158512,146956,53,37664,158528,148992,54,37856,158544,148996,55,38064,158560,149000,56,38256,158576,149004,57,38400,158592,151040,58,38592,158608,151044,59,38800,158624,151048,60,38992,158640,151052,61,39200,158656,153088,62,39392,158672,153092,63,39600,158688,153096,64,39792,158704,153100,
147488,0,0,147456,147536,0,1,147504,147584,0,2,147552,147632,0,3,147600,147680,0,4,147648,147728,0,5,147696,147776,0,6,147744,147824,0,7,147792,149536,4,0,147840,149584,4,1,147888,149632,4,2,147936,149680,4,3,147984,149728,4,4,148032,149776,4,5,148080,149824,4,6,148128,149872,4,7,148176,
151584,8,0,148224,151632,8,1,148272,151680,8,2,148320,151728,8,3,148368,151776,8,4,148416,151824,8,5,148464,151872,8,6,148512,151920,8,7,148560,151584,8,0,148608,151632,8,1,148656,151680,8,2,148704,151728,8,3,148752,151776,8,4,148800,151824,8,5,148848,151872,8,6,148896,151920,8,7,148944,
151584,8,0,147456,151632,8,1,147504,151680,8,2,147552,151728,8,3,147600,151776,8,4,147648,151824,8,5,147696,151872,8,6,147744,151920,8,7,147792,151584,8,0,147840,151632,8,1,147888,151680,8,2,147936,151728,8,3,147984,151776,8,4,148032,151824,8,5,148080,151872,8,6,148128,151920,8,7,148176,
151584,8,0,148224,151632,8,1,148272,151680,8,2,148320,151728,8,3,148368,151776,8,4,148416,151824,8,5,148464,151872,8,6,148512,151920,8,7,148560,151584,8,0,148608,151632,8,1,148656,151680,8,2,148704,151728,8,3,148752,151776,8,4,148800,151824,8,5,148848,151872,8,6,148896,151920,8,7,148944};
__device__ __forceinline__ float bf_lo(unsigned u) { return __uint_as_float(u << 16); }
__device__ __forceinline__ float bf_hi(unsigned u) { return __uint_as_float(u & 0xffff0000u); }
__device__ __forceinline__ unsigned cvt_pk(float lo, float hi) { bf16x2 r = {(__bf16)lo, (__bf16)hi}; return __builtin_bit_cast(unsigned, r); }
__device__ __forceinline__ float rb(float v) { return bf_lo(cvt_pk(v, 0.f)); }
__global__ __launch_bounds__(256) void dcn_kernel(const unsigned short* x_, const unsigned short* off_, const unsigned short* w_, unsigned short* out_, void* ws_, Tiles tl_) {
    __shared__ __attribute__((aligned(128))) unsigned char lds[163840];
    asm volatile("" ::"v"((__attribute__((address_space(3))) unsigned char*)lds));
    unsigned tid = threadIdx.x, bid = blockIdx.x;
    const __attribute__((address_space(4))) Args* ka = (const __attribute__((address_space(4))) Args*)__builtin_amdgcn_kernarg_segment_ptr();
    asm volatile(
        "s_load_dwordx2 s[24:25], %1, 16\n\t"
        "s_load_dwordx2 s[16:17], %1, 0\n\t"
        "s_lshl_b32 s8, %2, 2\n\t"
        "s_add_u32 s8, s8, 40\n\t"
        "s_load_dword s7, %1, s8\n\t"
        "s_load_dwordx2 s[20:21], %1, 8\n\t"
        "s_load_dwordx2 s[28:29], %1, 24\n\t"
        "v_mov_b32 v156, %0\n\t"
        "s_mov_b32 s42, %2\n\t"
        "s_getpc_b64 s[44:45]\n\t"
        "s_and_b32 s44, s44, 0xffffff00\n\t"
        "9:\n\t"
        "s_mov_b32 s46, 99f-9b+3700\n\t"
        "s_mov_b32 s47, 0x20000\n\t"
        "v_lshlrev_b32 v160, 10, v156\n\t"
        "s_lshl_b32 s43, s42, 4\n\t"
        "s_and_b32 s43, s43, 0x380\n\t"
        "s_mov_b32 m0, 161856\n\t"
        "v_cmp_gt_u32 vcc, 64, v156\n\t"
        "s_nop 1\n\t"
        "s_cbranch_vccz 10f\n\t"
        "buffer_load_dword v160, s[44:47], s43 offen lds\n\t"
        "10:\n\t"
        "v_and_b32 v157, 63, v156\n\t"
        "v_lshlrev_b32 v140, 4, v157\n\t"
        "s_mov_b64 s[64:65], %3\n\t"
        "s_mov_b32 s66, 71680\n\t"
        "s_mov_b32 s67, 0x20000\n\t"
        "v_lshlrev_b32 v155, 2, v157\n\t"
        "s_lshl_b32 s70, s42, 8\n\t"
        "s_mov_b32 s13, 65536\n\t"
        "s_mov_b32 s69, 69632\n\t"
        "buffer_load_dwordx4 v[128:131], v140, s[64:67], s13 offen\n\t"
        "buffer_load_dword v155, v155, s[64:67], s70 offen\n\t"
        "buffer_load_dwordx4 v[80:83], v140, s[64:67], s69 offen offset:1024\n\t"
        "buffer_load_dwordx4 v[116:119], v140, s[64:67], s69 offen offset:0\n\t"
        "buffer_load_dwordx4 v[144:147], v140, s[64:67], s13 offen offset:1024\n\t"
        "buffer_load_dwordx4 v[168:171], v140, s[64:67], s13 offen offset:2048\n\t"
        "buffer_load_dwordx4 v[184:187], v140, s[64:67], s13 offen offset:3072\n\t"
        "v_lshrrev_b32 v73, 6, v156\n\t"
        "v_and_b32 v158, 15, v156\n\t"
        "v_readfirstlane_b32 s4, v73\n\t"
        "s_mov_b32 s18, 9633792\n\t"
        "s_mov_b32 s19, 0x20000\n\t"
        "s_mov_b32 s26, 165888\n\t"
        "s_mov_b32 s27, 0x20000\n\t"
        "v_xor_b32 v142, 32, v140\n\t"
        "v_and_b32 v73, 32, v157\n\t"
        "v_xor_b32 v141, v73, v140\n\t"
        "s_lshr_b32 s68, s4, 1\n\t"
        "s_and_b32 s49, s4, 1\n\t"
        "s_mul_i32 s11, s49, 3072\n\t"
        "s_mul_i32 s36, s68, -55296\n\t"
        "s_add_u32 s36, s36, s11\n\t"
        "s_add_u32 s36, s36, 55296\n\t"
        "s_mul_i32 s37, s4, 3072\n\t"
        "s_add_u32 s37, s37, 86016\n\t"
        "s_lshl_b32 s8, s49, 5\n\t"
        "v_xor_b32 v152, s8, v140\n\t"
        "v_mul_u32_u24 v71, 171, v156\n\t"
        "v_lshrrev_b32 v71, 11, v71\n\t"
        "v_mul_u32_u24 v74, 12, v71\n\t"
        "v_sub_u32 v74, v156, v74\n\t"
        "v_lshrrev_b32 v75, 1, v71\n\t"
        "v_and_b32 v75, 2, v75\n\t"
        "v_xor_b32 v74, v74, v75\n\t"
        "v_lshlrev_b32 v72, 4, v74\n\t"
        "v_add_u32 v73, -1, v71\n\t"
        "v_cmp_lt_u32 vcc, 17, v73\n\t"
        "v_mov_b32 v75, 0x7ff00000\n\t"
        "v_add_u32 v161, -2, v71\n\t"
        "v_cndmask_b32 v162, v72, v75, vcc\n\t"
        "s_movk_i32 s9, 192\n\t"
        "s_lshl_b32 s35, s4, 10\n\t"
        "s_add_u32 m0, s37, 12288\n\t"
        "s_waitcnt lgkmcnt(0)\n\t"
        "buffer_load_dwordx4 v140, s[24:27], s36 offen offset:0 lds\n\t"
        "buffer_load_dwordx4 v141, s[24:27], s36 offen offset:1024 lds\n\t"
        "buffer_load_dwordx4 v142, s[24:27], s36 offen offset:2048 lds\n\t"
        "s_bfe_u32 s6, s7, 0x80008\n\t"
        "s_and_b32 s8, s7, 255\n\t"
        "v_add_u32 v161, s6, v161\n\t"
        "v_min_u32 v161, 223, v161\n\t"
        "s_mul_i32 s34, s8, 43008\n\t"
        "v_mad_u32_u24 v143, v161, s9, v162\n\t"
        "s_sub_u32 s34, s34, 43008\n\t"
        "s_mul_i32 s41, s8, 224\n\t"
        "s_add_u32 s41, s41, s6\n\t"
        "s_add_u32 m0, s35, 8192\n\t"
        "s_add_u32 s43, s34, 86016\n\t"
        "buffer_load_dwordx4 v143, s[16:19], s43 offen  lds\n\t"
        "s_add_u32 m0, s35, 24576\n\t"
        "s_add_u32 s43, s34, 258048\n\t"
        "buffer_load_dwordx4 v143, s[16:19], s43 offen  lds\n\t"
        "s_mul_i32 s43, s4, 1536\n\t"
        "s_add_u32 m0, s43, 110592\n\t"
        "s_add_u32 s43, s43, 110592\n\t"
        "buffer_load_dwordx4 v152, s[24:27], s43 offen lds\n\t"
        "buffer_load_dwordx4 v152, s[24:27], s43 offen offset:512 lds\n\t"
        "s_add_u32 m0, s35, 12288\n\t"
        "s_add_u32 s43, s34, 129024\n\t"
        "buffer_load_dwordx4 v143, s[16:19], s43 offen  lds\n\t"
        "s_add_u32 m0, s35, 28672\n\t"
        "s_add_u32 s43, s34, 301056\n\t"
        "s_min_u32 s43, s43, 9590784\n\t"
        "buffer_load_dwordx4 v143, s[16:19], s43 offen  lds\n\t"
        "s_add_u32 m0, s37, 36864\n\t"
        "s_add_u32 s36, s36, 6144\n\t"
        "buffer_load_dwordx4 v140, s[24:27], s36 offen offset:0 lds\n\t"
        "buffer_load_dwordx4 v141, s[24:27], s36 offen offset:1024 lds\n\t"
        "s_mul_i32 s39, s68, 6144\n\t"
        "s_add_u32 s39, s39, 55296\n\t"
        "s_mul_i32 s38, s68, 159744\n\t"
        "s_add_u32 s38, s38, -104448\n\t"
        "s_mul_i32 s5, s68, -165888\n\t"
        "s_add_u32 s5, s5, 61440\n\t"
        "s_mov_b32 s22, 1806336\n\t"
        "s_mov_b32 s23, 0x20000\n\t"
        "s_mov_b32 s30, 9633792\n\t"
        "s_mov_b32 s31, 0x20000\n\t"
        "s_mov_b32 s54, 0x7fff7fff\n\t"
        "s_lshl_b32 s8, s4, 2\n\t"
        "s_add_u32 s8, s8, 161792\n\t"
        "v_mov_b32 v153, s8\n\t"
        "v_mov_b32 v182, 161792\n\t"
        "v_mov_b32 v76, 0\n\t"
        "v_mov_b32 v77, 0\n\t"
        "v_lshlrev_b32 v73, 3, v156\n\t"
        "v_add_u32 v73, 147456, v73\n\t"
        "s_add_u32 s60, s35, 153600\n\t"
        "v_mov_b32 v74, s60\n\t"
        "s_cmp_lt_u32 s4, 3\n\t"
        "s_cselect_b64 exec, -1, 0\n\t"
        "ds_write_b64 v73, v[76:77] offset:0\n\t"
        "ds_write_b64 v73, v[76:77] offset:2048\n\t"
        "ds_write_b64 v73, v[76:77] offset:4096\n\t"
        "s_mov_b64 exec, 1\n\t"
        "ds_write_b32 v153, v76 offset:16\n\t"
        "ds_write_b64 v74, v[76:77]\n\t"
        "s_mov_b64 exec, -1\n\t"
        "v_bfe_i32 v74, v156, 5, 1\n\t"
        "v_and_b32 v74, 7, v74\n\t"
        "v_xor_b32 v74, s4, v74\n\t"
        "v_mul_u32_u24 v68, 11, v74\n\t"
        "v_lshrrev_b32 v68, 5, v68\n\t"
        "v_mul_u32_u24 v75, 3, v68\n\t"
        "v_sub_u32 v69, v74, v75\n\t"
        "v_lshlrev_b32 v70, 2, v74\n\t"
        "v_bfe_u32 v52, v156, 4, 1\n\t"
        "v_and_b32 v181, 48, v156\n\t"
        "s_mov_b64 s[56:57], 0\n\t"
        "s_mov_b32 s63, 0x435f0000\n\t"
        "s_bfe_u32 s8, s7, 0x20010\n\t"
        "s_cmp_eq_u32 s8, 0\n\t"
        "s_cbranch_scc1 20f\n\t"
        "s_mul_i32 s69, s68, 16384\n\t"
        "buffer_load_dwordx4 v142, s[24:27], s36 offen offset:2048 lds\n\t"
        "s_lshl_b32 s70, s49, 7\n\t"
        "s_lshl_b32 s71, s49, 5\n\t"
        "s_waitcnt vmcnt(18)\n\t"
        "v_add_u32 v128, s69, v128\n\t"
        "v_add_u32 v129, s69, v129\n\t"
        "v_add_u32 v130, s69, v130\n\t"
        "v_xad_u32 v133, v131, s71, 64\n\t"
        "v_add_u32 v131, s70, v131\n\t"
        "v_xor_b32 v132, 32, v131\n\t"
        "s_waitcnt vmcnt(16)\n\t"
        "ds_bpermute_b32 v67, v81, v155 offset:172\n\t"
        "s_add_u32 m0, s35, 4096\n\t"
        "s_add_u32 s43, s34, 43008\n\t"
        "buffer_load_dwordx4 v143, s[16:19], s43 offen  lds\n\t"
        "ds_bpermute_b32 v78, v81, v155 offset:148\n\t"
        "ds_bpermute_b32 v79, v81, v155 offset:160\n\t"
        "v_readlane_b32 s69, v155, 34\n\t"
        "v_readlane_b32 s70, v155, 35\n\t"
        "v_readlane_b32 s46, v155, 43\n\t"
        "v_readlane_b32 s47, v155, 44\n\t"
        "v_readlane_b32 s55, v155, 45\n\t"
        "s_lshl_b32 s71, s49, 3\n\t"
        "v_add_u32 v82, s71, v82\n\t"
        "s_add_u32 m0, s35, 20480\n\t"
        "s_add_u32 s43, s34, 215040\n\t"
        "buffer_load_dwordx4 v143, s[16:19], s43 offen  lds\n\t"
        "v_add_u32 v53, v70, v83\n\t"
        "v_mad_u32_u24 v70, v52, s69, v70\n\t"
        "v_mad_u32_u24 v70, v158, s70, v70\n\t"
        "s_mul_i32 s71, s69, s68\n\t"
        "s_waitcnt lgkmcnt(2)\n\t"
        "v_mad_u32_u24 v67, v82, s70, v67\n\t"
        "v_add_u32 v67, s71, v67\n\t"
        "v_readlane_b32 s11, v155, 30\n\t"
        "s_movk_i32 s10, 223\n\t"
        "v_mov_b32 v75, 0x7ff00000\n\t"
        "v_readlane_b32 s12, v155, 24\n\t"
        "v_readlane_b32 s13, v155, 25\n\t"
        "v_readlane_b32 s70, v155, 27\n\t"
        "s_nop 1\n\t"
        "s_or_b32 s42, s42, s70\n\t"
        "s_bfe_u32 s70, s12, 524288\n\t"
        "s_bfe_u32 s71, s13, 524288\n\t"
        "v_cmp_gt_u32 vcc, s71, v71\n\t"
        "v_add3_u32 v73, v71, s70, -2\n\t"
        "v_med3_i32 v73, v73, 0, s10\n\t"
        "v_cndmask_b32 v74, v75, v72, vcc\n\t"
        "v_mad_u32_u24 v134, v73, s11, v74\n\t"
        "s_bfe_u32 s70, s12, 524296\n\t"
        "s_bfe_u32 s71, s13, 524296\n\t"
        "v_cmp_gt_u32 vcc, s71, v71\n\t"
        "v_add3_u32 v73, v71, s70, -2\n\t"
        "v_med3_i32 v73, v73, 0, s10\n\t"
        "v_cndmask_b32 v74, v75, v72, vcc\n\t"
        "v_mad_u32_u24 v135, v73, s11, v74\n\t"
        "s_bfe_u32 s70, s12, 524304\n\t"
        "s_bfe_u32 s71, s13, 524304\n\t"
        "v_cmp_gt_u32 vcc, s71, v71\n\t"
        "v_add3_u32 v73, v71, s70, -2\n\t"
        "v_med3_i32 v73, v73, 0, s10\n\t"
        "v_cndmask_b32 v74, v75, v72, vcc\n\t"
        "v_mad_u32_u24 v136, v73, s11, v74\n\t"
        "s_bfe_u32 s70, s12, 524312\n\t"
        "s_bfe_u32 s71, s13, 524312\n\t"
        "v_cmp_gt_u32 vcc, s71, v71\n\t"
        "v_add3_u32 v73, v71, s70, -2\n\t"
        "v_med3_i32 v73, v73, 0, s10\n\t"
        "v_cndmask_b32 v74, v75, v72, vcc\n\t"
        "v_mad_u32_u24 v137, v73, s11, v74\n\t"
        "v_readlane_b32 s44, v155, 32\n\t"
        "v_readlane_b32 s45, v155, 33\n\t"
        "v_readlane_b32 s62, v155, 36\n\t"
        "s_mov_b32 exec_lo, 0\n\t"
        "s_mov_b32 exec_hi, 0xfff00000\n\t"
        "s_waitcnt vmcnt(17)\n\t"
        "ds_write_b32 v119, v155\n\t"
        "s_mov_b64 exec, -1\n\t"
        "s_lshl_b32 s70, s62, 5\n\t"
        "s_sub_u32 s71, s70, 16384\n\t"
        "s_mul_i32 s71, s71, s68\n\t"
        "v_add_u32 v172, s71, v128\n\t"
        "v_add_u32 v173, s71, v129\n\t"
        "v_add_u32 v174, s71, v130\n\t"
        "s_add_u32 s71, s71, s70\n\t"
        "v_add_u32 v175, s71, v128\n\t"
        "v_add_u32 v176, s71, v129\n\t"
        "v_add_u32 v177, s71, v130\n\t"
        "s_add_u32 s71, s71, s70\n\t"
        "v_add_u32 v178, s71, v128\n\t"
        "v_add_u32 v179, s71, v129\n\t"
        "v_add_u32 v180, s71, v130\n\t"
        "s_cmp_eq_u32 s69, 36\n\t"
        "s_cselect_b64 exec, -1, 0\n\t"
        "v_swap_b32 v68, v69\n\t"
        "v_swap_b32 v173, v175\n\t"
        "v_swap_b32 v174, v178\n\t"
        "v_swap_b32 v177, v179\n\t"
        "s_mov_b64 exec, -1\n\t"
        "v_add3_u32 v68, v68, v52, -1\n\t"
        "v_add3_u32 v69, v69, v158, -1\n\t"
        "v_cvt_f32_i32 v68, v68\n\t"
        "v_cvt_f32_i32 v69, v69\n\t"
        "s_add_u32 s70, s68, 1\n\t"
        "v_cvt_f32_u32 v73, s70\n\t"
        "s_waitcnt lgkmcnt(1)\n\t"
        "v_add_f32 v78, v78, v73\n\t"
        "v_add_u32 v73, 1, v82\n\t"
        "v_cvt_f32_u32 v73, v73\n\t"
        "v_add_f32 v79, v79, v73\n\t"
        "s_mul_i32 s70, s4, 384\n\t"
        "v_add_u32 v80, s70, v80\n\t"
        "s_waitcnt vmcnt(9) lgkmcnt(0)\n\t"
        "s_barrier\n\t"
        "ds_read_b64_tr_b16 a[0:1], v131 offset:12288\n\t"
        "ds_read_b64_tr_b16 a[2:3], v131 offset:13056\n\t"
        "ds_read_b64_tr_b16 a[4:5], v132 offset:12288\n\t"
        "ds_read_b64_tr_b16 a[6:7], v132 offset:13056\n\t"
        "ds_read_b64_tr_b16 a[8:9], v133 offset:12288\n\t"
        "ds_read_b64_tr_b16 a[10:11], v133 offset:13056\n\t"
        "ds_read_b128 v[84:87], v128 offset:8192\n\t"
        "ds_read_b64_tr_b16 a[12:13], v131 offset:18432\n\t"
        "ds_read_b64_tr_b16 a[14:15], v131 offset:19200\n\t"
        "ds_read_b64_tr_b16 a[16:17], v132 offset:18432\n\t"
        "ds_read_b64_tr_b16 a[18:19], v132 offset:19200\n\t"
        "ds_read_b64_tr_b16 a[20:21], v133 offset:18432\n\t"
        "ds_read_b64_tr_b16 a[22:23], v133 offset:19200\n\t"
        "ds_read_b128 v[88:91], v128 offset:8192\n\t"
        "s_add_u32 m0, s37, 49152\n\t"
        "s_add_u32 s36, s36, s39\n\t"
        "s_waitcnt lgkmcnt(7)\n\t"
        "v_mfma_f32_16x16x32_bf16 v[12:15], a[0:3], v[84:87], 0\n\t"
        "buffer_load_dwordx4 v140, s[24:27], s36 offen offset:0 lds\n\t"
        "buffer_load_dwordx4 v141, s[24:27], s36 offen offset:1024 lds\n\t"
        "buffer_load_dwordx4 v142, s[24:27], s36 offen offset:2048 lds\n\t"
        "s_add_u32 m0, s35, 16384\n\t"
        "s_add_u32 s43, s34, 172032\n\t"
        "buffer_load_dwordx4 v143, s[16:19], s43 offen  lds\n\t"
        "s_add_u32 m0, s35, 32768\n\t"
        "s_add_u32 s43, s34, 344064\n\t"
        "s_min_u32 s43, s43, 9590784\n\t"
        "buffer_load_dwordx4 v143, s[16:19], s43 offen  lds\n\t"
        "s_waitcnt vmcnt(10)\n\t"
        "s_barrier\n\t"
        "ds_read_b128 v[92:95], v128 offset:12288\n\t"
        "s_add_u32 m0, s37, 0\n\t"
        "ds_read_b64_tr_b16 a[24:25], v131 offset:24576\n\t"
        "s_add_u32 s36, s36, s38\n\t"
        "v_mfma_f32_16x16x32_bf16 v[16:19], a[4:7], v[84:87], 0\n\t"
        "buffer_load_dwordx4 v140, s[24:27], s36 offen offset:0 lds\n\t"
        "ds_read_b64_tr_b16 a[26:27], v131 offset:25344\n\t"
        "v_lshrrev_b32 v73, 5, v156\n\t"
        "v_mfma_f32_16x16x32_bf16 v[20:23], a[8:11], v[84:87], 0\n\t"
        "buffer_load_dwordx4 v141, s[24:27], s36 offen offset:1024 lds\n\t"
        "ds_read_b64_tr_b16 a[28:29], v132 offset:24576\n\t"
        "v_mul_u32_u24 v73, 8064, v73\n\t"
        "s_waitcnt lgkmcnt(4)\n\t"
        "v_mfma_f32_16x16x32_bf16 v[24:27], a[12:15], v[88:91], 0\n\t"
        "ds_read_b64_tr_b16 a[30:31], v132 offset:25344\n\t"
        "ds_read_b64_tr_b16 a[32:33], v133 offset:24576\n\t"
        "ds_read_b64_tr_b16 a[34:35], v133 offset:25344\n\t"
        "v_and_b32 v55, 496, v140\n\t"
        "v_mfma_f32_16x16x32_bf16 v[28:31], a[16:19], v[88:91], 0\n\t"
        "buffer_load_dwordx4 v142, s[24:27], s36 offen offset:2048 lds\n\t"
        "ds_read_b128 v[96:99], v128 offset:8192\n\t"
        "v_add_u32 v55, v55, v73\n\t"
        "v_mfma_f32_16x16x32_bf16 v[32:35], a[20:23], v[88:91], 0\n\t"
        "ds_read_b128 v[100:103], v128 offset:12288\n\t"
        "v_lshl_add_u32 v61, v158, 2, v73\n\t"
        "s_waitcnt lgkmcnt(8)\n\t"
        "v_mfma_f32_16x16x32_bf16 v[24:27], a[0:3], v[92:95], v[24:27]\n\t"
        "s_mul_i32 s48, s41, 36\n\t"
        "v_readlane_b32 s69, v155, 31\n\t"
        "v_mfma_f32_16x16x32_bf16 v[28:31], a[4:7], v[92:95], v[28:31]\n\t"
        "buffer_load_dword v67, v67, s[20:23], 0 offen offset:32\n\t"
        "v_mov_b32 v154, s69\n\t"
        "v_mfma_f32_16x16x32_bf16 v[32:35], a[8:11], v[92:95], v[32:35]\n\t"
        "v_mov_b32 v119, s62\n\t"
        "v_add_u32 v117, s37, v117\n\t"
        "v_mfma_f32_16x16x32_bf16 v[36:39], a[12:15], v[92:95], 0\n\t"
        "v_add_u32 v118, s35, v118\n\t"
        "v_add_u32 v185, s35, v185\n\t"
        "v_mfma_f32_16x16x32_bf16 v[40:43], a[16:19], v[92:95], 0\n\t"
        "v_add_u32 v186, s37, v186\n\t"
        "v_readlane_b32 s53, v155, 28\n\t"
        "v_mfma_f32_16x16x32_bf16 v[44:47], a[20:23], v[92:95], 0\n\t"
        "s_waitcnt vmcnt(9) lgkmcnt(2)\n\t"
        "s_barrier\n\t"
        "buffer_load_dword v64, v70, s[20:23], s46 offen\n\t"
        "ds_read_b128 v[104:107], v128 offset:4096\n\t"
        "s_waitcnt lgkmcnt(2)\n\t"
        "v_mfma_f32_16x16x32_bf16 v[0:3], a[24:27], v[96:99], 0\n\t"
        "ds_read_b64_tr_b16 a[36:37], v131 offset:36864\n\t"
        "ds_read_b64_tr_b16 a[38:39], v131 offset:37632\n\t"
        "s_lshl_b32 s69, s68, 10\n\t"
        "v_mfma_f32_16x16x32_bf16 v[4:7], a[28:31], v[96:99], 0\n\t"
        "buffer_load_dword v66, v70, s[20:23], s55 offen\n\t"
        "s_add_u32 m0, s37, 12288\n\t"
        "ds_read_b64_tr_b16 a[40:41], v132 offset:36864\n\t"
        "s_add_u32 s36, s36, s5\n\t"
        "v_mfma_f32_16x16x32_bf16 v[8:11], a[32:35], v[96:99], 0\n\t"
        "ds_read_b64_tr_b16 a[42:43], v132 offset:37632\n\t"
        "ds_read_b64_tr_b16 a[44:45], v133 offset:36864\n\t"
        "s_add_u32 s69, s69, 135168\n\t"
        "s_waitcnt lgkmcnt(6)\n\t"
        "v_mfma_f32_16x16x32_bf16 v[12:15], a[24:27], v[100:103], v[12:15]\n\t"
        "ds_read_b64_tr_b16 a[46:47], v133 offset:37632\n\t"
        "ds_read_b64_tr_b16 a[48:49], v131 offset:43008\n\t"
        "ds_read_b64_tr_b16 a[50:51], v131 offset:43776\n\t"
        "s_lshl_b32 s70, s49, 5\n\t"
        "v_mfma_f32_16x16x32_bf16 v[16:19], a[28:31], v[100:103], v[16:19]\n\t"
        "buffer_load_dwordx4 v140, s[24:27], s36 offen offset:0 lds\n\t"
        "ds_read_b64_tr_b16 a[52:53], v132 offset:43008\n\t"
        "ds_read_b64_tr_b16 a[54:55], v132 offset:43776\n\t"
        "v_xor_b32 v146, s70, v146\n\t"
        "v_mfma_f32_16x16x32_bf16 v[20:23], a[32:35], v[100:103], v[20:23]\n\t"
        "ds_read_b64_tr_b16 a[56:57], v133 offset:43008\n\t"
        "ds_read_b64_tr_b16 a[58:59], v133 offset:43776\n\t"
        "ds_read_b128 v[108:111], v128 offset:4160\n\t"
        "v_add_u32 v146, s69, v146\n\t"
        "s_waitcnt lgkmcnt(12)\n\t"
        "v_mfma_f32_16x16x32_bf16 v[0:3], a[0:3], v[104:107], v[0:3]\n\t"
        "s_lshl_b32 s70, s49, 11\n\t"
        "s_add_u32 s70, s70, s69\n\t"
        "v_mfma_f32_16x16x32_bf16 v[4:7], a[4:7], v[104:107], v[4:7]\n\t"
        "buffer_load_dwordx4 v141, s[24:27], s36 offen offset:1024 lds\n\t"
        "v_add_u32 v147, s70, v147\n\t"
        "v_mfma_f32_16x16x32_bf16 v[8:11], a[8:11], v[104:107], v[8:11]\n\t"
        "s_mul_i32 s41, s41, 192\n\t"
        "s_mul_i32 s69, s68, 172032\n\t"
        "v_mfma_f32_16x16x32_bf16 v[12:15], a[12:15], v[104:107], v[12:15]\n\t"
        "s_add_u32 s41, s41, s69\n\t"
        "s_mul_i32 s69, s49, 43008\n\t"
        "v_mfma_f32_16x16x32_bf16 v[16:19], a[16:19], v[104:107], v[16:19]\n\t"
        "s_add_u32 s50, s41, s69\n\t"
        "s_lshl_b32 s70, s49, 7\n\t"
        "v_mfma_f32_16x16x32_bf16 v[20:23], a[20:23], v[104:107], v[20:23]\n\t"
        "v_add_u32 v144, s70, v144\n\t"
        "s_mul_i32 s69, s68, 768\n\t"
        "s_waitcnt lgkmcnt(0)\n\t"
        "v_mfma_f32_16x16x32_bf16 v[0:3], a[36:39], v[108:111], v[0:3]\n\t"
        "buffer_load_dwordx4 v142, s[24:27], s36 offen offset:2048 lds\n\t"
        "s_add_u32 m0, s35, 0\n\t"
        "ds_read_b128 v[112:115], v128 offset:8256\n\t"
        "s_add_u32 s43, s34, 0\n\t"
        "v_mfma_f32_16x16x32_bf16 v[4:7], a[40:43], v[108:111], v[4:7]\n\t"
        "v_add_u32 v187, s69, v187\n\t"
        "v_readlane_b32 s9, v155, 46\n\t"
        "v_mfma_f32_16x16x32_bf16 v[8:11], a[44:47], v[108:111], v[8:11]\n\t"
        "s_sub_u32 s10, 43200, s11\n\t"
        "s_mul_i32 s10, s10, s68\n\t"
        "v_mfma_f32_16x16x32_bf16 v[12:15], a[48:51], v[108:111], v[12:15]\n\t"
        "s_sub_u32 s11, s11, 192\n\t"
        "v_mad_u32_u24 v148, v158, s11, v144\n\t"
        "v_mfma_f32_16x16x32_bf16 v[16:19], a[52:55], v[108:111], v[16:19]\n\t"
        "v_add_u32 v148, s10, v148\n\t"
        "v_lshrrev_b32 v73, 2, v157\n\t"
        "v_mfma_f32_16x16x32_bf16 v[20:23], a[56:59], v[108:111], v[20:23]\n\t"
        "v_mad_u32_u24 v150, v73, s11, v145\n\t"
        "s_lshl_b32 s70, s49, 1\n\t"
        "s_waitcnt lgkmcnt(0)\n\t"
        "v_mfma_f32_16x16x32_bf16 v[12:15], a[36:39], v[112:115], v[12:15]\n\t"
        "ds_read_b128 v[84:87], v128 offset:12352\n\t"
        "s_add_u32 s70, s70, 49\n\t"
        "v_mfma_f32_16x16x32_bf16 v[16:19], a[40:43], v[112:115], v[16:19]\n\t"
        "v_readlane_b32 s52, v155, s70\n\t"
        "v_mfma_f32_16x16x32_bf16 v[20:23], a[44:47], v[112:115], v[20:23]\n\t"
        "s_nop 0\n\t"
        "s_add_u32 s52, s52, s10\n\t"
        "v_mfma_f32_16x16x32_bf16 v[24:27], a[48:51], v[112:115], v[24:27]\n\t"
        "buffer_load_dwordx4 v143, s[16:19], s43 offen  lds\n\t"
        "s_add_u32 m0, s35, 36864\n\t"
        "s_add_u32 s43, s34, 387072\n\t"
        "s_min_u32 s43, s43, 9590784\n\t"
        "v_mfma_f32_16x16x32_bf16 v[28:31], a[52:55], v[112:115], v[28:31]\n\t"
        "v_mfma_f32_16x16x32_bf16 v[32:35], a[56:59], v[112:115], v[32:35]\n\t"
        "buffer_load_dwordx4 v143, s[16:19], s43 offen  lds\n\t"
        "s_waitcnt vmcnt(11)\n\t"
        "s_barrier\n\t"
        "ds_read_b128 v[88:91], v128 offset:16384\n\t"
        "s_add_u32 m0, s37, 24576\n\t"
        "ds_read_b64_tr_b16 a[60:61], v131 offset:49152\n\t"
        "s_add_u32 s36, s36, s39\n\t"
        "s_waitcnt lgkmcnt(2)\n\t"
        "v_mfma_f32_16x16x32_bf16 v[24:27], a[36:39], v[84:87], v[24:27]\n\t"
        "buffer_load_dwordx4 v140, s[24:27], s36 offen offset:0 lds\n\t"
        "ds_read_b64_tr_b16 a[62:63], v131 offset:49920\n\t"
        "v_mfma_f32_16x16x32_bf16 v[28:31], a[40:43], v[84:87], v[28:31]\n\t"
        "ds_read_b64_tr_b16 a[64:65], v132 offset:49152\n\t"
        "ds_read_b64_tr_b16 a[66:67], v132 offset:49920\n\t"
        "v_mfma_f32_16x16x32_bf16 v[32:35], a[44:47], v[84:87], v[32:35]\n\t"
        "buffer_load_dwordx4 v141, s[24:27], s36 offen offset:1024 lds\n\t"
        "ds_read_b64_tr_b16 a[68:69], v133 offset:49152\n\t"
        "v_mfma_f32_16x16x32_bf16 v[36:39], a[48:51], v[84:87], v[36:39]\n\t"
        "ds_read_b64_tr_b16 a[70:71], v133 offset:49920\n\t"
        "ds_read_b128 v[92:95], v128 offset:8256\n\t"
        "v_mfma_f32_16x16x32_bf16 v[40:43], a[52:55], v[84:87], v[40:43]\n\t"
        "ds_read_b128 v[96:99], v128 offset:12352\n\t"
        "ds_read_b128 v[100:103], v128 offset:16448\n\t"
        "v_mfma_f32_16x16x32_bf16 v[44:47], a[56:59], v[84:87], v[44:47]\n\t"
        "ds_read_b64_tr_b16 a[72:73], v131 offset:55296\n\t"
        "ds_read_b64_tr_b16 a[74:75], v131 offset:56064\n\t"
        "s_waitcnt lgkmcnt(11)\n\t"
        "v_mfma_f32_16x16x32_bf16 v[24:27], a[24:27], v[88:91], v[24:27]\n\t"
        "buffer_load_dwordx4 v142, s[24:27], s36 offen offset:2048 lds\n\t"
        "ds_read_b64_tr_b16 a[76:77], v132 offset:55296\n\t"
        "v_mfma_f32_16x16x32_bf16 v[28:31], a[28:31], v[88:91], v[28:31]\n\t"
        "ds_read_b64_tr_b16 a[78:79], v132 offset:56064\n\t"
        "ds_read_b64_tr_b16 a[80:81], v133 offset:55296\n\t"
        "v_mfma_f32_16x16x32_bf16 v[32:35], a[32:35], v[88:91], v[32:35]\n\t"
        "ds_read_b64_tr_b16 a[82:83], v133 offset:56064\n\t"
        "v_mfma_f32_16x16x32_bf16 v[36:39], a[0:3], v[88:91], v[36:39]\n\t"
        "v_mfma_f32_16x16x32_bf16 v[40:43], a[4:7], v[88:91], v[40:43]\n\t"
        "buffer_load_dwordx4 v[48:51], v55, s[20:23], s48 offen\n\t"
        "v_mfma_f32_16x16x32_bf16 v[44:47], a[8:11], v[88:91], v[44:47]\n\t"
        "s_waitcnt lgkmcnt(8)\n\t"
        "v_mfma_f32_16x16x32_bf16 v[0:3], a[60:63], v[92:95], v[0:3]\n\t"
        "v_mfma_f32_16x16x32_bf16 v[4:7], a[64:67], v[92:95], v[4:7]\n\t"
        "v_mfma_f32_16x16x32_bf16 v[8:11], a[68:71], v[92:95], v[8:11]\n\t"
        "buffer_load_dword v61, v61, s[20:23], s48 offen offset:512\n\t"
        "s_add_u32 m0, s37, 36864\n\t"
        "s_add_u32 s36, s36, s38\n\t"
        "s_waitcnt lgkmcnt(4)\n\t"
        "v_mfma_f32_16x16x32_bf16 v[12:15], a[60:63], v[96:99], v[12:15]\n\t"
        "v_mfma_f32_16x16x32_bf16 v[16:19], a[64:67], v[96:99], v[16:19]\n\t"
        "v_mfma_f32_16x16x32_bf16 v[20:23], a[68:71], v[96:99], v[20:23]\n\t"
        "v_mfma_f32_16x16x32_bf16 v[24:27], a[60:63], v[100:103], v[24:27]\n\t"
        "ds_read_b128 v[104:107], v128 offset:4224\n\t"
        "v_mfma_f32_16x16x32_bf16 v[28:31], a[64:67], v[100:103], v[28:31]\n\t"
        "v_mfma_f32_16x16x32_bf16 v[32:35], a[68:71], v[100:103], v[32:35]\n\t"
        "v_mfma_f32_16x16x32_bf16 v[36:39], a[36:39], v[100:103], v[36:39]\n\t"
        "ds_read_b128 v[108:111], v128 offset:8320\n\t"
        "v_mfma_f32_16x16x32_bf16 v[40:43], a[40:43], v[100:103], v[40:43]\n\t"
        "v_mfma_f32_16x16x32_bf16 v[44:47], a[44:47], v[100:103], v[44:47]\n\t"
        "s_waitcnt lgkmcnt(1)\n\t"
        "v_mfma_f32_16x16x32_bf16 v[0:3], a[72:75], v[104:107], v[0:3]\n\t"
        "s_waitcnt vmcnt(13)\n\t"
        "s_barrier\n\t"
        "buffer_load_dwordx4 v140, s[24:27], s36 offen offset:0 lds\n\t"
        "ds_read_b128 v[112:115], v128 offset:12416\n\t"
        "ds_read_b64_tr_b16 a[84:85], v131 offset:0\n\t"
        "v_mfma_f32_16x16x32_bf16 v[4:7], a[76:79], v[104:107], v[4:7]\n\t"
        "ds_read_b64_tr_b16 a[86:87], v131 offset:768\n\t"
        "ds_read_b64_tr_b16 a[88:89], v132 offset:0\n\t"
        "ds_read_b64_tr_b16 a[90:91], v132 offset:768\n\t"
        "v_mfma_f32_16x16x32_bf16 v[8:11], a[80:83], v[104:107], v[8:11]\n\t"
        "ds_read_b64_tr_b16 a[92:93], v133 offset:0\n\t"
        "ds_read_b64_tr_b16 a[94:95], v133 offset:768\n\t"
        "ds_read_b128 v[84:87], v128 offset:4224\n\t"
        "s_waitcnt lgkmcnt(8)\n\t"
        "v_mfma_f32_16x16x32_bf16 v[12:15], a[72:75], v[108:111], v[12:15]\n\t"
        "ds_read_b64_tr_b16 a[96:97], v131 offset:6144\n\t"
        "ds_read_b64_tr_b16 a[98:99], v131 offset:6912\n\t"
        "ds_read_b64_tr_b16 a[100:101], v132 offset:6144\n\t"
        "v_mfma_f32_16x16x32_bf16 v[16:19], a[76:79], v[108:111], v[16:19]\n\t"
        "buffer_load_dwordx4 v141, s[24:27], s36 offen offset:1024 lds\n\t"
        "ds_read_b64_tr_b16 a[102:103], v132 offset:6912\n\t"
        "ds_read_b64_tr_b16 a[104:105], v133 offset:6144\n\t"
        "v_mfma_f32_16x16x32_bf16 v[20:23], a[80:83], v[108:111], v[20:23]\n\t"
        "ds_read_b64_tr_b16 a[106:107], v133 offset:6912\n\t"
        "ds_read_b128 v[88:91], v128 offset:8320\n\t"
        "s_waitcnt lgkmcnt(14)\n\t"
        "v_mfma_f32_16x16x32_bf16 v[24:27], a[72:75], v[112:115], v[24:27]\n\t"
        "ds_read_b128 v[92:95], v128 offset:12416\n\t"
        "v_mfma_f32_16x16x32_bf16 v[28:31], a[76:79], v[112:115], v[28:31]\n\t"
        "ds_read_b128 v[96:99], v128 offset:16512\n\t"
        "v_mfma_f32_16x16x32_bf16 v[32:35], a[80:83], v[112:115], v[32:35]\n\t"
        "buffer_load_dwordx4 v142, s[24:27], s36 offen offset:2048 lds\n\t"
        "v_readlane_b32 s40, v155, 12\n\t"
        "v_readlane_b32 s43, v155, 0\n\t"
        "s_waitcnt lgkmcnt(9)\n\t"
        "v_mfma_f32_16x16x32_bf16 v[12:15], a[84:87], v[84:87], v[12:15]\n\t"
        "s_add_u32 m0, s40, s35\n\t"
        "v_mfma_f32_16x16x32_bf16 v[16:19], a[88:91], v[84:87], v[16:19]\n\t"
        "v_mfma_f32_16x16x32_bf16 v[20:23], a[92:95], v[84:87], v[20:23]\n\t"
        "v_readlane_b32 s46, v155, 37\n\t"
        "v_readlane_b32 s47, v155, 40\n\t"
        "s_waitcnt vmcnt(14)\n\t"
        "v_perm_b32 v56, v64, v64, s44\n\t"
        "s_waitcnt lgkmcnt(2)\n\t"
        "v_mfma_f32_16x16x32_bf16 v[0:3], a[96:99], v[88:91], v[0:3]\n\t"
        "buffer_load_dwordx4 v134, s[16:19], s43 offen  lds\n\t"
        "v_readlane_b32 s40, v155, 13\n\t"
        "v_mfma_f32_16x16x32_bf16 v[4:7], a[100:103], v[88:91], v[4:7]\n\t"
        "v_readlane_b32 s43, v155, 1\n\t"
        "s_add_u32 m0, s40, s35\n\t"
        "v_mfma_f32_16x16x32_bf16 v[8:11], a[104:107], v[88:91], v[8:11]\n\t"
        "v_perm_b32 v57, v64, v64, s45\n\t"
        "v_add_f32 v58, s46, v68\n\t"
        "v_mfma_f32_16x16x32_bf16 v[24:27], a[84:87], v[88:91], v[24:27]\n\t"
        "v_add_f32 v59, s47, v69\n\t"
        "v_add_f32 v56, v56, v58\n\t"
        "v_mfma_f32_16x16x32_bf16 v[28:31], a[88:91], v[88:91], v[28:31]\n\t"
        "v_add_f32 v57, v57, v59\n\t"
        "v_cvt_pk_bf16_f32 v52, v56, v57\n\t"
        "v_mfma_f32_16x16x32_bf16 v[32:35], a[92:95], v[88:91], v[32:35]\n\t"
        "v_cvt_pk_bf16_f32 v58, v58, v59\n\t"
        "v_cmp_ne_u32 s[64:65], v52, v58\n\t"
        "s_waitcnt lgkmcnt(0)\n\t"
        "v_mfma_f32_16x16x32_bf16 v[12:15], a[96:99], v[92:95], v[12:15]\n\t"
        "v_mbcnt_lo_u32_b32 v56, s64, 1\n\t"
        "v_mbcnt_hi_u32_b32 v56, s65, v56\n\t"
        "v_mfma_f32_16x16x32_bf16 v[16:19], a[100:103], v[92:95], v[16:19]\n\t"
        "s_bcnt1_i32_b64 s61, s[64:65]\n\t"
        "v_add_u32 v54, s61, 1\n\t"
        "v_lshl_add_u32 v56, v56, 3, s60\n\t"
        "v_mfma_f32_16x16x32_bf16 v[20:23], a[104:107], v[92:95], v[20:23]\n\t"
        "s_mov_b64 exec, s[64:65]\n\t"
        "ds_write_b64 v56, v[52:53]\n\t"
        "s_mov_b64 exec, -1\n\t"
        "v_mfma_f32_16x16x32_bf16 v[36:39], a[84:87], v[92:95], v[36:39]\n\t"
        "v_swap_b32 v53, v80\n\t"
        "v_perm_b32 v56, v67, v67, s44\n\t"
        "v_mfma_f32_16x16x32_bf16 v[40:43], a[88:91], v[92:95], v[40:43]\n\t"
        "buffer_load_dwordx4 v135, s[16:19], s43 offen  lds\n\t"
        "s_add_u32 m0, s37, 49152\n\t"
        "v_mfma_f32_16x16x32_bf16 v[44:47], a[92:95], v[92:95], v[44:47]\n\t"
        "s_waitcnt vmcnt(10)\n\t"
        "s_barrier\n\t"
        "ds_read_b128 v[100:103], v128 offset:0\n\t"
        "s_add_u32 s36, s36, s5\n\t"
        "ds_read_b128 v[104:107], v128 offset:64\n\t"
        "v_mfma_f32_16x16x32_bf16 v[24:27], a[96:99], v[96:99], v[24:27]\n\t"
        "ds_read_b128 v[108:111], v128 offset:128\n\t"
        "ds_read_b128 v[112:115], v128 offset:20480\n\t"
        "v_perm_b32 v57, v67, v67, s45\n\t"
        "v_mfma_f32_16x16x32_bf16 v[28:31], a[100:103], v[96:99], v[28:31]\n\t"
        "ds_read_b128 v[84:87], v128 offset:20544\n\t"
        "ds_read_b128 v[88:91], v128 offset:20608\n\t"
        "v_add_f32 v56, v56, v78\n\t"
        "v_mfma_f32_16x16x32_bf16 v[32:35], a[104:107], v[96:99], v[32:35]\n\t"
        "buffer_load_dwordx4 v140, s[24:27], s36 offen offset:0 lds\n\t"
        "ds_read_b64_tr_b16 a[108:109], v131 offset:12288\n\t"
        "v_add_f32 v57, v57, v79\n\t"
        "v_mfma_f32_16x16x32_bf16 v[36:39], a[72:75], v[96:99], v[36:39]\n\t"
        "ds_read_b64_tr_b16 a[110:111], v131 offset:13056\n\t"
        "ds_read_b64_tr_b16 a[112:113], v132 offset:12288\n\t"
        "v_cvt_pk_bf16_f32 v52, v56, v57\n\t"
        "v_mfma_f32_16x16x32_bf16 v[40:43], a[76:79], v[96:99], v[40:43]\n\t"
        "ds_read_b64_tr_b16 a[114:115], v132 offset:13056\n\t"
        "ds_read_b64_tr_b16 a[116:117], v133 offset:12288\n\t"
        "v_cvt_pk_bf16_f32 v58, v78, v79\n\t"
        "v_mfma_f32_16x16x32_bf16 v[44:47], a[80:83], v[96:99], v[44:47]\n\t"
        "ds_read_b64_tr_b16 a[118:119], v133 offset:13056\n\t"
        "ds_read_b64_tr_b16 a[120:121], v131 offset:18432\n\t"
        "v_cmp_ne_u32 s[64:65], v52, v58\n\t"
        "s_waitcnt lgkmcnt(12)\n\t"
        "v_mfma_f32_16x16x32_bf16 v[0:3], a[12:15], v[100:103], v[0:3]\n\t"
        "buffer_load_dwordx4 v141, s[24:27], s36 offen offset:1024 lds\n\t"
        "ds_read_b64_tr_b16 a[122:123], v131 offset:19200\n\t"
        "s_and_b64 s[64:65], s[64:65], 0xffffff\n\t"
        "v_mfma_f32_16x16x32_bf16 v[4:7], a[16:19], v[100:103], v[4:7]\n\t"
        "ds_read_b64_tr_b16 a[124:125], v132 offset:18432\n\t"
        "ds_read_b64_tr_b16 a[126:127], v132 offset:19200\n\t"
        "v_mbcnt_lo_u32_b32 v56, s64, v54\n\t"
        "v_mfma_f32_16x16x32_bf16 v[8:11], a[20:23], v[100:103], v[8:11]\n\t"
        "ds_read_b64_tr_b16 a[128:129], v133 offset:18432\n\t"
        "ds_read_b64_tr_b16 a[130:131], v133 offset:19200\n\t"
        "s_bcnt1_i32_b64 s46, s[64:65]\n\t"
        "s_waitcnt lgkmcnt(12)\n\t"
        "v_mfma_f32_16x16x32_bf16 v[0:3], a[48:51], v[104:107], v[0:3]\n\t"
        "ds_read_b128 v[92:95], v129 offset:0\n\t"
        "s_add_u32 s61, s61, s46\n\t"
        "v_mfma_f32_16x16x32_bf16 v[4:7], a[52:55], v[104:107], v[4:7]\n\t"
        "buffer_load_dwordx4 v142, s[24:27], s36 offen offset:2048 lds\n\t"
        "v_readlane_b32 s40, v155, 14\n\t"
        "v_mfma_f32_16x16x32_bf16 v[8:11], a[56:59], v[104:107], v[8:11]\n\t"
        "ds_read_b128 v[96:99], v129 offset:4096\n\t"
        "v_readlane_b32 s43, v155, 2\n\t"
        "v_mfma_f32_16x16x32_bf16 v[0:3], a[84:87], v[108:111], v[0:3]\n\t"
        "s_add_u32 m0, s40, s35\n\t"
        "v_add_u32 v54, s46, v54\n\t"
        "s_mov_b32 s3, s61\n\t"
        "v_mfma_f32_16x16x32_bf16 v[4:7], a[88:91], v[108:111], v[4:7]\n\t"
        "v_lshl_add_u32 v56, v56, 3, s60\n\t"
        "v_readlane_b32 s46, v155, 39\n\t"
        "v_mfma_f32_16x16x32_bf16 v[8:11], a[92:95], v[108:111], v[8:11]\n\t"
        "s_mov_b64 exec, s[64:65]\n\t"
        "ds_write_b64 v56, v[52:53]\n\t"
        "s_mov_b64 exec, -1\n\t"
        "v_mfma_f32_16x16x32_bf16 v[36:39], a[24:27], v[112:115], v[36:39]\n\t"
        "buffer_load_dwordx4 v134, s[16:19], s43 offen  lds\n\t"
        "v_readlane_b32 s40, v155, 15\n\t"
        "v_mfma_f32_16x16x32_bf16 v[40:43], a[28:31], v[112:115], v[40:43]\n\t"
        "v_readlane_b32 s43, v155, 3\n\t"
        "s_add_u32 m0, s40, s35\n\t"
        "v_mfma_f32_16x16x32_bf16 v[44:47], a[32:35], v[112:115], v[44:47]\n\t"
        "v_add_u32 v53, 4096, v80\n\t"
        "v_readlane_b32 s47, v155, 42\n\t"
        "v_mfma_f32_16x16x32_bf16 v[36:39], a[60:63], v[84:87], v[36:39]\n\t"
        "v_perm_b32 v56, v66, v66, s44\n\t"
        "v_perm_b32 v57, v66, v66, s45\n\t"
        "v_mfma_f32_16x16x32_bf16 v[40:43], a[64:67], v[84:87], v[40:43]\n\t"
        "buffer_load_dwordx4 v135, s[16:19], s43 offen  lds\n\t"
        "s_add_u32 m0, s37, 0\n\t"
        "v_mfma_f32_16x16x32_bf16 v[44:47], a[68:71], v[84:87], v[44:47]\n\t"
        "s_add_u32 s36, s36, s39\n\t"
        "v_add_f32 v58, s46, v68\n\t"
        "v_mfma_f32_16x16x32_bf16 v[36:39], a[96:99], v[88:91], v[36:39]\n\t"
        "v_add_f32 v59, s47, v69\n\t"
        "v_add_f32 v56, v56, v58\n\t"
        "v_mfma_f32_16x16x32_bf16 v[40:43], a[100:103], v[88:91], v[40:43]\n\t"
        "v_add_f32 v57, v57, v59\n\t"
        "v_max_f32 v56, 0, v56\n\t"
        "v_mfma_f32_16x16x32_bf16 v[44:47], a[104:107], v[88:91], v[44:47]\n\t"
        "s_waitcnt vmcnt(12) lgkmcnt(1)\n\t"
        "s_barrier\n\t"
        "buffer_load_dwordx4 v140, s[24:27], s36 offen offset:0 lds\n\t"
        "ds_read_b64_tr_b16 a[132:133], v131 offset:24576\n\t"
        "v_mfma_f32_16x16x32_bf16 v[0:3], a[120:123], v[92:95], v[0:3]\n\t"
        "ds_read_b64_tr_b16 a[134:135], v131 offset:25344\n\t"
        "ds_read_b64_tr_b16 a[136:137], v132 offset:24576\n\t"
        "ds_read_b64_tr_b16 a[138:139], v132 offset:25344\n\t"
        "v_max_f32 v57, 0, v57\n\t"
        "v_mfma_f32_16x16x32_bf16 v[4:7], a[124:127], v[92:95], v[4:7]\n\t"
        "ds_read_b64_tr_b16 a[140:141], v133 offset:24576\n\t"
        "ds_read_b64_tr_b16 a[142:143], v133 offset:25344\n\t"
        "ds_read_b128 v[100:103], v129 offset:8192\n\t"
        "v_max_f32 v58, 0, v58\n\t"
        "v_mfma_f32_16x16x32_bf16 v[8:11], a[128:131], v[92:95], v[8:11]\n\t"
        "ds_read_b128 v[104:107], v129 offset:12288\n\t"
        "ds_read_b64_tr_b16 a[144:145], v131 offset:30720\n\t"
        "v_max_f32 v59, 0, v59\n\t"
        "v_mfma_f32_16x16x32_bf16 v[0:3], a[108:111], v[96:99], v[0:3]\n\t"
        "buffer_load_dwordx4 v141, s[24:27], s36 offen offset:1024 lds\n\t"
        "ds_read_b64_tr_b16 a[146:147], v131 offset:31488\n\t"
        "v_cvt_pk_bf16_f32 v52, v56, v57\n\t"
        "v_mfma_f32_16x16x32_bf16 v[4:7], a[112:115], v[96:99], v[4:7]\n\t"
        "ds_read_b64_tr_b16 a[148:149], v132 offset:30720\n\t"
        "ds_read_b64_tr_b16 a[150:151], v132 offset:31488\n\t"
        "v_cvt_pk_bf16_f32 v58, v58, v59\n\t"
        "v_mfma_f32_16x16x32_bf16 v[8:11], a[116:119], v[96:99], v[8:11]\n\t"
        "ds_read_b64_tr_b16 a[152:153], v133 offset:30720\n\t"
        "ds_read_b64_tr_b16 a[154:155], v133 offset:31488\n\t"
        "v_cmp_ne_u32 s[64:65], v52, v58\n\t"
        "v_mfma_f32_16x16x32_bf16 v[12:15], a[120:123], v[96:99], v[12:15]\n\t"
        "ds_read_b128 v[108:111], v129 offset:16384\n\t"
        "v_mbcnt_lo_u32_b32 v56, s64, v54\n\t"
        "v_mfma_f32_16x16x32_bf16 v[16:19], a[124:127], v[96:99], v[16:19]\n\t"
        "buffer_load_dwordx4 v142, s[24:27], s36 offen offset:2048 lds\n\t"
        "v_readlane_b32 s40, v155, 16\n\t"
        "v_mfma_f32_16x16x32_bf16 v[20:23], a[128:131], v[96:99], v[20:23]\n\t"
        "v_readlane_b32 s43, v155, 4\n\t"
        "s_add_u32 m0, s40, s35\n\t"
        "s_waitcnt lgkmcnt(8)\n\t"
        "v_mfma_f32_16x16x32_bf16 v[0:3], a[132:135], v[100:103], v[0:3]\n\t"
        "v_mbcnt_hi_u32_b32 v56, s65, v56\n\t"
        "s_bcnt1_i32_b64 s46, s[64:65]\n\t"
        "v_mfma_f32_16x16x32_bf16 v[4:7], a[136:139], v[100:103], v[4:7]\n\t"
        "s_waitcnt vmcnt(14)\n\t"
        "v_and_b32 v48, s54, v48\n\t"
        "v_pk_max_u16 v60, v154, v48\n\t"
        "v_mfma_f32_16x16x32_bf16 v[8:11], a[140:143], v[100:103], v[8:11]\n\t"
        "buffer_load_dwordx4 v134, s[16:19], s43 offen  lds\n\t"
        "v_readlane_b32 s40, v155, 17\n\t"
        "v_mfma_f32_16x16x32_bf16 v[12:15], a[108:111], v[100:103], v[12:15]\n\t"
        "ds_read_b128 v[112:115], v129 offset:20480\n\t"
        "v_readlane_b32 s43, v155, 5\n\t"
        "v_mfma_f32_16x16x32_bf16 v[16:19], a[112:115], v[100:103], v[16:19]\n\t"
        "s_add_u32 m0, s40, s35\n\t"
        "v_and_b32 v49, s54, v49\n\t"
        "v_mfma_f32_16x16x32_bf16 v[20:23], a[116:119], v[100:103], v[20:23]\n\t"
        "v_pk_max_u16 v60, v60, v49\n\t"
        "v_and_b32 v50, s54, v50\n\t"
        "v_mfma_f32_16x16x32_bf16 v[24:27], a[120:123], v[100:103], v[24:27]\n\t"
        "v_pk_max_u16 v60, v60, v50\n\t"
        "v_and_b32 v51, s54, v51\n\t"
        "v_mfma_f32_16x16x32_bf16 v[28:31], a[124:127], v[100:103], v[28:31]\n\t"
        "v_pk_max_u16 v60, v60, v51\n\t"
        "s_waitcnt vmcnt(14)\n\t"
        "v_and_b32 v61, s54, v61\n\t"
        "v_mfma_f32_16x16x32_bf16 v[32:35], a[128:131], v[100:103], v[32:35]\n\t"
        "v_pk_max_u16 v60, v60, v61\n\t"
        "v_cmp_ne_u32 s[12:13], v60, v154\n\t"
        "s_waitcnt lgkmcnt(1)\n\t"
        "v_mfma_f32_16x16x32_bf16 v[12:15], a[132:135], v[104:107], v[12:15]\n\t"
        "buffer_load_dwordx4 v135, s[16:19], s43 offen  lds\n\t"
        "s_add_u32 m0, s37, 12288\n\t"
        "v_mfma_f32_16x16x32_bf16 v[16:19], a[136:139], v[104:107], v[16:19]\n\t"
        "s_add_u32 s36, s36, s38\n\t"
        "s_or_b64 s[56:57], s[56:57], s[12:13]\n\t"
        "v_mfma_f32_16x16x32_bf16 v[20:23], a[140:143], v[104:107], v[20:23]\n\t"
        "s_add_u32 s61, s61, s46\n\t"
        "v_min_u32 v56, 97, v56\n\t"
        "v_mfma_f32_16x16x32_bf16 v[24:27], a[108:111], v[104:107], v[24:27]\n\t"
        "v_lshl_add_u32 v56, v56, 3, s60\n\t"
        "v_mfma_f32_16x16x32_bf16 v[28:31], a[112:115], v[104:107], v[28:31]\n\t"
        "s_waitcnt vmcnt(10)\n\t"
        "s_barrier\n\t"
        "buffer_load_dwordx4 v140, s[24:27], s36 offen offset:0 lds\n\t"
        "ds_read_b64_tr_b16 a[156:157], v131 offset:36864\n\t"
        "v_mfma_f32_16x16x32_bf16 v[32:35], a[116:119], v[104:107], v[32:35]\n\t"
        "ds_read_b64_tr_b16 a[158:159], v131 offset:37632\n\t"
        "ds_read_b64_tr_b16 a[160:161], v132 offset:36864\n\t"
        "v_mfma_f32_16x16x32_bf16 v[36:39], a[120:123], v[104:107], v[36:39]\n\t"
        "ds_read_b64_tr_b16 a[162:163], v132 offset:37632\n\t"
        "ds_read_b64_tr_b16 a[164:165], v133 offset:36864\n\t"
        "v_mfma_f32_16x16x32_bf16 v[40:43], a[124:127], v[104:107], v[40:43]\n\t"
        "ds_read_b64_tr_b16 a[166:167], v133 offset:37632\n\t"
        "ds_read_b128 v[84:87], v129 offset:64\n\t"
        "v_mfma_f32_16x16x32_bf16 v[44:47], a[128:131], v[104:107], v[44:47]\n\t"
        "buffer_load_dwordx4 v141, s[24:27], s36 offen offset:1024 lds\n\t"
        "ds_read_b128 v[88:91], v129 offset:4160\n\t"
        "v_mfma_f32_16x16x32_bf16 v[24:27], a[132:135], v[108:111], v[24:27]\n\t"
        "s_mov_b64 exec, s[64:65]\n\t"
        "ds_write_b64 v56, v[52:53]\n\t"
        "s_mov_b64 exec, -1\n\t"
        "ds_read_b64_tr_b16 a[168:169], v131 offset:43008\n\t"
        "v_mfma_f32_16x16x32_bf16 v[28:31], a[136:139], v[108:111], v[28:31]\n\t"
        "s_cmp_gt_u32 s61, s53\n\t"
        "s_cselect_b32 s8, 1, 0\n\t"
        "ds_read_b64_tr_b16 a[170:171], v131 offset:43776\n\t"
        "ds_read_b64_tr_b16 a[172:173], v132 offset:43008\n\t"
        "v_mfma_f32_16x16x32_bf16 v[32:35], a[140:143], v[108:111], v[32:35]\n\t"
        "ds_read_b64_tr_b16 a[174:175], v132 offset:43776\n\t"
        "ds_read_b64_tr_b16 a[176:177], v133 offset:43008\n\t"
        "s_or_b32 s56, s56, s8\n\t"
        "v_mfma_f32_16x16x32_bf16 v[36:39], a[108:111], v[108:111], v[36:39]\n\t"
        "s_cmp_gt_u32 s3, 64\n\t"
        "s_cselect_b32 s8, 1, 0\n\t"
        "buffer_load_dwordx4 v142, s[24:27], s36 offen offset:2048 lds\n\t"
        "ds_read_b64_tr_b16 a[178:179], v133 offset:43776\n\t"
        "v_mfma_f32_16x16x32_bf16 v[40:43], a[112:115], v[108:111], v[40:43]\n\t"
        "ds_read_b128 v[92:95], v129 offset:8256\n\t"
        "v_readlane_b32 s40, v155, 18\n\t"
        "v_mfma_f32_16x16x32_bf16 v[44:47], a[116:119], v[108:111], v[44:47]\n\t"
        "v_readlane_b32 s43, v155, 6\n\t"
        "s_add_u32 m0, s40, s35\n\t"
        "s_waitcnt lgkmcnt(9)\n\t"
        "v_mfma_f32_16x16x32_bf16 v[36:39], a[132:135], v[112:115], v[36:39]\n\t"
        "ds_read_b128 v[96:99], v129 offset:12352\n\t"
        "s_or_b32 s56, s56, s8\n\t"
        "v_mfma_f32_16x16x32_bf16 v[40:43], a[136:139], v[112:115], v[40:43]\n\t"
        "buffer_load_dwordx4 v136, s[16:19], s43 offen  lds\n\t"
        "s_bitcmp1_b32 s42, 10\n\t"
        "s_cbranch_scc1 .Lx40%=\n\t"
        ".Lx41%=:\n\t"
        "v_mfma_f32_16x16x32_bf16 v[44:47], a[140:143], v[112:115], v[44:47]\n\t"
        "v_readlane_b32 s40, v155, 19\n\t"
        "v_readlane_b32 s43, v155, 7\n\t"
        "v_mfma_f32_16x16x32_bf16 v[0:3], a[156:159], v[84:87], v[0:3]\n\t"
        "s_add_u32 m0, s40, s35\n\t"
        "s_min_u32 s61, s61, 80\n\t"
        "v_mfma_f32_16x16x32_bf16 v[4:7], a[160:163], v[84:87], v[4:7]\n\t"
        "v_min_u32 v78, s61, v116\n\t"
        "v_lshl_add_u32 v78, v78, 3, s60\n\t"
        "v_mfma_f32_16x16x32_bf16 v[8:11], a[164:167], v[84:87], v[8:11]\n\t"
        "ds_read_b64 v[62:63], v78\n\t"
        "v_cmp_ge_u32 s[66:67], s61, v116\n\t"
        "s_waitcnt lgkmcnt(4)\n\t"
        "v_mfma_f32_16x16x32_bf16 v[0:3], a[144:147], v[88:91], v[0:3]\n\t"
        "s_mov_b32 s46, 0x07060302\n\t"
        "v_mfma_f32_16x16x32_bf16 v[4:7], a[148:151], v[88:91], v[4:7]\n\t"
        "v_mfma_f32_16x16x32_bf16 v[8:11], a[152:155], v[88:91], v[8:11]\n\t"
        "v_mfma_f32_16x16x32_bf16 v[12:15], a[156:159], v[88:91], v[12:15]\n\t"
        "ds_read_b128 v[100:103], v129 offset:16448\n\t"
        "v_mfma_f32_16x16x32_bf16 v[16:19], a[160:163], v[88:91], v[16:19]\n\t"
        "buffer_load_dwordx4 v137, s[16:19], s43 offen  lds\n\t"
        "s_add_u32 m0, s37, 24576\n\t"
        "v_mfma_f32_16x16x32_bf16 v[20:23], a[164:167], v[88:91], v[20:23]\n\t"
        "s_add_u32 s36, s36, s5\n\t"
        "s_waitcnt lgkmcnt(2)\n\t"
        "v_mfma_f32_16x16x32_bf16 v[0:3], a[168:171], v[92:95], v[0:3]\n\t"
        "v_mfma_f32_16x16x32_bf16 v[4:7], a[172:175], v[92:95], v[4:7]\n\t"
        "v_mfma_f32_16x16x32_bf16 v[8:11], a[176:179], v[92:95], v[8:11]\n\t"
        "s_mov_b64 exec, s[66:67]\n\t"
        "s_waitcnt lgkmcnt(1)\n\t"
        "ds_write_b32 v63, v117\n\t"
        "s_mov_b64 exec, -1\n\t"
        "v_mfma_f32_16x16x32_bf16 v[12:15], a[144:147], v[92:95], v[12:15]\n\t"
        "ds_read_b128 v[104:107], v129 offset:20544\n\t"
        "v_and_b32 v78, 0xfffff800, v63\n\t"
        "v_mfma_f32_16x16x32_bf16 v[16:19], a[148:151], v[92:95], v[16:19]\n\t"
        "s_waitcnt vmcnt(10)\n\t"
        "s_barrier\n\t"
        "buffer_load_dwordx4 v140, s[24:27], s36 offen offset:0 lds\n\t"
        "ds_read_b64_tr_b16 a[180:181], v131 offset:49152\n\t"
        "v_mfma_f32_16x16x32_bf16 v[20:23], a[152:155], v[92:95], v[20:23]\n\t"
        "ds_read_b128 v[64:67], v78 offset:1536\n\t"
        "ds_read_b64_tr_b16 a[182:183], v131 offset:49920\n\t"
        "v_lshlrev_b32 v72, 16, v62\n\t"
        "v_mfma_f32_16x16x32_bf16 v[24:27], a[156:159], v[92:95], v[24:27]\n\t"
        "ds_read_b64_tr_b16 a[184:185], v132 offset:49152\n\t"
        "ds_read_b64_tr_b16 a[186:187], v132 offset:49920\n\t"
        "v_and_b32 v73, 0xffff0000, v62\n\t"
        "v_mfma_f32_16x16x32_bf16 v[28:31], a[160:163], v[92:95], v[28:31]\n\t"
        "ds_read_b64_tr_b16 a[188:189], v133 offset:49152\n\t"
        "ds_read_b64_tr_b16 a[190:191], v133 offset:49920\n\t"
        "v_med3_f32 v72, v72, 0, s63\n\t"
        "v_mfma_f32_16x16x32_bf16 v[32:35], a[164:167], v[92:95], v[32:35]\n\t"
        "buffer_load_dwordx4 v141, s[24:27], s36 offen offset:1024 lds\n\t"
        "ds_read_b64_tr_b16 a[192:193], v131 offset:55296\n\t"
        "v_med3_f32 v73, v73, 0, s63\n\t"
        "v_mfma_f32_16x16x32_bf16 v[12:15], a[168:171], v[96:99], v[12:15]\n\t"
        "ds_read_b64_tr_b16 a[194:195], v131 offset:56064\n\t"
        "ds_read_b64_tr_b16 a[196:197], v132 offset:55296\n\t"
        "v_perm_b32 v62, v73, v72, s46\n\t"
        "v_mfma_f32_16x16x32_bf16 v[16:19], a[172:175], v[96:99], v[16:19]\n\t"
        "ds_read_b64_tr_b16 a[198:199], v132 offset:56064\n\t"
        "ds_read_b64_tr_b16 a[200:201], v133 offset:55296\n\t"
        "v_fract_f32 v74, v72\n\t"
        "v_mfma_f32_16x16x32_bf16 v[20:23], a[176:179], v[96:99], v[20:23]\n\t"
        "ds_read_b64_tr_b16 a[202:203], v133 offset:56064\n\t"
        "v_fract_f32 v75, v73\n\t"
        "v_mfma_f32_16x16x32_bf16 v[24:27], a[144:147], v[96:99], v[24:27]\n\t"
        "buffer_load_dwordx4 v142, s[24:27], s36 offen offset:2048 lds\n\t"
        "s_add_u32 m0, s37, 36864\n\t"
        "v_mfma_f32_16x16x32_bf16 v[28:31], a[148:151], v[96:99], v[28:31]\n\t"
        "ds_read_b128 v[108:111], v129 offset:128\n\t"
        "s_add_u32 s36, s36, s39\n\t"
        "v_mfma_f32_16x16x32_bf16 v[32:35], a[152:155], v[96:99], v[32:35]\n\t"
        "ds_read_b128 v[112:115], v129 offset:4224\n\t"
        "v_cvt_u32_f32 v76, v72\n\t"
        "v_mfma_f32_16x16x32_bf16 v[36:39], a[156:159], v[96:99], v[36:39]\n\t"
        "v_cvt_u32_f32 v77, v73\n\t"
        "v_cmp_neq_f32 s[64:65], 0, v74\n\t"
        "v_mfma_f32_16x16x32_bf16 v[40:43], a[160:163], v[96:99], v[40:43]\n\t"
        "buffer_load_dwordx4 v140, s[24:27], s36 offen offset:0 lds\n\t"
        "v_cmp_neq_f32 s[44:45], 0, v75\n\t"
        "v_mfma_f32_16x16x32_bf16 v[44:47], a[164:167], v[96:99], v[44:47]\n\t"
        "v_sub_f32 v78, 1.0, v74\n\t"
        "v_sub_f32 v79, 1.0, v75\n\t"
        "s_waitcnt lgkmcnt(7)\n\t"
        "v_mfma_f32_16x16x32_bf16 v[24:27], a[168:171], v[100:103], v[24:27]\n\t"
        "v_cvt_pk_bf16_f32 v79, v78, v79\n\t"
        "v_lshlrev_b32 v78, 16, v79\n\t"
        "v_mfma_f32_16x16x32_bf16 v[28:31], a[172:175], v[100:103], v[28:31]\n\t"
        "v_and_b32 v79, 0xffff0000, v79\n\t"
        "v_mul_f32 v80, v78, v79\n\t"
        "v_mfma_f32_16x16x32_bf16 v[32:35], a[176:179], v[100:103], v[32:35]\n\t"
        "v_mul_f32 v78, v78, v75\n\t"
        "v_mul_f32 v79, v74, v79\n\t"
        "v_mfma_f32_16x16x32_bf16 v[36:39], a[144:147], v[100:103], v[36:39]\n\t"
        "v_mul_f32 v81, v74, v75\n\t"
        "v_cndmask_b32 v78, v79, v78, s[44:45]\n\t"
        "v_mfma_f32_16x16x32_bf16 v[40:43], a[148:151], v[100:103], v[40:43]\n\t"
        "buffer_load_dwordx4 v141, s[24:27], s36 offen offset:1024 lds\n\t"
        "v_cndmask_b32 v79, 0, v79, s[44:45]\n\t"
        "v_mfma_f32_16x16x32_bf16 v[44:47], a[152:155], v[100:103], v[44:47]\n\t"
        "v_cvt_pk_bf16_f32 v70, v80, v78\n\t"
        "v_cvt_pk_bf16_f32 v71, v79, v81\n\t"
        "v_mfma_f32_16x16x32_bf16 v[36:39], a[168:171], v[104:107], v[36:39]\n\t"
        "v_pk_max_u16 v78, v62, v64\n\t"
        "v_pk_min_u16 v78, v78, v65\n\t"
        "v_mfma_f32_16x16x32_bf16 v[40:43], a[172:175], v[104:107], v[40:43]\n\t"
        "v_cmp_ne_u32 vcc, v78, v62\n\t"
        "v_sub_u32 v78, v77, v67\n\t"
        "v_mfma_f32_16x16x32_bf16 v[44:47], a[176:179], v[104:107], v[44:47]\n\t"
        "v_mad_u32_u24 v79, v77, 6, v66\n\t"
        "v_mad_u32_u24 v79, v76, s62, v79\n\t"
        "s_waitcnt lgkmcnt(0)\n\t"
        "v_mfma_f32_16x16x32_bf16 v[0:3], a[192:195], v[108:111], v[0:3]\n\t"
        "s_and_b64 vcc, vcc, s[66:67]\n\t"
        "s_or_b64 s[56:57], s[56:57], vcc\n\t"
        "v_mfma_f32_16x16x32_bf16 v[4:7], a[196:199], v[108:111], v[4:7]\n\t"
        "s_waitcnt vmcnt(10)\n\t"
        "s_barrier\n\t"
        "buffer_load_dwordx4 v142, s[24:27], s36 offen offset:2048 lds\n\t"
        "s_add_u32 m0, s37, 49152\n\t"
        "ds_read_b64_tr_b16 a[204:205], v131 offset:0\n\t"
        "s_add_u32 s36, s36, s38\n\t"
        "ds_read_b64_tr_b16 a[206:207], v131 offset:768\n\t"
        "v_mfma_f32_16x16x32_bf16 v[8:11], a[200:203], v[108:111], v[8:11]\n\t"
        "ds_read_b64_tr_b16 a[208:209], v132 offset:0\n\t"
        "ds_read_b64_tr_b16 a[210:211], v132 offset:768\n\t"
        "ds_read_b64_tr_b16 a[212:213], v133 offset:0\n\t"
        "ds_read_b64_tr_b16 a[214:215], v133 offset:768\n\t"
        "ds_read_b128 v[84:87], v129 offset:8320\n\t"
        "v_bfe_u32 v80, v78, 2, 1\n\t"
        "v_mfma_f32_16x16x32_bf16 v[0:3], a[180:183], v[112:115], v[0:3]\n\t"
        "ds_read_b128 v[88:91], v129 offset:12416\n\t"
        "ds_read_b64_tr_b16 a[216:217], v131 offset:6144\n\t"
        "v_or_b32 v80, v80, v79\n\t"
        "v_mfma_f32_16x16x32_bf16 v[4:7], a[184:187], v[112:115], v[4:7]\n\t"
        "ds_read_b64_tr_b16 a[218:219], v131 offset:6912\n\t"
        "ds_read_b64_tr_b16 a[220:221], v132 offset:6144\n\t"
        "v_add_u32 v78, 1, v78\n\t"
        "v_mfma_f32_16x16x32_bf16 v[8:11], a[188:191], v[112:115], v[8:11]\n\t"
        "buffer_load_dwordx4 v140, s[24:27], s36 offen offset:0 lds\n\t"
        "ds_read_b64_tr_b16 a[222:223], v132 offset:6912\n\t"
        "v_bfe_u32 v78, v78, 2, 1\n\t"
        "v_mfma_f32_16x16x32_bf16 v[12:15], a[192:195], v[112:115], v[12:15]\n\t"
        "ds_read_b128 v[92:95], v129 offset:16512\n\t"
        "ds_read_b64_tr_b16 a[224:225], v133 offset:6144\n\t"
        "v_add3_u32 v78, v79, v78, 6\n\t"
        "v_mfma_f32_16x16x32_bf16 v[16:19], a[196:199], v[112:115], v[16:19]\n\t"
        "ds_read_b64_tr_b16 a[226:227], v133 offset:6912\n\t"
        "v_cndmask_b32 v78, v80, v78, s[44:45]\n\t"
        "v_mfma_f32_16x16x32_bf16 v[20:23], a[200:203], v[112:115], v[20:23]\n\t"
        "v_cndmask_b32 v79, 0, v119, s[64:65]\n\t"
        "v_add_u32 v81, v80, v79\n\t"
        "s_waitcnt lgkmcnt(8)\n\t"
        "v_mfma_f32_16x16x32_bf16 v[0:3], a[204:207], v[84:87], v[0:3]\n\t"
        "buffer_load_dwordx4 v141, s[24:27], s36 offen offset:1024 lds\n\t"
        "v_add_u32 v82, v78, v79\n\t"
        "v_mfma_f32_16x16x32_bf16 v[4:7], a[208:211], v[84:87], v[4:7]\n\t"
        "v_cndmask_b32 v78, v81, v78, s[44:45]\n\t"
        "v_lshl_or_b32 v68, v78, 16, v80\n\t"
        "v_mfma_f32_16x16x32_bf16 v[8:11], a[212:215], v[84:87], v[8:11]\n\t"
        "v_lshl_or_b32 v69, v82, 16, v81\n\t"
        "v_mfma_f32_16x16x32_bf16 v[12:15], a[180:183], v[84:87], v[12:15]\n\t"
        "ds_write_b128 v118, v[68:71]\n\t"
        "v_mfma_f32_16x16x32_bf16 v[16:19], a[184:187], v[84:87], v[16:19]\n\t"
        "s_cmp_gt_u32 s3, 32\n\t"
        "s_cselect_b32 s8, 2, 0\n\t"
        "v_mfma_f32_16x16x32_bf16 v[20:23], a[188:191], v[84:87], v[20:23]\n\t"
        "s_cmp_lg_u64 s[56:57], 0\n\t"
        "s_addc_u32 s8, s8, 0\n\t"
        "v_mfma_f32_16x16x32_bf16 v[24:27], a[192:195], v[84:87], v[24:27]\n\t"
        "ds_read_b128 v[160:163], v185 offset:0\n\t"
        "v_mov_b32 v56, s8\n\t"
        "v_mfma_f32_16x16x32_bf16 v[28:31], a[196:199], v[84:87], v[28:31]\n\t"
        "s_mov_b64 exec, 1\n\t"
        "ds_write_b32 v153, v56\n\t"
        "s_mov_b64 exec, -1\n\t"
        "v_mfma_f32_16x16x32_bf16 v[32:35], a[200:203], v[84:87], v[32:35]\n\t"
        "buffer_load_dwordx4 v142, s[24:27], s36 offen offset:2048 lds\n\t"
        "v_xor_b32 v154, 32, v186\n\t"
        "s_waitcnt lgkmcnt(3)\n\t"
        "v_mfma_f32_16x16x32_bf16 v[12:15], a[204:207], v[88:91], v[12:15]\n\t"
        "ds_read_b128 v[96:99], v129 offset:20608\n\t"
        "v_mfma_f32_16x16x32_bf16 v[16:19], a[208:211], v[88:91], v[16:19]\n\t"
        "v_mfma_f32_16x16x32_bf16 v[20:23], a[212:215], v[88:91], v[20:23]\n\t"
        "v_mfma_f32_16x16x32_bf16 v[24:27], a[180:183], v[88:91], v[24:27]\n\t"
        "v_mfma_f32_16x16x32_bf16 v[28:31], a[184:187], v[88:91], v[28:31]\n\t"
        "v_mfma_f32_16x16x32_bf16 v[32:35], a[188:191], v[88:91], v[32:35]\n\t"
        "v_mfma_f32_16x16x32_bf16 v[36:39], a[192:195], v[88:91], v[36:39]\n\t"
        "s_waitcnt vmcnt(9)\n\t"
        "s_barrier\n\t"
        "ds_read_b64_tr_b16 a[228:229], v131 offset:12288\n\t"
        "ds_read_b64_tr_b16 a[230:231], v131 offset:13056\n\t"
        "v_mfma_f32_16x16x32_bf16 v[40:43], a[196:199], v[88:91], v[40:43]\n\t"
        "ds_read_b64_tr_b16 a[232:233], v132 offset:12288\n\t"
        "ds_read_b64_tr_b16 a[234:235], v132 offset:13056\n\t"
        "s_waitcnt lgkmcnt(5)\n\t"
        "v_lshlrev_b32_sdwa v157, 5, v160 dst_sel:DWORD dst_unused:UNUSED_PAD src0_sel:DWORD src1_sel:WORD_0\n\t"
        "v_mfma_f32_16x16x32_bf16 v[44:47], a[200:203], v[88:91], v[44:47]\n\t"
        "ds_read_b64_tr_b16 a[236:237], v133 offset:12288\n\t"
        "ds_read_b64_tr_b16 a[238:239], v133 offset:13056\n\t"
        "v_lshlrev_b32_sdwa v158, 5, v160 dst_sel:DWORD dst_unused:UNUSED_PAD src0_sel:DWORD src1_sel:WORD_1\n\t"
        "v_mfma_f32_16x16x32_bf16 v[24:27], a[204:207], v[92:95], v[24:27]\n\t"
        "ds_read_b128 v[100:103], v130 offset:0\n\t"
        "ds_read_b128 v[104:107], v130 offset:4096\n\t"
        "v_or_b32 v157, v184, v157\n\t"
        "v_mfma_f32_16x16x32_bf16 v[28:31], a[208:211], v[92:95], v[28:31]\n\t"
        "ds_read_b64_tr_b16 v[60:61], v157 offset:0\n\t"
        "ds_read_b64_tr_b16 a[240:241], v131 offset:18432\n\t"
        "v_or_b32 v158, v184, v158\n\t"
        "v_mfma_f32_16x16x32_bf16 v[32:35], a[212:215], v[92:95], v[32:35]\n\t"
        "ds_read_b64_tr_b16 a[242:243], v131 offset:19200\n\t"
        "ds_read_b64_tr_b16 a[244:245], v132 offset:18432\n\t"
        "v_xor_b32 v159, 32, v157\n\t"
        "v_mfma_f32_16x16x32_bf16 v[36:39], a[180:183], v[92:95], v[36:39]\n\t"
        "ds_read_b64_tr_b16 a[246:247], v132 offset:19200\n\t"
        "ds_read_b64_tr_b16 a[248:249], v133 offset:18432\n\t"
        "v_xor_b32 v152, 32, v158\n\t"
        "v_mfma_f32_16x16x32_bf16 v[40:43], a[184:187], v[92:95], v[40:43]\n\t"
        "ds_read_b64_tr_b16 a[250:251], v133 offset:19200\n\t"
        "v_perm_b32 v164, v162, v162, v168\n\t"
        "v_mfma_f32_16x16x32_bf16 v[44:47], a[188:191], v[92:95], v[44:47]\n\t"
        "ds_read_b128 v[108:111], v130 offset:8192\n\t"
        "v_perm_b32 v165, v162, v162, v169\n\t"
        "s_waitcnt lgkmcnt(12)\n\t"
        "v_mfma_f32_16x16x32_bf16 v[36:39], a[204:207], v[96:99], v[36:39]\n\t"
        "ds_read_b64_tr_b16 v[62:63], v158 offset:0\n\t"
        "v_perm_b32 v166, v162, v162, v170\n\t"
        "v_mfma_f32_16x16x32_bf16 v[40:43], a[208:211], v[96:99], v[40:43]\n\t"
        "ds_read_b64_tr_b16 v[64:65], v159 offset:0\n\t"
        "v_perm_b32 v167, v162, v162, v171\n\t"
        "v_mfma_f32_16x16x32_bf16 v[44:47], a[212:215], v[96:99], v[44:47]\n\t"
        "s_waitcnt vmcnt(6)\n\t"
        "s_barrier\n\t"
        "ds_read_b128 v[112:115], v130 offset:12288\n\t"
        "ds_read_b64_tr_b16 a[252:253], v131 offset:24576\n\t"
        "s_waitcnt lgkmcnt(11)\n\t"
        "v_mfma_f32_16x16x32_bf16 v[0:3], a[228:231], v[100:103], v[0:3]\n\t"
        "ds_read_b64_tr_b16 v[66:67], v152 offset:0\n\t"
        "ds_read_b64_tr_b16 a[254:255], v131 offset:25344\n\t"
        "v_cmp_ne_u32 vcc, 0, v163\n\t"
        "v_mfma_f32_16x16x32_bf16 v[4:7], a[232:235], v[100:103], v[4:7]\n\t"
        "ds_read_b64_tr_b16 v[68:69], v157 offset:64\n\t"
        "ds_read_b64_tr_b16 v[188:189], v132 offset:24576\n\t"
        "v_mfma_f32_16x16x32_bf16 v[8:11], a[236:239], v[100:103], v[8:11]\n\t"
        "ds_read_b64_tr_b16 v[70:71], v158 offset:64\n\t"
        "ds_read_b64_tr_b16 v[190:191], v132 offset:25344\n\t"
        "v_mfma_f32_16x16x32_bf16 v[0:3], a[216:219], v[104:107], v[0:3]\n\t"
        "ds_read_b64_tr_b16 v[72:73], v159 offset:64\n\t"
        "ds_read_b64_tr_b16 v[192:193], v133 offset:24576\n\t"
        "v_mfma_f32_16x16x32_bf16 v[4:7], a[220:223], v[104:107], v[4:7]\n\t"
        "ds_read_b64_tr_b16 v[74:75], v152 offset:64\n\t"
        "ds_read_b64_tr_b16 v[194:195], v133 offset:25344\n\t"
        "v_mfma_f32_16x16x32_bf16 v[8:11], a[224:227], v[104:107], v[8:11]\n\t"
        "ds_read_b64_tr_b16 v[76:77], v157 offset:128\n\t"
        "v_mfma_f32_16x16x32_bf16 v[12:15], a[228:231], v[104:107], v[12:15]\n\t"
        "ds_read_b64_tr_b16 v[78:79], v158 offset:128\n\t"
        "s_waitcnt lgkmcnt(14)\n\t"
        "v_mfma_f32_16x16x32_bf16 v[48:51], v[60:63], v[164:167], 0\n\t"
        "ds_read_b64_tr_b16 v[80:81], v159 offset:128\n\t"
        "v_mfma_f32_16x16x32_bf16 v[16:19], a[232:235], v[104:107], v[16:19]\n\t"
        "ds_read_b64_tr_b16 v[82:83], v152 offset:128\n\t"
        "v_mfma_f32_16x16x32_bf16 v[20:23], a[236:239], v[104:107], v[20:23]\n\t"
        "ds_read_b128 v[84:87], v130 offset:16384\n\t"
        "ds_read_b64_tr_b16 v[196:197], v131 offset:30720\n\t"
        "s_waitcnt lgkmcnt(13)\n\t"
        "v_mfma_f32_16x16x32_bf16 v[52:55], v[64:67], v[164:167], 0\n\t"
        "ds_read_b64_tr_b16 v[198:199], v131 offset:31488\n\t"
        "ds_read_b64_tr_b16 v[200:201], v132 offset:30720\n\t"
        "v_mfma_f32_16x16x32_bf16 v[0:3], a[240:243], v[108:111], v[0:3]\n\t"
        "ds_read_b64_tr_b16 v[202:203], v132 offset:31488\n\t"
        "ds_read_b64_tr_b16 v[204:205], v133 offset:30720\n\t"
        "s_waitcnt lgkmcnt(13)\n\t"
        "v_mfma_f32_16x16x32_bf16 v[56:59], v[68:71], v[164:167], 0\n\t"
        "ds_read_b64_tr_b16 v[206:207], v133 offset:31488\n\t"
        "v_mfma_f32_16x16x32_bf16 v[4:7], a[244:247], v[108:111], v[4:7]\n\t"
        "s_waitcnt lgkmcnt(12)\n\t"
        "v_mfma_f32_16x16x32_bf16 v[116:119], v[72:75], v[164:167], 0\n\t"
        "v_mfma_f32_16x16x32_bf16 v[8:11], a[248:251], v[108:111], v[8:11]\n\t"
        "s_waitcnt lgkmcnt(9)\n\t"
        "v_mfma_f32_16x16x32_bf16 v[120:123], v[76:79], v[164:167], 0\n\t"
        "v_mfma_f32_16x16x32_bf16 v[12:15], a[216:219], v[108:111], v[12:15]\n\t"
        "ds_read_b128 v[88:91], v130 offset:64\n\t"
        "s_waitcnt lgkmcnt(8)\n\t"
        "v_mfma_f32_16x16x32_bf16 v[124:127], v[80:83], v[164:167], 0\n\t"
        "s_cbranch_vccnz .Lx42%=\n\t"
        ".Lx43%=:\n\t"
        "v_cvt_pk_bf16_f32 v48, v48, v49\n\t"
        "v_mfma_f32_16x16x32_bf16 v[16:19], a[220:223], v[108:111], v[16:19]\n\t"
        "ds_read_b128 v[160:163], v185 offset:256\n\t"
        "v_cvt_pk_bf16_f32 v49, v50, v51\n\t"
        "v_mfma_f32_16x16x32_bf16 v[20:23], a[224:227], v[108:111], v[20:23]\n\t"
        "ds_write_b64 v186, v[48:49] offset:0\n\t"
        "v_mfma_f32_16x16x32_bf16 v[24:27], a[228:231], v[108:111], v[24:27]\n\t"
        "ds_read_b128 v[92:95], v130 offset:4160\n\t"
        "v_cvt_pk_bf16_f32 v52, v52, v53\n\t"
        "v_mfma_f32_16x16x32_bf16 v[28:31], a[232:235], v[108:111], v[28:31]\n\t"
        "v_cvt_pk_bf16_f32 v53, v54, v55\n\t"
        "v_cvt_pk_bf16_f32 v56, v56, v57\n\t"
        "v_mfma_f32_16x16x32_bf16 v[32:35], a[236:239], v[108:111], v[32:35]\n\t"
        "ds_write_b64 v154, v[52:53] offset:0\n\t"
        "v_mfma_f32_16x16x32_bf16 v[12:15], a[240:243], v[112:115], v[12:15]\n\t"
        "v_cvt_pk_bf16_f32 v57, v58, v59\n\t"
        "v_cvt_pk_bf16_f32 v116, v116, v117\n\t"
        "v_mfma_f32_16x16x32_bf16 v[16:19], a[244:247], v[112:115], v[16:19]\n\t"
        "ds_write_b64 v186, v[56:57] offset:64\n\t"
        "v_mfma_f32_16x16x32_bf16 v[20:23], a[248:251], v[112:115], v[20:23]\n\t"
        "v_cvt_pk_bf16_f32 v117, v118, v119\n\t"
        "v_cvt_pk_bf16_f32 v120, v120, v121\n\t"
        "v_mfma_f32_16x16x32_bf16 v[24:27], a[216:219], v[112:115], v[24:27]\n\t"
        "ds_write_b64 v154, v[116:117] offset:64\n\t"
        "v_mfma_f32_16x16x32_bf16 v[28:31], a[220:223], v[112:115], v[28:31]\n\t"
        "ds_read_b128 v[96:99], v130 offset:20480\n\t"
        "v_cvt_pk_bf16_f32 v121, v122, v123\n\t"
        "v_mfma_f32_16x16x32_bf16 v[32:35], a[224:227], v[112:115], v[32:35]\n\t"
        "s_waitcnt vmcnt(0) lgkmcnt(6)\n\t"
        "s_barrier\n\t"
        "ds_read_b64_tr_b16 v[208:209], v131 offset:36864\n\t"
        "ds_read_b64_tr_b16 v[210:211], v131 offset:37632\n\t"
        "v_mfma_f32_16x16x32_bf16 v[36:39], a[228:231], v[112:115], v[36:39]\n\t"
        "ds_write_b64 v186, v[120:121] offset:128\n\t"
        "ds_read_b64_tr_b16 v[212:213], v132 offset:36864\n\t"
        "v_mfma_f32_16x16x32_bf16 v[40:43], a[232:235], v[112:115], v[40:43]\n\t"
        "ds_read_b64_tr_b16 v[214:215], v132 offset:37632\n\t"
        "ds_read_b64_tr_b16 v[216:217], v133 offset:36864\n\t"
        "v_cvt_pk_bf16_f32 v124, v124, v125\n\t"
        "v_mfma_f32_16x16x32_bf16 v[44:47], a[236:239], v[112:115], v[44:47]\n\t"
        "ds_read_b64_tr_b16 v[218:219], v133 offset:37632\n\t"
        "v_cvt_pk_bf16_f32 v125, v126, v127\n\t"
        "v_mfma_f32_16x16x32_bf16 v[24:27], a[240:243], v[84:87], v[24:27]\n\t"
        "ds_write_b64 v154, v[124:125] offset:128\n\t"
        "v_mfma_f32_16x16x32_bf16 v[28:31], a[244:247], v[84:87], v[28:31]\n\t"
        "ds_read_b128 v[100:103], v130 offset:8256\n\t"
        "v_lshlrev_b32_sdwa v157, 5, v160 dst_sel:DWORD dst_unused:UNUSED_PAD src0_sel:DWORD src1_sel:WORD_0\n\t"
        "v_mfma_f32_16x16x32_bf16 v[32:35], a[248:251], v[84:87], v[32:35]\n\t"
        "ds_read_b64_tr_b16 v[220:221], v131 offset:43008\n\t"
        "v_lshlrev_b32_sdwa v158, 5, v160 dst_sel:DWORD dst_unused:UNUSED_PAD src0_sel:DWORD src1_sel:WORD_1\n\t"
        "v_mfma_f32_16x16x32_bf16 v[36:39], a[216:219], v[84:87], v[36:39]\n\t"
        "ds_read_b64_tr_b16 v[222:223], v131 offset:43776\n\t"
        "v_or_b32 v157, v184, v157\n\t"
        "v_mfma_f32_16x16x32_bf16 v[40:43], a[220:223], v[84:87], v[40:43]\n\t"
        "ds_read_b64_tr_b16 v[60:61], v157 offset:0\n\t"
        "v_or_b32 v158, v184, v158\n\t"
        "v_mfma_f32_16x16x32_bf16 v[44:47], a[224:227], v[84:87], v[44:47]\n\t"
        "ds_read_b64_tr_b16 v[62:63], v158 offset:0\n\t"
        "ds_read_b64_tr_b16 v[224:225], v132 offset:43008\n\t"
        "v_xor_b32 v159, 32, v157\n\t"
        "v_mfma_f32_16x16x32_bf16 v[0:3], v[196:199], v[88:91], v[0:3]\n\t"
        "ds_read_b64_tr_b16 v[64:65], v159 offset:0\n\t"
        "ds_read_b128 v[104:107], v130 offset:12352\n\t"
        "v_xor_b32 v152, 32, v158\n\t"
        "v_mfma_f32_16x16x32_bf16 v[4:7], v[200:203], v[88:91], v[4:7]\n\t"
        "ds_read_b64_tr_b16 v[66:67], v152 offset:0\n\t"
        "ds_read_b64_tr_b16 v[226:227], v132 offset:43776\n\t"
        "v_perm_b32 v164, v162, v162, v168\n\t"
        "v_mfma_f32_16x16x32_bf16 v[8:11], v[204:207], v[88:91], v[8:11]\n\t"
        "ds_read_b64_tr_b16 v[68:69], v157 offset:64\n\t"
        "ds_read_b64_tr_b16 v[228:229], v133 offset:43008\n\t"
        "v_perm_b32 v165, v162, v162, v169\n\t"
        "s_waitcnt lgkmcnt(12)\n\t"
        "v_mfma_f32_16x16x32_bf16 v[0:3], a[252:255], v[92:95], v[0:3]\n\t"
        "ds_read_b64_tr_b16 v[70:71], v158 offset:64\n\t"
        "ds_read_b64_tr_b16 v[230:231], v133 offset:43776\n\t"
        "v_perm_b32 v166, v162, v162, v170\n\t"
        "v_mfma_f32_16x16x32_bf16 v[4:7], v[188:191], v[92:95], v[4:7]\n\t"
        "ds_read_b64_tr_b16 v[72:73], v159 offset:64\n\t"
        "v_perm_b32 v167, v162, v162, v171\n\t"
        "v_mfma_f32_16x16x32_bf16 v[8:11], v[192:195], v[92:95], v[8:11]\n\t"
        "ds_read_b64_tr_b16 v[74:75], v152 offset:64\n\t"
        "v_cmp_ne_u32 vcc, 0, v163\n\t"
        "v_mfma_f32_16x16x32_bf16 v[12:15], v[196:199], v[92:95], v[12:15]\n\t"
        "ds_read_b64_tr_b16 v[76:77], v157 offset:128\n\t"
        "v_mfma_f32_16x16x32_bf16 v[16:19], v[200:203], v[92:95], v[16:19]\n\t"
        "ds_read_b64_tr_b16 v[78:79], v158 offset:128\n\t"
        "v_mfma_f32_16x16x32_bf16 v[20:23], v[204:207], v[92:95], v[20:23]\n\t"
        "ds_read_b64_tr_b16 v[80:81], v159 offset:128\n\t"
        "s_waitcnt lgkmcnt(11)\n\t"
        "v_mfma_f32_16x16x32_bf16 v[48:51], v[60:63], v[164:167], 0\n\t"
        "ds_read_b64_tr_b16 v[82:83], v152 offset:128\n\t"
        "v_mfma_f32_16x16x32_bf16 v[36:39], a[240:243], v[96:99], v[36:39]\n\t"
        "ds_read_b128 v[108:111], v130 offset:16448\n\t"
        "s_waitcnt lgkmcnt(11)\n\t"
        "v_mfma_f32_16x16x32_bf16 v[52:55], v[64:67], v[164:167], 0\n\t"
        "ds_read_b64_tr_b16 v[232:233], v131 offset:49152\n\t"
        "v_mfma_f32_16x16x32_bf16 v[40:43], a[244:247], v[96:99], v[40:43]\n\t"
        "ds_read_b64_tr_b16 v[234:235], v131 offset:49920\n\t"
        "s_waitcnt lgkmcnt(10)\n\t"
        "v_mfma_f32_16x16x32_bf16 v[56:59], v[68:71], v[164:167], 0\n\t"
        "ds_read_b64_tr_b16 v[236:237], v132 offset:49152\n\t"
        "v_mfma_f32_16x16x32_bf16 v[44:47], a[248:251], v[96:99], v[44:47]\n\t"
        "ds_read_b64_tr_b16 v[238:239], v132 offset:49920\n\t"
        "ds_read_b64_tr_b16 v[240:241], v133 offset:49152\n\t"
        "s_waitcnt lgkmcnt(10)\n\t"
        "v_mfma_f32_16x16x32_bf16 v[116:119], v[72:75], v[164:167], 0\n\t"
        "ds_read_b64_tr_b16 v[242:243], v133 offset:49920\n\t"
        "v_mfma_f32_16x16x32_bf16 v[0:3], v[208:211], v[100:103], v[0:3]\n\t"
        "ds_read_b64_tr_b16 v[244:245], v131 offset:55296\n\t"
        "s_waitcnt lgkmcnt(10)\n\t"
        "v_mfma_f32_16x16x32_bf16 v[120:123], v[76:79], v[164:167], 0\n\t"
        "ds_read_b64_tr_b16 v[246:247], v131 offset:56064\n\t"
        "v_mfma_f32_16x16x32_bf16 v[4:7], v[212:215], v[100:103], v[4:7]\n\t"
        "ds_read_b64_tr_b16 v[248:249], v132 offset:55296\n\t"
        "s_waitcnt lgkmcnt(10)\n\t"
        "v_mfma_f32_16x16x32_bf16 v[124:127], v[80:83], v[164:167], 0\n\t"
        "ds_read_b64_tr_b16 v[250:251], v132 offset:56064\n\t"
        "s_cbranch_vccnz .Lx44%=\n\t"
        ".Lx45%=:\n\t"
        "v_mfma_f32_16x16x32_bf16 v[8:11], v[216:219], v[100:103], v[8:11]\n\t"
        "ds_read_b64_tr_b16 v[252:253], v133 offset:55296\n\t"
        "v_cvt_pk_bf16_f32 v48, v48, v49\n\t"
        "v_mfma_f32_16x16x32_bf16 v[12:15], a[252:255], v[100:103], v[12:15]\n\t"
        "ds_read_b128 v[112:115], v130 offset:128\n\t"
        "v_cvt_pk_bf16_f32 v49, v50, v51\n\t"
        "v_mfma_f32_16x16x32_bf16 v[16:19], v[188:191], v[100:103], v[16:19]\n\t"
        "ds_write_b64 v186, v[48:49] offset:12288\n\t"
        "v_mfma_f32_16x16x32_bf16 v[20:23], v[192:195], v[100:103], v[20:23]\n\t"
        "ds_read_b64_tr_b16 v[254:255], v133 offset:56064\n\t"
        "v_cvt_pk_bf16_f32 v52, v52, v53\n\t"
        "v_mfma_f32_16x16x32_bf16 v[24:27], v[196:199], v[100:103], v[24:27]\n\t"
        "ds_read_b128 v[84:87], v130 offset:4224\n\t"
        "v_cvt_pk_bf16_f32 v53, v54, v55\n\t"
        "v_mfma_f32_16x16x32_bf16 v[28:31], v[200:203], v[100:103], v[28:31]\n\t"
        "ds_write_b64 v154, v[52:53] offset:12288\n\t"
        "v_mfma_f32_16x16x32_bf16 v[32:35], v[204:207], v[100:103], v[32:35]\n\t"
        "v_cvt_pk_bf16_f32 v56, v56, v57\n\t"
        "v_cvt_pk_bf16_f32 v57, v58, v59\n\t"
        "v_mfma_f32_16x16x32_bf16 v[12:15], v[208:211], v[104:107], v[12:15]\n\t"
        "ds_write_b64 v186, v[56:57] offset:12352\n\t"
        "v_mfma_f32_16x16x32_bf16 v[16:19], v[212:215], v[104:107], v[16:19]\n\t"
        "v_cvt_pk_bf16_f32 v116, v116, v117\n\t"
        "v_cvt_pk_bf16_f32 v117, v118, v119\n\t"
        "v_mfma_f32_16x16x32_bf16 v[20:23], v[216:219], v[104:107], v[20:23]\n\t"
        "ds_write_b64 v154, v[116:117] offset:12352\n\t"
        "v_mfma_f32_16x16x32_bf16 v[24:27], a[252:255], v[104:107], v[24:27]\n\t"
        "ds_read_b128 v[88:91], v130 offset:8320\n\t"
        "v_cvt_pk_bf16_f32 v120, v120, v121\n\t"
        "v_mfma_f32_16x16x32_bf16 v[28:31], v[188:191], v[104:107], v[28:31]\n\t"
        "v_cvt_pk_bf16_f32 v121, v122, v123\n\t"
        "v_cvt_pk_bf16_f32 v124, v124, v125\n\t"
        "v_mfma_f32_16x16x32_bf16 v[32:35], v[192:195], v[104:107], v[32:35]\n\t"
        "ds_write_b64 v186, v[120:121] offset:12416\n\t"
        "v_mfma_f32_16x16x32_bf16 v[36:39], v[196:199], v[104:107], v[36:39]\n\t"
        "v_cvt_pk_bf16_f32 v125, v126, v127\n\t"
        "v_mfma_f32_16x16x32_bf16 v[40:43], v[200:203], v[104:107], v[40:43]\n\t"
        "ds_write_b64 v154, v[124:125] offset:12416\n\t"
        "v_mfma_f32_16x16x32_bf16 v[44:47], v[204:207], v[104:107], v[44:47]\n\t"
        "s_waitcnt lgkmcnt(6)\n\t"
        "v_mfma_f32_16x16x32_bf16 v[24:27], v[208:211], v[108:111], v[24:27]\n\t"
        "v_mfma_f32_16x16x32_bf16 v[28:31], v[212:215], v[108:111], v[28:31]\n\t"
        "v_mfma_f32_16x16x32_bf16 v[32:35], v[216:219], v[108:111], v[32:35]\n\t"
        "v_mfma_f32_16x16x32_bf16 v[36:39], a[252:255], v[108:111], v[36:39]\n\t"
        "ds_read_b128 v[92:95], v130 offset:12416\n\t"
        "v_mfma_f32_16x16x32_bf16 v[40:43], v[188:191], v[108:111], v[40:43]\n\t"
        "s_waitcnt lgkmcnt(1)\n\t"
        "s_barrier\n\t"
        "v_mfma_f32_16x16x32_bf16 v[44:47], v[192:195], v[108:111], v[44:47]\n\t"
        "ds_read_b128 v[124:127], v182 offset:0\n\t"
        "v_mfma_f32_16x16x32_bf16 v[0:3], v[232:235], v[112:115], v[0:3]\n\t"
        "ds_read_b128 v[132:135], v187 offset:0\n\t"
        "v_mfma_f32_16x16x32_bf16 v[4:7], v[236:239], v[112:115], v[4:7]\n\t"
        "ds_read_b128 v[136:139], v187 offset:16\n\t"
        "v_mfma_f32_16x16x32_bf16 v[8:11], v[240:243], v[112:115], v[8:11]\n\t"
        "ds_read_b32 v140, v187 offset:32\n\t"
        "v_mfma_f32_16x16x32_bf16 v[0:3], v[220:223], v[84:87], v[0:3]\n\t"
        "v_mfma_f32_16x16x32_bf16 v[4:7], v[224:227], v[84:87], v[4:7]\n\t"
        "v_mfma_f32_16x16x32_bf16 v[8:11], v[228:231], v[84:87], v[8:11]\n\t"
        "v_mfma_f32_16x16x32_bf16 v[12:15], v[232:235], v[84:87], v[12:15]\n\t"
        "ds_read_b128 v[96:99], v130 offset:16512\n\t"
        "v_mfma_f32_16x16x32_bf16 v[16:19], v[236:239], v[84:87], v[16:19]\n\t"
        "v_mfma_f32_16x16x32_bf16 v[20:23], v[240:243], v[84:87], v[20:23]\n\t"
        "s_waitcnt lgkmcnt(4)\n\t"
        "v_or3_b32 v124, v124, v125, v126\n\t"
        "v_or_b32 v124, v124, v127\n\t"
        "v_mfma_f32_16x16x32_bf16 v[0:3], v[244:247], v[88:91], v[0:3]\n\t"
        "v_readfirstlane_b32 s8, v124\n\t"
        "v_mfma_f32_16x16x32_bf16 v[4:7], v[248:251], v[88:91], v[4:7]\n\t"
        "s_nop 0\n\t"
        "s_cmp_lg_u32 s8, 0\n\t"
        "s_cbranch_scc1 .Lx46%=\n\t"
        ".Lx47%=:\n\t"
        "v_mfma_f32_16x16x32_bf16 v[8:11], v[252:255], v[88:91], v[8:11]\n\t"
        "ds_read_b128 v[160:163], v185 offset:512\n\t"
        "s_waitcnt lgkmcnt(3)\n\t"
        "v_xor_b32 v132, v181, v132\n\t"
        "v_mfma_f32_16x16x32_bf16 v[12:15], v[220:223], v[88:91], v[12:15]\n\t"
        "ds_read_b128 v[100:103], v130 offset:20544\n\t"
        "v_add_u32 v151, s9, v172\n\t"
        "v_mfma_f32_16x16x32_bf16 v[16:19], v[224:227], v[88:91], v[16:19]\n\t"
        "v_max_u32 v151, v151, v132\n\t"
        "v_xor_b32 v133, v181, v133\n\t"
        "v_mfma_f32_16x16x32_bf16 v[20:23], v[228:231], v[88:91], v[20:23]\n\t"
        "v_add_u32 v183, s9, v173\n\t"
        "v_max_u32 v183, v183, v133\n\t"
        "v_mfma_f32_16x16x32_bf16 v[24:27], v[232:235], v[88:91], v[24:27]\n\t"
        "ds_read_b128 v[104:107], v130 offset:20608\n\t"
        "v_xor_b32 v134, v181, v134\n\t"
        "s_add_u32 s55, s41, 0\n\t"
        "v_mfma_f32_16x16x32_bf16 v[28:31], v[236:239], v[88:91], v[28:31]\n\t"
        "v_cvt_pk_bf16_f32 v116, v0, v1\n\t"
        "v_cvt_pk_bf16_f32 v117, v2, v3\n\t"
        "v_mfma_f32_16x16x32_bf16 v[32:35], v[240:243], v[88:91], v[32:35]\n\t"
        "v_cvt_pk_bf16_f32 v118, v4, v5\n\t"
        "v_cvt_pk_bf16_f32 v119, v6, v7\n\t"
        "v_mfma_f32_16x16x32_bf16 v[12:15], v[244:247], v[92:95], v[12:15]\n\t"
        "ds_read_b128 v[108:111], v151 offset:0\n\t"
        "v_cvt_pk_bf16_f32 v120, v8, v9\n\t"
        "v_mfma_f32_16x16x32_bf16 v[16:19], v[248:251], v[92:95], v[16:19]\n\t"
        "v_cvt_pk_bf16_f32 v121, v10, v11\n\t"
        "v_mfma_f32_16x16x32_bf16 v[20:23], v[252:255], v[92:95], v[20:23]\n\t"
        "v_permlane16_swap_b32 v116, v118\n\t"
        "s_waitcnt lgkmcnt(3)\n\t"
        "v_lshlrev_b32_sdwa v157, 5, v160 dst_sel:DWORD dst_unused:UNUSED_PAD src0_sel:DWORD src1_sel:WORD_0\n\t"
        "v_mfma_f32_16x16x32_bf16 v[24:27], v[220:223], v[92:95], v[24:27]\n\t"
        "v_permlane16_swap_b32 v117, v119\n\t"
        "v_mfma_f32_16x16x32_bf16 v[28:31], v[224:227], v[92:95], v[28:31]\n\t"
        "buffer_store_dwordx4 v[116:119], v144, s[28:31], s55 offen sc1\n\t"
        "v_mfma_f32_16x16x32_bf16 v[32:35], v[228:231], v[92:95], v[32:35]\n\t"
        "ds_write_b64 v146, v[120:121] offset:0\n\t"
        "v_mfma_f32_16x16x32_bf16 v[36:39], v[232:235], v[92:95], v[36:39]\n\t"
        "ds_read_b128 v[112:115], v151 offset:64\n\t"
        "s_add_u32 s55, s41, 43008\n\t"
        "v_mfma_f32_16x16x32_bf16 v[40:43], v[236:239], v[92:95], v[40:43]\n\t"
        "ds_read_b128 v[84:87], v151 offset:128\n\t"
        "v_add_u32 v151, s9, v174\n\t"
        "v_max_u32 v151, v151, v134\n\t"
        "v_mfma_f32_16x16x32_bf16 v[44:47], v[240:243], v[92:95], v[44:47]\n\t"
        "v_xor_b32 v135, v181, v135\n\t"
        "v_cvt_pk_bf16_f32 v116, v12, v13\n\t"
        "v_mfma_f32_16x16x32_bf16 v[24:27], v[244:247], v[96:99], v[24:27]\n\t"
        "ds_read_b128 v[88:91], v183 offset:0\n\t"
        "v_cvt_pk_bf16_f32 v117, v14, v15\n\t"
        "v_mfma_f32_16x16x32_bf16 v[28:31], v[248:251], v[96:99], v[28:31]\n\t"
        "v_cvt_pk_bf16_f32 v118, v16, v17\n\t"
        "v_cvt_pk_bf16_f32 v119, v18, v19\n\t"
        "v_mfma_f32_16x16x32_bf16 v[32:35], v[252:255], v[96:99], v[32:35]\n\t"
        "v_cvt_pk_bf16_f32 v120, v20, v21\n\t"
        "v_cvt_pk_bf16_f32 v121, v22, v23\n\t"
        "v_mfma_f32_16x16x32_bf16 v[36:39], v[220:223], v[96:99], v[36:39]\n\t"
        "v_permlane16_swap_b32 v116, v118\n\t"
        "v_lshlrev_b32_sdwa v158, 5, v160 dst_sel:DWORD dst_unused:UNUSED_PAD src0_sel:DWORD src1_sel:WORD_1\n\t"
        "v_mfma_f32_16x16x32_bf16 v[40:43], v[224:227], v[96:99], v[40:43]\n\t"
        "v_permlane16_swap_b32 v117, v119\n\t"
        "v_mfma_f32_16x16x32_bf16 v[44:47], v[228:231], v[96:99], v[44:47]\n\t"
        "ds_read_b128 v[92:95], v183 offset:64\n\t"
        "v_or_b32 v157, v184, v157\n\t"
        "s_waitcnt lgkmcnt(4)\n\t"
        "v_mfma_f32_16x16x32_bf16 v[36:39], v[208:211], v[100:103], v[36:39]\n\t"
        "ds_read_b64_tr_b16 v[60:61], v157 offset:0\n\t"
        "v_or_b32 v158, v184, v158\n\t"
        "v_mfma_f32_16x16x32_bf16 v[40:43], v[212:215], v[100:103], v[40:43]\n\t"
        "ds_read_b64_tr_b16 v[62:63], v158 offset:0\n\t"
        "v_xor_b32 v159, 32, v157\n\t"
        "v_mfma_f32_16x16x32_bf16 v[44:47], v[216:219], v[100:103], v[44:47]\n\t"
        "ds_read_b64_tr_b16 v[64:65], v159 offset:0\n\t"
        "v_xor_b32 v152, 32, v158\n\t"
        "v_perm_b32 v164, v162, v162, v168\n\t"
        "v_mfma_f32_16x16x32_bf16 v[36:39], v[244:247], v[104:107], v[36:39]\n\t"
        "buffer_store_dwordx4 v[116:119], v144, s[28:31], s55 offen sc1\n\t"
        "v_mfma_f32_16x16x32_bf16 v[40:43], v[248:251], v[104:107], v[40:43]\n\t"
        "ds_write_b64 v146, v[120:121] offset:2048\n\t"
        "v_mfma_f32_16x16x32_bf16 v[44:47], v[252:255], v[104:107], v[44:47]\n\t"
        "ds_read_b128 v[96:99], v183 offset:128\n\t"
        "v_add_u32 v183, s9, v175\n\t"
        "v_mfma_f32_16x16x32_bf16 v[48:51], a[12:15], v[108:111], 0\n\t"
        "ds_read_b64_tr_b16 v[66:67], v152 offset:0\n\t"
        "v_max_u32 v183, v183, v135\n\t"
        "v_mfma_f32_16x16x32_bf16 v[52:55], a[16:19], v[108:111], 0\n\t"
        "ds_read_b64_tr_b16 v[68:69], v157 offset:64\n\t"
        "v_xor_b32 v136, v181, v136\n\t"
        "s_add_u32 s55, s41, 86016\n\t"
        "v_mfma_f32_16x16x32_bf16 v[56:59], a[20:23], v[108:111], 0\n\t"
        "ds_read_b128 v[100:103], v151 offset:0\n\t"
        "v_cvt_pk_bf16_f32 v116, v24, v25\n\t"
        "s_waitcnt lgkmcnt(7)\n\t"
        "v_mfma_f32_16x16x32_bf16 v[48:51], a[48:51], v[112:115], v[48:51]\n\t"
        "ds_read_b64_tr_b16 v[70:71], v158 offset:64\n\t"
        "v_cvt_pk_bf16_f32 v117, v26, v27\n\t"
        "v_mfma_f32_16x16x32_bf16 v[52:55], a[52:55], v[112:115], v[52:55]\n\t"
        "ds_read_b64_tr_b16 v[72:73], v159 offset:64\n\t"
        "v_cvt_pk_bf16_f32 v118, v28, v29\n\t"
        "v_mfma_f32_16x16x32_bf16 v[56:59], a[56:59], v[112:115], v[56:59]\n\t"
        "ds_read_b128 v[104:107], v151 offset:64\n\t"
        "v_cvt_pk_bf16_f32 v119, v30, v31\n\t"
        "v_mfma_f32_16x16x32_bf16 v[48:51], a[84:87], v[84:87], v[48:51]\n\t"
        "ds_read_b64_tr_b16 v[74:75], v152 offset:64\n\t"
        "v_cvt_pk_bf16_f32 v120, v32, v33\n\t"
        "v_cvt_pk_bf16_f32 v121, v34, v35\n\t"
        "v_mfma_f32_16x16x32_bf16 v[52:55], a[88:91], v[84:87], v[52:55]\n\t"
        "v_permlane16_swap_b32 v116, v118\n\t"
        "v_mfma_f32_16x16x32_bf16 v[56:59], a[92:95], v[84:87], v[56:59]\n\t"
        "v_permlane16_swap_b32 v117, v119\n\t"
        "v_mfma_f32_16x16x32_bf16 v[48:51], a[120:123], v[88:91], v[48:51]\n\t"
        "ds_read_b128 v[108:111], v151 offset:128\n\t"
        "v_add_u32 v151, s9, v176\n\t"
        "v_mfma_f32_16x16x32_bf16 v[52:55], a[124:127], v[88:91], v[52:55]\n\t"
        "ds_read_b64_tr_b16 v[76:77], v157 offset:128\n\t"
        "v_max_u32 v151, v151, v136\n\t"
        "v_mfma_f32_16x16x32_bf16 v[56:59], a[128:131], v[88:91], v[56:59]\n\t"
        "buffer_store_dwordx4 v[116:119], v144, s[28:31], s55 offen sc1\n\t"
        "v_mfma_f32_16x16x32_bf16 v[48:51], a[156:159], v[92:95], v[48:51]\n\t"
        "ds_write_b64 v146, v[120:121] offset:4096\n\t"
        "v_mfma_f32_16x16x32_bf16 v[52:55], a[160:163], v[92:95], v[52:55]\n\t"
        "ds_read_b128 v[112:115], v183 offset:0\n\t"
        "v_xor_b32 v137, v181, v137\n\t"
        "s_add_u32 s55, s41, 129024\n\t"
        "v_mfma_f32_16x16x32_bf16 v[56:59], a[164:167], v[92:95], v[56:59]\n\t"
        "ds_read_b128 v[84:87], v183 offset:64\n\t"
        "v_cvt_pk_bf16_f32 v116, v36, v37\n\t"
        "s_waitcnt lgkmcnt(5)\n\t"
        "v_mfma_f32_16x16x32_bf16 v[48:51], a[192:195], v[96:99], v[48:51]\n\t"
        "ds_read_b64_tr_b16 v[78:79], v158 offset:128\n\t"
        "v_cvt_pk_bf16_f32 v117, v38, v39\n\t"
        "v_mfma_f32_16x16x32_bf16 v[52:55], a[196:199], v[96:99], v[52:55]\n\t"
        "ds_read_b64_tr_b16 v[80:81], v159 offset:128\n\t"
        "v_cvt_pk_bf16_f32 v118, v40, v41\n\t"
        "v_mfma_f32_16x16x32_bf16 v[56:59], a[200:203], v[96:99], v[56:59]\n\t"
        "ds_read_b128 v[88:91], v183 offset:128\n\t"
        "v_add_u32 v183, s9, v177\n\t"
        "v_max_u32 v183, v183, v137\n\t"
        "v_mfma_f32_16x16x32_bf16 v[48:51], a[228:231], v[100:103], v[48:51]\n\t"
        "ds_read_b64_tr_b16 v[82:83], v152 offset:128\n\t"
        "v_xor_b32 v138, v181, v138\n\t"
        "v_mfma_f32_16x16x32_bf16 v[52:55], a[232:235], v[100:103], v[52:55]\n\t"
        "ds_read_b128 v[92:95], v151 offset:0\n\t"
        "v_cvt_pk_bf16_f32 v119, v42, v43\n\t"
        "v_mfma_f32_16x16x32_bf16 v[56:59], a[236:239], v[100:103], v[56:59]\n\t"
        "ds_read_b128 v[96:99], v151 offset:64\n\t"
        "v_cvt_pk_bf16_f32 v120, v44, v45\n\t"
        "v_mfma_f32_16x16x32_bf16 v[48:51], v[196:199], v[104:107], v[48:51]\n\t"
        "ds_read_b128 v[100:103], v151 offset:128\n\t"
        "v_add_u32 v151, s9, v178\n\t"
        "v_mfma_f32_16x16x32_bf16 v[52:55], v[200:203], v[104:107], v[52:55]\n\t"
        "v_max_u32 v151, v151, v138\n\t"
        "v_xor_b32 v139, v181, v139\n\t"
        "v_cvt_pk_bf16_f32 v121, v46, v47\n\t"
        "v_mfma_f32_16x16x32_bf16 v[56:59], v[204:207], v[104:107], v[56:59]\n\t"
        "v_permlane16_swap_b32 v116, v118\n\t"
        "s_waitcnt lgkmcnt(7)\n\t"
        "v_mfma_f32_16x16x32_bf16 v[48:51], v[232:235], v[108:111], v[48:51]\n\t"
        "v_permlane16_swap_b32 v117, v119\n\t"
        "v_mfma_f32_16x16x32_bf16 v[52:55], v[236:239], v[108:111], v[52:55]\n\t"
        "buffer_store_dwordx4 v[116:119], v144, s[28:31], s55 offen sc1\n\t"
        "v_mfma_f32_16x16x32_bf16 v[56:59], v[240:243], v[108:111], v[56:59]\n\t"
        "ds_write_b64 v146, v[120:121] offset:6144\n\t"
        "v_mfma_f32_16x16x32_bf16 v[48:51], a[0:3], v[112:115], v[48:51]\n\t"
        "ds_read_b128 v[104:107], v183 offset:0\n\t"
        "v_perm_b32 v165, v162, v162, v169\n\t"
        "v_perm_b32 v166, v162, v162, v170\n\t"
        "v_mfma_f32_16x16x32_bf16 v[52:55], a[4:7], v[112:115], v[52:55]\n\t"
        "ds_read_b128 v[108:111], v183 offset:64\n\t"
        "v_perm_b32 v167, v162, v162, v171\n\t"
        "v_mfma_f32_16x16x32_bf16 v[56:59], a[8:11], v[112:115], v[56:59]\n\t"
        "v_cmp_ne_u32 vcc, 0, v163\n\t"
        "v_mfma_f32_16x16x32_bf16 v[0:3], v[60:63], v[164:167], 0\n\t"
        "v_mfma_f32_16x16x32_bf16 v[48:51], a[36:39], v[84:87], v[48:51]\n\t"
        "ds_read_b128 v[112:115], v183 offset:128\n\t"
        "v_add_u32 v183, s9, v179\n\t"
        "v_mfma_f32_16x16x32_bf16 v[4:7], v[64:67], v[164:167], 0\n\t"
        "v_max_u32 v183, v183, v139\n\t"
        "v_mfma_f32_16x16x32_bf16 v[52:55], a[40:43], v[84:87], v[52:55]\n\t"
        "v_mfma_f32_16x16x32_bf16 v[8:11], v[68:71], v[164:167], 0\n\t"
        "v_mfma_f32_16x16x32_bf16 v[56:59], a[44:47], v[84:87], v[56:59]\n\t"
        "v_mfma_f32_16x16x32_bf16 v[12:15], v[72:75], v[164:167], 0\n\t"
        "s_waitcnt lgkmcnt(3)\n\t"
        "v_mfma_f32_16x16x32_bf16 v[48:51], a[72:75], v[88:91], v[48:51]\n\t"
        "ds_read_b128 v[84:87], v151 offset:0\n\t"
        "v_mfma_f32_16x16x32_bf16 v[16:19], v[76:79], v[164:167], 0\n\t"
        "v_mfma_f32_16x16x32_bf16 v[52:55], a[76:79], v[88:91], v[52:55]\n\t"
        "v_mfma_f32_16x16x32_bf16 v[20:23], v[80:83], v[164:167], 0\n\t"
        "s_cbranch_vccnz .Lx48%=\n\t"
        ".Lx49%=:\n\t"
        "v_cvt_pk_bf16_f32 v0, v0, v1\n\t"
        "v_cvt_pk_bf16_f32 v1, v2, v3\n\t"
        "v_cvt_pk_bf16_f32 v4, v4, v5\n\t"
        "v_mfma_f32_16x16x32_bf16 v[56:59], a[80:83], v[88:91], v[56:59]\n\t"
        "ds_write_b64 v186, v[0:1] offset:24576\n\t"
        "v_mfma_f32_16x16x32_bf16 v[48:51], a[108:111], v[92:95], v[48:51]\n\t"
        "ds_read_b128 v[88:91], v151 offset:64\n\t"
        "v_cvt_pk_bf16_f32 v5, v6, v7\n\t"
        "v_cvt_pk_bf16_f32 v8, v8, v9\n\t"
        "v_mfma_f32_16x16x32_bf16 v[52:55], a[112:115], v[92:95], v[52:55]\n\t"
        "ds_write_b64 v154, v[4:5] offset:24576\n\t"
        "v_mfma_f32_16x16x32_bf16 v[56:59], a[116:119], v[92:95], v[56:59]\n\t"
        "v_cvt_pk_bf16_f32 v9, v10, v11\n\t"
        "v_cvt_pk_bf16_f32 v12, v12, v13\n\t"
        "v_mfma_f32_16x16x32_bf16 v[48:51], a[144:147], v[96:99], v[48:51]\n\t"
        "ds_write_b64 v186, v[8:9] offset:24640\n\t"
        "v_mfma_f32_16x16x32_bf16 v[52:55], a[148:151], v[96:99], v[52:55]\n\t"
        "ds_read_b128 v[92:95], v151 offset:128\n\t"
        "v_cvt_pk_bf16_f32 v13, v14, v15\n\t"
        "v_cvt_pk_bf16_f32 v16, v16, v17\n\t"
        "v_mfma_f32_16x16x32_bf16 v[56:59], a[152:155], v[96:99], v[56:59]\n\t"
        "ds_write_b64 v154, v[12:13] offset:24640\n\t"
        "v_mfma_f32_16x16x32_bf16 v[48:51], a[180:183], v[100:103], v[48:51]\n\t"
        "ds_read_b128 v[96:99], v183 offset:0\n\t"
        "v_cvt_pk_bf16_f32 v17, v18, v19\n\t"
        "v_mfma_f32_16x16x32_bf16 v[52:55], a[184:187], v[100:103], v[52:55]\n\t"
        "ds_write_b64 v186, v[16:17] offset:24704\n\t"
        "v_mfma_f32_16x16x32_bf16 v[56:59], a[188:191], v[100:103], v[56:59]\n\t"
        "v_cvt_pk_bf16_f32 v20, v20, v21\n\t"
        "v_cvt_pk_bf16_f32 v21, v22, v23\n\t"
        "s_waitcnt lgkmcnt(6)\n\t"
        "v_mfma_f32_16x16x32_bf16 v[48:51], a[216:219], v[104:107], v[48:51]\n\t"
        "ds_write_b64 v154, v[20:21] offset:24704\n\t"
        "v_mfma_f32_16x16x32_bf16 v[52:55], a[220:223], v[104:107], v[52:55]\n\t"
        "s_cmp_gt_u32 s61, 48\n\t"
        "s_cbranch_scc1 .Lx50%=\n\t"
        ".Lx51%=:\n\t"
        "v_mfma_f32_16x16x32_bf16 v[56:59], a[224:227], v[104:107], v[56:59]\n\t"
        "ds_read_b128 v[100:103], v183 offset:64\n\t"
        "v_mfma_f32_16x16x32_bf16 v[48:51], a[252:255], v[108:111], v[48:51]\n\t"
        "ds_read_b128 v[104:107], v183 offset:128\n\t"
        "v_mfma_f32_16x16x32_bf16 v[52:55], v[188:191], v[108:111], v[52:55]\n\t"
        "v_mfma_f32_16x16x32_bf16 v[56:59], v[192:195], v[108:111], v[56:59]\n\t"
        "v_mfma_f32_16x16x32_bf16 v[48:51], v[220:223], v[112:115], v[48:51]\n\t"
        "s_waitcnt lgkmcnt(2)\n\t"
        "s_barrier\n\t"
        "v_xor_b32 v140, v181, v140\n\t"
        "v_mfma_f32_16x16x32_bf16 v[52:55], v[224:227], v[112:115], v[52:55]\n\t"
        "ds_read_b128 v[132:135], v187 offset:4096\n\t"
        "v_mfma_f32_16x16x32_bf16 v[56:59], v[228:231], v[112:115], v[56:59]\n\t"
        "ds_read_b128 v[136:139], v187 offset:4112\n\t"
        "v_add_u32 v151, s9, v180\n\t"
        "v_mfma_f32_16x16x32_bf16 v[48:51], a[24:27], v[84:87], v[48:51]\n\t"
        "ds_read_b128 v[124:127], v182 offset:16\n\t"
        "v_max_u32 v151, v151, v140\n\t"
        "v_mfma_f32_16x16x32_bf16 v[52:55], a[28:31], v[84:87], v[52:55]\n\t"
        "ds_read_b32 v140, v187 offset:4128\n\t"
        "s_waitcnt lgkmcnt(3)\n\t"
        "v_xor_b32 v132, v181, v132\n\t"
        "v_mfma_f32_16x16x32_bf16 v[56:59], a[32:35], v[84:87], v[56:59]\n\t"
        "ds_read_b128 v[108:111], v151 offset:0\n\t"
        "v_readlane_b32 s9, v155, 48\n\t"
        "v_mfma_f32_16x16x32_bf16 v[48:51], a[60:63], v[88:91], v[48:51]\n\t"
        "ds_read_b128 v[112:115], v151 offset:64\n\t"
        "v_add_u32 v183, s9, v172\n\t"
        "v_mfma_f32_16x16x32_bf16 v[52:55], a[64:67], v[88:91], v[52:55]\n\t"
        "ds_read_b128 v[84:87], v151 offset:128\n\t"
        "v_max_u32 v183, v183, v132\n\t"
        "v_mfma_f32_16x16x32_bf16 v[56:59], a[68:71], v[88:91], v[56:59]\n\t"
        "v_xor_b32 v133, v181, v133\n\t"
        "v_add_u32 v151, s9, v173\n\t"
        "v_mfma_f32_16x16x32_bf16 v[48:51], a[96:99], v[92:95], v[48:51]\n\t"
        "ds_read_b128 v[88:91], v183 offset:0\n\t"
        "v_max_u32 v151, v151, v133\n\t"
        "v_mfma_f32_16x16x32_bf16 v[52:55], a[100:103], v[92:95], v[52:55]\n\t"
        "v_xor_b32 v134, v181, v134\n\t"
        "v_mfma_f32_16x16x32_bf16 v[56:59], a[104:107], v[92:95], v[56:59]\n\t"
        "v_mfma_f32_16x16x32_bf16 v[48:51], a[132:135], v[96:99], v[48:51]\n\t"
        "ds_read_b128 v[92:95], v183 offset:64\n\t"
        "s_waitcnt lgkmcnt(6)\n\t"
        "v_or3_b32 v124, v124, v125, v126\n\t"
        "v_mfma_f32_16x16x32_bf16 v[52:55], a[136:139], v[96:99], v[52:55]\n\t"
        "v_or_b32 v124, v124, v127\n\t"
        "v_mfma_f32_16x16x32_bf16 v[56:59], a[140:143], v[96:99], v[56:59]\n\t"
        "v_readfirstlane_b32 s8, v124\n\t"
        "v_mfma_f32_16x16x32_bf16 v[48:51], a[168:171], v[100:103], v[48:51]\n\t"
        "s_nop 0\n\t"
        "s_cmp_lg_u32 s8, 0\n\t"
        "s_cbranch_scc1 99f\n\t"
        "v_mfma_f32_16x16x32_bf16 v[52:55], a[172:175], v[100:103], v[52:55]\n\t"
        "ds_read_b128 v[124:127], v147 offset:0\n\t"
        "v_mfma_f32_16x16x32_bf16 v[56:59], a[176:179], v[100:103], v[56:59]\n\t"
        "ds_read_b128 v[96:99], v183 offset:128\n\t"
        "v_add_u32 v183, s9, v174\n\t"
        "v_mfma_f32_16x16x32_bf16 v[48:51], a[204:207], v[104:107], v[48:51]\n\t"
        "ds_read_b128 v[100:103], v151 offset:0\n\t"
        "v_max_u32 v183, v183, v134\n\t"
        "v_mfma_f32_16x16x32_bf16 v[52:55], a[208:211], v[104:107], v[52:55]\n\t"
        "v_xor_b32 v135, v181, v135\n\t"
        "v_mfma_f32_16x16x32_bf16 v[56:59], a[212:215], v[104:107], v[56:59]\n\t"
        "s_waitcnt lgkmcnt(4)\n\t"
        "v_mfma_f32_16x16x32_bf16 v[48:51], a[240:243], v[108:111], v[48:51]\n\t"
        "ds_read_b128 v[104:107], v151 offset:64\n\t"
        "v_mfma_f32_16x16x32_bf16 v[52:55], a[244:247], v[108:111], v[52:55]\n\t"
        "v_mfma_f32_16x16x32_bf16 v[56:59], a[248:251], v[108:111], v[56:59]\n\t"
        "v_mfma_f32_16x16x32_bf16 v[48:51], v[208:211], v[112:115], v[48:51]\n\t"
        "ds_read_b128 v[108:111], v151 offset:128\n\t"
        "v_add_u32 v151, s9, v175\n\t"
        "v_mfma_f32_16x16x32_bf16 v[52:55], v[212:215], v[112:115], v[52:55]\n\t"
        "s_waitcnt lgkmcnt(4)\n\t"
        "buffer_store_dwordx4 v[124:127], v145, s[28:31], s50 offen sc1\n\t"
        "v_mfma_f32_16x16x32_bf16 v[56:59], v[216:219], v[112:115], v[56:59]\n\t"
        "ds_read_b128 v[124:127], v147 offset:4096\n\t"
        "v_max_u32 v151, v151, v135\n\t"
        "v_mfma_f32_16x16x32_bf16 v[48:51], v[244:247], v[84:87], v[48:51]\n\t"
        "ds_read_b128 v[112:115], v183 offset:0\n\t"
        "v_xor_b32 v136, v181, v136\n\t"
        "v_mfma_f32_16x16x32_bf16 v[52:55], v[248:251], v[84:87], v[52:55]\n\t"
        "s_add_u32 s50, s50, 86016\n\t"
        "v_mfma_f32_16x16x32_bf16 v[56:59], v[252:255], v[84:87], v[56:59]\n\t"
        "v_mfma_f32_16x16x32_bf16 v[72:75], a[12:15], v[88:91], 0\n\t"
        "ds_read_b128 v[84:87], v183 offset:64\n\t"
        "v_mfma_f32_16x16x32_bf16 v[76:79], a[16:19], v[88:91], 0\n\t"
        "v_mfma_f32_16x16x32_bf16 v[80:83], a[20:23], v[88:91], 0\n\t"
        "v_mfma_f32_16x16x32_bf16 v[72:75], a[48:51], v[92:95], v[72:75]\n\t"
        "ds_read_b128 v[88:91], v183 offset:128\n\t"
        "v_add_u32 v183, s9, v176\n\t"
        "v_mfma_f32_16x16x32_bf16 v[76:79], a[52:55], v[92:95], v[76:79]\n\t"
        "v_max_u32 v183, v183, v136\n\t"
        "v_xor_b32 v137, v181, v137\n\t"
        "v_mfma_f32_16x16x32_bf16 v[80:83], a[56:59], v[92:95], v[80:83]\n\t"
        "s_waitcnt lgkmcnt(4)\n\t"
        "v_mfma_f32_16x16x32_bf16 v[72:75], a[84:87], v[96:99], v[72:75]\n\t"
        "ds_read_b128 v[92:95], v151 offset:0\n\t"
        "v_mfma_f32_16x16x32_bf16 v[76:79], a[88:91], v[96:99], v[76:79]\n\t"
        "v_mfma_f32_16x16x32_bf16 v[80:83], a[92:95], v[96:99], v[80:83]\n\t"
        "v_mfma_f32_16x16x32_bf16 v[72:75], a[120:123], v[100:103], v[72:75]\n\t"
        "s_waitcnt lgkmcnt(3)\n\t"
        "buffer_store_dwordx4 v[124:127], v145, s[28:31], s50 offen sc1\n\t"
        "v_mfma_f32_16x16x32_bf16 v[76:79], a[124:127], v[100:103], v[76:79]\n\t"
        "ds_read_b128 v[96:99], v151 offset:64\n\t"
        "v_readlane_b32 s55, v155, 49\n\t"
        "v_mfma_f32_16x16x32_bf16 v[80:83], a[128:131], v[100:103], v[80:83]\n\t"
        "v_cvt_pk_bf16_f32 v116, v48, v49\n\t"
        "v_cvt_pk_bf16_f32 v117, v50, v51\n\t"
        "v_mfma_f32_16x16x32_bf16 v[72:75], a[156:159], v[104:107], v[72:75]\n\t"
        "ds_read_b128 v[100:103], v151 offset:128\n\t"
        "v_add_u32 v151, s9, v177\n\t"
        "v_mfma_f32_16x16x32_bf16 v[76:79], a[160:163], v[104:107], v[76:79]\n\t"
        "v_max_u32 v151, v151, v137\n\t"
        "v_xor_b32 v138, v181, v138\n\t"
        "v_mfma_f32_16x16x32_bf16 v[80:83], a[164:167], v[104:107], v[80:83]\n\t"
        "v_cvt_pk_bf16_f32 v118, v52, v53\n\t"
        "v_cvt_pk_bf16_f32 v119, v54, v55\n\t"
        "v_mfma_f32_16x16x32_bf16 v[72:75], a[192:195], v[108:111], v[72:75]\n\t"
        "ds_read_b128 v[104:107], v183 offset:0\n\t"
        "v_cvt_pk_bf16_f32 v120, v56, v57\n\t"
        "v_mfma_f32_16x16x32_bf16 v[76:79], a[196:199], v[108:111], v[76:79]\n\t"
        "v_cvt_pk_bf16_f32 v121, v58, v59\n\t"
        "v_mfma_f32_16x16x32_bf16 v[80:83], a[200:203], v[108:111], v[80:83]\n\t"
        "v_permlane16_swap_b32 v116, v118\n\t"
        "v_mfma_f32_16x16x32_bf16 v[72:75], a[228:231], v[112:115], v[72:75]\n\t"
        "v_permlane16_swap_b32 v117, v119\n\t"
        "v_mfma_f32_16x16x32_bf16 v[76:79], a[232:235], v[112:115], v[76:79]\n\t"
        "ds_read_b128 v[108:111], v183 offset:64\n\t"
        "v_mfma_f32_16x16x32_bf16 v[80:83], a[236:239], v[112:115], v[80:83]\n\t"
        "s_waitcnt lgkmcnt(3)\n\t"
        "v_mfma_f32_16x16x32_bf16 v[72:75], v[196:199], v[84:87], v[72:75]\n\t"
        "ds_read_b128 v[112:115], v183 offset:128\n\t"
        "v_add_u32 v183, s9, v178\n\t"
        "v_mfma_f32_16x16x32_bf16 v[76:79], v[200:203], v[84:87], v[76:79]\n\t"
        "v_max_u32 v183, v183, v138\n\t"
        "v_xor_b32 v139, v181, v139\n\t"
        "v_mfma_f32_16x16x32_bf16 v[80:83], v[204:207], v[84:87], v[80:83]\n\t"
        "buffer_store_dwordx4 v[116:119], v148, s[28:31], s55 offen sc1\n\t"
        "v_mfma_f32_16x16x32_bf16 v[72:75], v[232:235], v[88:91], v[72:75]\n\t"
        "ds_write_b64 v146, v[120:121] offset:8192\n\t"
        "v_mfma_f32_16x16x32_bf16 v[76:79], v[236:239], v[88:91], v[76:79]\n\t"
        "ds_read_b128 v[84:87], v151 offset:0\n\t"
        "v_mfma_f32_16x16x32_bf16 v[80:83], v[240:243], v[88:91], v[80:83]\n\t"
        "v_mfma_f32_16x16x32_bf16 v[72:75], a[0:3], v[92:95], v[72:75]\n\t"
        "ds_read_b128 v[88:91], v151 offset:64\n\t"
        "v_mfma_f32_16x16x32_bf16 v[76:79], a[4:7], v[92:95], v[76:79]\n\t"
        "v_mfma_f32_16x16x32_bf16 v[80:83], a[8:11], v[92:95], v[80:83]\n\t"
        "v_mfma_f32_16x16x32_bf16 v[72:75], a[36:39], v[96:99], v[72:75]\n\t"
        "ds_read_b128 v[92:95], v151 offset:128\n\t"
        "v_add_u32 v151, s9, v179\n\t"
        "v_mfma_f32_16x16x32_bf16 v[76:79], a[40:43], v[96:99], v[76:79]\n\t"
        "v_max_u32 v151, v151, v139\n\t"
        "v_xor_b32 v140, v181, v140\n\t"
        "v_mfma_f32_16x16x32_bf16 v[80:83], a[44:47], v[96:99], v[80:83]\n\t"
        "s_waitcnt lgkmcnt(4)\n\t"
        "v_mfma_f32_16x16x32_bf16 v[72:75], a[72:75], v[100:103], v[72:75]\n\t"
        "ds_read_b128 v[96:99], v183 offset:0\n\t"
        "v_mfma_f32_16x16x32_bf16 v[76:79], a[76:79], v[100:103], v[76:79]\n\t"
        "v_mfma_f32_16x16x32_bf16 v[80:83], a[80:83], v[100:103], v[80:83]\n\t"
        "v_mfma_f32_16x16x32_bf16 v[72:75], a[108:111], v[104:107], v[72:75]\n\t"
        "ds_read_b128 v[100:103], v183 offset:64\n\t"
        "v_mfma_f32_16x16x32_bf16 v[76:79], a[112:115], v[104:107], v[76:79]\n\t"
        "v_mfma_f32_16x16x32_bf16 v[80:83], a[116:119], v[104:107], v[80:83]\n\t"
        "ds_read_b128 v[104:107], v183 offset:128\n\t"
        "v_add_u32 v183, s9, v180\n\t"
        "v_mfma_f32_16x16x32_bf16 v[72:75], a[144:147], v[108:111], v[72:75]\n\t"
        "v_max_u32 v183, v183, v140\n\t"
        "v_mfma_f32_16x16x32_bf16 v[76:79], a[148:151], v[108:111], v[76:79]\n\t"
        "v_mfma_f32_16x16x32_bf16 v[80:83], a[152:155], v[108:111], v[80:83]\n\t"
        "ds_read_b128 v[108:111], v151 offset:0\n\t"
        "v_mfma_f32_16x16x32_bf16 v[72:75], a[180:183], v[112:115], v[72:75]\n\t"
        "v_mfma_f32_16x16x32_bf16 v[76:79], a[184:187], v[112:115], v[76:79]\n\t"
        "v_mfma_f32_16x16x32_bf16 v[80:83], a[188:191], v[112:115], v[80:83]\n\t"
        "ds_read_b128 v[112:115], v151 offset:64\n\t"
        "s_waitcnt lgkmcnt(4)\n\t"
        "v_mfma_f32_16x16x32_bf16 v[72:75], a[216:219], v[84:87], v[72:75]\n\t"
        "v_mfma_f32_16x16x32_bf16 v[76:79], a[220:223], v[84:87], v[76:79]\n\t"
        "v_mfma_f32_16x16x32_bf16 v[80:83], a[224:227], v[84:87], v[80:83]\n\t"
        "ds_read_b128 v[84:87], v151 offset:128\n\t"
        "v_mfma_f32_16x16x32_bf16 v[72:75], a[252:255], v[88:91], v[72:75]\n\t"
        "v_mfma_f32_16x16x32_bf16 v[76:79], v[188:191], v[88:91], v[76:79]\n\t"
        "v_mfma_f32_16x16x32_bf16 v[80:83], v[192:195], v[88:91], v[80:83]\n\t"
        "ds_read_b128 v[88:91], v183 offset:0\n\t"
        "v_mfma_f32_16x16x32_bf16 v[72:75], v[220:223], v[92:95], v[72:75]\n\t"
        "v_mfma_f32_16x16x32_bf16 v[76:79], v[224:227], v[92:95], v[76:79]\n\t"
        "v_mfma_f32_16x16x32_bf16 v[80:83], v[228:231], v[92:95], v[80:83]\n\t"
        "ds_read_b128 v[92:95], v183 offset:64\n\t"
        "v_mfma_f32_16x16x32_bf16 v[72:75], a[24:27], v[96:99], v[72:75]\n\t"
        "v_mfma_f32_16x16x32_bf16 v[76:79], a[28:31], v[96:99], v[76:79]\n\t"
        "v_mfma_f32_16x16x32_bf16 v[80:83], a[32:35], v[96:99], v[80:83]\n\t"
        "ds_read_b128 v[96:99], v183 offset:128\n\t"
        "s_waitcnt lgkmcnt(4)\n\t"
        "v_mfma_f32_16x16x32_bf16 v[80:83], a[68:71], v[100:103], v[80:83]\n\t"
        "v_mfma_f32_16x16x32_bf16 v[80:83], a[104:107], v[104:107], v[80:83]\n\t"
        "v_mfma_f32_16x16x32_bf16 v[80:83], a[140:143], v[108:111], v[80:83]\n\t"
        "v_mfma_f32_16x16x32_bf16 v[80:83], a[176:179], v[112:115], v[80:83]\n\t"
        "s_waitcnt lgkmcnt(2)\n\t"
        "v_mfma_f32_16x16x32_bf16 v[80:83], a[212:215], v[84:87], v[80:83]\n\t"
        "v_mfma_f32_16x16x32_bf16 v[80:83], a[248:251], v[88:91], v[80:83]\n\t"
        "s_waitcnt lgkmcnt(1)\n\t"
        "v_mfma_f32_16x16x32_bf16 v[80:83], v[216:219], v[92:95], v[80:83]\n\t"
        "s_waitcnt lgkmcnt(0)\n\t"
        "v_mfma_f32_16x16x32_bf16 v[80:83], v[252:255], v[96:99], v[80:83]\n\t"
        "v_mfma_f32_16x16x32_bf16 v[72:75], a[60:63], v[100:103], v[72:75]\n\t"
        "v_mfma_f32_16x16x32_bf16 v[76:79], a[64:67], v[100:103], v[76:79]\n\t"
        "s_nop 5\n\t"
        "v_cvt_pk_bf16_f32 v120, v80, v81\n\t"
        "v_cvt_pk_bf16_f32 v121, v82, v83\n\t"
        "v_mfma_f32_16x16x32_bf16 v[72:75], a[96:99], v[104:107], v[72:75]\n\t"
        "ds_write_b64 v146, v[120:121] offset:10240\n\t"
        "v_mfma_f32_16x16x32_bf16 v[76:79], a[100:103], v[104:107], v[76:79]\n\t"
        "v_mfma_f32_16x16x32_bf16 v[72:75], a[132:135], v[108:111], v[72:75]\n\t"
        "v_mfma_f32_16x16x32_bf16 v[76:79], a[136:139], v[108:111], v[76:79]\n\t"
        "s_waitcnt lgkmcnt(0)\n\t"
        "s_barrier\n\t"
        "v_mfma_f32_16x16x32_bf16 v[72:75], a[168:171], v[112:115], v[72:75]\n\t"
        "ds_read_b128 v[124:127], v147 offset:8192\n\t"
        "v_readlane_b32 s55, v155, 51\n\t"
        "v_mfma_f32_16x16x32_bf16 v[76:79], a[172:175], v[112:115], v[76:79]\n\t"
        "v_mfma_f32_16x16x32_bf16 v[72:75], a[204:207], v[84:87], v[72:75]\n\t"
        "v_mfma_f32_16x16x32_bf16 v[76:79], a[208:211], v[84:87], v[76:79]\n\t"
        "v_mfma_f32_16x16x32_bf16 v[72:75], a[240:243], v[88:91], v[72:75]\n\t"
        "v_mfma_f32_16x16x32_bf16 v[76:79], a[244:247], v[88:91], v[76:79]\n\t"
        "v_mfma_f32_16x16x32_bf16 v[72:75], v[208:211], v[92:95], v[72:75]\n\t"
        "v_mfma_f32_16x16x32_bf16 v[76:79], v[212:215], v[92:95], v[76:79]\n\t"
        "v_mfma_f32_16x16x32_bf16 v[72:75], v[244:247], v[96:99], v[72:75]\n\t"
        "v_mfma_f32_16x16x32_bf16 v[76:79], v[248:251], v[96:99], v[76:79]\n\t"
        "s_nop 6\n\t"
        "v_cvt_pk_bf16_f32 v116, v72, v73\n\t"
        "v_cvt_pk_bf16_f32 v117, v74, v75\n\t"
        "v_cvt_pk_bf16_f32 v118, v76, v77\n\t"
        "v_cvt_pk_bf16_f32 v119, v78, v79\n\t"
        "s_nop 0\n\t"
        "v_permlane16_swap_b32 v116, v118\n\t"
        "v_permlane16_swap_b32 v117, v119\n\t"
        "buffer_store_dwordx4 v[116:119], v148, s[28:31], s55 offen sc1\n\t"
        "s_waitcnt lgkmcnt(0)\n\t"
        "buffer_store_dwordx4 v[124:127], v150, s[28:31], s52 offen sc1\n\t"
        "s_endpgm\n\t"
        "20:\n\t"
        "s_mul_i32 s69, s68, 16384\n\t"
        "buffer_load_dwordx4 v142, s[24:27], s36 offen offset:2048 lds\n\t"
        "s_lshl_b32 s70, s49, 7\n\t"
        "s_lshl_b32 s71, s49, 5\n\t"
        "s_waitcnt vmcnt(18)\n\t"
        "v_add_u32 v128, s69, v128\n\t"
        "v_add_u32 v129, s69, v129\n\t"
        "v_add_u32 v130, s69, v130\n\t"
        "v_xad_u32 v133, v131, s71, 64\n\t"
        "v_add_u32 v131, s70, v131\n\t"
        "v_xor_b32 v132, 32, v131\n\t"
        "s_waitcnt vmcnt(16)\n\t"
        "ds_bpermute_b32 v67, v81, v155 offset:172\n\t"
        "s_add_u32 m0, s35, 4096\n\t"
        "s_add_u32 s43, s34, 43008\n\t"
        "buffer_load_dwordx4 v143, s[16:19], s43 offen  lds\n\t"
        "ds_bpermute_b32 v78, v81, v155 offset:148\n\t"
        "ds_bpermute_b32 v79, v81, v155 offset:160\n\t"
        "v_readlane_b32 s69, v155, 34\n\t"
        "v_readlane_b32 s70, v155, 35\n\t"
        "v_readlane_b32 s46, v155, 43\n\t"
        "v_readlane_b32 s47, v155, 44\n\t"
        "v_readlane_b32 s55, v155, 45\n\t"
        "s_lshl_b32 s71, s49, 3\n\t"
        "v_add_u32 v82, s71, v82\n\t"
        "s_add_u32 m0, s35, 20480\n\t"
        "s_add_u32 s43, s34, 215040\n\t"
        "buffer_load_dwordx4 v143, s[16:19], s43 offen  lds\n\t"
        "v_add_u32 v53, v70, v83\n\t"
        "v_mad_u32_u24 v70, v52, s69, v70\n\t"
        "v_mad_u32_u24 v70, v158, s70, v70\n\t"
        "s_mul_i32 s71, s69, s68\n\t"
        "s_waitcnt lgkmcnt(2)\n\t"
        "v_mad_u32_u24 v67, v82, s70, v67\n\t"
        "v_add_u32 v67, s71, v67\n\t"
        "v_readlane_b32 s11, v155, 30\n\t"
        "s_movk_i32 s10, 223\n\t"
        "v_mov_b32 v75, 0x7ff00000\n\t"
        "v_readlane_b32 s12, v155, 24\n\t"
        "v_readlane_b32 s13, v155, 25\n\t"
        "v_readlane_b32 s70, v155, 27\n\t"
        "s_nop 1\n\t"
        "s_or_b32 s42, s42, s70\n\t"
        "s_bfe_u32 s70, s12, 524288\n\t"
        "s_bfe_u32 s71, s13, 524288\n\t"
        "v_cmp_gt_u32 vcc, s71, v71\n\t"
        "v_add3_u32 v73, v71, s70, -2\n\t"
        "v_med3_i32 v73, v73, 0, s10\n\t"
        "v_cndmask_b32 v74, v75, v72, vcc\n\t"
        "v_mad_u32_u24 v134, v73, s11, v74\n\t"
        "s_bfe_u32 s70, s12, 524296\n\t"
        "s_bfe_u32 s71, s13, 524296\n\t"
        "v_cmp_gt_u32 vcc, s71, v71\n\t"
        "v_add3_u32 v73, v71, s70, -2\n\t"
        "v_med3_i32 v73, v73, 0, s10\n\t"
        "v_cndmask_b32 v74, v75, v72, vcc\n\t"
        "v_mad_u32_u24 v135, v73, s11, v74\n\t"
        "s_bfe_u32 s70, s12, 524304\n\t"
        "s_bfe_u32 s71, s13, 524304\n\t"
        "v_cmp_gt_u32 vcc, s71, v71\n\t"
        "v_add3_u32 v73, v71, s70, -2\n\t"
        "v_med3_i32 v73, v73, 0, s10\n\t"
        "v_cndmask_b32 v74, v75, v72, vcc\n\t"
        "v_mad_u32_u24 v136, v73, s11, v74\n\t"
        "s_bfe_u32 s70, s12, 524312\n\t"
        "s_bfe_u32 s71, s13, 524312\n\t"
        "v_cmp_gt_u32 vcc, s71, v71\n\t"
        "v_add3_u32 v73, v71, s70, -2\n\t"
        "v_med3_i32 v73, v73, 0, s10\n\t"
        "v_cndmask_b32 v74, v75, v72, vcc\n\t"
        "v_mad_u32_u24 v137, v73, s11, v74\n\t"
        "v_readlane_b32 s44, v155, 32\n\t"
        "v_readlane_b32 s45, v155, 33\n\t"
        "v_readlane_b32 s62, v155, 36\n\t"
        "s_mov_b32 exec_lo, 0\n\t"
        "s_mov_b32 exec_hi, 0xfff00000\n\t"
        "s_waitcnt vmcnt(17)\n\t"
        "ds_write_b32 v119, v155\n\t"
        "s_mov_b64 exec, -1\n\t"
        "s_lshl_b32 s70, s62, 5\n\t"
        "s_sub_u32 s71, s70, 16384\n\t"
        "s_mul_i32 s71, s71, s68\n\t"
        "v_add_u32 v172, s71, v128\n\t"
        "v_add_u32 v173, s71, v129\n\t"
        "v_add_u32 v174, s71, v130\n\t"
        "s_add_u32 s71, s71, s70\n\t"
        "v_add_u32 v175, s71, v128\n\t"
        "v_add_u32 v176, s71, v129\n\t"
        "v_add_u32 v177, s71, v130\n\t"
        "s_add_u32 s71, s71, s70\n\t"
        "v_add_u32 v178, s71, v128\n\t"
        "v_add_u32 v179, s71, v129\n\t"
        "v_add_u32 v180, s71, v130\n\t"
        "s_cmp_eq_u32 s69, 36\n\t"
        "s_cselect_b64 exec, -1, 0\n\t"
        "v_swap_b32 v68, v69\n\t"
        "v_swap_b32 v173, v175\n\t"
        "v_swap_b32 v174, v178\n\t"
        "v_swap_b32 v177, v179\n\t"
        "s_mov_b64 exec, -1\n\t"
        "v_add3_u32 v68, v68, v52, -1\n\t"
        "v_add3_u32 v69, v69, v158, -1\n\t"
        "v_cvt_f32_i32 v68, v68\n\t"
        "v_cvt_f32_i32 v69, v69\n\t"
        "s_add_u32 s70, s68, 1\n\t"
        "v_cvt_f32_u32 v73, s70\n\t"
        "s_waitcnt lgkmcnt(1)\n\t"
        "v_add_f32 v78, v78, v73\n\t"
        "v_add_u32 v73, 1, v82\n\t"
        "v_cvt_f32_u32 v73, v73\n\t"
        "v_add_f32 v79, v79, v73\n\t"
        "s_mul_i32 s70, s4, 384\n\t"
        "v_add_u32 v80, s70, v80\n\t"
        "s_waitcnt vmcnt(9) lgkmcnt(0)\n\t"
        "s_barrier\n\t"
        "ds_read_b64_tr_b16 a[0:1], v131 offset:12288\n\t"
        "ds_read_b64_tr_b16 a[2:3], v131 offset:13056\n\t"
        "ds_read_b64_tr_b16 a[4:5], v132 offset:12288\n\t"
        "ds_read_b64_tr_b16 a[6:7], v132 offset:13056\n\t"
        "ds_read_b64_tr_b16 a[8:9], v133 offset:12288\n\t"
        "ds_read_b64_tr_b16 a[10:11], v133 offset:13056\n\t"
        "ds_read_b128 v[84:87], v128 offset:8192\n\t"
        "ds_read_b64_tr_b16 a[12:13], v131 offset:18432\n\t"
        "ds_read_b64_tr_b16 a[14:15], v131 offset:19200\n\t"
        "ds_read_b64_tr_b16 a[16:17], v132 offset:18432\n\t"
        "ds_read_b64_tr_b16 a[18:19], v132 offset:19200\n\t"
        "ds_read_b64_tr_b16 a[20:21], v133 offset:18432\n\t"
        "ds_read_b64_tr_b16 a[22:23], v133 offset:19200\n\t"
        "ds_read_b128 v[88:91], v128 offset:8192\n\t"
        "s_add_u32 m0, s37, 49152\n\t"
        "s_add_u32 s36, s36, s39\n\t"
        "s_waitcnt lgkmcnt(7)\n\t"
        "v_mfma_f32_16x16x32_bf16 v[12:15], a[0:3], v[84:87], 0\n\t"
        "buffer_load_dwordx4 v140, s[24:27], s36 offen offset:0 lds\n\t"
        "buffer_load_dwordx4 v141, s[24:27], s36 offen offset:1024 lds\n\t"
        "buffer_load_dwordx4 v142, s[24:27], s36 offen offset:2048 lds\n\t"
        "s_add_u32 m0, s35, 16384\n\t"
        "s_add_u32 s43, s34, 172032\n\t"
        "buffer_load_dwordx4 v143, s[16:19], s43 offen  lds\n\t"
        "s_add_u32 m0, s35, 32768\n\t"
        "s_add_u32 s43, s34, 344064\n\t"
        "s_min_u32 s43, s43, 9590784\n\t"
        "buffer_load_dwordx4 v143, s[16:19], s43 offen  lds\n\t"
        "s_waitcnt vmcnt(10)\n\t"
        "s_barrier\n\t"
        "ds_read_b128 v[92:95], v128 offset:12288\n\t"
        "s_add_u32 m0, s37, 0\n\t"
        "ds_read_b64_tr_b16 a[24:25], v131 offset:24576\n\t"
        "s_add_u32 s36, s36, s38\n\t"
        "v_mfma_f32_16x16x32_bf16 v[16:19], a[4:7], v[84:87], 0\n\t"
        "buffer_load_dwordx4 v140, s[24:27], s36 offen offset:0 lds\n\t"
        "ds_read_b64_tr_b16 a[26:27], v131 offset:25344\n\t"
        "v_lshrrev_b32 v73, 5, v156\n\t"
        "v_mfma_f32_16x16x32_bf16 v[20:23], a[8:11], v[84:87], 0\n\t"
        "buffer_load_dwordx4 v141, s[24:27], s36 offen offset:1024 lds\n\t"
        "ds_read_b64_tr_b16 a[28:29], v132 offset:24576\n\t"
        "v_mul_u32_u24 v73, 8064, v73\n\t"
        "s_waitcnt lgkmcnt(4)\n\t"
        "v_mfma_f32_16x16x32_bf16 v[24:27], a[12:15], v[88:91], 0\n\t"
        "ds_read_b64_tr_b16 a[30:31], v132 offset:25344\n\t"
        "ds_read_b64_tr_b16 a[32:33], v133 offset:24576\n\t"
        "ds_read_b64_tr_b16 a[34:35], v133 offset:25344\n\t"
        "v_and_b32 v55, 496, v140\n\t"
        "v_mfma_f32_16x16x32_bf16 v[28:31], a[16:19], v[88:91], 0\n\t"
        "buffer_load_dwordx4 v142, s[24:27], s36 offen offset:2048 lds\n\t"
        "ds_read_b128 v[96:99], v128 offset:8192\n\t"
        "v_add_u32 v55, v55, v73\n\t"
        "v_mfma_f32_16x16x32_bf16 v[32:35], a[20:23], v[88:91], 0\n\t"
        "ds_read_b128 v[100:103], v128 offset:12288\n\t"
        "v_lshl_add_u32 v61, v158, 2, v73\n\t"
        "s_waitcnt lgkmcnt(8)\n\t"
        "v_mfma_f32_16x16x32_bf16 v[24:27], a[0:3], v[92:95], v[24:27]\n\t"
        "s_mul_i32 s48, s41, 36\n\t"
        "v_readlane_b32 s69, v155, 31\n\t"
        "v_mfma_f32_16x16x32_bf16 v[28:31], a[4:7], v[92:95], v[28:31]\n\t"
        "buffer_load_dword v67, v67, s[20:23], 0 offen offset:32\n\t"
        "v_mov_b32 v154, s69\n\t"
        "v_mfma_f32_16x16x32_bf16 v[32:35], a[8:11], v[92:95], v[32:35]\n\t"
        "v_mov_b32 v119, s62\n\t"
        "v_add_u32 v117, s37, v117\n\t"
        "v_mfma_f32_16x16x32_bf16 v[36:39], a[12:15], v[92:95], 0\n\t"
        "v_add_u32 v118, s35, v118\n\t"
        "v_add_u32 v185, s35, v185\n\t"
        "v_mfma_f32_16x16x32_bf16 v[40:43], a[16:19], v[92:95], 0\n\t"
        "v_add_u32 v186, s37, v186\n\t"
        "v_readlane_b32 s53, v155, 28\n\t"
        "v_mfma_f32_16x16x32_bf16 v[44:47], a[20:23], v[92:95], 0\n\t"
        "s_waitcnt vmcnt(9) lgkmcnt(2)\n\t"
        "s_barrier\n\t"
        "buffer_load_dword v64, v70, s[20:23], s46 offen\n\t"
        "ds_read_b128 v[104:107], v128 offset:4096\n\t"
        "s_waitcnt lgkmcnt(2)\n\t"
        "v_mfma_f32_16x16x32_bf16 v[0:3], a[24:27], v[96:99], 0\n\t"
        "ds_read_b64_tr_b16 a[36:37], v131 offset:36864\n\t"
        "ds_read_b64_tr_b16 a[38:39], v131 offset:37632\n\t"
        "s_lshl_b32 s69, s68, 10\n\t"
        "v_mfma_f32_16x16x32_bf16 v[4:7], a[28:31], v[96:99], 0\n\t"
        "buffer_load_dword v65, v70, s[20:23], s47 offen\n\t"
        "ds_read_b64_tr_b16 a[40:41], v132 offset:36864\n\t"
        "s_add_u32 s69, s69, 135168\n\t"
        "v_mfma_f32_16x16x32_bf16 v[8:11], a[32:35], v[96:99], 0\n\t"
        "ds_read_b64_tr_b16 a[42:43], v132 offset:37632\n\t"
        "ds_read_b64_tr_b16 a[44:45], v133 offset:36864\n\t"
        "s_lshl_b32 s70, s49, 5\n\t"
        "s_waitcnt lgkmcnt(6)\n\t"
        "v_mfma_f32_16x16x32_bf16 v[12:15], a[24:27], v[100:103], v[12:15]\n\t"
        "ds_read_b64_tr_b16 a[46:47], v133 offset:37632\n\t"
        "ds_read_b64_tr_b16 a[48:49], v131 offset:43008\n\t"
        "ds_read_b64_tr_b16 a[50:51], v131 offset:43776\n\t"
        "v_xor_b32 v146, s70, v146\n\t"
        "v_mfma_f32_16x16x32_bf16 v[16:19], a[28:31], v[100:103], v[16:19]\n\t"
        "buffer_load_dword v66, v70, s[20:23], s55 offen\n\t"
        "s_add_u32 m0, s37, 12288\n\t"
        "ds_read_b64_tr_b16 a[52:53], v132 offset:43008\n\t"
        "s_add_u32 s36, s36, s5\n\t"
        "ds_read_b64_tr_b16 a[54:55], v132 offset:43776\n\t"
        "v_mfma_f32_16x16x32_bf16 v[20:23], a[32:35], v[100:103], v[20:23]\n\t"
        "ds_read_b64_tr_b16 a[56:57], v133 offset:43008\n\t"
        "ds_read_b64_tr_b16 a[58:59], v133 offset:43776\n\t"
        "ds_read_b128 v[108:111], v128 offset:4160\n\t"
        "v_add_u32 v146, s69, v146\n\t"
        "s_waitcnt lgkmcnt(12)\n\t"
        "v_mfma_f32_16x16x32_bf16 v[0:3], a[0:3], v[104:107], v[0:3]\n\t"
        "s_lshl_b32 s70, s49, 11\n\t"
        "s_add_u32 s70, s70, s69\n\t"
        "v_mfma_f32_16x16x32_bf16 v[4:7], a[4:7], v[104:107], v[4:7]\n\t"
        "buffer_load_dwordx4 v140, s[24:27], s36 offen offset:0 lds\n\t"
        "v_add_u32 v147, s70, v147\n\t"
        "v_mfma_f32_16x16x32_bf16 v[8:11], a[8:11], v[104:107], v[8:11]\n\t"
        "s_mul_i32 s41, s41, 192\n\t"
        "s_mul_i32 s69, s68, 172032\n\t"
        "v_mfma_f32_16x16x32_bf16 v[12:15], a[12:15], v[104:107], v[12:15]\n\t"
        "s_add_u32 s41, s41, s69\n\t"
        "s_mul_i32 s69, s49, 43008\n\t"
        "v_mfma_f32_16x16x32_bf16 v[16:19], a[16:19], v[104:107], v[16:19]\n\t"
        "s_add_u32 s50, s41, s69\n\t"
        "s_lshl_b32 s70, s49, 7\n\t"
        "v_mfma_f32_16x16x32_bf16 v[20:23], a[20:23], v[104:107], v[20:23]\n\t"
        "v_add_u32 v144, s70, v144\n\t"
        "s_mul_i32 s69, s68, 768\n\t"
        "s_waitcnt lgkmcnt(0)\n\t"
        "v_mfma_f32_16x16x32_bf16 v[0:3], a[36:39], v[108:111], v[0:3]\n\t"
        "buffer_load_dwordx4 v141, s[24:27], s36 offen offset:1024 lds\n\t"
        "ds_read_b128 v[112:115], v128 offset:8256\n\t"
        "v_add_u32 v187, s69, v187\n\t"
        "v_mfma_f32_16x16x32_bf16 v[4:7], a[40:43], v[108:111], v[4:7]\n\t"
        "v_readlane_b32 s9, v155, 46\n\t"
        "s_sub_u32 s10, 43200, s11\n\t"
        "v_mfma_f32_16x16x32_bf16 v[8:11], a[44:47], v[108:111], v[8:11]\n\t"
        "s_mul_i32 s10, s10, s68\n\t"
        "s_sub_u32 s11, s11, 192\n\t"
        "v_mfma_f32_16x16x32_bf16 v[12:15], a[48:51], v[108:111], v[12:15]\n\t"
        "v_mad_u32_u24 v148, v158, s11, v144\n\t"
        "v_add_u32 v148, s10, v148\n\t"
        "v_mfma_f32_16x16x32_bf16 v[16:19], a[52:55], v[108:111], v[16:19]\n\t"
        "v_lshrrev_b32 v73, 2, v157\n\t"
        "v_mad_u32_u24 v150, v73, s11, v145\n\t"
        "v_mfma_f32_16x16x32_bf16 v[20:23], a[56:59], v[108:111], v[20:23]\n\t"
        "buffer_load_dwordx4 v142, s[24:27], s36 offen offset:2048 lds\n\t"
        "s_add_u32 m0, s35, 0\n\t"
        "s_add_u32 s43, s34, 0\n\t"
        "s_waitcnt lgkmcnt(0)\n\t"
        "v_mfma_f32_16x16x32_bf16 v[12:15], a[36:39], v[112:115], v[12:15]\n\t"
        "ds_read_b128 v[84:87], v128 offset:12352\n\t"
        "s_lshl_b32 s70, s49, 0\n\t"
        "v_mfma_f32_16x16x32_bf16 v[16:19], a[40:43], v[112:115], v[16:19]\n\t"
        "s_add_u32 s70, s70, 50\n\t"
        "v_readlane_b32 s52, v155, s70\n\t"
        "v_mfma_f32_16x16x32_bf16 v[20:23], a[44:47], v[112:115], v[20:23]\n\t"
        "s_nop 0\n\t"
        "s_add_u32 s52, s52, s10\n\t"
        "v_readlane_b32 s51, v155, 49\n\t"
        "v_mfma_f32_16x16x32_bf16 v[24:27], a[48:51], v[112:115], v[24:27]\n\t"
        "buffer_load_dwordx4 v143, s[16:19], s43 offen  lds\n\t"
        "s_add_u32 m0, s35, 36864\n\t"
        "s_add_u32 s43, s34, 387072\n\t"
        "s_min_u32 s43, s43, 9590784\n\t"
        "v_mfma_f32_16x16x32_bf16 v[28:31], a[52:55], v[112:115], v[28:31]\n\t"
        "s_lshl_b32 s70, s49, 30\n\t"
        "s_add_u32 s70, s70, s10\n\t"
        "v_mfma_f32_16x16x32_bf16 v[32:35], a[56:59], v[112:115], v[32:35]\n\t"
        "buffer_load_dwordx4 v143, s[16:19], s43 offen  lds\n\t"
        "s_waitcnt vmcnt(12)\n\t"
        "s_barrier\n\t"
        "ds_read_b128 v[88:91], v128 offset:16384\n\t"
        "s_add_u32 m0, s37, 24576\n\t"
        "ds_read_b64_tr_b16 a[60:61], v131 offset:49152\n\t"
        "s_add_u32 s36, s36, s39\n\t"
        "s_waitcnt lgkmcnt(2)\n\t"
        "v_mfma_f32_16x16x32_bf16 v[24:27], a[36:39], v[84:87], v[24:27]\n\t"
        "buffer_load_dwordx4 v140, s[24:27], s36 offen offset:0 lds\n\t"
        "ds_read_b64_tr_b16 a[62:63], v131 offset:49920\n\t"
        "s_add_u32 s51, s51, s70\n\t"
        "v_mfma_f32_16x16x32_bf16 v[28:31], a[40:43], v[84:87], v[28:31]\n\t"
        "ds_read_b64_tr_b16 a[64:65], v132 offset:49152\n\t"
        "ds_read_b64_tr_b16 a[66:67], v132 offset:49920\n\t"
        "v_mfma_f32_16x16x32_bf16 v[32:35], a[44:47], v[84:87], v[32:35]\n\t"
        "buffer_load_dwordx4 v141, s[24:27], s36 offen offset:1024 lds\n\t"
        "ds_read_b64_tr_b16 a[68:69], v133 offset:49152\n\t"
        "v_mfma_f32_16x16x32_bf16 v[36:39], a[48:51], v[84:87], v[36:39]\n\t"
        "ds_read_b64_tr_b16 a[70:71], v133 offset:49920\n\t"
        "ds_read_b128 v[92:95], v128 offset:8256\n\t"
        "v_mfma_f32_16x16x32_bf16 v[40:43], a[52:55], v[84:87], v[40:43]\n\t"
        "ds_read_b128 v[96:99], v128 offset:12352\n\t"
        "ds_read_b128 v[100:103], v128 offset:16448\n\t"
        "v_mfma_f32_16x16x32_bf16 v[44:47], a[56:59], v[84:87], v[44:47]\n\t"
        "ds_read_b64_tr_b16 a[72:73], v131 offset:55296\n\t"
        "ds_read_b64_tr_b16 a[74:75], v131 offset:56064\n\t"
        "s_waitcnt lgkmcnt(11)\n\t"
        "v_mfma_f32_16x16x32_bf16 v[24:27], a[24:27], v[88:91], v[24:27]\n\t"
        "buffer_load_dwordx4 v142, s[24:27], s36 offen offset:2048 lds\n\t"
        "ds_read_b64_tr_b16 a[76:77], v132 offset:55296\n\t"
        "v_mfma_f32_16x16x32_bf16 v[28:31], a[28:31], v[88:91], v[28:31]\n\t"
        "ds_read_b64_tr_b16 a[78:79], v132 offset:56064\n\t"
        "ds_read_b64_tr_b16 a[80:81], v133 offset:55296\n\t"
        "v_mfma_f32_16x16x32_bf16 v[32:35], a[32:35], v[88:91], v[32:35]\n\t"
        "ds_read_b64_tr_b16 a[82:83], v133 offset:56064\n\t"
        "v_mfma_f32_16x16x32_bf16 v[36:39], a[0:3], v[88:91], v[36:39]\n\t"
        "v_mfma_f32_16x16x32_bf16 v[40:43], a[4:7], v[88:91], v[40:43]\n\t"
        "buffer_load_dwordx4 v[48:51], v55, s[20:23], s48 offen\n\t"
        "v_mfma_f32_16x16x32_bf16 v[44:47], a[8:11], v[88:91], v[44:47]\n\t"
        "s_waitcnt lgkmcnt(8)\n\t"
        "v_mfma_f32_16x16x32_bf16 v[0:3], a[60:63], v[92:95], v[0:3]\n\t"
        "v_mfma_f32_16x16x32_bf16 v[4:7], a[64:67], v[92:95], v[4:7]\n\t"
        "v_mfma_f32_16x16x32_bf16 v[8:11], a[68:71], v[92:95], v[8:11]\n\t"
        "s_waitcnt lgkmcnt(5)\n\t"
        "v_mfma_f32_16x16x32_bf16 v[12:15], a[60:63], v[96:99], v[12:15]\n\t"
        "v_mfma_f32_16x16x32_bf16 v[16:19], a[64:67], v[96:99], v[16:19]\n\t"
        "v_mfma_f32_16x16x32_bf16 v[20:23], a[68:71], v[96:99], v[20:23]\n\t"
        "v_mfma_f32_16x16x32_bf16 v[24:27], a[60:63], v[100:103], v[24:27]\n\t"
        "ds_read_b128 v[104:107], v128 offset:4224\n\t"
        "v_mfma_f32_16x16x32_bf16 v[28:31], a[64:67], v[100:103], v[28:31]\n\t"
        "buffer_load_dword v61, v61, s[20:23], s48 offen offset:512\n\t"
        "s_add_u32 m0, s37, 36864\n\t"
        "v_mfma_f32_16x16x32_bf16 v[32:35], a[68:71], v[100:103], v[32:35]\n\t"
        "s_add_u32 s36, s36, s38\n\t"
        "v_mfma_f32_16x16x32_bf16 v[36:39], a[36:39], v[100:103], v[36:39]\n\t"
        "ds_read_b128 v[108:111], v128 offset:8320\n\t"
        "v_mfma_f32_16x16x32_bf16 v[40:43], a[40:43], v[100:103], v[40:43]\n\t"
        "v_mfma_f32_16x16x32_bf16 v[44:47], a[44:47], v[100:103], v[44:47]\n\t"
        "s_waitcnt lgkmcnt(1)\n\t"
        "v_mfma_f32_16x16x32_bf16 v[0:3], a[72:75], v[104:107], v[0:3]\n\t"
        "ds_read_b128 v[112:115], v128 offset:12416\n\t"
        "v_mfma_f32_16x16x32_bf16 v[4:7], a[76:79], v[104:107], v[4:7]\n\t"
        "v_mfma_f32_16x16x32_bf16 v[8:11], a[80:83], v[104:107], v[8:11]\n\t"
        "s_waitcnt lgkmcnt(1)\n\t"
        "v_mfma_f32_16x16x32_bf16 v[12:15], a[72:75], v[108:111], v[12:15]\n\t"
        "ds_read_b128 v[84:87], v128 offset:16512\n\t"
        "v_mfma_f32_16x16x32_bf16 v[16:19], a[76:79], v[108:111], v[16:19]\n\t"
        "v_mfma_f32_16x16x32_bf16 v[20:23], a[80:83], v[108:111], v[20:23]\n\t"
        "s_waitcnt vmcnt(14)\n\t"
        "s_barrier\n\t"
        "buffer_load_dwordx4 v140, s[24:27], s36 offen offset:0 lds\n\t"
        "ds_read_b64_tr_b16 a[84:85], v131 offset:0\n\t"
        "ds_read_b64_tr_b16 a[86:87], v131 offset:768\n\t"
        "ds_read_b64_tr_b16 a[88:89], v132 offset:0\n\t"
        "ds_read_b64_tr_b16 a[90:91], v132 offset:768\n\t"
        "ds_read_b64_tr_b16 a[92:93], v133 offset:0\n\t"
        "ds_read_b64_tr_b16 a[94:95], v133 offset:768\n\t"
        "ds_read_b128 v[88:91], v128 offset:4224\n\t"
        "s_waitcnt lgkmcnt(8)\n\t"
        "v_mfma_f32_16x16x32_bf16 v[24:27], a[72:75], v[112:115], v[24:27]\n\t"
        "ds_read_b64_tr_b16 a[96:97], v131 offset:6144\n\t"
        "ds_read_b64_tr_b16 a[98:99], v131 offset:6912\n\t"
        "ds_read_b64_tr_b16 a[100:101], v132 offset:6144\n\t"
        "v_mfma_f32_16x16x32_bf16 v[28:31], a[76:79], v[112:115], v[28:31]\n\t"
        "ds_read_b64_tr_b16 a[102:103], v132 offset:6912\n\t"
        "ds_read_b64_tr_b16 a[104:105], v133 offset:6144\n\t"
        "ds_read_b64_tr_b16 a[106:107], v133 offset:6912\n\t"
        "v_mfma_f32_16x16x32_bf16 v[32:35], a[80:83], v[112:115], v[32:35]\n\t"
        "ds_read_b128 v[92:95], v128 offset:8320\n\t"
        "s_waitcnt lgkmcnt(14)\n\t"
        "v_mfma_f32_16x16x32_bf16 v[36:39], a[72:75], v[84:87], v[36:39]\n\t"
        "buffer_load_dwordx4 v141, s[24:27], s36 offen offset:1024 lds\n\t"
        "v_mfma_f32_16x16x32_bf16 v[40:43], a[76:79], v[84:87], v[40:43]\n\t"
        "ds_read_b128 v[96:99], v128 offset:12416\n\t"
        "v_mfma_f32_16x16x32_bf16 v[44:47], a[80:83], v[84:87], v[44:47]\n\t"
        "ds_read_b128 v[100:103], v128 offset:16512\n\t"
        "s_waitcnt lgkmcnt(9)\n\t"
        "v_mfma_f32_16x16x32_bf16 v[12:15], a[84:87], v[88:91], v[12:15]\n\t"
        "v_mfma_f32_16x16x32_bf16 v[16:19], a[88:91], v[88:91], v[16:19]\n\t"
        "buffer_load_dwordx4 v142, s[24:27], s36 offen offset:2048 lds\n\t"
        "v_readlane_b32 s40, v155, 12\n\t"
        "v_readlane_b32 s43, v155, 0\n\t"
        "v_mfma_f32_16x16x32_bf16 v[20:23], a[92:95], v[88:91], v[20:23]\n\t"
        "s_add_u32 m0, s40, s35\n\t"
        "s_waitcnt lgkmcnt(2)\n\t"
        "v_mfma_f32_16x16x32_bf16 v[0:3], a[96:99], v[92:95], v[0:3]\n\t"
        "v_mfma_f32_16x16x32_bf16 v[4:7], a[100:103], v[92:95], v[4:7]\n\t"
        "v_readlane_b32 s46, v155, 37\n\t"
        "v_readlane_b32 s47, v155, 40\n\t"
        "s_waitcnt vmcnt(15)\n\t"
        "v_perm_b32 v56, v64, v64, s44\n\t"
        "v_mfma_f32_16x16x32_bf16 v[8:11], a[104:107], v[92:95], v[8:11]\n\t"
        "buffer_load_dwordx4 v134, s[16:19], s43 offen  lds\n\t"
        "v_readlane_b32 s40, v155, 13\n\t"
        "v_mfma_f32_16x16x32_bf16 v[24:27], a[84:87], v[92:95], v[24:27]\n\t"
        "v_readlane_b32 s43, v155, 1\n\t"
        "s_add_u32 m0, s40, s35\n\t"
        "v_mfma_f32_16x16x32_bf16 v[28:31], a[88:91], v[92:95], v[28:31]\n\t"
        "v_perm_b32 v57, v64, v64, s45\n\t"
        "v_add_f32 v58, s46, v68\n\t"
        "v_mfma_f32_16x16x32_bf16 v[32:35], a[92:95], v[92:95], v[32:35]\n\t"
        "v_add_f32 v59, s47, v69\n\t"
        "v_add_f32 v56, v56, v58\n\t"
        "s_waitcnt lgkmcnt(0)\n\t"
        "v_mfma_f32_16x16x32_bf16 v[12:15], a[96:99], v[96:99], v[12:15]\n\t"
        "v_add_f32 v57, v57, v59\n\t"
        "v_cvt_pk_bf16_f32 v52, v56, v57\n\t"
        "v_mfma_f32_16x16x32_bf16 v[16:19], a[100:103], v[96:99], v[16:19]\n\t"
        "buffer_load_dwordx4 v135, s[16:19], s43 offen  lds\n\t"
        "s_add_u32 m0, s37, 49152\n\t"
        "v_mfma_f32_16x16x32_bf16 v[20:23], a[104:107], v[96:99], v[20:23]\n\t"
        "s_waitcnt vmcnt(10)\n\t"
        "s_barrier\n\t"
        "ds_read_b128 v[104:107], v128 offset:0\n\t"
        "s_add_u32 s36, s36, s5\n\t"
        "ds_read_b128 v[108:111], v128 offset:64\n\t"
        "v_mfma_f32_16x16x32_bf16 v[36:39], a[84:87], v[96:99], v[36:39]\n\t"
        "ds_read_b128 v[112:115], v128 offset:128\n\t"
        "ds_read_b128 v[84:87], v128 offset:20480\n\t"
        "v_cvt_pk_bf16_f32 v58, v58, v59\n\t"
        "v_mfma_f32_16x16x32_bf16 v[40:43], a[88:91], v[96:99], v[40:43]\n\t"
        "ds_read_b128 v[88:91], v128 offset:20544\n\t"
        "ds_read_b128 v[92:95], v128 offset:20608\n\t"
        "v_cmp_ne_u32 s[64:65], v52, v58\n\t"
        "v_mfma_f32_16x16x32_bf16 v[44:47], a[92:95], v[96:99], v[44:47]\n\t"
        "buffer_load_dwordx4 v140, s[24:27], s36 offen offset:0 lds\n\t"
        "ds_read_b64_tr_b16 a[108:109], v131 offset:12288\n\t"
        "v_mbcnt_lo_u32_b32 v56, s64, 1\n\t"
        "v_mfma_f32_16x16x32_bf16 v[24:27], a[96:99], v[100:103], v[24:27]\n\t"
        "ds_read_b64_tr_b16 a[110:111], v131 offset:13056\n\t"
        "ds_read_b64_tr_b16 a[112:113], v132 offset:12288\n\t"
        "v_mbcnt_hi_u32_b32 v56, s65, v56\n\t"
        "v_mfma_f32_16x16x32_bf16 v[28:31], a[100:103], v[100:103], v[28:31]\n\t"
        "ds_read_b64_tr_b16 a[114:115], v132 offset:13056\n\t"
        "ds_read_b64_tr_b16 a[116:117], v133 offset:12288\n\t"
        "s_bcnt1_i32_b64 s61, s[64:65]\n\t"
        "v_mfma_f32_16x16x32_bf16 v[32:35], a[104:107], v[100:103], v[32:35]\n\t"
        "ds_read_b64_tr_b16 a[118:119], v133 offset:13056\n\t"
        "ds_read_b64_tr_b16 a[120:121], v131 offset:18432\n\t"
        "v_add_u32 v54, s61, 1\n\t"
        "s_waitcnt lgkmcnt(12)\n\t"
        "v_mfma_f32_16x16x32_bf16 v[0:3], a[12:15], v[104:107], v[0:3]\n\t"
        "buffer_load_dwordx4 v141, s[24:27], s36 offen offset:1024 lds\n\t"
        "ds_read_b64_tr_b16 a[122:123], v131 offset:19200\n\t"
        "v_lshl_add_u32 v56, v56, 3, s60\n\t"
        "v_mfma_f32_16x16x32_bf16 v[4:7], a[16:19], v[104:107], v[4:7]\n\t"
        "s_mov_b64 exec, s[64:65]\n\t"
        "ds_write_b64 v56, v[52:53]\n\t"
        "s_mov_b64 exec, -1\n\t"
        "ds_read_b64_tr_b16 a[124:125], v132 offset:18432\n\t"
        "v_mfma_f32_16x16x32_bf16 v[8:11], a[20:23], v[104:107], v[8:11]\n\t"
        "ds_read_b64_tr_b16 a[126:127], v132 offset:19200\n\t"
        "ds_read_b64_tr_b16 a[128:129], v133 offset:18432\n\t"
        "v_swap_b32 v53, v80\n\t"
        "v_perm_b32 v56, v67, v67, s44\n\t"
        "s_waitcnt lgkmcnt(11)\n\t"
        "v_mfma_f32_16x16x32_bf16 v[0:3], a[48:51], v[108:111], v[0:3]\n\t"
        "ds_read_b64_tr_b16 a[130:131], v133 offset:19200\n\t"
        "v_perm_b32 v57, v67, v67, s45\n\t"
        "v_mfma_f32_16x16x32_bf16 v[4:7], a[52:55], v[108:111], v[4:7]\n\t"
        "buffer_load_dwordx4 v142, s[24:27], s36 offen offset:2048 lds\n\t"
        "v_readlane_b32 s40, v155, 14\n\t"
        "v_mfma_f32_16x16x32_bf16 v[8:11], a[56:59], v[108:111], v[8:11]\n\t"
        "ds_read_b128 v[96:99], v129 offset:0\n\t"
        "v_readlane_b32 s43, v155, 2\n\t"
        "v_mfma_f32_16x16x32_bf16 v[0:3], a[84:87], v[112:115], v[0:3]\n\t"
        "ds_read_b128 v[100:103], v129 offset:4096\n\t"
        "s_add_u32 m0, s40, s35\n\t"
        "v_mfma_f32_16x16x32_bf16 v[4:7], a[88:91], v[112:115], v[4:7]\n\t"
        "v_add_f32 v56, v56, v78\n\t"
        "v_add_f32 v57, v57, v79\n\t"
        "v_mfma_f32_16x16x32_bf16 v[8:11], a[92:95], v[112:115], v[8:11]\n\t"
        "buffer_load_dwordx4 v134, s[16:19], s43 offen  lds\n\t"
        "v_readlane_b32 s40, v155, 15\n\t"
        "v_mfma_f32_16x16x32_bf16 v[36:39], a[24:27], v[84:87], v[36:39]\n\t"
        "v_readlane_b32 s43, v155, 3\n\t"
        "s_add_u32 m0, s40, s35\n\t"
        "v_mfma_f32_16x16x32_bf16 v[40:43], a[28:31], v[84:87], v[40:43]\n\t"
        "v_cvt_pk_bf16_f32 v52, v56, v57\n\t"
        "v_cvt_pk_bf16_f32 v58, v78, v79\n\t"
        "v_mfma_f32_16x16x32_bf16 v[44:47], a[32:35], v[84:87], v[44:47]\n\t"
        "v_cmp_ne_u32 s[64:65], v52, v58\n\t"
        "v_readlane_b32 s47, v155, 41\n\t"
        "v_mfma_f32_16x16x32_bf16 v[36:39], a[60:63], v[88:91], v[36:39]\n\t"
        "s_and_b64 s[64:65], s[64:65], 0xffffff\n\t"
        "v_mbcnt_lo_u32_b32 v56, s64, v54\n\t"
        "v_mfma_f32_16x16x32_bf16 v[40:43], a[64:67], v[88:91], v[40:43]\n\t"
        "s_bcnt1_i32_b64 s46, s[64:65]\n\t"
        "s_add_u32 s61, s61, s46\n\t"
        "v_mfma_f32_16x16x32_bf16 v[44:47], a[68:71], v[88:91], v[44:47]\n\t"
        "v_add_u32 v54, s46, v54\n\t"
        "v_lshl_add_u32 v56, v56, 3, s60\n\t"
        "v_mfma_f32_16x16x32_bf16 v[36:39], a[96:99], v[92:95], v[36:39]\n\t"
        "s_mov_b64 exec, s[64:65]\n\t"
        "ds_write_b64 v56, v[52:53]\n\t"
        "s_mov_b64 exec, -1\n\t"
        "v_mfma_f32_16x16x32_bf16 v[40:43], a[100:103], v[92:95], v[40:43]\n\t"
        "buffer_load_dwordx4 v135, s[16:19], s43 offen  lds\n\t"
        "s_add_u32 m0, s37, 0\n\t"
        "v_mfma_f32_16x16x32_bf16 v[44:47], a[104:107], v[92:95], v[44:47]\n\t"
        "s_add_u32 s36, s36, s39\n\t"
        "v_add_u32 v53, 2048, v80\n\t"
        "s_waitcnt lgkmcnt(1)\n\t"
        "v_mfma_f32_16x16x32_bf16 v[0:3], a[120:123], v[96:99], v[0:3]\n\t"
        "v_readlane_b32 s46, v155, 38\n\t"
        "v_perm_b32 v56, v65, v65, s44\n\t"
        "v_mfma_f32_16x16x32_bf16 v[4:7], a[124:127], v[96:99], v[4:7]\n\t"
        "v_perm_b32 v57, v65, v65, s45\n\t"
        "v_add_f32 v58, s46, v68\n\t"
        "v_mfma_f32_16x16x32_bf16 v[8:11], a[128:131], v[96:99], v[8:11]\n\t"
        "v_add_f32 v59, s47, v69\n\t"
        "v_add_f32 v56, v56, v58\n\t"
        "v_mfma_f32_16x16x32_bf16 v[0:3], a[108:111], v[100:103], v[0:3]\n\t"
        "s_waitcnt vmcnt(12)\n\t"
        "s_barrier\n\t"
        "buffer_load_dwordx4 v140, s[24:27], s36 offen offset:0 lds\n\t"
        "ds_read_b128 v[104:107], v129 offset:8192\n\t"
        "v_mfma_f32_16x16x32_bf16 v[4:7], a[112:115], v[100:103], v[4:7]\n\t"
        "ds_read_b64_tr_b16 a[132:133], v131 offset:24576\n\t"
        "ds_read_b64_tr_b16 a[134:135], v131 offset:25344\n\t"
        "v_add_f32 v57, v57, v59\n\t"
        "v_mfma_f32_16x16x32_bf16 v[8:11], a[116:119], v[100:103], v[8:11]\n\t"
        "ds_read_b64_tr_b16 a[136:137], v132 offset:24576\n\t"
        "ds_read_b64_tr_b16 a[138:139], v132 offset:25344\n\t"
        "v_cvt_pk_bf16_f32 v52, v56, v57\n\t"
        "v_mfma_f32_16x16x32_bf16 v[12:15], a[120:123], v[100:103], v[12:15]\n\t"
        "ds_read_b64_tr_b16 a[140:141], v133 offset:24576\n\t"
        "ds_read_b64_tr_b16 a[142:143], v133 offset:25344\n\t"
        "ds_read_b128 v[108:111], v129 offset:8192\n\t"
        "v_cvt_pk_bf16_f32 v58, v58, v59\n\t"
        "v_mfma_f32_16x16x32_bf16 v[16:19], a[124:127], v[100:103], v[16:19]\n\t"
        "buffer_load_dwordx4 v141, s[24:27], s36 offen offset:1024 lds\n\t"
        "ds_read_b128 v[112:115], v129 offset:12288\n\t"
        "v_cmp_ne_u32 s[64:65], v52, v58\n\t"
        "v_mfma_f32_16x16x32_bf16 v[20:23], a[128:131], v[100:103], v[20:23]\n\t"
        "ds_read_b128 v[84:87], v129 offset:16384\n\t"
        "ds_read_b64_tr_b16 a[144:145], v131 offset:30720\n\t"
        "v_mbcnt_lo_u32_b32 v56, s64, v54\n\t"
        "s_waitcnt lgkmcnt(10)\n\t"
        "v_mfma_f32_16x16x32_bf16 v[12:15], a[108:111], v[104:107], v[12:15]\n\t"
        "ds_read_b64_tr_b16 a[146:147], v131 offset:31488\n\t"
        "ds_read_b64_tr_b16 a[148:149], v132 offset:30720\n\t"
        "v_mbcnt_hi_u32_b32 v56, s65, v56\n\t"
        "v_mfma_f32_16x16x32_bf16 v[16:19], a[112:115], v[104:107], v[16:19]\n\t"
        "ds_read_b64_tr_b16 a[150:151], v132 offset:31488\n\t"
        "ds_read_b64_tr_b16 a[152:153], v133 offset:30720\n\t"
        "s_bcnt1_i32_b64 s46, s[64:65]\n\t"
        "v_mfma_f32_16x16x32_bf16 v[20:23], a[116:119], v[104:107], v[20:23]\n\t"
        "buffer_load_dwordx4 v142, s[24:27], s36 offen offset:2048 lds\n\t"
        "ds_read_b64_tr_b16 a[154:155], v133 offset:31488\n\t"
        "v_readlane_b32 s40, v155, 16\n\t"
        "v_mfma_f32_16x16x32_bf16 v[24:27], a[120:123], v[104:107], v[24:27]\n\t"
        "ds_read_b128 v[88:91], v129 offset:20480\n\t"
        "v_readlane_b32 s43, v155, 4\n\t"
        "v_mfma_f32_16x16x32_bf16 v[28:31], a[124:127], v[104:107], v[28:31]\n\t"
        "s_add_u32 m0, s40, s35\n\t"
        "s_waitcnt vmcnt(14)\n\t"
        "v_and_b32 v48, s54, v48\n\t"
        "v_mfma_f32_16x16x32_bf16 v[32:35], a[128:131], v[104:107], v[32:35]\n\t"
        "v_pk_max_u16 v60, v154, v48\n\t"
        "v_and_b32 v49, s54, v49\n\t"
        "s_waitcnt lgkmcnt(9)\n\t"
        "v_mfma_f32_16x16x32_bf16 v[0:3], a[132:135], v[108:111], v[0:3]\n\t"
        "buffer_load_dwordx4 v134, s[16:19], s43 offen  lds\n\t"
        "v_readlane_b32 s40, v155, 17\n\t"
        "v_mfma_f32_16x16x32_bf16 v[4:7], a[136:139], v[108:111], v[4:7]\n\t"
        "v_readlane_b32 s43, v155, 5\n\t"
        "s_add_u32 m0, s40, s35\n\t"
        "v_mfma_f32_16x16x32_bf16 v[8:11], a[140:143], v[108:111], v[8:11]\n\t"
        "v_pk_max_u16 v60, v60, v49\n\t"
        "v_and_b32 v50, s54, v50\n\t"
        "s_waitcnt lgkmcnt(5)\n\t"
        "v_mfma_f32_16x16x32_bf16 v[12:15], a[132:135], v[112:115], v[12:15]\n\t"
        "v_pk_max_u16 v60, v60, v50\n\t"
        "v_and_b32 v51, s54, v51\n\t"
        "v_mfma_f32_16x16x32_bf16 v[16:19], a[136:139], v[112:115], v[16:19]\n\t"
        "buffer_load_dwordx4 v135, s[16:19], s43 offen  lds\n\t"
        "s_add_u32 m0, s37, 12288\n\t"
        "v_mfma_f32_16x16x32_bf16 v[20:23], a[140:143], v[112:115], v[20:23]\n\t"
        "s_add_u32 s36, s36, s38\n\t"
        "v_pk_max_u16 v60, v60, v51\n\t"
        "v_mfma_f32_16x16x32_bf16 v[24:27], a[108:111], v[112:115], v[24:27]\n\t"
        "s_waitcnt vmcnt(10) lgkmcnt(1)\n\t"
        "s_barrier\n\t"
        "ds_read_b64_tr_b16 a[156:157], v131 offset:36864\n\t"
        "ds_read_b64_tr_b16 a[158:159], v131 offset:37632\n\t"
        "v_mfma_f32_16x16x32_bf16 v[28:31], a[112:115], v[112:115], v[28:31]\n\t"
        "ds_read_b64_tr_b16 a[160:161], v132 offset:36864\n\t"
        "ds_read_b64_tr_b16 a[162:163], v132 offset:37632\n\t"
        "v_and_b32 v61, s54, v61\n\t"
        "v_mfma_f32_16x16x32_bf16 v[32:35], a[116:119], v[112:115], v[32:35]\n\t"
        "buffer_load_dwordx4 v140, s[24:27], s36 offen offset:0 lds\n\t"
        "ds_read_b64_tr_b16 a[164:165], v133 offset:36864\n\t"
        "v_pk_max_u16 v60, v60, v61\n\t"
        "v_mfma_f32_16x16x32_bf16 v[36:39], a[120:123], v[112:115], v[36:39]\n\t"
        "ds_read_b64_tr_b16 a[166:167], v133 offset:37632\n\t"
        "ds_read_b128 v[92:95], v129 offset:64\n\t"
        "v_cmp_ne_u32 s[12:13], v60, v154\n\t"
        "v_mfma_f32_16x16x32_bf16 v[40:43], a[124:127], v[112:115], v[40:43]\n\t"
        "ds_read_b128 v[96:99], v129 offset:4160\n\t"
        "ds_read_b64_tr_b16 a[168:169], v131 offset:43008\n\t"
        "s_or_b64 s[56:57], s[56:57], s[12:13]\n\t"
        "v_mfma_f32_16x16x32_bf16 v[44:47], a[128:131], v[112:115], v[44:47]\n\t"
        "ds_read_b64_tr_b16 a[170:171], v131 offset:43776\n\t"
        "ds_read_b64_tr_b16 a[172:173], v132 offset:43008\n\t"
        "s_add_u32 s61, s61, s46\n\t"
        "v_mfma_f32_16x16x32_bf16 v[24:27], a[132:135], v[84:87], v[24:27]\n\t"
        "buffer_load_dwordx4 v141, s[24:27], s36 offen offset:1024 lds\n\t"
        "ds_read_b64_tr_b16 a[174:175], v132 offset:43776\n\t"
        "v_add_u32 v54, s46, v54\n\t"
        "v_mfma_f32_16x16x32_bf16 v[28:31], a[136:139], v[84:87], v[28:31]\n\t"
        "ds_read_b64_tr_b16 a[176:177], v133 offset:43008\n\t"
        "ds_read_b64_tr_b16 a[178:179], v133 offset:43776\n\t"
        "v_min_u32 v56, 97, v56\n\t"
        "v_mfma_f32_16x16x32_bf16 v[32:35], a[140:143], v[84:87], v[32:35]\n\t"
        "ds_read_b128 v[100:103], v129 offset:8256\n\t"
        "v_lshl_add_u32 v56, v56, 3, s60\n\t"
        "v_mfma_f32_16x16x32_bf16 v[36:39], a[108:111], v[84:87], v[36:39]\n\t"
        "s_mov_b64 exec, s[64:65]\n\t"
        "ds_write_b64 v56, v[52:53]\n\t"
        "s_mov_b64 exec, -1\n\t"
        "v_mfma_f32_16x16x32_bf16 v[40:43], a[112:115], v[84:87], v[40:43]\n\t"
        "buffer_load_dwordx4 v142, s[24:27], s36 offen offset:2048 lds\n\t"
        "v_readlane_b32 s40, v155, 18\n\t"
        "v_mfma_f32_16x16x32_bf16 v[44:47], a[116:119], v[84:87], v[44:47]\n\t"
        "v_readlane_b32 s43, v155, 6\n\t"
        "s_add_u32 m0, s40, s35\n\t"
        "s_waitcnt lgkmcnt(7)\n\t"
        "v_mfma_f32_16x16x32_bf16 v[36:39], a[132:135], v[88:91], v[36:39]\n\t"
        "ds_read_b128 v[104:107], v129 offset:12352\n\t"
        "v_add_u32 v53, 2048, v53\n\t"
        "v_mfma_f32_16x16x32_bf16 v[40:43], a[136:139], v[88:91], v[40:43]\n\t"
        "v_readlane_b32 s46, v155, 39\n\t"
        "v_readlane_b32 s47, v155, 42\n\t"
        "v_mfma_f32_16x16x32_bf16 v[44:47], a[140:143], v[88:91], v[44:47]\n\t"
        "v_perm_b32 v56, v66, v66, s44\n\t"
        "v_perm_b32 v57, v66, v66, s45\n\t"
        "v_mfma_f32_16x16x32_bf16 v[0:3], a[156:159], v[92:95], v[0:3]\n\t"
        "v_add_f32 v58, s46, v68\n\t"
        "v_add_f32 v59, s47, v69\n\t"
        "v_mfma_f32_16x16x32_bf16 v[4:7], a[160:163], v[92:95], v[4:7]\n\t"
        "buffer_load_dwordx4 v136, s[16:19], s43 offen  lds\n\t"
        "s_bitcmp1_b32 s42, 10\n\t"
        "s_cbranch_scc1 .Lx52%=\n\t"
        ".Lx53%=:\n\t"
        "v_mfma_f32_16x16x32_bf16 v[8:11], a[164:167], v[92:95], v[8:11]\n\t"
        "v_readlane_b32 s40, v155, 19\n\t"
        "v_readlane_b32 s43, v155, 7\n\t"
        "v_mfma_f32_16x16x32_bf16 v[0:3], a[144:147], v[96:99], v[0:3]\n\t"
        "s_add_u32 m0, s40, s35\n\t"
        "v_add_f32 v56, v56, v58\n\t"
        "v_mfma_f32_16x16x32_bf16 v[4:7], a[148:151], v[96:99], v[4:7]\n\t"
        "v_add_f32 v57, v57, v59\n\t"
        "v_cvt_pk_bf16_f32 v52, v56, v57\n\t"
        "v_mfma_f32_16x16x32_bf16 v[8:11], a[152:155], v[96:99], v[8:11]\n\t"
        "v_cvt_pk_bf16_f32 v58, v58, v59\n\t"
        "v_cmp_ne_u32 s[64:65], v52, v58\n\t"
        "v_mfma_f32_16x16x32_bf16 v[12:15], a[156:159], v[96:99], v[12:15]\n\t"
        "ds_read_b128 v[108:111], v129 offset:16448\n\t"
        "v_mbcnt_lo_u32_b32 v56, s64, v54\n\t"
        "v_mfma_f32_16x16x32_bf16 v[16:19], a[160:163], v[96:99], v[16:19]\n\t"
        "v_mbcnt_hi_u32_b32 v56, s65, v56\n\t"
        "s_bcnt1_i32_b64 s46, s[64:65]\n\t"
        "v_mfma_f32_16x16x32_bf16 v[20:23], a[164:167], v[96:99], v[20:23]\n\t"
        "s_add_u32 s61, s61, s46\n\t"
        "s_mov_b32 s3, s61\n\t"
        "s_waitcnt lgkmcnt(1)\n\t"
        "v_mfma_f32_16x16x32_bf16 v[0:3], a[168:171], v[100:103], v[0:3]\n\t"
        "v_min_u32 v56, 97, v56\n\t"
        "v_lshl_add_u32 v56, v56, 3, s60\n\t"
        "v_mfma_f32_16x16x32_bf16 v[4:7], a[172:175], v[100:103], v[4:7]\n\t"
        "s_mov_b64 exec, s[64:65]\n\t"
        "ds_write_b64 v56, v[52:53]\n\t"
        "s_mov_b64 exec, -1\n\t"
        "v_mfma_f32_16x16x32_bf16 v[8:11], a[176:179], v[100:103], v[8:11]\n\t"
        "s_cmp_gt_u32 s3, 64\n\t"
        "s_cselect_b32 s8, 1, 0\n\t"
        "v_mfma_f32_16x16x32_bf16 v[12:15], a[144:147], v[100:103], v[12:15]\n\t"
        "buffer_load_dwordx4 v137, s[16:19], s43 offen  lds\n\t"
        "s_add_u32 m0, s37, 24576\n\t"
        "v_mfma_f32_16x16x32_bf16 v[16:19], a[148:151], v[100:103], v[16:19]\n\t"
        "ds_read_b128 v[112:115], v129 offset:20544\n\t"
        "s_add_u32 s36, s36, s5\n\t"
        "v_mfma_f32_16x16x32_bf16 v[20:23], a[152:155], v[100:103], v[20:23]\n\t"
        "s_waitcnt vmcnt(10)\n\t"
        "s_barrier\n\t"
        "ds_read_b64_tr_b16 a[180:181], v131 offset:49152\n\t"
        "ds_read_b64_tr_b16 a[182:183], v131 offset:49920\n\t"
        "v_mfma_f32_16x16x32_bf16 v[24:27], a[156:159], v[100:103], v[24:27]\n\t"
        "ds_read_b64_tr_b16 a[184:185], v132 offset:49152\n\t"
        "ds_read_b64_tr_b16 a[186:187], v132 offset:49920\n\t"
        "s_or_b32 s56, s56, s8\n\t"
        "v_mfma_f32_16x16x32_bf16 v[28:31], a[160:163], v[100:103], v[28:31]\n\t"
        "buffer_load_dwordx4 v140, s[24:27], s36 offen offset:0 lds\n\t"
        "ds_read_b64_tr_b16 a[188:189], v133 offset:49152\n\t"
        "s_min_u32 s61, s61, 80\n\t"
        "v_mfma_f32_16x16x32_bf16 v[32:35], a[164:167], v[100:103], v[32:35]\n\t"
        "ds_read_b64_tr_b16 a[190:191], v133 offset:49920\n\t"
        "ds_read_b64_tr_b16 a[192:193], v131 offset:55296\n\t"
        "v_min_u32 v78, s61, v116\n\t"
        "v_mfma_f32_16x16x32_bf16 v[12:15], a[168:171], v[104:107], v[12:15]\n\t"
        "ds_read_b64_tr_b16 a[194:195], v131 offset:56064\n\t"
        "ds_read_b64_tr_b16 a[196:197], v132 offset:55296\n\t"
        "v_lshl_add_u32 v78, v78, 3, s60\n\t"
        "v_mfma_f32_16x16x32_bf16 v[16:19], a[172:175], v[104:107], v[16:19]\n\t"
        "ds_read_b64 v[62:63], v78\n\t"
        "ds_read_b64_tr_b16 a[198:199], v132 offset:56064\n\t"
        "v_cmp_ge_u32 s[66:67], s61, v116\n\t"
        "v_mfma_f32_16x16x32_bf16 v[20:23], a[176:179], v[104:107], v[20:23]\n\t"
        "buffer_load_dwordx4 v141, s[24:27], s36 offen offset:1024 lds\n\t"
        "ds_read_b64_tr_b16 a[200:201], v133 offset:55296\n\t"
        "s_mov_b32 s46, 0x07060302\n\t"
        "v_mfma_f32_16x16x32_bf16 v[24:27], a[144:147], v[104:107], v[24:27]\n\t"
        "ds_read_b64_tr_b16 a[202:203], v133 offset:56064\n\t"
        "v_mfma_f32_16x16x32_bf16 v[28:31], a[148:151], v[104:107], v[28:31]\n\t"
        "ds_read_b128 v[84:87], v129 offset:128\n\t"
        "v_mfma_f32_16x16x32_bf16 v[32:35], a[152:155], v[104:107], v[32:35]\n\t"
        "ds_read_b128 v[88:91], v129 offset:4224\n\t"
        "v_mfma_f32_16x16x32_bf16 v[36:39], a[156:159], v[104:107], v[36:39]\n\t"
        "buffer_load_dwordx4 v142, s[24:27], s36 offen offset:2048 lds\n\t"
        "s_add_u32 m0, s37, 36864\n\t"
        "v_mfma_f32_16x16x32_bf16 v[40:43], a[160:163], v[104:107], v[40:43]\n\t"
        "s_add_u32 s36, s36, s39\n\t"
        "v_mfma_f32_16x16x32_bf16 v[44:47], a[164:167], v[104:107], v[44:47]\n\t"
        "s_waitcnt lgkmcnt(8)\n\t"
        "v_mfma_f32_16x16x32_bf16 v[24:27], a[168:171], v[108:111], v[24:27]\n\t"
        "v_mfma_f32_16x16x32_bf16 v[28:31], a[172:175], v[108:111], v[28:31]\n\t"
        "s_mov_b64 exec, s[66:67]\n\t"
        "s_waitcnt lgkmcnt(4)\n\t"
        "ds_write_b32 v63, v117\n\t"
        "s_mov_b64 exec, -1\n\t"
        "v_mfma_f32_16x16x32_bf16 v[32:35], a[176:179], v[108:111], v[32:35]\n\t"
        "buffer_load_dwordx4 v140, s[24:27], s36 offen offset:0 lds\n\t"
        "v_and_b32 v78, 0xfffff800, v63\n\t"
        "v_mfma_f32_16x16x32_bf16 v[36:39], a[144:147], v[108:111], v[36:39]\n\t"
        "ds_read_b128 v[64:67], v78 offset:1536\n\t"
        "v_lshlrev_b32 v72, 16, v62\n\t"
        "v_mfma_f32_16x16x32_bf16 v[40:43], a[148:151], v[108:111], v[40:43]\n\t"
        "v_and_b32 v73, 0xffff0000, v62\n\t"
        "v_med3_f32 v72, v72, 0, s63\n\t"
        "v_mfma_f32_16x16x32_bf16 v[44:47], a[152:155], v[108:111], v[44:47]\n\t"
        "v_med3_f32 v73, v73, 0, s63\n\t"
        "v_perm_b32 v62, v73, v72, s46\n\t"
        "v_mfma_f32_16x16x32_bf16 v[36:39], a[168:171], v[112:115], v[36:39]\n\t"
        "buffer_load_dwordx4 v141, s[24:27], s36 offen offset:1024 lds\n\t"
        "v_fract_f32 v74, v72\n\t"
        "v_mfma_f32_16x16x32_bf16 v[40:43], a[172:175], v[112:115], v[40:43]\n\t"
        "v_fract_f32 v75, v73\n\t"
        "v_cvt_u32_f32 v76, v72\n\t"
        "v_mfma_f32_16x16x32_bf16 v[44:47], a[176:179], v[112:115], v[44:47]\n\t"
        "v_cvt_u32_f32 v77, v73\n\t"
        "v_cmp_neq_f32 s[64:65], 0, v74\n\t"
        "s_waitcnt lgkmcnt(2)\n\t"
        "v_mfma_f32_16x16x32_bf16 v[0:3], a[192:195], v[84:87], v[0:3]\n\t"
        "v_cmp_neq_f32 s[44:45], 0, v75\n\t"
        "v_sub_f32 v78, 1.0, v74\n\t"
        "v_mfma_f32_16x16x32_bf16 v[4:7], a[196:199], v[84:87], v[4:7]\n\t"
        "v_sub_f32 v79, 1.0, v75\n\t"
        "v_cvt_pk_bf16_f32 v79, v78, v79\n\t"
        "v_mfma_f32_16x16x32_bf16 v[8:11], a[200:203], v[84:87], v[8:11]\n\t"
        "v_lshlrev_b32 v78, 16, v79\n\t"
        "v_and_b32 v79, 0xffff0000, v79\n\t"
        "v_mfma_f32_16x16x32_bf16 v[0:3], a[180:183], v[88:91], v[0:3]\n\t"
        "ds_read_b128 v[92:95], v129 offset:8320\n\t"
        "v_mul_f32 v80, v78, v79\n\t"
        "v_mfma_f32_16x16x32_bf16 v[4:7], a[184:187], v[88:91], v[4:7]\n\t"
        "s_waitcnt vmcnt(10)\n\t"
        "s_barrier\n\t"
        "buffer_load_dwordx4 v142, s[24:27], s36 offen offset:2048 lds\n\t"
        "s_add_u32 m0, s37, 49152\n\t"
        "ds_read_b64_tr_b16 a[204:205], v131 offset:0\n\t"
        "v_mfma_f32_16x16x32_bf16 v[8:11], a[188:191], v[88:91], v[8:11]\n\t"
        "ds_read_b64_tr_b16 a[206:207], v131 offset:768\n\t"
        "s_add_u32 s36, s36, s38\n\t"
        "ds_read_b64_tr_b16 a[208:209], v132 offset:0\n\t"
        "v_mfma_f32_16x16x32_bf16 v[12:15], a[192:195], v[88:91], v[12:15]\n\t"
        "ds_read_b64_tr_b16 a[210:211], v132 offset:768\n\t"
        "ds_read_b64_tr_b16 a[212:213], v133 offset:0\n\t"
        "ds_read_b64_tr_b16 a[214:215], v133 offset:768\n\t"
        "v_mul_f32 v78, v78, v75\n\t"
        "v_mfma_f32_16x16x32_bf16 v[16:19], a[196:199], v[88:91], v[16:19]\n\t"
        "ds_read_b128 v[96:99], v129 offset:8320\n\t"
        "ds_read_b128 v[100:103], v129 offset:12416\n\t"
        "v_mul_f32 v79, v74, v79\n\t"
        "v_mfma_f32_16x16x32_bf16 v[20:23], a[200:203], v[88:91], v[20:23]\n\t"
        "buffer_load_dwordx4 v140, s[24:27], s36 offen offset:0 lds\n\t"
        "ds_read_b128 v[104:107], v129 offset:16512\n\t"
        "v_mul_f32 v81, v74, v75\n\t"
        "s_waitcnt lgkmcnt(9)\n\t"
        "v_mfma_f32_16x16x32_bf16 v[12:15], a[180:183], v[92:95], v[12:15]\n\t"
        "ds_read_b64_tr_b16 a[216:217], v131 offset:6144\n\t"
        "ds_read_b64_tr_b16 a[218:219], v131 offset:6912\n\t"
        "v_cndmask_b32 v78, v79, v78, s[44:45]\n\t"
        "v_mfma_f32_16x16x32_bf16 v[16:19], a[184:187], v[92:95], v[16:19]\n\t"
        "ds_read_b64_tr_b16 a[220:221], v132 offset:6144\n\t"
        "ds_read_b64_tr_b16 a[222:223], v132 offset:6912\n\t"
        "v_cndmask_b32 v79, 0, v79, s[44:45]\n\t"
        "v_mfma_f32_16x16x32_bf16 v[20:23], a[188:191], v[92:95], v[20:23]\n\t"
        "ds_read_b64_tr_b16 a[224:225], v133 offset:6144\n\t"
        "ds_read_b64_tr_b16 a[226:227], v133 offset:6912\n\t"
        "v_cvt_pk_bf16_f32 v70, v80, v78\n\t"
        "v_mfma_f32_16x16x32_bf16 v[24:27], a[192:195], v[92:95], v[24:27]\n\t"
        "buffer_load_dwordx4 v141, s[24:27], s36 offen offset:1024 lds\n\t"
        "v_cvt_pk_bf16_f32 v71, v79, v81\n\t"
        "v_mfma_f32_16x16x32_bf16 v[28:31], a[196:199], v[92:95], v[28:31]\n\t"
        "ds_read_b128 v[108:111], v129 offset:20608\n\t"
        "v_pk_max_u16 v78, v62, v64\n\t"
        "v_mfma_f32_16x16x32_bf16 v[32:35], a[200:203], v[92:95], v[32:35]\n\t"
        "v_pk_min_u16 v78, v78, v65\n\t"
        "v_cmp_ne_u32 vcc, v78, v62\n\t"
        "s_waitcnt lgkmcnt(9)\n\t"
        "v_mfma_f32_16x16x32_bf16 v[0:3], a[204:207], v[96:99], v[0:3]\n\t"
        "v_sub_u32 v78, v77, v67\n\t"
        "v_mad_u32_u24 v79, v77, 6, v66\n\t"
        "v_mfma_f32_16x16x32_bf16 v[4:7], a[208:211], v[96:99], v[4:7]\n\t"
        "buffer_load_dwordx4 v142, s[24:27], s36 offen offset:2048 lds\n\t"
        "v_mad_u32_u24 v79, v76, s62, v79\n\t"
        "v_mfma_f32_16x16x32_bf16 v[8:11], a[212:215], v[96:99], v[8:11]\n\t"
        "s_and_b64 vcc, vcc, s[66:67]\n\t"
        "s_or_b64 s[56:57], s[56:57], vcc\n\t"
        "s_waitcnt lgkmcnt(6)\n\t"
        "v_mfma_f32_16x16x32_bf16 v[12:15], a[204:207], v[100:103], v[12:15]\n\t"
        "v_bfe_u32 v80, v78, 2, 1\n\t"
        "v_or_b32 v80, v80, v79\n\t"
        "v_mfma_f32_16x16x32_bf16 v[16:19], a[208:211], v[100:103], v[16:19]\n\t"
        "v_add_u32 v78, 1, v78\n\t"
        "v_bfe_u32 v78, v78, 2, 1\n\t"
        "v_mfma_f32_16x16x32_bf16 v[20:23], a[212:215], v[100:103], v[20:23]\n\t"
        "v_add3_u32 v78, v79, v78, 6\n\t"
        "v_cndmask_b32 v78, v80, v78, s[44:45]\n\t"
        "v_mfma_f32_16x16x32_bf16 v[24:27], a[180:183], v[100:103], v[24:27]\n\t"
        "v_cndmask_b32 v79, 0, v119, s[64:65]\n\t"
        "v_add_u32 v81, v80, v79\n\t"
        "v_mfma_f32_16x16x32_bf16 v[28:31], a[184:187], v[100:103], v[28:31]\n\t"
        "v_add_u32 v82, v78, v79\n\t"
        "v_cndmask_b32 v78, v81, v78, s[44:45]\n\t"
        "v_mfma_f32_16x16x32_bf16 v[32:35], a[188:191], v[100:103], v[32:35]\n\t"
        "v_lshl_or_b32 v68, v78, 16, v80\n\t"
        "v_lshl_or_b32 v69, v82, 16, v81\n\t"
        "v_mfma_f32_16x16x32_bf16 v[36:39], a[192:195], v[100:103], v[36:39]\n\t"
        "ds_write_b128 v118, v[68:71]\n\t"
        "v_mfma_f32_16x16x32_bf16 v[40:43], a[196:199], v[100:103], v[40:43]\n\t"
        "s_cmp_gt_u32 s3, 16\n\t"
        "s_cselect_b32 s8, 2, 0\n\t"
        "v_mfma_f32_16x16x32_bf16 v[44:47], a[200:203], v[100:103], v[44:47]\n\t"
        "s_waitcnt vmcnt(9) lgkmcnt(1)\n\t"
        "s_barrier\n\t"
        "ds_read_b64_tr_b16 a[228:229], v131 offset:12288\n\t"
        "ds_read_b64_tr_b16 a[230:231], v131 offset:13056\n\t"
        "v_mfma_f32_16x16x32_bf16 v[24:27], a[204:207], v[104:107], v[24:27]\n\t"
        "ds_read_b64_tr_b16 a[232:233], v132 offset:12288\n\t"
        "ds_read_b64_tr_b16 a[234:235], v132 offset:13056\n\t"
        "ds_read_b64_tr_b16 a[236:237], v133 offset:12288\n\t"
        "v_xor_b32 v154, 32, v186\n\t"
        "v_mfma_f32_16x16x32_bf16 v[28:31], a[208:211], v[104:107], v[28:31]\n\t"
        "ds_read_b64_tr_b16 a[238:239], v133 offset:13056\n\t"
        "ds_read_b128 v[112:115], v130 offset:0\n\t"
        "v_mfma_f32_16x16x32_bf16 v[32:35], a[212:215], v[104:107], v[32:35]\n\t"
        "ds_read_b128 v[84:87], v130 offset:4096\n\t"
        "ds_read_b64_tr_b16 a[240:241], v131 offset:18432\n\t"
        "v_mfma_f32_16x16x32_bf16 v[36:39], a[180:183], v[104:107], v[36:39]\n\t"
        "s_cmp_lg_u64 s[56:57], 0\n\t"
        "s_addc_u32 s8, s8, 0\n\t"
        "ds_read_b64_tr_b16 a[242:243], v131 offset:19200\n\t"
        "ds_read_b64_tr_b16 a[244:245], v132 offset:18432\n\t"
        "v_mfma_f32_16x16x32_bf16 v[40:43], a[184:187], v[104:107], v[40:43]\n\t"
        "ds_read_b64_tr_b16 a[246:247], v132 offset:19200\n\t"
        "ds_read_b64_tr_b16 a[248:249], v133 offset:18432\n\t"
        "v_mov_b32 v56, s8\n\t"
        "v_mfma_f32_16x16x32_bf16 v[44:47], a[188:191], v[104:107], v[44:47]\n\t"
        "ds_read_b64_tr_b16 a[250:251], v133 offset:19200\n\t"
        "v_mfma_f32_16x16x32_bf16 v[36:39], a[204:207], v[108:111], v[36:39]\n\t"
        "ds_read_b128 v[88:91], v130 offset:8192\n\t"
        "v_mfma_f32_16x16x32_bf16 v[40:43], a[208:211], v[108:111], v[40:43]\n\t"
        "s_mov_b64 exec, 1\n\t"
        "ds_write_b32 v153, v56\n\t"
        "s_mov_b64 exec, -1\n\t"
        "v_mfma_f32_16x16x32_bf16 v[44:47], a[212:215], v[108:111], v[44:47]\n\t"
        "ds_read_b128 v[160:163], v185 offset:0\n\t"
        "s_waitcnt lgkmcnt(10)\n\t"
        "v_mfma_f32_16x16x32_bf16 v[0:3], a[228:231], v[112:115], v[0:3]\n\t"
        "ds_read_b128 v[92:95], v130 offset:12288\n\t"
        "v_mfma_f32_16x16x32_bf16 v[4:7], a[232:235], v[112:115], v[4:7]\n\t"
        "v_mfma_f32_16x16x32_bf16 v[8:11], a[236:239], v[112:115], v[8:11]\n\t"
        "s_waitcnt lgkmcnt(9)\n\t"
        "v_mfma_f32_16x16x32_bf16 v[0:3], a[216:219], v[84:87], v[0:3]\n\t"
        "s_waitcnt vmcnt(6)\n\t"
        "s_barrier\n\t"
        "ds_read_b64_tr_b16 a[252:253], v131 offset:24576\n\t"
        "ds_read_b64_tr_b16 a[254:255], v131 offset:25344\n\t"
        "v_mfma_f32_16x16x32_bf16 v[4:7], a[220:223], v[84:87], v[4:7]\n\t"
        "ds_read_b64_tr_b16 v[188:189], v132 offset:24576\n\t"
        "ds_read_b64_tr_b16 v[190:191], v132 offset:25344\n\t"
        "v_mfma_f32_16x16x32_bf16 v[8:11], a[224:227], v[84:87], v[8:11]\n\t"
        "ds_read_b64_tr_b16 v[192:193], v133 offset:24576\n\t"
        "ds_read_b64_tr_b16 v[194:195], v133 offset:25344\n\t"
        "v_mfma_f32_16x16x32_bf16 v[12:15], a[228:231], v[84:87], v[12:15]\n\t"
        "ds_read_b128 v[96:99], v130 offset:16384\n\t"
        "v_mfma_f32_16x16x32_bf16 v[16:19], a[232:235], v[84:87], v[16:19]\n\t"
        "ds_read_b64_tr_b16 v[196:197], v131 offset:30720\n\t"
        "v_mfma_f32_16x16x32_bf16 v[20:23], a[236:239], v[84:87], v[20:23]\n\t"
        "ds_read_b64_tr_b16 v[198:199], v131 offset:31488\n\t"
        "ds_read_b64_tr_b16 v[200:201], v132 offset:30720\n\t"
        "s_waitcnt lgkmcnt(10)\n\t"
        "v_lshlrev_b32_sdwa v157, 5, v160 dst_sel:DWORD dst_unused:UNUSED_PAD src0_sel:DWORD src1_sel:WORD_0\n\t"
        "v_mfma_f32_16x16x32_bf16 v[0:3], a[240:243], v[88:91], v[0:3]\n\t"
        "ds_read_b64_tr_b16 v[202:203], v132 offset:31488\n\t"
        "ds_read_b64_tr_b16 v[204:205], v133 offset:30720\n\t"
        "v_lshlrev_b32_sdwa v158, 5, v160 dst_sel:DWORD dst_unused:UNUSED_PAD src0_sel:DWORD src1_sel:WORD_1\n\t"
        "v_mfma_f32_16x16x32_bf16 v[4:7], a[244:247], v[88:91], v[4:7]\n\t"
        "ds_read_b64_tr_b16 v[206:207], v133 offset:31488\n\t"
        "v_or_b32 v157, v184, v157\n\t"
        "v_mfma_f32_16x16x32_bf16 v[8:11], a[248:251], v[88:91], v[8:11]\n\t"
        "ds_read_b64_tr_b16 v[60:61], v157 offset:0\n\t"
        "v_or_b32 v158, v184, v158\n\t"
        "v_mfma_f32_16x16x32_bf16 v[12:15], a[216:219], v[88:91], v[12:15]\n\t"
        "ds_read_b64_tr_b16 v[62:63], v158 offset:0\n\t"
        "v_xor_b32 v159, 32, v157\n\t"
        "v_mfma_f32_16x16x32_bf16 v[16:19], a[220:223], v[88:91], v[16:19]\n\t"
        "ds_read_b64_tr_b16 v[64:65], v159 offset:0\n\t"
        "v_xor_b32 v152, 32, v158\n\t"
        "v_mfma_f32_16x16x32_bf16 v[20:23], a[224:227], v[88:91], v[20:23]\n\t"
        "ds_read_b64_tr_b16 v[66:67], v152 offset:0\n\t"
        "v_perm_b32 v164, v162, v162, v168\n\t"
        "v_mfma_f32_16x16x32_bf16 v[24:27], a[228:231], v[88:91], v[24:27]\n\t"
        "ds_read_b64_tr_b16 v[68:69], v157 offset:64\n\t"
        "v_perm_b32 v165, v162, v162, v169\n\t"
        "v_mfma_f32_16x16x32_bf16 v[28:31], a[232:235], v[88:91], v[28:31]\n\t"
        "ds_read_b64_tr_b16 v[70:71], v158 offset:64\n\t"
        "v_perm_b32 v166, v162, v162, v170\n\t"
        "v_mfma_f32_16x16x32_bf16 v[32:35], a[236:239], v[88:91], v[32:35]\n\t"
        "ds_read_b128 v[100:103], v130 offset:64\n\t"
        "v_perm_b32 v167, v162, v162, v171\n\t"
        "v_mfma_f32_16x16x32_bf16 v[12:15], a[240:243], v[92:95], v[12:15]\n\t"
        "ds_read_b64_tr_b16 v[72:73], v159 offset:64\n\t"
        "v_cmp_ne_u32 vcc, 0, v163\n\t"
        "v_mfma_f32_16x16x32_bf16 v[16:19], a[244:247], v[92:95], v[16:19]\n\t"
        "ds_read_b64_tr_b16 v[74:75], v152 offset:64\n\t"
        "v_mfma_f32_16x16x32_bf16 v[20:23], a[248:251], v[92:95], v[20:23]\n\t"
        "ds_read_b128 v[104:107], v130 offset:4160\n\t"
        "v_mfma_f32_16x16x32_bf16 v[24:27], a[216:219], v[92:95], v[24:27]\n\t"
        "ds_read_b64_tr_b16 v[76:77], v157 offset:128\n\t"
        "s_waitcnt lgkmcnt(9)\n\t"
        "v_mfma_f32_16x16x32_bf16 v[48:51], v[60:63], v[164:167], 0\n\t"
        "ds_read_b64_tr_b16 v[78:79], v158 offset:128\n\t"
        "v_mfma_f32_16x16x32_bf16 v[28:31], a[220:223], v[92:95], v[28:31]\n\t"
        "ds_read_b64_tr_b16 v[80:81], v159 offset:128\n\t"
        "s_waitcnt lgkmcnt(9)\n\t"
        "v_mfma_f32_16x16x32_bf16 v[52:55], v[64:67], v[164:167], 0\n\t"
        "ds_read_b64_tr_b16 v[82:83], v152 offset:128\n\t"
        "v_mfma_f32_16x16x32_bf16 v[32:35], a[224:227], v[92:95], v[32:35]\n\t"
        "ds_read_b128 v[108:111], v130 offset:20480\n\t"
        "s_waitcnt lgkmcnt(9)\n\t"
        "v_mfma_f32_16x16x32_bf16 v[56:59], v[68:71], v[164:167], 0\n\t"
        "v_mfma_f32_16x16x32_bf16 v[36:39], a[228:231], v[92:95], v[36:39]\n\t"
        "v_mfma_f32_16x16x32_bf16 v[40:43], a[232:235], v[92:95], v[40:43]\n\t"
        "s_waitcnt lgkmcnt(6)\n\t"
        "v_mfma_f32_16x16x32_bf16 v[116:119], v[72:75], v[164:167], 0\n\t"
        "v_mfma_f32_16x16x32_bf16 v[44:47], a[236:239], v[92:95], v[44:47]\n\t"
        "v_mfma_f32_16x16x32_bf16 v[24:27], a[240:243], v[96:99], v[24:27]\n\t"
        "s_waitcnt lgkmcnt(3)\n\t"
        "v_mfma_f32_16x16x32_bf16 v[120:123], v[76:79], v[164:167], 0\n\t"
        "s_waitcnt vmcnt(0)\n\t"
        "s_barrier\n\t"
        "ds_read_b64_tr_b16 v[208:209], v131 offset:36864\n\t"
        "ds_read_b64_tr_b16 v[210:211], v131 offset:37632\n\t"
        "v_mfma_f32_16x16x32_bf16 v[28:31], a[244:247], v[96:99], v[28:31]\n\t"
        "ds_read_b64_tr_b16 v[212:213], v132 offset:36864\n\t"
        "ds_read_b64_tr_b16 v[214:215], v132 offset:37632\n\t"
        "s_waitcnt lgkmcnt(5)\n\t"
        "v_mfma_f32_16x16x32_bf16 v[124:127], v[80:83], v[164:167], 0\n\t"
        "ds_read_b64_tr_b16 v[216:217], v133 offset:36864\n\t"
        "ds_read_b64_tr_b16 v[218:219], v133 offset:37632\n\t"
        "s_cbranch_vccnz .Lx54%=\n\t"
        ".Lx55%=:\n\t"
        "v_mfma_f32_16x16x32_bf16 v[32:35], a[248:251], v[96:99], v[32:35]\n\t"
        "ds_read_b128 v[112:115], v130 offset:8256\n\t"
        "v_cvt_pk_bf16_f32 v48, v48, v49\n\t"
        "v_mfma_f32_16x16x32_bf16 v[36:39], a[216:219], v[96:99], v[36:39]\n\t"
        "ds_read_b64_tr_b16 v[220:221], v131 offset:43008\n\t"
        "v_cvt_pk_bf16_f32 v49, v50, v51\n\t"
        "v_mfma_f32_16x16x32_bf16 v[40:43], a[220:223], v[96:99], v[40:43]\n\t"
        "ds_write_b64 v186, v[48:49] offset:0\n\t"
        "v_mfma_f32_16x16x32_bf16 v[44:47], a[224:227], v[96:99], v[44:47]\n\t"
        "ds_read_b64_tr_b16 v[222:223], v131 offset:43776\n\t"
        "ds_read_b64_tr_b16 v[224:225], v132 offset:43008\n\t"
        "v_cvt_pk_bf16_f32 v52, v52, v53\n\t"
        "v_mfma_f32_16x16x32_bf16 v[0:3], v[196:199], v[100:103], v[0:3]\n\t"
        "ds_read_b128 v[84:87], v130 offset:12352\n\t"
        "ds_read_b64_tr_b16 v[226:227], v132 offset:43776\n\t"
        "v_cvt_pk_bf16_f32 v53, v54, v55\n\t"
        "v_mfma_f32_16x16x32_bf16 v[4:7], v[200:203], v[100:103], v[4:7]\n\t"
        "ds_write_b64 v154, v[52:53] offset:0\n\t"
        "ds_read_b64_tr_b16 v[228:229], v133 offset:43008\n\t"
        "v_mfma_f32_16x16x32_bf16 v[8:11], v[204:207], v[100:103], v[8:11]\n\t"
        "ds_read_b64_tr_b16 v[230:231], v133 offset:43776\n\t"
        "v_cvt_pk_bf16_f32 v56, v56, v57\n\t"
        "v_mfma_f32_16x16x32_bf16 v[0:3], a[252:255], v[104:107], v[0:3]\n\t"
        "ds_read_b64_tr_b16 v[232:233], v131 offset:49152\n\t"
        "v_cvt_pk_bf16_f32 v57, v58, v59\n\t"
        "v_mfma_f32_16x16x32_bf16 v[4:7], v[188:191], v[104:107], v[4:7]\n\t"
        "ds_write_b64 v186, v[56:57] offset:64\n\t"
        "v_mfma_f32_16x16x32_bf16 v[8:11], v[192:195], v[104:107], v[8:11]\n\t"
        "ds_read_b64_tr_b16 v[234:235], v131 offset:49920\n\t"
        "v_cvt_pk_bf16_f32 v116, v116, v117\n\t"
        "v_mfma_f32_16x16x32_bf16 v[12:15], v[196:199], v[104:107], v[12:15]\n\t"
        "ds_read_b64_tr_b16 v[236:237], v132 offset:49152\n\t"
        "v_cvt_pk_bf16_f32 v117, v118, v119\n\t"
        "v_mfma_f32_16x16x32_bf16 v[16:19], v[200:203], v[104:107], v[16:19]\n\t"
        "ds_write_b64 v154, v[116:117] offset:64\n\t"
        "v_mfma_f32_16x16x32_bf16 v[20:23], v[204:207], v[104:107], v[20:23]\n\t"
        "ds_read_b64_tr_b16 v[238:239], v132 offset:49920\n\t"
        "v_cvt_pk_bf16_f32 v120, v120, v121\n\t"
        "s_waitcnt lgkmcnt(9)\n\t"
        "v_mfma_f32_16x16x32_bf16 v[36:39], a[240:243], v[108:111], v[36:39]\n\t"
        "ds_read_b128 v[88:91], v130 offset:16448\n\t"
        "v_cvt_pk_bf16_f32 v121, v122, v123\n\t"
        "v_mfma_f32_16x16x32_bf16 v[40:43], a[244:247], v[108:111], v[40:43]\n\t"
        "ds_write_b64 v186, v[120:121] offset:128\n\t"
        "v_mfma_f32_16x16x32_bf16 v[44:47], a[248:251], v[108:111], v[44:47]\n\t"
        "ds_read_b64_tr_b16 v[240:241], v133 offset:49152\n\t"
        "ds_read_b64_tr_b16 v[242:243], v133 offset:49920\n\t"
        "v_cvt_pk_bf16_f32 v124, v124, v125\n\t"
        "v_mfma_f32_16x16x32_bf16 v[0:3], v[208:211], v[112:115], v[0:3]\n\t"
        "ds_read_b64_tr_b16 v[244:245], v131 offset:55296\n\t"
        "v_cvt_pk_bf16_f32 v125, v126, v127\n\t"
        "v_mfma_f32_16x16x32_bf16 v[4:7], v[212:215], v[112:115], v[4:7]\n\t"
        "ds_write_b64 v154, v[124:125] offset:128\n\t"
        "v_mfma_f32_16x16x32_bf16 v[8:11], v[216:219], v[112:115], v[8:11]\n\t"
        "ds_read_b64_tr_b16 v[246:247], v131 offset:56064\n\t"
        "v_mfma_f32_16x16x32_bf16 v[12:15], a[252:255], v[112:115], v[12:15]\n\t"
        "ds_read_b128 v[92:95], v130 offset:128\n\t"
        "v_mfma_f32_16x16x32_bf16 v[16:19], v[188:191], v[112:115], v[16:19]\n\t"
        "ds_read_b64_tr_b16 v[248:249], v132 offset:55296\n\t"
        "v_mfma_f32_16x16x32_bf16 v[20:23], v[192:195], v[112:115], v[20:23]\n\t"
        "ds_read_b64_tr_b16 v[250:251], v132 offset:56064\n\t"
        "v_mfma_f32_16x16x32_bf16 v[24:27], v[196:199], v[112:115], v[24:27]\n\t"
        "ds_read_b128 v[96:99], v130 offset:4224\n\t"
        "v_mfma_f32_16x16x32_bf16 v[28:31], v[200:203], v[112:115], v[28:31]\n\t"
        "ds_read_b64_tr_b16 v[252:253], v133 offset:55296\n\t"
        "v_mfma_f32_16x16x32_bf16 v[32:35], v[204:207], v[112:115], v[32:35]\n\t"
        "ds_read_b64_tr_b16 v[254:255], v133 offset:56064\n\t"
        "v_mfma_f32_16x16x32_bf16 v[12:15], v[208:211], v[84:87], v[12:15]\n\t"
        "v_mfma_f32_16x16x32_bf16 v[16:19], v[212:215], v[84:87], v[16:19]\n\t"
        "v_mfma_f32_16x16x32_bf16 v[20:23], v[216:219], v[84:87], v[20:23]\n\t"
        "v_mfma_f32_16x16x32_bf16 v[24:27], a[252:255], v[84:87], v[24:27]\n\t"
        "ds_read_b128 v[100:103], v130 offset:8320\n\t"
        "v_mfma_f32_16x16x32_bf16 v[28:31], v[188:191], v[84:87], v[28:31]\n\t"
        "v_mfma_f32_16x16x32_bf16 v[32:35], v[192:195], v[84:87], v[32:35]\n\t"
        "v_mfma_f32_16x16x32_bf16 v[36:39], v[196:199], v[84:87], v[36:39]\n\t"
        "s_waitcnt lgkmcnt(1)\n\t"
        "s_barrier\n\t"
        "v_mfma_f32_16x16x32_bf16 v[40:43], v[200:203], v[84:87], v[40:43]\n\t"
        "ds_read_b128 v[124:127], v182 offset:0\n\t"
        "v_mfma_f32_16x16x32_bf16 v[44:47], v[204:207], v[84:87], v[44:47]\n\t"
        "ds_read_b128 v[132:135], v187 offset:0\n\t"
        "v_mfma_f32_16x16x32_bf16 v[24:27], v[208:211], v[88:91], v[24:27]\n\t"
        "ds_read_b128 v[136:139], v187 offset:16\n\t"
        "v_mfma_f32_16x16x32_bf16 v[28:31], v[212:215], v[88:91], v[28:31]\n\t"
        "ds_read_b32 v140, v187 offset:32\n\t"
        "v_mfma_f32_16x16x32_bf16 v[32:35], v[216:219], v[88:91], v[32:35]\n\t"
        "v_mfma_f32_16x16x32_bf16 v[36:39], a[252:255], v[88:91], v[36:39]\n\t"
        "ds_read_b128 v[104:107], v130 offset:12416\n\t"
        "v_mfma_f32_16x16x32_bf16 v[40:43], v[188:191], v[88:91], v[40:43]\n\t"
        "v_mfma_f32_16x16x32_bf16 v[44:47], v[192:195], v[88:91], v[44:47]\n\t"
        "v_mfma_f32_16x16x32_bf16 v[0:3], v[232:235], v[92:95], v[0:3]\n\t"
        "v_mfma_f32_16x16x32_bf16 v[4:7], v[236:239], v[92:95], v[4:7]\n\t"
        "s_waitcnt lgkmcnt(4)\n\t"
        "v_or3_b32 v124, v124, v125, v126\n\t"
        "v_or_b32 v124, v124, v127\n\t"
        "v_mfma_f32_16x16x32_bf16 v[8:11], v[240:243], v[92:95], v[8:11]\n\t"
        "v_readfirstlane_b32 s8, v124\n\t"
        "v_mfma_f32_16x16x32_bf16 v[0:3], v[220:223], v[96:99], v[0:3]\n\t"
        "s_nop 0\n\t"
        "s_cmp_lg_u32 s8, 0\n\t"
        "s_cbranch_scc1 .Lx56%=\n\t"
        ".Lx57%=:\n\t"
        "v_mfma_f32_16x16x32_bf16 v[4:7], v[224:227], v[96:99], v[4:7]\n\t"
        "s_waitcnt lgkmcnt(2)\n\t"
        "v_xor_b32 v132, v181, v132\n\t"
        "v_add_u32 v151, s9, v172\n\t"
        "v_mfma_f32_16x16x32_bf16 v[8:11], v[228:231], v[96:99], v[8:11]\n\t"
        "v_max_u32 v151, v151, v132\n\t"
        "v_xor_b32 v133, v181, v133\n\t"
        "v_mfma_f32_16x16x32_bf16 v[12:15], v[232:235], v[96:99], v[12:15]\n\t"
        "ds_read_b128 v[108:111], v130 offset:16512\n\t"
        "v_add_u32 v183, s9, v173\n\t"
        "v_mfma_f32_16x16x32_bf16 v[16:19], v[236:239], v[96:99], v[16:19]\n\t"
        "v_max_u32 v183, v183, v133\n\t"
        "v_xor_b32 v134, v181, v134\n\t"
        "v_mfma_f32_16x16x32_bf16 v[20:23], v[240:243], v[96:99], v[20:23]\n\t"
        "v_mfma_f32_16x16x32_bf16 v[0:3], v[244:247], v[100:103], v[0:3]\n\t"
        "v_mfma_f32_16x16x32_bf16 v[4:7], v[248:251], v[100:103], v[4:7]\n\t"
        "v_mfma_f32_16x16x32_bf16 v[8:11], v[252:255], v[100:103], v[8:11]\n\t"
        "v_mfma_f32_16x16x32_bf16 v[12:15], v[220:223], v[100:103], v[12:15]\n\t"
        "ds_read_b128 v[112:115], v130 offset:20544\n\t"
        "v_mfma_f32_16x16x32_bf16 v[16:19], v[224:227], v[100:103], v[16:19]\n\t"
        "v_mfma_f32_16x16x32_bf16 v[20:23], v[228:231], v[100:103], v[20:23]\n\t"
        "v_mfma_f32_16x16x32_bf16 v[24:27], v[232:235], v[100:103], v[24:27]\n\t"
        "ds_read_b128 v[84:87], v130 offset:20608\n\t"
        "s_add_u32 s55, s41, 0\n\t"
        "v_mfma_f32_16x16x32_bf16 v[28:31], v[236:239], v[100:103], v[28:31]\n\t"
        "v_cvt_pk_bf16_f32 v116, v0, v1\n\t"
        "v_cvt_pk_bf16_f32 v117, v2, v3\n\t"
        "v_mfma_f32_16x16x32_bf16 v[32:35], v[240:243], v[100:103], v[32:35]\n\t"
        "v_cvt_pk_bf16_f32 v118, v4, v5\n\t"
        "v_cvt_pk_bf16_f32 v119, v6, v7\n\t"
        "s_waitcnt lgkmcnt(2)\n\t"
        "v_mfma_f32_16x16x32_bf16 v[12:15], v[244:247], v[104:107], v[12:15]\n\t"
        "ds_read_b128 v[88:91], v151 offset:0\n\t"
        "v_cvt_pk_bf16_f32 v120, v8, v9\n\t"
        "v_mfma_f32_16x16x32_bf16 v[16:19], v[248:251], v[104:107], v[16:19]\n\t"
        "v_cvt_pk_bf16_f32 v121, v10, v11\n\t"
        "v_mfma_f32_16x16x32_bf16 v[20:23], v[252:255], v[104:107], v[20:23]\n\t"
        "v_permlane16_swap_b32 v116, v118\n\t"
        "v_mfma_f32_16x16x32_bf16 v[24:27], v[220:223], v[104:107], v[24:27]\n\t"
        "v_permlane16_swap_b32 v117, v119\n\t"
        "v_mfma_f32_16x16x32_bf16 v[28:31], v[224:227], v[104:107], v[28:31]\n\t"
        "buffer_store_dwordx4 v[116:119], v144, s[28:31], s55 offen sc1\n\t"
        "v_mfma_f32_16x16x32_bf16 v[32:35], v[228:231], v[104:107], v[32:35]\n\t"
        "ds_write_b64 v146, v[120:121] offset:0\n\t"
        "v_mfma_f32_16x16x32_bf16 v[36:39], v[232:235], v[104:107], v[36:39]\n\t"
        "ds_read_b128 v[92:95], v151 offset:64\n\t"
        "s_add_u32 s55, s41, 43008\n\t"
        "v_mfma_f32_16x16x32_bf16 v[40:43], v[236:239], v[104:107], v[40:43]\n\t"
        "ds_read_b128 v[96:99], v151 offset:128\n\t"
        "v_add_u32 v151, s9, v174\n\t"
        "v_mfma_f32_16x16x32_bf16 v[44:47], v[240:243], v[104:107], v[44:47]\n\t"
        "v_max_u32 v151, v151, v134\n\t"
        "v_xor_b32 v135, v181, v135\n\t"
        "v_mfma_f32_16x16x32_bf16 v[24:27], v[244:247], v[108:111], v[24:27]\n\t"
        "ds_read_b128 v[100:103], v183 offset:0\n\t"
        "v_cvt_pk_bf16_f32 v116, v12, v13\n\t"
        "v_mfma_f32_16x16x32_bf16 v[28:31], v[248:251], v[108:111], v[28:31]\n\t"
        "v_cvt_pk_bf16_f32 v117, v14, v15\n\t"
        "v_cvt_pk_bf16_f32 v118, v16, v17\n\t"
        "v_mfma_f32_16x16x32_bf16 v[32:35], v[252:255], v[108:111], v[32:35]\n\t"
        "v_cvt_pk_bf16_f32 v119, v18, v19\n\t"
        "v_cvt_pk_bf16_f32 v120, v20, v21\n\t"
        "v_mfma_f32_16x16x32_bf16 v[36:39], v[220:223], v[108:111], v[36:39]\n\t"
        "ds_read_b128 v[104:107], v183 offset:64\n\t"
        "v_cvt_pk_bf16_f32 v121, v22, v23\n\t"
        "v_mfma_f32_16x16x32_bf16 v[40:43], v[224:227], v[108:111], v[40:43]\n\t"
        "v_permlane16_swap_b32 v116, v118\n\t"
        "v_mfma_f32_16x16x32_bf16 v[44:47], v[228:231], v[108:111], v[44:47]\n\t"
        "v_permlane16_swap_b32 v117, v119\n\t"
        "s_waitcnt lgkmcnt(5)\n\t"
        "v_mfma_f32_16x16x32_bf16 v[36:39], v[208:211], v[112:115], v[36:39]\n\t"
        "ds_read_b128 v[108:111], v183 offset:128\n\t"
        "v_add_u32 v183, s9, v175\n\t"
        "v_mfma_f32_16x16x32_bf16 v[40:43], v[212:215], v[112:115], v[40:43]\n\t"
        "v_max_u32 v183, v183, v135\n\t"
        "v_xor_b32 v136, v181, v136\n\t"
        "v_mfma_f32_16x16x32_bf16 v[44:47], v[216:219], v[112:115], v[44:47]\n\t"
        "ds_read_b128 v[132:135], v187 offset:2048\n\t"
        "v_mfma_f32_16x16x32_bf16 v[36:39], v[244:247], v[84:87], v[36:39]\n\t"
        "buffer_store_dwordx4 v[116:119], v144, s[28:31], s55 offen sc1\n\t"
        "v_mfma_f32_16x16x32_bf16 v[40:43], v[248:251], v[84:87], v[40:43]\n\t"
        "ds_write_b64 v146, v[120:121] offset:2048\n\t"
        "v_mfma_f32_16x16x32_bf16 v[44:47], v[252:255], v[84:87], v[44:47]\n\t"
        "ds_read_b128 v[112:115], v151 offset:0\n\t"
        "s_add_u32 s55, s41, 86016\n\t"
        "v_mfma_f32_16x16x32_bf16 v[48:51], a[12:15], v[88:91], 0\n\t"
        "ds_read_b128 v[84:87], v151 offset:64\n\t"
        "v_cvt_pk_bf16_f32 v116, v24, v25\n\t"
        "v_mfma_f32_16x16x32_bf16 v[52:55], a[16:19], v[88:91], 0\n\t"
        "v_cvt_pk_bf16_f32 v117, v26, v27\n\t"
        "v_cvt_pk_bf16_f32 v118, v28, v29\n\t"
        "v_mfma_f32_16x16x32_bf16 v[56:59], a[20:23], v[88:91], 0\n\t"
        "v_cvt_pk_bf16_f32 v119, v30, v31\n\t"
        "v_cvt_pk_bf16_f32 v120, v32, v33\n\t"
        "s_waitcnt lgkmcnt(5)\n\t"
        "v_mfma_f32_16x16x32_bf16 v[48:51], a[48:51], v[92:95], v[48:51]\n\t"
        "ds_read_b128 v[88:91], v151 offset:128\n\t"
        "v_add_u32 v151, s9, v176\n\t"
        "v_mfma_f32_16x16x32_bf16 v[52:55], a[52:55], v[92:95], v[52:55]\n\t"
        "v_max_u32 v151, v151, v136\n\t"
        "v_xor_b32 v137, v181, v137\n\t"
        "v_mfma_f32_16x16x32_bf16 v[56:59], a[56:59], v[92:95], v[56:59]\n\t"
        "v_cvt_pk_bf16_f32 v121, v34, v35\n\t"
        "v_mfma_f32_16x16x32_bf16 v[48:51], a[84:87], v[96:99], v[48:51]\n\t"
        "v_permlane16_swap_b32 v116, v118\n\t"
        "v_mfma_f32_16x16x32_bf16 v[52:55], a[88:91], v[96:99], v[52:55]\n\t"
        "v_permlane16_swap_b32 v117, v119\n\t"
        "v_mfma_f32_16x16x32_bf16 v[56:59], a[92:95], v[96:99], v[56:59]\n\t"
        "ds_read_b128 v[92:95], v183 offset:0\n\t"
        "v_mfma_f32_16x16x32_bf16 v[48:51], a[120:123], v[100:103], v[48:51]\n\t"
        "ds_read_b128 v[96:99], v183 offset:64\n\t"
        "v_mfma_f32_16x16x32_bf16 v[52:55], a[124:127], v[100:103], v[52:55]\n\t"
        "v_mfma_f32_16x16x32_bf16 v[56:59], a[128:131], v[100:103], v[56:59]\n\t"
        "buffer_store_dwordx4 v[116:119], v144, s[28:31], s55 offen sc1\n\t"
        "v_mfma_f32_16x16x32_bf16 v[48:51], a[156:159], v[104:107], v[48:51]\n\t"
        "ds_write_b64 v146, v[120:121] offset:4096\n\t"
        "v_mfma_f32_16x16x32_bf16 v[52:55], a[160:163], v[104:107], v[52:55]\n\t"
        "ds_read_b128 v[100:103], v183 offset:128\n\t"
        "v_add_u32 v183, s9, v177\n\t"
        "v_mfma_f32_16x16x32_bf16 v[56:59], a[164:167], v[104:107], v[56:59]\n\t"
        "v_max_u32 v183, v183, v137\n\t"
        "v_xor_b32 v138, v181, v138\n\t"
        "s_waitcnt lgkmcnt(4)\n\t"
        "v_mfma_f32_16x16x32_bf16 v[48:51], a[192:195], v[108:111], v[48:51]\n\t"
        "ds_read_b128 v[104:107], v151 offset:0\n\t"
        "s_add_u32 s55, s41, 129024\n\t"
        "v_mfma_f32_16x16x32_bf16 v[52:55], a[196:199], v[108:111], v[52:55]\n\t"
        "v_cvt_pk_bf16_f32 v116, v36, v37\n\t"
        "v_cvt_pk_bf16_f32 v117, v38, v39\n\t"
        "v_mfma_f32_16x16x32_bf16 v[56:59], a[200:203], v[108:111], v[56:59]\n\t"
        "v_cvt_pk_bf16_f32 v118, v40, v41\n\t"
        "v_cvt_pk_bf16_f32 v119, v42, v43\n\t"
        "v_mfma_f32_16x16x32_bf16 v[48:51], a[228:231], v[112:115], v[48:51]\n\t"
        "ds_read_b128 v[108:111], v151 offset:64\n\t"
        "v_cvt_pk_bf16_f32 v120, v44, v45\n\t"
        "v_mfma_f32_16x16x32_bf16 v[52:55], a[232:235], v[112:115], v[52:55]\n\t"
        "v_cvt_pk_bf16_f32 v121, v46, v47\n\t"
        "v_mfma_f32_16x16x32_bf16 v[56:59], a[236:239], v[112:115], v[56:59]\n\t"
        "v_permlane16_swap_b32 v116, v118\n\t"
        "v_mfma_f32_16x16x32_bf16 v[48:51], v[196:199], v[84:87], v[48:51]\n\t"
        "v_permlane16_swap_b32 v117, v119\n\t"
        "v_mfma_f32_16x16x32_bf16 v[52:55], v[200:203], v[84:87], v[52:55]\n\t"
        "ds_read_b128 v[112:115], v151 offset:128\n\t"
        "v_add_u32 v151, s9, v178\n\t"
        "v_mfma_f32_16x16x32_bf16 v[56:59], v[204:207], v[84:87], v[56:59]\n\t"
        "v_max_u32 v151, v151, v138\n\t"
        "v_xor_b32 v139, v181, v139\n\t"
        "v_mfma_f32_16x16x32_bf16 v[48:51], v[232:235], v[88:91], v[48:51]\n\t"
        "ds_read_b128 v[84:87], v183 offset:0\n\t"
        "v_mfma_f32_16x16x32_bf16 v[52:55], v[236:239], v[88:91], v[52:55]\n\t"
        "buffer_store_dwordx4 v[116:119], v144, s[28:31], s55 offen sc1\n\t"
        "v_mfma_f32_16x16x32_bf16 v[56:59], v[240:243], v[88:91], v[56:59]\n\t"
        "ds_write_b64 v146, v[120:121] offset:6144\n\t"
        "s_waitcnt lgkmcnt(4)\n\t"
        "v_mfma_f32_16x16x32_bf16 v[48:51], a[0:3], v[92:95], v[48:51]\n\t"
        "ds_read_b128 v[88:91], v183 offset:64\n\t"
        "v_mfma_f32_16x16x32_bf16 v[52:55], a[4:7], v[92:95], v[52:55]\n\t"
        "v_mfma_f32_16x16x32_bf16 v[56:59], a[8:11], v[92:95], v[56:59]\n\t"
        "v_mfma_f32_16x16x32_bf16 v[48:51], a[36:39], v[96:99], v[48:51]\n\t"
        "ds_read_b128 v[92:95], v183 offset:128\n\t"
        "v_add_u32 v183, s9, v179\n\t"
        "v_mfma_f32_16x16x32_bf16 v[52:55], a[40:43], v[96:99], v[52:55]\n\t"
        "v_max_u32 v183, v183, v139\n\t"
        "v_xor_b32 v140, v181, v140\n\t"
        "v_mfma_f32_16x16x32_bf16 v[56:59], a[44:47], v[96:99], v[56:59]\n\t"
        "s_waitcnt lgkmcnt(2)\n\t"
        "s_barrier\n\t"
        "v_mfma_f32_16x16x32_bf16 v[48:51], a[72:75], v[100:103], v[48:51]\n\t"
        "ds_read_b128 v[136:139], v187 offset:2064\n\t"
        "v_mfma_f32_16x16x32_bf16 v[52:55], a[76:79], v[100:103], v[52:55]\n\t"
        "ds_read_b128 v[124:127], v147 offset:0\n\t"
        "v_mfma_f32_16x16x32_bf16 v[56:59], a[80:83], v[100:103], v[56:59]\n\t"
        "ds_read_b128 v[96:99], v151 offset:0\n\t"
        "v_mfma_f32_16x16x32_bf16 v[48:51], a[108:111], v[104:107], v[48:51]\n\t"
        "ds_read_b128 v[100:103], v151 offset:64\n\t"
        "v_mfma_f32_16x16x32_bf16 v[52:55], a[112:115], v[104:107], v[52:55]\n\t"
        "v_mfma_f32_16x16x32_bf16 v[56:59], a[116:119], v[104:107], v[56:59]\n\t"
        "v_mfma_f32_16x16x32_bf16 v[48:51], a[144:147], v[108:111], v[48:51]\n\t"
        "ds_read_b128 v[104:107], v151 offset:128\n\t"
        "v_add_u32 v151, s9, v180\n\t"
        "v_mfma_f32_16x16x32_bf16 v[52:55], a[148:151], v[108:111], v[52:55]\n\t"
        "v_max_u32 v151, v151, v140\n\t"
        "v_xor_b32 v132, v181, v132\n\t"
        "v_mfma_f32_16x16x32_bf16 v[56:59], a[152:155], v[108:111], v[56:59]\n\t"
        "ds_read_b32 v140, v187 offset:2080\n\t"
        "v_mfma_f32_16x16x32_bf16 v[48:51], a[180:183], v[112:115], v[48:51]\n\t"
        "ds_read_b128 v[108:111], v183 offset:0\n\t"
        "v_mfma_f32_16x16x32_bf16 v[52:55], a[184:187], v[112:115], v[52:55]\n\t"
        "s_waitcnt lgkmcnt(5)\n\t"
        "buffer_store_dwordx4 v[124:127], v145, s[28:31], s50 offen sc1\n\t"
        "v_mfma_f32_16x16x32_bf16 v[56:59], a[188:191], v[112:115], v[56:59]\n\t"
        "ds_read_b128 v[124:127], v147 offset:4096\n\t"
        "s_add_u32 s50, s50, 86016\n\t"
        "v_mfma_f32_16x16x32_bf16 v[48:51], a[216:219], v[84:87], v[48:51]\n\t"
        "ds_read_b128 v[112:115], v183 offset:64\n\t"
        "v_mfma_f32_16x16x32_bf16 v[52:55], a[220:223], v[84:87], v[52:55]\n\t"
        "v_mfma_f32_16x16x32_bf16 v[56:59], a[224:227], v[84:87], v[56:59]\n\t"
        "v_mfma_f32_16x16x32_bf16 v[48:51], a[252:255], v[88:91], v[48:51]\n\t"
        "ds_read_b128 v[84:87], v183 offset:128\n\t"
        "v_readlane_b32 s9, v155, 47\n\t"
        "v_mfma_f32_16x16x32_bf16 v[52:55], v[188:191], v[88:91], v[52:55]\n\t"
        "s_nop 0\n\t"
        "v_add_u32 v183, s9, v172\n\t"
        "v_max_u32 v183, v183, v132\n\t"
        "v_mfma_f32_16x16x32_bf16 v[56:59], v[192:195], v[88:91], v[56:59]\n\t"
        "v_xor_b32 v133, v181, v133\n\t"
        "v_mfma_f32_16x16x32_bf16 v[48:51], v[220:223], v[92:95], v[48:51]\n\t"
        "ds_read_b128 v[88:91], v151 offset:0\n\t"
        "v_mfma_f32_16x16x32_bf16 v[52:55], v[224:227], v[92:95], v[52:55]\n\t"
        "v_mfma_f32_16x16x32_bf16 v[56:59], v[228:231], v[92:95], v[56:59]\n\t"
        "s_waitcnt lgkmcnt(4)\n\t"
        "v_mfma_f32_16x16x32_bf16 v[48:51], a[24:27], v[96:99], v[48:51]\n\t"
        "ds_read_b128 v[92:95], v151 offset:64\n\t"
        "v_mfma_f32_16x16x32_bf16 v[52:55], a[28:31], v[96:99], v[52:55]\n\t"
        "v_mfma_f32_16x16x32_bf16 v[56:59], a[32:35], v[96:99], v[56:59]\n\t"
        "v_mfma_f32_16x16x32_bf16 v[48:51], a[60:63], v[100:103], v[48:51]\n\t"
        "s_waitcnt lgkmcnt(3)\n\t"
        "buffer_store_dwordx4 v[124:127], v145, s[28:31], s50 offen sc1\n\t"
        "v_mfma_f32_16x16x32_bf16 v[52:55], a[64:67], v[100:103], v[52:55]\n\t"
        "ds_read_b128 v[96:99], v151 offset:128\n\t"
        "v_add_u32 v151, s9, v173\n\t"
        "v_mfma_f32_16x16x32_bf16 v[56:59], a[68:71], v[100:103], v[56:59]\n\t"
        "v_max_u32 v151, v151, v133\n\t"
        "v_xor_b32 v134, v181, v134\n\t"
        "v_mfma_f32_16x16x32_bf16 v[48:51], a[96:99], v[104:107], v[48:51]\n\t"
        "ds_read_b128 v[100:103], v183 offset:0\n\t"
        "v_mfma_f32_16x16x32_bf16 v[52:55], a[100:103], v[104:107], v[52:55]\n\t"
        "v_mfma_f32_16x16x32_bf16 v[56:59], a[104:107], v[104:107], v[56:59]\n\t"
        "v_mfma_f32_16x16x32_bf16 v[48:51], a[132:135], v[108:111], v[48:51]\n\t"
        "ds_read_b128 v[104:107], v183 offset:64\n\t"
        "v_mfma_f32_16x16x32_bf16 v[52:55], a[136:139], v[108:111], v[52:55]\n\t"
        "v_mfma_f32_16x16x32_bf16 v[56:59], a[140:143], v[108:111], v[56:59]\n\t"
        "v_mfma_f32_16x16x32_bf16 v[48:51], a[168:171], v[112:115], v[48:51]\n\t"
        "ds_read_b128 v[108:111], v183 offset:128\n\t"
        "v_add_u32 v183, s9, v174\n\t"
        "v_mfma_f32_16x16x32_bf16 v[52:55], a[172:175], v[112:115], v[52:55]\n\t"
        "v_max_u32 v183, v183, v134\n\t"
        "v_xor_b32 v135, v181, v135\n\t"
        "v_mfma_f32_16x16x32_bf16 v[56:59], a[176:179], v[112:115], v[56:59]\n\t"
        "s_waitcnt lgkmcnt(3)\n\t"
        "v_mfma_f32_16x16x32_bf16 v[48:51], a[204:207], v[84:87], v[48:51]\n\t"
        "ds_read_b128 v[112:115], v151 offset:0\n\t"
        "v_mfma_f32_16x16x32_bf16 v[52:55], a[208:211], v[84:87], v[52:55]\n\t"
        "v_mfma_f32_16x16x32_bf16 v[56:59], a[212:215], v[84:87], v[56:59]\n\t"
        "v_mfma_f32_16x16x32_bf16 v[48:51], a[240:243], v[88:91], v[48:51]\n\t"
        "ds_read_b128 v[84:87], v151 offset:64\n\t"
        "v_mfma_f32_16x16x32_bf16 v[52:55], a[244:247], v[88:91], v[52:55]\n\t"
        "v_mfma_f32_16x16x32_bf16 v[56:59], a[248:251], v[88:91], v[56:59]\n\t"
        "v_mfma_f32_16x16x32_bf16 v[48:51], v[208:211], v[92:95], v[48:51]\n\t"
        "ds_read_b128 v[88:91], v151 offset:128\n\t"
        "v_add_u32 v151, s9, v175\n\t"
        "v_mfma_f32_16x16x32_bf16 v[52:55], v[212:215], v[92:95], v[52:55]\n\t"
        "v_max_u32 v151, v151, v135\n\t"
        "v_xor_b32 v136, v181, v136\n\t"
        "v_mfma_f32_16x16x32_bf16 v[56:59], v[216:219], v[92:95], v[56:59]\n\t"
        "ds_read_b128 v[132:135], v187 offset:4096\n\t"
        "v_mfma_f32_16x16x32_bf16 v[48:51], v[244:247], v[96:99], v[48:51]\n\t"
        "ds_read_b128 v[92:95], v183 offset:0\n\t"
        "v_mfma_f32_16x16x32_bf16 v[52:55], v[248:251], v[96:99], v[52:55]\n\t"
        "v_mfma_f32_16x16x32_bf16 v[56:59], v[252:255], v[96:99], v[56:59]\n\t"
        "s_waitcnt lgkmcnt(4)\n\t"
        "v_mfma_f32_16x16x32_bf16 v[60:63], a[12:15], v[100:103], 0\n\t"
        "ds_read_b128 v[96:99], v183 offset:64\n\t"
        "v_mfma_f32_16x16x32_bf16 v[64:67], a[16:19], v[100:103], 0\n\t"
        "v_mfma_f32_16x16x32_bf16 v[68:71], a[20:23], v[100:103], 0\n\t"
        "v_mfma_f32_16x16x32_bf16 v[60:63], a[48:51], v[104:107], v[60:63]\n\t"
        "ds_read_b128 v[100:103], v183 offset:128\n\t"
        "v_add_u32 v183, s9, v176\n\t"
        "v_mfma_f32_16x16x32_bf16 v[64:67], a[52:55], v[104:107], v[64:67]\n\t"
        "v_max_u32 v183, v183, v136\n\t"
        "v_xor_b32 v137, v181, v137\n\t"
        "v_mfma_f32_16x16x32_bf16 v[68:71], a[56:59], v[104:107], v[68:71]\n\t"
        "v_readlane_b32 s55, v155, 49\n\t"
        "v_cvt_pk_bf16_f32 v116, v48, v49\n\t"
        "v_mfma_f32_16x16x32_bf16 v[60:63], a[84:87], v[108:111], v[60:63]\n\t"
        "ds_read_b128 v[104:107], v151 offset:0\n\t"
        "v_cvt_pk_bf16_f32 v117, v50, v51\n\t"
        "v_mfma_f32_16x16x32_bf16 v[64:67], a[88:91], v[108:111], v[64:67]\n\t"
        "v_cvt_pk_bf16_f32 v118, v52, v53\n\t"
        "v_cvt_pk_bf16_f32 v119, v54, v55\n\t"
        "v_mfma_f32_16x16x32_bf16 v[68:71], a[92:95], v[108:111], v[68:71]\n\t"
        "v_cvt_pk_bf16_f32 v120, v56, v57\n\t"
        "v_cvt_pk_bf16_f32 v121, v58, v59\n\t"
        "v_mfma_f32_16x16x32_bf16 v[60:63], a[120:123], v[112:115], v[60:63]\n\t"
        "v_permlane16_swap_b32 v116, v118\n\t"
        "v_mfma_f32_16x16x32_bf16 v[64:67], a[124:127], v[112:115], v[64:67]\n\t"
        "v_permlane16_swap_b32 v117, v119\n\t"
        "v_mfma_f32_16x16x32_bf16 v[68:71], a[128:131], v[112:115], v[68:71]\n\t"
        "buffer_store_dwordx4 v[116:119], v148, s[28:31], s55 offen sc1\n\t"
        "s_waitcnt lgkmcnt(2)\n\t"
        "v_mfma_f32_16x16x32_bf16 v[60:63], a[156:159], v[84:87], v[60:63]\n\t"
        "ds_write_b64 v146, v[120:121] offset:8192\n\t"
        "v_mfma_f32_16x16x32_bf16 v[64:67], a[160:163], v[84:87], v[64:67]\n\t"
        "ds_read_b128 v[108:111], v151 offset:64\n\t"
        "v_mfma_f32_16x16x32_bf16 v[68:71], a[164:167], v[84:87], v[68:71]\n\t"
        "ds_read_b128 v[112:115], v151 offset:128\n\t"
        "v_add_u32 v151, s9, v177\n\t"
        "v_mfma_f32_16x16x32_bf16 v[60:63], a[192:195], v[88:91], v[60:63]\n\t"
        "ds_read_b128 v[84:87], v183 offset:0\n\t"
        "v_max_u32 v151, v151, v137\n\t"
        "v_mfma_f32_16x16x32_bf16 v[64:67], a[196:199], v[88:91], v[64:67]\n\t"
        "v_xor_b32 v138, v181, v138\n\t"
        "v_mfma_f32_16x16x32_bf16 v[68:71], a[200:203], v[88:91], v[68:71]\n\t"
        "v_mfma_f32_16x16x32_bf16 v[60:63], a[228:231], v[92:95], v[60:63]\n\t"
        "s_waitcnt lgkmcnt(3)\n\t"
        "s_barrier\n\t"
        "v_mfma_f32_16x16x32_bf16 v[64:67], a[232:235], v[92:95], v[64:67]\n\t"
        "ds_read_b128 v[124:127], v147 offset:8192\n\t"
        "v_mfma_f32_16x16x32_bf16 v[68:71], a[236:239], v[92:95], v[68:71]\n\t"
        "ds_read_b128 v[88:91], v183 offset:64\n\t"
        "v_mfma_f32_16x16x32_bf16 v[60:63], v[196:199], v[96:99], v[60:63]\n\t"
        "ds_read_b128 v[92:95], v183 offset:128\n\t"
        "v_add_u32 v183, s9, v178\n\t"
        "v_mfma_f32_16x16x32_bf16 v[64:67], v[200:203], v[96:99], v[64:67]\n\t"
        "v_max_u32 v183, v183, v138\n\t"
        "v_xor_b32 v139, v181, v139\n\t"
        "v_mfma_f32_16x16x32_bf16 v[68:71], v[204:207], v[96:99], v[68:71]\n\t"
        "v_mfma_f32_16x16x32_bf16 v[60:63], v[232:235], v[100:103], v[60:63]\n\t"
        "ds_read_b128 v[96:99], v151 offset:0\n\t"
        "v_mfma_f32_16x16x32_bf16 v[64:67], v[236:239], v[100:103], v[64:67]\n\t"
        "v_mfma_f32_16x16x32_bf16 v[68:71], v[240:243], v[100:103], v[68:71]\n\t"
        "v_mfma_f32_16x16x32_bf16 v[60:63], a[0:3], v[104:107], v[60:63]\n\t"
        "ds_read_b128 v[100:103], v151 offset:64\n\t"
        "v_mfma_f32_16x16x32_bf16 v[64:67], a[4:7], v[104:107], v[64:67]\n\t"
        "s_waitcnt lgkmcnt(4)\n\t"
        "buffer_store_dwordx4 v[124:127], v150, s[28:31], s51 offen sc1\n\t"
        "v_mfma_f32_16x16x32_bf16 v[68:71], a[8:11], v[104:107], v[68:71]\n\t"
        "v_mfma_f32_16x16x32_bf16 v[60:63], a[36:39], v[108:111], v[60:63]\n\t"
        "ds_read_b128 v[104:107], v151 offset:128\n\t"
        "v_add_u32 v151, s9, v179\n\t"
        "v_mfma_f32_16x16x32_bf16 v[64:67], a[40:43], v[108:111], v[64:67]\n\t"
        "v_max_u32 v151, v151, v139\n\t"
        "v_xor_b32 v140, v181, v140\n\t"
        "v_mfma_f32_16x16x32_bf16 v[68:71], a[44:47], v[108:111], v[68:71]\n\t"
        "ds_read_b128 v[136:139], v187 offset:4112\n\t"
        "v_mfma_f32_16x16x32_bf16 v[60:63], a[72:75], v[112:115], v[60:63]\n\t"
        "ds_read_b128 v[108:111], v183 offset:0\n\t"
        "v_mfma_f32_16x16x32_bf16 v[64:67], a[76:79], v[112:115], v[64:67]\n\t"
        "v_mfma_f32_16x16x32_bf16 v[68:71], a[80:83], v[112:115], v[68:71]\n\t"
        "v_mfma_f32_16x16x32_bf16 v[60:63], a[108:111], v[84:87], v[60:63]\n\t"
        "ds_read_b128 v[112:115], v183 offset:64\n\t"
        "v_mfma_f32_16x16x32_bf16 v[64:67], a[112:115], v[84:87], v[64:67]\n\t"
        "v_mfma_f32_16x16x32_bf16 v[68:71], a[116:119], v[84:87], v[68:71]\n\t"
        "s_waitcnt lgkmcnt(4)\n\t"
        "v_mfma_f32_16x16x32_bf16 v[60:63], a[144:147], v[88:91], v[60:63]\n\t"
        "ds_read_b128 v[84:87], v183 offset:128\n\t"
        "v_add_u32 v183, s9, v180\n\t"
        "v_mfma_f32_16x16x32_bf16 v[64:67], a[148:151], v[88:91], v[64:67]\n\t"
        "v_max_u32 v183, v183, v140\n\t"
        "v_xor_b32 v132, v181, v132\n\t"
        "v_mfma_f32_16x16x32_bf16 v[68:71], a[152:155], v[88:91], v[68:71]\n\t"
        "ds_read_b32 v140, v187 offset:4128\n\t"
        "v_mfma_f32_16x16x32_bf16 v[60:63], a[180:183], v[92:95], v[60:63]\n\t"
        "ds_read_b128 v[88:91], v151 offset:0\n\t"
        "v_mfma_f32_16x16x32_bf16 v[64:67], a[184:187], v[92:95], v[64:67]\n\t"
        "v_mfma_f32_16x16x32_bf16 v[68:71], a[188:191], v[92:95], v[68:71]\n\t"
        "v_mfma_f32_16x16x32_bf16 v[60:63], a[216:219], v[96:99], v[60:63]\n\t"
        "ds_read_b128 v[92:95], v151 offset:64\n\t"
        "v_mfma_f32_16x16x32_bf16 v[64:67], a[220:223], v[96:99], v[64:67]\n\t"
        "v_mfma_f32_16x16x32_bf16 v[68:71], a[224:227], v[96:99], v[68:71]\n\t"
        "v_mfma_f32_16x16x32_bf16 v[60:63], a[252:255], v[100:103], v[60:63]\n\t"
        "ds_read_b128 v[96:99], v151 offset:128\n\t"
        "v_readlane_b32 s9, v155, 48\n\t"
        "v_mfma_f32_16x16x32_bf16 v[64:67], v[188:191], v[100:103], v[64:67]\n\t"
        "s_nop 0\n\t"
        "v_add_u32 v151, s9, v172\n\t"
        "v_max_u32 v151, v151, v132\n\t"
        "v_mfma_f32_16x16x32_bf16 v[68:71], v[192:195], v[100:103], v[68:71]\n\t"
        "v_xor_b32 v133, v181, v133\n\t"
        "s_waitcnt lgkmcnt(4)\n\t"
        "v_mfma_f32_16x16x32_bf16 v[60:63], v[220:223], v[104:107], v[60:63]\n\t"
        "ds_read_b128 v[100:103], v183 offset:0\n\t"
        "v_mfma_f32_16x16x32_bf16 v[64:67], v[224:227], v[104:107], v[64:67]\n\t"
        "v_mfma_f32_16x16x32_bf16 v[68:71], v[228:231], v[104:107], v[68:71]\n\t"
        "v_mfma_f32_16x16x32_bf16 v[60:63], a[24:27], v[108:111], v[60:63]\n\t"
        "ds_read_b128 v[104:107], v183 offset:64\n\t"
        "v_mfma_f32_16x16x32_bf16 v[64:67], a[28:31], v[108:111], v[64:67]\n\t"
        "v_mfma_f32_16x16x32_bf16 v[68:71], a[32:35], v[108:111], v[68:71]\n\t"
        "v_mfma_f32_16x16x32_bf16 v[60:63], a[60:63], v[112:115], v[60:63]\n\t"
        "ds_read_b128 v[108:111], v183 offset:128\n\t"
        "v_add_u32 v183, s9, v173\n\t"
        "v_mfma_f32_16x16x32_bf16 v[64:67], a[64:67], v[112:115], v[64:67]\n\t"
        "v_max_u32 v183, v183, v133\n\t"
        "v_xor_b32 v134, v181, v134\n\t"
        "v_mfma_f32_16x16x32_bf16 v[68:71], a[68:71], v[112:115], v[68:71]\n\t"
        "v_mfma_f32_16x16x32_bf16 v[60:63], a[96:99], v[84:87], v[60:63]\n\t"
        "ds_read_b128 v[112:115], v151 offset:0\n\t"
        "v_mfma_f32_16x16x32_bf16 v[64:67], a[100:103], v[84:87], v[64:67]\n\t"
        "v_mfma_f32_16x16x32_bf16 v[68:71], a[104:107], v[84:87], v[68:71]\n\t"
        "s_waitcnt lgkmcnt(3)\n\t"
        "v_mfma_f32_16x16x32_bf16 v[60:63], a[132:135], v[88:91], v[60:63]\n\t"
        "ds_read_b128 v[84:87], v151 offset:64\n\t"
        "v_mfma_f32_16x16x32_bf16 v[64:67], a[136:139], v[88:91], v[64:67]\n\t"
        "v_mfma_f32_16x16x32_bf16 v[68:71], a[140:143], v[88:91], v[68:71]\n\t"
        "v_mfma_f32_16x16x32_bf16 v[60:63], a[168:171], v[92:95], v[60:63]\n\t"
        "ds_read_b128 v[88:91], v151 offset:128\n\t"
        "v_add_u32 v151, s9, v174\n\t"
        "v_mfma_f32_16x16x32_bf16 v[64:67], a[172:175], v[92:95], v[64:67]\n\t"
        "v_max_u32 v151, v151, v134\n\t"
        "v_xor_b32 v135, v181, v135\n\t"
        "v_mfma_f32_16x16x32_bf16 v[68:71], a[176:179], v[92:95], v[68:71]\n\t"
        "v_mfma_f32_16x16x32_bf16 v[60:63], a[204:207], v[96:99], v[60:63]\n\t"
        "ds_read_b128 v[92:95], v183 offset:0\n\t"
        "v_mfma_f32_16x16x32_bf16 v[64:67], a[208:211], v[96:99], v[64:67]\n\t"
        "v_mfma_f32_16x16x32_bf16 v[68:71], a[212:215], v[96:99], v[68:71]\n\t"
        "v_mfma_f32_16x16x32_bf16 v[60:63], a[240:243], v[100:103], v[60:63]\n\t"
        "ds_read_b128 v[96:99], v183 offset:64\n\t"
        "v_mfma_f32_16x16x32_bf16 v[64:67], a[244:247], v[100:103], v[64:67]\n\t"
        "v_mfma_f32_16x16x32_bf16 v[68:71], a[248:251], v[100:103], v[68:71]\n\t"
        "s_waitcnt lgkmcnt(3)\n\t"
        "v_mfma_f32_16x16x32_bf16 v[60:63], v[208:211], v[104:107], v[60:63]\n\t"
        "ds_read_b128 v[100:103], v183 offset:128\n\t"
        "v_add_u32 v183, s9, v175\n\t"
        "v_mfma_f32_16x16x32_bf16 v[64:67], v[212:215], v[104:107], v[64:67]\n\t"
        "v_max_u32 v183, v183, v135\n\t"
        "v_xor_b32 v136, v181, v136\n\t"
        "v_mfma_f32_16x16x32_bf16 v[68:71], v[216:219], v[104:107], v[68:71]\n\t"
        "v_mfma_f32_16x16x32_bf16 v[60:63], v[244:247], v[108:111], v[60:63]\n\t"
        "ds_read_b128 v[104:107], v151 offset:0\n\t"
        "v_mfma_f32_16x16x32_bf16 v[64:67], v[248:251], v[108:111], v[64:67]\n\t"
        "v_mfma_f32_16x16x32_bf16 v[68:71], v[252:255], v[108:111], v[68:71]\n\t"
        "v_mfma_f32_16x16x32_bf16 v[72:75], a[12:15], v[112:115], 0\n\t"
        "ds_read_b128 v[108:111], v151 offset:64\n\t"
        "v_mfma_f32_16x16x32_bf16 v[76:79], a[16:19], v[112:115], 0\n\t"
        "v_mfma_f32_16x16x32_bf16 v[80:83], a[20:23], v[112:115], 0\n\t"
        "v_mfma_f32_16x16x32_bf16 v[72:75], a[48:51], v[84:87], v[72:75]\n\t"
        "ds_read_b128 v[112:115], v151 offset:128\n\t"
        "v_add_u32 v151, s9, v176\n\t"
        "v_mfma_f32_16x16x32_bf16 v[76:79], a[52:55], v[84:87], v[76:79]\n\t"
        "v_max_u32 v151, v151, v136\n\t"
        "v_xor_b32 v137, v181, v137\n\t"
        "v_mfma_f32_16x16x32_bf16 v[80:83], a[56:59], v[84:87], v[80:83]\n\t"
        "v_readlane_b32 s55, v155, 50\n\t"
        "v_cvt_pk_bf16_f32 v116, v60, v61\n\t"
        "s_waitcnt lgkmcnt(3)\n\t"
        "v_mfma_f32_16x16x32_bf16 v[72:75], a[84:87], v[88:91], v[72:75]\n\t"
        "ds_read_b128 v[84:87], v183 offset:0\n\t"
        "v_cvt_pk_bf16_f32 v117, v62, v63\n\t"
        "v_mfma_f32_16x16x32_bf16 v[76:79], a[88:91], v[88:91], v[76:79]\n\t"
        "v_cvt_pk_bf16_f32 v118, v64, v65\n\t"
        "v_cvt_pk_bf16_f32 v119, v66, v67\n\t"
        "v_mfma_f32_16x16x32_bf16 v[80:83], a[92:95], v[88:91], v[80:83]\n\t"
        "v_cvt_pk_bf16_f32 v120, v68, v69\n\t"
        "v_cvt_pk_bf16_f32 v121, v70, v71\n\t"
        "v_mfma_f32_16x16x32_bf16 v[72:75], a[120:123], v[92:95], v[72:75]\n\t"
        "v_permlane16_swap_b32 v116, v118\n\t"
        "v_mfma_f32_16x16x32_bf16 v[76:79], a[124:127], v[92:95], v[76:79]\n\t"
        "v_permlane16_swap_b32 v117, v119\n\t"
        "v_mfma_f32_16x16x32_bf16 v[80:83], a[128:131], v[92:95], v[80:83]\n\t"
        "buffer_store_dwordx4 v[116:119], v148, s[28:31], s55 offen sc1\n\t"
        "v_mfma_f32_16x16x32_bf16 v[72:75], a[156:159], v[96:99], v[72:75]\n\t"
        "ds_write_b64 v146, v[120:121] offset:0\n\t"
        "v_mfma_f32_16x16x32_bf16 v[76:79], a[160:163], v[96:99], v[76:79]\n\t"
        "ds_read_b128 v[88:91], v183 offset:64\n\t"
        "v_mfma_f32_16x16x32_bf16 v[80:83], a[164:167], v[96:99], v[80:83]\n\t"
        "ds_read_b128 v[92:95], v183 offset:128\n\t"
        "v_add_u32 v183, s9, v177\n\t"
        "v_mfma_f32_16x16x32_bf16 v[72:75], a[192:195], v[100:103], v[72:75]\n\t"
        "ds_read_b128 v[96:99], v151 offset:0\n\t"
        "v_max_u32 v183, v183, v137\n\t"
        "v_mfma_f32_16x16x32_bf16 v[76:79], a[196:199], v[100:103], v[76:79]\n\t"
        "v_xor_b32 v138, v181, v138\n\t"
        "v_mfma_f32_16x16x32_bf16 v[80:83], a[200:203], v[100:103], v[80:83]\n\t"
        "s_waitcnt lgkmcnt(4)\n\t"
        "v_mfma_f32_16x16x32_bf16 v[72:75], a[228:231], v[104:107], v[72:75]\n\t"
        "ds_read_b128 v[100:103], v151 offset:64\n\t"
        "v_mfma_f32_16x16x32_bf16 v[76:79], a[232:235], v[104:107], v[76:79]\n\t"
        "v_mfma_f32_16x16x32_bf16 v[80:83], a[236:239], v[104:107], v[80:83]\n\t"
        "v_mfma_f32_16x16x32_bf16 v[72:75], v[196:199], v[108:111], v[72:75]\n\t"
        "ds_read_b128 v[104:107], v151 offset:128\n\t"
        "v_add_u32 v151, s9, v178\n\t"
        "v_mfma_f32_16x16x32_bf16 v[76:79], v[200:203], v[108:111], v[76:79]\n\t"
        "v_max_u32 v151, v151, v138\n\t"
        "v_xor_b32 v139, v181, v139\n\t"
        "v_mfma_f32_16x16x32_bf16 v[80:83], v[204:207], v[108:111], v[80:83]\n\t"
        "v_mfma_f32_16x16x32_bf16 v[72:75], v[232:235], v[112:115], v[72:75]\n\t"
        "ds_read_b128 v[108:111], v183 offset:0\n\t"
        "v_mfma_f32_16x16x32_bf16 v[76:79], v[236:239], v[112:115], v[76:79]\n\t"
        "v_mfma_f32_16x16x32_bf16 v[80:83], v[240:243], v[112:115], v[80:83]\n\t"
        "v_mfma_f32_16x16x32_bf16 v[72:75], a[0:3], v[84:87], v[72:75]\n\t"
        "ds_read_b128 v[112:115], v183 offset:64\n\t"
        "v_mfma_f32_16x16x32_bf16 v[76:79], a[4:7], v[84:87], v[76:79]\n\t"
        "v_mfma_f32_16x16x32_bf16 v[80:83], a[8:11], v[84:87], v[80:83]\n\t"
        "s_waitcnt lgkmcnt(3)\n\t"
        "v_mfma_f32_16x16x32_bf16 v[72:75], a[36:39], v[88:91], v[72:75]\n\t"
        "ds_read_b128 v[84:87], v183 offset:128\n\t"
        "v_add_u32 v183, s9, v179\n\t"
        "v_mfma_f32_16x16x32_bf16 v[76:79], a[40:43], v[88:91], v[76:79]\n\t"
        "v_max_u32 v183, v183, v139\n\t"
        "v_xor_b32 v140, v181, v140\n\t"
        "v_mfma_f32_16x16x32_bf16 v[80:83], a[44:47], v[88:91], v[80:83]\n\t"
        "v_mfma_f32_16x16x32_bf16 v[72:75], a[72:75], v[92:95], v[72:75]\n\t"
        "ds_read_b128 v[88:91], v151 offset:0\n\t"
        "v_mfma_f32_16x16x32_bf16 v[76:79], a[76:79], v[92:95], v[76:79]\n\t"
        "v_mfma_f32_16x16x32_bf16 v[80:83], a[80:83], v[92:95], v[80:83]\n\t"
        "v_mfma_f32_16x16x32_bf16 v[72:75], a[108:111], v[96:99], v[72:75]\n\t"
        "ds_read_b128 v[92:95], v151 offset:64\n\t"
        "v_mfma_f32_16x16x32_bf16 v[76:79], a[112:115], v[96:99], v[76:79]\n\t"
        "v_mfma_f32_16x16x32_bf16 v[80:83], a[116:119], v[96:99], v[80:83]\n\t"
        "ds_read_b128 v[96:99], v151 offset:128\n\t"
        "v_add_u32 v151, s9, v180\n\t"
        "v_mfma_f32_16x16x32_bf16 v[72:75], a[144:147], v[100:103], v[72:75]\n\t"
        "v_max_u32 v151, v151, v140\n\t"
        "v_mfma_f32_16x16x32_bf16 v[76:79], a[148:151], v[100:103], v[76:79]\n\t"
        "v_mfma_f32_16x16x32_bf16 v[80:83], a[152:155], v[100:103], v[80:83]\n\t"
        "ds_read_b128 v[100:103], v183 offset:0\n\t"
        "s_waitcnt lgkmcnt(4)\n\t"
        "v_mfma_f32_16x16x32_bf16 v[72:75], a[180:183], v[104:107], v[72:75]\n\t"
        "v_mfma_f32_16x16x32_bf16 v[76:79], a[184:187], v[104:107], v[76:79]\n\t"
        "v_mfma_f32_16x16x32_bf16 v[80:83], a[188:191], v[104:107], v[80:83]\n\t"
        "ds_read_b128 v[104:107], v183 offset:64\n\t"
        "v_mfma_f32_16x16x32_bf16 v[72:75], a[216:219], v[108:111], v[72:75]\n\t"
        "v_mfma_f32_16x16x32_bf16 v[76:79], a[220:223], v[108:111], v[76:79]\n\t"
        "v_mfma_f32_16x16x32_bf16 v[80:83], a[224:227], v[108:111], v[80:83]\n\t"
        "ds_read_b128 v[108:111], v183 offset:128\n\t"
        "v_mfma_f32_16x16x32_bf16 v[72:75], a[252:255], v[112:115], v[72:75]\n\t"
        "v_mfma_f32_16x16x32_bf16 v[76:79], v[188:191], v[112:115], v[76:79]\n\t"
        "v_mfma_f32_16x16x32_bf16 v[80:83], v[192:195], v[112:115], v[80:83]\n\t"
        "ds_read_b128 v[112:115], v151 offset:0\n\t"
        "v_mfma_f32_16x16x32_bf16 v[72:75], v[220:223], v[84:87], v[72:75]\n\t"
        "v_mfma_f32_16x16x32_bf16 v[76:79], v[224:227], v[84:87], v[76:79]\n\t"
        "v_mfma_f32_16x16x32_bf16 v[80:83], v[228:231], v[84:87], v[80:83]\n\t"
        "ds_read_b128 v[84:87], v151 offset:64\n\t"
        "s_waitcnt lgkmcnt(4)\n\t"
        "v_mfma_f32_16x16x32_bf16 v[72:75], a[24:27], v[88:91], v[72:75]\n\t"
        "v_mfma_f32_16x16x32_bf16 v[76:79], a[28:31], v[88:91], v[76:79]\n\t"
        "v_mfma_f32_16x16x32_bf16 v[80:83], a[32:35], v[88:91], v[80:83]\n\t"
        "ds_read_b128 v[88:91], v151 offset:128\n\t"
        "v_mfma_f32_16x16x32_bf16 v[80:83], a[68:71], v[92:95], v[80:83]\n\t"
        "v_mfma_f32_16x16x32_bf16 v[80:83], a[104:107], v[96:99], v[80:83]\n\t"
        "v_mfma_f32_16x16x32_bf16 v[80:83], a[140:143], v[100:103], v[80:83]\n\t"
        "s_waitcnt lgkmcnt(3)\n\t"
        "v_mfma_f32_16x16x32_bf16 v[80:83], a[176:179], v[104:107], v[80:83]\n\t"
        "v_mfma_f32_16x16x32_bf16 v[80:83], a[212:215], v[108:111], v[80:83]\n\t"
        "s_waitcnt lgkmcnt(2)\n\t"
        "v_mfma_f32_16x16x32_bf16 v[80:83], a[248:251], v[112:115], v[80:83]\n\t"
        "s_waitcnt lgkmcnt(1)\n\t"
        "v_mfma_f32_16x16x32_bf16 v[80:83], v[216:219], v[84:87], v[80:83]\n\t"
        "s_waitcnt lgkmcnt(0)\n\t"
        "v_mfma_f32_16x16x32_bf16 v[80:83], v[252:255], v[88:91], v[80:83]\n\t"
        "v_mfma_f32_16x16x32_bf16 v[72:75], a[60:63], v[92:95], v[72:75]\n\t"
        "v_mfma_f32_16x16x32_bf16 v[76:79], a[64:67], v[92:95], v[76:79]\n\t"
        "s_nop 5\n\t"
        "v_cvt_pk_bf16_f32 v120, v80, v81\n\t"
        "v_cvt_pk_bf16_f32 v121, v82, v83\n\t"
        "v_mfma_f32_16x16x32_bf16 v[72:75], a[96:99], v[96:99], v[72:75]\n\t"
        "ds_write_b64 v146, v[120:121] offset:2048\n\t"
        "v_mfma_f32_16x16x32_bf16 v[76:79], a[100:103], v[96:99], v[76:79]\n\t"
        "v_mfma_f32_16x16x32_bf16 v[72:75], a[132:135], v[100:103], v[72:75]\n\t"
        "v_mfma_f32_16x16x32_bf16 v[76:79], a[136:139], v[100:103], v[76:79]\n\t"
        "s_waitcnt lgkmcnt(0)\n\t"
        "s_barrier\n\t"
        "v_mfma_f32_16x16x32_bf16 v[72:75], a[168:171], v[104:107], v[72:75]\n\t"
        "ds_read_b128 v[124:127], v147 offset:0\n\t"
        "v_readlane_b32 s55, v155, 51\n\t"
        "v_mfma_f32_16x16x32_bf16 v[76:79], a[172:175], v[104:107], v[76:79]\n\t"
        "v_mfma_f32_16x16x32_bf16 v[72:75], a[204:207], v[108:111], v[72:75]\n\t"
        "v_mfma_f32_16x16x32_bf16 v[76:79], a[208:211], v[108:111], v[76:79]\n\t"
        "v_mfma_f32_16x16x32_bf16 v[72:75], a[240:243], v[112:115], v[72:75]\n\t"
        "v_mfma_f32_16x16x32_bf16 v[76:79], a[244:247], v[112:115], v[76:79]\n\t"
        "v_mfma_f32_16x16x32_bf16 v[72:75], v[208:211], v[84:87], v[72:75]\n\t"
        "v_mfma_f32_16x16x32_bf16 v[76:79], v[212:215], v[84:87], v[76:79]\n\t"
        "v_mfma_f32_16x16x32_bf16 v[72:75], v[244:247], v[88:91], v[72:75]\n\t"
        "v_mfma_f32_16x16x32_bf16 v[76:79], v[248:251], v[88:91], v[76:79]\n\t"
        "s_nop 6\n\t"
        "v_cvt_pk_bf16_f32 v116, v72, v73\n\t"
        "v_cvt_pk_bf16_f32 v117, v74, v75\n\t"
        "v_cvt_pk_bf16_f32 v118, v76, v77\n\t"
        "v_cvt_pk_bf16_f32 v119, v78, v79\n\t"
        "s_nop 0\n\t"
        "v_permlane16_swap_b32 v116, v118\n\t"
        "v_permlane16_swap_b32 v117, v119\n\t"
        "buffer_store_dwordx4 v[116:119], v148, s[28:31], s55 offen sc1\n\t"
        "s_waitcnt lgkmcnt(0)\n\t"
        "buffer_store_dwordx4 v[124:127], v150, s[28:31], s52 offen sc1\n\t"
        "s_endpgm\n\t"
        ".Lx40%=:\n\t"
        "v_readlane_b32 s40, v155, 20\n\t"
        "v_readlane_b32 s43, v155, 8\n\t"
        "s_nop 0\n\t"
        "s_add_u32 m0, s40, s35\n\t"
        "s_nop 2\n\t"
        "buffer_load_dwordx4 v136, s[16:19], s43 offen  lds\n\t"
        "v_readlane_b32 s40, v155, 21\n\t"
        "v_readlane_b32 s43, v155, 9\n\t"
        "s_nop 0\n\t"
        "s_add_u32 m0, s40, s35\n\t"
        "s_nop 2\n\t"
        "buffer_load_dwordx4 v137, s[16:19], s43 offen  lds\n\t"
        "v_readlane_b32 s40, v155, 22\n\t"
        "v_readlane_b32 s43, v155, 10\n\t"
        "s_nop 0\n\t"
        "s_add_u32 m0, s40, s35\n\t"
        "s_nop 2\n\t"
        "buffer_load_dwordx4 v136, s[16:19], s43 offen  lds\n\t"
        "v_readlane_b32 s40, v155, 23\n\t"
        "v_readlane_b32 s43, v155, 11\n\t"
        "s_nop 0\n\t"
        "s_add_u32 m0, s40, s35\n\t"
        "s_nop 2\n\t"
        "buffer_load_dwordx4 v137, s[16:19], s43 offen  lds\n\t"
        "s_branch .Lx41%=\n\t"
        ".Lx42%=:\n\t"
        "v_lshlrev_b32_sdwa v157, 5, v161 dst_sel:DWORD dst_unused:UNUSED_PAD src0_sel:DWORD src1_sel:WORD_0\n\t"
        "v_lshlrev_b32_sdwa v158, 5, v161 dst_sel:DWORD dst_unused:UNUSED_PAD src0_sel:DWORD src1_sel:WORD_1\n\t"
        "v_or_b32 v157, v184, v157\n\t"
        "v_or_b32 v158, v184, v158\n\t"
        "v_xor_b32 v159, 32, v157\n\t"
        "v_xor_b32 v152, 32, v158\n\t"
        "ds_read_b64_tr_b16 v[60:61], v157 offset:0\n\t"
        "ds_read_b64_tr_b16 v[62:63], v158 offset:0\n\t"
        "ds_read_b64_tr_b16 v[64:65], v159 offset:0\n\t"
        "ds_read_b64_tr_b16 v[66:67], v152 offset:0\n\t"
        "ds_read_b64_tr_b16 v[68:69], v157 offset:64\n\t"
        "ds_read_b64_tr_b16 v[70:71], v158 offset:64\n\t"
        "ds_read_b64_tr_b16 v[72:73], v159 offset:64\n\t"
        "ds_read_b64_tr_b16 v[74:75], v152 offset:64\n\t"
        "ds_read_b64_tr_b16 v[76:77], v157 offset:128\n\t"
        "ds_read_b64_tr_b16 v[78:79], v158 offset:128\n\t"
        "ds_read_b64_tr_b16 v[80:81], v159 offset:128\n\t"
        "ds_read_b64_tr_b16 v[82:83], v152 offset:128\n\t"
        "v_perm_b32 v164, v163, v163, v168\n\t"
        "v_perm_b32 v165, v163, v163, v169\n\t"
        "v_perm_b32 v166, v163, v163, v170\n\t"
        "v_perm_b32 v167, v163, v163, v171\n\t"
        "s_waitcnt lgkmcnt(10)\n\t"
        "s_nop 0\n\t"
        "v_mfma_f32_16x16x32_bf16 v[48:51], v[60:63], v[164:167], v[48:51]\n\t"
        "s_waitcnt lgkmcnt(8)\n\t"
        "v_mfma_f32_16x16x32_bf16 v[52:55], v[64:67], v[164:167], v[52:55]\n\t"
        "s_waitcnt lgkmcnt(6)\n\t"
        "v_mfma_f32_16x16x32_bf16 v[56:59], v[68:71], v[164:167], v[56:59]\n\t"
        "s_waitcnt lgkmcnt(4)\n\t"
        "v_mfma_f32_16x16x32_bf16 v[116:119], v[72:75], v[164:167], v[116:119]\n\t"
        "s_waitcnt lgkmcnt(2)\n\t"
        "v_mfma_f32_16x16x32_bf16 v[120:123], v[76:79], v[164:167], v[120:123]\n\t"
        "s_waitcnt lgkmcnt(0)\n\t"
        "v_mfma_f32_16x16x32_bf16 v[124:127], v[80:83], v[164:167], v[124:127]\n\t"
        "s_nop 4\n\t"
        "s_branch .Lx43%=\n\t"
        ".Lx44%=:\n\t"
        "v_lshlrev_b32_sdwa v157, 5, v161 dst_sel:DWORD dst_unused:UNUSED_PAD src0_sel:DWORD src1_sel:WORD_0\n\t"
        "v_lshlrev_b32_sdwa v158, 5, v161 dst_sel:DWORD dst_unused:UNUSED_PAD src0_sel:DWORD src1_sel:WORD_1\n\t"
        "v_or_b32 v157, v184, v157\n\t"
        "v_or_b32 v158, v184, v158\n\t"
        "v_xor_b32 v159, 32, v157\n\t"
        "v_xor_b32 v152, 32, v158\n\t"
        "ds_read_b64_tr_b16 v[60:61], v157 offset:0\n\t"
        "ds_read_b64_tr_b16 v[62:63], v158 offset:0\n\t"
        "ds_read_b64_tr_b16 v[64:65], v159 offset:0\n\t"
        "ds_read_b64_tr_b16 v[66:67], v152 offset:0\n\t"
        "ds_read_b64_tr_b16 v[68:69], v157 offset:64\n\t"
        "ds_read_b64_tr_b16 v[70:71], v158 offset:64\n\t"
        "ds_read_b64_tr_b16 v[72:73], v159 offset:64\n\t"
        "ds_read_b64_tr_b16 v[74:75], v152 offset:64\n\t"
        "ds_read_b64_tr_b16 v[76:77], v157 offset:128\n\t"
        "ds_read_b64_tr_b16 v[78:79], v158 offset:128\n\t"
        "ds_read_b64_tr_b16 v[80:81], v159 offset:128\n\t"
        "ds_read_b64_tr_b16 v[82:83], v152 offset:128\n\t"
        "v_perm_b32 v164, v163, v163, v168\n\t"
        "v_perm_b32 v165, v163, v163, v169\n\t"
        "v_perm_b32 v166, v163, v163, v170\n\t"
        "v_perm_b32 v167, v163, v163, v171\n\t"
        "s_waitcnt lgkmcnt(10)\n\t"
        "s_nop 0\n\t"
        "v_mfma_f32_16x16x32_bf16 v[48:51], v[60:63], v[164:167], v[48:51]\n\t"
        "s_waitcnt lgkmcnt(8)\n\t"
        "v_mfma_f32_16x16x32_bf16 v[52:55], v[64:67], v[164:167], v[52:55]\n\t"
        "s_waitcnt lgkmcnt(6)\n\t"
        "v_mfma_f32_16x16x32_bf16 v[56:59], v[68:71], v[164:167], v[56:59]\n\t"
        "s_waitcnt lgkmcnt(4)\n\t"
        "v_mfma_f32_16x16x32_bf16 v[116:119], v[72:75], v[164:167], v[116:119]\n\t"
        "s_waitcnt lgkmcnt(2)\n\t"
        "v_mfma_f32_16x16x32_bf16 v[120:123], v[76:79], v[164:167], v[120:123]\n\t"
        "s_waitcnt lgkmcnt(0)\n\t"
        "v_mfma_f32_16x16x32_bf16 v[124:127], v[80:83], v[164:167], v[124:127]\n\t"
        "s_nop 4\n\t"
        "s_branch .Lx45%=\n\t"
        ".Lx46%=:\n\t"
        "s_bitcmp1_b32 s8, 0\n\t"
        "s_cbranch_scc1 99f\n\t"
        "v_mov_b32 v151, v185\n\t"
        "v_mov_b32 v183, v186\n\t"
        "s_mov_b32 s8, s3\n\t"
        "s_waitcnt lgkmcnt(0)\n\t"
        "30:\n\t"
        "ds_read_b128 v[160:163], v185\n\t"
        "s_waitcnt lgkmcnt(0)\n\t"
        "v_lshlrev_b32_sdwa v157, 5, v160 dst_sel:DWORD dst_unused:UNUSED_PAD src0_sel:DWORD src1_sel:WORD_0\n\t"
        "v_lshlrev_b32_sdwa v158, 5, v160 dst_sel:DWORD dst_unused:UNUSED_PAD src0_sel:DWORD src1_sel:WORD_1\n\t"
        "v_or_b32 v157, v184, v157\n\t"
        "v_or_b32 v158, v184, v158\n\t"
        "v_xor_b32 v159, 32, v157\n\t"
        "v_xor_b32 v152, 32, v158\n\t"
        "ds_read_b64_tr_b16 v[60:61], v157 offset:0\n\t"
        "ds_read_b64_tr_b16 v[62:63], v158 offset:0\n\t"
        "ds_read_b64_tr_b16 v[64:65], v159 offset:0\n\t"
        "ds_read_b64_tr_b16 v[66:67], v152 offset:0\n\t"
        "ds_read_b64_tr_b16 v[68:69], v157 offset:64\n\t"
        "ds_read_b64_tr_b16 v[70:71], v158 offset:64\n\t"
        "ds_read_b64_tr_b16 v[72:73], v159 offset:64\n\t"
        "ds_read_b64_tr_b16 v[74:75], v152 offset:64\n\t"
        "ds_read_b64_tr_b16 v[76:77], v157 offset:128\n\t"
        "ds_read_b64_tr_b16 v[78:79], v158 offset:128\n\t"
        "ds_read_b64_tr_b16 v[80:81], v159 offset:128\n\t"
        "ds_read_b64_tr_b16 v[82:83], v152 offset:128\n\t"
        "v_perm_b32 v164, v162, v162, v168\n\t"
        "v_perm_b32 v165, v162, v162, v169\n\t"
        "v_perm_b32 v166, v162, v162, v170\n\t"
        "v_perm_b32 v167, v162, v162, v171\n\t"
        "v_cmp_ne_u32 vcc, 0, v163\n\t"
        "s_waitcnt lgkmcnt(10)\n\t"
        "v_mfma_f32_16x16x32_bf16 v[48:51], v[60:63], v[164:167], 0\n\t"
        "s_waitcnt lgkmcnt(8)\n\t"
        "v_mfma_f32_16x16x32_bf16 v[52:55], v[64:67], v[164:167], 0\n\t"
        "s_waitcnt lgkmcnt(6)\n\t"
        "v_mfma_f32_16x16x32_bf16 v[56:59], v[68:71], v[164:167], 0\n\t"
        "s_waitcnt lgkmcnt(4)\n\t"
        "v_mfma_f32_16x16x32_bf16 v[116:119], v[72:75], v[164:167], 0\n\t"
        "s_waitcnt lgkmcnt(2)\n\t"
        "v_mfma_f32_16x16x32_bf16 v[120:123], v[76:79], v[164:167], 0\n\t"
        "s_waitcnt lgkmcnt(0)\n\t"
        "v_mfma_f32_16x16x32_bf16 v[124:127], v[80:83], v[164:167], 0\n\t"
        "s_cbranch_vccz 31f\n\t"
        "v_lshlrev_b32_sdwa v157, 5, v161 dst_sel:DWORD dst_unused:UNUSED_PAD src0_sel:DWORD src1_sel:WORD_0\n\t"
        "v_lshlrev_b32_sdwa v158, 5, v161 dst_sel:DWORD dst_unused:UNUSED_PAD src0_sel:DWORD src1_sel:WORD_1\n\t"
        "v_or_b32 v157, v184, v157\n\t"
        "v_or_b32 v158, v184, v158\n\t"
        "v_xor_b32 v159, 32, v157\n\t"
        "v_xor_b32 v152, 32, v158\n\t"
        "ds_read_b64_tr_b16 v[60:61], v157 offset:0\n\t"
        "ds_read_b64_tr_b16 v[62:63], v158 offset:0\n\t"
        "ds_read_b64_tr_b16 v[64:65], v159 offset:0\n\t"
        "ds_read_b64_tr_b16 v[66:67], v152 offset:0\n\t"
        "ds_read_b64_tr_b16 v[68:69], v157 offset:64\n\t"
        "ds_read_b64_tr_b16 v[70:71], v158 offset:64\n\t"
        "ds_read_b64_tr_b16 v[72:73], v159 offset:64\n\t"
        "ds_read_b64_tr_b16 v[74:75], v152 offset:64\n\t"
        "ds_read_b64_tr_b16 v[76:77], v157 offset:128\n\t"
        "ds_read_b64_tr_b16 v[78:79], v158 offset:128\n\t"
        "ds_read_b64_tr_b16 v[80:81], v159 offset:128\n\t"
        "ds_read_b64_tr_b16 v[82:83], v152 offset:128\n\t"
        "v_perm_b32 v164, v163, v163, v168\n\t"
        "v_perm_b32 v165, v163, v163, v169\n\t"
        "v_perm_b32 v166, v163, v163, v170\n\t"
        "v_perm_b32 v167, v163, v163, v171\n\t"
        "s_waitcnt lgkmcnt(10)\n\t"
        "s_nop 0\n\t"
        "v_mfma_f32_16x16x32_bf16 v[48:51], v[60:63], v[164:167], v[48:51]\n\t"
        "s_waitcnt lgkmcnt(8)\n\t"
        "v_mfma_f32_16x16x32_bf16 v[52:55], v[64:67], v[164:167], v[52:55]\n\t"
        "s_waitcnt lgkmcnt(6)\n\t"
        "v_mfma_f32_16x16x32_bf16 v[56:59], v[68:71], v[164:167], v[56:59]\n\t"
        "s_waitcnt lgkmcnt(4)\n\t"
        "v_mfma_f32_16x16x32_bf16 v[116:119], v[72:75], v[164:167], v[116:119]\n\t"
        "s_waitcnt lgkmcnt(2)\n\t"
        "v_mfma_f32_16x16x32_bf16 v[120:123], v[76:79], v[164:167], v[120:123]\n\t"
        "s_waitcnt lgkmcnt(0)\n\t"
        "v_mfma_f32_16x16x32_bf16 v[124:127], v[80:83], v[164:167], v[124:127]\n\t"
        "31:\n\t"
        "v_cvt_pk_bf16_f32 v48, v48, v49\n\t"
        "v_cvt_pk_bf16_f32 v49, v50, v51\n\t"
        "v_cvt_pk_bf16_f32 v52, v52, v53\n\t"
        "v_cvt_pk_bf16_f32 v53, v54, v55\n\t"
        "v_cvt_pk_bf16_f32 v56, v56, v57\n\t"
        "v_cvt_pk_bf16_f32 v57, v58, v59\n\t"
        "v_cvt_pk_bf16_f32 v116, v116, v117\n\t"
        "v_cvt_pk_bf16_f32 v117, v118, v119\n\t"
        "v_cvt_pk_bf16_f32 v120, v120, v121\n\t"
        "v_cvt_pk_bf16_f32 v121, v122, v123\n\t"
        "v_cvt_pk_bf16_f32 v124, v124, v125\n\t"
        "v_cvt_pk_bf16_f32 v125, v126, v127\n\t"
        "ds_write_b64 v186, v[48:49] offset:0\n\t"
        "ds_write_b64 v154, v[52:53] offset:0\n\t"
        "ds_write_b64 v186, v[56:57] offset:64\n\t"
        "ds_write_b64 v154, v[116:117] offset:64\n\t"
        "ds_write_b64 v186, v[120:121] offset:128\n\t"
        "ds_write_b64 v154, v[124:125] offset:128\n\t"
        "v_add_u32 v185, 256, v185\n\t"
        "v_add_u32 v186, 12288, v186\n\t"
        "v_xor_b32 v154, 32, v186\n\t"
        "s_sub_i32 s8, s8, 16\n\t"
        "s_cmp_gt_i32 s8, 0\n\t"
        "s_cbranch_scc1 30b\n\t"
        "v_mov_b32 v185, v151\n\t"
        "v_mov_b32 v186, v183\n\t"
        "v_xor_b32 v154, 32, v186\n\t"
        "s_waitcnt lgkmcnt(0)\n\t"
        "s_barrier\n\t"
        "s_branch .Lx47%=\n\t"
        ".Lx48%=:\n\t"
        "v_lshlrev_b32_sdwa v157, 5, v161 dst_sel:DWORD dst_unused:UNUSED_PAD src0_sel:DWORD src1_sel:WORD_0\n\t"
        "v_lshlrev_b32_sdwa v158, 5, v161 dst_sel:DWORD dst_unused:UNUSED_PAD src0_sel:DWORD src1_sel:WORD_1\n\t"
        "v_or_b32 v157, v184, v157\n\t"
        "v_or_b32 v158, v184, v158\n\t"
        "v_xor_b32 v159, 32, v157\n\t"
        "v_xor_b32 v152, 32, v158\n\t"
        "ds_read_b64_tr_b16 v[60:61], v157 offset:0\n\t"
        "ds_read_b64_tr_b16 v[62:63], v158 offset:0\n\t"
        "ds_read_b64_tr_b16 v[64:65], v159 offset:0\n\t"
        "ds_read_b64_tr_b16 v[66:67], v152 offset:0\n\t"
        "ds_read_b64_tr_b16 v[68:69], v157 offset:64\n\t"
        "ds_read_b64_tr_b16 v[70:71], v158 offset:64\n\t"
        "ds_read_b64_tr_b16 v[72:73], v159 offset:64\n\t"
        "ds_read_b64_tr_b16 v[74:75], v152 offset:64\n\t"
        "ds_read_b64_tr_b16 v[76:77], v157 offset:128\n\t"
        "ds_read_b64_tr_b16 v[78:79], v158 offset:128\n\t"
        "ds_read_b64_tr_b16 v[80:81], v159 offset:128\n\t"
        "ds_read_b64_tr_b16 v[82:83], v152 offset:128\n\t"
        "v_perm_b32 v164, v163, v163, v168\n\t"
        "v_perm_b32 v165, v163, v163, v169\n\t"
        "v_perm_b32 v166, v163, v163, v170\n\t"
        "v_perm_b32 v167, v163, v163, v171\n\t"
        "s_waitcnt lgkmcnt(10)\n\t"
        "s_nop 0\n\t"
        "v_mfma_f32_16x16x32_bf16 v[0:3], v[60:63], v[164:167], v[0:3]\n\t"
        "s_waitcnt lgkmcnt(8)\n\t"
        "v_mfma_f32_16x16x32_bf16 v[4:7], v[64:67], v[164:167], v[4:7]\n\t"
        "s_waitcnt lgkmcnt(6)\n\t"
        "v_mfma_f32_16x16x32_bf16 v[8:11], v[68:71], v[164:167], v[8:11]\n\t"
        "s_waitcnt lgkmcnt(4)\n\t"
        "v_mfma_f32_16x16x32_bf16 v[12:15], v[72:75], v[164:167], v[12:15]\n\t"
        "s_waitcnt lgkmcnt(2)\n\t"
        "v_mfma_f32_16x16x32_bf16 v[16:19], v[76:79], v[164:167], v[16:19]\n\t"
        "s_waitcnt lgkmcnt(0)\n\t"
        "v_mfma_f32_16x16x32_bf16 v[20:23], v[80:83], v[164:167], v[20:23]\n\t"
        "s_nop 4\n\t"
        "s_branch .Lx49%=\n\t"
        ".Lx50%=:\n\t"
        "v_add_u32 v185, 768, v185\n\t"
        "v_add_u32 v186, 36864, v186\n\t"
        "v_xor_b32 v154, 32, v186\n\t"
        "s_min_u32 s47, s61, 64\n\t"
        "s_sub_u32 s47, s47, 48\n\t"
        "s_waitcnt lgkmcnt(0)\n\t"
        "40:\n\t"
        "ds_read_b128 v[160:163], v185\n\t"
        "s_waitcnt lgkmcnt(0)\n\t"
        "v_lshlrev_b32_sdwa v157, 5, v160 dst_sel:DWORD dst_unused:UNUSED_PAD src0_sel:DWORD src1_sel:WORD_0\n\t"
        "v_lshlrev_b32_sdwa v158, 5, v160 dst_sel:DWORD dst_unused:UNUSED_PAD src0_sel:DWORD src1_sel:WORD_1\n\t"
        "v_or_b32 v157, v184, v157\n\t"
        "v_or_b32 v158, v184, v158\n\t"
        "v_xor_b32 v159, 32, v157\n\t"
        "v_xor_b32 v152, 32, v158\n\t"
        "ds_read_b64_tr_b16 v[60:61], v157 offset:0\n\t"
        "ds_read_b64_tr_b16 v[62:63], v158 offset:0\n\t"
        "ds_read_b64_tr_b16 v[64:65], v159 offset:0\n\t"
        "ds_read_b64_tr_b16 v[66:67], v152 offset:0\n\t"
        "ds_read_b64_tr_b16 v[68:69], v157 offset:64\n\t"
        "ds_read_b64_tr_b16 v[70:71], v158 offset:64\n\t"
        "ds_read_b64_tr_b16 v[72:73], v159 offset:64\n\t"
        "ds_read_b64_tr_b16 v[74:75], v152 offset:64\n\t"
        "ds_read_b64_tr_b16 v[76:77], v157 offset:128\n\t"
        "ds_read_b64_tr_b16 v[78:79], v158 offset:128\n\t"
        "ds_read_b64_tr_b16 v[80:81], v159 offset:128\n\t"
        "ds_read_b64_tr_b16 v[82:83], v152 offset:128\n\t"
        "v_perm_b32 v164, v162, v162, v168\n\t"
        "v_perm_b32 v165, v162, v162, v169\n\t"
        "v_perm_b32 v166, v162, v162, v170\n\t"
        "v_perm_b32 v167, v162, v162, v171\n\t"
        "v_cmp_ne_u32 vcc, 0, v163\n\t"
        "s_waitcnt lgkmcnt(10)\n\t"
        "v_mfma_f32_16x16x32_bf16 v[0:3], v[60:63], v[164:167], 0\n\t"
        "s_waitcnt lgkmcnt(8)\n\t"
        "v_mfma_f32_16x16x32_bf16 v[4:7], v[64:67], v[164:167], 0\n\t"
        "s_waitcnt lgkmcnt(6)\n\t"
        "v_mfma_f32_16x16x32_bf16 v[8:11], v[68:71], v[164:167], 0\n\t"
        "s_waitcnt lgkmcnt(4)\n\t"
        "v_mfma_f32_16x16x32_bf16 v[12:15], v[72:75], v[164:167], 0\n\t"
        "s_waitcnt lgkmcnt(2)\n\t"
        "v_mfma_f32_16x16x32_bf16 v[16:19], v[76:79], v[164:167], 0\n\t"
        "s_waitcnt lgkmcnt(0)\n\t"
        "v_mfma_f32_16x16x32_bf16 v[20:23], v[80:83], v[164:167], 0\n\t"
        "s_cbranch_vccz 41f\n\t"
        "v_lshlrev_b32_sdwa v157, 5, v161 dst_sel:DWORD dst_unused:UNUSED_PAD src0_sel:DWORD src1_sel:WORD_0\n\t"
        "v_lshlrev_b32_sdwa v158, 5, v161 dst_sel:DWORD dst_unused:UNUSED_PAD src0_sel:DWORD src1_sel:WORD_1\n\t"
        "v_or_b32 v157, v184, v157\n\t"
        "v_or_b32 v158, v184, v158\n\t"
        "v_xor_b32 v159, 32, v157\n\t"
        "v_xor_b32 v152, 32, v158\n\t"
        "ds_read_b64_tr_b16 v[60:61], v157 offset:0\n\t"
        "ds_read_b64_tr_b16 v[62:63], v158 offset:0\n\t"
        "ds_read_b64_tr_b16 v[64:65], v159 offset:0\n\t"
        "ds_read_b64_tr_b16 v[66:67], v152 offset:0\n\t"
        "ds_read_b64_tr_b16 v[68:69], v157 offset:64\n\t"
        "ds_read_b64_tr_b16 v[70:71], v158 offset:64\n\t"
        "ds_read_b64_tr_b16 v[72:73], v159 offset:64\n\t"
        "ds_read_b64_tr_b16 v[74:75], v152 offset:64\n\t"
        "ds_read_b64_tr_b16 v[76:77], v157 offset:128\n\t"
        "ds_read_b64_tr_b16 v[78:79], v158 offset:128\n\t"
        "ds_read_b64_tr_b16 v[80:81], v159 offset:128\n\t"
        "ds_read_b64_tr_b16 v[82:83], v152 offset:128\n\t"
        "v_perm_b32 v164, v163, v163, v168\n\t"
        "v_perm_b32 v165, v163, v163, v169\n\t"
        "v_perm_b32 v166, v163, v163, v170\n\t"
        "v_perm_b32 v167, v163, v163, v171\n\t"
        "s_waitcnt lgkmcnt(10)\n\t"
        "s_nop 0\n\t"
        "v_mfma_f32_16x16x32_bf16 v[0:3], v[60:63], v[164:167], v[0:3]\n\t"
        "s_waitcnt lgkmcnt(8)\n\t"
        "v_mfma_f32_16x16x32_bf16 v[4:7], v[64:67], v[164:167], v[4:7]\n\t"
        "s_waitcnt lgkmcnt(6)\n\t"
        "v_mfma_f32_16x16x32_bf16 v[8:11], v[68:71], v[164:167], v[8:11]\n\t"
        "s_waitcnt lgkmcnt(4)\n\t"
        "v_mfma_f32_16x16x32_bf16 v[12:15], v[72:75], v[164:167], v[12:15]\n\t"
        "s_waitcnt lgkmcnt(2)\n\t"
        "v_mfma_f32_16x16x32_bf16 v[16:19], v[76:79], v[164:167], v[16:19]\n\t"
        "s_waitcnt lgkmcnt(0)\n\t"
        "v_mfma_f32_16x16x32_bf16 v[20:23], v[80:83], v[164:167], v[20:23]\n\t"
        "41:\n\t"
        "v_cvt_pk_bf16_f32 v0, v0, v1\n\t"
        "v_cvt_pk_bf16_f32 v1, v2, v3\n\t"
        "v_cvt_pk_bf16_f32 v4, v4, v5\n\t"
        "v_cvt_pk_bf16_f32 v5, v6, v7\n\t"
        "v_cvt_pk_bf16_f32 v8, v8, v9\n\t"
        "v_cvt_pk_bf16_f32 v9, v10, v11\n\t"
        "v_cvt_pk_bf16_f32 v12, v12, v13\n\t"
        "v_cvt_pk_bf16_f32 v13, v14, v15\n\t"
        "v_cvt_pk_bf16_f32 v16, v16, v17\n\t"
        "v_cvt_pk_bf16_f32 v17, v18, v19\n\t"
        "v_cvt_pk_bf16_f32 v20, v20, v21\n\t"
        "v_cvt_pk_bf16_f32 v21, v22, v23\n\t"
        "ds_write_b64 v186, v[0:1] offset:0\n\t"
        "ds_write_b64 v154, v[4:5] offset:0\n\t"
        "ds_write_b64 v186, v[8:9] offset:64\n\t"
        "ds_write_b64 v154, v[12:13] offset:64\n\t"
        "ds_write_b64 v186, v[16:17] offset:128\n\t"
        "ds_write_b64 v154, v[20:21] offset:128\n\t"
        "v_add_u32 v185, 256, v185\n\t"
        "v_add_u32 v186, 12288, v186\n\t"
        "v_xor_b32 v154, 32, v186\n\t"
        "s_sub_i32 s47, s47, 16\n\t"
        "s_cmp_gt_i32 s47, 0\n\t"
        "s_cbranch_scc1 40b\n\t"
        "s_cmp_le_u32 s61, 64\n\t"
        "s_cbranch_scc1 52f\n\t"
        "v_and_b32 v64, 63, v156\n\t"
        "v_add_u32 v60, 65, v64\n\t"
        "s_mul_i32 s8, s4, 3072\n\t"
        "s_add_u32 s8, s8, 73728\n\t"
        "v_mov_b32 v63, s62\n\t"
        "v_lshrrev_b32 v65, 4, v64\n\t"
        "v_mul_u32_u24 v65, 9216, v65\n\t"
        "v_lshlrev_b32 v66, 3, v64\n\t"
        "v_and_or_b32 v65, v66, 48, v65\n\t"
        "v_mul_u32_u24 v66, 192, v64\n\t"
        "v_add3_u32 v61, v65, v66, s8\n\t"
        "s_add_u32 s8, s35, 157696\n\t"
        "v_lshl_add_u32 v62, v64, 4, s8\n\t"
        "v_subrev_u32 v185, 1024, v185\n\t"
        "v_subrev_u32 v186, 61440, v186\n\t"
        "v_xor_b32 v154, 32, v186\n\t"
        "v_min_u32 v16, s61, v60\n\t"
        "v_lshl_add_u32 v16, v16, 3, s60\n\t"
        "ds_read_b64 v[0:1], v16\n\t"
        "v_cmp_ge_u32 s[66:67], s61, v60\n\t"
        "s_mov_b32 s46, 0x07060302\n\t"
        "s_waitcnt lgkmcnt(0)\n\t"
        "v_and_b32 v16, 0xfffff800, v1\n\t"
        "ds_read_b128 v[2:5], v16 offset:1536\n\t"
        "v_lshlrev_b32 v10, 16, v0\n\t"
        "v_and_b32 v11, 0xffff0000, v0\n\t"
        "v_med3_f32 v10, v10, 0, s63\n\t"
        "v_med3_f32 v11, v11, 0, s63\n\t"
        "s_mov_b64 exec, s[66:67]\n\t"
        "ds_write_b32 v1, v61\n\t"
        "s_mov_b64 exec, -1\n\t"
        "v_perm_b32 v0, v11, v10, s46\n\t"
        "v_fract_f32 v12, v10\n\t"
        "v_fract_f32 v13, v11\n\t"
        "v_cvt_u32_f32 v14, v10\n\t"
        "v_cvt_u32_f32 v15, v11\n\t"
        "v_cmp_neq_f32 s[64:65], 0, v12\n\t"
        "v_cmp_neq_f32 s[44:45], 0, v13\n\t"
        "v_sub_f32 v16, 1.0, v12\n\t"
        "v_sub_f32 v17, 1.0, v13\n\t"
        "v_cvt_pk_bf16_f32 v17, v16, v17\n\t"
        "v_lshlrev_b32 v16, 16, v17\n\t"
        "v_and_b32 v17, 0xffff0000, v17\n\t"
        "v_mul_f32 v18, v16, v17\n\t"
        "v_mul_f32 v16, v16, v13\n\t"
        "v_mul_f32 v17, v12, v17\n\t"
        "v_mul_f32 v19, v12, v13\n\t"
        "v_cndmask_b32 v16, v17, v16, s[44:45]\n\t"
        "v_cndmask_b32 v17, 0, v17, s[44:45]\n\t"
        "v_cvt_pk_bf16_f32 v8, v18, v16\n\t"
        "v_cvt_pk_bf16_f32 v9, v17, v19\n\t"
        "s_waitcnt lgkmcnt(1)\n\t"
        "v_pk_max_u16 v16, v0, v2\n\t"
        "v_pk_min_u16 v16, v16, v3\n\t"
        "v_cmp_ne_u32 vcc, v16, v0\n\t"
        "v_sub_u32 v16, v15, v5\n\t"
        "v_mad_u32_u24 v17, v15, 6, v4\n\t"
        "v_mad_u32_u24 v17, v14, s62, v17\n\t"
        "s_and_b64 vcc, vcc, s[66:67]\n\t"
        "s_or_b64 s[56:57], s[56:57], vcc\n\t"
        "v_bfe_u32 v18, v16, 2, 1\n\t"
        "v_or_b32 v18, v18, v17\n\t"
        "v_add_u32 v16, 1, v16\n\t"
        "v_bfe_u32 v16, v16, 2, 1\n\t"
        "v_add3_u32 v16, v17, v16, 6\n\t"
        "v_cndmask_b32 v16, v18, v16, s[44:45]\n\t"
        "v_cndmask_b32 v17, 0, v63, s[64:65]\n\t"
        "v_add_u32 v19, v18, v17\n\t"
        "v_add_u32 v20, v16, v17\n\t"
        "v_cndmask_b32 v16, v19, v16, s[44:45]\n\t"
        "v_lshl_or_b32 v6, v16, 16, v18\n\t"
        "v_lshl_or_b32 v7, v20, 16, v19\n\t"
        "ds_write_b128 v62, v[6:9]\n\t"
        "s_or_b32 s8, s56, s57\n\t"
        "v_mov_b32 v60, s8\n\t"
        "ds_write_b32 v153, v60 offset:16\n\t"
        "s_sub_u32 s47, s61, 64\n\t"
        "s_waitcnt lgkmcnt(0)\n\t"
        "50:\n\t"
        "ds_read_b128 v[160:163], v185\n\t"
        "s_waitcnt lgkmcnt(0)\n\t"
        "v_lshlrev_b32_sdwa v157, 5, v160 dst_sel:DWORD dst_unused:UNUSED_PAD src0_sel:DWORD src1_sel:WORD_0\n\t"
        "v_lshlrev_b32_sdwa v158, 5, v160 dst_sel:DWORD dst_unused:UNUSED_PAD src0_sel:DWORD src1_sel:WORD_1\n\t"
        "v_or_b32 v157, v184, v157\n\t"
        "v_or_b32 v158, v184, v158\n\t"
        "v_xor_b32 v159, 32, v157\n\t"
        "v_xor_b32 v152, 32, v158\n\t"
        "ds_read_b64_tr_b16 v[60:61], v157 offset:0\n\t"
        "ds_read_b64_tr_b16 v[62:63], v158 offset:0\n\t"
        "ds_read_b64_tr_b16 v[64:65], v159 offset:0\n\t"
        "ds_read_b64_tr_b16 v[66:67], v152 offset:0\n\t"
        "ds_read_b64_tr_b16 v[68:69], v157 offset:64\n\t"
        "ds_read_b64_tr_b16 v[70:71], v158 offset:64\n\t"
        "ds_read_b64_tr_b16 v[72:73], v159 offset:64\n\t"
        "ds_read_b64_tr_b16 v[74:75], v152 offset:64\n\t"
        "ds_read_b64_tr_b16 v[76:77], v157 offset:128\n\t"
        "ds_read_b64_tr_b16 v[78:79], v158 offset:128\n\t"
        "ds_read_b64_tr_b16 v[80:81], v159 offset:128\n\t"
        "ds_read_b64_tr_b16 v[82:83], v152 offset:128\n\t"
        "v_perm_b32 v164, v162, v162, v168\n\t"
        "v_perm_b32 v165, v162, v162, v169\n\t"
        "v_perm_b32 v166, v162, v162, v170\n\t"
        "v_perm_b32 v167, v162, v162, v171\n\t"
        "v_cmp_ne_u32 vcc, 0, v163\n\t"
        "s_waitcnt lgkmcnt(10)\n\t"
        "v_mfma_f32_16x16x32_bf16 v[0:3], v[60:63], v[164:167], 0\n\t"
        "s_waitcnt lgkmcnt(8)\n\t"
        "v_mfma_f32_16x16x32_bf16 v[4:7], v[64:67], v[164:167], 0\n\t"
        "s_waitcnt lgkmcnt(6)\n\t"
        "v_mfma_f32_16x16x32_bf16 v[8:11], v[68:71], v[164:167], 0\n\t"
        "s_waitcnt lgkmcnt(4)\n\t"
        "v_mfma_f32_16x16x32_bf16 v[12:15], v[72:75], v[164:167], 0\n\t"
        "s_waitcnt lgkmcnt(2)\n\t"
        "v_mfma_f32_16x16x32_bf16 v[16:19], v[76:79], v[164:167], 0\n\t"
        "s_waitcnt lgkmcnt(0)\n\t"
        "v_mfma_f32_16x16x32_bf16 v[20:23], v[80:83], v[164:167], 0\n\t"
        "s_cbranch_vccz 51f\n\t"
        "v_lshlrev_b32_sdwa v157, 5, v161 dst_sel:DWORD dst_unused:UNUSED_PAD src0_sel:DWORD src1_sel:WORD_0\n\t"
        "v_lshlrev_b32_sdwa v158, 5, v161 dst_sel:DWORD dst_unused:UNUSED_PAD src0_sel:DWORD src1_sel:WORD_1\n\t"
        "v_or_b32 v157, v184, v157\n\t"
        "v_or_b32 v158, v184, v158\n\t"
        "v_xor_b32 v159, 32, v157\n\t"
        "v_xor_b32 v152, 32, v158\n\t"
        "ds_read_b64_tr_b16 v[60:61], v157 offset:0\n\t"
        "ds_read_b64_tr_b16 v[62:63], v158 offset:0\n\t"
        "ds_read_b64_tr_b16 v[64:65], v159 offset:0\n\t"
        "ds_read_b64_tr_b16 v[66:67], v152 offset:0\n\t"
        "ds_read_b64_tr_b16 v[68:69], v157 offset:64\n\t"
        "ds_read_b64_tr_b16 v[70:71], v158 offset:64\n\t"
        "ds_read_b64_tr_b16 v[72:73], v159 offset:64\n\t"
        "ds_read_b64_tr_b16 v[74:75], v152 offset:64\n\t"
        "ds_read_b64_tr_b16 v[76:77], v157 offset:128\n\t"
        "ds_read_b64_tr_b16 v[78:79], v158 offset:128\n\t"
        "ds_read_b64_tr_b16 v[80:81], v159 offset:128\n\t"
        "ds_read_b64_tr_b16 v[82:83], v152 offset:128\n\t"
        "v_perm_b32 v164, v163, v163, v168\n\t"
        "v_perm_b32 v165, v163, v163, v169\n\t"
        "v_perm_b32 v166, v163, v163, v170\n\t"
        "v_perm_b32 v167, v163, v163, v171\n\t"
        "s_waitcnt lgkmcnt(10)\n\t"
        "s_nop 0\n\t"
        "v_mfma_f32_16x16x32_bf16 v[0:3], v[60:63], v[164:167], v[0:3]\n\t"
        "s_waitcnt lgkmcnt(8)\n\t"
        "v_mfma_f32_16x16x32_bf16 v[4:7], v[64:67], v[164:167], v[4:7]\n\t"
        "s_waitcnt lgkmcnt(6)\n\t"
        "v_mfma_f32_16x16x32_bf16 v[8:11], v[68:71], v[164:167], v[8:11]\n\t"
        "s_waitcnt lgkmcnt(4)\n\t"
        "v_mfma_f32_16x16x32_bf16 v[12:15], v[72:75], v[164:167], v[12:15]\n\t"
        "s_waitcnt lgkmcnt(2)\n\t"
        "v_mfma_f32_16x16x32_bf16 v[16:19], v[76:79], v[164:167], v[16:19]\n\t"
        "s_waitcnt lgkmcnt(0)\n\t"
        "v_mfma_f32_16x16x32_bf16 v[20:23], v[80:83], v[164:167], v[20:23]\n\t"
        "51:\n\t"
        "v_cvt_pk_bf16_f32 v0, v0, v1\n\t"
        "v_cvt_pk_bf16_f32 v1, v2, v3\n\t"
        "v_cvt_pk_bf16_f32 v4, v4, v5\n\t"
        "v_cvt_pk_bf16_f32 v5, v6, v7\n\t"
        "v_cvt_pk_bf16_f32 v8, v8, v9\n\t"
        "v_cvt_pk_bf16_f32 v9, v10, v11\n\t"
        "v_cvt_pk_bf16_f32 v12, v12, v13\n\t"
        "v_cvt_pk_bf16_f32 v13, v14, v15\n\t"
        "v_cvt_pk_bf16_f32 v16, v16, v17\n\t"
        "v_cvt_pk_bf16_f32 v17, v18, v19\n\t"
        "v_cvt_pk_bf16_f32 v20, v20, v21\n\t"
        "v_cvt_pk_bf16_f32 v21, v22, v23\n\t"
        "ds_write_b64 v186, v[0:1] offset:0\n\t"
        "ds_write_b64 v154, v[4:5] offset:0\n\t"
        "ds_write_b64 v186, v[8:9] offset:64\n\t"
        "ds_write_b64 v154, v[12:13] offset:64\n\t"
        "ds_write_b64 v186, v[16:17] offset:128\n\t"
        "ds_write_b64 v154, v[20:21] offset:128\n\t"
        "v_add_u32 v185, 256, v185\n\t"
        "v_add_u32 v186, 12288, v186\n\t"
        "v_xor_b32 v154, 32, v186\n\t"
        "s_sub_i32 s47, s47, 16\n\t"
        "s_cmp_gt_i32 s47, 0\n\t"
        "s_cbranch_scc1 50b\n\t"
        "52:\n\t"
        "s_branch .Lx51%=\n\t"
        ".Lx52%=:\n\t"
        "v_readlane_b32 s40, v155, 20\n\t"
        "v_readlane_b32 s43, v155, 8\n\t"
        "s_nop 0\n\t"
        "s_add_u32 m0, s40, s35\n\t"
        "s_nop 2\n\t"
        "buffer_load_dwordx4 v136, s[16:19], s43 offen  lds\n\t"
        "v_readlane_b32 s40, v155, 21\n\t"
        "v_readlane_b32 s43, v155, 9\n\t"
        "s_nop 0\n\t"
        "s_add_u32 m0, s40, s35\n\t"
        "s_nop 2\n\t"
        "buffer_load_dwordx4 v137, s[16:19], s43 offen  lds\n\t"
        "v_readlane_b32 s40, v155, 22\n\t"
        "v_readlane_b32 s43, v155, 10\n\t"
        "s_nop 0\n\t"
        "s_add_u32 m0, s40, s35\n\t"
        "s_nop 2\n\t"
        "buffer_load_dwordx4 v136, s[16:19], s43 offen  lds\n\t"
        "v_readlane_b32 s40, v155, 23\n\t"
        "v_readlane_b32 s43, v155, 11\n\t"
        "s_nop 0\n\t"
        "s_add_u32 m0, s40, s35\n\t"
        "s_nop 2\n\t"
        "buffer_load_dwordx4 v137, s[16:19], s43 offen  lds\n\t"
        "s_branch .Lx53%=\n\t"
        ".Lx54%=:\n\t"
        "v_lshlrev_b32_sdwa v157, 5, v161 dst_sel:DWORD dst_unused:UNUSED_PAD src0_sel:DWORD src1_sel:WORD_0\n\t"
        "v_lshlrev_b32_sdwa v158, 5, v161 dst_sel:DWORD dst_unused:UNUSED_PAD src0_sel:DWORD src1_sel:WORD_1\n\t"
        "v_or_b32 v157, v184, v157\n\t"
        "v_or_b32 v158, v184, v158\n\t"
        "v_xor_b32 v159, 32, v157\n\t"
        "v_xor_b32 v152, 32, v158\n\t"
        "ds_read_b64_tr_b16 v[60:61], v157 offset:0\n\t"
        "ds_read_b64_tr_b16 v[62:63], v158 offset:0\n\t"
        "ds_read_b64_tr_b16 v[64:65], v159 offset:0\n\t"
        "ds_read_b64_tr_b16 v[66:67], v152 offset:0\n\t"
        "ds_read_b64_tr_b16 v[68:69], v157 offset:64\n\t"
        "ds_read_b64_tr_b16 v[70:71], v158 offset:64\n\t"
        "ds_read_b64_tr_b16 v[72:73], v159 offset:64\n\t"
        "ds_read_b64_tr_b16 v[74:75], v152 offset:64\n\t"
        "ds_read_b64_tr_b16 v[76:77], v157 offset:128\n\t"
        "ds_read_b64_tr_b16 v[78:79], v158 offset:128\n\t"
        "ds_read_b64_tr_b16 v[80:81], v159 offset:128\n\t"
        "ds_read_b64_tr_b16 v[82:83], v152 offset:128\n\t"
        "v_perm_b32 v164, v163, v163, v168\n\t"
        "v_perm_b32 v165, v163, v163, v169\n\t"
        "v_perm_b32 v166, v163, v163, v170\n\t"
        "v_perm_b32 v167, v163, v163, v171\n\t"
        "s_waitcnt lgkmcnt(10)\n\t"
        "s_nop 0\n\t"
        "v_mfma_f32_16x16x32_bf16 v[48:51], v[60:63], v[164:167], v[48:51]\n\t"
        "s_waitcnt lgkmcnt(8)\n\t"
        "v_mfma_f32_16x16x32_bf16 v[52:55], v[64:67], v[164:167], v[52:55]\n\t"
        "s_waitcnt lgkmcnt(6)\n\t"
        "v_mfma_f32_16x16x32_bf16 v[56:59], v[68:71], v[164:167], v[56:59]\n\t"
        "s_waitcnt lgkmcnt(4)\n\t"
        "v_mfma_f32_16x16x32_bf16 v[116:119], v[72:75], v[164:167], v[116:119]\n\t"
        "s_waitcnt lgkmcnt(2)\n\t"
        "v_mfma_f32_16x16x32_bf16 v[120:123], v[76:79], v[164:167], v[120:123]\n\t"
        "s_waitcnt lgkmcnt(0)\n\t"
        "v_mfma_f32_16x16x32_bf16 v[124:127], v[80:83], v[164:167], v[124:127]\n\t"
        "s_nop 4\n\t"
        "s_branch .Lx55%=\n\t"
        ".Lx56%=:\n\t"
        "s_bitcmp1_b32 s8, 0\n\t"
        "s_cbranch_scc1 99f\n\t"
        "v_mov_b32 v151, v185\n\t"
        "v_mov_b32 v183, v186\n\t"
        "s_mov_b32 s8, s3\n\t"
        "s_waitcnt lgkmcnt(0)\n\t"
        "30:\n\t"
        "ds_read_b128 v[160:163], v185\n\t"
        "s_waitcnt lgkmcnt(0)\n\t"
        "v_lshlrev_b32_sdwa v157, 5, v160 dst_sel:DWORD dst_unused:UNUSED_PAD src0_sel:DWORD src1_sel:WORD_0\n\t"
        "v_lshlrev_b32_sdwa v158, 5, v160 dst_sel:DWORD dst_unused:UNUSED_PAD src0_sel:DWORD src1_sel:WORD_1\n\t"
        "v_or_b32 v157, v184, v157\n\t"
        "v_or_b32 v158, v184, v158\n\t"
        "v_xor_b32 v159, 32, v157\n\t"
        "v_xor_b32 v152, 32, v158\n\t"
        "ds_read_b64_tr_b16 v[60:61], v157 offset:0\n\t"
        "ds_read_b64_tr_b16 v[62:63], v158 offset:0\n\t"
        "ds_read_b64_tr_b16 v[64:65], v159 offset:0\n\t"
        "ds_read_b64_tr_b16 v[66:67], v152 offset:0\n\t"
        "ds_read_b64_tr_b16 v[68:69], v157 offset:64\n\t"
        "ds_read_b64_tr_b16 v[70:71], v158 offset:64\n\t"
        "ds_read_b64_tr_b16 v[72:73], v159 offset:64\n\t"
        "ds_read_b64_tr_b16 v[74:75], v152 offset:64\n\t"
        "ds_read_b64_tr_b16 v[76:77], v157 offset:128\n\t"
        "ds_read_b64_tr_b16 v[78:79], v158 offset:128\n\t"
        "ds_read_b64_tr_b16 v[80:81], v159 offset:128\n\t"
        "ds_read_b64_tr_b16 v[82:83], v152 offset:128\n\t"
        "v_perm_b32 v164, v162, v162, v168\n\t"
        "v_perm_b32 v165, v162, v162, v169\n\t"
        "v_perm_b32 v166, v162, v162, v170\n\t"
        "v_perm_b32 v167, v162, v162, v171\n\t"
        "v_cmp_ne_u32 vcc, 0, v163\n\t"
        "s_waitcnt lgkmcnt(10)\n\t"
        "v_mfma_f32_16x16x32_bf16 v[48:51], v[60:63], v[164:167], 0\n\t"
        "s_waitcnt lgkmcnt(8)\n\t"
        "v_mfma_f32_16x16x32_bf16 v[52:55], v[64:67], v[164:167], 0\n\t"
        "s_waitcnt lgkmcnt(6)\n\t"
        "v_mfma_f32_16x16x32_bf16 v[56:59], v[68:71], v[164:167], 0\n\t"
        "s_waitcnt lgkmcnt(4)\n\t"
        "v_mfma_f32_16x16x32_bf16 v[116:119], v[72:75], v[164:167], 0\n\t"
        "s_waitcnt lgkmcnt(2)\n\t"
        "v_mfma_f32_16x16x32_bf16 v[120:123], v[76:79], v[164:167], 0\n\t"
        "s_waitcnt lgkmcnt(0)\n\t"
        "v_mfma_f32_16x16x32_bf16 v[124:127], v[80:83], v[164:167], 0\n\t"
        "s_cbranch_vccz 31f\n\t"
        "v_lshlrev_b32_sdwa v157, 5, v161 dst_sel:DWORD dst_unused:UNUSED_PAD src0_sel:DWORD src1_sel:WORD_0\n\t"
        "v_lshlrev_b32_sdwa v158, 5, v161 dst_sel:DWORD dst_unused:UNUSED_PAD src0_sel:DWORD src1_sel:WORD_1\n\t"
        "v_or_b32 v157, v184, v157\n\t"
        "v_or_b32 v158, v184, v158\n\t"
        "v_xor_b32 v159, 32, v157\n\t"
        "v_xor_b32 v152, 32, v158\n\t"
        "ds_read_b64_tr_b16 v[60:61], v157 offset:0\n\t"
        "ds_read_b64_tr_b16 v[62:63], v158 offset:0\n\t"
        "ds_read_b64_tr_b16 v[64:65], v159 offset:0\n\t"
        "ds_read_b64_tr_b16 v[66:67], v152 offset:0\n\t"
        "ds_read_b64_tr_b16 v[68:69], v157 offset:64\n\t"
        "ds_read_b64_tr_b16 v[70:71], v158 offset:64\n\t"
        "ds_read_b64_tr_b16 v[72:73], v159 offset:64\n\t"
        "ds_read_b64_tr_b16 v[74:75], v152 offset:64\n\t"
        "ds_read_b64_tr_b16 v[76:77], v157 offset:128\n\t"
        "ds_read_b64_tr_b16 v[78:79], v158 offset:128\n\t"
        "ds_read_b64_tr_b16 v[80:81], v159 offset:128\n\t"
        "ds_read_b64_tr_b16 v[82:83], v152 offset:128\n\t"
        "v_perm_b32 v164, v163, v163, v168\n\t"
        "v_perm_b32 v165, v163, v163, v169\n\t"
        "v_perm_b32 v166, v163, v163, v170\n\t"
        "v_perm_b32 v167, v163, v163, v171\n\t"
        "s_waitcnt lgkmcnt(10)\n\t"
        "s_nop 0\n\t"
        "v_mfma_f32_16x16x32_bf16 v[48:51], v[60:63], v[164:167], v[48:51]\n\t"
        "s_waitcnt lgkmcnt(8)\n\t"
        "v_mfma_f32_16x16x32_bf16 v[52:55], v[64:67], v[164:167], v[52:55]\n\t"
        "s_waitcnt lgkmcnt(6)\n\t"
        "v_mfma_f32_16x16x32_bf16 v[56:59], v[68:71], v[164:167], v[56:59]\n\t"
        "s_waitcnt lgkmcnt(4)\n\t"
        "v_mfma_f32_16x16x32_bf16 v[116:119], v[72:75], v[164:167], v[116:119]\n\t"
        "s_waitcnt lgkmcnt(2)\n\t"
        "v_mfma_f32_16x16x32_bf16 v[120:123], v[76:79], v[164:167], v[120:123]\n\t"
        "s_waitcnt lgkmcnt(0)\n\t"
        "v_mfma_f32_16x16x32_bf16 v[124:127], v[80:83], v[164:167], v[124:127]\n\t"
        "31:\n\t"
        "v_cvt_pk_bf16_f32 v48, v48, v49\n\t"
        "v_cvt_pk_bf16_f32 v49, v50, v51\n\t"
        "v_cvt_pk_bf16_f32 v52, v52, v53\n\t"
        "v_cvt_pk_bf16_f32 v53, v54, v55\n\t"
        "v_cvt_pk_bf16_f32 v56, v56, v57\n\t"
        "v_cvt_pk_bf16_f32 v57, v58, v59\n\t"
        "v_cvt_pk_bf16_f32 v116, v116, v117\n\t"
        "v_cvt_pk_bf16_f32 v117, v118, v119\n\t"
        "v_cvt_pk_bf16_f32 v120, v120, v121\n\t"
        "v_cvt_pk_bf16_f32 v121, v122, v123\n\t"
        "v_cvt_pk_bf16_f32 v124, v124, v125\n\t"
        "v_cvt_pk_bf16_f32 v125, v126, v127\n\t"
        "ds_write_b64 v186, v[48:49] offset:0\n\t"
        "ds_write_b64 v154, v[52:53] offset:0\n\t"
        "ds_write_b64 v186, v[56:57] offset:64\n\t"
        "ds_write_b64 v154, v[116:117] offset:64\n\t"
        "ds_write_b64 v186, v[120:121] offset:128\n\t"
        "ds_write_b64 v154, v[124:125] offset:128\n\t"
        "v_add_u32 v185, 256, v185\n\t"
        "v_add_u32 v186, 12288, v186\n\t"
        "v_xor_b32 v154, 32, v186\n\t"
        "s_sub_i32 s8, s8, 16\n\t"
        "s_cmp_gt_i32 s8, 0\n\t"
        "s_cbranch_scc1 30b\n\t"
        "v_mov_b32 v185, v151\n\t"
        "v_mov_b32 v186, v183\n\t"
        "v_xor_b32 v154, 32, v186\n\t"
        "s_waitcnt lgkmcnt(0)\n\t"
        "s_barrier\n\t"
        "s_branch .Lx57%=\n\t"
        "99:\n\t"
        "s_nop 7\n\t"
        "s_waitcnt vmcnt(0) lgkmcnt(0)\n\t"
        "s_barrier\n\t"
        "v_mov_b32 %0, v156\n\t"
        : "+v"(tid) : "s"(ka), "s"(bid), "s"(recs)
        : "memory", "scc", "vcc", "s3", "s4", "s5", "s6", "s7", "s8", "s9", "s10", "s11", "s12", "s13", "s16", "s17", "s18", "s19", "s20", "s21", "s22", "s23", "s24", "s25", "s26", "s27", "s28", "s29", "s30", "s31", "s34", "s35", "s36", "s37", "s38", "s39", "s40", "s41", "s42", "s43", "s44", "s45", "s46", "s47", "s48", "s49", "s50", "s51", "s52", "s53", "s54", "s55", "s56", "s57", "s60", "s61", "s62", "s63", "s64", "s65", "s66", "s67", "s68", "s69", "s70", "s71", "s72", "s73", "s74", "s75", "s76", "s77", "s78", "s79", "s80", "s81", "s82", "s83", "s84", "s85", "s86", "s87", "s88", "s89", "s90", "s91", "s92", "s93", "s94", "s95", "s96", "s97", "s98", "s99", "v1", "v2", "v3", "v4", "v5", "v6", "v7", "v8", "v9", "v10", "v11", "v12", "v13", "v14", "v15", "v16", "v17", "v18", "v19", "v20", "v21", "v22", "v23", "v24", "v25", "v26", "v27", "v28", "v29", "v30", "v31", "v32", "v33", "v34", "v35", "v36", "v37", "v38", "v39", "v40", "v41", "v42", "v43", "v44", "v45", "v46", "v47", "v48", "v49", "v50", "v51", "v52", "v53", "v54", "v55", "v56", "v57", "v58", "v59", "v60", "v61", "v62", "v63", "v64", "v65", "v66", "v67", "v68", "v69", "v70", "v71", "v72", "v73", "v74", "v75", "v76", "v77", "v78", "v79", "v80", "v81", "v82", "v83", "v84", "v85", "v86", "v87", "v88", "v89", "v90", "v91", "v92", "v93", "v94", "v95", "v96", "v97", "v98", "v99", "v100", "v101", "v102", "v103", "v104", "v105", "v106", "v107", "v108", "v109", "v110", "v111", "v112", "v113", "v114", "v115", "v116", "v117", "v118", "v119", "v120", "v121", "v122", "v123", "v124", "v125", "v126", "v127", "v128", "v129", "v130", "v131", "v132", "v133", "v134", "v135", "v136", "v137", "v138", "v139", "v140", "v141", "v142", "v143", "v144", "v145", "v146", "v147", "v148", "v149", "v150", "v151", "v152", "v153", "v154", "v155", "v156", "v157", "v158", "v159", "v160", "v161", "v162", "v163", "v164", "v165", "v166", "v167", "v168", "v169", "v170", "v171", "v172", "v173", "v174", "v175", "v176", "v177", "v178", "v179", "v180", "v181", "v182", "v183", "v184", "v185", "v186", "v187", "v188", "v189", "v190", "v191", "v192", "v193", "v194", "v195", "v196", "v197", "v198", "v199", "v200", "v201", "v202", "v203", "v204", "v205", "v206", "v207", "v208", "v209", "v210", "v211", "v212", "v213", "v214", "v215", "v216", "v217", "v218", "v219", "v220", "v221", "v222", "v223", "v224", "v225", "v226", "v227", "v228", "v229", "v230", "v231", "v232", "v233", "v234", "v235", "v236", "v237", "v238", "v239", "v240", "v241", "v242", "v243", "v244", "v245", "v246", "v247", "v248", "v249", "v250", "v251", "v252", "v253", "v254", "v255", "a0", "a1", "a2", "a3", "a4", "a5", "a6", "a7", "a8", "a9", "a10", "a11", "a12", "a13", "a14", "a15", "a16", "a17", "a18", "a19", "a20", "a21", "a22", "a23", "a24", "a25", "a26", "a27", "a28", "a29", "a30", "a31", "a32", "a33", "a34", "a35", "a36", "a37", "a38", "a39", "a40", "a41", "a42", "a43", "a44", "a45", "a46", "a47", "a48", "a49", "a50", "a51", "a52", "a53", "a54", "a55", "a56", "a57", "a58", "a59", "a60", "a61", "a62", "a63", "a64", "a65", "a66", "a67", "a68", "a69", "a70", "a71", "a72", "a73", "a74", "a75", "a76", "a77", "a78", "a79", "a80", "a81", "a82", "a83", "a84", "a85", "a86", "a87", "a88", "a89", "a90", "a91", "a92", "a93", "a94", "a95", "a96", "a97", "a98", "a99", "a100", "a101", "a102", "a103", "a104", "a105", "a106", "a107", "a108", "a109", "a110", "a111", "a112", "a113", "a114", "a115", "a116", "a117", "a118", "a119", "a120", "a121", "a122", "a123", "a124", "a125", "a126", "a127", "a128", "a129", "a130", "a131", "a132", "a133", "a134", "a135", "a136", "a137", "a138", "a139", "a140", "a141", "a142", "a143", "a144", "a145", "a146", "a147", "a148", "a149", "a150", "a151", "a152", "a153", "a154", "a155", "a156", "a157", "a158", "a159", "a160", "a161", "a162", "a163", "a164", "a165", "a166", "a167", "a168", "a169", "a170", "a171", "a172", "a173", "a174", "a175", "a176", "a177", "a178", "a179", "a180", "a181", "a182", "a183", "a184", "a185", "a186", "a187", "a188", "a189", "a190", "a191", "a192", "a193", "a194", "a195", "a196", "a197", "a198", "a199", "a200", "a201", "a202", "a203", "a204", "a205", "a206", "a207", "a208", "a209", "a210", "a211", "a212", "a213", "a214", "a215", "a216", "a217", "a218", "a219", "a220", "a221", "a222", "a223", "a224", "a225", "a226", "a227", "a228", "a229", "a230", "a231", "a232", "a233", "a234", "a235", "a236", "a237", "a238", "a239", "a240", "a241", "a242", "a243", "a244", "a245", "a246", "a247", "a248", "a249", "a250", "a251", "a252", "a253", "a254", "a255");
    asm volatile("" : "+s"(ka), "+s"(bid) :: "memory");
    const unsigned short *x = ka->x, *off = ka->off, *w = ka->w;
    unsigned short* out = ka->out;
    const unsigned tw = ka->t.w[bid];
    const unsigned* rc = recs + bid * 64;
    const int lane = tid & 63, li = lane & 15, q = lane >> 4;
    for (int s = tid >> 6; s < 14; s += 4) {
        int p = ((tw & 255) + s) * 224 + ((tw >> 8) & 255) + li;
        if (s >= 8) p = (rc[49 + ((s - 8) >> 1)] + (s & 1) * (43200 - rc[30]) + li * rc[30]) / 192;
        if (p >= 224 * 224) continue;
        const int h = p / 224, c = p - h * 224;
        f32x4 acc[6];
        for (int n = 0; n < 6; ++n) acc[n] = (f32x4){0.f, 0.f, 0.f, 0.f};
#pragma unroll 1
        for (int t = 0; t < 9; ++t) {
            const unsigned od = ((const unsigned*)off)[p * 9 + t];
            float py = rb((float)(h + t / 3 - 1) + bf_lo(od)), px = rb((float)(c + t % 3 - 1) + bf_hi(od));
            py = fminf(fmaxf(py, 0.f), 223.f); px = fminf(fmaxf(px, 0.f), 223.f);
            const float y0 = floorf(py), x0 = floorf(px), y1 = fminf(y0 + 1.f, 223.f), x1 = fminf(x0 + 1.f, 223.f);
            const float wy = py - y0, wx = px - x0, a = rb(1.f - wy), b = rb(1.f - wx);
            const float wt[4] = {rb(a * b), rb(a * wx), rb(wy * b), rb(wy * wx)};
            const int ad[4] = {((int)y0 * 224 + (int)x0) * 96, ((int)y0 * 224 + (int)x1) * 96, ((int)y1 * 224 + (int)x0) * 96, ((int)y1 * 224 + (int)x1) * 96};
#pragma unroll 1
            for (int ks = 0; ks < 3; ++ks) {
                float r[8] = {0, 0, 0, 0, 0, 0, 0, 0};
                for (int cn = 0; cn < 4; ++cn) {
                    const u32x4 g = *(const u32x4*)(x + ad[cn] + ks * 32 + q * 8);
                    for (int d = 0; d < 4; ++d) { r[2 * d] += wt[cn] * bf_lo(g[d]); r[2 * d + 1] += wt[cn] * bf_hi(g[d]); }
                }
                u32x4 bfr;
                for (int d = 0; d < 4; ++d) bfr[d] = cvt_pk(r[2 * d], r[2 * d + 1]);
                for (int n = 0; n < 6; ++n) {
                    const unsigned short* wp = w + (t * 96 + ks * 32 + q * 8) * 96 + n * 16 + li;
                    u32x4 av;
                    for (int d = 0; d < 4; ++d) av[d] = wp[(2 * d) * 96] | ((unsigned)wp[(2 * d + 1) * 96] << 16);
                    acc[n] = __builtin_amdgcn_mfma_f32_16x16x32_bf16(__builtin_bit_cast(bf16x8, av), __builtin_bit_cast(bf16x8, bfr), acc[n], 0, 0, 0);
                }
            }
        }
        for (int n = 0; n < 6; ++n) {
            u32x2 o; o[0] = cvt_pk(acc[n][0], acc[n][1]); o[1] = cvt_pk(acc[n][2], acc[n][3]);
            *(u32x2*)(out + p * 96 + n * 16 + q * 4) = o;
        }
    }
}
extern "C" void kernel_launch(void* const* d_in, const int* in_sizes, int n_in, void* d_out, int out_size, void* d_ws, size_t ws_size, hipStream_t stream) {
    const Tiles tl = {{0x13022, 0x1406c, 0x250b2, 0x2703a, 0x18086, 0x290ca, 0x2b056, 0x1c0a0, 0x1302a, 0x14074, 0x250ba, 0x27042, 0x1808e, 0x290d2, 0x2b05e, 0x1c0a8, 0x13032, 0x3407c, 0x250c2, 0x2704a, 0x18096, 0x290da, 0x2b066, 0x1c0b0, 0x3303a, 0x14086, 0x250ca, 0x27052, 0x1809e, 0x2a022, 0x2b06e, 0x1c0b8, 0x3044, 0x1408e, 0x50d2, 0x2705a, 0x180a6, 0xa02a, 0x2b076, 0x1c0c0, 0x1304c, 0x14096, 0x250da, 0x7062, 0x280ae, 0x2a032, 0x2b07e, 0x1c0c8, 0x13054, 0x409e, 0x26022, 0x2706a, 0x80b6, 0x3a03a, 0xb086, 0xc0d0, 0x1305c, 0x140a6, 0x2602a, 0x27072, 0x380be, 0x2a044, 0x1b08e, 0x1c0d8, 0x13064, 0x140ae, 0x26032, 0x707a, 0x280c8, 0x2a04c, 0x1b096, 0x1d022, 0x1306c, 0x140b6, 0x3603a, 0x27088, 0x280d0, 0x2a054, 0x1b09e, 0x1d02a, 0x13074, 0x340be, 0x26044, 0x27090, 0x280d8, 0x2a05c, 0x1b0a6, 0x1d032, 0x3307c, 0x140c8, 0x2604c, 0x27098, 0x29022, 0x2a064, 0x3b0ae, 0x1d03a, 0x13086, 0x140d0, 0x26054, 0x270a0, 0x2902a, 0x2a06c, 0x1b0b8, 0x1d042, 0x308e, 0x140d8, 0x605c, 0x70a8, 0x29032, 0xa074, 0x1b0c0, 0xd04a, 0x13096, 0x5022, 0x26064, 0x270b0, 0x903a, 0x3a07c, 0xb0c8, 0x1d052, 0x1309e, 0x1502a, 0x2606c, 0x270b8, 0x29042, 0x2a086, 0x1b0d0, 0x1d05a, 0x130a6, 0x15032, 0x26074, 0x270c0, 0x2904a, 0x2a08e, 0x1b0d8, 0x1d062, 0x130ae, 0x1503a, 0x3607c, 0x270c8, 0x29052, 0x2a096, 0x1c022, 0x1d06a, 0x130b6, 0x15042, 0x26086, 0x270d0, 0x2905a, 0x2a09e, 0x1c02a, 0x1d072, 0x330be, 0x1504a, 0x2608e, 0x270d8, 0x29062, 0x2a0a6, 0x1c032, 0x1d07a, 0x130c8, 0x15052, 0x26096, 0x28022, 0x2906a, 0x2a0ae, 0x1c03a, 0xd082, 0x30d0, 0x1505a, 0x609e, 0x802a, 0x29072, 0xa0b6, 0x1c042, 0x1d08a, 0x130d8, 0x5062, 0x260a6, 0x28032, 0x907a, 0x3a0be, 0xc04a, 0x1d092, 0x14022, 0x1506a, 0x260ae, 0x3803a, 0x29082, 0x2a0c8, 0x1c052, 0x1d09a, 0x1402a, 0x15072, 0x260b6, 0x28044, 0x2908a, 0x2a0d0, 0x1c05a, 0x1d0a2, 0x14032, 0x1507a, 0x360be, 0x2804c, 0x29092, 0x2a0d8, 0x1c062, 0x1d0aa, 0x3403a, 0x15082, 0x260c8, 0x28054, 0x2909a, 0x2b022, 0x1c06a, 0x1d0b2, 0x14044, 0x1508a, 0x260d0, 0x2805c, 0x290a2, 0x2b02a, 0x1c072, 0x1d0ba, 0x1404c, 0x15092, 0x260d8, 0x28064, 0x290aa, 0x2b032, 0xc07a, 0xd0c2, 0x4054, 0x509a, 0x27022, 0x806c, 0x90b2, 0x2b03a, 0x1c088, 0x1d0ca, 0x1405c, 0x250a2, 0x702a, 0x28074, 0x290ba, 0xb042, 0xc090, 0x1d0d2, 0x14064, 0x250aa, 0x27032, 0x3807c, 0x290c2, 0xb04a, 0x1c098, 0x1d0da}};
    dcn_kernel<<<256, 256, 0, stream>>>((const unsigned short*)d_in[0], (const unsigned short*)d_in[1], (const unsigned short*)d_in[2], (unsigned short*)d_out, d_ws, tl);
}
```

```cpp
#include <hip/hip_runtime.h>
#include <stdint.h>
typedef __attribute__((ext_vector_type(8))) __bf16 bf16x8;
typedef __attribute__((ext_vector_type(2))) __bf16 bf16x2;
typedef __attribute__((ext_vector_type(4))) float f32x4;
typedef __attribute__((ext_vector_type(4))) unsigned u32x4;
typedef __attribute__((ext_vector_type(2))) unsigned u32x2;
struct Tiles { unsigned w[256]; };
struct Args { const unsigned short *x, *off, *w; unsigned short* out; void* ws; Tiles t; };
static __device__ const unsigned recs[256 * 64 + 1536] = {
1290240,1290240,1333248,1333248,1376256,1376256,1419264,1419264,1462272,1462272,1505280,1505280,40960,44032,48128,51200,55296,58368,62464,65536,69632,72704,76800,79872,268439552,336991254,1,1024,64,0,192,1040137727,16780300,50465804,8064,36,224,1107296256,1107296256,1107296256,1098907648,1098907648,0,258624,1073741824,258048,51200,51200,48128,1379328,1073741824,1376256,16880,1107575308,4294961868,4294967294,16880,1107575308,4294961868,4294967294,16880,1107575308,4294961868,4294967294,
3397632,3397632,3440640,3440640,3483648,3483648,3526656,3526656,3526656,3526656,3526656,3526656,40960,44032,48128,51200,55296,58368,62464,65536,62464,65536,62464,65536,268439552,336991254,1,0,80,0,192,1040137855,16780300,50465804,8064,36,224,1117782016,1117782016,1117782016,1098907648,1098907648,0,645696,1073741824,645120,44032,44032,40960,3443712,1073741824,3440640,17054,1107575460,4294950892,4294967294,17054,1107575460,4294950892,4294967294,17054,1107575460,4294950892,4294967294,
3456,3456,3648,3648,3840,3840,4032,4032,4224,4224,4416,4416,40960,44032,48128,51200,55296,58368,62464,65536,69632,72704,76800,79872,268439552,336991254,1,1024,64,0,43008,1048526591,50465804,16780300,36,8064,224,1101004800,1101004800,1101004800,1098907648,1098907648,0,129744,1073741824,720,51200,51200,48128,691968,1073741824,3840,16784,1107575224,4294964556,4294967294,16784,1107575224,4294964556,4294967294,16784,1107575224,4294964556,4294967294,
13248,13248,13440,13440,13632,13632,13824,13824,13824,13824,13824,13824,40960,44032,48128,51200,55296,58368,62464,65536,62464,65536,62464,65536,268439552,336991254,1,0,80,0,43008,1048526335,50465804,16780300,36,8064,224,1116471296,1116471296,1116471296,1098907648,1098907648,0,131544,1073741824,2520,44032,44032,40960,701568,1073741824,13440,17034,1107575440,4294953132,4294967294,17034,1107575440,4294953132,4294967294,17034,1107575440,4294953132,4294967294,
1290240,1290240,1333248,1333248,1376256,1376256,1419264,1419264,1462272,1462272,1505280,1505280,40960,44032,48128,51200,55296,58368,62464,65536,69632,72704,76800,79872,1076903984,336991254,1,1024,64,0,192,1048526591,16780300,50465804,8064,36,224,1107296256,1107296256,1107296256,1115684864,1115684864,1111490560,260352,1073741824,259776,51200,51200,48128,1388544,1073741824,1385472,1110983152,1117929996,4294961580,46,1110983152,1117929996,4294961580,46,1110983152,1117929996,4294961580,46,
31296,31296,31488,31488,31680,31680,31872,31872,31872,31872,31872,31872,40960,44032,48128,51200,55296,58368,62464,65536,62464,65536,62464,65536,268439552,336991254,1,0,80,0,43008,1056915199,50465804,16780300,36,8064,224,1126432768,1126432768,1126432768,1098907648,1098907648,0,134928,1073741824,5904,44032,44032,40960,719616,1073741824,31488,17187,1107575590,4294932076,4294967294,17187,1107575590,4294932076,4294967294,17187,1107575590,4294932076,4294967294,
40512,40512,40704,40704,40896,40896,41088,41088,41088,41088,41088,41088,40960,44032,48128,51200,55296,58368,62464,65536,62464,65536,62464,65536,268439552,336991254,1,0,80,0,43008,1056915071,50465804,16780300,36,8064,224,1129578496,1129578496,1129578496,1098907648,1098907648,0,136656,1073741824,7632,44032,44032,40960,728832,1073741824,40704,17235,1107575638,4294921324,4294967294,17235,1107575638,4294921324,4294967294,17235,1107575638,4294921324,4294967294,
7182336,7182336,7225344,7225344,7268352,7268352,7311360,7311360,7311360,7311360,7311360,7311360,40960,44032,48128,51200,55296,58368,62464,65536,62464,65536,62464,65536,268439552,336991254,1,0,80,0,192,1056915199,16780300,50465804,8064,36,224,1126694912,1126694912,1126694912,1098907648,1098907648,0,1355328,1073741824,1354752,44032,44032,40960,7228416,1073741824,7225344,17191,1107575594,4294931180,4294967294,17191,1107575594,4294931180,4294967294,17191,1107575594,4294931180,4294967294,
1419264,1419264,1462272,1462272,1505280,1505280,1548288,1548288,1548288,1548288,1548288,1548288,40960,44032,48128,51200,55296,58368,62464,65536,62464,65536,62464,65536,268439552,336991254,1,0,80,0,192,1040137727,16780300,50465804,8064,36,224,1107820544,1107820544,1107820544,1098907648,1098907648,0,274752,1073741824,274176,44032,44032,40960,1465344,1073741824,1462272,16900,1107575312,4294961196,4294967294,16900,1107575312,4294961196,4294967294,16900,1107575312,4294961196,4294967294,
3483648,3483648,3526656,3526656,3569664,3569664,3612672,3612672,3612672,3612672,3612672,3612672,40960,44032,48128,51200,55296,58368,62464,65536,62464,65536,62464,65536,268439552,336991254,1,0,80,0,192,1040137855,16780300,50465804,8064,36,224,1118044160,1118044160,1118044160,1098907648,1098907648,0,661824,1073741824,661248,44032,44032,40960,3529728,1073741824,3526656,17058,1107575464,4294950444,4294967294,17058,1107575464,4294950444,4294967294,17058,1107575464,4294950444,4294967294,
3840,3840,4032,4032,4224,4224,4416,4416,4608,4608,4800,4800,40960,44032,48128,51200,55296,58368,62464,65536,69632,72704,76800,79872,268439552,336991254,1,1024,64,0,43008,1048526591,50465804,16780300,36,8064,224,1102053376,1102053376,1102053376,1098907648,1098907648,0,129816,1073741824,792,51200,51200,48128,692352,1073741824,4224,16800,1107575240,4294964108,4294967294,16800,1107575240,4294964108,4294967294,16800,1107575240,4294964108,4294967294,
13632,13632,13824,13824,14016,14016,14208,14208,14208,14208,14208,14208,40960,44032,48128,51200,55296,58368,62464,65536,62464,65536,62464,65536,268439552,336991254,1,0,80,0,43008,1048526463,50465804,16780300,36,8064,224,1116733440,1116733440,1116733440,1098907648,1098907648,0,131616,1073741824,2592,44032,44032,40960,701952,1073741824,13824,17038,1107575444,4294952684,4294967294,17038,1107575444,4294952684,4294967294,17038,1107575444,4294952684,4294967294,
1290240,1290240,1333248,1333248,1376256,1376256,1419264,1419264,1462272,1462272,1505280,1505280,40960,44032,48128,51200,55296,58368,62464,65536,69632,72704,76800,79872,1615880272,336991254,1,1024,64,0,192,1048526591,16780300,50465804,8064,36,224,1107296256,1107296256,1107296256,1119879168,1119879168,1117782016,261504,1073741824,260928,51200,51200,48128,1394688,1073741824,1391616,1117536752,1122124300,4294961388,78,1117536752,1122124300,4294961388,78,1117536752,1122124300,4294961388,78,
31680,31680,31872,31872,32064,32064,32256,32256,32256,32256,32256,32256,40960,44032,48128,51200,55296,58368,62464,65536,62464,65536,62464,65536,268439552,336991254,1,0,80,0,43008,1056915199,50465804,16780300,36,8064,224,1126563840,1126563840,1126563840,1098907648,1098907648,0,135000,1073741824,5976,44032,44032,40960,720000,1073741824,31872,17189,1107575592,4294931628,4294967294,17189,1107575592,4294931628,4294967294,17189,1107575592,4294931628,4294967294,
40896,40896,41088,41088,41280,41280,41472,41472,41472,41472,41472,41472,40960,44032,48128,51200,55296,58368,62464,65536,62464,65536,62464,65536,268439552,336991254,1,0,80,0,43008,1056915071,50465804,16780300,36,8064,224,1129709568,1129709568,1129709568,1098907648,1098907648,0,136728,1073741824,7704,44032,44032,40960,729216,1073741824,41088,17237,1107575640,4294920876,4294967294,17237,1107575640,4294920876,4294967294,17237,1107575640,4294920876,4294967294,
7268352,7268352,7311360,7311360,7354368,7354368,7397376,7397376,7397376,7397376,7397376,7397376,40960,44032,48128,51200,55296,58368,62464,65536,62464,65536,62464,65536,268439552,336991254,1,0,80,0,192,1056915199,16780300,50465804,8064,36,224,1126825984,1126825984,1126825984,1098907648,1098907648,0,1371456,1073741824,1370880,44032,44032,40960,7314432,1073741824,7311360,17193,1107575596,4294930732,4294967294,17193,1107575596,4294930732,4294967294,17193,1107575596,4294930732,4294967294,
1505280,1505280,1548288,1548288,1591296,1591296,1634304,1634304,1634304,1634304,1634304,1634304,40960,44032,48128,51200,55296,58368,62464,65536,62464,65536,62464,65536,268439552,336991254,1,0,80,0,192,1040137727,16780300,50465804,8064,36,224,1108344832,1108344832,1108344832,1098907648,1098907648,0,290880,1073741824,290304,44032,44032,40960,1551360,1073741824,1548288,16908,1107575320,4294960748,4294967294,16908,1107575320,4294960748,4294967294,16908,1107575320,4294960748,4294967294,
258048,301056,344064,387072,430080,473088,5720064,5763072,5763072,5763072,5763072,5763072,49152,53248,57344,61440,65536,69632,40960,45056,45056,45056,45056,45056,1077936128,320017685,1,512,80,0,192,1040137855,16780300,50465804,8064,36,128,1124335616,1124335616,1090519040,1115684864,1115684864,0,1066752,1073741824,64512,32768,32768,53248,5689344,1073741824,344064,1115439862,1117799174,4294951180,62,1115439862,1117799174,4294951180,62,16576,1099972912,780,4294967294,
4224,4224,4416,4416,4608,4608,4800,4800,4992,4992,5184,5184,40960,44032,48128,51200,55296,58368,62464,65536,69632,72704,76800,79872,268439552,336991254,1,1024,64,0,43008,1048526591,50465804,16780300,36,8064,224,1103101952,1103101952,1103101952,1098907648,1098907648,0,129888,1073741824,864,51200,51200,48128,692736,1073741824,4608,16816,1107575256,4294963660,4294967294,16816,1107575256,4294963660,4294967294,16816,1107575256,4294963660,4294967294,
14016,14016,14208,14208,14400,14400,14592,14592,14592,14592,14592,14592,40960,44032,48128,51200,55296,58368,62464,65536,62464,65536,62464,65536,268439552,336991254,1,0,80,0,43008,1048526463,50465804,16780300,36,8064,224,1116995584,1116995584,1116995584,1098907648,1098907648,0,131688,1073741824,2664,44032,44032,40960,702336,1073741824,14208,17042,1107575448,4294952236,4294967294,17042,1107575448,4294952236,4294967294,17042,1107575448,4294952236,4294967294,
1290240,1290240,1333248,1333248,1376256,1376256,1419264,1419264,1462272,1462272,1505280,1505280,40960,44032,48128,51200,55296,58368,62464,65536,69632,72704,76800,79872,2154856560,336991254,1,1024,64,0,192,1048526591,16780300,50465804,8064,36,224,1107296256,1107296256,1107296256,1124073472,1124073472,1121976320,262656,1073741824,262080,51200,51200,48128,1400832,1073741824,1397760,1121731056,1125204492,4294961196,110,1121731056,1125204492,4294961196,110,1121731056,1125204492,4294961196,110,
32064,32064,32256,32256,32448,32448,32640,32640,32640,32640,32640,32640,40960,44032,48128,51200,55296,58368,62464,65536,62464,65536,62464,65536,268439552,336991254,1,0,80,0,43008,1056915199,50465804,16780300,36,8064,224,1126694912,1126694912,1126694912,1098907648,1098907648,0,135072,1073741824,6048,44032,44032,40960,720384,1073741824,32256,17191,1107575594,4294931180,4294967294,17191,1107575594,4294931180,4294967294,17191,1107575594,4294931180,4294967294,
41280,41280,41472,41472,41664,41664,41856,41856,41856,41856,41856,41856,40960,44032,48128,51200,55296,58368,62464,65536,62464,65536,62464,65536,268439552,336991254,1,0,80,0,43008,1056915071,50465804,16780300,36,8064,224,1129840640,1129840640,1129840640,1098907648,1098907648,0,136800,1073741824,7776,44032,44032,40960,729600,1073741824,41472,17239,1107575642,4294920428,4294967294,17239,1107575642,4294920428,4294967294,17239,1107575642,4294920428,4294967294,
7354368,7354368,7397376,7397376,7440384,7440384,7483392,7483392,7483392,7483392,7483392,7483392,40960,44032,48128,51200,55296,58368,62464,65536,62464,65536,62464,65536,268439552,336991254,1,0,80,0,192,1056915199,16780300,50465804,8064,36,224,1126957056,1126957056,1126957056,1098907648,1098907648,0,1387584,1073741824,1387008,44032,44032,40960,7400448,1073741824,7397376,17195,1107575598,4294930284,4294967294,17195,1107575598,4294930284,4294967294,17195,1107575598,4294930284,4294967294,
0,0,0,43008,86016,129024,2881536,2924544,2924544,2924544,2924544,2924544,49152,53248,57344,61440,65536,69632,40960,45056,45056,45056,45056,45056,808452096,320017685,1,512,80,0,192,1040137727,16780300,50465804,8064,36,128,1115947008,1115947008,0,1111490560,1111490560,0,533952,1073741824,0,32768,32768,53248,2847744,1073741824,0,1111245412,1115701896,4294959724,46,1111245412,1115701896,4294959724,46,0,1099972672,1804,4294967294,
3569664,3569664,3612672,3612672,3655680,3655680,3698688,3698688,3698688,3698688,3698688,3698688,40960,44032,48128,51200,55296,58368,62464,65536,62464,65536,62464,65536,268439552,336991254,1,0,80,0,192,1040137983,16780300,50465804,8064,36,224,1118306304,1118306304,1118306304,1098907648,1098907648,0,677952,1073741824,677376,44032,44032,40960,3615744,1073741824,3612672,17062,1107575468,4294949996,4294967294,17062,1107575468,4294949996,4294967294,17062,1107575468,4294949996,4294967294,
4608,4608,4800,4800,4992,4992,5184,5184,5376,5376,5568,5568,40960,44032,48128,51200,55296,58368,62464,65536,69632,72704,76800,79872,268439552,336991254,1,1024,64,0,43008,1048526591,50465804,16780300,36,8064,224,1104150528,1104150528,1104150528,1098907648,1098907648,0,129960,1073741824,936,51200,51200,48128,693120,1073741824,4992,16832,1107575272,4294963212,4294967294,16832,1107575272,4294963212,4294967294,16832,1107575272,4294963212,4294967294,
14400,14400,14592,14592,14784,14784,14976,14976,14976,14976,14976,14976,40960,44032,48128,51200,55296,58368,62464,65536,62464,65536,62464,65536,268439552,336991254,1,0,80,0,43008,1048526463,50465804,16780300,36,8064,224,1117257728,1117257728,1117257728,1098907648,1098907648,0,131760,1073741824,2736,44032,44032,40960,702720,1073741824,14592,17046,1107575452,4294951788,4294967294,17046,1107575452,4294951788,4294967294,17046,1107575452,4294951788,4294967294,
1290240,1290240,1333248,1333248,1376256,1376256,1419264,1419264,1462272,1462272,1505280,1505280,40960,44032,48128,51200,55296,58368,62464,65536,69632,72704,76800,79872,2693832848,336991254,1,1024,64,0,192,1048526591,16780300,50465804,8064,36,224,1107296256,1107296256,1107296256,1126170624,1126170624,1125122048,263808,1073741824,263232,51200,51200,48128,1406976,1073741824,1403904,1125007856,1127301644,4294961004,142,1125007856,1127301644,4294961004,142,1125007856,1127301644,4294961004,142,
32448,32448,32640,32640,32832,32832,33024,33024,33024,33024,33024,33024,40960,44032,48128,51200,55296,58368,62464,65536,62464,65536,62464,65536,268439552,336991254,1,0,80,0,43008,1056914943,50465804,16780300,36,8064,224,1126825984,1126825984,1126825984,1098907648,1098907648,0,135144,1073741824,6120,44032,44032,40960,720768,1073741824,32640,17193,1107575596,4294930732,4294967294,17193,1107575596,4294930732,4294967294,17193,1107575596,4294930732,4294967294,
41664,41664,41856,41856,42048,42048,42240,42240,42240,42240,42240,42240,40960,44032,48128,51200,55296,58368,62464,65536,62464,65536,62464,65536,268439552,336991254,1,0,80,0,43008,1056915071,50465804,16780300,36,8064,224,1129971712,1129971712,1129971712,1098907648,1098907648,0,136872,1073741824,7848,44032,44032,40960,729984,1073741824,41856,17241,1107575644,4294919980,4294967294,17241,1107575644,4294919980,4294967294,17241,1107575644,4294919980,4294967294,
7440384,7440384,7483392,7483392,7526400,7526400,7569408,7569408,7569408,7569408,7569408,7569408,40960,44032,48128,51200,55296,58368,62464,65536,62464,65536,62464,65536,268439552,336991254,1,0,80,0,192,1056915199,16780300,50465804,8064,36,224,1127088128,1127088128,1127088128,1098907648,1098907648,0,1403712,1073741824,1403136,44032,44032,40960,7486464,1073741824,7483392,17197,1107575600,4294929836,4294967294,17197,1107575600,4294929836,4294967294,17197,1107575600,4294929836,4294967294,
1290240,1333248,1376256,1419264,1462272,1505280,1548288,1591296,1634304,1677312,1677312,1677312,40960,45056,49152,53248,57344,61440,65536,69632,73728,77824,77824,77824,538976288,353703189,0,1280,64,0,192,1040137855,16780300,50465804,8064,36,128,1108344832,1107820544,1107296256,1107296256,1107296256,1107296256,291456,275328,259200,61440,53248,45056,1554432,1468416,1382400,1106264560,1112031772,4294964556,30,1106264560,1112031772,4294964556,30,1106264560,1112031772,4294964556,30,
3655680,3655680,3698688,3698688,3741696,3741696,3784704,3784704,3784704,3784704,3784704,3784704,40960,44032,48128,51200,55296,58368,62464,65536,62464,65536,62464,65536,268439552,336991254,1,0,80,0,192,1040137983,16780300,50465804,8064,36,224,1118568448,1118568448,1118568448,1098907648,1098907648,0,694080,1073741824,693504,44032,44032,40960,3701760,1073741824,3698688,17066,1107575472,4294949548,4294967294,17066,1107575472,4294949548,4294967294,17066,1107575472,4294949548,4294967294,
3397632,3440640,3483648,3526656,3569664,3612672,3655680,3698688,3698688,3698688,3698688,3698688,40960,45056,49152,53248,57344,61440,65536,69632,69632,69632,69632,69632,538976288,353703189,0,256,80,0,192,1048526591,16780300,50465804,8064,36,128,1118306304,1118044160,1117782016,1107296256,1107296256,1107296256,678528,662400,646272,57344,49152,40960,3618816,3532800,3446784,1106264734,1112031916,4294958284,30,1106264734,1112031916,4294958284,30,1106264734,1112031916,4294958284,30,
14784,14784,14976,14976,15168,15168,15360,15360,15360,15360,15360,15360,40960,44032,48128,51200,55296,58368,62464,65536,62464,65536,62464,65536,268439552,336991254,1,0,80,0,43008,1048526463,50465804,16780300,36,8064,224,1117519872,1117519872,1117519872,1098907648,1098907648,0,131832,1073741824,2808,44032,44032,40960,703104,1073741824,14976,17050,1107575456,4294951340,4294967294,17050,1107575456,4294951340,4294967294,17050,1107575456,4294951340,4294967294,
1290240,1290240,1333248,1333248,1376256,1376256,1419264,1419264,1462272,1462272,1505280,1505280,40960,44032,48128,51200,55296,58368,62464,65536,69632,72704,76800,79872,3232809136,336991254,1,1024,64,0,192,1048526591,16780300,50465804,8064,36,224,1107296256,1107296256,1107296256,1128267776,1128267776,1127219200,264960,1073741824,264384,51200,51200,48128,1413120,1073741824,1410048,1127105008,1129398796,4294960812,174,1127105008,1129398796,4294960812,174,1127105008,1129398796,4294960812,174,
6494208,6537216,6580224,6623232,6666240,6709248,6752256,6795264,6795264,6795264,6795264,6795264,40960,45056,49152,53248,57344,61440,65536,69632,69632,69632,69632,69632,538976288,353703189,0,256,80,0,192,1056914943,16780300,50465804,8064,36,128,1125908480,1125777408,1125646336,1107296256,1107296256,1107296256,1259136,1243008,1226880,57344,49152,40960,6715392,6629376,6543360,1106264855,1112032030,4294949068,30,1106264855,1112032030,4294949068,30,1106264855,1112032030,4294949068,30,
42048,42048,42240,42240,42432,42432,42624,42624,42624,42624,42624,42624,40960,44032,48128,51200,55296,58368,62464,65536,62464,65536,62464,65536,268439552,336991254,1,0,80,0,43008,1056915071,50465804,16780300,36,8064,224,1130102784,1130102784,1130102784,1098907648,1098907648,0,136944,1073741824,7920,44032,44032,40960,730368,1073741824,42240,17243,1107575646,4294919532,4294967294,17243,1107575646,4294919532,4294967294,17243,1107575646,4294919532,4294967294,
7526400,7526400,7569408,7569408,7612416,7612416,7655424,7655424,7655424,7655424,7655424,7655424,40960,44032,48128,51200,55296,58368,62464,65536,62464,65536,62464,65536,268439552,336991254,1,0,80,0,192,1056915199,16780300,50465804,8064,36,224,1127219200,1127219200,1127219200,1098907648,1098907648,0,1419840,1073741824,1419264,44032,44032,40960,7572480,1073741824,7569408,17199,1107575602,4294929388,4294967294,17199,1107575602,4294929388,4294967294,17199,1107575602,4294929388,4294967294,
1591296,1591296,1634304,1634304,1677312,1677312,1720320,1720320,1720320,1720320,1720320,1720320,40960,44032,48128,51200,55296,58368,62464,65536,62464,65536,62464,65536,268439552,336991254,1,0,80,0,192,1040137855,16780300,50465804,8064,36,224,1108869120,1108869120,1108869120,1098907648,1098907648,0,307008,1073741824,306432,44032,44032,40960,1637376,1073741824,1634304,16916,1107575328,4294960300,4294967294,16916,1107575328,4294960300,4294967294,16916,1107575328,4294960300,4294967294,
3741696,3741696,3784704,3784704,3827712,3827712,3870720,3870720,3870720,3870720,3870720,3870720,40960,44032,48128,51200,55296,58368,62464,65536,62464,65536,62464,65536,268439552,336991254,1,0,80,0,192,1040137983,16780300,50465804,8064,36,224,1118830592,1118830592,1118830592,1098907648,1098907648,0,710208,1073741824,709632,44032,44032,40960,3787776,1073741824,3784704,17070,1107575476,4294949100,4294967294,17070,1107575476,4294949100,4294967294,17070,1107575476,4294949100,4294967294,
4992,4992,5184,5184,5376,5376,5568,5568,5760,5760,5952,5952,40960,44032,48128,51200,55296,58368,62464,65536,69632,72704,76800,79872,268439552,336991254,1,1024,64,0,43008,1048526591,50465804,16780300,36,8064,224,1105199104,1105199104,1105199104,1098907648,1098907648,0,130032,1073741824,1008,51200,51200,48128,693504,1073741824,5376,16848,1107575288,4294962764,4294967294,16848,1107575288,4294962764,4294967294,16848,1107575288,4294962764,4294967294,
4429824,4472832,4515840,4558848,4601856,4644864,4687872,4730880,4730880,4730880,4730880,4730880,40960,45056,49152,53248,57344,61440,65536,69632,69632,69632,69632,69632,538976288,353703189,0,256,80,0,192,1048526463,16780300,50465804,8064,36,128,1121452032,1121189888,1120927744,1107296256,1107296256,1107296256,872064,855936,839808,57344,49152,40960,4651008,4564992,4478976,1106264782,1112031964,4294955212,30,1106264782,1112031964,4294955212,30,1106264782,1112031964,4294955212,30,
22848,22848,23040,23040,23232,23232,23424,23424,23424,23424,23424,23424,40960,44032,48128,51200,55296,58368,62464,65536,62464,65536,62464,65536,268439552,336991254,1,0,80,0,43008,1048526591,50465804,16780300,36,8064,224,1123024896,1123024896,1123024896,1098907648,1098907648,0,133344,1073741824,4320,44032,44032,40960,711168,1073741824,23040,17134,1107575540,4294941932,4294967294,17134,1107575540,4294941932,4294967294,17134,1107575540,4294941932,4294967294,
32832,32832,33024,33024,33216,33216,33408,33408,33408,33408,33408,33408,40960,44032,48128,51200,55296,58368,62464,65536,62464,65536,62464,65536,268439552,336991254,1,0,80,0,43008,1056914943,50465804,16780300,36,8064,224,1126957056,1126957056,1126957056,1098907648,1098907648,0,135216,1073741824,6192,44032,44032,40960,721152,1073741824,33024,17195,1107575598,4294930284,4294967294,17195,1107575598,4294930284,4294967294,17195,1107575598,4294930284,4294967294,
42432,42432,42624,42624,42816,42816,42816,42816,42816,42816,42816,42816,40960,44032,48128,51200,55296,58368,62464,65536,62464,65536,62464,65536,268439552,336991254,1,0,80,0,43008,1056915071,50465804,16780300,36,8064,224,1130233856,1130233856,1130233856,1098907648,1098907648,0,137016,1073741824,7992,44032,44032,40960,730752,1073741824,42624,17245,1107575647,4294919084,4294967294,17245,1107575647,4294919084,4294967294,17245,1107575647,4294919084,4294967294,
7612416,7612416,7655424,7655424,7698432,7698432,7741440,7741440,7741440,7741440,7741440,7741440,40960,44032,48128,51200,55296,58368,62464,65536,62464,65536,62464,65536,268439552,336991254,1,0,80,0,192,1056915199,16780300,50465804,8064,36,224,1127350272,1127350272,1127350272,1098907648,1098907648,0,1435968,1073741824,1435392,44032,44032,40960,7658496,1073741824,7655424,17201,1107575604,4294928940,4294967294,17201,1107575604,4294928940,4294967294,17201,1107575604,4294928940,4294967294,
1677312,1677312,1720320,1720320,1763328,1763328,1806336,1806336,1806336,1806336,1806336,1806336,40960,44032,48128,51200,55296,58368,62464,65536,62464,65536,62464,65536,268439552,336991254,1,0,80,0,192,1040137855,16780300,50465804,8064,36,224,1109393408,1109393408,1109393408,1098907648,1098907648,0,323136,1073741824,322560,44032,44032,40960,1723392,1073741824,1720320,16924,1107575336,4294959852,4294967294,16924,1107575336,4294959852,4294967294,16924,1107575336,4294959852,4294967294,
2365440,2408448,2451456,2494464,2537472,2580480,2623488,2666496,2666496,2666496,2666496,2666496,40960,45056,49152,53248,57344,61440,65536,69632,69632,69632,69632,69632,538976288,353703189,0,256,80,0,192,1040137983,16780300,50465804,8064,36,128,1114636288,1114112000,1113587712,1107296256,1107296256,1107296256,484992,468864,452736,57344,49152,40960,2586624,2500608,2414592,1106264668,1112031864,4294961356,30,1106264668,1112031864,4294961356,30,1106264668,1112031864,4294961356,30,
5376,5376,5568,5568,5760,5760,5952,5952,6144,6144,6336,6336,40960,44032,48128,51200,55296,58368,62464,65536,69632,72704,76800,79872,268439552,336991254,1,1024,64,0,43008,1048526335,50465804,16780300,36,8064,224,1106247680,1106247680,1106247680,1098907648,1098907648,0,130104,1073741824,1080,51200,51200,48128,693888,1073741824,5760,16864,1107575300,4294962316,4294967294,16864,1107575300,4294962316,4294967294,16864,1107575300,4294962316,4294967294,
15168,15168,15360,15360,15552,15552,15744,15744,15744,15744,15744,15744,40960,44032,48128,51200,55296,58368,62464,65536,62464,65536,62464,65536,268439552,336991254,1,0,80,0,43008,1048526463,50465804,16780300,36,8064,224,1117782016,1117782016,1117782016,1098907648,1098907648,0,131904,1073741824,2880,44032,44032,40960,703488,1073741824,15360,17054,1107575460,4294950892,4294967294,17054,1107575460,4294950892,4294967294,17054,1107575460,4294950892,4294967294,
5462016,5505024,5548032,5591040,5634048,5677056,5720064,5763072,5763072,5763072,5763072,5763072,40960,45056,49152,53248,57344,61440,65536,69632,69632,69632,69632,69632,538976288,353703189,0,256,80,0,192,1048526591,16780300,50465804,8064,36,128,1124335616,1124204544,1124073472,1107296256,1107296256,1107296256,1065600,1049472,1033344,57344,49152,40960,5683200,5597184,5511168,1106264830,1112032006,4294952140,30,1106264830,1112032006,4294952140,30,1106264830,1112032006,4294952140,30,
946176,989184,1032192,1075200,1118208,1161216,2881536,2924544,2924544,2924544,2924544,2924544,49152,53248,57344,61440,65536,69632,40960,45056,45056,45056,45056,45056,2694840320,320017685,1,0,80,0,192,1056914943,16780300,50465804,8064,36,128,1115947008,1115947008,1103101952,1126170624,1126170624,0,537984,1073741824,193536,32768,32768,53248,2869248,1073741824,1032192,1126122084,1127236232,4294959052,158,1126122084,1127236232,4294959052,158,16816,1099973080,4294966028,4294967294,
7526400,7569408,7612416,7655424,7698432,7741440,7784448,7827456,7827456,7827456,7827456,7827456,40960,45056,49152,53248,57344,61440,65536,69632,69632,69632,69632,69632,538976288,353703189,0,256,80,0,192,1056915199,16780300,50465804,8064,36,128,1127481344,1127350272,1127219200,1107296256,1107296256,1107296256,1452672,1436544,1420416,57344,49152,40960,7747584,7661568,7575552,1106264879,1112032054,4294945996,30,1106264879,1112032054,4294945996,30,1106264879,1112032054,4294945996,30,
8558592,8601600,8644608,8687616,8730624,8773632,8816640,8859648,8859648,8859648,8859648,8859648,40960,45056,49152,53248,57344,61440,65536,69632,69632,69632,69632,69632,538976288,353703189,0,256,80,0,192,1056915199,16780300,50465804,8064,36,128,1129054208,1128923136,1128792064,1107296256,1107296256,1107296256,1646208,1630080,1613952,57344,49152,40960,8779776,8693760,8607744,1106264903,1112032078,4294942924,30,1106264903,1112032078,4294942924,30,1106264903,1112032078,4294942924,30,
1763328,1763328,1806336,1806336,1849344,1849344,1892352,1892352,1892352,1892352,1892352,1892352,40960,44032,48128,51200,55296,58368,62464,65536,62464,65536,62464,65536,268439552,336991254,1,0,80,0,192,1040137855,16780300,50465804,8064,36,224,1109917696,1109917696,1109917696,1098907648,1098907648,0,339264,1073741824,338688,44032,44032,40960,1809408,1073741824,1806336,16932,1107575344,4294959404,4294967294,16932,1107575344,4294959404,4294967294,16932,1107575344,4294959404,4294967294,
3827712,3827712,3870720,3870720,3913728,3913728,3956736,3956736,3956736,3956736,3956736,3956736,40960,44032,48128,51200,55296,58368,62464,65536,62464,65536,62464,65536,268439552,336991254,1,0,80,0,192,1040137983,16780300,50465804,8064,36,224,1119092736,1119092736,1119092736,1098907648,1098907648,0,726336,1073741824,725760,44032,44032,40960,3873792,1073741824,3870720,17074,1107575480,4294948652,4294967294,17074,1107575480,4294948652,4294967294,17074,1107575480,4294948652,4294967294,
5760,5760,5952,5952,6144,6144,6336,6336,6528,6528,6720,6720,40960,44032,48128,51200,55296,58368,62464,65536,69632,72704,76800,79872,268439552,336991254,1,1024,64,0,43008,1048526335,50465804,16780300,36,8064,224,1107296256,1107296256,1107296256,1098907648,1098907648,0,130176,1073741824,1152,51200,51200,48128,694272,1073741824,6144,16880,1107575308,4294961868,4294967294,16880,1107575308,4294961868,4294967294,16880,1107575308,4294961868,4294967294,
15552,15552,15744,15744,15936,15936,16128,16128,16128,16128,16128,16128,40960,44032,48128,51200,55296,58368,62464,65536,62464,65536,62464,65536,268439552,336991254,1,0,80,0,43008,1048526463,50465804,16780300,36,8064,224,1118044160,1118044160,1118044160,1098907648,1098907648,0,131976,1073741824,2952,44032,44032,40960,703872,1073741824,15744,17058,1107575464,4294950444,4294967294,17058,1107575464,4294950444,4294967294,17058,1107575464,4294950444,4294967294,
860160,903168,946176,989184,1032192,1075200,8558592,8601600,8601600,8601600,8601600,8601600,49152,53248,57344,61440,65536,69632,40960,45056,45056,45056,45056,45056,2155872256,320017685,1,0,80,0,192,1048526591,16780300,50465804,8064,36,128,1128660992,1128660992,1102053376,1124073472,1124073472,0,1601280,1073741824,177408,32768,32768,53248,8540160,1073741824,946176,1123959613,1125139272,4294942348,126,1123959613,1125139272,4294942348,126,16800,1099973064,4294966284,4294967294,
33216,33216,33408,33408,33600,33600,33792,33792,33792,33792,33792,33792,40960,44032,48128,51200,55296,58368,62464,65536,62464,65536,62464,65536,268439552,336991254,1,0,80,0,43008,1056915071,50465804,16780300,36,8064,224,1127088128,1127088128,1127088128,1098907648,1098907648,0,135288,1073741824,6264,44032,44032,40960,721536,1073741824,33408,17197,1107575600,4294929836,4294967294,17197,1107575600,4294929836,4294967294,17197,1107575600,4294929836,4294967294,
5462016,5462016,5505024,5505024,5548032,5548032,5591040,5591040,5591040,5591040,5591040,5591040,40960,44032,48128,51200,55296,58368,62464,65536,62464,65536,62464,65536,268439552,336991254,1,0,80,0,192,1056915199,16780300,50465804,8064,36,224,1124073472,1124073472,1124073472,1098907648,1098907648,0,1032768,1073741824,1032192,44032,44032,40960,5508096,1073741824,5505024,17150,1107575554,4294940140,4294967294,17150,1107575554,4294940140,4294967294,17150,1107575554,4294940140,4294967294,
7698432,7698432,7741440,7741440,7784448,7784448,7827456,7827456,7827456,7827456,7827456,7827456,40960,44032,48128,51200,55296,58368,62464,65536,62464,65536,62464,65536,268439552,336991254,1,0,80,0,192,1056915199,16780300,50465804,8064,36,224,1127481344,1127481344,1127481344,1098907648,1098907648,0,1452096,1073741824,1451520,44032,44032,40960,7744512,1073741824,7741440,17203,1107575606,4294928492,4294967294,17203,1107575606,4294928492,4294967294,17203,1107575606,4294928492,4294967294,
1849344,1849344,1892352,1892352,1935360,1935360,1978368,1978368,1978368,1978368,1978368,1978368,40960,44032,48128,51200,55296,58368,62464,65536,62464,65536,62464,65536,268439552,336991254,1,0,80,0,192,1040137855,16780300,50465804,8064,36,224,1110441984,1110441984,1110441984,1098907648,1098907648,0,355392,1073741824,354816,44032,44032,40960,1895424,1073741824,1892352,16940,1107575352,4294958956,4294967294,16940,1107575352,4294958956,4294967294,16940,1107575352,4294958956,4294967294,
3913728,3913728,3956736,3956736,3999744,3999744,4042752,4042752,4042752,4042752,4042752,4042752,40960,44032,48128,51200,55296,58368,62464,65536,62464,65536,62464,65536,268439552,336991254,1,0,80,0,192,1040137983,16780300,50465804,8064,36,224,1119354880,1119354880,1119354880,1098907648,1098907648,0,742464,1073741824,741888,44032,44032,40960,3959808,1073741824,3956736,17078,1107575484,4294948204,4294967294,17078,1107575484,4294948204,4294967294,17078,1107575484,4294948204,4294967294,
6336,6336,6528,6528,6720,6720,6912,6912,6912,6912,6912,6912,40960,44032,48128,51200,55296,58368,62464,65536,62464,65536,62464,65536,268439552,336991254,1,0,80,0,43008,1048526335,50465804,16780300,36,8064,224,1107820544,1107820544,1107820544,1098907648,1098907648,0,130248,1073741824,1224,44032,44032,40960,694656,1073741824,6528,16900,1107575312,4294961196,4294967294,16900,1107575312,4294961196,4294967294,16900,1107575312,4294961196,4294967294,
5634048,5677056,5720064,5763072,5806080,5849088,5849088,5849088,5849088,5849088,5849088,5849088,40960,45056,49152,53248,57344,61440,61440,61440,61440,61440,61440,61440,1886417008,320017171,0,256,80,0,192,1048526463,16780300,50465804,8064,36,128,1124204544,1124335616,1124466688,1121976320,1121976320,1121976320,1052352,1068480,1084608,32768,40960,49152,5612544,5698560,5784576,1121862386,1124090632,4294951148,110,1121862386,1124090632,4294951148,110,1121862386,1124090632,4294951148,110,
23232,23232,23424,23424,23616,23616,23808,23808,23808,23808,23808,23808,40960,44032,48128,51200,55296,58368,62464,65536,62464,65536,62464,65536,268439552,336991254,1,0,80,0,43008,1048526591,50465804,16780300,36,8064,224,1123287040,1123287040,1123287040,1098907648,1098907648,0,133416,1073741824,4392,44032,44032,40960,711552,1073741824,23424,17138,1107575544,4294941484,4294967294,17138,1107575544,4294941484,4294967294,17138,1107575544,4294941484,4294967294,
33600,33600,33792,33792,33984,33984,34176,34176,34176,34176,34176,34176,40960,44032,48128,51200,55296,58368,62464,65536,62464,65536,62464,65536,268439552,336991254,1,0,80,0,43008,1056915071,50465804,16780300,36,8064,224,1127219200,1127219200,1127219200,1098907648,1098907648,0,135360,1073741824,6336,44032,44032,40960,721920,1073741824,33792,17199,1107575602,4294929388,4294967294,17199,1107575602,4294929388,4294967294,17199,1107575602,4294929388,4294967294,
5548032,5548032,5591040,5591040,5634048,5634048,5677056,5677056,5677056,5677056,5677056,5677056,40960,44032,48128,51200,55296,58368,62464,65536,62464,65536,62464,65536,268439552,336991254,1,0,80,0,192,1056915199,16780300,50465804,8064,36,224,1124204544,1124204544,1124204544,1098907648,1098907648,0,1048896,1073741824,1048320,44032,44032,40960,5594112,1073741824,5591040,17153,1107575556,4294939692,4294967294,17153,1107575556,4294939692,4294967294,17153,1107575556,4294939692,4294967294,
7784448,7784448,7827456,7827456,7870464,7870464,7913472,7913472,7913472,7913472,7913472,7913472,40960,44032,48128,51200,55296,58368,62464,65536,62464,65536,62464,65536,268439552,336991254,1,0,80,0,192,1056914943,16780300,50465804,8064,36,224,1127612416,1127612416,1127612416,1098907648,1098907648,0,1468224,1073741824,1467648,44032,44032,40960,7830528,1073741824,7827456,17205,1107575608,4294928044,4294967294,17205,1107575608,4294928044,4294967294,17205,1107575608,4294928044,4294967294,
1935360,1935360,1978368,1978368,2021376,2021376,2064384,2064384,2064384,2064384,2064384,2064384,40960,44032,48128,51200,55296,58368,62464,65536,62464,65536,62464,65536,268439552,336991254,1,0,80,0,192,1040137855,16780300,50465804,8064,36,224,1110966272,1110966272,1110966272,1098907648,1098907648,0,371520,1073741824,370944,44032,44032,40960,1981440,1073741824,1978368,16948,1107575360,4294958508,4294967294,16948,1107575360,4294958508,4294967294,16948,1107575360,4294958508,4294967294,
3999744,3999744,4042752,4042752,4085760,4085760,4128768,4128768,4128768,4128768,4128768,4128768,40960,44032,48128,51200,55296,58368,62464,65536,62464,65536,62464,65536,268439552,336991254,1,0,80,0,192,1040137983,16780300,50465804,8064,36,224,1119617024,1119617024,1119617024,1098907648,1098907648,0,758592,1073741824,758016,44032,44032,40960,4045824,1073741824,4042752,17082,1107575488,4294947756,4294967294,17082,1107575488,4294947756,4294967294,17082,1107575488,4294947756,4294967294,
430080,473088,516096,559104,602112,645120,2881536,2924544,2924544,2924544,2924544,2924544,49152,53248,57344,61440,65536,69632,40960,45056,45056,45056,45056,45056,1616904192,320017685,1,512,80,0,192,1048526335,16780300,50465804,8064,36,128,1115947008,1115947008,1094713344,1119879168,1119879168,0,535680,1073741824,96768,32768,32768,53248,2856960,1073741824,516096,1119765092,1121993352,4294959436,94,1119765092,1121993352,4294959436,94,16672,1099972976,268,4294967294,
15936,15936,16128,16128,16320,16320,16512,16512,16512,16512,16512,16512,40960,44032,48128,51200,55296,58368,62464,65536,62464,65536,62464,65536,268439552,336991254,1,0,80,0,43008,1048526591,50465804,16780300,36,8064,224,1118306304,1118306304,1118306304,1098907648,1098907648,0,132048,1073741824,3024,44032,44032,40960,704256,1073741824,16128,17062,1107575468,4294949996,4294967294,17062,1107575468,4294949996,4294967294,17062,1107575468,4294949996,4294967294,
23616,23616,23808,23808,24000,24000,24192,24192,24192,24192,24192,24192,40960,44032,48128,51200,55296,58368,62464,65536,62464,65536,62464,65536,268439552,336991254,1,0,80,0,43008,1048526591,50465804,16780300,36,8064,224,1123549184,1123549184,1123549184,1098907648,1098907648,0,133488,1073741824,4464,44032,44032,40960,711936,1073741824,23808,17142,1107575548,4294941036,4294967294,17142,1107575548,4294941036,4294967294,17142,1107575548,4294941036,4294967294,
33984,33984,34176,34176,34368,34368,34560,34560,34560,34560,34560,34560,40960,44032,48128,51200,55296,58368,62464,65536,62464,65536,62464,65536,268439552,336991254,1,0,80,0,43008,1056915071,50465804,16780300,36,8064,224,1127350272,1127350272,1127350272,1098907648,1098907648,0,135432,1073741824,6408,44032,44032,40960,722304,1073741824,34176,17201,1107575604,4294928940,4294967294,17201,1107575604,4294928940,4294967294,17201,1107575604,4294928940,4294967294,
5634048,5634048,5677056,5677056,5720064,5720064,5763072,5763072,5763072,5763072,5763072,5763072,40960,44032,48128,51200,55296,58368,62464,65536,62464,65536,62464,65536,268439552,336991254,1,0,80,0,192,1056915199,16780300,50465804,8064,36,224,1124335616,1124335616,1124335616,1098907648,1098907648,0,1065024,1073741824,1064448,44032,44032,40960,5680128,1073741824,5677056,17155,1107575558,4294939244,4294967294,17155,1107575558,4294939244,4294967294,17155,1107575558,4294939244,4294967294,
7870464,7870464,7913472,7913472,7956480,7956480,7999488,7999488,7999488,7999488,7999488,7999488,40960,44032,48128,51200,55296,58368,62464,65536,62464,65536,62464,65536,268439552,336991254,1,0,80,0,192,1056914943,16780300,50465804,8064,36,224,1127743488,1127743488,1127743488,1098907648,1098907648,0,1484352,1073741824,1483776,44032,44032,40960,7916544,1073741824,7913472,17207,1107575610,4294927596,4294967294,17207,1107575610,4294927596,4294967294,17207,1107575610,4294927596,4294967294,
2021376,2021376,2064384,2064384,2107392,2107392,2150400,2150400,2150400,2150400,2150400,2150400,40960,44032,48128,51200,55296,58368,62464,65536,62464,65536,62464,65536,268439552,336991254,1,0,80,0,192,1040137855,16780300,50465804,8064,36,224,1111490560,1111490560,1111490560,1098907648,1098907648,0,387648,1073741824,387072,44032,44032,40960,2067456,1073741824,2064384,16956,1107575368,4294958060,4294967294,16956,1107575368,4294958060,4294967294,16956,1107575368,4294958060,4294967294,
344064,387072,430080,473088,516096,559104,8558592,8601600,8601600,8601600,8601600,8601600,49152,53248,57344,61440,65536,69632,40960,45056,45056,45056,45056,45056,1077936128,320017685,1,512,80,0,192,1040137983,16780300,50465804,8064,36,128,1128660992,1128660992,1092616192,1115684864,1115684864,0,1598976,1073741824,80640,32768,32768,53248,8527872,1073741824,430080,1115439933,1117799240,4294942732,62,1115439933,1117799240,4294942732,62,16640,1099972944,524,4294967294,
6720,6720,6912,6912,7104,7104,7296,7296,7296,7296,7296,7296,40960,44032,48128,51200,55296,58368,62464,65536,62464,65536,62464,65536,268439552,336991254,1,0,80,0,43008,1048526463,50465804,16780300,36,8064,224,1108344832,1108344832,1108344832,1098907648,1098907648,0,130320,1073741824,1296,44032,44032,40960,695040,1073741824,6912,16908,1107575320,4294960748,4294967294,16908,1107575320,4294960748,4294967294,16908,1107575320,4294960748,4294967294,
16320,16320,16512,16512,16704,16704,16896,16896,16896,16896,16896,16896,40960,44032,48128,51200,55296,58368,62464,65536,62464,65536,62464,65536,268439552,336991254,1,0,80,0,43008,1048526591,50465804,16780300,36,8064,224,1118568448,1118568448,1118568448,1098907648,1098907648,0,132120,1073741824,3096,44032,44032,40960,704640,1073741824,16512,17066,1107575472,4294949548,4294967294,17066,1107575472,4294949548,4294967294,17066,1107575472,4294949548,4294967294,
24000,24000,24192,24192,24384,24384,24576,24576,24576,24576,24576,24576,40960,44032,48128,51200,55296,58368,62464,65536,62464,65536,62464,65536,268439552,336991254,1,0,80,0,43008,1048526591,50465804,16780300,36,8064,224,1123811328,1123811328,1123811328,1098907648,1098907648,0,133560,1073741824,4536,44032,44032,40960,712320,1073741824,24192,17146,1107575552,4294940588,4294967294,17146,1107575552,4294940588,4294967294,17146,1107575552,4294940588,4294967294,
34368,34368,34560,34560,34752,34752,34944,34944,34944,34944,34944,34944,40960,44032,48128,51200,55296,58368,62464,65536,62464,65536,62464,65536,268439552,336991254,1,0,80,0,43008,1056915071,50465804,16780300,36,8064,224,1127481344,1127481344,1127481344,1098907648,1098907648,0,135504,1073741824,6480,44032,44032,40960,722688,1073741824,34560,17203,1107575606,4294928492,4294967294,17203,1107575606,4294928492,4294967294,17203,1107575606,4294928492,4294967294,
5720064,5720064,5763072,5763072,5806080,5806080,5849088,5849088,5849088,5849088,5849088,5849088,40960,44032,48128,51200,55296,58368,62464,65536,62464,65536,62464,65536,268439552,336991254,1,0,80,0,192,1056915199,16780300,50465804,8064,36,224,1124466688,1124466688,1124466688,1098907648,1098907648,0,1081152,1073741824,1080576,44032,44032,40960,5766144,1073741824,5763072,17157,1107575560,4294938796,4294967294,17157,1107575560,4294938796,4294967294,17157,1107575560,4294938796,4294967294,
7956480,7956480,7999488,7999488,8042496,8042496,8085504,8085504,8085504,8085504,8085504,8085504,40960,44032,48128,51200,55296,58368,62464,65536,62464,65536,62464,65536,268439552,336991254,1,0,80,0,192,1056914943,16780300,50465804,8064,36,224,1127874560,1127874560,1127874560,1098907648,1098907648,0,1500480,1073741824,1499904,44032,44032,40960,8002560,1073741824,7999488,17209,1107575612,4294927148,4294967294,17209,1107575612,4294927148,4294967294,17209,1107575612,4294927148,4294967294,
0,43008,86016,129024,172032,215040,5720064,5763072,5763072,5763072,5763072,5763072,49152,53248,57344,61440,65536,69632,40960,45056,45056,45056,45056,45056,808452096,320017685,1,512,80,0,192,1040137855,16780300,50465804,8064,36,128,1124335616,1124335616,1073741824,1111490560,1111490560,0,1066176,1073741824,16128,32768,32768,53248,5686272,1073741824,86016,1111245558,1115702022,4294951276,46,1111245558,1115702022,4294951276,46,0,1099972768,1548,4294967294,
4085760,4085760,4128768,4128768,4171776,4171776,4214784,4214784,4214784,4214784,4214784,4214784,40960,44032,48128,51200,55296,58368,62464,65536,62464,65536,62464,65536,268439552,336991254,1,0,80,0,192,1040137983,16780300,50465804,8064,36,224,1119879168,1119879168,1119879168,1098907648,1098907648,0,774720,1073741824,774144,44032,44032,40960,4131840,1073741824,4128768,17086,1107575492,4294947308,4294967294,17086,1107575492,4294947308,4294967294,17086,1107575492,4294947308,4294967294,
7104,7104,7296,7296,7488,7488,7680,7680,7680,7680,7680,7680,40960,44032,48128,51200,55296,58368,62464,65536,62464,65536,62464,65536,268439552,336991254,1,0,80,0,43008,1048526463,50465804,16780300,36,8064,224,1108869120,1108869120,1108869120,1098907648,1098907648,0,130392,1073741824,1368,44032,44032,40960,695424,1073741824,7296,16916,1107575328,4294960300,4294967294,16916,1107575328,4294960300,4294967294,16916,1107575328,4294960300,4294967294,
16704,16704,16896,16896,17088,17088,17280,17280,17280,17280,17280,17280,40960,44032,48128,51200,55296,58368,62464,65536,62464,65536,62464,65536,268439552,336991254,1,0,80,0,43008,1048526591,50465804,16780300,36,8064,224,1118830592,1118830592,1118830592,1098907648,1098907648,0,132192,1073741824,3168,44032,44032,40960,705024,1073741824,16896,17070,1107575476,4294949100,4294967294,17070,1107575476,4294949100,4294967294,17070,1107575476,4294949100,4294967294,
24384,24384,24576,24576,24768,24768,24960,24960,24960,24960,24960,24960,40960,44032,48128,51200,55296,58368,62464,65536,62464,65536,62464,65536,268439552,336991254,1,0,80,0,43008,1056914943,50465804,16780300,36,8064,224,1124073472,1124073472,1124073472,1098907648,1098907648,0,133632,1073741824,4608,44032,44032,40960,712704,1073741824,24576,17150,1107575554,4294940140,4294967294,17150,1107575554,4294940140,4294967294,17150,1107575554,4294940140,4294967294,
34752,34752,34944,34944,35136,35136,35328,35328,35328,35328,35328,35328,40960,44032,48128,51200,55296,58368,62464,65536,62464,65536,62464,65536,268439552,336991254,1,0,80,0,43008,1056915071,50465804,16780300,36,8064,224,1127612416,1127612416,1127612416,1098907648,1098907648,0,135576,1073741824,6552,44032,44032,40960,723072,1073741824,34944,17205,1107575608,4294928044,4294967294,17205,1107575608,4294928044,4294967294,17205,1107575608,4294928044,4294967294,
1204224,1247232,1290240,1333248,1376256,1419264,7870464,7913472,7913472,7913472,7913472,7913472,49152,53248,57344,61440,65536,69632,40960,45056,45056,45056,45056,45056,2964324352,320017685,1,0,80,0,192,1056915199,16780300,50465804,8064,36,128,1127612416,1127612416,1106247680,1127219200,1127219200,0,1473984,1073741824,241920,32768,32768,53248,7861248,1073741824,1290240,1127170861,1128284984,4294944108,174,1127170861,1128284984,4294944108,174,16864,1099973124,4294965260,4294967294,
8042496,8042496,8085504,8085504,8128512,8128512,8171520,8171520,8171520,8171520,8171520,8171520,40960,44032,48128,51200,55296,58368,62464,65536,62464,65536,62464,65536,268439552,336991254,1,0,80,0,192,1056914943,16780300,50465804,8064,36,224,1128005632,1128005632,1128005632,1098907648,1098907648,0,1516608,1073741824,1516032,44032,44032,40960,8088576,1073741824,8085504,17211,1107575614,4294926700,4294967294,17211,1107575614,4294926700,4294967294,17211,1107575614,4294926700,4294967294,
2107392,2107392,2150400,2150400,2193408,2193408,2236416,2236416,2236416,2236416,2236416,2236416,40960,44032,48128,51200,55296,58368,62464,65536,62464,65536,62464,65536,268439552,336991254,1,0,80,0,192,1040137983,16780300,50465804,8064,36,224,1112014848,1112014848,1112014848,1098907648,1098907648,0,403776,1073741824,403200,44032,44032,40960,2153472,1073741824,2150400,16964,1107575376,4294957612,4294967294,16964,1107575376,4294957612,4294967294,16964,1107575376,4294957612,4294967294,
4171776,4171776,4214784,4214784,4257792,4257792,4300800,4300800,4300800,4300800,4300800,4300800,40960,44032,48128,51200,55296,58368,62464,65536,62464,65536,62464,65536,268439552,336991254,1,0,80,0,192,1040137983,16780300,50465804,8064,36,224,1120141312,1120141312,1120141312,1098907648,1098907648,0,790848,1073741824,790272,44032,44032,40960,4217856,1073741824,4214784,17090,1107575496,4294946860,4294967294,17090,1107575496,4294946860,4294967294,17090,1107575496,4294946860,4294967294,
7488,7488,7680,7680,7872,7872,8064,8064,8064,8064,8064,8064,40960,44032,48128,51200,55296,58368,62464,65536,62464,65536,62464,65536,268439552,336991254,1,0,80,0,43008,1048526463,50465804,16780300,36,8064,224,1109393408,1109393408,1109393408,1098907648,1098907648,0,130464,1073741824,1440,44032,44032,40960,695808,1073741824,7680,16924,1107575336,4294959852,4294967294,16924,1107575336,4294959852,4294967294,16924,1107575336,4294959852,4294967294,
17088,17088,17280,17280,17472,17472,17664,17664,17664,17664,17664,17664,40960,44032,48128,51200,55296,58368,62464,65536,62464,65536,62464,65536,268439552,336991254,1,0,80,0,43008,1048526591,50465804,16780300,36,8064,224,1119092736,1119092736,1119092736,1098907648,1098907648,0,132264,1073741824,3240,44032,44032,40960,705408,1073741824,17280,17074,1107575480,4294948652,4294967294,17074,1107575480,4294948652,4294967294,17074,1107575480,4294948652,4294967294,
24768,24768,24960,24960,25152,25152,25344,25344,25344,25344,25344,25344,40960,44032,48128,51200,55296,58368,62464,65536,62464,65536,62464,65536,268439552,336991254,1,0,80,0,43008,1056914943,50465804,16780300,36,8064,224,1124204544,1124204544,1124204544,1098907648,1098907648,0,133704,1073741824,4680,44032,44032,40960,713088,1073741824,24960,17153,1107575556,4294939692,4294967294,17153,1107575556,4294939692,4294967294,17153,1107575556,4294939692,4294967294,
35136,35136,35328,35328,35520,35520,35712,35712,35712,35712,35712,35712,40960,44032,48128,51200,55296,58368,62464,65536,62464,65536,62464,65536,268439552,336991254,1,0,80,0,43008,1056915071,50465804,16780300,36,8064,224,1127743488,1127743488,1127743488,1098907648,1098907648,0,135648,1073741824,6624,44032,44032,40960,723456,1073741824,35328,17207,1107575610,4294927596,4294967294,17207,1107575610,4294927596,4294967294,17207,1107575610,4294927596,4294967294,
5806080,5806080,5849088,5849088,5892096,5892096,5935104,5935104,5935104,5935104,5935104,5935104,40960,44032,48128,51200,55296,58368,62464,65536,62464,65536,62464,65536,268439552,336991254,1,0,80,0,192,1056915199,16780300,50465804,8064,36,224,1124597760,1124597760,1124597760,1098907648,1098907648,0,1097280,1073741824,1096704,44032,44032,40960,5852160,1073741824,5849088,17159,1107575562,4294938348,4294967294,17159,1107575562,4294938348,4294967294,17159,1107575562,4294938348,4294967294,
8128512,8128512,8171520,8171520,8214528,8214528,8257536,8257536,8257536,8257536,8257536,8257536,40960,44032,48128,51200,55296,58368,62464,65536,62464,65536,62464,65536,268439552,336991254,1,0,80,0,192,1056915071,16780300,50465804,8064,36,224,1128136704,1128136704,1128136704,1098907648,1098907648,0,1532736,1073741824,1532160,44032,44032,40960,8174592,1073741824,8171520,17213,1107575616,4294926252,4294967294,17213,1107575616,4294926252,4294967294,17213,1107575616,4294926252,4294967294,
1591296,1634304,1677312,1720320,1763328,1806336,1849344,1892352,1892352,1892352,1892352,1892352,40960,45056,49152,53248,57344,61440,65536,69632,69632,69632,69632,69632,538976288,353703189,0,256,80,0,192,1040137983,16780300,50465804,8064,36,128,1109917696,1109393408,1108869120,1107296256,1107296256,1107296256,339840,323712,307584,57344,49152,40960,1812480,1726464,1640448,1106264596,1112031792,4294963660,30,1106264596,1112031792,4294963660,30,1106264596,1112031792,4294963660,30,
4257792,4257792,4300800,4300800,4343808,4343808,4386816,4386816,4386816,4386816,4386816,4386816,40960,44032,48128,51200,55296,58368,62464,65536,62464,65536,62464,65536,268439552,336991254,1,0,80,0,192,1040137983,16780300,50465804,8064,36,224,1120403456,1120403456,1120403456,1098907648,1098907648,0,806976,1073741824,806400,44032,44032,40960,4303872,1073741824,4300800,17094,1107575500,4294946412,4294967294,17094,1107575500,4294946412,4294967294,17094,1107575500,4294946412,4294967294,
3655680,3698688,3741696,3784704,3827712,3870720,3913728,3956736,3956736,3956736,3956736,3956736,40960,45056,49152,53248,57344,61440,65536,69632,69632,69632,69632,69632,538976288,353703189,0,256,80,0,192,1048526463,16780300,50465804,8064,36,128,1119092736,1118830592,1118568448,1107296256,1107296256,1107296256,726912,710784,694656,57344,49152,40960,3876864,3790848,3704832,1106264746,1112031928,4294957516,30,1106264746,1112031928,4294957516,30,1106264746,1112031928,4294957516,30,
4687872,4730880,4773888,4816896,4859904,4902912,4945920,4988928,4988928,4988928,4988928,4988928,40960,45056,49152,53248,57344,61440,65536,69632,69632,69632,69632,69632,538976288,353703189,0,256,80,0,192,1048526591,16780300,50465804,8064,36,128,1122238464,1121976320,1121714176,1107296256,1107296256,1107296256,920448,904320,888192,57344,49152,40960,4909056,4823040,4737024,1106264794,1112031976,4294954444,30,1106264794,1112031976,4294954444,30,1106264794,1112031976,4294954444,30,
25152,25152,25344,25344,25536,25536,25728,25728,25728,25728,25728,25728,40960,44032,48128,51200,55296,58368,62464,65536,62464,65536,62464,65536,268439552,336991254,1,0,80,0,43008,1056914943,50465804,16780300,36,8064,224,1124335616,1124335616,1124335616,1098907648,1098907648,0,133776,1073741824,4752,44032,44032,40960,713472,1073741824,25344,17155,1107575558,4294939244,4294967294,17155,1107575558,4294939244,4294967294,17155,1107575558,4294939244,4294967294,
6752256,6795264,6838272,6881280,6924288,6967296,7010304,7053312,7053312,7053312,7053312,7053312,40960,45056,49152,53248,57344,61440,65536,69632,69632,69632,69632,69632,538976288,353703189,0,256,80,0,192,1056915071,16780300,50465804,8064,36,128,1126301696,1126170624,1126039552,1107296256,1107296256,1107296256,1307520,1291392,1275264,57344,49152,40960,6973440,6887424,6801408,1106264861,1112032036,4294948300,30,1106264861,1112032036,4294948300,30,1106264861,1112032036,4294948300,30,
5892096,5892096,5935104,5935104,5978112,5978112,6021120,6021120,6021120,6021120,6021120,6021120,40960,44032,48128,51200,55296,58368,62464,65536,62464,65536,62464,65536,268439552,336991254,1,0,80,0,192,1056915199,16780300,50465804,8064,36,224,1124728832,1124728832,1124728832,1098907648,1098907648,0,1113408,1073741824,1112832,44032,44032,40960,5938176,1073741824,5935104,17161,1107575564,4294937900,4294967294,17161,1107575564,4294937900,4294967294,17161,1107575564,4294937900,4294967294,
8816640,8859648,8902656,8945664,8988672,9031680,9074688,9117696,9117696,9117696,9117696,9117696,40960,45056,49152,53248,57344,61440,65536,69632,69632,69632,69632,69632,538976288,353703189,0,256,80,0,192,1056915071,16780300,50465804,8064,36,128,1129447424,1129316352,1129185280,1107296256,1107296256,1107296256,1694592,1678464,1662336,57344,49152,40960,9037824,8951808,8865792,1106264909,1112032084,4294942156,30,1106264909,1112032084,4294942156,30,1106264909,1112032084,4294942156,30,
2193408,2193408,2236416,2236416,2279424,2279424,2322432,2322432,2322432,2322432,2322432,2322432,40960,44032,48128,51200,55296,58368,62464,65536,62464,65536,62464,65536,268439552,336991254,1,0,80,0,192,1040137983,16780300,50465804,8064,36,224,1112539136,1112539136,1112539136,1098907648,1098907648,0,419904,1073741824,419328,44032,44032,40960,2239488,1073741824,2236416,16972,1107575384,4294957164,4294967294,16972,1107575384,4294957164,4294967294,16972,1107575384,4294957164,4294967294,
2623488,2666496,2709504,2752512,2795520,2838528,2881536,2924544,2924544,2924544,2924544,2924544,40960,45056,49152,53248,57344,61440,65536,69632,69632,69632,69632,69632,538976288,353703189,0,256,80,0,192,1048526335,16780300,50465804,8064,36,128,1115947008,1115684864,1115160576,1107296256,1107296256,1107296256,533376,517248,501120,57344,49152,40960,2844672,2758656,2672640,1106264692,1112031880,4294960588,30,1106264692,1112031880,4294960588,30,1106264692,1112031880,4294960588,30,
7872,7872,8064,8064,8256,8256,8448,8448,8448,8448,8448,8448,40960,44032,48128,51200,55296,58368,62464,65536,62464,65536,62464,65536,268439552,336991254,1,0,80,0,43008,1048526463,50465804,16780300,36,8064,224,1109917696,1109917696,1109917696,1098907648,1098907648,0,130536,1073741824,1512,44032,44032,40960,696192,1073741824,8064,16932,1107575344,4294959404,4294967294,16932,1107575344,4294959404,4294967294,16932,1107575344,4294959404,4294967294,
17472,17472,17664,17664,17856,17856,18048,18048,18048,18048,18048,18048,40960,44032,48128,51200,55296,58368,62464,65536,62464,65536,62464,65536,268439552,336991254,1,0,80,0,43008,1048526591,50465804,16780300,36,8064,224,1119354880,1119354880,1119354880,1098907648,1098907648,0,132336,1073741824,3312,44032,44032,40960,705792,1073741824,17664,17078,1107575484,4294948204,4294967294,17078,1107575484,4294948204,4294967294,17078,1107575484,4294948204,4294967294,
5720064,5763072,5806080,5849088,5892096,5935104,5978112,6021120,6021120,6021120,6021120,6021120,40960,45056,49152,53248,57344,61440,65536,69632,69632,69632,69632,69632,538976288,353703189,0,256,80,0,192,1056914943,16780300,50465804,8064,36,128,1124728832,1124597760,1124466688,1107296256,1107296256,1107296256,1113984,1097856,1081728,57344,49152,40960,5941248,5855232,5769216,1106264837,1112032012,4294951372,30,1106264837,1112032012,4294951372,30,1106264837,1112032012,4294951372,30,
1032192,1075200,1118208,1161216,1204224,1247232,5720064,5763072,5763072,5763072,5763072,5763072,49152,53248,57344,61440,65536,69632,40960,45056,45056,45056,45056,45056,2694840320,320017685,1,0,80,0,192,1056915071,16780300,50465804,8064,36,128,1124335616,1124335616,1104150528,1126170624,1126170624,0,1070208,1073741824,209664,32768,32768,53248,5707776,1073741824,1118208,1126122230,1127236358,4294950604,158,1126122230,1127236358,4294950604,158,16832,1099973096,4294965772,4294967294,
7784448,7827456,7870464,7913472,7956480,7999488,8042496,8085504,8085504,8085504,8085504,8085504,40960,45056,49152,53248,57344,61440,65536,69632,69632,69632,69632,69632,538976288,353703189,0,256,80,0,192,1056915199,16780300,50465804,8064,36,128,1127874560,1127743488,1127612416,1107296256,1107296256,1107296256,1501056,1484928,1468800,57344,49152,40960,8005632,7919616,7833600,1106264885,1112032060,4294945228,30,1106264885,1112032060,4294945228,30,1106264885,1112032060,4294945228,30,
8214528,8214528,8257536,8257536,8300544,8300544,8343552,8343552,8343552,8343552,8343552,8343552,40960,44032,48128,51200,55296,58368,62464,65536,62464,65536,62464,65536,268439552,336991254,1,0,80,0,192,1056915071,16780300,50465804,8064,36,224,1128267776,1128267776,1128267776,1098907648,1098907648,0,1548864,1073741824,1548288,44032,44032,40960,8260608,1073741824,8257536,17215,1107575618,4294925804,4294967294,17215,1107575618,4294925804,4294967294,17215,1107575618,4294925804,4294967294,
2279424,2279424,2322432,2322432,2365440,2365440,2408448,2408448,2408448,2408448,2408448,2408448,40960,44032,48128,51200,55296,58368,62464,65536,62464,65536,62464,65536,268439552,336991254,1,0,80,0,192,1040137983,16780300,50465804,8064,36,224,1113063424,1113063424,1113063424,1098907648,1098907648,0,436032,1073741824,435456,44032,44032,40960,2325504,1073741824,2322432,16980,1107575392,4294956716,4294967294,16980,1107575392,4294956716,4294967294,16980,1107575392,4294956716,4294967294,
4343808,4343808,4386816,4386816,4429824,4429824,4472832,4472832,4472832,4472832,4472832,4472832,40960,44032,48128,51200,55296,58368,62464,65536,62464,65536,62464,65536,268439552,336991254,1,0,80,0,192,1048526335,16780300,50465804,8064,36,224,1120665600,1120665600,1120665600,1098907648,1098907648,0,823104,1073741824,822528,44032,44032,40960,4389888,1073741824,4386816,17098,1107575504,4294945964,4294967294,17098,1107575504,4294945964,4294967294,17098,1107575504,4294945964,4294967294,
8256,8256,8448,8448,8640,8640,8832,8832,8832,8832,8832,8832,40960,44032,48128,51200,55296,58368,62464,65536,62464,65536,62464,65536,268439552,336991254,1,0,80,0,43008,1048526463,50465804,16780300,36,8064,224,1110441984,1110441984,1110441984,1098907648,1098907648,0,130608,1073741824,1584,44032,44032,40960,696576,1073741824,8448,16940,1107575352,4294958956,4294967294,16940,1107575352,4294958956,4294967294,16940,1107575352,4294958956,4294967294,
17856,17856,18048,18048,18240,18240,18432,18432,18432,18432,18432,18432,40960,44032,48128,51200,55296,58368,62464,65536,62464,65536,62464,65536,268439552,336991254,1,0,80,0,43008,1048526591,50465804,16780300,36,8064,224,1119617024,1119617024,1119617024,1098907648,1098907648,0,132408,1073741824,3384,44032,44032,40960,706176,1073741824,18048,17082,1107575488,4294947756,4294967294,17082,1107575488,4294947756,4294967294,17082,1107575488,4294947756,4294967294,
25536,25536,25728,25728,25920,25920,26112,26112,26112,26112,26112,26112,40960,44032,48128,51200,55296,58368,62464,65536,62464,65536,62464,65536,268439552,336991254,1,0,80,0,43008,1056915071,50465804,16780300,36,8064,224,1124466688,1124466688,1124466688,1098907648,1098907648,0,133848,1073741824,4824,44032,44032,40960,713856,1073741824,25728,17157,1107575560,4294938796,4294967294,17157,1107575560,4294938796,4294967294,17157,1107575560,4294938796,4294967294,
35520,35520,35712,35712,35904,35904,36096,36096,36096,36096,36096,36096,40960,44032,48128,51200,55296,58368,62464,65536,62464,65536,62464,65536,268439552,336991254,1,0,80,0,43008,1056915199,50465804,16780300,36,8064,224,1127874560,1127874560,1127874560,1098907648,1098907648,0,135720,1073741824,6696,44032,44032,40960,723840,1073741824,35712,17209,1107575612,4294927148,4294967294,17209,1107575612,4294927148,4294967294,17209,1107575612,4294927148,4294967294,
5978112,5978112,6021120,6021120,6064128,6064128,6107136,6107136,6107136,6107136,6107136,6107136,40960,44032,48128,51200,55296,58368,62464,65536,62464,65536,62464,65536,268439552,336991254,1,0,80,0,192,1056915199,16780300,50465804,8064,36,224,1124859904,1124859904,1124859904,1098907648,1098907648,0,1129536,1073741824,1128960,44032,44032,40960,6024192,1073741824,6021120,17163,1107575566,4294937452,4294967294,17163,1107575566,4294937452,4294967294,17163,1107575566,4294937452,4294967294,
8300544,8300544,8343552,8343552,8386560,8386560,8429568,8429568,8429568,8429568,8429568,8429568,40960,44032,48128,51200,55296,58368,62464,65536,62464,65536,62464,65536,268439552,336991254,1,0,80,0,192,1056915071,16780300,50465804,8064,36,224,1128398848,1128398848,1128398848,1098907648,1098907648,0,1564992,1073741824,1564416,44032,44032,40960,8346624,1073741824,8343552,17217,1107575620,4294925356,4294967294,17217,1107575620,4294925356,4294967294,17217,1107575620,4294925356,4294967294,
2365440,2365440,2408448,2408448,2451456,2451456,2494464,2494464,2494464,2494464,2494464,2494464,40960,44032,48128,51200,55296,58368,62464,65536,62464,65536,62464,65536,268439552,336991254,1,0,80,0,192,1040137983,16780300,50465804,8064,36,224,1113587712,1113587712,1113587712,1098907648,1098907648,0,452160,1073741824,451584,44032,44032,40960,2411520,1073741824,2408448,16988,1107575400,4294956268,4294967294,16988,1107575400,4294956268,4294967294,16988,1107575400,4294956268,4294967294,
4429824,4429824,4472832,4472832,4515840,4515840,4558848,4558848,4558848,4558848,4558848,4558848,40960,44032,48128,51200,55296,58368,62464,65536,62464,65536,62464,65536,268439552,336991254,1,0,80,0,192,1048526335,16780300,50465804,8064,36,224,1120927744,1120927744,1120927744,1098907648,1098907648,0,839232,1073741824,838656,44032,44032,40960,4475904,1073741824,4472832,17102,1107575508,4294945516,4294967294,17102,1107575508,4294945516,4294967294,17102,1107575508,4294945516,4294967294,
8640,8640,8832,8832,9024,9024,9216,9216,9216,9216,9216,9216,40960,44032,48128,51200,55296,58368,62464,65536,62464,65536,62464,65536,268439552,336991254,1,0,80,0,43008,1048526463,50465804,16780300,36,8064,224,1110966272,1110966272,1110966272,1098907648,1098907648,0,130680,1073741824,1656,44032,44032,40960,696960,1073741824,8832,16948,1107575360,4294958508,4294967294,16948,1107575360,4294958508,4294967294,16948,1107575360,4294958508,4294967294,
18240,18240,18432,18432,18624,18624,18816,18816,18816,18816,18816,18816,40960,44032,48128,51200,55296,58368,62464,65536,62464,65536,62464,65536,268439552,336991254,1,0,80,0,43008,1048526591,50465804,16780300,36,8064,224,1119879168,1119879168,1119879168,1098907648,1098907648,0,132480,1073741824,3456,44032,44032,40960,706560,1073741824,18432,17086,1107575492,4294947308,4294967294,17086,1107575492,4294947308,4294967294,17086,1107575492,4294947308,4294967294,
25920,25920,26112,26112,26304,26304,26496,26496,26496,26496,26496,26496,40960,44032,48128,51200,55296,58368,62464,65536,62464,65536,62464,65536,268439552,336991254,1,0,80,0,43008,1056915071,50465804,16780300,36,8064,224,1124597760,1124597760,1124597760,1098907648,1098907648,0,133920,1073741824,4896,44032,44032,40960,714240,1073741824,26112,17159,1107575562,4294938348,4294967294,17159,1107575562,4294938348,4294967294,17159,1107575562,4294938348,4294967294,
35904,35904,36096,36096,36288,36288,36480,36480,36480,36480,36480,36480,40960,44032,48128,51200,55296,58368,62464,65536,62464,65536,62464,65536,268439552,336991254,1,0,80,0,43008,1056915199,50465804,16780300,36,8064,224,1128005632,1128005632,1128005632,1098907648,1098907648,0,135792,1073741824,6768,44032,44032,40960,724224,1073741824,36096,17211,1107575614,4294926700,4294967294,17211,1107575614,4294926700,4294967294,17211,1107575614,4294926700,4294967294,
6064128,6064128,6107136,6107136,6150144,6150144,6193152,6193152,6193152,6193152,6193152,6193152,40960,44032,48128,51200,55296,58368,62464,65536,62464,65536,62464,65536,268439552,336991254,1,0,80,0,192,1056915199,16780300,50465804,8064,36,224,1124990976,1124990976,1124990976,1098907648,1098907648,0,1145664,1073741824,1145088,44032,44032,40960,6110208,1073741824,6107136,17165,1107575568,4294937004,4294967294,17165,1107575568,4294937004,4294967294,17165,1107575568,4294937004,4294967294,
8386560,8386560,8429568,8429568,8472576,8472576,8515584,8515584,8515584,8515584,8515584,8515584,40960,44032,48128,51200,55296,58368,62464,65536,62464,65536,62464,65536,268439552,336991254,1,0,80,0,192,1056915071,16780300,50465804,8064,36,224,1128529920,1128529920,1128529920,1098907648,1098907648,0,1581120,1073741824,1580544,44032,44032,40960,8432640,1073741824,8429568,17219,1107575622,4294924908,4294967294,17219,1107575622,4294924908,4294967294,17219,1107575622,4294924908,4294967294,
2451456,2451456,2494464,2494464,2537472,2537472,2580480,2580480,2580480,2580480,2580480,2580480,40960,44032,48128,51200,55296,58368,62464,65536,62464,65536,62464,65536,268439552,336991254,1,0,80,0,192,1040137983,16780300,50465804,8064,36,224,1114112000,1114112000,1114112000,1098907648,1098907648,0,468288,1073741824,467712,44032,44032,40960,2497536,1073741824,2494464,16996,1107575408,4294955820,4294967294,16996,1107575408,4294955820,4294967294,16996,1107575408,4294955820,4294967294,
4515840,4515840,4558848,4558848,4601856,4601856,4644864,4644864,4644864,4644864,4644864,4644864,40960,44032,48128,51200,55296,58368,62464,65536,62464,65536,62464,65536,268439552,336991254,1,0,80,0,192,1048526335,16780300,50465804,8064,36,224,1121189888,1121189888,1121189888,1098907648,1098907648,0,855360,1073741824,854784,44032,44032,40960,4561920,1073741824,4558848,17106,1107575512,4294945068,4294967294,17106,1107575512,4294945068,4294967294,17106,1107575512,4294945068,4294967294,
516096,559104,602112,645120,688128,731136,5720064,5763072,5763072,5763072,5763072,5763072,49152,53248,57344,61440,65536,69632,40960,45056,45056,45056,45056,45056,1616904192,320017685,1,512,80,0,192,1048526463,16780300,50465804,8064,36,128,1124335616,1124335616,1096810496,1119879168,1119879168,0,1067904,1073741824,112896,32768,32768,53248,5695488,1073741824,602112,1119765238,1121993478,4294950988,94,1119765238,1121993478,4294950988,94,16704,1099973000,12,4294967294,
18624,18624,18816,18816,19008,19008,19200,19200,19200,19200,19200,19200,40960,44032,48128,51200,55296,58368,62464,65536,62464,65536,62464,65536,268439552,336991254,1,0,80,0,43008,1048526591,50465804,16780300,36,8064,224,1120141312,1120141312,1120141312,1098907648,1098907648,0,132552,1073741824,3528,44032,44032,40960,706944,1073741824,18816,17090,1107575496,4294946860,4294967294,17090,1107575496,4294946860,4294967294,17090,1107575496,4294946860,4294967294,
26304,26304,26496,26496,26688,26688,26880,26880,26880,26880,26880,26880,40960,44032,48128,51200,55296,58368,62464,65536,62464,65536,62464,65536,268439552,336991254,1,0,80,0,43008,1056915071,50465804,16780300,36,8064,224,1124728832,1124728832,1124728832,1098907648,1098907648,0,133992,1073741824,4968,44032,44032,40960,714624,1073741824,26496,17161,1107575564,4294937900,4294967294,17161,1107575564,4294937900,4294967294,17161,1107575564,4294937900,4294967294,
36288,36288,36480,36480,36672,36672,36864,36864,36864,36864,36864,36864,40960,44032,48128,51200,55296,58368,62464,65536,62464,65536,62464,65536,268439552,336991254,1,0,80,0,43008,1056915199,50465804,16780300,36,8064,224,1128136704,1128136704,1128136704,1098907648,1098907648,0,135864,1073741824,6840,44032,44032,40960,724608,1073741824,36480,17213,1107575616,4294926252,4294967294,17213,1107575616,4294926252,4294967294,17213,1107575616,4294926252,4294967294,
6150144,6150144,6193152,6193152,6236160,6236160,6279168,6279168,6279168,6279168,6279168,6279168,40960,44032,48128,51200,55296,58368,62464,65536,62464,65536,62464,65536,268439552,336991254,1,0,80,0,192,1056914943,16780300,50465804,8064,36,224,1125122048,1125122048,1125122048,1098907648,1098907648,0,1161792,1073741824,1161216,44032,44032,40960,6196224,1073741824,6193152,17167,1107575570,4294936556,4294967294,17167,1107575570,4294936556,4294967294,17167,1107575570,4294936556,4294967294,
8472576,8472576,8515584,8515584,8558592,8558592,8601600,8601600,8601600,8601600,8601600,8601600,40960,44032,48128,51200,55296,58368,62464,65536,62464,65536,62464,65536,268439552,336991254,1,0,80,0,192,1056915071,16780300,50465804,8064,36,224,1128660992,1128660992,1128660992,1098907648,1098907648,0,1597248,1073741824,1596672,44032,44032,40960,8518656,1073741824,8515584,17221,1107575624,4294924460,4294967294,17221,1107575624,4294924460,4294967294,17221,1107575624,4294924460,4294967294,
2537472,2537472,2580480,2580480,2623488,2623488,2666496,2666496,2666496,2666496,2666496,2666496,40960,44032,48128,51200,55296,58368,62464,65536,62464,65536,62464,65536,268439552,336991254,1,0,80,0,192,1040137983,16780300,50465804,8064,36,224,1114636288,1114636288,1114636288,1098907648,1098907648,0,484416,1073741824,483840,44032,44032,40960,2583552,1073741824,2580480,17004,1107575416,4294955372,4294967294,17004,1107575416,4294955372,4294967294,17004,1107575416,4294955372,4294967294,
4601856,4601856,4644864,4644864,4687872,4687872,4730880,4730880,4730880,4730880,4730880,4730880,40960,44032,48128,51200,55296,58368,62464,65536,62464,65536,62464,65536,268439552,336991254,1,0,80,0,192,1048526463,16780300,50465804,8064,36,224,1121452032,1121452032,1121452032,1098907648,1098907648,0,871488,1073741824,870912,44032,44032,40960,4647936,1073741824,4644864,17110,1107575516,4294944620,4294967294,17110,1107575516,4294944620,4294967294,17110,1107575516,4294944620,4294967294,
9024,9024,9216,9216,9408,9408,9600,9600,9600,9600,9600,9600,40960,44032,48128,51200,55296,58368,62464,65536,62464,65536,62464,65536,268439552,336991254,1,0,80,0,43008,1048526591,50465804,16780300,36,8064,224,1111490560,1111490560,1111490560,1098907648,1098907648,0,130752,1073741824,1728,44032,44032,40960,697344,1073741824,9216,16956,1107575368,4294958060,4294967294,16956,1107575368,4294958060,4294967294,16956,1107575368,4294958060,4294967294,
19008,19008,19200,19200,19392,19392,19584,19584,19584,19584,19584,19584,40960,44032,48128,51200,55296,58368,62464,65536,62464,65536,62464,65536,268439552,336991254,1,0,80,0,43008,1048526591,50465804,16780300,36,8064,224,1120403456,1120403456,1120403456,1098907648,1098907648,0,132624,1073741824,3600,44032,44032,40960,707328,1073741824,19200,17094,1107575500,4294946412,4294967294,17094,1107575500,4294946412,4294967294,17094,1107575500,4294946412,4294967294,
26688,26688,26880,26880,27072,27072,27264,27264,27264,27264,27264,27264,40960,44032,48128,51200,55296,58368,62464,65536,62464,65536,62464,65536,268439552,336991254,1,0,80,0,43008,1056915071,50465804,16780300,36,8064,224,1124859904,1124859904,1124859904,1098907648,1098907648,0,134064,1073741824,5040,44032,44032,40960,715008,1073741824,26880,17163,1107575566,4294937452,4294967294,17163,1107575566,4294937452,4294967294,17163,1107575566,4294937452,4294967294,
36672,36672,36864,36864,37056,37056,37248,37248,37248,37248,37248,37248,40960,44032,48128,51200,55296,58368,62464,65536,62464,65536,62464,65536,268439552,336991254,1,0,80,0,43008,1056915199,50465804,16780300,36,8064,224,1128267776,1128267776,1128267776,1098907648,1098907648,0,135936,1073741824,6912,44032,44032,40960,724992,1073741824,36864,17215,1107575618,4294925804,4294967294,17215,1107575618,4294925804,4294967294,17215,1107575618,4294925804,4294967294,
6236160,6236160,6279168,6279168,6322176,6322176,6365184,6365184,6365184,6365184,6365184,6365184,40960,44032,48128,51200,55296,58368,62464,65536,62464,65536,62464,65536,268439552,336991254,1,0,80,0,192,1056914943,16780300,50465804,8064,36,224,1125253120,1125253120,1125253120,1098907648,1098907648,0,1177920,1073741824,1177344,44032,44032,40960,6282240,1073741824,6279168,17169,1107575572,4294936108,4294967294,17169,1107575572,4294936108,4294967294,17169,1107575572,4294936108,4294967294,
8558592,8558592,8601600,8601600,8644608,8644608,8687616,8687616,8687616,8687616,8687616,8687616,40960,44032,48128,51200,55296,58368,62464,65536,62464,65536,62464,65536,268439552,336991254,1,0,80,0,192,1056915071,16780300,50465804,8064,36,224,1128792064,1128792064,1128792064,1098907648,1098907648,0,1613376,1073741824,1612800,44032,44032,40960,8604672,1073741824,8601600,17223,1107575626,4294924012,4294967294,17223,1107575626,4294924012,4294967294,17223,1107575626,4294924012,4294967294,
86016,129024,172032,215040,258048,301056,8558592,8601600,8601600,8601600,8601600,8601600,49152,53248,57344,61440,65536,69632,40960,45056,45056,45056,45056,45056,808452096,320017685,1,512,80,0,192,1040137983,16780300,50465804,8064,36,128,1128660992,1128660992,1082130432,1111490560,1111490560,0,1598400,1073741824,32256,32768,32768,53248,8524800,1073741824,172032,1111245629,1115702088,4294942828,46,1111245629,1115702088,4294942828,46,16384,1099972832,1292,4294967294,
4687872,4687872,4730880,4730880,4773888,4773888,4816896,4816896,4816896,4816896,4816896,4816896,40960,44032,48128,51200,55296,58368,62464,65536,62464,65536,62464,65536,268439552,336991254,1,0,80,0,192,1048526463,16780300,50465804,8064,36,224,1121714176,1121714176,1121714176,1098907648,1098907648,0,887616,1073741824,887040,44032,44032,40960,4733952,1073741824,4730880,17114,1107575520,4294944172,4294967294,17114,1107575520,4294944172,4294967294,17114,1107575520,4294944172,4294967294,
9408,9408,9600,9600,9792,9792,9984,9984,9984,9984,9984,9984,40960,44032,48128,51200,55296,58368,62464,65536,62464,65536,62464,65536,268439552,336991254,1,0,80,0,43008,1048526591,50465804,16780300,36,8064,224,1112014848,1112014848,1112014848,1098907648,1098907648,0,130824,1073741824,1800,44032,44032,40960,697728,1073741824,9600,16964,1107575376,4294957612,4294967294,16964,1107575376,4294957612,4294967294,16964,1107575376,4294957612,4294967294,
19392,19392,19584,19584,19776,19776,19968,19968,19968,19968,19968,19968,40960,44032,48128,51200,55296,58368,62464,65536,62464,65536,62464,65536,268439552,336991254,1,0,80,0,43008,1048526591,50465804,16780300,36,8064,224,1120665600,1120665600,1120665600,1098907648,1098907648,0,132696,1073741824,3672,44032,44032,40960,707712,1073741824,19584,17098,1107575504,4294945964,4294967294,17098,1107575504,4294945964,4294967294,17098,1107575504,4294945964,4294967294,
27072,27072,27264,27264,27456,27456,27648,27648,27648,27648,27648,27648,40960,44032,48128,51200,55296,58368,62464,65536,62464,65536,62464,65536,268439552,336991254,1,0,80,0,43008,1056915071,50465804,16780300,36,8064,224,1124990976,1124990976,1124990976,1098907648,1098907648,0,134136,1073741824,5112,44032,44032,40960,715392,1073741824,27264,17165,1107575568,4294937004,4294967294,17165,1107575568,4294937004,4294967294,17165,1107575568,4294937004,4294967294,
37056,37056,37248,37248,37440,37440,37632,37632,37632,37632,37632,37632,40960,44032,48128,51200,55296,58368,62464,65536,62464,65536,62464,65536,268439552,336991254,1,0,80,0,43008,1056915199,50465804,16780300,36,8064,224,1128398848,1128398848,1128398848,1098907648,1098907648,0,136008,1073741824,6984,44032,44032,40960,725376,1073741824,37248,17217,1107575620,4294925356,4294967294,17217,1107575620,4294925356,4294967294,17217,1107575620,4294925356,4294967294,
6322176,6322176,6365184,6365184,6408192,6408192,6451200,6451200,6451200,6451200,6451200,6451200,40960,44032,48128,51200,55296,58368,62464,65536,62464,65536,62464,65536,268439552,336991254,1,0,80,0,192,1056914943,16780300,50465804,8064,36,224,1125384192,1125384192,1125384192,1098907648,1098907648,0,1194048,1073741824,1193472,44032,44032,40960,6368256,1073741824,6365184,17171,1107575574,4294935660,4294967294,17171,1107575574,4294935660,4294967294,17171,1107575574,4294935660,4294967294,
8644608,8644608,8687616,8687616,8730624,8730624,8773632,8773632,8773632,8773632,8773632,8773632,40960,44032,48128,51200,55296,58368,62464,65536,62464,65536,62464,65536,268439552,336991254,1,0,80,0,192,1056915071,16780300,50465804,8064,36,224,1128923136,1128923136,1128923136,1098907648,1098907648,0,1629504,1073741824,1628928,44032,44032,40960,8690688,1073741824,8687616,17225,1107575628,4294923564,4294967294,17225,1107575628,4294923564,4294967294,17225,1107575628,4294923564,4294967294,
2623488,2623488,2666496,2666496,2709504,2709504,2752512,2752512,2752512,2752512,2752512,2752512,40960,44032,48128,51200,55296,58368,62464,65536,62464,65536,62464,65536,268439552,336991254,1,0,80,0,192,1040137983,16780300,50465804,8064,36,224,1115160576,1115160576,1115160576,1098907648,1098907648,0,500544,1073741824,499968,44032,44032,40960,2669568,1073741824,2666496,17012,1107575424,4294954924,4294967294,17012,1107575424,4294954924,4294967294,17012,1107575424,4294954924,4294967294,
4773888,4773888,4816896,4816896,4859904,4859904,4902912,4902912,4902912,4902912,4902912,4902912,40960,44032,48128,51200,55296,58368,62464,65536,62464,65536,62464,65536,268439552,336991254,1,0,80,0,192,1048526463,16780300,50465804,8064,36,224,1121976320,1121976320,1121976320,1098907648,1098907648,0,903744,1073741824,903168,44032,44032,40960,4819968,1073741824,4816896,17118,1107575524,4294943724,4294967294,17118,1107575524,4294943724,4294967294,17118,1107575524,4294943724,4294967294,
9792,9792,9984,9984,10176,10176,10368,10368,10368,10368,10368,10368,40960,44032,48128,51200,55296,58368,62464,65536,62464,65536,62464,65536,268439552,336991254,1,0,80,0,43008,1048526591,50465804,16780300,36,8064,224,1112539136,1112539136,1112539136,1098907648,1098907648,0,130896,1073741824,1872,44032,44032,40960,698112,1073741824,9984,16972,1107575384,4294957164,4294967294,16972,1107575384,4294957164,4294967294,16972,1107575384,4294957164,4294967294,
19776,19776,19968,19968,20160,20160,20352,20352,20352,20352,20352,20352,40960,44032,48128,51200,55296,58368,62464,65536,62464,65536,62464,65536,268439552,336991254,1,0,80,0,43008,1048526335,50465804,16780300,36,8064,224,1120927744,1120927744,1120927744,1098907648,1098907648,0,132768,1073741824,3744,44032,44032,40960,708096,1073741824,19968,17102,1107575508,4294945516,4294967294,17102,1107575508,4294945516,4294967294,17102,1107575508,4294945516,4294967294,
27456,27456,27648,27648,27840,27840,28032,28032,28032,28032,28032,28032,40960,44032,48128,51200,55296,58368,62464,65536,62464,65536,62464,65536,268439552,336991254,1,0,80,0,43008,1056915071,50465804,16780300,36,8064,224,1125122048,1125122048,1125122048,1098907648,1098907648,0,134208,1073741824,5184,44032,44032,40960,715776,1073741824,27648,17167,1107575570,4294936556,4294967294,17167,1107575570,4294936556,4294967294,17167,1107575570,4294936556,4294967294,
37440,37440,37632,37632,37824,37824,38016,38016,38016,38016,38016,38016,40960,44032,48128,51200,55296,58368,62464,65536,62464,65536,62464,65536,268439552,336991254,1,0,80,0,43008,1056915199,50465804,16780300,36,8064,224,1128529920,1128529920,1128529920,1098907648,1098907648,0,136080,1073741824,7056,44032,44032,40960,725760,1073741824,37632,17219,1107575622,4294924908,4294967294,17219,1107575622,4294924908,4294967294,17219,1107575622,4294924908,4294967294,
6408192,6408192,6451200,6451200,6494208,6494208,6537216,6537216,6537216,6537216,6537216,6537216,40960,44032,48128,51200,55296,58368,62464,65536,62464,65536,62464,65536,268439552,336991254,1,0,80,0,192,1056914943,16780300,50465804,8064,36,224,1125515264,1125515264,1125515264,1098907648,1098907648,0,1210176,1073741824,1209600,44032,44032,40960,6454272,1073741824,6451200,17173,1107575576,4294935212,4294967294,17173,1107575576,4294935212,4294967294,17173,1107575576,4294935212,4294967294,
9074688,9117696,9160704,9203712,9246720,9289728,9332736,9375744,9375744,9375744,9375744,9375744,40960,45056,49152,53248,57344,61440,65536,69632,69632,69632,69632,69632,538976288,353703189,0,256,80,0,192,1056915199,16780300,50465804,8064,36,128,1129840640,1129709568,1129578496,1107296256,1107296256,1107296256,1742976,1726848,1710720,57344,49152,40960,9295872,9209856,9123840,1106264915,1112032090,4294941388,30,1106264915,1112032090,4294941388,30,1106264915,1112032090,4294941388,30,
1849344,1892352,1935360,1978368,2021376,2064384,2107392,2150400,2150400,2150400,2150400,2150400,40960,45056,49152,53248,57344,61440,65536,69632,69632,69632,69632,69632,538976288,353703189,0,256,80,0,192,1040137983,16780300,50465804,8064,36,128,1111490560,1110966272,1110441984,1107296256,1107296256,1107296256,388224,372096,355968,57344,49152,40960,2070528,1984512,1898496,1106264620,1112031816,4294962892,30,1106264620,1112031816,4294962892,30,1106264620,1112031816,4294962892,30,
4859904,4859904,4902912,4902912,4945920,4945920,4988928,4988928,4988928,4988928,4988928,4988928,40960,44032,48128,51200,55296,58368,62464,65536,62464,65536,62464,65536,268439552,336991254,1,0,80,0,192,1048526463,16780300,50465804,8064,36,224,1122238464,1122238464,1122238464,1098907648,1098907648,0,919872,1073741824,919296,44032,44032,40960,4905984,1073741824,4902912,17122,1107575528,4294943276,4294967294,17122,1107575528,4294943276,4294967294,17122,1107575528,4294943276,4294967294,
3913728,3956736,3999744,4042752,4085760,4128768,4171776,4214784,4214784,4214784,4214784,4214784,40960,45056,49152,53248,57344,61440,65536,69632,69632,69632,69632,69632,538976288,353703189,0,256,80,0,192,1048526591,16780300,50465804,8064,36,128,1119879168,1119617024,1119354880,1107296256,1107296256,1107296256,775296,759168,743040,57344,49152,40960,4134912,4048896,3962880,1106264758,1112031940,4294956748,30,1106264758,1112031940,4294956748,30,1106264758,1112031940,4294956748,30,
4945920,4988928,5031936,5074944,5117952,5160960,5203968,5246976,5246976,5246976,5246976,5246976,40960,45056,49152,53248,57344,61440,65536,69632,69632,69632,69632,69632,538976288,353703189,0,256,80,0,192,1048526335,16780300,50465804,8064,36,128,1123024896,1122762752,1122500608,1107296256,1107296256,1107296256,968832,952704,936576,57344,49152,40960,5167104,5081088,4995072,1106264806,1112031988,4294953676,30,1106264806,1112031988,4294953676,30,1106264806,1112031988,4294953676,30,
27840,27840,28032,28032,28224,28224,28416,28416,28416,28416,28416,28416,40960,44032,48128,51200,55296,58368,62464,65536,62464,65536,62464,65536,268439552,336991254,1,0,80,0,43008,1056915071,50465804,16780300,36,8064,224,1125253120,1125253120,1125253120,1098907648,1098907648,0,134280,1073741824,5256,44032,44032,40960,716160,1073741824,28032,17169,1107575572,4294936108,4294967294,17169,1107575572,4294936108,4294967294,17169,1107575572,4294936108,4294967294,
7010304,7053312,7096320,7139328,7182336,7225344,7268352,7311360,7311360,7311360,7311360,7311360,40960,45056,49152,53248,57344,61440,65536,69632,69632,69632,69632,69632,538976288,353703189,0,256,80,0,192,1056915199,16780300,50465804,8064,36,128,1126694912,1126563840,1126432768,1107296256,1107296256,1107296256,1355904,1339776,1323648,57344,49152,40960,7231488,7145472,7059456,1106264867,1112032042,4294947532,30,1106264867,1112032042,4294947532,30,1106264867,1112032042,4294947532,30,
6494208,6494208,6537216,6537216,6580224,6580224,6623232,6623232,6623232,6623232,6623232,6623232,40960,44032,48128,51200,55296,58368,62464,65536,62464,65536,62464,65536,268439552,336991254,1,0,80,0,192,1056915071,16780300,50465804,8064,36,224,1125646336,1125646336,1125646336,1098907648,1098907648,0,1226304,1073741824,1225728,44032,44032,40960,6540288,1073741824,6537216,17175,1107575578,4294934764,4294967294,17175,1107575578,4294934764,4294967294,17175,1107575578,4294934764,4294967294,
8730624,8730624,8773632,8773632,8816640,8816640,8859648,8859648,8859648,8859648,8859648,8859648,40960,44032,48128,51200,55296,58368,62464,65536,62464,65536,62464,65536,268439552,336991254,1,0,80,0,192,1056915199,16780300,50465804,8064,36,224,1129054208,1129054208,1129054208,1098907648,1098907648,0,1645632,1073741824,1645056,44032,44032,40960,8776704,1073741824,8773632,17227,1107575630,4294923116,4294967294,17227,1107575630,4294923116,4294967294,17227,1107575630,4294923116,4294967294,
2709504,2709504,2752512,2752512,2795520,2795520,2838528,2838528,2838528,2838528,2838528,2838528,40960,44032,48128,51200,55296,58368,62464,65536,62464,65536,62464,65536,268439552,336991254,1,0,80,0,192,1040137983,16780300,50465804,8064,36,224,1115684864,1115684864,1115684864,1098907648,1098907648,0,516672,1073741824,516096,44032,44032,40960,2755584,1073741824,2752512,17020,1107575428,4294954476,4294967294,17020,1107575428,4294954476,4294967294,17020,1107575428,4294954476,4294967294,
2881536,2924544,2967552,3010560,3053568,3096576,3139584,3182592,3182592,3182592,3182592,3182592,40960,45056,49152,53248,57344,61440,65536,69632,69632,69632,69632,69632,538976288,353703189,0,256,80,0,192,1048526463,16780300,50465804,8064,36,128,1116733440,1116471296,1116209152,1107296256,1107296256,1107296256,581760,565632,549504,57344,49152,40960,3102720,3016704,2930688,1106264710,1112031892,4294959820,30,1106264710,1112031892,4294959820,30,1106264710,1112031892,4294959820,30,
10176,10176,10368,10368,10560,10560,10752,10752,10752,10752,10752,10752,40960,44032,48128,51200,55296,58368,62464,65536,62464,65536,62464,65536,268439552,336991254,1,0,80,0,43008,1048526591,50465804,16780300,36,8064,224,1113063424,1113063424,1113063424,1098907648,1098907648,0,130968,1073741824,1944,44032,44032,40960,698496,1073741824,10368,16980,1107575392,4294956716,4294967294,16980,1107575392,4294956716,4294967294,16980,1107575392,4294956716,4294967294,
20160,20160,20352,20352,20544,20544,20736,20736,20736,20736,20736,20736,40960,44032,48128,51200,55296,58368,62464,65536,62464,65536,62464,65536,268439552,336991254,1,0,80,0,43008,1048526335,50465804,16780300,36,8064,224,1121189888,1121189888,1121189888,1098907648,1098907648,0,132840,1073741824,3816,44032,44032,40960,708480,1073741824,20352,17106,1107575512,4294945068,4294967294,17106,1107575512,4294945068,4294967294,17106,1107575512,4294945068,4294967294,
5978112,6021120,6064128,6107136,6150144,6193152,6236160,6279168,6279168,6279168,6279168,6279168,40960,45056,49152,53248,57344,61440,65536,69632,69632,69632,69632,69632,538976288,353703189,0,256,80,0,192,1056915071,16780300,50465804,8064,36,128,1125122048,1124990976,1124859904,1107296256,1107296256,1107296256,1162368,1146240,1130112,57344,49152,40960,6199296,6113280,6027264,1106264843,1112032018,4294950604,30,1106264843,1112032018,4294950604,30,1106264843,1112032018,4294950604,30,
1118208,1161216,1204224,1247232,1290240,1333248,8558592,8601600,8601600,8601600,8601600,8601600,49152,53248,57344,61440,65536,69632,40960,45056,45056,45056,45056,45056,2694840320,320017685,1,0,80,0,192,1056915199,16780300,50465804,8064,36,128,1128660992,1128660992,1105199104,1126170624,1126170624,0,1602432,1073741824,225792,32768,32768,53248,8546304,1073741824,1204224,1126122301,1127236424,4294942156,158,1126122301,1127236424,4294942156,158,16848,1099973112,4294965516,4294967294,
8042496,8085504,8128512,8171520,8214528,8257536,8300544,8343552,8343552,8343552,8343552,8343552,40960,45056,49152,53248,57344,61440,65536,69632,69632,69632,69632,69632,538976288,353703189,0,256,80,0,192,1056915071,16780300,50465804,8064,36,128,1128267776,1128136704,1128005632,1107296256,1107296256,1107296256,1549440,1533312,1517184,57344,49152,40960,8263680,8177664,8091648,1106264891,1112032066,4294944460,30,1106264891,1112032066,4294944460,30,1106264891,1112032066,4294944460,30,
8816640,8816640,8859648,8859648,8902656,8902656,8945664,8945664,8945664,8945664,8945664,8945664,40960,44032,48128,51200,55296,58368,62464,65536,62464,65536,62464,65536,268439552,336991254,1,0,80,0,192,1056915199,16780300,50465804,8064,36,224,1129185280,1129185280,1129185280,1098907648,1098907648,0,1661760,1073741824,1661184,44032,44032,40960,8862720,1073741824,8859648,17229,1107575632,4294922668,4294967294,17229,1107575632,4294922668,4294967294,17229,1107575632,4294922668,4294967294,
2795520,2795520,2838528,2838528,2881536,2881536,2924544,2924544,2924544,2924544,2924544,2924544,40960,44032,48128,51200,55296,58368,62464,65536,62464,65536,62464,65536,268439552,336991254,1,0,80,0,192,1040137727,16780300,50465804,8064,36,224,1115947008,1115947008,1115947008,1098907648,1098907648,0,532800,1073741824,532224,44032,44032,40960,2841600,1073741824,2838528,17026,1107575432,4294954028,4294967294,17026,1107575432,4294954028,4294967294,17026,1107575432,4294954028,4294967294,
4945920,4945920,4988928,4988928,5031936,5031936,5074944,5074944,5074944,5074944,5074944,5074944,40960,44032,48128,51200,55296,58368,62464,65536,62464,65536,62464,65536,268439552,336991254,1,0,80,0,192,1048526463,16780300,50465804,8064,36,224,1122500608,1122500608,1122500608,1098907648,1098907648,0,936000,1073741824,935424,44032,44032,40960,4992000,1073741824,4988928,17126,1107575532,4294942828,4294967294,17126,1107575532,4294942828,4294967294,17126,1107575532,4294942828,4294967294,
10560,10560,10752,10752,10944,10944,11136,11136,11136,11136,11136,11136,40960,44032,48128,51200,55296,58368,62464,65536,62464,65536,62464,65536,268439552,336991254,1,0,80,0,43008,1048526591,50465804,16780300,36,8064,224,1113587712,1113587712,1113587712,1098907648,1098907648,0,131040,1073741824,2016,44032,44032,40960,698880,1073741824,10752,16988,1107575400,4294956268,4294967294,16988,1107575400,4294956268,4294967294,16988,1107575400,4294956268,4294967294,
688128,731136,774144,817152,860160,903168,2881536,2924544,2924544,2924544,2924544,2924544,49152,53248,57344,61440,65536,69632,40960,45056,45056,45056,45056,45056,2155872256,320017685,1,0,80,0,192,1048526335,16780300,50465804,8064,36,128,1115947008,1115947008,1099956224,1124073472,1124073472,0,536832,1073741824,145152,32768,32768,53248,2863104,1073741824,774144,1123959396,1125139080,4294959244,126,1123959396,1125139080,4294959244,126,16768,1099973032,4294966796,4294967294,
28224,28224,28416,28416,28608,28608,28800,28800,28800,28800,28800,28800,40960,44032,48128,51200,55296,58368,62464,65536,62464,65536,62464,65536,268439552,336991254,1,0,80,0,43008,1056915199,50465804,16780300,36,8064,224,1125384192,1125384192,1125384192,1098907648,1098907648,0,134352,1073741824,5328,44032,44032,40960,716544,1073741824,28416,17171,1107575574,4294935660,4294967294,17171,1107575574,4294935660,4294967294,17171,1107575574,4294935660,4294967294,
37824,37824,38016,38016,38208,38208,38400,38400,38400,38400,38400,38400,40960,44032,48128,51200,55296,58368,62464,65536,62464,65536,62464,65536,268439552,336991254,1,0,80,0,43008,1056915199,50465804,16780300,36,8064,224,1128660992,1128660992,1128660992,1098907648,1098907648,0,136152,1073741824,7128,44032,44032,40960,726144,1073741824,38016,17221,1107575624,4294924460,4294967294,17221,1107575624,4294924460,4294967294,17221,1107575624,4294924460,4294967294,
6580224,6580224,6623232,6623232,6666240,6666240,6709248,6709248,6709248,6709248,6709248,6709248,40960,44032,48128,51200,55296,58368,62464,65536,62464,65536,62464,65536,268439552,336991254,1,0,80,0,192,1056915071,16780300,50465804,8064,36,224,1125777408,1125777408,1125777408,1098907648,1098907648,0,1242432,1073741824,1241856,44032,44032,40960,6626304,1073741824,6623232,17177,1107575580,4294934316,4294967294,17177,1107575580,4294934316,4294967294,17177,1107575580,4294934316,4294967294,
8902656,8902656,8945664,8945664,8988672,8988672,9031680,9031680,9031680,9031680,9031680,9031680,40960,44032,48128,51200,55296,58368,62464,65536,62464,65536,62464,65536,268439552,336991254,1,0,80,0,192,1056915199,16780300,50465804,8064,36,224,1129316352,1129316352,1129316352,1098907648,1098907648,0,1677888,1073741824,1677312,44032,44032,40960,8948736,1073741824,8945664,17231,1107575634,4294922220,4294967294,17231,1107575634,4294922220,4294967294,17231,1107575634,4294922220,4294967294,
2881536,2881536,2924544,2924544,2967552,2967552,3010560,3010560,3010560,3010560,3010560,3010560,40960,44032,48128,51200,55296,58368,62464,65536,62464,65536,62464,65536,268439552,336991254,1,0,80,0,192,1040137727,16780300,50465804,8064,36,224,1116209152,1116209152,1116209152,1098907648,1098907648,0,548928,1073741824,548352,44032,44032,40960,2927616,1073741824,2924544,17030,1107575436,4294953580,4294967294,17030,1107575436,4294953580,4294967294,17030,1107575436,4294953580,4294967294,
5031936,5031936,5074944,5074944,5117952,5117952,5160960,5160960,5160960,5160960,5160960,5160960,40960,44032,48128,51200,55296,58368,62464,65536,62464,65536,62464,65536,268439552,336991254,1,0,80,0,192,1048526463,16780300,50465804,8064,36,224,1122762752,1122762752,1122762752,1098907648,1098907648,0,952128,1073741824,951552,44032,44032,40960,5078016,1073741824,5074944,17130,1107575536,4294942380,4294967294,17130,1107575536,4294942380,4294967294,17130,1107575536,4294942380,4294967294,
10944,10944,11136,11136,11328,11328,11520,11520,11520,11520,11520,11520,40960,44032,48128,51200,55296,58368,62464,65536,62464,65536,62464,65536,268439552,336991254,1,0,80,0,43008,1048526591,50465804,16780300,36,8064,224,1114112000,1114112000,1114112000,1098907648,1098907648,0,131112,1073741824,2088,44032,44032,40960,699264,1073741824,11136,16996,1107575408,4294955820,4294967294,16996,1107575408,4294955820,4294967294,16996,1107575408,4294955820,4294967294,
20544,20544,20736,20736,20928,20928,21120,21120,21120,21120,21120,21120,40960,44032,48128,51200,55296,58368,62464,65536,62464,65536,62464,65536,268439552,336991254,1,0,80,0,43008,1048526463,50465804,16780300,36,8064,224,1121452032,1121452032,1121452032,1098907648,1098907648,0,132912,1073741824,3888,44032,44032,40960,708864,1073741824,20736,17110,1107575516,4294944620,4294967294,17110,1107575516,4294944620,4294967294,17110,1107575516,4294944620,4294967294,
28608,28608,28800,28800,28992,28992,29184,29184,29184,29184,29184,29184,40960,44032,48128,51200,55296,58368,62464,65536,62464,65536,62464,65536,268439552,336991254,1,0,80,0,43008,1056915199,50465804,16780300,36,8064,224,1125515264,1125515264,1125515264,1098907648,1098907648,0,134424,1073741824,5400,44032,44032,40960,716928,1073741824,28800,17173,1107575576,4294935212,4294967294,17173,1107575576,4294935212,4294967294,17173,1107575576,4294935212,4294967294,
38208,38208,38400,38400,38592,38592,38784,38784,38784,38784,38784,38784,40960,44032,48128,51200,55296,58368,62464,65536,62464,65536,62464,65536,268439552,336991254,1,0,80,0,43008,1056915199,50465804,16780300,36,8064,224,1128792064,1128792064,1128792064,1098907648,1098907648,0,136224,1073741824,7200,44032,44032,40960,726528,1073741824,38400,17223,1107575626,4294924012,4294967294,17223,1107575626,4294924012,4294967294,17223,1107575626,4294924012,4294967294,
6666240,6666240,6709248,6709248,6752256,6752256,6795264,6795264,6795264,6795264,6795264,6795264,40960,44032,48128,51200,55296,58368,62464,65536,62464,65536,62464,65536,268439552,336991254,1,0,80,0,192,1056915071,16780300,50465804,8064,36,224,1125908480,1125908480,1125908480,1098907648,1098907648,0,1258560,1073741824,1257984,44032,44032,40960,6712320,1073741824,6709248,17179,1107575582,4294933868,4294967294,17179,1107575582,4294933868,4294967294,17179,1107575582,4294933868,4294967294,
8988672,8988672,9031680,9031680,9074688,9074688,9117696,9117696,9117696,9117696,9117696,9117696,40960,44032,48128,51200,55296,58368,62464,65536,62464,65536,62464,65536,268439552,336991254,1,0,80,0,192,1056915199,16780300,50465804,8064,36,224,1129447424,1129447424,1129447424,1098907648,1098907648,0,1694016,1073741824,1693440,44032,44032,40960,9034752,1073741824,9031680,17233,1107575636,4294921772,4294967294,17233,1107575636,4294921772,4294967294,17233,1107575636,4294921772,4294967294,
2967552,2967552,3010560,3010560,3053568,3053568,3096576,3096576,3096576,3096576,3096576,3096576,40960,44032,48128,51200,55296,58368,62464,65536,62464,65536,62464,65536,268439552,336991254,1,0,80,0,192,1040137727,16780300,50465804,8064,36,224,1116471296,1116471296,1116471296,1098907648,1098907648,0,565056,1073741824,564480,44032,44032,40960,3013632,1073741824,3010560,17034,1107575440,4294953132,4294967294,17034,1107575440,4294953132,4294967294,17034,1107575440,4294953132,4294967294,
5117952,5117952,5160960,5160960,5203968,5203968,5246976,5246976,5246976,5246976,5246976,5246976,40960,44032,48128,51200,55296,58368,62464,65536,62464,65536,62464,65536,268439552,336991254,1,0,80,0,192,1048526463,16780300,50465804,8064,36,224,1123024896,1123024896,1123024896,1098907648,1098907648,0,968256,1073741824,967680,44032,44032,40960,5164032,1073741824,5160960,17134,1107575540,4294941932,4294967294,17134,1107575540,4294941932,4294967294,17134,1107575540,4294941932,4294967294,
602112,645120,688128,731136,774144,817152,8558592,8601600,8601600,8601600,8601600,8601600,49152,53248,57344,61440,65536,69632,40960,45056,45056,45056,45056,45056,1616904192,320017685,1,0,80,0,192,1048526591,16780300,50465804,8064,36,128,1128660992,1128660992,1098907648,1119879168,1119879168,0,1600128,1073741824,129024,32768,32768,53248,8534016,1073741824,688128,1119765309,1121993544,4294942540,94,1119765309,1121993544,4294942540,94,16736,1099973016,4294967052,4294967294,
20928,20928,21120,21120,21312,21312,21504,21504,21504,21504,21504,21504,40960,44032,48128,51200,55296,58368,62464,65536,62464,65536,62464,65536,268439552,336991254,1,0,80,0,43008,1048526463,50465804,16780300,36,8064,224,1121714176,1121714176,1121714176,1098907648,1098907648,0,132984,1073741824,3960,44032,44032,40960,709248,1073741824,21120,17114,1107575520,4294944172,4294967294,17114,1107575520,4294944172,4294967294,17114,1107575520,4294944172,4294967294,
28992,28992,29184,29184,29376,29376,29568,29568,29568,29568,29568,29568,40960,44032,48128,51200,55296,58368,62464,65536,62464,65536,62464,65536,268439552,336991254,1,0,80,0,43008,1056915199,50465804,16780300,36,8064,224,1125646336,1125646336,1125646336,1098907648,1098907648,0,134496,1073741824,5472,44032,44032,40960,717312,1073741824,29184,17175,1107575578,4294934764,4294967294,17175,1107575578,4294934764,4294967294,17175,1107575578,4294934764,4294967294,
38592,38592,38784,38784,38976,38976,39168,39168,39168,39168,39168,39168,40960,44032,48128,51200,55296,58368,62464,65536,62464,65536,62464,65536,268439552,336991254,1,0,80,0,43008,1056915199,50465804,16780300,36,8064,224,1128923136,1128923136,1128923136,1098907648,1098907648,0,136296,1073741824,7272,44032,44032,40960,726912,1073741824,38784,17225,1107575628,4294923564,4294967294,17225,1107575628,4294923564,4294967294,17225,1107575628,4294923564,4294967294,
6752256,6752256,6795264,6795264,6838272,6838272,6881280,6881280,6881280,6881280,6881280,6881280,40960,44032,48128,51200,55296,58368,62464,65536,62464,65536,62464,65536,268439552,336991254,1,0,80,0,192,1056915071,16780300,50465804,8064,36,224,1126039552,1126039552,1126039552,1098907648,1098907648,0,1274688,1073741824,1274112,44032,44032,40960,6798336,1073741824,6795264,17181,1107575584,4294933420,4294967294,17181,1107575584,4294933420,4294967294,17181,1107575584,4294933420,4294967294,
9074688,9074688,9117696,9117696,9160704,9160704,9203712,9203712,9203712,9203712,9203712,9203712,40960,44032,48128,51200,55296,58368,62464,65536,62464,65536,62464,65536,268439552,336991254,1,0,80,0,192,1056915199,16780300,50465804,8064,36,224,1129578496,1129578496,1129578496,1098907648,1098907648,0,1710144,1073741824,1709568,44032,44032,40960,9120768,1073741824,9117696,17235,1107575638,4294921324,4294967294,17235,1107575638,4294921324,4294967294,17235,1107575638,4294921324,4294967294,
172032,215040,258048,301056,344064,387072,2881536,2924544,2924544,2924544,2924544,2924544,49152,53248,57344,61440,65536,69632,40960,45056,45056,45056,45056,45056,1077936128,320017685,1,512,80,0,192,1040137727,16780300,50465804,8064,36,128,1115947008,1115947008,1086324736,1115684864,1115684864,0,534528,1073741824,48384,32768,32768,53248,2850816,1073741824,258048,1115439716,1117799048,4294959628,62,1115439716,1117799048,4294959628,62,16512,1099972880,1036,4294967294,
5203968,5203968,5246976,5246976,5289984,5289984,5332992,5332992,5332992,5332992,5332992,5332992,40960,44032,48128,51200,55296,58368,62464,65536,62464,65536,62464,65536,268439552,336991254,1,0,80,0,192,1048526591,16780300,50465804,8064,36,224,1123287040,1123287040,1123287040,1098907648,1098907648,0,984384,1073741824,983808,44032,44032,40960,5250048,1073741824,5246976,17138,1107575544,4294941484,4294967294,17138,1107575544,4294941484,4294967294,17138,1107575544,4294941484,4294967294,
11328,11328,11520,11520,11712,11712,11904,11904,11904,11904,11904,11904,40960,44032,48128,51200,55296,58368,62464,65536,62464,65536,62464,65536,268439552,336991254,1,0,80,0,43008,1048526591,50465804,16780300,36,8064,224,1114636288,1114636288,1114636288,1098907648,1098907648,0,131184,1073741824,2160,44032,44032,40960,699648,1073741824,11520,17004,1107575416,4294955372,4294967294,17004,1107575416,4294955372,4294967294,17004,1107575416,4294955372,4294967294,
21312,21312,21504,21504,21696,21696,21888,21888,21888,21888,21888,21888,40960,44032,48128,51200,55296,58368,62464,65536,62464,65536,62464,65536,268439552,336991254,1,0,80,0,43008,1048526463,50465804,16780300,36,8064,224,1121976320,1121976320,1121976320,1098907648,1098907648,0,133056,1073741824,4032,44032,44032,40960,709632,1073741824,21504,17118,1107575524,4294943724,4294967294,17118,1107575524,4294943724,4294967294,17118,1107575524,4294943724,4294967294,
29376,29376,29568,29568,29760,29760,29952,29952,29952,29952,29952,29952,40960,44032,48128,51200,55296,58368,62464,65536,62464,65536,62464,65536,268439552,336991254,1,0,80,0,43008,1056915199,50465804,16780300,36,8064,224,1125777408,1125777408,1125777408,1098907648,1098907648,0,134568,1073741824,5544,44032,44032,40960,717696,1073741824,29568,17177,1107575580,4294934316,4294967294,17177,1107575580,4294934316,4294967294,17177,1107575580,4294934316,4294967294,
38976,38976,39168,39168,39360,39360,39552,39552,39552,39552,39552,39552,40960,44032,48128,51200,55296,58368,62464,65536,62464,65536,62464,65536,268439552,336991254,1,0,80,0,43008,1056914943,50465804,16780300,36,8064,224,1129054208,1129054208,1129054208,1098907648,1098907648,0,136368,1073741824,7344,44032,44032,40960,727296,1073741824,39168,17227,1107575630,4294923116,4294967294,17227,1107575630,4294923116,4294967294,17227,1107575630,4294923116,4294967294,
6838272,6838272,6881280,6881280,6924288,6924288,6967296,6967296,6967296,6967296,6967296,6967296,40960,44032,48128,51200,55296,58368,62464,65536,62464,65536,62464,65536,268439552,336991254,1,0,80,0,192,1056915071,16780300,50465804,8064,36,224,1126170624,1126170624,1126170624,1098907648,1098907648,0,1290816,1073741824,1290240,44032,44032,40960,6884352,1073741824,6881280,17183,1107575586,4294932972,4294967294,17183,1107575586,4294932972,4294967294,17183,1107575586,4294932972,4294967294,
9160704,9160704,9203712,9203712,9246720,9246720,9289728,9289728,9289728,9289728,9289728,9289728,40960,44032,48128,51200,55296,58368,62464,65536,62464,65536,62464,65536,268439552,336991254,1,0,80,0,192,1056915199,16780300,50465804,8064,36,224,1129709568,1129709568,1129709568,1098907648,1098907648,0,1726272,1073741824,1725696,44032,44032,40960,9206784,1073741824,9203712,17237,1107575640,4294920876,4294967294,17237,1107575640,4294920876,4294967294,17237,1107575640,4294920876,4294967294,
3053568,3053568,3096576,3096576,3139584,3139584,3182592,3182592,3182592,3182592,3182592,3182592,40960,44032,48128,51200,55296,58368,62464,65536,62464,65536,62464,65536,268439552,336991254,1,0,80,0,192,1040137855,16780300,50465804,8064,36,224,1116733440,1116733440,1116733440,1098907648,1098907648,0,581184,1073741824,580608,44032,44032,40960,3099648,1073741824,3096576,17038,1107575444,4294952684,4294967294,17038,1107575444,4294952684,4294967294,17038,1107575444,4294952684,4294967294,
5289984,5289984,5332992,5332992,5376000,5376000,5419008,5419008,5419008,5419008,5419008,5419008,40960,44032,48128,51200,55296,58368,62464,65536,62464,65536,62464,65536,268439552,336991254,1,0,80,0,192,1048526591,16780300,50465804,8064,36,224,1123549184,1123549184,1123549184,1098907648,1098907648,0,1000512,1073741824,999936,44032,44032,40960,5336064,1073741824,5332992,17142,1107575548,4294941036,4294967294,17142,1107575548,4294941036,4294967294,17142,1107575548,4294941036,4294967294,
11712,11712,11904,11904,12096,12096,12288,12288,12288,12288,12288,12288,40960,44032,48128,51200,55296,58368,62464,65536,62464,65536,62464,65536,268439552,336991254,1,0,80,0,43008,1048526591,50465804,16780300,36,8064,224,1115160576,1115160576,1115160576,1098907648,1098907648,0,131256,1073741824,2232,44032,44032,40960,700032,1073741824,11904,17012,1107575424,4294954924,4294967294,17012,1107575424,4294954924,4294967294,17012,1107575424,4294954924,4294967294,
21696,21696,21888,21888,22080,22080,22272,22272,22272,22272,22272,22272,40960,44032,48128,51200,55296,58368,62464,65536,62464,65536,62464,65536,268439552,336991254,1,0,80,0,43008,1048526463,50465804,16780300,36,8064,224,1122238464,1122238464,1122238464,1098907648,1098907648,0,133128,1073741824,4104,44032,44032,40960,710016,1073741824,21888,17122,1107575528,4294943276,4294967294,17122,1107575528,4294943276,4294967294,17122,1107575528,4294943276,4294967294,
29760,29760,29952,29952,30144,30144,30336,30336,30336,30336,30336,30336,40960,44032,48128,51200,55296,58368,62464,65536,62464,65536,62464,65536,268439552,336991254,1,0,80,0,43008,1056915199,50465804,16780300,36,8064,224,1125908480,1125908480,1125908480,1098907648,1098907648,0,134640,1073741824,5616,44032,44032,40960,718080,1073741824,29952,17179,1107575582,4294933868,4294967294,17179,1107575582,4294933868,4294967294,17179,1107575582,4294933868,4294967294,
39360,39360,39552,39552,39744,39744,39936,39936,39936,39936,39936,39936,40960,44032,48128,51200,55296,58368,62464,65536,62464,65536,62464,65536,268439552,336991254,1,0,80,0,43008,1056914943,50465804,16780300,36,8064,224,1129185280,1129185280,1129185280,1098907648,1098907648,0,136440,1073741824,7416,44032,44032,40960,727680,1073741824,39552,17229,1107575632,4294922668,4294967294,17229,1107575632,4294922668,4294967294,17229,1107575632,4294922668,4294967294,
6924288,6924288,6967296,6967296,7010304,7010304,7053312,7053312,7053312,7053312,7053312,7053312,40960,44032,48128,51200,55296,58368,62464,65536,62464,65536,62464,65536,268439552,336991254,1,0,80,0,192,1056915071,16780300,50465804,8064,36,224,1126301696,1126301696,1126301696,1098907648,1098907648,0,1306944,1073741824,1306368,44032,44032,40960,6970368,1073741824,6967296,17185,1107575588,4294932524,4294967294,17185,1107575588,4294932524,4294967294,17185,1107575588,4294932524,4294967294,
9246720,9246720,9289728,9289728,9332736,9332736,9375744,9375744,9375744,9375744,9375744,9375744,40960,44032,48128,51200,55296,58368,62464,65536,62464,65536,62464,65536,268439552,336991254,1,0,80,0,192,1056915199,16780300,50465804,8064,36,224,1129840640,1129840640,1129840640,1098907648,1098907648,0,1742400,1073741824,1741824,44032,44032,40960,9292800,1073741824,9289728,17239,1107575642,4294920428,4294967294,17239,1107575642,4294920428,4294967294,17239,1107575642,4294920428,4294967294,
3139584,3139584,3182592,3182592,3225600,3225600,3268608,3268608,3268608,3268608,3268608,3268608,40960,44032,48128,51200,55296,58368,62464,65536,62464,65536,62464,65536,268439552,336991254,1,0,80,0,192,1040137855,16780300,50465804,8064,36,224,1116995584,1116995584,1116995584,1098907648,1098907648,0,597312,1073741824,596736,44032,44032,40960,3185664,1073741824,3182592,17042,1107575448,4294952236,4294967294,17042,1107575448,4294952236,4294967294,17042,1107575448,4294952236,4294967294,
5376000,5376000,5419008,5419008,5462016,5462016,5505024,5505024,5505024,5505024,5505024,5505024,40960,44032,48128,51200,55296,58368,62464,65536,62464,65536,62464,65536,268439552,336991254,1,0,80,0,192,1048526591,16780300,50465804,8064,36,224,1123811328,1123811328,1123811328,1098907648,1098907648,0,1016640,1073741824,1016064,44032,44032,40960,5422080,1073741824,5419008,17146,1107575552,4294940588,4294967294,17146,1107575552,4294940588,4294967294,17146,1107575552,4294940588,4294967294,
12096,12096,12288,12288,12480,12480,12672,12672,12672,12672,12672,12672,40960,44032,48128,51200,55296,58368,62464,65536,62464,65536,62464,65536,268439552,336991254,1,0,80,0,43008,1048526591,50465804,16780300,36,8064,224,1115684864,1115684864,1115684864,1098907648,1098907648,0,131328,1073741824,2304,44032,44032,40960,700416,1073741824,12288,17020,1107575428,4294954476,4294967294,17020,1107575428,4294954476,4294967294,17020,1107575428,4294954476,4294967294,
22080,22080,22272,22272,22464,22464,22656,22656,22656,22656,22656,22656,40960,44032,48128,51200,55296,58368,62464,65536,62464,65536,62464,65536,268439552,336991254,1,0,80,0,43008,1048526463,50465804,16780300,36,8064,224,1122500608,1122500608,1122500608,1098907648,1098907648,0,133200,1073741824,4176,44032,44032,40960,710400,1073741824,22272,17126,1107575532,4294942828,4294967294,17126,1107575532,4294942828,4294967294,17126,1107575532,4294942828,4294967294,
30144,30144,30336,30336,30528,30528,30720,30720,30720,30720,30720,30720,40960,44032,48128,51200,55296,58368,62464,65536,62464,65536,62464,65536,268439552,336991254,1,0,80,0,43008,1056915199,50465804,16780300,36,8064,224,1126039552,1126039552,1126039552,1098907648,1098907648,0,134712,1073741824,5688,44032,44032,40960,718464,1073741824,30336,17181,1107575584,4294933420,4294967294,17181,1107575584,4294933420,4294967294,17181,1107575584,4294933420,4294967294,
39744,39744,39936,39936,40128,40128,40320,40320,40320,40320,40320,40320,40960,44032,48128,51200,55296,58368,62464,65536,62464,65536,62464,65536,268439552,336991254,1,0,80,0,43008,1056914943,50465804,16780300,36,8064,224,1129316352,1129316352,1129316352,1098907648,1098907648,0,136512,1073741824,7488,44032,44032,40960,728064,1073741824,39936,17231,1107575634,4294922220,4294967294,17231,1107575634,4294922220,4294967294,17231,1107575634,4294922220,4294967294,
5634048,5677056,5720064,5763072,5806080,5849088,5849088,5849088,5849088,5849088,5849088,5849088,40960,45056,49152,53248,57344,61440,61440,61440,61440,61440,61440,61440,3233857728,320017171,0,256,80,0,192,1056915071,16780300,50465804,8064,36,128,1124204544,1124335616,1124466688,1128267776,1128267776,1128267776,1055232,1071360,1087488,32768,40960,49152,5627904,5713920,5799936,1128219378,1129333512,4294950668,190,1128219378,1129333512,4294950668,190,1128219378,1129333512,4294950668,190,
9332736,9375744,9418752,9461760,9504768,9547776,9590784,9590784,9590784,9590784,9590784,9590784,40960,45056,49152,53248,57344,61440,65536,69632,69632,69632,69632,69632,538976288,353703189,0,256,80,0,192,1056915199,16780300,50465804,8064,36,128,1130233856,1130102784,1129971712,1107296256,1107296256,1107296256,1791360,1775232,1759104,57344,49152,40960,9553920,9467904,9381888,1106264921,1112032095,4294940620,30,1106264921,1112032095,4294940620,30,1106264921,1112032095,4294940620,30,
2107392,2150400,2193408,2236416,2279424,2322432,2365440,2408448,2408448,2408448,2408448,2408448,40960,45056,49152,53248,57344,61440,65536,69632,69632,69632,69632,69632,538976288,353703189,0,256,80,0,192,1040137855,16780300,50465804,8064,36,128,1113063424,1112539136,1112014848,1107296256,1107296256,1107296256,436608,420480,404352,57344,49152,40960,2328576,2242560,2156544,1106264644,1112031840,4294962124,30,1106264644,1112031840,4294962124,30,1106264644,1112031840,4294962124,30,
3139584,3182592,3225600,3268608,3311616,3354624,3397632,3440640,3440640,3440640,3440640,3440640,40960,45056,49152,53248,57344,61440,65536,69632,69632,69632,69632,69632,538976288,353703189,0,256,80,0,192,1048526591,16780300,50465804,8064,36,128,1117519872,1117257728,1116995584,1107296256,1107296256,1107296256,630144,614016,597888,57344,49152,40960,3360768,3274752,3188736,1106264722,1112031904,4294959052,30,1106264722,1112031904,4294959052,30,1106264722,1112031904,4294959052,30,
12480,12480,12672,12672,12864,12864,13056,13056,13056,13056,13056,13056,40960,44032,48128,51200,55296,58368,62464,65536,62464,65536,62464,65536,268439552,336991254,1,0,80,0,43008,1048526335,50465804,16780300,36,8064,224,1115947008,1115947008,1115947008,1098907648,1098907648,0,131400,1073741824,2376,44032,44032,40960,700800,1073741824,12672,17026,1107575432,4294954028,4294967294,17026,1107575432,4294954028,4294967294,17026,1107575432,4294954028,4294967294,
5203968,5246976,5289984,5332992,5376000,5419008,5462016,5505024,5505024,5505024,5505024,5505024,40960,45056,49152,53248,57344,61440,65536,69632,69632,69632,69632,69632,538976288,353703189,0,256,80,0,192,1048526463,16780300,50465804,8064,36,128,1123811328,1123549184,1123287040,1107296256,1107296256,1107296256,1017216,1001088,984960,57344,49152,40960,5425152,5339136,5253120,1106264818,1112032000,4294952908,30,1106264818,1112032000,4294952908,30,1106264818,1112032000,4294952908,30,
6236160,6279168,6322176,6365184,6408192,6451200,6494208,6537216,6537216,6537216,6537216,6537216,40960,45056,49152,53248,57344,61440,65536,69632,69632,69632,69632,69632,538976288,353703189,0,256,80,0,192,1056915199,16780300,50465804,8064,36,128,1125515264,1125384192,1125253120,1107296256,1107296256,1107296256,1210752,1194624,1178496,57344,49152,40960,6457344,6371328,6285312,1106264849,1112032024,4294949836,30,1106264849,1112032024,4294949836,30,1106264849,1112032024,4294949836,30,
40128,40128,40320,40320,40512,40512,40704,40704,40704,40704,40704,40704,40960,44032,48128,51200,55296,58368,62464,65536,62464,65536,62464,65536,268439552,336991254,1,0,80,0,43008,1056914943,50465804,16780300,36,8064,224,1129447424,1129447424,1129447424,1098907648,1098907648,0,136584,1073741824,7560,44032,44032,40960,728448,1073741824,40320,17233,1107575636,4294921772,4294967294,17233,1107575636,4294921772,4294967294,17233,1107575636,4294921772,4294967294,
7010304,7010304,7053312,7053312,7096320,7096320,7139328,7139328,7139328,7139328,7139328,7139328,40960,44032,48128,51200,55296,58368,62464,65536,62464,65536,62464,65536,268439552,336991254,1,0,80,0,192,1056915199,16780300,50465804,8064,36,224,1126432768,1126432768,1126432768,1098907648,1098907648,0,1323072,1073741824,1322496,44032,44032,40960,7056384,1073741824,7053312,17187,1107575590,4294932076,4294967294,17187,1107575590,4294932076,4294967294,17187,1107575590,4294932076,4294967294,
9332736,9332736,9375744,9375744,9418752,9418752,9461760,9461760,9461760,9461760,9461760,9461760,40960,44032,48128,51200,55296,58368,62464,65536,62464,65536,62464,65536,268439552,336991254,1,0,80,0,192,1056915199,16780300,50465804,8064,36,224,1129971712,1129971712,1129971712,1098907648,1098907648,0,1758528,1073741824,1757952,44032,44032,40960,9378816,1073741824,9375744,17241,1107575644,4294919980,4294967294,17241,1107575644,4294919980,4294967294,17241,1107575644,4294919980,4294967294,
3225600,3225600,3268608,3268608,3311616,3311616,3354624,3354624,3354624,3354624,3354624,3354624,40960,44032,48128,51200,55296,58368,62464,65536,62464,65536,62464,65536,268439552,336991254,1,0,80,0,192,1040137855,16780300,50465804,8064,36,224,1117257728,1117257728,1117257728,1098907648,1098907648,0,613440,1073741824,612864,44032,44032,40960,3271680,1073741824,3268608,17046,1107575452,4294951788,4294967294,17046,1107575452,4294951788,4294967294,17046,1107575452,4294951788,4294967294,
2688,2688,2880,2880,3072,3072,3264,3264,3456,3456,3648,3648,40960,44032,48128,51200,55296,58368,62464,65536,69632,72704,76800,79872,268439552,336991254,1,1024,64,0,43008,1048526591,50465804,16780300,36,8064,224,1098907648,1098907648,1098907648,1098907648,1098907648,0,129600,1073741824,576,51200,51200,48128,691200,1073741824,3072,16736,1107575192,4294965452,4294967294,16736,1107575192,4294965452,4294967294,16736,1107575192,4294965452,4294967294,
4171776,4214784,4257792,4300800,4343808,4386816,4429824,4472832,4472832,4472832,4472832,4472832,40960,45056,49152,53248,57344,61440,65536,69632,69632,69632,69632,69632,538976288,353703189,0,256,80,0,192,1048526335,16780300,50465804,8064,36,128,1120665600,1120403456,1120141312,1107296256,1107296256,1107296256,823680,807552,791424,57344,49152,40960,4392960,4306944,4220928,1106264770,1112031952,4294955980,30,1106264770,1112031952,4294955980,30,1106264770,1112031952,4294955980,30,
22464,22464,22656,22656,22848,22848,23040,23040,23040,23040,23040,23040,40960,44032,48128,51200,55296,58368,62464,65536,62464,65536,62464,65536,268439552,336991254,1,0,80,0,43008,1048526463,50465804,16780300,36,8064,224,1122762752,1122762752,1122762752,1098907648,1098907648,0,133272,1073741824,4248,44032,44032,40960,710784,1073741824,22656,17130,1107575536,4294942380,4294967294,17130,1107575536,4294942380,4294967294,17130,1107575536,4294942380,4294967294,
30528,30528,30720,30720,30912,30912,31104,31104,31104,31104,31104,31104,40960,44032,48128,51200,55296,58368,62464,65536,62464,65536,62464,65536,268439552,336991254,1,0,80,0,43008,1056915199,50465804,16780300,36,8064,224,1126170624,1126170624,1126170624,1098907648,1098907648,0,134784,1073741824,5760,44032,44032,40960,718848,1073741824,30720,17183,1107575586,4294932972,4294967294,17183,1107575586,4294932972,4294967294,17183,1107575586,4294932972,4294967294,
7268352,7311360,7354368,7397376,7440384,7483392,7526400,7569408,7569408,7569408,7569408,7569408,40960,45056,49152,53248,57344,61440,65536,69632,69632,69632,69632,69632,538976288,353703189,0,256,80,0,192,1056915071,16780300,50465804,8064,36,128,1127088128,1126957056,1126825984,1107296256,1107296256,1107296256,1404288,1388160,1372032,57344,49152,40960,7489536,7403520,7317504,1106264873,1112032048,4294946764,30,1106264873,1112032048,4294946764,30,1106264873,1112032048,4294946764,30,
8300544,8343552,8386560,8429568,8472576,8515584,8558592,8601600,8601600,8601600,8601600,8601600,40960,45056,49152,53248,57344,61440,65536,69632,69632,69632,69632,69632,538976288,353703189,0,256,80,0,192,1056915199,16780300,50465804,8064,36,128,1128660992,1128529920,1128398848,1107296256,1107296256,1107296256,1597824,1581696,1565568,57344,49152,40960,8521728,8435712,8349696,1106264897,1112032072,4294943692,30,1106264897,1112032072,4294943692,30,1106264897,1112032072,4294943692,30,
9418752,9418752,9461760,9461760,9504768,9504768,9547776,9547776,9547776,9547776,9547776,9547776,40960,44032,48128,51200,55296,58368,62464,65536,62464,65536,62464,65536,268439552,336991254,1,0,80,0,192,1056915199,16780300,50465804,8064,36,224,1130102784,1130102784,1130102784,1098907648,1098907648,0,1774656,1073741824,1774080,44032,44032,40960,9464832,1073741824,9461760,17243,1107575646,4294919532,4294967294,17243,1107575646,4294919532,4294967294,17243,1107575646,4294919532,4294967294,
3311616,3311616,3354624,3354624,3397632,3397632,3440640,3440640,3440640,3440640,3440640,3440640,40960,44032,48128,51200,55296,58368,62464,65536,62464,65536,62464,65536,268439552,336991254,1,0,80,0,192,1040137855,16780300,50465804,8064,36,224,1117519872,1117519872,1117519872,1098907648,1098907648,0,629568,1073741824,628992,44032,44032,40960,3357696,1073741824,3354624,17050,1107575456,4294951340,4294967294,17050,1107575456,4294951340,4294967294,17050,1107575456,4294951340,4294967294,
3072,3072,3264,3264,3456,3456,3648,3648,3840,3840,4032,4032,40960,44032,48128,51200,55296,58368,62464,65536,69632,72704,76800,79872,268439552,336991254,1,1024,64,0,43008,1048526591,50465804,16780300,36,8064,224,1099956224,1099956224,1099956224,1098907648,1098907648,0,129672,1073741824,648,51200,51200,48128,691584,1073741824,3456,16768,1107575208,4294965004,4294967294,16768,1107575208,4294965004,4294967294,16768,1107575208,4294965004,4294967294,
12864,12864,13056,13056,13248,13248,13440,13440,13440,13440,13440,13440,40960,44032,48128,51200,55296,58368,62464,65536,62464,65536,62464,65536,268439552,336991254,1,0,80,0,43008,1048526335,50465804,16780300,36,8064,224,1116209152,1116209152,1116209152,1098907648,1098907648,0,131472,1073741824,2448,44032,44032,40960,701184,1073741824,13056,17030,1107575436,4294953580,4294967294,17030,1107575436,4294953580,4294967294,17030,1107575436,4294953580,4294967294,
774144,817152,860160,903168,946176,989184,5720064,5763072,5763072,5763072,5763072,5763072,49152,53248,57344,61440,65536,69632,40960,45056,45056,45056,45056,45056,2155872256,320017685,1,0,80,0,192,1048526463,16780300,50465804,8064,36,128,1124335616,1124335616,1101004800,1124073472,1124073472,0,1069056,1073741824,161280,32768,32768,53248,5701632,1073741824,860160,1123959542,1125139206,4294950796,126,1123959542,1125139206,4294950796,126,16784,1099973048,4294966540,4294967294,
30912,30912,31104,31104,31296,31296,31488,31488,31488,31488,31488,31488,40960,44032,48128,51200,55296,58368,62464,65536,62464,65536,62464,65536,268439552,336991254,1,0,80,0,43008,1056915199,50465804,16780300,36,8064,224,1126301696,1126301696,1126301696,1098907648,1098907648,0,134856,1073741824,5832,44032,44032,40960,719232,1073741824,31104,17185,1107575588,4294932524,4294967294,17185,1107575588,4294932524,4294967294,17185,1107575588,4294932524,4294967294,
1290240,1333248,1376256,1419264,1462272,1505280,3569664,3612672,3655680,3698688,3698688,3698688,57344,61440,65536,69632,73728,77824,40960,45056,49152,53248,53248,53248,2964377808,320017685,0,1280,64,0,192,1056915071,16780300,50465804,8064,36,128,1118044160,1118306304,1107296256,1127219200,1127219200,1129316352,667584,683712,265536,32768,40960,61440,3560448,3646464,1416192,1127170706,1128284844,4294956908,174,1127170706,1128284844,4294956908,174,1129202160,1130316300,4294964012,206,
7096320,7096320,7139328,7139328,7182336,7182336,7225344,7225344,7225344,7225344,7225344,7225344,40960,44032,48128,51200,55296,58368,62464,65536,62464,65536,62464,65536,268439552,336991254,1,0,80,0,192,1056915199,16780300,50465804,8064,36,224,1126563840,1126563840,1126563840,1098907648,1098907648,0,1339200,1073741824,1338624,44032,44032,40960,7142400,1073741824,7139328,17189,1107575592,4294931628,4294967294,17189,1107575592,4294931628,4294967294,17189,1107575592,4294931628,4294967294,
9504768,9504768,9547776,9547776,9590784,9590784,9590784,9590784,9590784,9590784,9590784,9590784,40960,44032,48128,51200,55296,58368,62464,65536,62464,65536,62464,65536,268439552,336991254,1,0,80,0,192,1056915199,16780300,50465804,8064,36,224,1130233856,1130233856,1130233856,1098907648,1098907648,0,1790784,1073741824,1790208,44032,44032,40960,9550848,1073741824,9547776,17245,1107575647,4294919084,4294967294,17245,1107575647,4294919084,4294967294,17245,1107575647,4294919084,4294967294,
192,384,576,86016,384,576,800,86024,576,800,992,86032,800,992,1184,86040,992,1184,1376,86208,1184,1376,1536,86216,1376,1536,1728,86224,1536,1728,1920,86232,1728,1920,2112,86400,1920,2112,2336,86408,2112,2336,2528,86416,2336,2528,2720,86424,2528,2720,2912,86592,2720,2912,3072,86600,2912,3072,3264,86608,3072,3264,3456,86616,
208,400,592,87584,400,592,816,87592,592,816,1008,87600,816,1008,1200,87608,1008,1200,1392,87776,1200,1392,1552,87784,1392,1552,1744,87792,1552,1744,1936,87800,1744,1936,2128,87968,1936,2128,2352,87976,2128,2352,2544,87984,2352,2544,2736,87992,2544,2736,2928,88160,2736,2928,3088,88168,2928,3088,3280,88176,3088,3280,3472,88184,
224,416,608,89088,416,608,768,89096,608,768,960,89104,768,960,1152,89112,960,1152,1344,89280,1152,1344,1568,89288,1344,1568,1760,89296,1568,1760,1952,89304,1760,1952,2144,89472,1952,2144,2304,89480,2144,2304,2496,89488,2304,2496,2688,89496,2496,2688,2880,89664,2688,2880,3104,89672,2880,3104,3296,89680,3104,3296,3488,89688,
240,432,624,90656,432,624,784,90664,624,784,976,90672,784,976,1168,90680,976,1168,1360,90848,1168,1360,1584,90856,1360,1584,1776,90864,1584,1776,1968,90872,1776,1968,2160,91040,1968,2160,2320,91048,2160,2320,2512,91056,2320,2512,2704,91064,2512,2704,2896,91232,2704,2896,3120,91240,2896,3120,3312,91248,3120,3312,3504,91256,
0,64,0,0,192,80,64,16,384,96,144,32,576,112,208,48,768,256,288,64,960,272,352,80,1152,288,432,96,1344,304,496,112,1536,448,512,144,1728,464,576,128,1920,480,656,176,2112,496,720,160,2304,640,800,208,2496,656,864,192,2688,672,944,240,2880,688,1008,224,
32,832,8,288,224,848,72,304,416,864,152,256,608,880,216,272,800,1024,296,352,992,1040,360,368,1184,1056,440,320,1376,1072,504,336,1568,1216,520,432,1760,1232,584,416,1952,1248,664,400,2144,1264,728,384,2336,1408,808,496,2528,1424,872,480,2720,1440,952,464,2912,1456,1016,448,
16,1600,16,512,208,1616,80,528,400,1632,128,544,592,1648,192,560,784,1792,304,576,976,1808,368,592,1168,1824,416,608,1360,1840,480,624,1552,1984,528,656,1744,2000,592,640,1936,2016,640,688,2128,2032,704,672,2320,2176,816,720,2512,2192,880,704,2704,2208,928,752,2896,2224,992,736,
48,2368,24,800,240,2384,88,816,432,2400,136,768,624,2416,200,784,816,2560,312,864,1008,2576,376,880,1200,2592,424,832,1392,2608,488,848,1584,2752,536,944,1776,2768,600,928,1968,2784,648,912,2160,2800,712,896,2352,2944,824,1008,2544,2960,888,992,2736,2976,936,976,2928,2992,1000,960,
202113280,202116108,202113794,202116108,202116108,202116108,202116108,202116108,202116108,202116108,202116108,202116108,202116108,202116108,202116108,202116108,16780300,202116108,50465804,202116108,202116108,202116108,202116108,202116108,202116108,202116108,202116108,202116108,202116108,202116108,202116108,202116108,202116108,202116108,202116108,202116108,202116108,202116108,202116108,202116108,202116108,202113280,202116108,202113794,202116108,202116108,202116108,202116108,202116108,202116108,202116108,202116108,202116108,202116108,202116108,202116108,202116108,16780300,202116108,50465804,202116108,202116108,202116108,202116108,
202116108,202116108,202116108,202116108,202113280,202116108,202113794,202116108,202116108,202116108,202116108,202116108,202116108,202116108,202116108,202116108,202116108,202116108,202116108,202116108,16780300,202116108,50465804,202116108,202116108,202116108,202116108,202116108,202116108,202116108,202116108,202116108,202116108,202116108,202116108,202116108,202116108,202116108,202116108,202116108,202116108,202116108,202116108,202116108,202116108,202113280,202116108,202113794,202116108,202116108,202116108,202116108,202116108,202116108,202116108,202116108,202116108,202116108,202116108,202116108,202116108,16780300,202116108,50465804,
202116108,202116108,202116108,202116108,202116108,202116108,202116108,202116108,202113280,202116108,202113794,202116108,202116108,202116108,202116108,202116108,202116108,202116108,202116108,202116108,202116108,202116108,202116108,202116108,16780300,202116108,50465804,202116108,202116108,202116108,202116108,202116108,202116108,202113280,202116108,202113794,202116108,202116108,202116108,202116108,202116108,202116108,202116108,202116108,202116108,202116108,202116108,202116108,202116108,16780300,202116108,50465804,202116108,202116108,202116108,202116108,202116108,202116108,202116108,202116108,202116108,202116108,202116108,202116108,
202116108,202116108,202116108,202116108,202116108,202116108,202116108,202116108,202116108,202116108,202116108,202116108,202113280,202116108,202113794,202116108,202116108,202116108,202116108,202116108,202116108,202116108,202116108,202116108,202116108,202116108,202116108,202116108,16780300,202116108,50465804,202116108,202116108,202116108,202116108,202116108,202116108,202113280,202116108,202113794,202116108,202116108,202116108,202116108,202116108,202116108,202116108,202116108,202116108,202116108,202116108,202116108,202116108,16780300,202116108,50465804,202116108,202116108,202116108,202116108,202116108,202116108,202116108,202116108,
0,157696,0,147456,8,157696,192,147504,16,157696,400,147552,24,157696,592,147600,0,157760,800,147648,8,157760,992,147696,16,157760,1200,147744,24,157760,1392,147792,0,157856,1536,147840,8,157856,1728,147888,16,157856,1936,147936,24,157856,2128,147984,0,157920,2336,148032,8,157920,2528,148080,16,157920,2736,148128,24,157920,2928,148176,
0,157712,8,147456,8,157712,200,147504,16,157712,408,147552,24,157712,600,147600,0,157776,808,147648,8,157776,1000,147696,16,157776,1208,147744,24,157776,1400,147792,0,157872,1544,147840,8,157872,1736,147888,16,157872,1944,147936,24,157872,2136,147984,0,157936,2344,148032,8,157936,2536,148080,16,157936,2744,148128,24,157936,2936,148176,
0,157728,16,147456,8,157728,208,147504,16,157728,384,147552,24,157728,576,147600,0,157792,816,147648,8,157792,1008,147696,16,157792,1184,147744,24,157792,1376,147792,0,157824,1552,147840,8,157824,1744,147888,16,157824,1920,147936,24,157824,2112,147984,0,157888,2352,148032,8,157888,2544,148080,16,157888,2720,148128,24,157888,2912,148176,
0,157744,24,147456,8,157744,216,147504,16,157744,392,147552,24,157744,584,147600,0,157808,824,147648,8,157808,1016,147696,16,157808,1192,147744,24,157808,1384,147792,0,157840,1560,147840,8,157840,1752,147888,16,157840,1928,147936,24,157840,2120,147984,0,157904,2360,148032,8,157904,2552,148080,16,157904,2728,148128,24,157904,2920,148176,
1,0,157696,122368,2,192,157712,122372,3,400,157728,122376,4,592,157744,122380,5,800,157760,124416,6,992,157776,124420,7,1200,157792,124424,8,1392,157808,124428,9,1536,157824,126464,10,1728,157840,126468,11,1936,157856,126472,12,2128,157872,126476,13,2336,157888,128512,14,2528,157904,128516,15,2736,157920,128520,16,2928,157936,128524,
17,12288,157952,130560,18,12480,157968,130564,19,12688,157984,130568,20,12880,158000,130572,21,13088,158016,132608,22,13280,158032,132612,23,13488,158048,132616,24,13680,158064,132620,25,13824,158080,134656,26,14016,158096,134660,27,14224,158112,134664,28,14416,158128,134668,29,14624,158144,136704,30,14816,158160,136708,31,15024,158176,136712,32,15216,158192,136716,
33,24576,158208,138752,34,24768,158224,138756,35,24976,158240,138760,36,25168,158256,138764,37,25376,158272,140800,38,25568,158288,140804,39,25776,158304,140808,40,25968,158320,140812,41,26112,158336,142848,42,26304,158352,142852,43,26512,158368,142856,44,26704,158384,142860,45,26912,158400,144896,46,27104,158416,144900,47,27312,158432,144904,48,27504,158448,144908,
49,36864,158464,146944,50,37056,158480,146948,51,37264,158496,146952,52,37456,158512,146956,53,37664,158528,148992,54,37856,158544,148996,55,38064,158560,149000,56,38256,158576,149004,57,38400,158592,151040,58,38592,158608,151044,59,38800,158624,151048,60,38992,158640,151052,61,39200,158656,153088,62,39392,158672,153092,63,39600,158688,153096,64,39792,158704,153100,
147488,0,0,147456,147536,0,1,147504,147584,0,2,147552,147632,0,3,147600,147680,0,4,147648,147728,0,5,147696,147776,0,6,147744,147824,0,7,147792,149536,4,0,147840,149584,4,1,147888,149632,4,2,147936,149680,4,3,147984,149728,4,4,148032,149776,4,5,148080,149824,4,6,148128,149872,4,7,148176,
151584,8,0,148224,151632,8,1,148272,151680,8,2,148320,151728,8,3,148368,151776,8,4,148416,151824,8,5,148464,151872,8,6,148512,151920,8,7,148560,151584,8,0,148608,151632,8,1,148656,151680,8,2,148704,151728,8,3,148752,151776,8,4,148800,151824,8,5,148848,151872,8,6,148896,151920,8,7,148944,
151584,8,0,147456,151632,8,1,147504,151680,8,2,147552,151728,8,3,147600,151776,8,4,147648,151824,8,5,147696,151872,8,6,147744,151920,8,7,147792,151584,8,0,147840,151632,8,1,147888,151680,8,2,147936,151728,8,3,147984,151776,8,4,148032,151824,8,5,148080,151872,8,6,148128,151920,8,7,148176,
151584,8,0,148224,151632,8,1,148272,151680,8,2,148320,151728,8,3,148368,151776,8,4,148416,151824,8,5,148464,151872,8,6,148512,151920,8,7,148560,151584,8,0,148608,151632,8,1,148656,151680,8,2,148704,151728,8,3,148752,151776,8,4,148800,151824,8,5,148848,151872,8,6,148896,151920,8,7,148944};
__device__ __forceinline__ float bf_lo(unsigned u) { return __uint_as_float(u << 16); }
__device__ __forceinline__ float bf_hi(unsigned u) { return __uint_as_float(u & 0xffff0000u); }
__device__ __forceinline__ unsigned cvt_pk(float lo, float hi) { bf16x2 r = {(__bf16)lo, (__bf16)hi}; return __builtin_bit_cast(unsigned, r); }
__device__ __forceinline__ float rb(float v) { return bf_lo(cvt_pk(v, 0.f)); }
__global__ __launch_bounds__(256) __attribute__((aligned(4096))) void dcn_kernel(const unsigned short* x_, const unsigned short* off_, const unsigned short* w_, unsigned short* out_, void* ws_, Tiles tl_) {
    __shared__ __attribute__((aligned(128))) unsigned char lds[163840];
    asm volatile("" ::"v"((__attribute__((address_space(3))) unsigned char*)lds));
    unsigned tid = threadIdx.x, bid = blockIdx.x;
    const __attribute__((address_space(4))) Args* ka = (const __attribute__((address_space(4))) Args*)__builtin_amdgcn_kernarg_segment_ptr();
    asm volatile(
        "s_load_dwordx2 s[24:25], %1, 16\n\t"
        "s_load_dwordx2 s[16:17], %1, 0\n\t"
        "s_lshl_b32 s8, %2, 2\n\t"
        "s_add_u32 s8, s8, 40\n\t"
        "s_load_dword s7, %1, s8\n\t"
        "s_load_dwordx2 s[20:21], %1, 8\n\t"
        "s_load_dwordx2 s[28:29], %1, 24\n\t"
        "v_mov_b32 v156, %0\n\t"
        "s_mov_b32 s42, %2\n\t"
        "s_getpc_b64 s[44:45]\n\t"
        "s_and_b32 s44, s44, 0xffffff00\n\t"
        "9:\n\t"
        "s_mov_b32 s46, 99f-9b+3300\n\t"
        "s_mov_b32 s47, 0x20000\n\t"
        "v_lshlrev_b32 v160, 12, v156\n\t"
        "s_lshl_b32 s43, s42, 4\n\t"
        "s_and_b32 s43, s43, 3968\n\t"
        "s_mov_b32 m0, 161856\n\t"
        "v_cmp_gt_u32 vcc, 64, v156\n\t"
        "s_nop 1\n\t"
        "s_cbranch_vccz 10f\n\t"
        "buffer_load_dword v160, s[44:47], s43 offen lds\n\t"
        "10:\n\t"
        "v_and_b32 v157, 63, v156\n\t"
        "v_lshlrev_b32 v140, 4, v157\n\t"
        "s_mov_b64 s[64:65], %3\n\t"
        "s_mov_b32 s66, 71680\n\t"
        "s_mov_b32 s67, 0x20000\n\t"
        "v_lshlrev_b32 v155, 2, v157\n\t"
        "s_lshl_b32 s70, s42, 8\n\t"
        "s_mov_b32 s13, 65536\n\t"
        "s_mov_b32 s69, 69632\n\t"
        "buffer_load_dwordx4 v[128:131], v140, s[64:67], s13 offen\n\t"
        "buffer_load_dword v155, v155, s[64:67], s70 offen\n\t"
        "buffer_load_dwordx4 v[80:83], v140, s[64:67], s69 offen offset:1024\n\t"
        "buffer_load_dwordx4 v[116:119], v140, s[64:67], s69 offen offset:0\n\t"
        "buffer_load_dwordx4 v[144:147], v140, s[64:67], s13 offen offset:1024\n\t"
        "buffer_load_dwordx4 v[168:171], v140, s[64:67], s13 offen offset:2048\n\t"
        "buffer_load_dwordx4 v[184:187], v140, s[64:67], s13 offen offset:3072\n\t"
        "v_lshrrev_b32 v73, 6, v156\n\t"
        "v_and_b32 v158, 15, v156\n\t"
        "v_readfirstlane_b32 s4, v73\n\t"
        "s_mov_b32 s18, 9633792\n\t"
        "s_mov_b32 s19, 0x20000\n\t"
        "s_mov_b32 s26, 165888\n\t"
        "s_mov_b32 s27, 0x20000\n\t"
        "v_xor_b32 v142, 32, v140\n\t"
        "v_and_b32 v73, 32, v157\n\t"
        "v_xor_b32 v141, v73, v140\n\t"
        "s_lshr_b32 s68, s4, 1\n\t"
        "s_and_b32 s49, s4, 1\n\t"
        "s_mul_i32 s11, s49, 3072\n\t"
        "s_mul_i32 s36, s68, -55296\n\t"
        "s_add_u32 s36, s36, s11\n\t"
        "s_add_u32 s36, s36, 55296\n\t"
        "s_mul_i32 s37, s4, 3072\n\t"
        "s_add_u32 s37, s37, 86016\n\t"
        "s_lshl_b32 s8, s49, 5\n\t"
        "v_xor_b32 v152, s8, v140\n\t"
        "v_mul_u32_u24 v71, 171, v156\n\t"
        "v_lshrrev_b32 v71, 11, v71\n\t"
        "v_mul_u32_u24 v74, 12, v71\n\t"
        "v_sub_u32 v74, v156, v74\n\t"
        "v_lshrrev_b32 v75, 1, v71\n\t"
        "v_and_b32 v75, 2, v75\n\t"
        "v_xor_b32 v74, v74, v75\n\t"
        "v_lshlrev_b32 v72, 4, v74\n\t"
        "v_add_u32 v73, -1, v71\n\t"
        "v_cmp_lt_u32 vcc, 17, v73\n\t"
        "v_mov_b32 v75, 0x7ff00000\n\t"
        "v_add_u32 v161, -2, v71\n\t"
        "v_cndmask_b32 v162, v72, v75, vcc\n\t"
        "s_movk_i32 s9, 192\n\t"
        "s_lshl_b32 s35, s4, 10\n\t"
        "s_lshr_b32 s12, s42, 3\n\t"
        "s_lshl_b32 s12, s12, 2\n\t"
        "s_add_u32 s12, s12, s4\n\t"
        "s_mul_i32 s12, s12, 1296\n\t"
        "s_and_b32 s12, s12, 0xffffff80\n\t"
        "v_min_u32 v164, 11, v157\n\t"
        "v_lshlrev_b32 v164, 7, v164\n\t"
        "s_add_u32 m0, s37, -36864\n\t"
        "s_waitcnt lgkmcnt(0)\n\t"
        "buffer_load_dwordx4 v140, s[24:27], s36 offen offset:0 lds\n\t"
        "buffer_load_dwordx4 v141, s[24:27], s36 offen offset:1024 lds\n\t"
        "buffer_load_dwordx4 v142, s[24:27], s36 offen offset:2048 lds\n\t"
        "s_bfe_u32 s6, s7, 0x80008\n\t"
        "s_and_b32 s8, s7, 255\n\t"
        "v_add_u32 v161, s6, v161\n\t"
        "v_min_u32 v161, 223, v161\n\t"
        "s_mul_i32 s34, s8, 43008\n\t"
        "v_mad_u32_u24 v143, v161, s9, v162\n\t"
        "s_sub_u32 s34, s34, 43008\n\t"
        "s_mul_i32 s41, s8, 224\n\t"
        "s_add_u32 s41, s41, s6\n\t"
        "buffer_load_dword v165, v164, s[24:27], s12 offen\n\t"
        "s_add_u32 m0, s35, 8192\n\t"
        "s_add_u32 s43, s34, 86016\n\t"
        "buffer_load_dwordx4 v143, s[16:19], s43 offen  lds\n\t"
        "s_add_u32 m0, s35, 24576\n\t"
        "s_add_u32 s43, s34, 258048\n\t"
        "buffer_load_dwordx4 v143, s[16:19], s43 offen  lds\n\t"
        "s_mul_i32 s43, s4, 1536\n\t"
        "s_add_u32 m0, s43, 43008\n\t"
        "s_add_u32 s43, s43, 110592\n\t"
        "buffer_load_dwordx4 v152, s[24:27], s43 offen lds\n\t"
        "buffer_load_dwordx4 v152, s[24:27], s43 offen offset:512 lds\n\t"
        "s_add_u32 m0, s35, 12288\n\t"
        "s_add_u32 s43, s34, 129024\n\t"
        "buffer_load_dwordx4 v143, s[16:19], s43 offen  lds\n\t"
        "s_add_u32 m0, s35, 28672\n\t"
        "s_add_u32 s43, s34, 301056\n\t"
        "s_min_u32 s43, s43, 9590784\n\t"
        "buffer_load_dwordx4 v143, s[16:19], s43 offen  lds\n\t"
        "s_add_u32 m0, s37, -24576\n\t"
        "s_add_u32 s36, s36, 6144\n\t"
        "buffer_load_dwordx4 v140, s[24:27], s36 offen offset:0 lds\n\t"
        "buffer_load_dwordx4 v141, s[24:27], s36 offen offset:1024 lds\n\t"
        "s_mul_i32 s39, s68, 6144\n\t"
        "s_add_u32 s39, s39, 55296\n\t"
        "s_mul_i32 s38, s68, 159744\n\t"
        "s_add_u32 s38, s38, -104448\n\t"
        "s_mul_i32 s5, s68, -165888\n\t"
        "s_add_u32 s5, s5, 61440\n\t"
        "s_mov_b32 s22, 1806336\n\t"
        "s_mov_b32 s23, 0x20000\n\t"
        "s_mov_b32 s30, 9633792\n\t"
        "s_mov_b32 s31, 0x20000\n\t"
        "s_mov_b32 s54, 0x7fff7fff\n\t"
        "s_lshl_b32 s8, s4, 2\n\t"
        "s_add_u32 s8, s8, 161792\n\t"
        "v_mov_b32 v153, s8\n\t"
        "v_mov_b32 v182, 161792\n\t"
        "v_mov_b32 v76, 0\n\t"
        "v_mov_b32 v77, 0\n\t"
        "v_lshlrev_b32 v73, 3, v156\n\t"
        "v_add_u32 v73, 147456, v73\n\t"
        "s_add_u32 s60, s35, 153600\n\t"
        "v_mov_b32 v74, s60\n\t"
        "s_cmp_lt_u32 s4, 3\n\t"
        "s_cselect_b64 exec, -1, 0\n\t"
        "ds_write_b64 v73, v[76:77] offset:0\n\t"
        "ds_write_b64 v73, v[76:77] offset:2048\n\t"
        "ds_write_b64 v73, v[76:77] offset:4096\n\t"
        "s_mov_b64 exec, 1\n\t"
        "ds_write_b32 v153, v76 offset:16\n\t"
        "ds_write_b64 v74, v[76:77]\n\t"
        "s_mov_b64 exec, -1\n\t"
        "v_bfe_i32 v74, v156, 5, 1\n\t"
        "v_and_b32 v74, 7, v74\n\t"
        "v_xor_b32 v74, s4, v74\n\t"
        "v_mul_u32_u24 v68, 11, v74\n\t"
        "v_lshrrev_b32 v68, 5, v68\n\t"
        "v_mul_u32_u24 v75, 3, v68\n\t"
        "v_sub_u32 v69, v74, v75\n\t"
        "v_lshlrev_b32 v70, 2, v74\n\t"
        "v_bfe_u32 v52, v156, 4, 1\n\t"
        "v_and_b32 v181, 48, v156\n\t"
        "s_mov_b64 s[56:57], 0\n\t"
        "s_mov_b32 s63, 0x435f0000\n\t"
        "s_bfe_u32 s8, s7, 0x20010\n\t"
        "s_cmp_eq_u32 s8, 0\n\t"
        "s_cbranch_scc1 20f\n\t"
        "s_bitcmp1_b32 s7, 18\n\t"
        "s_cbranch_scc1 21f\n\t"
        "s_mul_i32 s69, s68, 16384\n\t"
        "buffer_load_dwordx4 v142, s[24:27], s36 offen offset:2048 lds\n\t"
        "s_lshl_b32 s70, s49, 7\n\t"
        "s_lshl_b32 s71, s49, 5\n\t"
        "s_waitcnt vmcnt(19)\n\t"
        "v_add_u32 v128, s69, v128\n\t"
        "v_add_u32 v129, s69, v129\n\t"
        "v_add_u32 v130, s69, v130\n\t"
        "v_xad_u32 v133, v131, s71, 64\n\t"
        "v_add_u32 v131, s70, v131\n\t"
        "v_xor_b32 v132, 32, v131\n\t"
        "v_subrev_u32 v138, 43008, v131\n\t"
        "s_add_u32 m0, s35, 4096\n\t"
        "s_add_u32 s43, s34, 43008\n\t"
        "buffer_load_dwordx4 v143, s[16:19], s43 offen  lds\n\t"
        "v_subrev_u32 v139, 43008, v132\n\t"
        "v_subrev_u32 v149, 43008, v133\n\t"
        "s_waitcnt vmcnt(18)\n\t"
        "ds_bpermute_b32 v67, v81, v155 offset:172\n\t"
        "ds_bpermute_b32 v78, v81, v155 offset:148\n\t"
        "ds_bpermute_b32 v79, v81, v155 offset:160\n\t"
        "v_readlane_b32 s69, v155, 34\n\t"
        "v_readlane_b32 s70, v155, 35\n\t"
        "v_readlane_b32 s46, v155, 43\n\t"
        "v_readlane_b32 s47, v155, 44\n\t"
        "s_add_u32 m0, s35, 20480\n\t"
        "s_add_u32 s43, s34, 215040\n\t"
        "buffer_load_dwordx4 v143, s[16:19], s43 offen  lds\n\t"
        "v_readlane_b32 s55, v155, 45\n\t"
        "s_lshl_b32 s71, s49, 3\n\t"
        "v_add_u32 v82, s71, v82\n\t"
        "v_add_u32 v53, v70, v83\n\t"
        "v_mad_u32_u24 v70, v52, s69, v70\n\t"
        "v_mad_u32_u24 v70, v158, s70, v70\n\t"
        "s_mul_i32 s71, s69, s68\n\t"
        "s_waitcnt lgkmcnt(2)\n\t"
        "v_mad_u32_u24 v67, v82, s70, v67\n\t"
        "v_add_u32 v67, s71, v67\n\t"
        "v_readlane_b32 s11, v155, 30\n\t"
        "s_movk_i32 s10, 223\n\t"
        "v_mov_b32 v75, 0x7ff00000\n\t"
        "v_readlane_b32 s12, v155, 24\n\t"
        "v_readlane_b32 s13, v155, 25\n\t"
        "v_readlane_b32 s70, v155, 27\n\t"
        "s_nop 1\n\t"
        "s_or_b32 s42, s42, s70\n\t"
        "s_bfe_u32 s70, s12, 524288\n\t"
        "s_bfe_u32 s71, s13, 524288\n\t"
        "v_cmp_gt_u32 vcc, s71, v71\n\t"
        "v_add3_u32 v73, v71, s70, -2\n\t"
        "v_med3_i32 v73, v73, 0, s10\n\t"
        "v_cndmask_b32 v74, v75, v72, vcc\n\t"
        "v_mad_u32_u24 v134, v73, s11, v74\n\t"
        "s_bfe_u32 s70, s12, 524296\n\t"
        "s_bfe_u32 s71, s13, 524296\n\t"
        "v_cmp_gt_u32 vcc, s71, v71\n\t"
        "v_add3_u32 v73, v71, s70, -2\n\t"
        "v_med3_i32 v73, v73, 0, s10\n\t"
        "v_cndmask_b32 v74, v75, v72, vcc\n\t"
        "v_mad_u32_u24 v135, v73, s11, v74\n\t"
        "s_bfe_u32 s70, s12, 524304\n\t"
        "s_bfe_u32 s71, s13, 524304\n\t"
        "v_cmp_gt_u32 vcc, s71, v71\n\t"
        "v_add3_u32 v73, v71, s70, -2\n\t"
        "v_med3_i32 v73, v73, 0, s10\n\t"
        "v_cndmask_b32 v74, v75, v72, vcc\n\t"
        "v_mad_u32_u24 v136, v73, s11, v74\n\t"
        "s_bfe_u32 s70, s12, 524312\n\t"
        "s_bfe_u32 s71, s13, 524312\n\t"
        "v_cmp_gt_u32 vcc, s71, v71\n\t"
        "v_add3_u32 v73, v71, s70, -2\n\t"
        "v_med3_i32 v73, v73, 0, s10\n\t"
        "v_cndmask_b32 v74, v75, v72, vcc\n\t"
        "v_mad_u32_u24 v137, v73, s11, v74\n\t"
        "v_readlane_b32 s44, v155, 32\n\t"
        "v_readlane_b32 s45, v155, 33\n\t"
        "v_readlane_b32 s62, v155, 36\n\t"
        "s_mov_b32 exec_lo, 0\n\t"
        "s_mov_b32 exec_hi, 0xfff00000\n\t"
        "s_waitcnt vmcnt(18)\n\t"
        "ds_write_b32 v119, v155\n\t"
        "s_mov_b64 exec, -1\n\t"
        "s_lshl_b32 s70, s62, 5\n\t"
        "s_sub_u32 s71, s70, 16384\n\t"
        "s_mul_i32 s71, s71, s68\n\t"
        "v_add_u32 v172, s71, v128\n\t"
        "v_add_u32 v173, s71, v129\n\t"
        "v_add_u32 v174, s71, v130\n\t"
        "s_add_u32 s71, s71, s70\n\t"
        "v_add_u32 v175, s71, v128\n\t"
        "v_add_u32 v176, s71, v129\n\t"
        "v_add_u32 v177, s71, v130\n\t"
        "s_add_u32 s71, s71, s70\n\t"
        "v_add_u32 v178, s71, v128\n\t"
        "v_add_u32 v179, s71, v129\n\t"
        "v_add_u32 v180, s71, v130\n\t"
        "s_cmp_eq_u32 s69, 36\n\t"
        "s_cselect_b64 exec, -1, 0\n\t"
        "v_swap_b32 v68, v69\n\t"
        "v_swap_b32 v173, v175\n\t"
        "v_swap_b32 v174, v178\n\t"
        "v_swap_b32 v177, v179\n\t"
        "s_mov_b64 exec, -1\n\t"
        "v_add3_u32 v68, v68, v52, -1\n\t"
        "v_add3_u32 v69, v69, v158, -1\n\t"
        "v_cvt_f32_i32 v68, v68\n\t"
        "v_cvt_f32_i32 v69, v69\n\t"
        "s_add_u32 s70, s68, 1\n\t"
        "v_cvt_f32_u32 v73, s70\n\t"
        "s_waitcnt lgkmcnt(1)\n\t"
        "v_add_f32 v78, v78, v73\n\t"
        "v_add_u32 v73, 1, v82\n\t"
        "v_cvt_f32_u32 v73, v73\n\t"
        "v_add_f32 v79, v79, v73\n\t"
        "s_mul_i32 s70, s4, 384\n\t"
        "v_add_u32 v80, s70, v80\n\t"
        "s_mov_b64 exec, 0\n\t"
        "s_cmp_eq_u32 s4, 0\n\t"
        "s_cbranch_scc1 .LpM0%=\n\t"
        "s_cmp_eq_u32 s4, 1\n\t"
        "s_cbranch_scc1 .LpM1%=\n\t"
        "s_cmp_eq_u32 s4, 2\n\t"
        "s_cbranch_scc1 .LpM2%=\n\t"
        ".LpM2%=:\n\t"
        ".LrM%=:\n\t"
        "s_mov_b64 exec, -1\n\t"
        "s_waitcnt vmcnt(9) lgkmcnt(0)\n\t"
        "s_barrier\n\t"
        "ds_read_b64_tr_b16 a[0:1], v138 offset:6144\n\t"
        "ds_read_b64_tr_b16 a[2:3], v138 offset:6912\n\t"
        "ds_read_b64_tr_b16 a[4:5], v139 offset:6144\n\t"
        "ds_read_b64_tr_b16 a[6:7], v139 offset:6912\n\t"
        "ds_read_b64_tr_b16 a[8:9], v149 offset:6144\n\t"
        "ds_read_b64_tr_b16 a[10:11], v149 offset:6912\n\t"
        "ds_read_b128 v[84:87], v128 offset:8192\n\t"
        "ds_read_b64_tr_b16 a[12:13], v138 offset:12288\n\t"
        "ds_read_b64_tr_b16 a[14:15], v138 offset:13056\n\t"
        "ds_read_b64_tr_b16 a[16:17], v139 offset:12288\n\t"
        "ds_read_b64_tr_b16 a[18:19], v139 offset:13056\n\t"
        "ds_read_b64_tr_b16 a[20:21], v149 offset:12288\n\t"
        "ds_read_b64_tr_b16 a[22:23], v149 offset:13056\n\t"
        "ds_read_b128 v[88:91], v128 offset:8192\n\t"
        "s_add_u32 m0, s37, -12288\n\t"
        "s_add_u32 s36, s36, s39\n\t"
        "s_waitcnt lgkmcnt(7)\n\t"
        "v_mfma_f32_16x16x32_bf16 v[12:15], a[0:3], v[84:87], 0\n\t"
        "buffer_load_dwordx4 v140, s[24:27], s36 offen offset:0 lds\n\t"
        "buffer_load_dwordx4 v141, s[24:27], s36 offen offset:1024 lds\n\t"
        "buffer_load_dwordx4 v142, s[24:27], s36 offen offset:2048 lds\n\t"
        "s_add_u32 m0, s35, 16384\n\t"
        "s_add_u32 s43, s34, 172032\n\t"
        "buffer_load_dwordx4 v143, s[16:19], s43 offen  lds\n\t"
        "s_add_u32 m0, s35, 32768\n\t"
        "s_add_u32 s43, s34, 344064\n\t"
        "s_min_u32 s43, s43, 9590784\n\t"
        "buffer_load_dwordx4 v143, s[16:19], s43 offen  lds\n\t"
        "s_waitcnt vmcnt(10)\n\t"
        "s_barrier\n\t"
        "ds_read_b128 v[92:95], v128 offset:12288\n\t"
        "s_add_u32 m0, s37, 0\n\t"
        "ds_read_b64_tr_b16 a[24:25], v138 offset:0\n\t"
        "s_add_u32 s36, s36, s38\n\t"
        "v_mfma_f32_16x16x32_bf16 v[16:19], a[4:7], v[84:87], 0\n\t"
        "buffer_load_dwordx4 v140, s[24:27], s36 offen offset:0 lds\n\t"
        "ds_read_b64_tr_b16 a[26:27], v138 offset:768\n\t"
        "v_lshrrev_b32 v73, 5, v156\n\t"
        "v_mfma_f32_16x16x32_bf16 v[20:23], a[8:11], v[84:87], 0\n\t"
        "buffer_load_dwordx4 v141, s[24:27], s36 offen offset:1024 lds\n\t"
        "ds_read_b64_tr_b16 a[28:29], v139 offset:0\n\t"
        "v_mul_u32_u24 v73, 8064, v73\n\t"
        "s_waitcnt lgkmcnt(4)\n\t"
        "v_mfma_f32_16x16x32_bf16 v[24:27], a[12:15], v[88:91], 0\n\t"
        "ds_read_b64_tr_b16 a[30:31], v139 offset:768\n\t"
        "ds_read_b64_tr_b16 a[32:33], v149 offset:0\n\t"
        "ds_read_b64_tr_b16 a[34:35], v149 offset:768\n\t"
        "v_and_b32 v55, 496, v140\n\t"
        "v_mfma_f32_16x16x32_bf16 v[28:31], a[16:19], v[88:91], 0\n\t"
        "buffer_load_dwordx4 v142, s[24:27], s36 offen offset:2048 lds\n\t"
        "ds_read_b128 v[96:99], v128 offset:8192\n\t"
        "v_add_u32 v55, v55, v73\n\t"
        "v_mfma_f32_16x16x32_bf16 v[32:35], a[20:23], v[88:91], 0\n\t"
        "ds_read_b128 v[100:103], v128 offset:12288\n\t"
        "v_lshl_add_u32 v61, v158, 2, v73\n\t"
        "s_waitcnt lgkmcnt(8)\n\t"
        "v_mfma_f32_16x16x32_bf16 v[24:27], a[0:3], v[92:95], v[24:27]\n\t"
        "s_mul_i32 s48, s41, 36\n\t"
        "v_readlane_b32 s69, v155, 31\n\t"
        "v_mfma_f32_16x16x32_bf16 v[28:31], a[4:7], v[92:95], v[28:31]\n\t"
        "buffer_load_dword v67, v67, s[20:23], 0 offen offset:32\n\t"
        "v_mov_b32 v154, s69\n\t"
        "v_mfma_f32_16x16x32_bf16 v[32:35], a[8:11], v[92:95], v[32:35]\n\t"
        "v_mov_b32 v119, s62\n\t"
        "v_add_u32 v117, s37, v117\n\t"
        "v_mfma_f32_16x16x32_bf16 v[36:39], a[12:15], v[92:95], 0\n\t"
        "v_add_u32 v118, s35, v118\n\t"
        "v_add_u32 v185, s35, v185\n\t"
        "v_mfma_f32_16x16x32_bf16 v[40:43], a[16:19], v[92:95], 0\n\t"
        "v_add_u32 v186, s37, v186\n\t"
        "v_readlane_b32 s53, v155, 28\n\t"
        "v_mfma_f32_16x16x32_bf16 v[44:47], a[20:23], v[92:95], 0\n\t"
        "s_waitcnt vmcnt(9) lgkmcnt(2)\n\t"
        "s_barrier\n\t"
        "buffer_load_dword v64, v70, s[20:23], s46 offen\n\t"
        "ds_read_b128 v[104:107], v128 offset:4096\n\t"
        "s_waitcnt lgkmcnt(2)\n\t"
        "v_mfma_f32_16x16x32_bf16 v[0:3], a[24:27], v[96:99], 0\n\t"
        "ds_read_b64_tr_b16 a[36:37], v138 offset:18432\n\t"
        "ds_read_b64_tr_b16 a[38:39], v138 offset:19200\n\t"
        "s_lshl_b32 s69, s68, 10\n\t"
        "v_mfma_f32_16x16x32_bf16 v[4:7], a[28:31], v[96:99], 0\n\t"
        "buffer_load_dword v66, v70, s[20:23], s55 offen\n\t"
        "s_add_u32 m0, s37, 12288\n\t"
        "ds_read_b64_tr_b16 a[40:41], v139 offset:18432\n\t"
        "s_add_u32 s36, s36, s5\n\t"
        "v_mfma_f32_16x16x32_bf16 v[8:11], a[32:35], v[96:99], 0\n\t"
        "ds_read_b64_tr_b16 a[42:43], v139 offset:19200\n\t"
        "ds_read_b64_tr_b16 a[44:45], v149 offset:18432\n\t"
        "s_add_u32 s69, s69, 135168\n\t"
        "s_waitcnt lgkmcnt(6)\n\t"
        "v_mfma_f32_16x16x32_bf16 v[12:15], a[24:27], v[100:103], v[12:15]\n\t"
        "ds_read_b64_tr_b16 a[46:47], v149 offset:19200\n\t"
        "ds_read_b64_tr_b16 a[48:49], v138 offset:24576\n\t"
        "ds_read_b64_tr_b16 a[50:51], v138 offset:25344\n\t"
        "s_lshl_b32 s70, s49, 5\n\t"
        "v_mfma_f32_16x16x32_bf16 v[16:19], a[28:31], v[100:103], v[16:19]\n\t"
        "buffer_load_dwordx4 v140, s[24:27], s36 offen offset:0 lds\n\t"
        "ds_read_b64_tr_b16 a[52:53], v139 offset:24576\n\t"
        "ds_read_b64_tr_b16 a[54:55], v139 offset:25344\n\t"
        "v_xor_b32 v146, s70, v146\n\t"
        "v_mfma_f32_16x16x32_bf16 v[20:23], a[32:35], v[100:103], v[20:23]\n\t"
        "ds_read_b64_tr_b16 a[56:57], v149 offset:24576\n\t"
        "ds_read_b64_tr_b16 a[58:59], v149 offset:25344\n\t"
        "ds_read_b128 v[108:111], v128 offset:4160\n\t"
        "v_add_u32 v146, s69, v146\n\t"
        "s_waitcnt lgkmcnt(12)\n\t"
        "v_mfma_f32_16x16x32_bf16 v[0:3], a[0:3], v[104:107], v[0:3]\n\t"
        "s_lshl_b32 s70, s49, 11\n\t"
        "s_add_u32 s70, s70, s69\n\t"
        "v_mfma_f32_16x16x32_bf16 v[4:7], a[4:7], v[104:107], v[4:7]\n\t"
        "buffer_load_dwordx4 v141, s[24:27], s36 offen offset:1024 lds\n\t"
        "v_add_u32 v147, s70, v147\n\t"
        "v_mfma_f32_16x16x32_bf16 v[8:11], a[8:11], v[104:107], v[8:11]\n\t"
        "s_mul_i32 s41, s41, 192\n\t"
        "s_mul_i32 s69, s68, 172032\n\t"
        "v_mfma_f32_16x16x32_bf16 v[12:15], a[12:15], v[104:107], v[12:15]\n\t"
        "s_add_u32 s41, s41, s69\n\t"
        "s_mul_i32 s69, s49, 43008\n\t"
        "v_mfma_f32_16x16x32_bf16 v[16:19], a[16:19], v[104:107], v[16:19]\n\t"
        "s_add_u32 s50, s41, s69\n\t"
        "s_lshl_b32 s70, s49, 7\n\t"
        "v_mfma_f32_16x16x32_bf16 v[20:23], a[20:23], v[104:107], v[20:23]\n\t"
        "v_add_u32 v144, s70, v144\n\t"
        "s_mul_i32 s69, s68, 768\n\t"
        "s_waitcnt lgkmcnt(0)\n\t"
        "v_mfma_f32_16x16x32_bf16 v[0:3], a[36:39], v[108:111], v[0:3]\n\t"
        "buffer_load_dwordx4 v142, s[24:27], s36 offen offset:2048 lds\n\t"
        "s_add_u32 m0, s35, 0\n\t"
        "ds_read_b128 v[112:115], v128 offset:8256\n\t"
        "s_add_u32 s43, s34, 0\n\t"
        "v_mfma_f32_16x16x32_bf16 v[4:7], a[40:43], v[108:111], v[4:7]\n\t"
        "v_add_u32 v187, s69, v187\n\t"
        "v_readlane_b32 s9, v155, 46\n\t"
        "v_mfma_f32_16x16x32_bf16 v[8:11], a[44:47], v[108:111], v[8:11]\n\t"
        "s_sub_u32 s10, 43200, s11\n\t"
        "s_mul_i32 s10, s10, s68\n\t"
        "v_mfma_f32_16x16x32_bf16 v[12:15], a[48:51], v[108:111], v[12:15]\n\t"
        "s_sub_u32 s11, s11, 192\n\t"
        "v_mad_u32_u24 v148, v158, s11, v144\n\t"
        "v_mfma_f32_16x16x32_bf16 v[16:19], a[52:55], v[108:111], v[16:19]\n\t"
        "v_add_u32 v148, s10, v148\n\t"
        "v_lshrrev_b32 v73, 2, v157\n\t"
        "v_mfma_f32_16x16x32_bf16 v[20:23], a[56:59], v[108:111], v[20:23]\n\t"
        "v_mad_u32_u24 v150, v73, s11, v145\n\t"
        "s_lshl_b32 s70, s49, 1\n\t"
        "s_waitcnt lgkmcnt(0)\n\t"
        "v_mfma_f32_16x16x32_bf16 v[12:15], a[36:39], v[112:115], v[12:15]\n\t"
        "ds_read_b128 v[84:87], v128 offset:12352\n\t"
        "s_add_u32 s70, s70, 49\n\t"
        "v_readlane_b32 s52, v155, s70\n\t"
        "v_mfma_f32_16x16x32_bf16 v[16:19], a[40:43], v[112:115], v[16:19]\n\t"
        "s_nop 0\n\t"
        "s_add_u32 s52, s52, s10\n\t"
        "v_mfma_f32_16x16x32_bf16 v[20:23], a[44:47], v[112:115], v[20:23]\n\t"
        "v_mfma_f32_16x16x32_bf16 v[24:27], a[48:51], v[112:115], v[24:27]\n\t"
        "buffer_load_dwordx4 v143, s[16:19], s43 offen  lds\n\t"
        "s_add_u32 m0, s35, 36864\n\t"
        "s_add_u32 s43, s34, 387072\n\t"
        "s_min_u32 s43, s43, 9590784\n\t"
        "v_mfma_f32_16x16x32_bf16 v[28:31], a[52:55], v[112:115], v[28:31]\n\t"
        "v_mfma_f32_16x16x32_bf16 v[32:35], a[56:59], v[112:115], v[32:35]\n\t"
        "buffer_load_dwordx4 v143, s[16:19], s43 offen  lds\n\t"
        "s_waitcnt vmcnt(11)\n\t"
        "s_barrier\n\t"
        "ds_read_b128 v[88:91], v128 offset:16384\n\t"
        "s_add_u32 m0, s37, 24576\n\t"
        "ds_read_b64_tr_b16 a[60:61], v138 offset:30720\n\t"
        "s_add_u32 s36, s36, s39\n\t"
        "s_waitcnt lgkmcnt(2)\n\t"
        "v_mfma_f32_16x16x32_bf16 v[24:27], a[36:39], v[84:87], v[24:27]\n\t"
        "buffer_load_dwordx4 v140, s[24:27], s36 offen offset:0 lds\n\t"
        "ds_read_b64_tr_b16 a[62:63], v138 offset:31488\n\t"
        "v_mfma_f32_16x16x32_bf16 v[28:31], a[40:43], v[84:87], v[28:31]\n\t"
        "ds_read_b64_tr_b16 a[64:65], v139 offset:30720\n\t"
        "ds_read_b64_tr_b16 a[66:67], v139 offset:31488\n\t"
        "v_mfma_f32_16x16x32_bf16 v[32:35], a[44:47], v[84:87], v[32:35]\n\t"
        "buffer_load_dwordx4 v141, s[24:27], s36 offen offset:1024 lds\n\t"
        "ds_read_b64_tr_b16 a[68:69], v149 offset:30720\n\t"
        "v_mfma_f32_16x16x32_bf16 v[36:39], a[48:51], v[84:87], v[36:39]\n\t"
        "ds_read_b64_tr_b16 a[70:71], v149 offset:31488\n\t"
        "ds_read_b128 v[92:95], v128 offset:8256\n\t"
        "v_mfma_f32_16x16x32_bf16 v[40:43], a[52:55], v[84:87], v[40:43]\n\t"
        "ds_read_b128 v[96:99], v128 offset:12352\n\t"
        "ds_read_b128 v[100:103], v128 offset:16448\n\t"
        "v_mfma_f32_16x16x32_bf16 v[44:47], a[56:59], v[84:87], v[44:47]\n\t"
        "ds_read_b64_tr_b16 a[72:73], v138 offset:36864\n\t"
        "ds_read_b64_tr_b16 a[74:75], v138 offset:37632\n\t"
        "s_waitcnt lgkmcnt(11)\n\t"
        "v_mfma_f32_16x16x32_bf16 v[24:27], a[24:27], v[88:91], v[24:27]\n\t"
        "buffer_load_dwordx4 v142, s[24:27], s36 offen offset:2048 lds\n\t"
        "ds_read_b64_tr_b16 a[76:77], v139 offset:36864\n\t"
        "v_mfma_f32_16x16x32_bf16 v[28:31], a[28:31], v[88:91], v[28:31]\n\t"
        "ds_read_b64_tr_b16 a[78:79], v139 offset:37632\n\t"
        "ds_read_b64_tr_b16 a[80:81], v149 offset:36864\n\t"
        "v_mfma_f32_16x16x32_bf16 v[32:35], a[32:35], v[88:91], v[32:35]\n\t"
        "ds_read_b64_tr_b16 a[82:83], v149 offset:37632\n\t"
        "v_mfma_f32_16x16x32_bf16 v[36:39], a[0:3], v[88:91], v[36:39]\n\t"
        "v_mfma_f32_16x16x32_bf16 v[40:43], a[4:7], v[88:91], v[40:43]\n\t"
        "buffer_load_dwordx4 v[48:51], v55, s[20:23], s48 offen\n\t"
        "v_mfma_f32_16x16x32_bf16 v[44:47], a[8:11], v[88:91], v[44:47]\n\t"
        "s_waitcnt lgkmcnt(8)\n\t"
        "v_mfma_f32_16x16x32_bf16 v[0:3], a[60:63], v[92:95], v[0:3]\n\t"
        "v_mfma_f32_16x16x32_bf16 v[4:7], a[64:67], v[92:95], v[4:7]\n\t"
        "v_mfma_f32_16x16x32_bf16 v[8:11], a[68:71], v[92:95], v[8:11]\n\t"
        "buffer_load_dword v61, v61, s[20:23], s48 offen offset:512\n\t"
        "s_add_u32 m0, s37, 36864\n\t"
        "s_waitcnt lgkmcnt(4)\n\t"
        "v_mfma_f32_16x16x32_bf16 v[12:15], a[60:63], v[96:99], v[12:15]\n\t"
        "s_add_u32 s36, s36, s38\n\t"
        "v_mfma_f32_16x16x32_bf16 v[16:19], a[64:67], v[96:99], v[16:19]\n\t"
        "v_mfma_f32_16x16x32_bf16 v[20:23], a[68:71], v[96:99], v[20:23]\n\t"
        "v_mfma_f32_16x16x32_bf16 v[24:27], a[60:63], v[100:103], v[24:27]\n\t"
        "ds_read_b128 v[104:107], v128 offset:4224\n\t"
        "v_mfma_f32_16x16x32_bf16 v[28:31], a[64:67], v[100:103], v[28:31]\n\t"
        "v_mfma_f32_16x16x32_bf16 v[32:35], a[68:71], v[100:103], v[32:35]\n\t"
        "v_mfma_f32_16x16x32_bf16 v[36:39], a[36:39], v[100:103], v[36:39]\n\t"
        "ds_read_b128 v[108:111], v128 offset:8320\n\t"
        "v_mfma_f32_16x16x32_bf16 v[40:43], a[40:43], v[100:103], v[40:43]\n\t"
        "v_mfma_f32_16x16x32_bf16 v[44:47], a[44:47], v[100:103], v[44:47]\n\t"
        "s_waitcnt lgkmcnt(1)\n\t"
        "v_mfma_f32_16x16x32_bf16 v[0:3], a[72:75], v[104:107], v[0:3]\n\t"
        "ds_read_b128 v[112:115], v128 offset:12416\n\t"
        "v_mfma_f32_16x16x32_bf16 v[4:7], a[76:79], v[104:107], v[4:7]\n\t"
        "s_waitcnt vmcnt(13)\n\t"
        "s_barrier\n\t"
        "buffer_load_dwordx4 v140, s[24:27], s36 offen offset:0 lds\n\t"
        "ds_read_b64_tr_b16 a[84:85], v131 offset:0\n\t"
        "ds_read_b64_tr_b16 a[86:87], v131 offset:768\n\t"
        "v_mfma_f32_16x16x32_bf16 v[8:11], a[80:83], v[104:107], v[8:11]\n\t"
        "ds_read_b64_tr_b16 a[88:89], v132 offset:0\n\t"
        "ds_read_b64_tr_b16 a[90:91], v132 offset:768\n\t"
        "ds_read_b64_tr_b16 a[92:93], v133 offset:0\n\t"
        "ds_read_b64_tr_b16 a[94:95], v133 offset:768\n\t"
        "ds_read_b128 v[84:87], v128 offset:4224\n\t"
        "s_waitcnt lgkmcnt(8)\n\t"
        "v_mfma_f32_16x16x32_bf16 v[12:15], a[72:75], v[108:111], v[12:15]\n\t"
        "ds_read_b64_tr_b16 a[96:97], v131 offset:6144\n\t"
        "ds_read_b64_tr_b16 a[98:99], v131 offset:6912\n\t"
        "ds_read_b64_tr_b16 a[100:101], v132 offset:6144\n\t"
        "v_mfma_f32_16x16x32_bf16 v[16:19], a[76:79], v[108:111], v[16:19]\n\t"
        "ds_read_b64_tr_b16 a[102:103], v132 offset:6912\n\t"
        "ds_read_b64_tr_b16 a[104:105], v133 offset:6144\n\t"
        "ds_read_b64_tr_b16 a[106:107], v133 offset:6912\n\t"
        "v_mfma_f32_16x16x32_bf16 v[20:23], a[80:83], v[108:111], v[20:23]\n\t"
        "buffer_load_dwordx4 v141, s[24:27], s36 offen offset:1024 lds\n\t"
        "ds_read_b128 v[88:91], v128 offset:8320\n\t"
        "s_waitcnt lgkmcnt(14)\n\t"
        "v_mfma_f32_16x16x32_bf16 v[24:27], a[72:75], v[112:115], v[24:27]\n\t"
        "ds_read_b128 v[92:95], v128 offset:12416\n\t"
        "v_mfma_f32_16x16x32_bf16 v[28:31], a[76:79], v[112:115], v[28:31]\n\t"
        "ds_read_b128 v[96:99], v128 offset:16512\n\t"
        "v_mfma_f32_16x16x32_bf16 v[32:35], a[80:83], v[112:115], v[32:35]\n\t"
        "s_waitcnt lgkmcnt(9)\n\t"
        "v_mfma_f32_16x16x32_bf16 v[12:15], a[84:87], v[84:87], v[12:15]\n\t"
        "buffer_load_dwordx4 v142, s[24:27], s36 offen offset:2048 lds\n\t"
        "v_readlane_b32 s40, v155, 12\n\t"
        "v_readlane_b32 s43, v155, 0\n\t"
        "v_mfma_f32_16x16x32_bf16 v[16:19], a[88:91], v[84:87], v[16:19]\n\t"
        "s_add_u32 m0, s40, s35\n\t"
        "v_mfma_f32_16x16x32_bf16 v[20:23], a[92:95], v[84:87], v[20:23]\n\t"
        "v_readlane_b32 s46, v155, 37\n\t"
        "v_readlane_b32 s47, v155, 40\n\t"
        "s_waitcnt vmcnt(14)\n\t"
        "v_perm_b32 v56, v64, v64, s44\n\t"
        "s_waitcnt lgkmcnt(2)\n\t"
        "v_mfma_f32_16x16x32_bf16 v[0:3], a[96:99], v[88:91], v[0:3]\n\t"
        "v_perm_b32 v57, v64, v64, s45\n\t"
        "v_add_f32 v58, s46, v68\n\t"
        "v_mfma_f32_16x16x32_bf16 v[4:7], a[100:103], v[88:91], v[4:7]\n\t"
        "buffer_load_dwordx4 v134, s[16:19], s43 offen  lds\n\t"
        "v_readlane_b32 s40, v155, 13\n\t"
        "v_mfma_f32_16x16x32_bf16 v[8:11], a[104:107], v[88:91], v[8:11]\n\t"
        "v_readlane_b32 s43, v155, 1\n\t"
        "s_add_u32 m0, s40, s35\n\t"
        "v_mfma_f32_16x16x32_bf16 v[24:27], a[84:87], v[88:91], v[24:27]\n\t"
        "v_add_f32 v59, s47, v69\n\t"
        "v_add_f32 v56, v56, v58\n\t"
        "v_mfma_f32_16x16x32_bf16 v[28:31], a[88:91], v[88:91], v[28:31]\n\t"
        "v_add_f32 v57, v57, v59\n\t"
        "v_cvt_pk_bf16_f32 v52, v56, v57\n\t"
        "v_mfma_f32_16x16x32_bf16 v[32:35], a[92:95], v[88:91], v[32:35]\n\t"
        "v_cvt_pk_bf16_f32 v58, v58, v59\n\t"
        "v_cmp_ne_u32 s[64:65], v52, v58\n\t"
        "s_waitcnt lgkmcnt(0)\n\t"
        "v_mfma_f32_16x16x32_bf16 v[12:15], a[96:99], v[92:95], v[12:15]\n\t"
        "v_mbcnt_lo_u32_b32 v56, s64, 1\n\t"
        "v_mbcnt_hi_u32_b32 v56, s65, v56\n\t"
        "v_mfma_f32_16x16x32_bf16 v[16:19], a[100:103], v[92:95], v[16:19]\n\t"
        "s_bcnt1_i32_b64 s61, s[64:65]\n\t"
        "v_add_u32 v54, s61, 1\n\t"
        "v_lshl_add_u32 v56, v56, 3, s60\n\t"
        "v_mfma_f32_16x16x32_bf16 v[20:23], a[104:107], v[92:95], v[20:23]\n\t"
        "s_mov_b64 exec, s[64:65]\n\t"
        "ds_write_b64 v56, v[52:53]\n\t"
        "s_mov_b64 exec, -1\n\t"
        "v_mfma_f32_16x16x32_bf16 v[36:39], a[84:87], v[92:95], v[36:39]\n\t"
        "v_swap_b32 v53, v80\n\t"
        "v_perm_b32 v56, v67, v67, s44\n\t"
        "v_mfma_f32_16x16x32_bf16 v[40:43], a[88:91], v[92:95], v[40:43]\n\t"
        "buffer_load_dwordx4 v135, s[16:19], s43 offen  lds\n\t"
        "s_add_u32 m0, s37, 49152\n\t"
        "v_mfma_f32_16x16x32_bf16 v[44:47], a[92:95], v[92:95], v[44:47]\n\t"
        "s_waitcnt vmcnt(10)\n\t"
        "s_barrier\n\t"
        "ds_read_b128 v[100:103], v128 offset:0\n\t"
        "s_add_u32 s36, s36, s5\n\t"
        "ds_read_b128 v[104:107], v128 offset:64\n\t"
        "v_mfma_f32_16x16x32_bf16 v[24:27], a[96:99], v[96:99], v[24:27]\n\t"
        "ds_read_b128 v[108:111], v128 offset:128\n\t"
        "ds_read_b128 v[112:115], v128 offset:20480\n\t"
        "v_perm_b32 v57, v67, v67, s45\n\t"
        "v_mfma_f32_16x16x32_bf16 v[28:31], a[100:103], v[96:99], v[28:31]\n\t"
        "ds_read_b128 v[84:87], v128 offset:20544\n\t"
        "ds_read_b128 v[88:91], v128 offset:20608\n\t"
        "v_add_f32 v56, v56, v78\n\t"
        "v_mfma_f32_16x16x32_bf16 v[32:35], a[104:107], v[96:99], v[32:35]\n\t"
        "buffer_load_dwordx4 v140, s[24:27], s36 offen offset:0 lds\n\t"
        "ds_read_b64_tr_b16 a[108:109], v131 offset:12288\n\t"
        "v_add_f32 v57, v57, v79\n\t"
        "v_mfma_f32_16x16x32_bf16 v[36:39], a[72:75], v[96:99], v[36:39]\n\t"
        "ds_read_b64_tr_b16 a[110:111], v131 offset:13056\n\t"
        "ds_read_b64_tr_b16 a[112:113], v132 offset:12288\n\t"
        "v_cvt_pk_bf16_f32 v52, v56, v57\n\t"
        "v_mfma_f32_16x16x32_bf16 v[40:43], a[76:79], v[96:99], v[40:43]\n\t"
        "ds_read_b64_tr_b16 a[114:115], v132 offset:13056\n\t"
        "ds_read_b64_tr_b16 a[116:117], v133 offset:12288\n\t"
        "v_cvt_pk_bf16_f32 v58, v78, v79\n\t"
        "v_mfma_f32_16x16x32_bf16 v[44:47], a[80:83], v[96:99], v[44:47]\n\t"
        "ds_read_b64_tr_b16 a[118:119], v133 offset:13056\n\t"
        ".LpM0%=:\n\t"
        "s_cbranch_execz .LrM%=\n\t"
        "ds_read_b64_tr_b16 a[120:121], v131 offset:18432\n\t"
        "v_cmp_ne_u32 s[64:65], v52, v58\n\t"
        "s_waitcnt lgkmcnt(12)\n\t"
        "v_mfma_f32_16x16x32_bf16 v[0:3], a[12:15], v[100:103], v[0:3]\n\t"
        "buffer_load_dwordx4 v141, s[24:27], s36 offen offset:1024 lds\n\t"
        "ds_read_b64_tr_b16 a[122:123], v131 offset:19200\n\t"
        "s_and_b64 s[64:65], s[64:65], 0xffffff\n\t"
        "v_mfma_f32_16x16x32_bf16 v[4:7], a[16:19], v[100:103], v[4:7]\n\t"
        "ds_read_b64_tr_b16 a[124:125], v132 offset:18432\n\t"
        "ds_read_b64_tr_b16 a[126:127], v132 offset:19200\n\t"
        "v_mbcnt_lo_u32_b32 v56, s64, v54\n\t"
        "v_mfma_f32_16x16x32_bf16 v[8:11], a[20:23], v[100:103], v[8:11]\n\t"
        "ds_read_b64_tr_b16 a[128:129], v133 offset:18432\n\t"
        "ds_read_b64_tr_b16 a[130:131], v133 offset:19200\n\t"
        "s_bcnt1_i32_b64 s46, s[64:65]\n\t"
        "s_waitcnt lgkmcnt(12)\n\t"
        "v_mfma_f32_16x16x32_bf16 v[0:3], a[48:51], v[104:107], v[0:3]\n\t"
        "ds_read_b128 v[92:95], v129 offset:0\n\t"
        "s_add_u32 s61, s61, s46\n\t"
        "v_mfma_f32_16x16x32_bf16 v[4:7], a[52:55], v[104:107], v[4:7]\n\t"
        "buffer_load_dwordx4 v142, s[24:27], s36 offen offset:2048 lds\n\t"
        "v_readlane_b32 s40, v155, 14\n\t"
        "v_mfma_f32_16x16x32_bf16 v[8:11], a[56:59], v[104:107], v[8:11]\n\t"
        "ds_read_b128 v[96:99], v129 offset:4096\n\t"
        "v_readlane_b32 s43, v155, 2\n\t"
        "s_add_u32 m0, s40, s35\n\t"
        "v_mfma_f32_16x16x32_bf16 v[0:3], a[84:87], v[108:111], v[0:3]\n\t"
        "v_add_u32 v54, s46, v54\n\t"
        "s_mov_b32 s3, s61\n\t"
        "v_mfma_f32_16x16x32_bf16 v[4:7], a[88:91], v[108:111], v[4:7]\n\t"
        "v_lshl_add_u32 v56, v56, 3, s60\n\t"
        "v_readlane_b32 s46, v155, 39\n\t"
        "v_mfma_f32_16x16x32_bf16 v[8:11], a[92:95], v[108:111], v[8:11]\n\t"
        "s_mov_b64 exec, s[64:65]\n\t"
        "ds_write_b64 v56, v[52:53]\n\t"
        "s_mov_b64 exec, -1\n\t"
        "v_mfma_f32_16x16x32_bf16 v[36:39], a[24:27], v[112:115], v[36:39]\n\t"
        "buffer_load_dwordx4 v134, s[16:19], s43 offen  lds\n\t"
        "v_readlane_b32 s40, v155, 15\n\t"
        "v_mfma_f32_16x16x32_bf16 v[40:43], a[28:31], v[112:115], v[40:43]\n\t"
        "v_readlane_b32 s43, v155, 3\n\t"
        "s_add_u32 m0, s40, s35\n\t"
        "v_mfma_f32_16x16x32_bf16 v[44:47], a[32:35], v[112:115], v[44:47]\n\t"
        "v_add_u32 v53, 4096, v80\n\t"
        "v_readlane_b32 s47, v155, 42\n\t"
        "v_mfma_f32_16x16x32_bf16 v[36:39], a[60:63], v[84:87], v[36:39]\n\t"
        "v_perm_b32 v56, v66, v66, s44\n\t"
        "v_perm_b32 v57, v66, v66, s45\n\t"
        "v_mfma_f32_16x16x32_bf16 v[40:43], a[64:67], v[84:87], v[40:43]\n\t"
        "buffer_load_dwordx4 v135, s[16:19], s43 offen  lds\n\t"
        "s_add_u32 m0, s37, 0\n\t"
        "v_mfma_f32_16x16x32_bf16 v[44:47], a[68:71], v[84:87], v[44:47]\n\t"
        "s_add_u32 s36, s36, s39\n\t"
        "v_add_f32 v58, s46, v68\n\t"
        "v_mfma_f32_16x16x32_bf16 v[36:39], a[96:99], v[88:91], v[36:39]\n\t"
        "v_add_f32 v59, s47, v69\n\t"
        "v_add_f32 v56, v56, v58\n\t"
        "v_mfma_f32_16x16x32_bf16 v[40:43], a[100:103], v[88:91], v[40:43]\n\t"
        "v_add_f32 v57, v57, v59\n\t"
        "v_max_f32 v56, 0, v56\n\t"
        "v_mfma_f32_16x16x32_bf16 v[44:47], a[104:107], v[88:91], v[44:47]\n\t"
        "s_waitcnt vmcnt(12) lgkmcnt(1)\n\t"
        "s_barrier\n\t"
        "buffer_load_dwordx4 v140, s[24:27], s36 offen offset:0 lds\n\t"
        "ds_read_b64_tr_b16 a[132:133], v131 offset:24576\n\t"
        "v_mfma_f32_16x16x32_bf16 v[0:3], a[120:123], v[92:95], v[0:3]\n\t"
        "ds_read_b64_tr_b16 a[134:135], v131 offset:25344\n\t"
        "ds_read_b64_tr_b16 a[136:137], v132 offset:24576\n\t"
        "ds_read_b64_tr_b16 a[138:139], v132 offset:25344\n\t"
        "v_max_f32 v57, 0, v57\n\t"
        "v_mfma_f32_16x16x32_bf16 v[4:7], a[124:127], v[92:95], v[4:7]\n\t"
        "ds_read_b64_tr_b16 a[140:141], v133 offset:24576\n\t"
        "ds_read_b64_tr_b16 a[142:143], v133 offset:25344\n\t"
        "ds_read_b128 v[100:103], v129 offset:8192\n\t"
        "v_max_f32 v58, 0, v58\n\t"
        "v_mfma_f32_16x16x32_bf16 v[8:11], a[128:131], v[92:95], v[8:11]\n\t"
        "ds_read_b128 v[104:107], v129 offset:12288\n\t"
        "ds_read_b64_tr_b16 a[144:145], v131 offset:30720\n\t"
        "v_max_f32 v59, 0, v59\n\t"
        "v_mfma_f32_16x16x32_bf16 v[0:3], a[108:111], v[96:99], v[0:3]\n\t"
        "buffer_load_dwordx4 v141, s[24:27], s36 offen offset:1024 lds\n\t"
        "ds_read_b64_tr_b16 a[146:147], v131 offset:31488\n\t"
        "v_cvt_pk_bf16_f32 v52, v56, v57\n\t"
        "v_mfma_f32_16x16x32_bf16 v[4:7], a[112:115], v[96:99], v[4:7]\n\t"
        "ds_read_b64_tr_b16 a[148:149], v132 offset:30720\n\t"
        "ds_read_b64_tr_b16 a[150:151], v132 offset:31488\n\t"
        "v_cvt_pk_bf16_f32 v58, v58, v59\n\t"
        "v_mfma_f32_16x16x32_bf16 v[8:11], a[116:119], v[96:99], v[8:11]\n\t"
        "ds_read_b64_tr_b16 a[152:153], v133 offset:30720\n\t"
        "ds_read_b64_tr_b16 a[154:155], v133 offset:31488\n\t"
        "v_cmp_ne_u32 s[64:65], v52, v58\n\t"
        "v_mfma_f32_16x16x32_bf16 v[12:15], a[120:123], v[96:99], v[12:15]\n\t"
        "ds_read_b128 v[108:111], v129 offset:16384\n\t"
        "v_mbcnt_lo_u32_b32 v56, s64, v54\n\t"
        "v_mfma_f32_16x16x32_bf16 v[16:19], a[124:127], v[96:99], v[16:19]\n\t"
        "buffer_load_dwordx4 v142, s[24:27], s36 offen offset:2048 lds\n\t"
        "v_readlane_b32 s40, v155, 16\n\t"
        "v_mfma_f32_16x16x32_bf16 v[20:23], a[128:131], v[96:99], v[20:23]\n\t"
        "v_readlane_b32 s43, v155, 4\n\t"
        "s_add_u32 m0, s40, s35\n\t"
        "s_waitcnt lgkmcnt(8)\n\t"
        "v_mfma_f32_16x16x32_bf16 v[0:3], a[132:135], v[100:103], v[0:3]\n\t"
        "v_mbcnt_hi_u32_b32 v56, s65, v56\n\t"
        "s_bcnt1_i32_b64 s46, s[64:65]\n\t"
        "v_mfma_f32_16x16x32_bf16 v[4:7], a[136:139], v[100:103], v[4:7]\n\t"
        "s_waitcnt vmcnt(14)\n\t"
        "v_and_b32 v48, s54, v48\n\t"
        "v_pk_max_u16 v60, v154, v48\n\t"
        "v_mfma_f32_16x16x32_bf16 v[8:11], a[140:143], v[100:103], v[8:11]\n\t"
        "buffer_load_dwordx4 v134, s[16:19], s43 offen  lds\n\t"
        "v_readlane_b32 s40, v155, 17\n\t"
        "v_mfma_f32_16x16x32_bf16 v[12:15], a[108:111], v[100:103], v[12:15]\n\t"
        "ds_read_b128 v[112:115], v129 offset:20480\n\t"
        "v_readlane_b32 s43, v155, 5\n\t"
        "v_mfma_f32_16x16x32_bf16 v[16:19], a[112:115], v[100:103], v[16:19]\n\t"
        "s_add_u32 m0, s40, s35\n\t"
        "v_and_b32 v49, s54, v49\n\t"
        "v_mfma_f32_16x16x32_bf16 v[20:23], a[116:119], v[100:103], v[20:23]\n\t"
        "v_pk_max_u16 v60, v60, v49\n\t"
        "v_and_b32 v50, s54, v50\n\t"
        "v_mfma_f32_16x16x32_bf16 v[24:27], a[120:123], v[100:103], v[24:27]\n\t"
        "v_pk_max_u16 v60, v60, v50\n\t"
        "v_and_b32 v51, s54, v51\n\t"
        "v_mfma_f32_16x16x32_bf16 v[28:31], a[124:127], v[100:103], v[28:31]\n\t"
        "v_pk_max_u16 v60, v60, v51\n\t"
        "s_waitcnt vmcnt(14)\n\t"
        "v_and_b32 v61, s54, v61\n\t"
        "v_mfma_f32_16x16x32_bf16 v[32:35], a[128:131], v[100:103], v[32:35]\n\t"
        "v_pk_max_u16 v60, v60, v61\n\t"
        "v_cmp_ne_u32 s[12:13], v60, v154\n\t"
        "s_waitcnt lgkmcnt(1)\n\t"
        "v_mfma_f32_16x16x32_bf16 v[12:15], a[132:135], v[104:107], v[12:15]\n\t"
        "buffer_load_dwordx4 v135, s[16:19], s43 offen  lds\n\t"
        "s_add_u32 m0, s37, 12288\n\t"
        "v_mfma_f32_16x16x32_bf16 v[16:19], a[136:139], v[104:107], v[16:19]\n\t"
        "s_add_u32 s36, s36, s38\n\t"
        "s_lshl_b32 s6, 256, s4\n\t"
        "v_mfma_f32_16x16x32_bf16 v[20:23], a[140:143], v[104:107], v[20:23]\n\t"
        "s_cmp_lg_u64 s[12:13], 0\n\t"
        "s_cselect_b32 s6, s6, 0\n\t"
        "v_mfma_f32_16x16x32_bf16 v[24:27], a[108:111], v[104:107], v[24:27]\n\t"
        "s_add_u32 s61, s61, s46\n\t"
        "v_min_u32 v56, 97, v56\n\t"
        "v_mfma_f32_16x16x32_bf16 v[28:31], a[112:115], v[104:107], v[28:31]\n\t"
        "s_waitcnt vmcnt(10)\n\t"
        "s_barrier\n\t"
        "buffer_load_dwordx4 v140, s[24:27], s36 offen offset:0 lds\n\t"
        "ds_read_b64_tr_b16 a[156:157], v131 offset:36864\n\t"
        "v_mfma_f32_16x16x32_bf16 v[32:35], a[116:119], v[104:107], v[32:35]\n\t"
        "ds_read_b64_tr_b16 a[158:159], v131 offset:37632\n\t"
        "ds_read_b64_tr_b16 a[160:161], v132 offset:36864\n\t"
        "v_lshl_add_u32 v56, v56, 3, s60\n\t"
        "v_mfma_f32_16x16x32_bf16 v[36:39], a[120:123], v[104:107], v[36:39]\n\t"
        "ds_read_b64_tr_b16 a[162:163], v132 offset:37632\n\t"
        "ds_read_b64_tr_b16 a[164:165], v133 offset:36864\n\t"
        "v_mfma_f32_16x16x32_bf16 v[40:43], a[124:127], v[104:107], v[40:43]\n\t"
        "ds_read_b64_tr_b16 a[166:167], v133 offset:37632\n\t"
        "ds_read_b128 v[84:87], v129 offset:64\n\t"
        "v_mfma_f32_16x16x32_bf16 v[44:47], a[128:131], v[104:107], v[44:47]\n\t"
        "buffer_load_dwordx4 v141, s[24:27], s36 offen offset:1024 lds\n\t"
        "ds_read_b128 v[88:91], v129 offset:4160\n\t"
        "v_mfma_f32_16x16x32_bf16 v[24:27], a[132:135], v[108:111], v[24:27]\n\t"
        "s_mov_b64 exec, s[64:65]\n\t"
        "ds_write_b64 v56, v[52:53]\n\t"
        "s_mov_b64 exec, -1\n\t"
        "ds_read_b64_tr_b16 a[168:169], v131 offset:43008\n\t"
        "v_mfma_f32_16x16x32_bf16 v[28:31], a[136:139], v[108:111], v[28:31]\n\t"
        "s_cmp_gt_u32 s61, s53\n\t"
        "s_cselect_b32 s8, 1, 0\n\t"
        "ds_read_b64_tr_b16 a[170:171], v131 offset:43776\n\t"
        "ds_read_b64_tr_b16 a[172:173], v132 offset:43008\n\t"
        "v_mfma_f32_16x16x32_bf16 v[32:35], a[140:143], v[108:111], v[32:35]\n\t"
        "ds_read_b64_tr_b16 a[174:175], v132 offset:43776\n\t"
        "ds_read_b64_tr_b16 a[176:177], v133 offset:43008\n\t"
        "s_or_b32 s56, s56, s8\n\t"
        "v_mfma_f32_16x16x32_bf16 v[36:39], a[108:111], v[108:111], v[36:39]\n\t"
        "s_cmp_gt_u32 s3, 64\n\t"
        "s_cselect_b32 s8, 1, 0\n\t"
        "buffer_load_dwordx4 v142, s[24:27], s36 offen offset:2048 lds\n\t"
        "ds_read_b64_tr_b16 a[178:179], v133 offset:43776\n\t"
        "v_mfma_f32_16x16x32_bf16 v[40:43], a[112:115], v[108:111], v[40:43]\n\t"
        "ds_read_b128 v[92:95], v129 offset:8256\n\t"
        "v_readlane_b32 s40, v155, 18\n\t"
        "v_mfma_f32_16x16x32_bf16 v[44:47], a[116:119], v[108:111], v[44:47]\n\t"
        "v_readlane_b32 s43, v155, 6\n\t"
        "s_add_u32 m0, s40, s35\n\t"
        "s_waitcnt lgkmcnt(9)\n\t"
        "v_mfma_f32_16x16x32_bf16 v[36:39], a[132:135], v[112:115], v[36:39]\n\t"
        "ds_read_b128 v[96:99], v129 offset:12352\n\t"
        "s_or_b32 s56, s56, s8\n\t"
        "v_mfma_f32_16x16x32_bf16 v[40:43], a[136:139], v[112:115], v[40:43]\n\t"
        "buffer_load_dwordx4 v136, s[16:19], s43 offen  lds\n\t"
        "s_bitcmp1_b32 s42, 10\n\t"
        "s_cbranch_scc1 .Lx40%=\n\t"
        ".Lx41%=:\n\t"
        "v_mfma_f32_16x16x32_bf16 v[44:47], a[140:143], v[112:115], v[44:47]\n\t"
        "v_readlane_b32 s40, v155, 19\n\t"
        "v_readlane_b32 s43, v155, 7\n\t"
        "v_mfma_f32_16x16x32_bf16 v[0:3], a[156:159], v[84:87], v[0:3]\n\t"
        "s_add_u32 m0, s40, s35\n\t"
        "s_min_u32 s61, s61, 80\n\t"
        "v_mfma_f32_16x16x32_bf16 v[4:7], a[160:163], v[84:87], v[4:7]\n\t"
        "v_min_u32 v78, s61, v116\n\t"
        "v_lshl_add_u32 v78, v78, 3, s60\n\t"
        "v_mfma_f32_16x16x32_bf16 v[8:11], a[164:167], v[84:87], v[8:11]\n\t"
        "ds_read_b64 v[62:63], v78\n\t"
        "v_cmp_ge_u32 s[66:67], s61, v116\n\t"
        "s_waitcnt lgkmcnt(4)\n\t"
        "v_mfma_f32_16x16x32_bf16 v[0:3], a[144:147], v[88:91], v[0:3]\n\t"
        "s_mov_b32 s46, 0x07060302\n\t"
        "v_mfma_f32_16x16x32_bf16 v[4:7], a[148:151], v[88:91], v[4:7]\n\t"
        "v_mfma_f32_16x16x32_bf16 v[8:11], a[152:155], v[88:91], v[8:11]\n\t"
        "v_mfma_f32_16x16x32_bf16 v[12:15], a[156:159], v[88:91], v[12:15]\n\t"
        "ds_read_b128 v[100:103], v129 offset:16448\n\t"
        "v_mfma_f32_16x16x32_bf16 v[16:19], a[160:163], v[88:91], v[16:19]\n\t"
        "buffer_load_dwordx4 v137, s[16:19], s43 offen  lds\n\t"
        "s_add_u32 m0, s37, 24576\n\t"
        "v_mfma_f32_16x16x32_bf16 v[20:23], a[164:167], v[88:91], v[20:23]\n\t"
        "s_add_u32 s36, s36, s5\n\t"
        "s_waitcnt lgkmcnt(2)\n\t"
        "v_mfma_f32_16x16x32_bf16 v[0:3], a[168:171], v[92:95], v[0:3]\n\t"
        "v_mfma_f32_16x16x32_bf16 v[4:7], a[172:175], v[92:95], v[4:7]\n\t"
        "v_mfma_f32_16x16x32_bf16 v[8:11], a[176:179], v[92:95], v[8:11]\n\t"
        "s_mov_b64 exec, s[66:67]\n\t"
        "s_waitcnt lgkmcnt(1)\n\t"
        "ds_write_b32 v63, v117\n\t"
        "s_mov_b64 exec, -1\n\t"
        "v_mfma_f32_16x16x32_bf16 v[12:15], a[144:147], v[92:95], v[12:15]\n\t"
        "ds_read_b128 v[104:107], v129 offset:20544\n\t"
        "v_and_b32 v78, 0xfffff800, v63\n\t"
        "v_mfma_f32_16x16x32_bf16 v[16:19], a[148:151], v[92:95], v[16:19]\n\t"
        "s_waitcnt vmcnt(10)\n\t"
        "s_barrier\n\t"
        "buffer_load_dwordx4 v140, s[24:27], s36 offen offset:0 lds\n\t"
        "ds_read_b64_tr_b16 a[180:181], v131 offset:49152\n\t"
        "v_mfma_f32_16x16x32_bf16 v[20:23], a[152:155], v[92:95], v[20:23]\n\t"
        "ds_read_b128 v[64:67], v78 offset:1536\n\t"
        "ds_read_b64_tr_b16 a[182:183], v131 offset:49920\n\t"
        "v_lshlrev_b32 v72, 16, v62\n\t"
        "v_mfma_f32_16x16x32_bf16 v[24:27], a[156:159], v[92:95], v[24:27]\n\t"
        "ds_read_b64_tr_b16 a[184:185], v132 offset:49152\n\t"
        "ds_read_b64_tr_b16 a[186:187], v132 offset:49920\n\t"
        "v_and_b32 v73, 0xffff0000, v62\n\t"
        "v_mfma_f32_16x16x32_bf16 v[28:31], a[160:163], v[92:95], v[28:31]\n\t"
        "ds_read_b64_tr_b16 a[188:189], v133 offset:49152\n\t"
        "ds_read_b64_tr_b16 a[190:191], v133 offset:49920\n\t"
        "v_med3_f32 v72, v72, 0, s63\n\t"
        "v_mfma_f32_16x16x32_bf16 v[32:35], a[164:167], v[92:95], v[32:35]\n\t"
        "buffer_load_dwordx4 v141, s[24:27], s36 offen offset:1024 lds\n\t"
        "ds_read_b64_tr_b16 a[192:193], v131 offset:55296\n\t"
        "v_med3_f32 v73, v73, 0, s63\n\t"
        "v_mfma_f32_16x16x32_bf16 v[12:15], a[168:171], v[96:99], v[12:15]\n\t"
        "ds_read_b64_tr_b16 a[194:195], v131 offset:56064\n\t"
        "ds_read_b64_tr_b16 a[196:197], v132 offset:55296\n\t"
        "v_perm_b32 v62, v73, v72, s46\n\t"
        "v_mfma_f32_16x16x32_bf16 v[16:19], a[172:175], v[96:99], v[16:19]\n\t"
        "ds_read_b64_tr_b16 a[198:199], v132 offset:56064\n\t"
        "ds_read_b64_tr_b16 a[200:201], v133 offset:55296\n\t"
        "v_fract_f32 v74, v72\n\t"
        "v_mfma_f32_16x16x32_bf16 v[20:23], a[176:179], v[96:99], v[20:23]\n\t"
        "ds_read_b64_tr_b16 a[202:203], v133 offset:56064\n\t"
        "v_fract_f32 v75, v73\n\t"
        "v_mfma_f32_16x16x32_bf16 v[24:27], a[144:147], v[96:99], v[24:27]\n\t"
        "buffer_load_dwordx4 v142, s[24:27], s36 offen offset:2048 lds\n\t"
        "s_add_u32 m0, s37, 36864\n\t"
        "v_mfma_f32_16x16x32_bf16 v[28:31], a[148:151], v[96:99], v[28:31]\n\t"
        "ds_read_b128 v[108:111], v129 offset:128\n\t"
        "s_add_u32 s36, s36, s39\n\t"
        "v_mfma_f32_16x16x32_bf16 v[32:35], a[152:155], v[96:99], v[32:35]\n\t"
        "ds_read_b128 v[112:115], v129 offset:4224\n\t"
        "v_cvt_u32_f32 v76, v72\n\t"
        "v_mfma_f32_16x16x32_bf16 v[36:39], a[156:159], v[96:99], v[36:39]\n\t"
        "v_cvt_u32_f32 v77, v73\n\t"
        "v_cmp_neq_f32 s[64:65], 0, v74\n\t"
        "v_mfma_f32_16x16x32_bf16 v[40:43], a[160:163], v[96:99], v[40:43]\n\t"
        "buffer_load_dwordx4 v140, s[24:27], s36 offen offset:0 lds\n\t"
        "v_cmp_neq_f32 s[44:45], 0, v75\n\t"
        "v_mfma_f32_16x16x32_bf16 v[44:47], a[164:167], v[96:99], v[44:47]\n\t"
        "v_sub_f32 v78, 1.0, v74\n\t"
        "v_sub_f32 v79, 1.0, v75\n\t"
        "s_waitcnt lgkmcnt(7)\n\t"
        "v_mfma_f32_16x16x32_bf16 v[24:27], a[168:171], v[100:103], v[24:27]\n\t"
        "v_cvt_pk_bf16_f32 v79, v78, v79\n\t"
        "v_lshlrev_b32 v78, 16, v79\n\t"
        "v_mfma_f32_16x16x32_bf16 v[28:31], a[172:175], v[100:103], v[28:31]\n\t"
        "v_and_b32 v79, 0xffff0000, v79\n\t"
        "v_mul_f32 v80, v78, v79\n\t"
        "v_mfma_f32_16x16x32_bf16 v[32:35], a[176:179], v[100:103], v[32:35]\n\t"
        "v_mul_f32 v78, v78, v75\n\t"
        "v_mul_f32 v79, v74, v79\n\t"
        "v_mfma_f32_16x16x32_bf16 v[36:39], a[144:147], v[100:103], v[36:39]\n\t"
        "v_mul_f32 v81, v74, v75\n\t"
        "v_cndmask_b32 v78, v79, v78, s[44:45]\n\t"
        "v_mfma_f32_16x16x32_bf16 v[40:43], a[148:151], v[100:103], v[40:43]\n\t"
        "buffer_load_dwordx4 v141, s[24:27], s36 offen offset:1024 lds\n\t"
        "v_cndmask_b32 v79, 0, v79, s[44:45]\n\t"
        "v_mfma_f32_16x16x32_bf16 v[44:47], a[152:155], v[100:103], v[44:47]\n\t"
        "v_cvt_pk_bf16_f32 v70, v80, v78\n\t"
        "v_cvt_pk_bf16_f32 v71, v79, v81\n\t"
        "v_mfma_f32_16x16x32_bf16 v[36:39], a[168:171], v[104:107], v[36:39]\n\t"
        "v_pk_max_u16 v78, v62, v64\n\t"
        "v_pk_min_u16 v78, v78, v65\n\t"
        "v_mfma_f32_16x16x32_bf16 v[40:43], a[172:175], v[104:107], v[40:43]\n\t"
        "v_cmp_ne_u32 vcc, v78, v62\n\t"
        "v_sub_u32 v78, v77, v67\n\t"
        "v_mfma_f32_16x16x32_bf16 v[44:47], a[176:179], v[104:107], v[44:47]\n\t"
        "v_mad_u32_u24 v79, v77, 6, v66\n\t"
        "v_mad_u32_u24 v79, v76, s62, v79\n\t"
        "s_waitcnt lgkmcnt(0)\n\t"
        "v_mfma_f32_16x16x32_bf16 v[0:3], a[192:195], v[108:111], v[0:3]\n\t"
        "s_and_b64 vcc, vcc, s[66:67]\n\t"
        "s_or_b64 s[56:57], s[56:57], vcc\n\t"
        "v_mfma_f32_16x16x32_bf16 v[4:7], a[196:199], v[108:111], v[4:7]\n\t"
        "s_waitcnt vmcnt(10)\n\t"
        "s_barrier\n\t"
        "buffer_load_dwordx4 v142, s[24:27], s36 offen offset:2048 lds\n\t"
        "s_add_u32 m0, s37, 49152\n\t"
        "ds_read_b64_tr_b16 a[204:205], v131 offset:0\n\t"
        "s_add_u32 s36, s36, s38\n\t"
        "ds_read_b64_tr_b16 a[206:207], v131 offset:768\n\t"
        "v_mfma_f32_16x16x32_bf16 v[8:11], a[200:203], v[108:111], v[8:11]\n\t"
        "ds_read_b64_tr_b16 a[208:209], v132 offset:0\n\t"
        "ds_read_b64_tr_b16 a[210:211], v132 offset:768\n\t"
        "ds_read_b64_tr_b16 a[212:213], v133 offset:0\n\t"
        "ds_read_b64_tr_b16 a[214:215], v133 offset:768\n\t"
        "ds_read_b128 v[84:87], v129 offset:8320\n\t"
        "v_bfe_u32 v80, v78, 2, 1\n\t"
        "v_mfma_f32_16x16x32_bf16 v[0:3], a[180:183], v[112:115], v[0:3]\n\t"
        "ds_read_b128 v[88:91], v129 offset:12416\n\t"
        "ds_read_b64_tr_b16 a[216:217], v131 offset:6144\n\t"
        "v_or_b32 v80, v80, v79\n\t"
        "v_mfma_f32_16x16x32_bf16 v[4:7], a[184:187], v[112:115], v[4:7]\n\t"
        "ds_read_b64_tr_b16 a[218:219], v131 offset:6912\n\t"
        "ds_read_b64_tr_b16 a[220:221], v132 offset:6144\n\t"
        "v_add_u32 v78, 1, v78\n\t"
        "v_mfma_f32_16x16x32_bf16 v[8:11], a[188:191], v[112:115], v[8:11]\n\t"
        "buffer_load_dwordx4 v140, s[24:27], s36 offen offset:0 lds\n\t"
        "ds_read_b64_tr_b16 a[222:223], v132 offset:6912\n\t"
        "v_bfe_u32 v78, v78, 2, 1\n\t"
        "v_mfma_f32_16x16x32_bf16 v[12:15], a[192:195], v[112:115], v[12:15]\n\t"
        "ds_read_b128 v[92:95], v129 offset:16512\n\t"
        "ds_read_b64_tr_b16 a[224:225], v133 offset:6144\n\t"
        "v_add3_u32 v78, v79, v78, 6\n\t"
        "v_mfma_f32_16x16x32_bf16 v[16:19], a[196:199], v[112:115], v[16:19]\n\t"
        "ds_read_b64_tr_b16 a[226:227], v133 offset:6912\n\t"
        "v_cndmask_b32 v78, v80, v78, s[44:45]\n\t"
        "v_mfma_f32_16x16x32_bf16 v[20:23], a[200:203], v[112:115], v[20:23]\n\t"
        "v_cndmask_b32 v79, 0, v119, s[64:65]\n\t"
        "v_add_u32 v81, v80, v79\n\t"
        "s_waitcnt lgkmcnt(8)\n\t"
        "v_mfma_f32_16x16x32_bf16 v[0:3], a[204:207], v[84:87], v[0:3]\n\t"
        "buffer_load_dwordx4 v141, s[24:27], s36 offen offset:1024 lds\n\t"
        "v_add_u32 v82, v78, v79\n\t"
        "v_mfma_f32_16x16x32_bf16 v[4:7], a[208:211], v[84:87], v[4:7]\n\t"
        "v_cndmask_b32 v78, v81, v78, s[44:45]\n\t"
        "v_lshl_or_b32 v68, v78, 16, v80\n\t"
        "v_mfma_f32_16x16x32_bf16 v[8:11], a[212:215], v[84:87], v[8:11]\n\t"
        "v_lshl_or_b32 v69, v82, 16, v81\n\t"
        "v_mfma_f32_16x16x32_bf16 v[12:15], a[180:183], v[84:87], v[12:15]\n\t"
        "ds_write_b128 v118, v[68:71]\n\t"
        "v_mfma_f32_16x16x32_bf16 v[16:19], a[184:187], v[84:87], v[16:19]\n\t"
        "s_cmp_gt_u32 s3, 32\n\t"
        "s_cselect_b32 s8, 2, 0\n\t"
        "v_mfma_f32_16x16x32_bf16 v[20:23], a[188:191], v[84:87], v[20:23]\n\t"
        "s_cmp_lg_u64 s[56:57], 0\n\t"
        "s_addc_u32 s8, s8, s6\n\t"
        "v_mfma_f32_16x16x32_bf16 v[24:27], a[192:195], v[84:87], v[24:27]\n\t"
        "ds_read_b128 v[160:163], v185 offset:0\n\t"
        "v_mov_b32 v56, s8\n\t"
        "v_mfma_f32_16x16x32_bf16 v[28:31], a[196:199], v[84:87], v[28:31]\n\t"
        "s_mov_b64 exec, 1\n\t"
        "ds_write_b32 v153, v56\n\t"
        "s_mov_b64 exec, -1\n\t"
        "v_mfma_f32_16x16x32_bf16 v[32:35], a[200:203], v[84:87], v[32:35]\n\t"
        "buffer_load_dwordx4 v142, s[24:27], s36 offen offset:2048 lds\n\t"
        "v_xor_b32 v154, 32, v186\n\t"
        "s_waitcnt lgkmcnt(3)\n\t"
        "v_mfma_f32_16x16x32_bf16 v[12:15], a[204:207], v[88:91], v[12:15]\n\t"
        "ds_read_b128 v[96:99], v129 offset:20608\n\t"
        "v_mfma_f32_16x16x32_bf16 v[16:19], a[208:211], v[88:91], v[16:19]\n\t"
        "v_mfma_f32_16x16x32_bf16 v[20:23], a[212:215], v[88:91], v[20:23]\n\t"
        "v_mfma_f32_16x16x32_bf16 v[24:27], a[180:183], v[88:91], v[24:27]\n\t"
        "v_mfma_f32_16x16x32_bf16 v[28:31], a[184:187], v[88:91], v[28:31]\n\t"
        "v_mfma_f32_16x16x32_bf16 v[32:35], a[188:191], v[88:91], v[32:35]\n\t"
        "v_mfma_f32_16x16x32_bf16 v[36:39], a[192:195], v[88:91], v[36:39]\n\t"
        "s_waitcnt vmcnt(9)\n\t"
        "s_barrier\n\t"
        "ds_read_b64_tr_b16 a[228:229], v131 offset:12288\n\t"
        "ds_read_b64_tr_b16 a[230:231], v131 offset:13056\n\t"
        "v_mfma_f32_16x16x32_bf16 v[40:43], a[196:199], v[88:91], v[40:43]\n\t"
        "ds_read_b64_tr_b16 a[232:233], v132 offset:12288\n\t"
        "ds_read_b64_tr_b16 a[234:235], v132 offset:13056\n\t"
        "s_waitcnt lgkmcnt(5)\n\t"
        "v_lshlrev_b32_sdwa v157, 5, v160 dst_sel:DWORD dst_unused:UNUSED_PAD src0_sel:DWORD src1_sel:WORD_0\n\t"
        "v_mfma_f32_16x16x32_bf16 v[44:47], a[200:203], v[88:91], v[44:47]\n\t"
        "ds_read_b64_tr_b16 a[236:237], v133 offset:12288\n\t"
        "ds_read_b64_tr_b16 a[238:239], v133 offset:13056\n\t"
        "v_lshlrev_b32_sdwa v158, 5, v160 dst_sel:DWORD dst_unused:UNUSED_PAD src0_sel:DWORD src1_sel:WORD_1\n\t"
        "v_mfma_f32_16x16x32_bf16 v[24:27], a[204:207], v[92:95], v[24:27]\n\t"
        "ds_read_b128 v[100:103], v130 offset:0\n\t"
        "ds_read_b128 v[104:107], v130 offset:4096\n\t"
        "v_or_b32 v157, v184, v157\n\t"
        "v_mfma_f32_16x16x32_bf16 v[28:31], a[208:211], v[92:95], v[28:31]\n\t"
        "ds_read_b64_tr_b16 v[60:61], v157 offset:0\n\t"
        "ds_read_b64_tr_b16 a[240:241], v131 offset:18432\n\t"
        "v_or_b32 v158, v184, v158\n\t"
        "v_mfma_f32_16x16x32_bf16 v[32:35], a[212:215], v[92:95], v[32:35]\n\t"
        "ds_read_b64_tr_b16 a[242:243], v131 offset:19200\n\t"
        "ds_read_b64_tr_b16 a[244:245], v132 offset:18432\n\t"
        "v_xor_b32 v159, 32, v157\n\t"
        "v_mfma_f32_16x16x32_bf16 v[36:39], a[180:183], v[92:95], v[36:39]\n\t"
        "ds_read_b64_tr_b16 a[246:247], v132 offset:19200\n\t"
        "ds_read_b64_tr_b16 a[248:249], v133 offset:18432\n\t"
        "v_xor_b32 v152, 32, v158\n\t"
        "v_mfma_f32_16x16x32_bf16 v[40:43], a[184:187], v[92:95], v[40:43]\n\t"
        "ds_read_b64_tr_b16 a[250:251], v133 offset:19200\n\t"
        "v_perm_b32 v164, v162, v162, v168\n\t"
        "v_mfma_f32_16x16x32_bf16 v[44:47], a[188:191], v[92:95], v[44:47]\n\t"
        "ds_read_b128 v[108:111], v130 offset:8192\n\t"
        "v_perm_b32 v165, v162, v162, v169\n\t"
        "s_waitcnt lgkmcnt(12)\n\t"
        "v_mfma_f32_16x16x32_bf16 v[36:39], a[204:207], v[96:99], v[36:39]\n\t"
        "ds_read_b64_tr_b16 v[62:63], v158 offset:0\n\t"
        "v_perm_b32 v166, v162, v162, v170\n\t"
        "v_mfma_f32_16x16x32_bf16 v[40:43], a[208:211], v[96:99], v[40:43]\n\t"
        "s_waitcnt vmcnt(6)\n\t"
        "s_barrier\n\t"
        "ds_read_b128 v[112:115], v130 offset:12288\n\t"
        "ds_read_b64_tr_b16 a[252:253], v131 offset:24576\n\t"
        "v_mfma_f32_16x16x32_bf16 v[44:47], a[212:215], v[96:99], v[44:47]\n\t"
        "ds_read_b64_tr_b16 v[64:65], v159 offset:0\n\t"
        "ds_read_b64_tr_b16 a[254:255], v131 offset:25344\n\t"
        "v_perm_b32 v167, v162, v162, v171\n\t"
        "s_waitcnt lgkmcnt(11)\n\t"
        "v_mfma_f32_16x16x32_bf16 v[0:3], a[228:231], v[100:103], v[0:3]\n\t"
        "ds_read_b64_tr_b16 v[66:67], v152 offset:0\n\t"
        "ds_read_b64_tr_b16 v[188:189], v132 offset:24576\n\t"
        "v_cmp_ne_u32 vcc, 0, v163\n\t"
        "v_mfma_f32_16x16x32_bf16 v[4:7], a[232:235], v[100:103], v[4:7]\n\t"
        "ds_read_b64_tr_b16 v[68:69], v157 offset:64\n\t"
        "ds_read_b64_tr_b16 v[190:191], v132 offset:25344\n\t"
        "v_mfma_f32_16x16x32_bf16 v[8:11], a[236:239], v[100:103], v[8:11]\n\t"
        "ds_read_b64_tr_b16 v[70:71], v158 offset:64\n\t"
        "ds_read_b64_tr_b16 v[192:193], v133 offset:24576\n\t"
        "v_mfma_f32_16x16x32_bf16 v[0:3], a[216:219], v[104:107], v[0:3]\n\t"
        "ds_read_b64_tr_b16 v[72:73], v159 offset:64\n\t"
        "ds_read_b64_tr_b16 v[194:195], v133 offset:25344\n\t"
        "v_mfma_f32_16x16x32_bf16 v[4:7], a[220:223], v[104:107], v[4:7]\n\t"
        "ds_read_b64_tr_b16 v[74:75], v152 offset:64\n\t"
        "v_mfma_f32_16x16x32_bf16 v[8:11], a[224:227], v[104:107], v[8:11]\n\t"
        "ds_read_b64_tr_b16 v[76:77], v157 offset:128\n\t"
        "v_mfma_f32_16x16x32_bf16 v[12:15], a[228:231], v[104:107], v[12:15]\n\t"
        "ds_read_b64_tr_b16 v[78:79], v158 offset:128\n\t"
        "s_waitcnt lgkmcnt(13)\n\t"
        "v_mfma_f32_16x16x32_bf16 v[48:51], v[60:63], v[164:167], 0\n\t"
        "ds_read_b64_tr_b16 v[80:81], v159 offset:128\n\t"
        "v_mfma_f32_16x16x32_bf16 v[16:19], a[232:235], v[104:107], v[16:19]\n\t"
        "ds_read_b64_tr_b16 v[82:83], v152 offset:128\n\t"
        "v_mfma_f32_16x16x32_bf16 v[20:23], a[236:239], v[104:107], v[20:23]\n\t"
        "ds_read_b128 v[84:87], v130 offset:16384\n\t"
        "ds_read_b64_tr_b16 v[196:197], v131 offset:30720\n\t"
        "s_waitcnt lgkmcnt(13)\n\t"
        "v_mfma_f32_16x16x32_bf16 v[52:55], v[64:67], v[164:167], 0\n\t"
        "ds_read_b64_tr_b16 v[198:199], v131 offset:31488\n\t"
        "ds_read_b64_tr_b16 v[200:201], v132 offset:30720\n\t"
        "v_mfma_f32_16x16x32_bf16 v[0:3], a[240:243], v[108:111], v[0:3]\n\t"
        "ds_read_b64_tr_b16 v[202:203], v132 offset:31488\n\t"
        "ds_read_b64_tr_b16 v[204:205], v133 offset:30720\n\t"
        "s_waitcnt lgkmcnt(13)\n\t"
        "v_mfma_f32_16x16x32_bf16 v[56:59], v[68:71], v[164:167], 0\n\t"
        "ds_read_b64_tr_b16 v[206:207], v133 offset:31488\n\t"
        "v_mfma_f32_16x16x32_bf16 v[4:7], a[244:247], v[108:111], v[4:7]\n\t"
        "s_waitcnt lgkmcnt(11)\n\t"
        "v_mfma_f32_16x16x32_bf16 v[116:119], v[72:75], v[164:167], 0\n\t"
        "v_mfma_f32_16x16x32_bf16 v[8:11], a[248:251], v[108:111], v[8:11]\n\t"
        "s_waitcnt lgkmcnt(9)\n\t"
        "v_mfma_f32_16x16x32_bf16 v[120:123], v[76:79], v[164:167], 0\n\t"
        "v_mfma_f32_16x16x32_bf16 v[12:15], a[216:219], v[108:111], v[12:15]\n\t"
        "ds_read_b128 v[88:91], v130 offset:64\n\t"
        "s_waitcnt lgkmcnt(8)\n\t"
        "v_mfma_f32_16x16x32_bf16 v[124:127], v[80:83], v[164:167], 0\n\t"
        "s_cbranch_vccnz .Lx42%=\n\t"
        ".Lx43%=:\n\t"
        "v_cvt_pk_bf16_f32 v48, v48, v49\n\t"
        "v_mfma_f32_16x16x32_bf16 v[16:19], a[220:223], v[108:111], v[16:19]\n\t"
        "ds_read_b128 v[160:163], v185 offset:256\n\t"
        "v_cvt_pk_bf16_f32 v49, v50, v51\n\t"
        "v_mfma_f32_16x16x32_bf16 v[20:23], a[224:227], v[108:111], v[20:23]\n\t"
        "ds_write_b64 v186, v[48:49] offset:0\n\t"
        "v_mfma_f32_16x16x32_bf16 v[24:27], a[228:231], v[108:111], v[24:27]\n\t"
        "ds_read_b128 v[92:95], v130 offset:4160\n\t"
        "v_cvt_pk_bf16_f32 v52, v52, v53\n\t"
        "v_mfma_f32_16x16x32_bf16 v[28:31], a[232:235], v[108:111], v[28:31]\n\t"
        "v_cvt_pk_bf16_f32 v53, v54, v55\n\t"
        "v_cvt_pk_bf16_f32 v56, v56, v57\n\t"
        "v_mfma_f32_16x16x32_bf16 v[32:35], a[236:239], v[108:111], v[32:35]\n\t"
        "ds_write_b64 v154, v[52:53] offset:0\n\t"
        "v_mfma_f32_16x16x32_bf16 v[12:15], a[240:243], v[112:115], v[12:15]\n\t"
        "v_cvt_pk_bf16_f32 v57, v58, v59\n\t"
        "v_cvt_pk_bf16_f32 v116, v116, v117\n\t"
        "v_mfma_f32_16x16x32_bf16 v[16:19], a[244:247], v[112:115], v[16:19]\n\t"
        "ds_write_b64 v186, v[56:57] offset:64\n\t"
        "v_mfma_f32_16x16x32_bf16 v[20:23], a[248:251], v[112:115], v[20:23]\n\t"
        "v_cvt_pk_bf16_f32 v117, v118, v119\n\t"
        "v_cvt_pk_bf16_f32 v120, v120, v121\n\t"
        "v_mfma_f32_16x16x32_bf16 v[24:27], a[216:219], v[112:115], v[24:27]\n\t"
        "ds_write_b64 v154, v[116:117] offset:64\n\t"
        "v_mfma_f32_16x16x32_bf16 v[28:31], a[220:223], v[112:115], v[28:31]\n\t"
        "s_waitcnt vmcnt(0) lgkmcnt(5)\n\t"
        "s_barrier\n\t"
        "ds_read_b128 v[96:99], v130 offset:20480\n\t"
        "ds_read_b64_tr_b16 v[208:209], v131 offset:36864\n\t"
        "v_mfma_f32_16x16x32_bf16 v[32:35], a[224:227], v[112:115], v[32:35]\n\t"
        "ds_read_b64_tr_b16 v[210:211], v131 offset:37632\n\t"
        "ds_read_b64_tr_b16 v[212:213], v132 offset:36864\n\t"
        "v_cvt_pk_bf16_f32 v121, v122, v123\n\t"
        "v_mfma_f32_16x16x32_bf16 v[36:39], a[228:231], v[112:115], v[36:39]\n\t"
        "ds_write_b64 v186, v[120:121] offset:128\n\t"
        "ds_read_b64_tr_b16 v[214:215], v132 offset:37632\n\t"
        "v_mfma_f32_16x16x32_bf16 v[40:43], a[232:235], v[112:115], v[40:43]\n\t"
        "ds_read_b64_tr_b16 v[216:217], v133 offset:36864\n\t"
        "ds_read_b64_tr_b16 v[218:219], v133 offset:37632\n\t"
        "v_cvt_pk_bf16_f32 v124, v124, v125\n\t"
        "v_mfma_f32_16x16x32_bf16 v[44:47], a[236:239], v[112:115], v[44:47]\n\t"
        "ds_read_b128 v[100:103], v130 offset:8256\n\t"
        "v_cvt_pk_bf16_f32 v125, v126, v127\n\t"
        "v_mfma_f32_16x16x32_bf16 v[24:27], a[240:243], v[84:87], v[24:27]\n\t"
        "ds_write_b64 v154, v[124:125] offset:128\n\t"
        "v_mfma_f32_16x16x32_bf16 v[28:31], a[244:247], v[84:87], v[28:31]\n\t"
        "ds_read_b64_tr_b16 v[220:221], v131 offset:43008\n\t"
        "v_lshlrev_b32_sdwa v157, 5, v160 dst_sel:DWORD dst_unused:UNUSED_PAD src0_sel:DWORD src1_sel:WORD_0\n\t"
        "v_mfma_f32_16x16x32_bf16 v[32:35], a[248:251], v[84:87], v[32:35]\n\t"
        "ds_read_b64_tr_b16 v[222:223], v131 offset:43776\n\t"
        "v_lshlrev_b32_sdwa v158, 5, v160 dst_sel:DWORD dst_unused:UNUSED_PAD src0_sel:DWORD src1_sel:WORD_1\n\t"
        "v_mfma_f32_16x16x32_bf16 v[36:39], a[216:219], v[84:87], v[36:39]\n\t"
        "ds_read_b64_tr_b16 v[224:225], v132 offset:43008\n\t"
        "v_or_b32 v157, v184, v157\n\t"
        "v_mfma_f32_16x16x32_bf16 v[40:43], a[220:223], v[84:87], v[40:43]\n\t"
        "ds_read_b64_tr_b16 v[60:61], v157 offset:0\n\t"
        "v_or_b32 v158, v184, v158\n\t"
        "v_mfma_f32_16x16x32_bf16 v[44:47], a[224:227], v[84:87], v[44:47]\n\t"
        "ds_read_b64_tr_b16 v[62:63], v158 offset:0\n\t"
        "ds_read_b64_tr_b16 v[226:227], v132 offset:43776\n\t"
        "v_xor_b32 v159, 32, v157\n\t"
        "v_mfma_f32_16x16x32_bf16 v[0:3], v[196:199], v[88:91], v[0:3]\n\t"
        "ds_read_b64_tr_b16 v[64:65], v159 offset:0\n\t"
        "ds_read_b128 v[104:107], v130 offset:12352\n\t"
        "v_xor_b32 v152, 32, v158\n\t"
        "v_mfma_f32_16x16x32_bf16 v[4:7], v[200:203], v[88:91], v[4:7]\n\t"
        "ds_read_b64_tr_b16 v[66:67], v152 offset:0\n\t"
        "ds_read_b64_tr_b16 v[228:229], v133 offset:43008\n\t"
        "v_perm_b32 v164, v162, v162, v168\n\t"
        "v_mfma_f32_16x16x32_bf16 v[8:11], v[204:207], v[88:91], v[8:11]\n\t"
        "ds_read_b64_tr_b16 v[68:69], v157 offset:64\n\t"
        "ds_read_b64_tr_b16 v[230:231], v133 offset:43776\n\t"
        "v_perm_b32 v165, v162, v162, v169\n\t"
        "s_waitcnt lgkmcnt(12)\n\t"
        "v_mfma_f32_16x16x32_bf16 v[0:3], a[252:255], v[92:95], v[0:3]\n\t"
        "ds_read_b64_tr_b16 v[70:71], v158 offset:64\n\t"
        "v_perm_b32 v166, v162, v162, v170\n\t"
        "v_mfma_f32_16x16x32_bf16 v[4:7], v[188:191], v[92:95], v[4:7]\n\t"
        "ds_read_b64_tr_b16 v[72:73], v159 offset:64\n\t"
        "v_perm_b32 v167, v162, v162, v171\n\t"
        "v_mfma_f32_16x16x32_bf16 v[8:11], v[192:195], v[92:95], v[8:11]\n\t"
        "ds_read_b64_tr_b16 v[74:75], v152 offset:64\n\t"
        "v_cmp_ne_u32 vcc, 0, v163\n\t"
        "v_mfma_f32_16x16x32_bf16 v[12:15], v[196:199], v[92:95], v[12:15]\n\t"
        "ds_read_b64_tr_b16 v[76:77], v157 offset:128\n\t"
        "v_mfma_f32_16x16x32_bf16 v[16:19], v[200:203], v[92:95], v[16:19]\n\t"
        "ds_read_b64_tr_b16 v[78:79], v158 offset:128\n\t"
        "v_mfma_f32_16x16x32_bf16 v[20:23], v[204:207], v[92:95], v[20:23]\n\t"
        "ds_read_b64_tr_b16 v[80:81], v159 offset:128\n\t"
        "s_waitcnt lgkmcnt(11)\n\t"
        "v_mfma_f32_16x16x32_bf16 v[48:51], v[60:63], v[164:167], 0\n\t"
        "ds_read_b64_tr_b16 v[82:83], v152 offset:128\n\t"
        "v_mfma_f32_16x16x32_bf16 v[36:39], a[240:243], v[96:99], v[36:39]\n\t"
        ".LpM1%=:\n\t"
        "s_cbranch_execz .LrM%=\n\t"
        "ds_read_b128 v[108:111], v130 offset:16448\n\t"
        "s_waitcnt lgkmcnt(10)\n\t"
        "v_mfma_f32_16x16x32_bf16 v[52:55], v[64:67], v[164:167], 0\n\t"
        "ds_read_b64_tr_b16 v[232:233], v131 offset:49152\n\t"
        "v_mfma_f32_16x16x32_bf16 v[40:43], a[244:247], v[96:99], v[40:43]\n\t"
        "ds_read_b64_tr_b16 v[234:235], v131 offset:49920\n\t"
        "s_waitcnt lgkmcnt(9)\n\t"
        "v_mfma_f32_16x16x32_bf16 v[56:59], v[68:71], v[164:167], 0\n\t"
        "ds_read_b64_tr_b16 v[236:237], v132 offset:49152\n\t"
        "v_mfma_f32_16x16x32_bf16 v[44:47], a[248:251], v[96:99], v[44:47]\n\t"
        "ds_read_b64_tr_b16 v[238:239], v132 offset:49920\n\t"
        "ds_read_b64_tr_b16 v[240:241], v133 offset:49152\n\t"
        "s_waitcnt lgkmcnt(10)\n\t"
        "v_mfma_f32_16x16x32_bf16 v[116:119], v[72:75], v[164:167], 0\n\t"
        "ds_read_b64_tr_b16 v[242:243], v133 offset:49920\n\t"
        "v_mfma_f32_16x16x32_bf16 v[0:3], v[208:211], v[100:103], v[0:3]\n\t"
        "ds_read_b64_tr_b16 v[244:245], v131 offset:55296\n\t"
        "s_waitcnt lgkmcnt(10)\n\t"
        "v_mfma_f32_16x16x32_bf16 v[120:123], v[76:79], v[164:167], 0\n\t"
        "ds_read_b64_tr_b16 v[246:247], v131 offset:56064\n\t"
        "v_mfma_f32_16x16x32_bf16 v[4:7], v[212:215], v[100:103], v[4:7]\n\t"
        "ds_read_b64_tr_b16 v[248:249], v132 offset:55296\n\t"
        "s_waitcnt lgkmcnt(10)\n\t"
        "v_mfma_f32_16x16x32_bf16 v[124:127], v[80:83], v[164:167], 0\n\t"
        "ds_read_b64_tr_b16 v[250:251], v132 offset:56064\n\t"
        "s_cbranch_vccnz .Lx44%=\n\t"
        ".Lx45%=:\n\t"
        "v_mfma_f32_16x16x32_bf16 v[8:11], v[216:219], v[100:103], v[8:11]\n\t"
        "ds_read_b64_tr_b16 v[252:253], v133 offset:55296\n\t"
        "v_cvt_pk_bf16_f32 v48, v48, v49\n\t"
        "v_mfma_f32_16x16x32_bf16 v[12:15], a[252:255], v[100:103], v[12:15]\n\t"
        "ds_read_b128 v[112:115], v130 offset:128\n\t"
        "v_cvt_pk_bf16_f32 v49, v50, v51\n\t"
        "v_mfma_f32_16x16x32_bf16 v[16:19], v[188:191], v[100:103], v[16:19]\n\t"
        "ds_write_b64 v186, v[48:49] offset:12288\n\t"
        "v_mfma_f32_16x16x32_bf16 v[20:23], v[192:195], v[100:103], v[20:23]\n\t"
        "ds_read_b64_tr_b16 v[254:255], v133 offset:56064\n\t"
        "v_cvt_pk_bf16_f32 v52, v52, v53\n\t"
        "v_mfma_f32_16x16x32_bf16 v[24:27], v[196:199], v[100:103], v[24:27]\n\t"
        "ds_read_b128 v[84:87], v130 offset:4224\n\t"
        "v_cvt_pk_bf16_f32 v53, v54, v55\n\t"
        "v_mfma_f32_16x16x32_bf16 v[28:31], v[200:203], v[100:103], v[28:31]\n\t"
        "ds_write_b64 v154, v[52:53] offset:12288\n\t"
        "v_mfma_f32_16x16x32_bf16 v[32:35], v[204:207], v[100:103], v[32:35]\n\t"
        "v_cvt_pk_bf16_f32 v56, v56, v57\n\t"
        "v_cvt_pk_bf16_f32 v57, v58, v59\n\t"
        "v_mfma_f32_16x16x32_bf16 v[12:15], v[208:211], v[104:107], v[12:15]\n\t"
        "ds_write_b64 v186, v[56:57] offset:12352\n\t"
        "v_mfma_f32_16x16x32_bf16 v[16:19], v[212:215], v[104:107], v[16:19]\n\t"
        "v_cvt_pk_bf16_f32 v116, v116, v117\n\t"
        "v_cvt_pk_bf16_f32 v117, v118, v119\n\t"
        "v_mfma_f32_16x16x32_bf16 v[20:23], v[216:219], v[104:107], v[20:23]\n\t"
        "ds_write_b64 v154, v[116:117] offset:12352\n\t"
        "v_mfma_f32_16x16x32_bf16 v[24:27], a[252:255], v[104:107], v[24:27]\n\t"
        "ds_read_b128 v[88:91], v130 offset:8320\n\t"
        "v_cvt_pk_bf16_f32 v120, v120, v121\n\t"
        "v_mfma_f32_16x16x32_bf16 v[28:31], v[188:191], v[104:107], v[28:31]\n\t"
        "v_cvt_pk_bf16_f32 v121, v122, v123\n\t"
        "v_cvt_pk_bf16_f32 v124, v124, v125\n\t"
        "v_mfma_f32_16x16x32_bf16 v[32:35], v[192:195], v[104:107], v[32:35]\n\t"
        "ds_write_b64 v186, v[120:121] offset:12416\n\t"
        "v_mfma_f32_16x16x32_bf16 v[36:39], v[196:199], v[104:107], v[36:39]\n\t"
        "v_cvt_pk_bf16_f32 v125, v126, v127\n\t"
        "v_mfma_f32_16x16x32_bf16 v[40:43], v[200:203], v[104:107], v[40:43]\n\t"
        "ds_write_b64 v154, v[124:125] offset:12416\n\t"
        "v_mfma_f32_16x16x32_bf16 v[44:47], v[204:207], v[104:107], v[44:47]\n\t"
        "s_waitcnt lgkmcnt(6)\n\t"
        "v_mfma_f32_16x16x32_bf16 v[24:27], v[208:211], v[108:111], v[24:27]\n\t"
        "v_mfma_f32_16x16x32_bf16 v[28:31], v[212:215], v[108:111], v[28:31]\n\t"
        "v_mfma_f32_16x16x32_bf16 v[32:35], v[216:219], v[108:111], v[32:35]\n\t"
        "v_mfma_f32_16x16x32_bf16 v[36:39], a[252:255], v[108:111], v[36:39]\n\t"
        "ds_read_b128 v[92:95], v130 offset:12416\n\t"
        "v_mfma_f32_16x16x32_bf16 v[40:43], v[188:191], v[108:111], v[40:43]\n\t"
        "s_waitcnt lgkmcnt(1)\n\t"
        "s_barrier\n\t"
        "v_mfma_f32_16x16x32_bf16 v[44:47], v[192:195], v[108:111], v[44:47]\n\t"
        "ds_read_b128 v[124:127], v182 offset:0\n\t"
        "v_mfma_f32_16x16x32_bf16 v[0:3], v[232:235], v[112:115], v[0:3]\n\t"
        "ds_read_b128 v[132:135], v187 offset:0\n\t"
        "v_mfma_f32_16x16x32_bf16 v[4:7], v[236:239], v[112:115], v[4:7]\n\t"
        "ds_read_b128 v[136:139], v187 offset:16\n\t"
        "v_mfma_f32_16x16x32_bf16 v[8:11], v[240:243], v[112:115], v[8:11]\n\t"
        "ds_read_b32 v140, v187 offset:32\n\t"
        "v_mfma_f32_16x16x32_bf16 v[0:3], v[220:223], v[84:87], v[0:3]\n\t"
        "v_mfma_f32_16x16x32_bf16 v[4:7], v[224:227], v[84:87], v[4:7]\n\t"
        "v_mfma_f32_16x16x32_bf16 v[8:11], v[228:231], v[84:87], v[8:11]\n\t"
        "v_mfma_f32_16x16x32_bf16 v[12:15], v[232:235], v[84:87], v[12:15]\n\t"
        "ds_read_b128 v[96:99], v130 offset:16512\n\t"
        "v_mfma_f32_16x16x32_bf16 v[16:19], v[236:239], v[84:87], v[16:19]\n\t"
        "v_mfma_f32_16x16x32_bf16 v[20:23], v[240:243], v[84:87], v[20:23]\n\t"
        "s_waitcnt lgkmcnt(4)\n\t"
        "v_or3_b32 v124, v124, v125, v126\n\t"
        "v_or_b32 v124, v124, v127\n\t"
        "v_mfma_f32_16x16x32_bf16 v[0:3], v[244:247], v[88:91], v[0:3]\n\t"
        "v_readfirstlane_b32 s6, v124\n\t"
        "v_mfma_f32_16x16x32_bf16 v[4:7], v[248:251], v[88:91], v[4:7]\n\t"
        "s_nop 0\n\t"
        "s_bitcmp1_b32 s6, 1\n\t"
        "s_cbranch_scc1 .Lx46%=\n\t"
        ".Lx47%=:\n\t"
        "v_mfma_f32_16x16x32_bf16 v[8:11], v[252:255], v[88:91], v[8:11]\n\t"
        "ds_read_b128 v[160:163], v185 offset:512\n\t"
        "s_waitcnt lgkmcnt(3)\n\t"
        "v_xor_b32 v132, v181, v132\n\t"
        "v_mfma_f32_16x16x32_bf16 v[12:15], v[220:223], v[88:91], v[12:15]\n\t"
        "ds_read_b128 v[100:103], v130 offset:20544\n\t"
        "v_add_u32 v151, s9, v172\n\t"
        "v_mfma_f32_16x16x32_bf16 v[16:19], v[224:227], v[88:91], v[16:19]\n\t"
        "v_max_u32 v151, v151, v132\n\t"
        "v_xor_b32 v133, v181, v133\n\t"
        "v_mfma_f32_16x16x32_bf16 v[20:23], v[228:231], v[88:91], v[20:23]\n\t"
        "v_add_u32 v183, s9, v173\n\t"
        "v_max_u32 v183, v183, v133\n\t"
        "v_mfma_f32_16x16x32_bf16 v[24:27], v[232:235], v[88:91], v[24:27]\n\t"
        "ds_read_b128 v[104:107], v130 offset:20608\n\t"
        "v_xor_b32 v134, v181, v134\n\t"
        "s_add_u32 s55, s41, 0\n\t"
        "v_mfma_f32_16x16x32_bf16 v[28:31], v[236:239], v[88:91], v[28:31]\n\t"
        "v_cvt_pk_bf16_f32 v116, v0, v1\n\t"
        "v_cvt_pk_bf16_f32 v117, v2, v3\n\t"
        "v_mfma_f32_16x16x32_bf16 v[32:35], v[240:243], v[88:91], v[32:35]\n\t"
        "v_cvt_pk_bf16_f32 v118, v4, v5\n\t"
        "v_cvt_pk_bf16_f32 v119, v6, v7\n\t"
        "v_mfma_f32_16x16x32_bf16 v[12:15], v[244:247], v[92:95], v[12:15]\n\t"
        "ds_read_b128 v[108:111], v151 offset:0\n\t"
        "v_cvt_pk_bf16_f32 v120, v8, v9\n\t"
        "v_mfma_f32_16x16x32_bf16 v[16:19], v[248:251], v[92:95], v[16:19]\n\t"
        "v_cvt_pk_bf16_f32 v121, v10, v11\n\t"
        "v_mfma_f32_16x16x32_bf16 v[20:23], v[252:255], v[92:95], v[20:23]\n\t"
        "v_permlane16_swap_b32 v116, v118\n\t"
        "s_waitcnt lgkmcnt(3)\n\t"
        "v_lshlrev_b32_sdwa v157, 5, v160 dst_sel:DWORD dst_unused:UNUSED_PAD src0_sel:DWORD src1_sel:WORD_0\n\t"
        "v_mfma_f32_16x16x32_bf16 v[24:27], v[220:223], v[92:95], v[24:27]\n\t"
        "v_permlane16_swap_b32 v117, v119\n\t"
        "v_mfma_f32_16x16x32_bf16 v[28:31], v[224:227], v[92:95], v[28:31]\n\t"
        "buffer_store_dwordx4 v[116:119], v144, s[28:31], s55 offen sc1\n\t"
        "v_mfma_f32_16x16x32_bf16 v[32:35], v[228:231], v[92:95], v[32:35]\n\t"
        "ds_write_b64 v146, v[120:121] offset:0\n\t"
        "v_mfma_f32_16x16x32_bf16 v[36:39], v[232:235], v[92:95], v[36:39]\n\t"
        "ds_read_b128 v[112:115], v151 offset:64\n\t"
        "s_add_u32 s55, s41, 43008\n\t"
        "v_mfma_f32_16x16x32_bf16 v[40:43], v[236:239], v[92:95], v[40:43]\n\t"
        "ds_read_b128 v[84:87], v151 offset:128\n\t"
        "v_add_u32 v151, s9, v174\n\t"
        "v_max_u32 v151, v151, v134\n\t"
        "v_mfma_f32_16x16x32_bf16 v[44:47], v[240:243], v[92:95], v[44:47]\n\t"
        "v_xor_b32 v135, v181, v135\n\t"
        "v_cvt_pk_bf16_f32 v116, v12, v13\n\t"
        "v_mfma_f32_16x16x32_bf16 v[24:27], v[244:247], v[96:99], v[24:27]\n\t"
        "ds_read_b128 v[88:91], v183 offset:0\n\t"
        "v_cvt_pk_bf16_f32 v117, v14, v15\n\t"
        "v_mfma_f32_16x16x32_bf16 v[28:31], v[248:251], v[96:99], v[28:31]\n\t"
        "v_cvt_pk_bf16_f32 v118, v16, v17\n\t"
        "v_cvt_pk_bf16_f32 v119, v18, v19\n\t"
        "v_mfma_f32_16x16x32_bf16 v[32:35], v[252:255], v[96:99], v[32:35]\n\t"
        "v_cvt_pk_bf16_f32 v120, v20, v21\n\t"
        "v_cvt_pk_bf16_f32 v121, v22, v23\n\t"
        "v_mfma_f32_16x16x32_bf16 v[36:39], v[220:223], v[96:99], v[36:39]\n\t"
        "v_permlane16_swap_b32 v116, v118\n\t"
        "v_lshlrev_b32_sdwa v158, 5, v160 dst_sel:DWORD dst_unused:UNUSED_PAD src0_sel:DWORD src1_sel:WORD_1\n\t"
        "v_mfma_f32_16x16x32_bf16 v[40:43], v[224:227], v[96:99], v[40:43]\n\t"
        "v_permlane16_swap_b32 v117, v119\n\t"
        "v_mfma_f32_16x16x32_bf16 v[44:47], v[228:231], v[96:99], v[44:47]\n\t"
        "ds_read_b128 v[92:95], v183 offset:64\n\t"
        "v_or_b32 v157, v184, v157\n\t"
        "s_waitcnt lgkmcnt(4)\n\t"
        "v_mfma_f32_16x16x32_bf16 v[36:39], v[208:211], v[100:103], v[36:39]\n\t"
        "ds_read_b64_tr_b16 v[60:61], v157 offset:0\n\t"
        "v_or_b32 v158, v184, v158\n\t"
        "v_mfma_f32_16x16x32_bf16 v[40:43], v[212:215], v[100:103], v[40:43]\n\t"
        "ds_read_b64_tr_b16 v[62:63], v158 offset:0\n\t"
        "v_xor_b32 v159, 32, v157\n\t"
        "v_mfma_f32_16x16x32_bf16 v[44:47], v[216:219], v[100:103], v[44:47]\n\t"
        "ds_read_b64_tr_b16 v[64:65], v159 offset:0\n\t"
        "v_xor_b32 v152, 32, v158\n\t"
        "v_perm_b32 v164, v162, v162, v168\n\t"
        "v_mfma_f32_16x16x32_bf16 v[36:39], v[244:247], v[104:107], v[36:39]\n\t"
        "buffer_store_dwordx4 v[116:119], v144, s[28:31], s55 offen sc1\n\t"
        "v_mfma_f32_16x16x32_bf16 v[40:43], v[248:251], v[104:107], v[40:43]\n\t"
        "ds_write_b64 v146, v[120:121] offset:2048\n\t"
        "v_mfma_f32_16x16x32_bf16 v[44:47], v[252:255], v[104:107], v[44:47]\n\t"
        "ds_read_b128 v[96:99], v183 offset:128\n\t"
        "v_add_u32 v183, s9, v175\n\t"
        "v_mfma_f32_16x16x32_bf16 v[48:51], a[12:15], v[108:111], 0\n\t"
        "ds_read_b64_tr_b16 v[66:67], v152 offset:0\n\t"
        "v_max_u32 v183, v183, v135\n\t"
        "v_mfma_f32_16x16x32_bf16 v[52:55], a[16:19], v[108:111], 0\n\t"
        "ds_read_b64_tr_b16 v[68:69], v157 offset:64\n\t"
        "v_xor_b32 v136, v181, v136\n\t"
        "s_add_u32 s55, s41, 86016\n\t"
        "v_mfma_f32_16x16x32_bf16 v[56:59], a[20:23], v[108:111], 0\n\t"
        "ds_read_b128 v[100:103], v151 offset:0\n\t"
        "v_cvt_pk_bf16_f32 v116, v24, v25\n\t"
        "s_waitcnt lgkmcnt(7)\n\t"
        "v_mfma_f32_16x16x32_bf16 v[48:51], a[48:51], v[112:115], v[48:51]\n\t"
        "ds_read_b64_tr_b16 v[70:71], v158 offset:64\n\t"
        "v_cvt_pk_bf16_f32 v117, v26, v27\n\t"
        "v_mfma_f32_16x16x32_bf16 v[52:55], a[52:55], v[112:115], v[52:55]\n\t"
        "ds_read_b64_tr_b16 v[72:73], v159 offset:64\n\t"
        "v_cvt_pk_bf16_f32 v118, v28, v29\n\t"
        "v_mfma_f32_16x16x32_bf16 v[56:59], a[56:59], v[112:115], v[56:59]\n\t"
        "ds_read_b128 v[104:107], v151 offset:64\n\t"
        "v_cvt_pk_bf16_f32 v119, v30, v31\n\t"
        "v_mfma_f32_16x16x32_bf16 v[48:51], a[84:87], v[84:87], v[48:51]\n\t"
        "ds_read_b64_tr_b16 v[74:75], v152 offset:64\n\t"
        "v_cvt_pk_bf16_f32 v120, v32, v33\n\t"
        "v_cvt_pk_bf16_f32 v121, v34, v35\n\t"
        "v_mfma_f32_16x16x32_bf16 v[52:55], a[88:91], v[84:87], v[52:55]\n\t"
        "v_permlane16_swap_b32 v116, v118\n\t"
        "v_mfma_f32_16x16x32_bf16 v[56:59], a[92:95], v[84:87], v[56:59]\n\t"
        "v_permlane16_swap_b32 v117, v119\n\t"
        "v_mfma_f32_16x16x32_bf16 v[48:51], a[120:123], v[88:91], v[48:51]\n\t"
        "ds_read_b128 v[108:111], v151 offset:128\n\t"
        "v_add_u32 v151, s9, v176\n\t"
        "v_mfma_f32_16x16x32_bf16 v[52:55], a[124:127], v[88:91], v[52:55]\n\t"
        "ds_read_b64_tr_b16 v[76:77], v157 offset:128\n\t"
        "v_max_u32 v151, v151, v136\n\t"
        "v_mfma_f32_16x16x32_bf16 v[56:59], a[128:131], v[88:91], v[56:59]\n\t"
        "buffer_store_dwordx4 v[116:119], v144, s[28:31], s55 offen sc1\n\t"
        "v_mfma_f32_16x16x32_bf16 v[48:51], a[156:159], v[92:95], v[48:51]\n\t"
        "ds_write_b64 v146, v[120:121] offset:4096\n\t"
        "v_mfma_f32_16x16x32_bf16 v[52:55], a[160:163], v[92:95], v[52:55]\n\t"
        "ds_read_b128 v[112:115], v183 offset:0\n\t"
        "v_xor_b32 v137, v181, v137\n\t"
        "s_add_u32 s55, s41, 129024\n\t"
        "v_mfma_f32_16x16x32_bf16 v[56:59], a[164:167], v[92:95], v[56:59]\n\t"
        "ds_read_b128 v[84:87], v183 offset:64\n\t"
        "v_cvt_pk_bf16_f32 v116, v36, v37\n\t"
        "s_waitcnt lgkmcnt(5)\n\t"
        "v_mfma_f32_16x16x32_bf16 v[48:51], a[192:195], v[96:99], v[48:51]\n\t"
        "ds_read_b64_tr_b16 v[78:79], v158 offset:128\n\t"
        "v_cvt_pk_bf16_f32 v117, v38, v39\n\t"
        "v_mfma_f32_16x16x32_bf16 v[52:55], a[196:199], v[96:99], v[52:55]\n\t"
        "ds_read_b64_tr_b16 v[80:81], v159 offset:128\n\t"
        "v_cvt_pk_bf16_f32 v118, v40, v41\n\t"
        "v_mfma_f32_16x16x32_bf16 v[56:59], a[200:203], v[96:99], v[56:59]\n\t"
        "ds_read_b128 v[88:91], v183 offset:128\n\t"
        "v_add_u32 v183, s9, v177\n\t"
        "v_max_u32 v183, v183, v137\n\t"
        "v_mfma_f32_16x16x32_bf16 v[48:51], a[228:231], v[100:103], v[48:51]\n\t"
        "ds_read_b64_tr_b16 v[82:83], v152 offset:128\n\t"
        "v_xor_b32 v138, v181, v138\n\t"
        "v_mfma_f32_16x16x32_bf16 v[52:55], a[232:235], v[100:103], v[52:55]\n\t"
        "ds_read_b128 v[92:95], v151 offset:0\n\t"
        "v_cvt_pk_bf16_f32 v119, v42, v43\n\t"
        "v_mfma_f32_16x16x32_bf16 v[56:59], a[236:239], v[100:103], v[56:59]\n\t"
        "ds_read_b128 v[96:99], v151 offset:64\n\t"
        "v_cvt_pk_bf16_f32 v120, v44, v45\n\t"
        "v_mfma_f32_16x16x32_bf16 v[48:51], v[196:199], v[104:107], v[48:51]\n\t"
        "ds_read_b128 v[100:103], v151 offset:128\n\t"
        "v_add_u32 v151, s9, v178\n\t"
        "v_mfma_f32_16x16x32_bf16 v[52:55], v[200:203], v[104:107], v[52:55]\n\t"
        "v_max_u32 v151, v151, v138\n\t"
        "v_xor_b32 v139, v181, v139\n\t"
        "v_cvt_pk_bf16_f32 v121, v46, v47\n\t"
        "v_mfma_f32_16x16x32_bf16 v[56:59], v[204:207], v[104:107], v[56:59]\n\t"
        "v_permlane16_swap_b32 v116, v118\n\t"
        "s_waitcnt lgkmcnt(7)\n\t"
        "v_mfma_f32_16x16x32_bf16 v[48:51], v[232:235], v[108:111], v[48:51]\n\t"
        "v_permlane16_swap_b32 v117, v119\n\t"
        "v_mfma_f32_16x16x32_bf16 v[52:55], v[236:239], v[108:111], v[52:55]\n\t"
        "buffer_store_dwordx4 v[116:119], v144, s[28:31], s55 offen sc1\n\t"
        "v_mfma_f32_16x16x32_bf16 v[56:59], v[240:243], v[108:111], v[56:59]\n\t"
        "ds_write_b64 v146, v[120:121] offset:6144\n\t"
        "v_mfma_f32_16x16x32_bf16 v[48:51], a[0:3], v[112:115], v[48:51]\n\t"
        "ds_read_b128 v[104:107], v183 offset:0\n\t"
        "v_perm_b32 v165, v162, v162, v169\n\t"
        "v_perm_b32 v166, v162, v162, v170\n\t"
        "v_mfma_f32_16x16x32_bf16 v[52:55], a[4:7], v[112:115], v[52:55]\n\t"
        "ds_read_b128 v[108:111], v183 offset:64\n\t"
        "v_perm_b32 v167, v162, v162, v171\n\t"
        "v_mfma_f32_16x16x32_bf16 v[56:59], a[8:11], v[112:115], v[56:59]\n\t"
        "v_cmp_ne_u32 vcc, 0, v163\n\t"
        "v_mfma_f32_16x16x32_bf16 v[0:3], v[60:63], v[164:167], 0\n\t"
        "v_mfma_f32_16x16x32_bf16 v[48:51], a[36:39], v[84:87], v[48:51]\n\t"
        "ds_read_b128 v[112:115], v183 offset:128\n\t"
        "v_add_u32 v183, s9, v179\n\t"
        "v_mfma_f32_16x16x32_bf16 v[4:7], v[64:67], v[164:167], 0\n\t"
        "v_max_u32 v183, v183, v139\n\t"
        "v_mfma_f32_16x16x32_bf16 v[52:55], a[40:43], v[84:87], v[52:55]\n\t"
        "v_mfma_f32_16x16x32_bf16 v[8:11], v[68:71], v[164:167], 0\n\t"
        "v_mfma_f32_16x16x32_bf16 v[56:59], a[44:47], v[84:87], v[56:59]\n\t"
        "v_mfma_f32_16x16x32_bf16 v[12:15], v[72:75], v[164:167], 0\n\t"
        "s_waitcnt lgkmcnt(3)\n\t"
        "v_mfma_f32_16x16x32_bf16 v[48:51], a[72:75], v[88:91], v[48:51]\n\t"
        "ds_read_b128 v[84:87], v151 offset:0\n\t"
        "v_mfma_f32_16x16x32_bf16 v[16:19], v[76:79], v[164:167], 0\n\t"
        "v_mfma_f32_16x16x32_bf16 v[52:55], a[76:79], v[88:91], v[52:55]\n\t"
        "v_mfma_f32_16x16x32_bf16 v[20:23], v[80:83], v[164:167], 0\n\t"
        "s_cbranch_vccnz .Lx48%=\n\t"
        ".Lx49%=:\n\t"
        "v_cvt_pk_bf16_f32 v0, v0, v1\n\t"
        "v_cvt_pk_bf16_f32 v1, v2, v3\n\t"
        "v_cvt_pk_bf16_f32 v4, v4, v5\n\t"
        "v_mfma_f32_16x16x32_bf16 v[56:59], a[80:83], v[88:91], v[56:59]\n\t"
        "ds_write_b64 v186, v[0:1] offset:24576\n\t"
        "v_mfma_f32_16x16x32_bf16 v[48:51], a[108:111], v[92:95], v[48:51]\n\t"
        "ds_read_b128 v[88:91], v151 offset:64\n\t"
        "v_cvt_pk_bf16_f32 v5, v6, v7\n\t"
        "v_cvt_pk_bf16_f32 v8, v8, v9\n\t"
        "v_mfma_f32_16x16x32_bf16 v[52:55], a[112:115], v[92:95], v[52:55]\n\t"
        "ds_write_b64 v154, v[4:5] offset:24576\n\t"
        "v_mfma_f32_16x16x32_bf16 v[56:59], a[116:119], v[92:95], v[56:59]\n\t"
        "v_cvt_pk_bf16_f32 v9, v10, v11\n\t"
        "v_cvt_pk_bf16_f32 v12, v12, v13\n\t"
        "v_mfma_f32_16x16x32_bf16 v[48:51], a[144:147], v[96:99], v[48:51]\n\t"
        "ds_write_b64 v186, v[8:9] offset:24640\n\t"
        "v_mfma_f32_16x16x32_bf16 v[52:55], a[148:151], v[96:99], v[52:55]\n\t"
        "ds_read_b128 v[92:95], v151 offset:128\n\t"
        "v_cvt_pk_bf16_f32 v13, v14, v15\n\t"
        "v_cvt_pk_bf16_f32 v16, v16, v17\n\t"
        "v_mfma_f32_16x16x32_bf16 v[56:59], a[152:155], v[96:99], v[56:59]\n\t"
        "ds_write_b64 v154, v[12:13] offset:24640\n\t"
        "v_mfma_f32_16x16x32_bf16 v[48:51], a[180:183], v[100:103], v[48:51]\n\t"
        "ds_read_b128 v[96:99], v183 offset:0\n\t"
        "v_cvt_pk_bf16_f32 v17, v18, v19\n\t"
        "v_mfma_f32_16x16x32_bf16 v[52:55], a[184:187], v[100:103], v[52:55]\n\t"
        "ds_write_b64 v186, v[16:17] offset:24704\n\t"
        "v_mfma_f32_16x16x32_bf16 v[56:59], a[188:191], v[100:103], v[56:59]\n\t"
        "v_cvt_pk_bf16_f32 v20, v20, v21\n\t"
        "v_cvt_pk_bf16_f32 v21, v22, v23\n\t"
        "s_waitcnt lgkmcnt(6)\n\t"
        "v_mfma_f32_16x16x32_bf16 v[48:51], a[216:219], v[104:107], v[48:51]\n\t"
        "ds_write_b64 v154, v[20:21] offset:24704\n\t"
        "v_mfma_f32_16x16x32_bf16 v[52:55], a[220:223], v[104:107], v[52:55]\n\t"
        "s_cmp_gt_u32 s61, 48\n\t"
        "s_cbranch_scc1 .Lx50%=\n\t"
        ".Lx51%=:\n\t"
        "v_mfma_f32_16x16x32_bf16 v[56:59], a[224:227], v[104:107], v[56:59]\n\t"
        "ds_read_b128 v[100:103], v183 offset:64\n\t"
        "v_mfma_f32_16x16x32_bf16 v[48:51], a[252:255], v[108:111], v[48:51]\n\t"
        "ds_read_b128 v[104:107], v183 offset:128\n\t"
        "v_mfma_f32_16x16x32_bf16 v[52:55], v[188:191], v[108:111], v[52:55]\n\t"
        "v_mfma_f32_16x16x32_bf16 v[56:59], v[192:195], v[108:111], v[56:59]\n\t"
        "v_mfma_f32_16x16x32_bf16 v[48:51], v[220:223], v[112:115], v[48:51]\n\t"
        "s_waitcnt lgkmcnt(2)\n\t"
        "s_barrier\n\t"
        "v_xor_b32 v140, v181, v140\n\t"
        "v_mfma_f32_16x16x32_bf16 v[52:55], v[224:227], v[112:115], v[52:55]\n\t"
        "ds_read_b128 v[132:135], v187 offset:4096\n\t"
        "v_mfma_f32_16x16x32_bf16 v[56:59], v[228:231], v[112:115], v[56:59]\n\t"
        "ds_read_b128 v[136:139], v187 offset:4112\n\t"
        "v_add_u32 v151, s9, v180\n\t"
        "v_mfma_f32_16x16x32_bf16 v[48:51], a[24:27], v[84:87], v[48:51]\n\t"
        "ds_read_b128 v[124:127], v182 offset:16\n\t"
        "v_max_u32 v151, v151, v140\n\t"
        "v_mfma_f32_16x16x32_bf16 v[52:55], a[28:31], v[84:87], v[52:55]\n\t"
        "ds_read_b32 v140, v187 offset:4128\n\t"
        "s_waitcnt lgkmcnt(3)\n\t"
        "v_xor_b32 v132, v181, v132\n\t"
        "v_mfma_f32_16x16x32_bf16 v[56:59], a[32:35], v[84:87], v[56:59]\n\t"
        "ds_read_b128 v[108:111], v151 offset:0\n\t"
        "v_readlane_b32 s9, v155, 48\n\t"
        "v_mfma_f32_16x16x32_bf16 v[48:51], a[60:63], v[88:91], v[48:51]\n\t"
        "ds_read_b128 v[112:115], v151 offset:64\n\t"
        "v_add_u32 v183, s9, v172\n\t"
        "v_mfma_f32_16x16x32_bf16 v[52:55], a[64:67], v[88:91], v[52:55]\n\t"
        "ds_read_b128 v[84:87], v151 offset:128\n\t"
        "v_max_u32 v183, v183, v132\n\t"
        "v_mfma_f32_16x16x32_bf16 v[56:59], a[68:71], v[88:91], v[56:59]\n\t"
        "v_xor_b32 v133, v181, v133\n\t"
        "v_add_u32 v151, s9, v173\n\t"
        "v_mfma_f32_16x16x32_bf16 v[48:51], a[96:99], v[92:95], v[48:51]\n\t"
        "ds_read_b128 v[88:91], v183 offset:0\n\t"
        "v_max_u32 v151, v151, v133\n\t"
        "v_mfma_f32_16x16x32_bf16 v[52:55], a[100:103], v[92:95], v[52:55]\n\t"
        "v_xor_b32 v134, v181, v134\n\t"
        "v_mfma_f32_16x16x32_bf16 v[56:59], a[104:107], v[92:95], v[56:59]\n\t"
        "v_mfma_f32_16x16x32_bf16 v[48:51], a[132:135], v[96:99], v[48:51]\n\t"
        "ds_read_b128 v[92:95], v183 offset:64\n\t"
        "s_waitcnt lgkmcnt(6)\n\t"
        "v_or3_b32 v124, v124, v125, v126\n\t"
        "v_mfma_f32_16x16x32_bf16 v[52:55], a[136:139], v[96:99], v[52:55]\n\t"
        "v_or_b32 v124, v124, v127\n\t"
        "v_mfma_f32_16x16x32_bf16 v[56:59], a[140:143], v[96:99], v[56:59]\n\t"
        "v_readfirstlane_b32 s8, v124\n\t"
        "v_mfma_f32_16x16x32_bf16 v[48:51], a[168:171], v[100:103], v[48:51]\n\t"
        "ds_read_b128 v[124:127], v147 offset:0\n\t"
        "s_or_b32 s6, s6, s8\n\t"
        "v_mfma_f32_16x16x32_bf16 v[52:55], a[172:175], v[100:103], v[52:55]\n\t"
        "ds_read_b128 v[96:99], v183 offset:128\n\t"
        "v_add_u32 v183, s9, v174\n\t"
        "v_mfma_f32_16x16x32_bf16 v[56:59], a[176:179], v[100:103], v[56:59]\n\t"
        "v_max_u32 v183, v183, v134\n\t"
        "v_xor_b32 v135, v181, v135\n\t"
        "v_mfma_f32_16x16x32_bf16 v[48:51], a[204:207], v[104:107], v[48:51]\n\t"
        "ds_read_b128 v[100:103], v151 offset:0\n\t"
        "v_mfma_f32_16x16x32_bf16 v[52:55], a[208:211], v[104:107], v[52:55]\n\t"
        "v_mfma_f32_16x16x32_bf16 v[56:59], a[212:215], v[104:107], v[56:59]\n\t"
        "s_waitcnt lgkmcnt(4)\n\t"
        "v_mfma_f32_16x16x32_bf16 v[48:51], a[240:243], v[108:111], v[48:51]\n\t"
        "ds_read_b128 v[104:107], v151 offset:64\n\t"
        "v_mfma_f32_16x16x32_bf16 v[52:55], a[244:247], v[108:111], v[52:55]\n\t"
        "v_mfma_f32_16x16x32_bf16 v[56:59], a[248:251], v[108:111], v[56:59]\n\t"
        "v_mfma_f32_16x16x32_bf16 v[48:51], v[208:211], v[112:115], v[48:51]\n\t"
        "s_waitcnt lgkmcnt(3)\n\t"
        "buffer_store_dwordx4 v[124:127], v145, s[28:31], s50 offen sc1\n\t"
        "v_mfma_f32_16x16x32_bf16 v[52:55], v[212:215], v[112:115], v[52:55]\n\t"
        "ds_read_b128 v[124:127], v147 offset:4096\n\t"
        "s_add_u32 s50, s50, 86016\n\t"
        "v_mfma_f32_16x16x32_bf16 v[56:59], v[216:219], v[112:115], v[56:59]\n\t"
        "ds_read_b128 v[108:111], v151 offset:128\n\t"
        "v_add_u32 v151, s9, v175\n\t"
        "v_mfma_f32_16x16x32_bf16 v[48:51], v[244:247], v[84:87], v[48:51]\n\t"
        "ds_read_b128 v[112:115], v183 offset:0\n\t"
        "v_max_u32 v151, v151, v135\n\t"
        "v_mfma_f32_16x16x32_bf16 v[52:55], v[248:251], v[84:87], v[52:55]\n\t"
        "v_xor_b32 v136, v181, v136\n\t"
        "v_mfma_f32_16x16x32_bf16 v[56:59], v[252:255], v[84:87], v[56:59]\n\t"
        "v_mfma_f32_16x16x32_bf16 v[72:75], a[12:15], v[88:91], 0\n\t"
        "ds_read_b128 v[84:87], v183 offset:64\n\t"
        "v_mfma_f32_16x16x32_bf16 v[76:79], a[16:19], v[88:91], 0\n\t"
        "v_mfma_f32_16x16x32_bf16 v[80:83], a[20:23], v[88:91], 0\n\t"
        "v_mfma_f32_16x16x32_bf16 v[72:75], a[48:51], v[92:95], v[72:75]\n\t"
        "ds_read_b128 v[88:91], v183 offset:128\n\t"
        "v_add_u32 v183, s9, v176\n\t"
        "v_mfma_f32_16x16x32_bf16 v[76:79], a[52:55], v[92:95], v[76:79]\n\t"
        "v_max_u32 v183, v183, v136\n\t"
        "v_xor_b32 v137, v181, v137\n\t"
        "v_mfma_f32_16x16x32_bf16 v[80:83], a[56:59], v[92:95], v[80:83]\n\t"
        "s_waitcnt lgkmcnt(4)\n\t"
        "v_mfma_f32_16x16x32_bf16 v[72:75], a[84:87], v[96:99], v[72:75]\n\t"
        "ds_read_b128 v[92:95], v151 offset:0\n\t"
        "v_mfma_f32_16x16x32_bf16 v[76:79], a[88:91], v[96:99], v[76:79]\n\t"
        "v_mfma_f32_16x16x32_bf16 v[80:83], a[92:95], v[96:99], v[80:83]\n\t"
        "buffer_store_dwordx4 v[124:127], v145, s[28:31], s50 offen sc1\n\t"
        "v_mfma_f32_16x16x32_bf16 v[72:75], a[120:123], v[100:103], v[72:75]\n\t"
        "ds_read_b128 v[96:99], v151 offset:64\n\t"
        "v_readlane_b32 s55, v155, 49\n\t"
        "v_mfma_f32_16x16x32_bf16 v[76:79], a[124:127], v[100:103], v[76:79]\n\t"
        "v_cvt_pk_bf16_f32 v116, v48, v49\n\t"
        "v_cvt_pk_bf16_f32 v117, v50, v51\n\t"
        "v_mfma_f32_16x16x32_bf16 v[80:83], a[128:131], v[100:103], v[80:83]\n\t"
        "v_cvt_pk_bf16_f32 v118, v52, v53\n\t"
        "v_cvt_pk_bf16_f32 v119, v54, v55\n\t"
        "v_mfma_f32_16x16x32_bf16 v[72:75], a[156:159], v[104:107], v[72:75]\n\t"
        "ds_read_b128 v[100:103], v151 offset:128\n\t"
        "v_add_u32 v151, s9, v177\n\t"
        "v_mfma_f32_16x16x32_bf16 v[76:79], a[160:163], v[104:107], v[76:79]\n\t"
        "v_max_u32 v151, v151, v137\n\t"
        "v_xor_b32 v138, v181, v138\n\t"
        "v_mfma_f32_16x16x32_bf16 v[80:83], a[164:167], v[104:107], v[80:83]\n\t"
        "v_cvt_pk_bf16_f32 v120, v56, v57\n\t"
        "v_cvt_pk_bf16_f32 v121, v58, v59\n\t"
        "s_waitcnt lgkmcnt(3)\n\t"
        "v_mfma_f32_16x16x32_bf16 v[72:75], a[192:195], v[108:111], v[72:75]\n\t"
        "v_permlane16_swap_b32 v116, v118\n\t"
        "v_mfma_f32_16x16x32_bf16 v[76:79], a[196:199], v[108:111], v[76:79]\n\t"
        "v_permlane16_swap_b32 v117, v119\n\t"
        "v_mfma_f32_16x16x32_bf16 v[80:83], a[200:203], v[108:111], v[80:83]\n\t"
        "ds_read_b128 v[104:107], v183 offset:0\n\t"
        "v_mfma_f32_16x16x32_bf16 v[72:75], a[228:231], v[112:115], v[72:75]\n\t"
        "ds_read_b128 v[108:111], v183 offset:64\n\t"
        "v_mfma_f32_16x16x32_bf16 v[76:79], a[232:235], v[112:115], v[76:79]\n\t"
        "v_mfma_f32_16x16x32_bf16 v[80:83], a[236:239], v[112:115], v[80:83]\n\t"
        "v_mfma_f32_16x16x32_bf16 v[72:75], v[196:199], v[84:87], v[72:75]\n\t"
        "ds_read_b128 v[112:115], v183 offset:128\n\t"
        "v_add_u32 v183, s9, v178\n\t"
        "v_mfma_f32_16x16x32_bf16 v[76:79], v[200:203], v[84:87], v[76:79]\n\t"
        "buffer_store_dwordx4 v[116:119], v148, s[28:31], s55 offen sc1\n\t"
        "v_mfma_f32_16x16x32_bf16 v[80:83], v[204:207], v[84:87], v[80:83]\n\t"
        "ds_write_b64 v146, v[120:121] offset:8192\n\t"
        "v_mfma_f32_16x16x32_bf16 v[72:75], v[232:235], v[88:91], v[72:75]\n\t"
        "ds_read_b128 v[84:87], v151 offset:0\n\t"
        "v_max_u32 v183, v183, v138\n\t"
        "v_mfma_f32_16x16x32_bf16 v[76:79], v[236:239], v[88:91], v[76:79]\n\t"
        "v_xor_b32 v139, v181, v139\n\t"
        "v_mfma_f32_16x16x32_bf16 v[80:83], v[240:243], v[88:91], v[80:83]\n\t"
        "s_waitcnt lgkmcnt(4)\n\t"
        "v_mfma_f32_16x16x32_bf16 v[72:75], a[0:3], v[92:95], v[72:75]\n\t"
        "ds_read_b128 v[88:91], v151 offset:64\n\t"
        "v_mfma_f32_16x16x32_bf16 v[76:79], a[4:7], v[92:95], v[76:79]\n\t"
        "v_mfma_f32_16x16x32_bf16 v[80:83], a[8:11], v[92:95], v[80:83]\n\t"
        "v_mfma_f32_16x16x32_bf16 v[72:75], a[36:39], v[96:99], v[72:75]\n\t"
        "ds_read_b128 v[92:95], v151 offset:128\n\t"
        "v_add_u32 v151, s9, v179\n\t"
        "v_mfma_f32_16x16x32_bf16 v[76:79], a[40:43], v[96:99], v[76:79]\n\t"
        "v_max_u32 v151, v151, v139\n\t"
        "v_xor_b32 v140, v181, v140\n\t"
        "v_mfma_f32_16x16x32_bf16 v[80:83], a[44:47], v[96:99], v[80:83]\n\t"
        "v_mfma_f32_16x16x32_bf16 v[72:75], a[72:75], v[100:103], v[72:75]\n\t"
        "ds_read_b128 v[96:99], v183 offset:0\n\t"
        "v_mfma_f32_16x16x32_bf16 v[76:79], a[76:79], v[100:103], v[76:79]\n\t"
        "v_mfma_f32_16x16x32_bf16 v[80:83], a[80:83], v[100:103], v[80:83]\n\t"
        "v_mfma_f32_16x16x32_bf16 v[72:75], a[108:111], v[104:107], v[72:75]\n\t"
        "ds_read_b128 v[100:103], v183 offset:64\n\t"
        "v_mfma_f32_16x16x32_bf16 v[76:79], a[112:115], v[104:107], v[76:79]\n\t"
        "v_mfma_f32_16x16x32_bf16 v[80:83], a[116:119], v[104:107], v[80:83]\n\t"
        "ds_read_b128 v[104:107], v183 offset:128\n\t"
        "v_add_u32 v183, s9, v180\n\t"
        "s_waitcnt lgkmcnt(4)\n\t"
        "v_mfma_f32_16x16x32_bf16 v[72:75], a[144:147], v[108:111], v[72:75]\n\t"
        "v_max_u32 v183, v183, v140\n\t"
        "v_mfma_f32_16x16x32_bf16 v[76:79], a[148:151], v[108:111], v[76:79]\n\t"
        "v_mfma_f32_16x16x32_bf16 v[80:83], a[152:155], v[108:111], v[80:83]\n\t"
        "ds_read_b128 v[108:111], v151 offset:0\n\t"
        "v_mfma_f32_16x16x32_bf16 v[72:75], a[180:183], v[112:115], v[72:75]\n\t"
        "v_mfma_f32_16x16x32_bf16 v[76:79], a[184:187], v[112:115], v[76:79]\n\t"
        "v_mfma_f32_16x16x32_bf16 v[80:83], a[188:191], v[112:115], v[80:83]\n\t"
        "ds_read_b128 v[112:115], v151 offset:64\n\t"
        "v_mfma_f32_16x16x32_bf16 v[72:75], a[216:219], v[84:87], v[72:75]\n\t"
        "v_mfma_f32_16x16x32_bf16 v[76:79], a[220:223], v[84:87], v[76:79]\n\t"
        "v_mfma_f32_16x16x32_bf16 v[80:83], a[224:227], v[84:87], v[80:83]\n\t"
        "ds_read_b128 v[84:87], v151 offset:128\n\t"
        "v_mfma_f32_16x16x32_bf16 v[72:75], a[252:255], v[88:91], v[72:75]\n\t"
        "v_mfma_f32_16x16x32_bf16 v[76:79], v[188:191], v[88:91], v[76:79]\n\t"
        "v_mfma_f32_16x16x32_bf16 v[80:83], v[192:195], v[88:91], v[80:83]\n\t"
        "ds_read_b128 v[88:91], v183 offset:0\n\t"
        "s_waitcnt lgkmcnt(4)\n\t"
        "v_mfma_f32_16x16x32_bf16 v[72:75], v[220:223], v[92:95], v[72:75]\n\t"
        "v_mfma_f32_16x16x32_bf16 v[76:79], v[224:227], v[92:95], v[76:79]\n\t"
        "v_mfma_f32_16x16x32_bf16 v[80:83], v[228:231], v[92:95], v[80:83]\n\t"
        "ds_read_b128 v[92:95], v183 offset:64\n\t"
        "v_mfma_f32_16x16x32_bf16 v[72:75], a[24:27], v[96:99], v[72:75]\n\t"
        "v_mfma_f32_16x16x32_bf16 v[76:79], a[28:31], v[96:99], v[76:79]\n\t"
        "v_mfma_f32_16x16x32_bf16 v[80:83], a[32:35], v[96:99], v[80:83]\n\t"
        "ds_read_b128 v[96:99], v183 offset:128\n\t"
        "v_mfma_f32_16x16x32_bf16 v[80:83], a[68:71], v[100:103], v[80:83]\n\t"
        "v_mfma_f32_16x16x32_bf16 v[80:83], a[104:107], v[104:107], v[80:83]\n\t"
        "s_waitcnt lgkmcnt(3)\n\t"
        "v_mfma_f32_16x16x32_bf16 v[80:83], a[140:143], v[108:111], v[80:83]\n\t"
        "v_mfma_f32_16x16x32_bf16 v[80:83], a[176:179], v[112:115], v[80:83]\n\t"
        "v_mfma_f32_16x16x32_bf16 v[80:83], a[212:215], v[84:87], v[80:83]\n\t"
        "s_waitcnt lgkmcnt(2)\n\t"
        "v_mfma_f32_16x16x32_bf16 v[80:83], a[248:251], v[88:91], v[80:83]\n\t"
        "s_waitcnt lgkmcnt(1)\n\t"
        "v_mfma_f32_16x16x32_bf16 v[80:83], v[216:219], v[92:95], v[80:83]\n\t"
        "s_waitcnt lgkmcnt(0)\n\t"
        "v_mfma_f32_16x16x32_bf16 v[80:83], v[252:255], v[96:99], v[80:83]\n\t"
        "v_mfma_f32_16x16x32_bf16 v[72:75], a[60:63], v[100:103], v[72:75]\n\t"
        "v_mfma_f32_16x16x32_bf16 v[76:79], a[64:67], v[100:103], v[76:79]\n\t"
        "s_nop 5\n\t"
        "v_cvt_pk_bf16_f32 v120, v80, v81\n\t"
        "v_cvt_pk_bf16_f32 v121, v82, v83\n\t"
        "v_mfma_f32_16x16x32_bf16 v[72:75], a[96:99], v[104:107], v[72:75]\n\t"
        "ds_write_b64 v146, v[120:121] offset:10240\n\t"
        "v_mfma_f32_16x16x32_bf16 v[76:79], a[100:103], v[104:107], v[76:79]\n\t"
        "v_mfma_f32_16x16x32_bf16 v[72:75], a[132:135], v[108:111], v[72:75]\n\t"
        "v_mfma_f32_16x16x32_bf16 v[76:79], a[136:139], v[108:111], v[76:79]\n\t"
        "s_waitcnt lgkmcnt(0)\n\t"
        "s_barrier\n\t"
        "v_mfma_f32_16x16x32_bf16 v[72:75], a[168:171], v[112:115], v[72:75]\n\t"
        "ds_read_b128 v[124:127], v147 offset:8192\n\t"
        "v_readlane_b32 s55, v155, 51\n\t"
        "v_mfma_f32_16x16x32_bf16 v[76:79], a[172:175], v[112:115], v[76:79]\n\t"
        "v_mfma_f32_16x16x32_bf16 v[72:75], a[204:207], v[84:87], v[72:75]\n\t"
        "v_mfma_f32_16x16x32_bf16 v[76:79], a[208:211], v[84:87], v[76:79]\n\t"
        "v_mfma_f32_16x16x32_bf16 v[72:75], a[240:243], v[88:91], v[72:75]\n\t"
        "v_mfma_f32_16x16x32_bf16 v[76:79], a[244:247], v[88:91], v[76:79]\n\t"
        "v_mfma_f32_16x16x32_bf16 v[72:75], v[208:211], v[92:95], v[72:75]\n\t"
        "v_mfma_f32_16x16x32_bf16 v[76:79], v[212:215], v[92:95], v[76:79]\n\t"
        "v_mfma_f32_16x16x32_bf16 v[72:75], v[244:247], v[96:99], v[72:75]\n\t"
        "v_mfma_f32_16x16x32_bf16 v[76:79], v[248:251], v[96:99], v[76:79]\n\t"
        "s_nop 6\n\t"
        "v_cvt_pk_bf16_f32 v116, v72, v73\n\t"
        "v_cvt_pk_bf16_f32 v117, v74, v75\n\t"
        "v_cvt_pk_bf16_f32 v118, v76, v77\n\t"
        "v_cvt_pk_bf16_f32 v119, v78, v79\n\t"
        "s_nop 0\n\t"
        "v_permlane16_swap_b32 v116, v118\n\t"
        "v_permlane16_swap_b32 v117, v119\n\t"
        "buffer_store_dwordx4 v[116:119], v148, s[28:31], s55 offen  \n\t"
        "s_waitcnt lgkmcnt(0)\n\t"
        "buffer_store_dwordx4 v[124:127], v150, s[28:31], s52 offen  \n\t"
        "s_andn2_b32 s6, s6, 2\n\t"
        "s_cbranch_scc1 99f\n\t"
        "s_endpgm\n\t"
        "20:\n\t"
        "s_mul_i32 s69, s68, 16384\n\t"
        "buffer_load_dwordx4 v142, s[24:27], s36 offen offset:2048 lds\n\t"
        "s_lshl_b32 s70, s49, 7\n\t"
        "s_lshl_b32 s71, s49, 5\n\t"
        "s_waitcnt vmcnt(19)\n\t"
        "v_add_u32 v128, s69, v128\n\t"
        "v_add_u32 v129, s69, v129\n\t"
        "v_add_u32 v130, s69, v130\n\t"
        "v_xad_u32 v133, v131, s71, 64\n\t"
        "v_add_u32 v131, s70, v131\n\t"
        "v_xor_b32 v132, 32, v131\n\t"
        "v_subrev_u32 v138, 43008, v131\n\t"
        "s_add_u32 m0, s35, 4096\n\t"
        "s_add_u32 s43, s34, 43008\n\t"
        "buffer_load_dwordx4 v143, s[16:19], s43 offen  lds\n\t"
        "v_subrev_u32 v139, 43008, v132\n\t"
        "v_subrev_u32 v149, 43008, v133\n\t"
        "s_waitcnt vmcnt(18)\n\t"
        "ds_bpermute_b32 v67, v81, v155 offset:172\n\t"
        "ds_bpermute_b32 v78, v81, v155 offset:148\n\t"
        "ds_bpermute_b32 v79, v81, v155 offset:160\n\t"
        "v_readlane_b32 s69, v155, 34\n\t"
        "v_readlane_b32 s70, v155, 35\n\t"
        "v_readlane_b32 s46, v155, 43\n\t"
        "v_readlane_b32 s47, v155, 44\n\t"
        "s_add_u32 m0, s35, 20480\n\t"
        "s_add_u32 s43, s34, 215040\n\t"
        "buffer_load_dwordx4 v143, s[16:19], s43 offen  lds\n\t"
        "v_readlane_b32 s55, v155, 45\n\t"
        "s_lshl_b32 s71, s49, 3\n\t"
        "v_add_u32 v82, s71, v82\n\t"
        "v_add_u32 v53, v70, v83\n\t"
        "v_mad_u32_u24 v70, v52, s69, v70\n\t"
        "v_mad_u32_u24 v70, v158, s70, v70\n\t"
        "s_mul_i32 s71, s69, s68\n\t"
        "s_waitcnt lgkmcnt(2)\n\t"
        "v_mad_u32_u24 v67, v82, s70, v67\n\t"
        "v_add_u32 v67, s71, v67\n\t"
        "v_readlane_b32 s11, v155, 30\n\t"
        "s_movk_i32 s10, 223\n\t"
        "v_mov_b32 v75, 0x7ff00000\n\t"
        "v_readlane_b32 s12, v155, 24\n\t"
        "v_readlane_b32 s13, v155, 25\n\t"
        "v_readlane_b32 s70, v155, 27\n\t"
        "s_nop 1\n\t"
        "s_or_b32 s42, s42, s70\n\t"
        "s_bfe_u32 s70, s12, 524288\n\t"
        "s_bfe_u32 s71, s13, 524288\n\t"
        "v_cmp_gt_u32 vcc, s71, v71\n\t"
        "v_add3_u32 v73, v71, s70, -2\n\t"
        "v_med3_i32 v73, v73, 0, s10\n\t"
        "v_cndmask_b32 v74, v75, v72, vcc\n\t"
        "v_mad_u32_u24 v134, v73, s11, v74\n\t"
        "s_bfe_u32 s70, s12, 524296\n\t"
        "s_bfe_u32 s71, s13, 524296\n\t"
        "v_cmp_gt_u32 vcc, s71, v71\n\t"
        "v_add3_u32 v73, v71, s70, -2\n\t"
        "v_med3_i32 v73, v73, 0, s10\n\t"
        "v_cndmask_b32 v74, v75, v72, vcc\n\t"
        "v_mad_u32_u24 v135, v73, s11, v74\n\t"
        "s_bfe_u32 s70, s12, 524304\n\t"
        "s_bfe_u32 s71, s13, 524304\n\t"
        "v_cmp_gt_u32 vcc, s71, v71\n\t"
        "v_add3_u32 v73, v71, s70, -2\n\t"
        "v_med3_i32 v73, v73, 0, s10\n\t"
        "v_cndmask_b32 v74, v75, v72, vcc\n\t"
        "v_mad_u32_u24 v136, v73, s11, v74\n\t"
        "s_bfe_u32 s70, s12, 524312\n\t"
        "s_bfe_u32 s71, s13, 524312\n\t"
        "v_cmp_gt_u32 vcc, s71, v71\n\t"
        "v_add3_u32 v73, v71, s70, -2\n\t"
        "v_med3_i32 v73, v73, 0, s10\n\t"
        "v_cndmask_b32 v74, v75, v72, vcc\n\t"
        "v_mad_u32_u24 v137, v73, s11, v74\n\t"
        "v_readlane_b32 s44, v155, 32\n\t"
        "v_readlane_b32 s45, v155, 33\n\t"
        "v_readlane_b32 s62, v155, 36\n\t"
        "s_mov_b32 exec_lo, 0\n\t"
        "s_mov_b32 exec_hi, 0xfff00000\n\t"
        "s_waitcnt vmcnt(18)\n\t"
        "ds_write_b32 v119, v155\n\t"
        "s_mov_b64 exec, -1\n\t"
        "s_lshl_b32 s70, s62, 5\n\t"
        "s_sub_u32 s71, s70, 16384\n\t"
        "s_mul_i32 s71, s71, s68\n\t"
        "v_add_u32 v172, s71, v128\n\t"
        "v_add_u32 v173, s71, v129\n\t"
        "v_add_u32 v174, s71, v130\n\t"
        "s_add_u32 s71, s71, s70\n\t"
        "v_add_u32 v175, s71, v128\n\t"
        "v_add_u32 v176, s71, v129\n\t"
        "v_add_u32 v177, s71, v130\n\t"
        "s_add_u32 s71, s71, s70\n\t"
        "v_add_u32 v178, s71, v128\n\t"
        "v_add_u32 v179, s71, v129\n\t"
        "v_add_u32 v180, s71, v130\n\t"
        "s_cmp_eq_u32 s69, 36\n\t"
        "s_cselect_b64 exec, -1, 0\n\t"
        "v_swap_b32 v68, v69\n\t"
        "v_swap_b32 v173, v175\n\t"
        "v_swap_b32 v174, v178\n\t"
        "v_swap_b32 v177, v179\n\t"
        "s_mov_b64 exec, -1\n\t"
        "v_add3_u32 v68, v68, v52, -1\n\t"
        "v_add3_u32 v69, v69, v158, -1\n\t"
        "v_cvt_f32_i32 v68, v68\n\t"
        "v_cvt_f32_i32 v69, v69\n\t"
        "s_add_u32 s70, s68, 1\n\t"
        "v_cvt_f32_u32 v73, s70\n\t"
        "s_waitcnt lgkmcnt(1)\n\t"
        "v_add_f32 v78, v78, v73\n\t"
        "v_add_u32 v73, 1, v82\n\t"
        "v_cvt_f32_u32 v73, v73\n\t"
        "v_add_f32 v79, v79, v73\n\t"
        "s_mul_i32 s70, s4, 384\n\t"
        "v_add_u32 v80, s70, v80\n\t"
        "s_mov_b64 exec, 0\n\t"
        "s_cmp_eq_u32 s4, 0\n\t"
        "s_cbranch_scc1 .LpS0%=\n\t"
        "s_cmp_eq_u32 s4, 1\n\t"
        "s_cbranch_scc1 .LpS1%=\n\t"
        "s_cmp_eq_u32 s4, 2\n\t"
        "s_cbranch_scc1 .LpS2%=\n\t"
        ".LpS2%=:\n\t"
        ".LrS%=:\n\t"
        "s_mov_b64 exec, -1\n\t"
        "s_waitcnt vmcnt(9) lgkmcnt(0)\n\t"
        "s_barrier\n\t"
        "ds_read_b64_tr_b16 a[0:1], v138 offset:6144\n\t"
        "ds_read_b64_tr_b16 a[2:3], v138 offset:6912\n\t"
        "ds_read_b64_tr_b16 a[4:5], v139 offset:6144\n\t"
        "ds_read_b64_tr_b16 a[6:7], v139 offset:6912\n\t"
        "ds_read_b64_tr_b16 a[8:9], v149 offset:6144\n\t"
        "ds_read_b64_tr_b16 a[10:11], v149 offset:6912\n\t"
        "ds_read_b128 v[84:87], v128 offset:8192\n\t"
        "ds_read_b64_tr_b16 a[12:13], v138 offset:12288\n\t"
        "ds_read_b64_tr_b16 a[14:15], v138 offset:13056\n\t"
        "ds_read_b64_tr_b16 a[16:17], v139 offset:12288\n\t"
        "ds_read_b64_tr_b16 a[18:19], v139 offset:13056\n\t"
        "ds_read_b64_tr_b16 a[20:21], v149 offset:12288\n\t"
        "ds_read_b64_tr_b16 a[22:23], v149 offset:13056\n\t"
        "ds_read_b128 v[88:91], v128 offset:8192\n\t"
        "s_add_u32 m0, s37, -12288\n\t"
        "s_add_u32 s36, s36, s39\n\t"
        "s_waitcnt lgkmcnt(7)\n\t"
        "v_mfma_f32_16x16x32_bf16 v[12:15], a[0:3], v[84:87], 0\n\t"
        "buffer_load_dwordx4 v140, s[24:27], s36 offen offset:0 lds\n\t"
        "buffer_load_dwordx4 v141, s[24:27], s36 offen offset:1024 lds\n\t"
        "buffer_load_dwordx4 v142, s[24:27], s36 offen offset:2048 lds\n\t"
        "s_add_u32 m0, s35, 16384\n\t"
        "s_add_u32 s43, s34, 172032\n\t"
        "buffer_load_dwordx4 v143, s[16:19], s43 offen  lds\n\t"
        "s_add_u32 m0, s35, 32768\n\t"
        "s_add_u32 s43, s34, 344064\n\t"
        "s_min_u32 s43, s43, 9590784\n\t"
        "buffer_load_dwordx4 v143, s[16:19], s43 offen  lds\n\t"
        "s_waitcnt vmcnt(10)\n\t"
        "s_barrier\n\t"
        "ds_read_b128 v[92:95], v128 offset:12288\n\t"
        "s_add_u32 m0, s37, 0\n\t"
        "ds_read_b64_tr_b16 a[24:25], v138 offset:0\n\t"
        "s_add_u32 s36, s36, s38\n\t"
        "v_mfma_f32_16x16x32_bf16 v[16:19], a[4:7], v[84:87], 0\n\t"
        "buffer_load_dwordx4 v140, s[24:27], s36 offen offset:0 lds\n\t"
        "ds_read_b64_tr_b16 a[26:27], v138 offset:768\n\t"
        "v_lshrrev_b32 v73, 5, v156\n\t"
        "v_mfma_f32_16x16x32_bf16 v[20:23], a[8:11], v[84:87], 0\n\t"
        "buffer_load_dwordx4 v141, s[24:27], s36 offen offset:1024 lds\n\t"
        "ds_read_b64_tr_b16 a[28:29], v139 offset:0\n\t"
        "v_mul_u32_u24 v73, 8064, v73\n\t"
        "s_waitcnt lgkmcnt(4)\n\t"
        "v_mfma_f32_16x16x32_bf16 v[24:27], a[12:15], v[88:91], 0\n\t"
        "ds_read_b64_tr_b16 a[30:31], v139 offset:768\n\t"
        "ds_read_b64_tr_b16 a[32:33], v149 offset:0\n\t"
        "ds_read_b64_tr_b16 a[34:35], v149 offset:768\n\t"
        "v_and_b32 v55, 496, v140\n\t"
        "v_mfma_f32_16x16x32_bf16 v[28:31], a[16:19], v[88:91], 0\n\t"
        "buffer_load_dwordx4 v142, s[24:27], s36 offen offset:2048 lds\n\t"
        "ds_read_b128 v[96:99], v128 offset:8192\n\t"
        "v_add_u32 v55, v55, v73\n\t"
        "v_mfma_f32_16x16x32_bf16 v[32:35], a[20:23], v[88:91], 0\n\t"
        "ds_read_b128 v[100:103], v128 offset:12288\n\t"
        "v_lshl_add_u32 v61, v158, 2, v73\n\t"
        "s_waitcnt lgkmcnt(8)\n\t"
        "v_mfma_f32_16x16x32_bf16 v[24:27], a[0:3], v[92:95], v[24:27]\n\t"
        "s_mul_i32 s48, s41, 36\n\t"
        "v_readlane_b32 s69, v155, 31\n\t"
        "v_mfma_f32_16x16x32_bf16 v[28:31], a[4:7], v[92:95], v[28:31]\n\t"
        "buffer_load_dword v67, v67, s[20:23], 0 offen offset:32\n\t"
        "v_mov_b32 v154, s69\n\t"
        "v_mfma_f32_16x16x32_bf16 v[32:35], a[8:11], v[92:95], v[32:35]\n\t"
        "v_mov_b32 v119, s62\n\t"
        "v_add_u32 v117, s37, v117\n\t"
        "v_mfma_f32_16x16x32_bf16 v[36:39], a[12:15], v[92:95], 0\n\t"
        "v_add_u32 v118, s35, v118\n\t"
        "v_add_u32 v185, s35, v185\n\t"
        "v_mfma_f32_16x16x32_bf16 v[40:43], a[16:19], v[92:95], 0\n\t"
        "v_add_u32 v186, s37, v186\n\t"
        "v_readlane_b32 s53, v155, 28\n\t"
        "v_mfma_f32_16x16x32_bf16 v[44:47], a[20:23], v[92:95], 0\n\t"
        "s_waitcnt vmcnt(9) lgkmcnt(2)\n\t"
        "s_barrier\n\t"
        "buffer_load_dword v64, v70, s[20:23], s46 offen\n\t"
        "ds_read_b128 v[104:107], v128 offset:4096\n\t"
        "s_waitcnt lgkmcnt(2)\n\t"
        "v_mfma_f32_16x16x32_bf16 v[0:3], a[24:27], v[96:99], 0\n\t"
        "ds_read_b64_tr_b16 a[36:37], v138 offset:18432\n\t"
        "ds_read_b64_tr_b16 a[38:39], v138 offset:19200\n\t"
        "s_lshl_b32 s69, s68, 10\n\t"
        "v_mfma_f32_16x16x32_bf16 v[4:7], a[28:31], v[96:99], 0\n\t"
        "buffer_load_dword v65, v70, s[20:23], s47 offen\n\t"
        "ds_read_b64_tr_b16 a[40:41], v139 offset:18432\n\t"
        "s_add_u32 s69, s69, 135168\n\t"
        "v_mfma_f32_16x16x32_bf16 v[8:11], a[32:35], v[96:99], 0\n\t"
        "ds_read_b64_tr_b16 a[42:43], v139 offset:19200\n\t"
        "ds_read_b64_tr_b16 a[44:45], v149 offset:18432\n\t"
        "s_lshl_b32 s70, s49, 5\n\t"
        "s_waitcnt lgkmcnt(6)\n\t"
        "v_mfma_f32_16x16x32_bf16 v[12:15], a[24:27], v[100:103], v[12:15]\n\t"
        "ds_read_b64_tr_b16 a[46:47], v149 offset:19200\n\t"
        "ds_read_b64_tr_b16 a[48:49], v138 offset:24576\n\t"
        "ds_read_b64_tr_b16 a[50:51], v138 offset:25344\n\t"
        "v_xor_b32 v146, s70, v146\n\t"
        "v_mfma_f32_16x16x32_bf16 v[16:19], a[28:31], v[100:103], v[16:19]\n\t"
        "buffer_load_dword v66, v70, s[20:23], s55 offen\n\t"
        "s_add_u32 m0, s37, 12288\n\t"
        "ds_read_b64_tr_b16 a[52:53], v139 offset:24576\n\t"
        "s_add_u32 s36, s36, s5\n\t"
        "ds_read_b64_tr_b16 a[54:55], v139 offset:25344\n\t"
        "v_mfma_f32_16x16x32_bf16 v[20:23], a[32:35], v[100:103], v[20:23]\n\t"
        "ds_read_b64_tr_b16 a[56:57], v149 offset:24576\n\t"
        "ds_read_b64_tr_b16 a[58:59], v149 offset:25344\n\t"
        "ds_read_b128 v[108:111], v128 offset:4160\n\t"
        "v_add_u32 v146, s69, v146\n\t"
        "s_waitcnt lgkmcnt(12)\n\t"
        "v_mfma_f32_16x16x32_bf16 v[0:3], a[0:3], v[104:107], v[0:3]\n\t"
        "s_lshl_b32 s70, s49, 11\n\t"
        "s_add_u32 s70, s70, s69\n\t"
        "v_mfma_f32_16x16x32_bf16 v[4:7], a[4:7], v[104:107], v[4:7]\n\t"
        "buffer_load_dwordx4 v140, s[24:27], s36 offen offset:0 lds\n\t"
        "v_add_u32 v147, s70, v147\n\t"
        "v_mfma_f32_16x16x32_bf16 v[8:11], a[8:11], v[104:107], v[8:11]\n\t"
        "s_mul_i32 s41, s41, 192\n\t"
        "s_mul_i32 s69, s68, 172032\n\t"
        "v_mfma_f32_16x16x32_bf16 v[12:15], a[12:15], v[104:107], v[12:15]\n\t"
        "s_add_u32 s41, s41, s69\n\t"
        "s_mul_i32 s69, s49, 43008\n\t"
        "v_mfma_f32_16x16x32_bf16 v[16:19], a[16:19], v[104:107], v[16:19]\n\t"
        "s_add_u32 s50, s41, s69\n\t"
        "s_lshl_b32 s70, s49, 7\n\t"
        "v_mfma_f32_16x16x32_bf16 v[20:23], a[20:23], v[104:107], v[20:23]\n\t"
        "v_add_u32 v144, s70, v144\n\t"
        "s_mul_i32 s69, s68, 768\n\t"
        "s_waitcnt lgkmcnt(0)\n\t"
        "v_mfma_f32_16x16x32_bf16 v[0:3], a[36:39], v[108:111], v[0:3]\n\t"
        "buffer_load_dwordx4 v141, s[24:27], s36 offen offset:1024 lds\n\t"
        "ds_read_b128 v[112:115], v128 offset:8256\n\t"
        "v_add_u32 v187, s69, v187\n\t"
        "v_mfma_f32_16x16x32_bf16 v[4:7], a[40:43], v[108:111], v[4:7]\n\t"
        "v_readlane_b32 s9, v155, 46\n\t"
        "s_sub_u32 s10, 43200, s11\n\t"
        "v_mfma_f32_16x16x32_bf16 v[8:11], a[44:47], v[108:111], v[8:11]\n\t"
        "s_mul_i32 s10, s10, s68\n\t"
        "s_sub_u32 s11, s11, 192\n\t"
        "v_mfma_f32_16x16x32_bf16 v[12:15], a[48:51], v[108:111], v[12:15]\n\t"
        "v_mad_u32_u24 v148, v158, s11, v144\n\t"
        "v_add_u32 v148, s10, v148\n\t"
        "v_mfma_f32_16x16x32_bf16 v[16:19], a[52:55], v[108:111], v[16:19]\n\t"
        "v_lshrrev_b32 v73, 2, v157\n\t"
        "v_mad_u32_u24 v150, v73, s11, v145\n\t"
        "v_mfma_f32_16x16x32_bf16 v[20:23], a[56:59], v[108:111], v[20:23]\n\t"
        "buffer_load_dwordx4 v142, s[24:27], s36 offen offset:2048 lds\n\t"
        "s_add_u32 m0, s35, 0\n\t"
        "s_add_u32 s43, s34, 0\n\t"
        "s_waitcnt lgkmcnt(0)\n\t"
        "v_mfma_f32_16x16x32_bf16 v[12:15], a[36:39], v[112:115], v[12:15]\n\t"
        "ds_read_b128 v[84:87], v128 offset:12352\n\t"
        "s_lshl_b32 s70, s49, 0\n\t"
        "v_mfma_f32_16x16x32_bf16 v[16:19], a[40:43], v[112:115], v[16:19]\n\t"
        "s_add_u32 s70, s70, 50\n\t"
        "v_readlane_b32 s52, v155, s70\n\t"
        "v_mfma_f32_16x16x32_bf16 v[20:23], a[44:47], v[112:115], v[20:23]\n\t"
        "s_nop 0\n\t"
        "s_add_u32 s52, s52, s10\n\t"
        "v_readlane_b32 s51, v155, 49\n\t"
        "v_mfma_f32_16x16x32_bf16 v[24:27], a[48:51], v[112:115], v[24:27]\n\t"
        "buffer_load_dwordx4 v143, s[16:19], s43 offen  lds\n\t"
        "s_add_u32 m0, s35, 36864\n\t"
        "s_add_u32 s43, s34, 387072\n\t"
        "s_min_u32 s43, s43, 9590784\n\t"
        "v_mfma_f32_16x16x32_bf16 v[28:31], a[52:55], v[112:115], v[28:31]\n\t"
        "s_lshl_b32 s70, s49, 30\n\t"
        "s_add_u32 s70, s70, s10\n\t"
        "v_mfma_f32_16x16x32_bf16 v[32:35], a[56:59], v[112:115], v[32:35]\n\t"
        "buffer_load_dwordx4 v143, s[16:19], s43 offen  lds\n\t"
        "s_waitcnt vmcnt(12)\n\t"
        "s_barrier\n\t"
        "ds_read_b128 v[88:91], v128 offset:16384\n\t"
        "s_add_u32 m0, s37, 24576\n\t"
        "ds_read_b64_tr_b16 a[60:61], v138 offset:30720\n\t"
        "s_add_u32 s36, s36, s39\n\t"
        "s_waitcnt lgkmcnt(2)\n\t"
        "v_mfma_f32_16x16x32_bf16 v[24:27], a[36:39], v[84:87], v[24:27]\n\t"
        "buffer_load_dwordx4 v140, s[24:27], s36 offen offset:0 lds\n\t"
        "ds_read_b64_tr_b16 a[62:63], v138 offset:31488\n\t"
        "s_add_u32 s51, s51, s70\n\t"
        "v_mfma_f32_16x16x32_bf16 v[28:31], a[40:43], v[84:87], v[28:31]\n\t"
        "ds_read_b64_tr_b16 a[64:65], v139 offset:30720\n\t"
        "ds_read_b64_tr_b16 a[66:67], v139 offset:31488\n\t"
        "v_mfma_f32_16x16x32_bf16 v[32:35], a[44:47], v[84:87], v[32:35]\n\t"
        "buffer_load_dwordx4 v141, s[24:27], s36 offen offset:1024 lds\n\t"
        "ds_read_b64_tr_b16 a[68:69], v149 offset:30720\n\t"
        "v_mfma_f32_16x16x32_bf16 v[36:39], a[48:51], v[84:87], v[36:39]\n\t"
        "ds_read_b64_tr_b16 a[70:71], v149 offset:31488\n\t"
        "ds_read_b128 v[92:95], v128 offset:8256\n\t"
        "v_mfma_f32_16x16x32_bf16 v[40:43], a[52:55], v[84:87], v[40:43]\n\t"
        "ds_read_b128 v[96:99], v128 offset:12352\n\t"
        "ds_read_b128 v[100:103], v128 offset:16448\n\t"
        "v_mfma_f32_16x16x32_bf16 v[44:47], a[56:59], v[84:87], v[44:47]\n\t"
        "ds_read_b64_tr_b16 a[72:73], v138 offset:36864\n\t"
        "ds_read_b64_tr_b16 a[74:75], v138 offset:37632\n\t"
        "s_waitcnt lgkmcnt(11)\n\t"
        "v_mfma_f32_16x16x32_bf16 v[24:27], a[24:27], v[88:91], v[24:27]\n\t"
        "buffer_load_dwordx4 v142, s[24:27], s36 offen offset:2048 lds\n\t"
        "ds_read_b64_tr_b16 a[76:77], v139 offset:36864\n\t"
        "v_mfma_f32_16x16x32_bf16 v[28:31], a[28:31], v[88:91], v[28:31]\n\t"
        "ds_read_b64_tr_b16 a[78:79], v139 offset:37632\n\t"
        "ds_read_b64_tr_b16 a[80:81], v149 offset:36864\n\t"
        "v_mfma_f32_16x16x32_bf16 v[32:35], a[32:35], v[88:91], v[32:35]\n\t"
        "ds_read_b64_tr_b16 a[82:83], v149 offset:37632\n\t"
        "v_mfma_f32_16x16x32_bf16 v[36:39], a[0:3], v[88:91], v[36:39]\n\t"
        "v_mfma_f32_16x16x32_bf16 v[40:43], a[4:7], v[88:91], v[40:43]\n\t"
        "buffer_load_dwordx4 v[48:51], v55, s[20:23], s48 offen\n\t"
        "v_mfma_f32_16x16x32_bf16 v[44:47], a[8:11], v[88:91], v[44:47]\n\t"
        "s_waitcnt lgkmcnt(8)\n\t"
        "v_mfma_f32_16x16x32_bf16 v[0:3], a[60:63], v[92:95], v[0:3]\n\t"
        "v_mfma_f32_16x16x32_bf16 v[4:7], a[64:67], v[92:95], v[4:7]\n\t"
        "v_mfma_f32_16x16x32_bf16 v[8:11], a[68:71], v[92:95], v[8:11]\n\t"
        "s_waitcnt lgkmcnt(5)\n\t"
        "v_mfma_f32_16x16x32_bf16 v[12:15], a[60:63], v[96:99], v[12:15]\n\t"
        "v_mfma_f32_16x16x32_bf16 v[16:19], a[64:67], v[96:99], v[16:19]\n\t"
        "v_mfma_f32_16x16x32_bf16 v[20:23], a[68:71], v[96:99], v[20:23]\n\t"
        "v_mfma_f32_16x16x32_bf16 v[24:27], a[60:63], v[100:103], v[24:27]\n\t"
        "ds_read_b128 v[104:107], v128 offset:4224\n\t"
        "v_mfma_f32_16x16x32_bf16 v[28:31], a[64:67], v[100:103], v[28:31]\n\t"
        "buffer_load_dword v61, v61, s[20:23], s48 offen offset:512\n\t"
        "s_add_u32 m0, s37, 36864\n\t"
        "v_mfma_f32_16x16x32_bf16 v[32:35], a[68:71], v[100:103], v[32:35]\n\t"
        "s_add_u32 s36, s36, s38\n\t"
        "v_mfma_f32_16x16x32_bf16 v[36:39], a[36:39], v[100:103], v[36:39]\n\t"
        "ds_read_b128 v[108:111], v128 offset:8320\n\t"
        "v_mfma_f32_16x16x32_bf16 v[40:43], a[40:43], v[100:103], v[40:43]\n\t"
        "v_mfma_f32_16x16x32_bf16 v[44:47], a[44:47], v[100:103], v[44:47]\n\t"
        "s_waitcnt lgkmcnt(1)\n\t"
        "v_mfma_f32_16x16x32_bf16 v[0:3], a[72:75], v[104:107], v[0:3]\n\t"
        "ds_read_b128 v[112:115], v128 offset:12416\n\t"
        "v_mfma_f32_16x16x32_bf16 v[4:7], a[76:79], v[104:107], v[4:7]\n\t"
        "v_mfma_f32_16x16x32_bf16 v[8:11], a[80:83], v[104:107], v[8:11]\n\t"
        "s_waitcnt lgkmcnt(1)\n\t"
        "v_mfma_f32_16x16x32_bf16 v[12:15], a[72:75], v[108:111], v[12:15]\n\t"
        "ds_read_b128 v[84:87], v128 offset:16512\n\t"
        "v_mfma_f32_16x16x32_bf16 v[16:19], a[76:79], v[108:111], v[16:19]\n\t"
        "v_mfma_f32_16x16x32_bf16 v[20:23], a[80:83], v[108:111], v[20:23]\n\t"
        "s_waitcnt vmcnt(14)\n\t"
        "s_barrier\n\t"
        "buffer_load_dwordx4 v140, s[24:27], s36 offen offset:0 lds\n\t"
        "ds_read_b64_tr_b16 a[84:85], v131 offset:0\n\t"
        "ds_read_b64_tr_b16 a[86:87], v131 offset:768\n\t"
        "ds_read_b64_tr_b16 a[88:89], v132 offset:0\n\t"
        "ds_read_b64_tr_b16 a[90:91], v132 offset:768\n\t"
        "ds_read_b64_tr_b16 a[92:93], v133 offset:0\n\t"
        "ds_read_b64_tr_b16 a[94:95], v133 offset:768\n\t"
        "ds_read_b128 v[88:91], v128 offset:4224\n\t"
        "s_waitcnt lgkmcnt(8)\n\t"
        "v_mfma_f32_16x16x32_bf16 v[24:27], a[72:75], v[112:115], v[24:27]\n\t"
        "ds_read_b64_tr_b16 a[96:97], v131 offset:6144\n\t"
        "ds_read_b64_tr_b16 a[98:99], v131 offset:6912\n\t"
        "ds_read_b64_tr_b16 a[100:101], v132 offset:6144\n\t"
        "v_mfma_f32_16x16x32_bf16 v[28:31], a[76:79], v[112:115], v[28:31]\n\t"
        "ds_read_b64_tr_b16 a[102:103], v132 offset:6912\n\t"
        "ds_read_b64_tr_b16 a[104:105], v133 offset:6144\n\t"
        "ds_read_b64_tr_b16 a[106:107], v133 offset:6912\n\t"
        "v_mfma_f32_16x16x32_bf16 v[32:35], a[80:83], v[112:115], v[32:35]\n\t"
        "ds_read_b128 v[92:95], v128 offset:8320\n\t"
        "s_waitcnt lgkmcnt(14)\n\t"
        "v_mfma_f32_16x16x32_bf16 v[36:39], a[72:75], v[84:87], v[36:39]\n\t"
        "buffer_load_dwordx4 v141, s[24:27], s36 offen offset:1024 lds\n\t"
        "v_mfma_f32_16x16x32_bf16 v[40:43], a[76:79], v[84:87], v[40:43]\n\t"
        "ds_read_b128 v[96:99], v128 offset:12416\n\t"
        "v_mfma_f32_16x16x32_bf16 v[44:47], a[80:83], v[84:87], v[44:47]\n\t"
        "ds_read_b128 v[100:103], v128 offset:16512\n\t"
        "s_waitcnt lgkmcnt(9)\n\t"
        "v_mfma_f32_16x16x32_bf16 v[12:15], a[84:87], v[88:91], v[12:15]\n\t"
        "v_mfma_f32_16x16x32_bf16 v[16:19], a[88:91], v[88:91], v[16:19]\n\t"
        "buffer_load_dwordx4 v142, s[24:27], s36 offen offset:2048 lds\n\t"
        "v_readlane_b32 s40, v155, 12\n\t"
        "v_readlane_b32 s43, v155, 0\n\t"
        "v_mfma_f32_16x16x32_bf16 v[20:23], a[92:95], v[88:91], v[20:23]\n\t"
        "s_add_u32 m0, s40, s35\n\t"
        "s_waitcnt lgkmcnt(2)\n\t"
        "v_mfma_f32_16x16x32_bf16 v[0:3], a[96:99], v[92:95], v[0:3]\n\t"
        "v_mfma_f32_16x16x32_bf16 v[4:7], a[100:103], v[92:95], v[4:7]\n\t"
        "v_readlane_b32 s46, v155, 37\n\t"
        "v_readlane_b32 s47, v155, 40\n\t"
        "s_waitcnt vmcnt(15)\n\t"
        "v_perm_b32 v56, v64, v64, s44\n\t"
        "v_mfma_f32_16x16x32_bf16 v[8:11], a[104:107], v[92:95], v[8:11]\n\t"
        "buffer_load_dwordx4 v134, s[16:19], s43 offen  lds\n\t"
        "v_readlane_b32 s40, v155, 13\n\t"
        "v_mfma_f32_16x16x32_bf16 v[24:27], a[84:87], v[92:95], v[24:27]\n\t"
        "v_readlane_b32 s43, v155, 1\n\t"
        "s_add_u32 m0, s40, s35\n\t"
        "v_mfma_f32_16x16x32_bf16 v[28:31], a[88:91], v[92:95], v[28:31]\n\t"
        "v_perm_b32 v57, v64, v64, s45\n\t"
        "v_add_f32 v58, s46, v68\n\t"
        "v_mfma_f32_16x16x32_bf16 v[32:35], a[92:95], v[92:95], v[32:35]\n\t"
        "v_add_f32 v59, s47, v69\n\t"
        "v_add_f32 v56, v56, v58\n\t"
        "s_waitcnt lgkmcnt(0)\n\t"
        "v_mfma_f32_16x16x32_bf16 v[12:15], a[96:99], v[96:99], v[12:15]\n\t"
        "v_add_f32 v57, v57, v59\n\t"
        "v_cvt_pk_bf16_f32 v52, v56, v57\n\t"
        "v_mfma_f32_16x16x32_bf16 v[16:19], a[100:103], v[96:99], v[16:19]\n\t"
        "buffer_load_dwordx4 v135, s[16:19], s43 offen  lds\n\t"
        "s_add_u32 m0, s37, 49152\n\t"
        "v_mfma_f32_16x16x32_bf16 v[20:23], a[104:107], v[96:99], v[20:23]\n\t"
        "s_waitcnt vmcnt(10)\n\t"
        "s_barrier\n\t"
        "ds_read_b128 v[104:107], v128 offset:0\n\t"
        "s_add_u32 s36, s36, s5\n\t"
        "ds_read_b128 v[108:111], v128 offset:64\n\t"
        "v_mfma_f32_16x16x32_bf16 v[36:39], a[84:87], v[96:99], v[36:39]\n\t"
        "ds_read_b128 v[112:115], v128 offset:128\n\t"
        "ds_read_b128 v[84:87], v128 offset:20480\n\t"
        "v_cvt_pk_bf16_f32 v58, v58, v59\n\t"
        "v_mfma_f32_16x16x32_bf16 v[40:43], a[88:91], v[96:99], v[40:43]\n\t"
        "ds_read_b128 v[88:91], v128 offset:20544\n\t"
        "ds_read_b128 v[92:95], v128 offset:20608\n\t"
        "v_cmp_ne_u32 s[64:65], v52, v58\n\t"
        "v_mfma_f32_16x16x32_bf16 v[44:47], a[92:95], v[96:99], v[44:47]\n\t"
        "buffer_load_dwordx4 v140, s[24:27], s36 offen offset:0 lds\n\t"
        "ds_read_b64_tr_b16 a[108:109], v131 offset:12288\n\t"
        "v_mbcnt_lo_u32_b32 v56, s64, 1\n\t"
        "v_mfma_f32_16x16x32_bf16 v[24:27], a[96:99], v[100:103], v[24:27]\n\t"
        "ds_read_b64_tr_b16 a[110:111], v131 offset:13056\n\t"
        "ds_read_b64_tr_b16 a[112:113], v132 offset:12288\n\t"
        "v_mbcnt_hi_u32_b32 v56, s65, v56\n\t"
        "s_bcnt1_i32_b64 s61, s[64:65]\n\t"
        "v_mfma_f32_16x16x32_bf16 v[28:31], a[100:103], v[100:103], v[28:31]\n\t"
        "ds_read_b64_tr_b16 a[114:115], v132 offset:13056\n\t"
        "ds_read_b64_tr_b16 a[116:117], v133 offset:12288\n\t"
        "v_add_u32 v54, s61, 1\n\t"
        "v_mfma_f32_16x16x32_bf16 v[32:35], a[104:107], v[100:103], v[32:35]\n\t"
        "ds_read_b64_tr_b16 a[118:119], v133 offset:13056\n\t"
        "ds_read_b64_tr_b16 a[120:121], v131 offset:18432\n\t"
        "v_lshl_add_u32 v56, v56, 3, s60\n\t"
        "s_waitcnt lgkmcnt(12)\n\t"
        "v_mfma_f32_16x16x32_bf16 v[0:3], a[12:15], v[104:107], v[0:3]\n\t"
        "s_mov_b64 exec, s[64:65]\n\t"
        "ds_write_b64 v56, v[52:53]\n\t"
        "s_mov_b64 exec, -1\n\t"
        "buffer_load_dwordx4 v141, s[24:27], s36 offen offset:1024 lds\n\t"
        "v_mfma_f32_16x16x32_bf16 v[4:7], a[16:19], v[104:107], v[4:7]\n\t"
        "ds_read_b64_tr_b16 a[122:123], v131 offset:19200\n\t"
        "ds_read_b64_tr_b16 a[124:125], v132 offset:18432\n\t"
        "v_swap_b32 v53, v80\n\t"
        "v_mfma_f32_16x16x32_bf16 v[8:11], a[20:23], v[104:107], v[8:11]\n\t"
        "ds_read_b64_tr_b16 a[126:127], v132 offset:19200\n\t"
        "ds_read_b64_tr_b16 a[128:129], v133 offset:18432\n\t"
        "v_perm_b32 v56, v67, v67, s44\n\t"
        "s_waitcnt lgkmcnt(11)\n\t"
        "v_mfma_f32_16x16x32_bf16 v[0:3], a[48:51], v[108:111], v[0:3]\n\t"
        "ds_read_b64_tr_b16 a[130:131], v133 offset:19200\n\t"
        "v_perm_b32 v57, v67, v67, s45\n\t"
        "v_mfma_f32_16x16x32_bf16 v[4:7], a[52:55], v[108:111], v[4:7]\n\t"
        "buffer_load_dwordx4 v142, s[24:27], s36 offen offset:2048 lds\n\t"
        "v_readlane_b32 s40, v155, 14\n\t"
        "v_mfma_f32_16x16x32_bf16 v[8:11], a[56:59], v[108:111], v[8:11]\n\t"
        "ds_read_b128 v[96:99], v129 offset:0\n\t"
        "v_readlane_b32 s43, v155, 2\n\t"
        "v_mfma_f32_16x16x32_bf16 v[0:3], a[84:87], v[112:115], v[0:3]\n\t"
        "ds_read_b128 v[100:103], v129 offset:4096\n\t"
        "s_add_u32 m0, s40, s35\n\t"
        "v_mfma_f32_16x16x32_bf16 v[4:7], a[88:91], v[112:115], v[4:7]\n\t"
        "v_add_f32 v56, v56, v78\n\t"
        "v_add_f32 v57, v57, v79\n\t"
        "v_mfma_f32_16x16x32_bf16 v[8:11], a[92:95], v[112:115], v[8:11]\n\t"
        "buffer_load_dwordx4 v134, s[16:19], s43 offen  lds\n\t"
        "v_readlane_b32 s40, v155, 15\n\t"
        "v_mfma_f32_16x16x32_bf16 v[36:39], a[24:27], v[84:87], v[36:39]\n\t"
        "v_readlane_b32 s43, v155, 3\n\t"
        "s_add_u32 m0, s40, s35\n\t"
        "v_mfma_f32_16x16x32_bf16 v[40:43], a[28:31], v[84:87], v[40:43]\n\t"
        "v_cvt_pk_bf16_f32 v52, v56, v57\n\t"
        "v_cvt_pk_bf16_f32 v58, v78, v79\n\t"
        "v_mfma_f32_16x16x32_bf16 v[44:47], a[32:35], v[84:87], v[44:47]\n\t"
        "v_cmp_ne_u32 s[64:65], v52, v58\n\t"
        "v_readlane_b32 s47, v155, 41\n\t"
        "v_mfma_f32_16x16x32_bf16 v[36:39], a[60:63], v[88:91], v[36:39]\n\t"
        "s_and_b64 s[64:65], s[64:65], 0xffffff\n\t"
        "v_mbcnt_lo_u32_b32 v56, s64, v54\n\t"
        "v_mfma_f32_16x16x32_bf16 v[40:43], a[64:67], v[88:91], v[40:43]\n\t"
        "s_bcnt1_i32_b64 s46, s[64:65]\n\t"
        "s_add_u32 s61, s61, s46\n\t"
        "v_mfma_f32_16x16x32_bf16 v[44:47], a[68:71], v[88:91], v[44:47]\n\t"
        "v_add_u32 v54, s46, v54\n\t"
        "v_lshl_add_u32 v56, v56, 3, s60\n\t"
        "v_mfma_f32_16x16x32_bf16 v[36:39], a[96:99], v[92:95], v[36:39]\n\t"
        "s_mov_b64 exec, s[64:65]\n\t"
        "ds_write_b64 v56, v[52:53]\n\t"
        "s_mov_b64 exec, -1\n\t"
        "v_mfma_f32_16x16x32_bf16 v[40:43], a[100:103], v[92:95], v[40:43]\n\t"
        "buffer_load_dwordx4 v135, s[16:19], s43 offen  lds\n\t"
        "s_add_u32 m0, s37, 0\n\t"
        "v_mfma_f32_16x16x32_bf16 v[44:47], a[104:107], v[92:95], v[44:47]\n\t"
        "s_add_u32 s36, s36, s39\n\t"
        "v_add_u32 v53, 2048, v80\n\t"
        "s_waitcnt lgkmcnt(1)\n\t"
        "v_mfma_f32_16x16x32_bf16 v[0:3], a[120:123], v[96:99], v[0:3]\n\t"
        "v_readlane_b32 s46, v155, 38\n\t"
        "v_perm_b32 v56, v65, v65, s44\n\t"
        "v_mfma_f32_16x16x32_bf16 v[4:7], a[124:127], v[96:99], v[4:7]\n\t"
        "v_perm_b32 v57, v65, v65, s45\n\t"
        "v_add_f32 v58, s46, v68\n\t"
        "v_mfma_f32_16x16x32_bf16 v[8:11], a[128:131], v[96:99], v[8:11]\n\t"
        "v_add_f32 v59, s47, v69\n\t"
        "v_add_f32 v56, v56, v58\n\t"
        "v_mfma_f32_16x16x32_bf16 v[0:3], a[108:111], v[100:103], v[0:3]\n\t"
        "s_waitcnt vmcnt(12)\n\t"
        "s_barrier\n\t"
        "buffer_load_dwordx4 v140, s[24:27], s36 offen offset:0 lds\n\t"
        "ds_read_b128 v[104:107], v129 offset:8192\n\t"
        "v_mfma_f32_16x16x32_bf16 v[4:7], a[112:115], v[100:103], v[4:7]\n\t"
        "ds_read_b64_tr_b16 a[132:133], v131 offset:24576\n\t"
        "ds_read_b64_tr_b16 a[134:135], v131 offset:25344\n\t"
        "v_add_f32 v57, v57, v59\n\t"
        "v_mfma_f32_16x16x32_bf16 v[8:11], a[116:119], v[100:103], v[8:11]\n\t"
        "ds_read_b64_tr_b16 a[136:137], v132 offset:24576\n\t"
        "ds_read_b64_tr_b16 a[138:139], v132 offset:25344\n\t"
        "v_cvt_pk_bf16_f32 v52, v56, v57\n\t"
        "v_mfma_f32_16x16x32_bf16 v[12:15], a[120:123], v[100:103], v[12:15]\n\t"
        "ds_read_b64_tr_b16 a[140:141], v133 offset:24576\n\t"
        "ds_read_b64_tr_b16 a[142:143], v133 offset:25344\n\t"
        "ds_read_b128 v[108:111], v129 offset:8192\n\t"
        "v_cvt_pk_bf16_f32 v58, v58, v59\n\t"
        "v_mfma_f32_16x16x32_bf16 v[16:19], a[124:127], v[100:103], v[16:19]\n\t"
        "buffer_load_dwordx4 v141, s[24:27], s36 offen offset:1024 lds\n\t"
        "ds_read_b128 v[112:115], v129 offset:12288\n\t"
        "v_cmp_ne_u32 s[64:65], v52, v58\n\t"
        "v_mfma_f32_16x16x32_bf16 v[20:23], a[128:131], v[100:103], v[20:23]\n\t"
        "ds_read_b128 v[84:87], v129 offset:16384\n\t"
        "ds_read_b64_tr_b16 a[144:145], v131 offset:30720\n\t"
        "v_mbcnt_lo_u32_b32 v56, s64, v54\n\t"
        "s_waitcnt lgkmcnt(10)\n\t"
        "v_mfma_f32_16x16x32_bf16 v[12:15], a[108:111], v[104:107], v[12:15]\n\t"
        "ds_read_b64_tr_b16 a[146:147], v131 offset:31488\n\t"
        "ds_read_b64_tr_b16 a[148:149], v132 offset:30720\n\t"
        "v_mbcnt_hi_u32_b32 v56, s65, v56\n\t"
        "v_mfma_f32_16x16x32_bf16 v[16:19], a[112:115], v[104:107], v[16:19]\n\t"
        "ds_read_b64_tr_b16 a[150:151], v132 offset:31488\n\t"
        "ds_read_b64_tr_b16 a[152:153], v133 offset:30720\n\t"
        "s_bcnt1_i32_b64 s46, s[64:65]\n\t"
        "v_mfma_f32_16x16x32_bf16 v[20:23], a[116:119], v[104:107], v[20:23]\n\t"
        "buffer_load_dwordx4 v142, s[24:27], s36 offen offset:2048 lds\n\t"
        "ds_read_b64_tr_b16 a[154:155], v133 offset:31488\n\t"
        "v_readlane_b32 s40, v155, 16\n\t"
        "v_mfma_f32_16x16x32_bf16 v[24:27], a[120:123], v[104:107], v[24:27]\n\t"
        "ds_read_b128 v[88:91], v129 offset:20480\n\t"
        "v_readlane_b32 s43, v155, 4\n\t"
        "v_mfma_f32_16x16x32_bf16 v[28:31], a[124:127], v[104:107], v[28:31]\n\t"
        "s_add_u32 m0, s40, s35\n\t"
        "s_waitcnt vmcnt(14)\n\t"
        "v_and_b32 v48, s54, v48\n\t"
        "v_mfma_f32_16x16x32_bf16 v[32:35], a[128:131], v[104:107], v[32:35]\n\t"
        "v_pk_max_u16 v60, v154, v48\n\t"
        "v_and_b32 v49, s54, v49\n\t"
        "s_waitcnt lgkmcnt(9)\n\t"
        "v_mfma_f32_16x16x32_bf16 v[0:3], a[132:135], v[108:111], v[0:3]\n\t"
        "buffer_load_dwordx4 v134, s[16:19], s43 offen  lds\n\t"
        "v_readlane_b32 s40, v155, 17\n\t"
        "v_mfma_f32_16x16x32_bf16 v[4:7], a[136:139], v[108:111], v[4:7]\n\t"
        "v_readlane_b32 s43, v155, 5\n\t"
        "s_add_u32 m0, s40, s35\n\t"
        "v_mfma_f32_16x16x32_bf16 v[8:11], a[140:143], v[108:111], v[8:11]\n\t"
        "v_pk_max_u16 v60, v60, v49\n\t"
        "v_and_b32 v50, s54, v50\n\t"
        "s_waitcnt lgkmcnt(5)\n\t"
        "v_mfma_f32_16x16x32_bf16 v[12:15], a[132:135], v[112:115], v[12:15]\n\t"
        "v_pk_max_u16 v60, v60, v50\n\t"
        "v_and_b32 v51, s54, v51\n\t"
        "v_mfma_f32_16x16x32_bf16 v[16:19], a[136:139], v[112:115], v[16:19]\n\t"
        "buffer_load_dwordx4 v135, s[16:19], s43 offen  lds\n\t"
        "s_add_u32 m0, s37, 12288\n\t"
        "v_mfma_f32_16x16x32_bf16 v[20:23], a[140:143], v[112:115], v[20:23]\n\t"
        "s_add_u32 s36, s36, s38\n\t"
        "v_pk_max_u16 v60, v60, v51\n\t"
        "v_mfma_f32_16x16x32_bf16 v[24:27], a[108:111], v[112:115], v[24:27]\n\t"
        "s_waitcnt vmcnt(10) lgkmcnt(1)\n\t"
        "s_barrier\n\t"
        "ds_read_b64_tr_b16 a[156:157], v131 offset:36864\n\t"
        "ds_read_b64_tr_b16 a[158:159], v131 offset:37632\n\t"
        "v_mfma_f32_16x16x32_bf16 v[28:31], a[112:115], v[112:115], v[28:31]\n\t"
        "ds_read_b64_tr_b16 a[160:161], v132 offset:36864\n\t"
        "ds_read_b64_tr_b16 a[162:163], v132 offset:37632\n\t"
        "v_and_b32 v61, s54, v61\n\t"
        "v_mfma_f32_16x16x32_bf16 v[32:35], a[116:119], v[112:115], v[32:35]\n\t"
        "buffer_load_dwordx4 v140, s[24:27], s36 offen offset:0 lds\n\t"
        "ds_read_b64_tr_b16 a[164:165], v133 offset:36864\n\t"
        "v_pk_max_u16 v60, v60, v61\n\t"
        "v_mfma_f32_16x16x32_bf16 v[36:39], a[120:123], v[112:115], v[36:39]\n\t"
        "ds_read_b64_tr_b16 a[166:167], v133 offset:37632\n\t"
        "ds_read_b128 v[92:95], v129 offset:64\n\t"
        "v_cmp_ne_u32 s[12:13], v60, v154\n\t"
        "v_mfma_f32_16x16x32_bf16 v[40:43], a[124:127], v[112:115], v[40:43]\n\t"
        "ds_read_b128 v[96:99], v129 offset:4160\n\t"
        "ds_read_b64_tr_b16 a[168:169], v131 offset:43008\n\t"
        "s_lshl_b32 s6, 256, s4\n\t"
        ".LpS0%=:\n\t"
        "s_cbranch_execz .LrS%=\n\t"
        "v_mfma_f32_16x16x32_bf16 v[44:47], a[128:131], v[112:115], v[44:47]\n\t"
        "s_cmp_lg_u64 s[12:13], 0\n\t"
        "s_cselect_b32 s6, s6, 0\n\t"
        "ds_read_b64_tr_b16 a[170:171], v131 offset:43776\n\t"
        "ds_read_b64_tr_b16 a[172:173], v132 offset:43008\n\t"
        "v_mfma_f32_16x16x32_bf16 v[24:27], a[132:135], v[84:87], v[24:27]\n\t"
        "buffer_load_dwordx4 v141, s[24:27], s36 offen offset:1024 lds\n\t"
        "ds_read_b64_tr_b16 a[174:175], v132 offset:43776\n\t"
        "s_add_u32 s61, s61, s46\n\t"
        "v_mfma_f32_16x16x32_bf16 v[28:31], a[136:139], v[84:87], v[28:31]\n\t"
        "ds_read_b64_tr_b16 a[176:177], v133 offset:43008\n\t"
        "ds_read_b64_tr_b16 a[178:179], v133 offset:43776\n\t"
        "v_add_u32 v54, s46, v54\n\t"
        "v_mfma_f32_16x16x32_bf16 v[32:35], a[140:143], v[84:87], v[32:35]\n\t"
        "ds_read_b128 v[100:103], v129 offset:8256\n\t"
        "v_min_u32 v56, 97, v56\n\t"
        "v_mfma_f32_16x16x32_bf16 v[36:39], a[108:111], v[84:87], v[36:39]\n\t"
        "v_lshl_add_u32 v56, v56, 3, s60\n\t"
        "v_readlane_b32 s46, v155, 39\n\t"
        "v_mfma_f32_16x16x32_bf16 v[40:43], a[112:115], v[84:87], v[40:43]\n\t"
        "s_mov_b64 exec, s[64:65]\n\t"
        "ds_write_b64 v56, v[52:53]\n\t"
        "s_mov_b64 exec, -1\n\t"
        "v_mfma_f32_16x16x32_bf16 v[44:47], a[116:119], v[84:87], v[44:47]\n\t"
        "buffer_load_dwordx4 v142, s[24:27], s36 offen offset:2048 lds\n\t"
        "v_readlane_b32 s40, v155, 18\n\t"
        "s_waitcnt lgkmcnt(7)\n\t"
        "v_mfma_f32_16x16x32_bf16 v[36:39], a[132:135], v[88:91], v[36:39]\n\t"
        "ds_read_b128 v[104:107], v129 offset:12352\n\t"
        "v_readlane_b32 s43, v155, 6\n\t"
        "v_mfma_f32_16x16x32_bf16 v[40:43], a[136:139], v[88:91], v[40:43]\n\t"
        "s_add_u32 m0, s40, s35\n\t"
        "v_add_u32 v53, 2048, v53\n\t"
        "v_mfma_f32_16x16x32_bf16 v[44:47], a[140:143], v[88:91], v[44:47]\n\t"
        "v_readlane_b32 s47, v155, 42\n\t"
        "v_perm_b32 v56, v66, v66, s44\n\t"
        "v_mfma_f32_16x16x32_bf16 v[0:3], a[156:159], v[92:95], v[0:3]\n\t"
        "v_perm_b32 v57, v66, v66, s45\n\t"
        "v_add_f32 v58, s46, v68\n\t"
        "v_mfma_f32_16x16x32_bf16 v[4:7], a[160:163], v[92:95], v[4:7]\n\t"
        "buffer_load_dwordx4 v136, s[16:19], s43 offen  lds\n\t"
        "s_bitcmp1_b32 s42, 10\n\t"
        "s_cbranch_scc1 .Lx52%=\n\t"
        ".Lx53%=:\n\t"
        "v_mfma_f32_16x16x32_bf16 v[8:11], a[164:167], v[92:95], v[8:11]\n\t"
        "v_readlane_b32 s40, v155, 19\n\t"
        "v_readlane_b32 s43, v155, 7\n\t"
        "v_mfma_f32_16x16x32_bf16 v[0:3], a[144:147], v[96:99], v[0:3]\n\t"
        "s_add_u32 m0, s40, s35\n\t"
        "v_add_f32 v59, s47, v69\n\t"
        "v_mfma_f32_16x16x32_bf16 v[4:7], a[148:151], v[96:99], v[4:7]\n\t"
        "v_add_f32 v56, v56, v58\n\t"
        "v_add_f32 v57, v57, v59\n\t"
        "v_mfma_f32_16x16x32_bf16 v[8:11], a[152:155], v[96:99], v[8:11]\n\t"
        "v_cvt_pk_bf16_f32 v52, v56, v57\n\t"
        "v_cvt_pk_bf16_f32 v58, v58, v59\n\t"
        "v_mfma_f32_16x16x32_bf16 v[12:15], a[156:159], v[96:99], v[12:15]\n\t"
        "ds_read_b128 v[108:111], v129 offset:16448\n\t"
        "v_cmp_ne_u32 s[64:65], v52, v58\n\t"
        "v_mfma_f32_16x16x32_bf16 v[16:19], a[160:163], v[96:99], v[16:19]\n\t"
        "s_nop 0\n\t"
        "v_mbcnt_lo_u32_b32 v56, s64, v54\n\t"
        "v_mbcnt_hi_u32_b32 v56, s65, v56\n\t"
        "v_mfma_f32_16x16x32_bf16 v[20:23], a[164:167], v[96:99], v[20:23]\n\t"
        "s_bcnt1_i32_b64 s46, s[64:65]\n\t"
        "s_add_u32 s61, s61, s46\n\t"
        "s_waitcnt lgkmcnt(1)\n\t"
        "v_mfma_f32_16x16x32_bf16 v[0:3], a[168:171], v[100:103], v[0:3]\n\t"
        "s_mov_b32 s3, s61\n\t"
        "v_min_u32 v56, 97, v56\n\t"
        "v_mfma_f32_16x16x32_bf16 v[4:7], a[172:175], v[100:103], v[4:7]\n\t"
        "buffer_load_dwordx4 v137, s[16:19], s43 offen  lds\n\t"
        "s_add_u32 m0, s37, 24576\n\t"
        "v_mfma_f32_16x16x32_bf16 v[8:11], a[176:179], v[100:103], v[8:11]\n\t"
        "s_add_u32 s36, s36, s5\n\t"
        "v_lshl_add_u32 v56, v56, 3, s60\n\t"
        "v_mfma_f32_16x16x32_bf16 v[12:15], a[144:147], v[100:103], v[12:15]\n\t"
        "s_mov_b64 exec, s[64:65]\n\t"
        "ds_write_b64 v56, v[52:53]\n\t"
        "s_mov_b64 exec, -1\n\t"
        "v_mfma_f32_16x16x32_bf16 v[16:19], a[148:151], v[100:103], v[16:19]\n\t"
        "s_cmp_gt_u32 s3, 64\n\t"
        "s_cselect_b32 s8, 1, 0\n\t"
        "v_mfma_f32_16x16x32_bf16 v[20:23], a[152:155], v[100:103], v[20:23]\n\t"
        "s_waitcnt vmcnt(10)\n\t"
        "s_barrier\n\t"
        "buffer_load_dwordx4 v140, s[24:27], s36 offen offset:0 lds\n\t"
        "ds_read_b128 v[112:115], v129 offset:20544\n\t"
        "v_mfma_f32_16x16x32_bf16 v[24:27], a[156:159], v[100:103], v[24:27]\n\t"
        "ds_read_b64_tr_b16 a[180:181], v131 offset:49152\n\t"
        "ds_read_b64_tr_b16 a[182:183], v131 offset:49920\n\t"
        "s_or_b32 s56, s56, s8\n\t"
        "v_mfma_f32_16x16x32_bf16 v[28:31], a[160:163], v[100:103], v[28:31]\n\t"
        "ds_read_b64_tr_b16 a[184:185], v132 offset:49152\n\t"
        "ds_read_b64_tr_b16 a[186:187], v132 offset:49920\n\t"
        "s_min_u32 s61, s61, 80\n\t"
        "v_mfma_f32_16x16x32_bf16 v[32:35], a[164:167], v[100:103], v[32:35]\n\t"
        "ds_read_b64_tr_b16 a[188:189], v133 offset:49152\n\t"
        "ds_read_b64_tr_b16 a[190:191], v133 offset:49920\n\t"
        "v_min_u32 v78, s61, v116\n\t"
        "v_mfma_f32_16x16x32_bf16 v[12:15], a[168:171], v[104:107], v[12:15]\n\t"
        "buffer_load_dwordx4 v141, s[24:27], s36 offen offset:1024 lds\n\t"
        "ds_read_b64_tr_b16 a[192:193], v131 offset:55296\n\t"
        "v_lshl_add_u32 v78, v78, 3, s60\n\t"
        "v_mfma_f32_16x16x32_bf16 v[16:19], a[172:175], v[104:107], v[16:19]\n\t"
        "ds_read_b64 v[62:63], v78\n\t"
        "ds_read_b64_tr_b16 a[194:195], v131 offset:56064\n\t"
        "v_cmp_ge_u32 s[66:67], s61, v116\n\t"
        "v_mfma_f32_16x16x32_bf16 v[20:23], a[176:179], v[104:107], v[20:23]\n\t"
        "ds_read_b64_tr_b16 a[196:197], v132 offset:55296\n\t"
        "ds_read_b64_tr_b16 a[198:199], v132 offset:56064\n\t"
        "s_mov_b32 s46, 0x07060302\n\t"
        "v_mfma_f32_16x16x32_bf16 v[24:27], a[144:147], v[104:107], v[24:27]\n\t"
        "ds_read_b64_tr_b16 a[200:201], v133 offset:55296\n\t"
        "ds_read_b64_tr_b16 a[202:203], v133 offset:56064\n\t"
        "v_mfma_f32_16x16x32_bf16 v[28:31], a[148:151], v[104:107], v[28:31]\n\t"
        "buffer_load_dwordx4 v142, s[24:27], s36 offen offset:2048 lds\n\t"
        "s_add_u32 m0, s37, 36864\n\t"
        "v_mfma_f32_16x16x32_bf16 v[32:35], a[152:155], v[104:107], v[32:35]\n\t"
        "ds_read_b128 v[84:87], v129 offset:128\n\t"
        "s_add_u32 s36, s36, s39\n\t"
        "v_mfma_f32_16x16x32_bf16 v[36:39], a[156:159], v[104:107], v[36:39]\n\t"
        "ds_read_b128 v[88:91], v129 offset:4224\n\t"
        "v_mfma_f32_16x16x32_bf16 v[40:43], a[160:163], v[104:107], v[40:43]\n\t"
        "v_mfma_f32_16x16x32_bf16 v[44:47], a[164:167], v[104:107], v[44:47]\n\t"
        "buffer_load_dwordx4 v140, s[24:27], s36 offen offset:0 lds\n\t"
        "s_waitcnt lgkmcnt(9)\n\t"
        "v_mfma_f32_16x16x32_bf16 v[24:27], a[168:171], v[108:111], v[24:27]\n\t"
        "v_mfma_f32_16x16x32_bf16 v[28:31], a[172:175], v[108:111], v[28:31]\n\t"
        "s_mov_b64 exec, s[66:67]\n\t"
        "s_waitcnt lgkmcnt(6)\n\t"
        "ds_write_b32 v63, v117\n\t"
        "s_mov_b64 exec, -1\n\t"
        "v_mfma_f32_16x16x32_bf16 v[32:35], a[176:179], v[108:111], v[32:35]\n\t"
        "v_and_b32 v78, 0xfffff800, v63\n\t"
        "v_lshlrev_b32 v72, 16, v62\n\t"
        "v_mfma_f32_16x16x32_bf16 v[36:39], a[144:147], v[108:111], v[36:39]\n\t"
        "buffer_load_dwordx4 v141, s[24:27], s36 offen offset:1024 lds\n\t"
        "v_and_b32 v73, 0xffff0000, v62\n\t"
        "v_mfma_f32_16x16x32_bf16 v[40:43], a[148:151], v[108:111], v[40:43]\n\t"
        "ds_read_b128 v[64:67], v78 offset:1536\n\t"
        "v_med3_f32 v72, v72, 0, s63\n\t"
        "v_mfma_f32_16x16x32_bf16 v[44:47], a[152:155], v[108:111], v[44:47]\n\t"
        "v_med3_f32 v73, v73, 0, s63\n\t"
        "v_perm_b32 v62, v73, v72, s46\n\t"
        "v_mfma_f32_16x16x32_bf16 v[36:39], a[168:171], v[112:115], v[36:39]\n\t"
        "v_fract_f32 v74, v72\n\t"
        "v_fract_f32 v75, v73\n\t"
        "v_mfma_f32_16x16x32_bf16 v[40:43], a[172:175], v[112:115], v[40:43]\n\t"
        "v_cvt_u32_f32 v76, v72\n\t"
        "v_cvt_u32_f32 v77, v73\n\t"
        "v_mfma_f32_16x16x32_bf16 v[44:47], a[176:179], v[112:115], v[44:47]\n\t"
        "v_cmp_neq_f32 s[64:65], 0, v74\n\t"
        "v_cmp_neq_f32 s[44:45], 0, v75\n\t"
        "s_waitcnt lgkmcnt(2)\n\t"
        "v_mfma_f32_16x16x32_bf16 v[0:3], a[192:195], v[84:87], v[0:3]\n\t"
        "v_sub_f32 v78, 1.0, v74\n\t"
        "v_sub_f32 v79, 1.0, v75\n\t"
        "v_mfma_f32_16x16x32_bf16 v[4:7], a[196:199], v[84:87], v[4:7]\n\t"
        "v_cvt_pk_bf16_f32 v79, v78, v79\n\t"
        "v_lshlrev_b32 v78, 16, v79\n\t"
        "v_mfma_f32_16x16x32_bf16 v[8:11], a[200:203], v[84:87], v[8:11]\n\t"
        "v_and_b32 v79, 0xffff0000, v79\n\t"
        "v_mul_f32 v80, v78, v79\n\t"
        "v_mfma_f32_16x16x32_bf16 v[0:3], a[180:183], v[88:91], v[0:3]\n\t"
        "ds_read_b128 v[92:95], v129 offset:8320\n\t"
        "v_mul_f32 v78, v78, v75\n\t"
        "v_mfma_f32_16x16x32_bf16 v[4:7], a[184:187], v[88:91], v[4:7]\n\t"
        "s_waitcnt vmcnt(10)\n\t"
        "s_barrier\n\t"
        "buffer_load_dwordx4 v142, s[24:27], s36 offen offset:2048 lds\n\t"
        "s_add_u32 m0, s37, 49152\n\t"
        "ds_read_b64_tr_b16 a[204:205], v131 offset:0\n\t"
        "v_mfma_f32_16x16x32_bf16 v[8:11], a[188:191], v[88:91], v[8:11]\n\t"
        "ds_read_b64_tr_b16 a[206:207], v131 offset:768\n\t"
        "s_add_u32 s36, s36, s38\n\t"
        "ds_read_b64_tr_b16 a[208:209], v132 offset:0\n\t"
        "v_mfma_f32_16x16x32_bf16 v[12:15], a[192:195], v[88:91], v[12:15]\n\t"
        "ds_read_b64_tr_b16 a[210:211], v132 offset:768\n\t"
        "ds_read_b64_tr_b16 a[212:213], v133 offset:0\n\t"
        "ds_read_b64_tr_b16 a[214:215], v133 offset:768\n\t"
        "v_mul_f32 v79, v74, v79\n\t"
        "v_mfma_f32_16x16x32_bf16 v[16:19], a[196:199], v[88:91], v[16:19]\n\t"
        "ds_read_b128 v[96:99], v129 offset:8320\n\t"
        "ds_read_b128 v[100:103], v129 offset:12416\n\t"
        "v_mul_f32 v81, v74, v75\n\t"
        "v_mfma_f32_16x16x32_bf16 v[20:23], a[200:203], v[88:91], v[20:23]\n\t"
        "buffer_load_dwordx4 v140, s[24:27], s36 offen offset:0 lds\n\t"
        "ds_read_b128 v[104:107], v129 offset:16512\n\t"
        "v_cndmask_b32 v78, v79, v78, s[44:45]\n\t"
        "s_waitcnt lgkmcnt(9)\n\t"
        "v_mfma_f32_16x16x32_bf16 v[12:15], a[180:183], v[92:95], v[12:15]\n\t"
        "ds_read_b64_tr_b16 a[216:217], v131 offset:6144\n\t"
        "ds_read_b64_tr_b16 a[218:219], v131 offset:6912\n\t"
        "v_cndmask_b32 v79, 0, v79, s[44:45]\n\t"
        "v_mfma_f32_16x16x32_bf16 v[16:19], a[184:187], v[92:95], v[16:19]\n\t"
        "ds_read_b64_tr_b16 a[220:221], v132 offset:6144\n\t"
        "ds_read_b64_tr_b16 a[222:223], v132 offset:6912\n\t"
        "v_cvt_pk_bf16_f32 v70, v80, v78\n\t"
        "v_mfma_f32_16x16x32_bf16 v[20:23], a[188:191], v[92:95], v[20:23]\n\t"
        "ds_read_b64_tr_b16 a[224:225], v133 offset:6144\n\t"
        "ds_read_b64_tr_b16 a[226:227], v133 offset:6912\n\t"
        "v_cvt_pk_bf16_f32 v71, v79, v81\n\t"
        "v_mfma_f32_16x16x32_bf16 v[24:27], a[192:195], v[92:95], v[24:27]\n\t"
        "buffer_load_dwordx4 v141, s[24:27], s36 offen offset:1024 lds\n\t"
        "v_pk_max_u16 v78, v62, v64\n\t"
        "v_mfma_f32_16x16x32_bf16 v[28:31], a[196:199], v[92:95], v[28:31]\n\t"
        "ds_read_b128 v[108:111], v129 offset:20608\n\t"
        "v_pk_min_u16 v78, v78, v65\n\t"
        "v_mfma_f32_16x16x32_bf16 v[32:35], a[200:203], v[92:95], v[32:35]\n\t"
        "v_cmp_ne_u32 vcc, v78, v62\n\t"
        "v_sub_u32 v78, v77, v67\n\t"
        "s_waitcnt lgkmcnt(9)\n\t"
        "v_mfma_f32_16x16x32_bf16 v[0:3], a[204:207], v[96:99], v[0:3]\n\t"
        "v_mad_u32_u24 v79, v77, 6, v66\n\t"
        "v_mad_u32_u24 v79, v76, s62, v79\n\t"
        "v_mfma_f32_16x16x32_bf16 v[4:7], a[208:211], v[96:99], v[4:7]\n\t"
        "buffer_load_dwordx4 v142, s[24:27], s36 offen offset:2048 lds\n\t"
        "s_and_b64 vcc, vcc, s[66:67]\n\t"
        "v_mfma_f32_16x16x32_bf16 v[8:11], a[212:215], v[96:99], v[8:11]\n\t"
        "s_or_b64 s[56:57], s[56:57], vcc\n\t"
        "v_bfe_u32 v80, v78, 2, 1\n\t"
        "s_waitcnt lgkmcnt(6)\n\t"
        "v_mfma_f32_16x16x32_bf16 v[12:15], a[204:207], v[100:103], v[12:15]\n\t"
        "v_or_b32 v80, v80, v79\n\t"
        "v_add_u32 v78, 1, v78\n\t"
        "v_mfma_f32_16x16x32_bf16 v[16:19], a[208:211], v[100:103], v[16:19]\n\t"
        "v_bfe_u32 v78, v78, 2, 1\n\t"
        "v_add3_u32 v78, v79, v78, 6\n\t"
        "v_mfma_f32_16x16x32_bf16 v[20:23], a[212:215], v[100:103], v[20:23]\n\t"
        "v_cndmask_b32 v78, v80, v78, s[44:45]\n\t"
        "v_cndmask_b32 v79, 0, v119, s[64:65]\n\t"
        "v_mfma_f32_16x16x32_bf16 v[24:27], a[180:183], v[100:103], v[24:27]\n\t"
        "v_add_u32 v81, v80, v79\n\t"
        "v_add_u32 v82, v78, v79\n\t"
        "v_mfma_f32_16x16x32_bf16 v[28:31], a[184:187], v[100:103], v[28:31]\n\t"
        "v_cndmask_b32 v78, v81, v78, s[44:45]\n\t"
        "v_lshl_or_b32 v68, v78, 16, v80\n\t"
        "v_mfma_f32_16x16x32_bf16 v[32:35], a[188:191], v[100:103], v[32:35]\n\t"
        "v_lshl_or_b32 v69, v82, 16, v81\n\t"
        "v_mfma_f32_16x16x32_bf16 v[36:39], a[192:195], v[100:103], v[36:39]\n\t"
        "ds_write_b128 v118, v[68:71]\n\t"
        "v_mfma_f32_16x16x32_bf16 v[40:43], a[196:199], v[100:103], v[40:43]\n\t"
        "s_cmp_gt_u32 s3, 16\n\t"
        "s_cselect_b32 s8, 2, 0\n\t"
        "v_mfma_f32_16x16x32_bf16 v[44:47], a[200:203], v[100:103], v[44:47]\n\t"
        "s_waitcnt vmcnt(9) lgkmcnt(1)\n\t"
        "s_barrier\n\t"
        "ds_read_b64_tr_b16 a[228:229], v131 offset:12288\n\t"
        "ds_read_b64_tr_b16 a[230:231], v131 offset:13056\n\t"
        "v_mfma_f32_16x16x32_bf16 v[24:27], a[204:207], v[104:107], v[24:27]\n\t"
        "ds_read_b64_tr_b16 a[232:233], v132 offset:12288\n\t"
        "ds_read_b64_tr_b16 a[234:235], v132 offset:13056\n\t"
        "ds_read_b64_tr_b16 a[236:237], v133 offset:12288\n\t"
        "v_xor_b32 v154, 32, v186\n\t"
        "v_mfma_f32_16x16x32_bf16 v[28:31], a[208:211], v[104:107], v[28:31]\n\t"
        "ds_read_b64_tr_b16 a[238:239], v133 offset:13056\n\t"
        "ds_read_b128 v[112:115], v130 offset:0\n\t"
        "v_mfma_f32_16x16x32_bf16 v[32:35], a[212:215], v[104:107], v[32:35]\n\t"
        "ds_read_b128 v[84:87], v130 offset:4096\n\t"
        "ds_read_b64_tr_b16 a[240:241], v131 offset:18432\n\t"
        "v_mfma_f32_16x16x32_bf16 v[36:39], a[180:183], v[104:107], v[36:39]\n\t"
        "s_cmp_lg_u64 s[56:57], 0\n\t"
        "s_addc_u32 s8, s8, s6\n\t"
        "ds_read_b64_tr_b16 a[242:243], v131 offset:19200\n\t"
        "ds_read_b64_tr_b16 a[244:245], v132 offset:18432\n\t"
        "v_mfma_f32_16x16x32_bf16 v[40:43], a[184:187], v[104:107], v[40:43]\n\t"
        "ds_read_b64_tr_b16 a[246:247], v132 offset:19200\n\t"
        "ds_read_b64_tr_b16 a[248:249], v133 offset:18432\n\t"
        "v_mov_b32 v56, s8\n\t"
        "v_mfma_f32_16x16x32_bf16 v[44:47], a[188:191], v[104:107], v[44:47]\n\t"
        "ds_read_b64_tr_b16 a[250:251], v133 offset:19200\n\t"
        "v_mfma_f32_16x16x32_bf16 v[36:39], a[204:207], v[108:111], v[36:39]\n\t"
        "ds_read_b128 v[88:91], v130 offset:8192\n\t"
        "v_mfma_f32_16x16x32_bf16 v[40:43], a[208:211], v[108:111], v[40:43]\n\t"
        "s_mov_b64 exec, 1\n\t"
        "ds_write_b32 v153, v56\n\t"
        "s_mov_b64 exec, -1\n\t"
        "v_mfma_f32_16x16x32_bf16 v[44:47], a[212:215], v[108:111], v[44:47]\n\t"
        "ds_read_b128 v[160:163], v185 offset:0\n\t"
        "s_waitcnt lgkmcnt(10)\n\t"
        "v_mfma_f32_16x16x32_bf16 v[0:3], a[228:231], v[112:115], v[0:3]\n\t"
        "ds_read_b128 v[92:95], v130 offset:12288\n\t"
        "v_mfma_f32_16x16x32_bf16 v[4:7], a[232:235], v[112:115], v[4:7]\n\t"
        "v_mfma_f32_16x16x32_bf16 v[8:11], a[236:239], v[112:115], v[8:11]\n\t"
        "s_waitcnt vmcnt(6)\n\t"
        "s_barrier\n\t"
        "ds_read_b64_tr_b16 a[252:253], v131 offset:24576\n\t"
        "ds_read_b64_tr_b16 a[254:255], v131 offset:25344\n\t"
        "s_waitcnt lgkmcnt(9)\n\t"
        "v_mfma_f32_16x16x32_bf16 v[0:3], a[216:219], v[84:87], v[0:3]\n\t"
        "ds_read_b64_tr_b16 v[188:189], v132 offset:24576\n\t"
        "ds_read_b64_tr_b16 v[190:191], v132 offset:25344\n\t"
        "v_mfma_f32_16x16x32_bf16 v[4:7], a[220:223], v[84:87], v[4:7]\n\t"
        "ds_read_b64_tr_b16 v[192:193], v133 offset:24576\n\t"
        "ds_read_b64_tr_b16 v[194:195], v133 offset:25344\n\t"
        "v_mfma_f32_16x16x32_bf16 v[8:11], a[224:227], v[84:87], v[8:11]\n\t"
        "ds_read_b64_tr_b16 v[196:197], v131 offset:30720\n\t"
        "v_mfma_f32_16x16x32_bf16 v[12:15], a[228:231], v[84:87], v[12:15]\n\t"
        "ds_read_b128 v[96:99], v130 offset:16384\n\t"
        "v_mfma_f32_16x16x32_bf16 v[16:19], a[232:235], v[84:87], v[16:19]\n\t"
        "ds_read_b64_tr_b16 v[198:199], v131 offset:31488\n\t"
        "v_mfma_f32_16x16x32_bf16 v[20:23], a[236:239], v[84:87], v[20:23]\n\t"
        "ds_read_b64_tr_b16 v[200:201], v132 offset:30720\n\t"
        "ds_read_b64_tr_b16 v[202:203], v132 offset:31488\n\t"
        "s_waitcnt lgkmcnt(11)\n\t"
        "v_lshlrev_b32_sdwa v157, 5, v160 dst_sel:DWORD dst_unused:UNUSED_PAD src0_sel:DWORD src1_sel:WORD_0\n\t"
        "v_mfma_f32_16x16x32_bf16 v[0:3], a[240:243], v[88:91], v[0:3]\n\t"
        "ds_read_b64_tr_b16 v[204:205], v133 offset:30720\n\t"
        "ds_read_b64_tr_b16 v[206:207], v133 offset:31488\n\t"
        "v_lshlrev_b32_sdwa v158, 5, v160 dst_sel:DWORD dst_unused:UNUSED_PAD src0_sel:DWORD src1_sel:WORD_1\n\t"
        "v_mfma_f32_16x16x32_bf16 v[4:7], a[244:247], v[88:91], v[4:7]\n\t"
        "v_or_b32 v157, v184, v157\n\t"
        "v_or_b32 v158, v184, v158\n\t"
        "v_mfma_f32_16x16x32_bf16 v[8:11], a[248:251], v[88:91], v[8:11]\n\t"
        "ds_read_b64_tr_b16 v[60:61], v157 offset:0\n\t"
        "v_xor_b32 v159, 32, v157\n\t"
        "v_mfma_f32_16x16x32_bf16 v[12:15], a[216:219], v[88:91], v[12:15]\n\t"
        "ds_read_b64_tr_b16 v[62:63], v158 offset:0\n\t"
        "v_xor_b32 v152, 32, v158\n\t"
        "v_mfma_f32_16x16x32_bf16 v[16:19], a[220:223], v[88:91], v[16:19]\n\t"
        "ds_read_b64_tr_b16 v[64:65], v159 offset:0\n\t"
        "v_perm_b32 v164, v162, v162, v168\n\t"
        "v_mfma_f32_16x16x32_bf16 v[20:23], a[224:227], v[88:91], v[20:23]\n\t"
        "ds_read_b64_tr_b16 v[66:67], v152 offset:0\n\t"
        "v_perm_b32 v165, v162, v162, v169\n\t"
        "v_mfma_f32_16x16x32_bf16 v[24:27], a[228:231], v[88:91], v[24:27]\n\t"
        "ds_read_b64_tr_b16 v[68:69], v157 offset:64\n\t"
        "v_perm_b32 v166, v162, v162, v170\n\t"
        "v_mfma_f32_16x16x32_bf16 v[28:31], a[232:235], v[88:91], v[28:31]\n\t"
        "ds_read_b64_tr_b16 v[70:71], v158 offset:64\n\t"
        "v_perm_b32 v167, v162, v162, v171\n\t"
        "v_mfma_f32_16x16x32_bf16 v[32:35], a[236:239], v[88:91], v[32:35]\n\t"
        "ds_read_b128 v[100:103], v130 offset:64\n\t"
        "v_cmp_ne_u32 vcc, 0, v163\n\t"
        "v_mfma_f32_16x16x32_bf16 v[12:15], a[240:243], v[92:95], v[12:15]\n\t"
        "ds_read_b64_tr_b16 v[72:73], v159 offset:64\n\t"
        "v_mfma_f32_16x16x32_bf16 v[16:19], a[244:247], v[92:95], v[16:19]\n\t"
        "ds_read_b64_tr_b16 v[74:75], v152 offset:64\n\t"
        "v_mfma_f32_16x16x32_bf16 v[20:23], a[248:251], v[92:95], v[20:23]\n\t"
        "ds_read_b128 v[104:107], v130 offset:4160\n\t"
        "v_mfma_f32_16x16x32_bf16 v[24:27], a[216:219], v[92:95], v[24:27]\n\t"
        "ds_read_b64_tr_b16 v[76:77], v157 offset:128\n\t"
        "s_waitcnt lgkmcnt(9)\n\t"
        "v_mfma_f32_16x16x32_bf16 v[48:51], v[60:63], v[164:167], 0\n\t"
        "ds_read_b64_tr_b16 v[78:79], v158 offset:128\n\t"
        "v_mfma_f32_16x16x32_bf16 v[28:31], a[220:223], v[92:95], v[28:31]\n\t"
        "ds_read_b64_tr_b16 v[80:81], v159 offset:128\n\t"
        "s_waitcnt lgkmcnt(9)\n\t"
        "v_mfma_f32_16x16x32_bf16 v[52:55], v[64:67], v[164:167], 0\n\t"
        "ds_read_b64_tr_b16 v[82:83], v152 offset:128\n\t"
        "v_mfma_f32_16x16x32_bf16 v[32:35], a[224:227], v[92:95], v[32:35]\n\t"
        "ds_read_b128 v[108:111], v130 offset:20480\n\t"
        "s_waitcnt lgkmcnt(9)\n\t"
        "v_mfma_f32_16x16x32_bf16 v[56:59], v[68:71], v[164:167], 0\n\t"
        "v_mfma_f32_16x16x32_bf16 v[36:39], a[228:231], v[92:95], v[36:39]\n\t"
        "v_mfma_f32_16x16x32_bf16 v[40:43], a[232:235], v[92:95], v[40:43]\n\t"
        "s_waitcnt lgkmcnt(6)\n\t"
        "v_mfma_f32_16x16x32_bf16 v[116:119], v[72:75], v[164:167], 0\n\t"
        "v_mfma_f32_16x16x32_bf16 v[44:47], a[236:239], v[92:95], v[44:47]\n\t"
        "v_mfma_f32_16x16x32_bf16 v[24:27], a[240:243], v[96:99], v[24:27]\n\t"
        "s_waitcnt lgkmcnt(3)\n\t"
        "v_mfma_f32_16x16x32_bf16 v[120:123], v[76:79], v[164:167], 0\n\t"
        "s_waitcnt vmcnt(0)\n\t"
        "s_barrier\n\t"
        "ds_read_b64_tr_b16 v[208:209], v131 offset:36864\n\t"
        "ds_read_b64_tr_b16 v[210:211], v131 offset:37632\n\t"
        "v_mfma_f32_16x16x32_bf16 v[28:31], a[244:247], v[96:99], v[28:31]\n\t"
        "ds_read_b64_tr_b16 v[212:213], v132 offset:36864\n\t"
        "ds_read_b64_tr_b16 v[214:215], v132 offset:37632\n\t"
        "s_waitcnt lgkmcnt(5)\n\t"
        "v_mfma_f32_16x16x32_bf16 v[124:127], v[80:83], v[164:167], 0\n\t"
        "ds_read_b64_tr_b16 v[216:217], v133 offset:36864\n\t"
        "ds_read_b64_tr_b16 v[218:219], v133 offset:37632\n\t"
        "s_cbranch_vccnz .Lx54%=\n\t"
        ".Lx55%=:\n\t"
        "v_mfma_f32_16x16x32_bf16 v[32:35], a[248:251], v[96:99], v[32:35]\n\t"
        "ds_read_b128 v[112:115], v130 offset:8256\n\t"
        "v_cvt_pk_bf16_f32 v48, v48, v49\n\t"
        "v_mfma_f32_16x16x32_bf16 v[36:39], a[216:219], v[96:99], v[36:39]\n\t"
        "ds_read_b64_tr_b16 v[220:221], v131 offset:43008\n\t"
        "v_cvt_pk_bf16_f32 v49, v50, v51\n\t"
        "v_mfma_f32_16x16x32_bf16 v[40:43], a[220:223], v[96:99], v[40:43]\n\t"
        "ds_write_b64 v186, v[48:49] offset:0\n\t"
        "v_mfma_f32_16x16x32_bf16 v[44:47], a[224:227], v[96:99], v[44:47]\n\t"
        "ds_read_b64_tr_b16 v[222:223], v131 offset:43776\n\t"
        "ds_read_b64_tr_b16 v[224:225], v132 offset:43008\n\t"
        "v_cvt_pk_bf16_f32 v52, v52, v53\n\t"
        "v_mfma_f32_16x16x32_bf16 v[0:3], v[196:199], v[100:103], v[0:3]\n\t"
        "ds_read_b128 v[84:87], v130 offset:12352\n\t"
        "ds_read_b64_tr_b16 v[226:227], v132 offset:43776\n\t"
        "v_cvt_pk_bf16_f32 v53, v54, v55\n\t"
        "v_mfma_f32_16x16x32_bf16 v[4:7], v[200:203], v[100:103], v[4:7]\n\t"
        "ds_write_b64 v154, v[52:53] offset:0\n\t"
        "ds_read_b64_tr_b16 v[228:229], v133 offset:43008\n\t"
        "v_mfma_f32_16x16x32_bf16 v[8:11], v[204:207], v[100:103], v[8:11]\n\t"
        "ds_read_b64_tr_b16 v[230:231], v133 offset:43776\n\t"
        "v_cvt_pk_bf16_f32 v56, v56, v57\n\t"
        "v_mfma_f32_16x16x32_bf16 v[0:3], a[252:255], v[104:107], v[0:3]\n\t"
        "ds_read_b64_tr_b16 v[232:233], v131 offset:49152\n\t"
        "v_cvt_pk_bf16_f32 v57, v58, v59\n\t"
        "v_mfma_f32_16x16x32_bf16 v[4:7], v[188:191], v[104:107], v[4:7]\n\t"
        "ds_write_b64 v186, v[56:57] offset:64\n\t"
        "v_mfma_f32_16x16x32_bf16 v[8:11], v[192:195], v[104:107], v[8:11]\n\t"
        "ds_read_b64_tr_b16 v[234:235], v131 offset:49920\n\t"
        "v_cvt_pk_bf16_f32 v116, v116, v117\n\t"
        "v_mfma_f32_16x16x32_bf16 v[12:15], v[196:199], v[104:107], v[12:15]\n\t"
        "ds_read_b64_tr_b16 v[236:237], v132 offset:49152\n\t"
        "v_cvt_pk_bf16_f32 v117, v118, v119\n\t"
        "v_mfma_f32_16x16x32_bf16 v[16:19], v[200:203], v[104:107], v[16:19]\n\t"
        "ds_write_b64 v154, v[116:117] offset:64\n\t"
        "v_mfma_f32_16x16x32_bf16 v[20:23], v[204:207], v[104:107], v[20:23]\n\t"
        "ds_read_b64_tr_b16 v[238:239], v132 offset:49920\n\t"
        "v_cvt_pk_bf16_f32 v120, v120, v121\n\t"
        "s_waitcnt lgkmcnt(9)\n\t"
        "v_mfma_f32_16x16x32_bf16 v[36:39], a[240:243], v[108:111], v[36:39]\n\t"
        "ds_read_b128 v[88:91], v130 offset:16448\n\t"
        "v_cvt_pk_bf16_f32 v121, v122, v123\n\t"
        "v_mfma_f32_16x16x32_bf16 v[40:43], a[244:247], v[108:111], v[40:43]\n\t"
        "ds_write_b64 v186, v[120:121] offset:128\n\t"
        "v_mfma_f32_16x16x32_bf16 v[44:47], a[248:251], v[108:111], v[44:47]\n\t"
        "ds_read_b64_tr_b16 v[240:241], v133 offset:49152\n\t"
        "ds_read_b64_tr_b16 v[242:243], v133 offset:49920\n\t"
        "v_cvt_pk_bf16_f32 v124, v124, v125\n\t"
        "v_mfma_f32_16x16x32_bf16 v[0:3], v[208:211], v[112:115], v[0:3]\n\t"
        "ds_read_b64_tr_b16 v[244:245], v131 offset:55296\n\t"
        "v_cvt_pk_bf16_f32 v125, v126, v127\n\t"
        "v_mfma_f32_16x16x32_bf16 v[4:7], v[212:215], v[112:115], v[4:7]\n\t"
        "ds_write_b64 v154, v[124:125] offset:128\n\t"
        "v_mfma_f32_16x16x32_bf16 v[8:11], v[216:219], v[112:115], v[8:11]\n\t"
        "ds_read_b64_tr_b16 v[246:247], v131 offset:56064\n\t"
        "v_mfma_f32_16x16x32_bf16 v[12:15], a[252:255], v[112:115], v[12:15]\n\t"
        "ds_read_b128 v[92:95], v130 offset:128\n\t"
        "v_mfma_f32_16x16x32_bf16 v[16:19], v[188:191], v[112:115], v[16:19]\n\t"
        "ds_read_b64_tr_b16 v[248:249], v132 offset:55296\n\t"
        "v_mfma_f32_16x16x32_bf16 v[20:23], v[192:195], v[112:115], v[20:23]\n\t"
        "ds_read_b64_tr_b16 v[250:251], v132 offset:56064\n\t"
        "v_mfma_f32_16x16x32_bf16 v[24:27], v[196:199], v[112:115], v[24:27]\n\t"
        "ds_read_b128 v[96:99], v130 offset:4224\n\t"
        "v_mfma_f32_16x16x32_bf16 v[28:31], v[200:203], v[112:115], v[28:31]\n\t"
        "ds_read_b64_tr_b16 v[252:253], v133 offset:55296\n\t"
        "v_mfma_f32_16x16x32_bf16 v[32:35], v[204:207], v[112:115], v[32:35]\n\t"
        "ds_read_b64_tr_b16 v[254:255], v133 offset:56064\n\t"
        "v_mfma_f32_16x16x32_bf16 v[12:15], v[208:211], v[84:87], v[12:15]\n\t"
        "v_mfma_f32_16x16x32_bf16 v[16:19], v[212:215], v[84:87], v[16:19]\n\t"
        "v_mfma_f32_16x16x32_bf16 v[20:23], v[216:219], v[84:87], v[20:23]\n\t"
        "v_mfma_f32_16x16x32_bf16 v[24:27], a[252:255], v[84:87], v[24:27]\n\t"
        "ds_read_b128 v[100:103], v130 offset:8320\n\t"
        "v_mfma_f32_16x16x32_bf16 v[28:31], v[188:191], v[84:87], v[28:31]\n\t"
        "v_mfma_f32_16x16x32_bf16 v[32:35], v[192:195], v[84:87], v[32:35]\n\t"
        "v_mfma_f32_16x16x32_bf16 v[36:39], v[196:199], v[84:87], v[36:39]\n\t"
        "s_waitcnt lgkmcnt(1)\n\t"
        "s_barrier\n\t"
        "v_mfma_f32_16x16x32_bf16 v[40:43], v[200:203], v[84:87], v[40:43]\n\t"
        "ds_read_b128 v[124:127], v182 offset:0\n\t"
        "v_mfma_f32_16x16x32_bf16 v[44:47], v[204:207], v[84:87], v[44:47]\n\t"
        "ds_read_b128 v[132:135], v187 offset:0\n\t"
        "v_mfma_f32_16x16x32_bf16 v[24:27], v[208:211], v[88:91], v[24:27]\n\t"
        "ds_read_b128 v[136:139], v187 offset:16\n\t"
        "v_mfma_f32_16x16x32_bf16 v[28:31], v[212:215], v[88:91], v[28:31]\n\t"
        "ds_read_b32 v140, v187 offset:32\n\t"
        "v_mfma_f32_16x16x32_bf16 v[32:35], v[216:219], v[88:91], v[32:35]\n\t"
        "v_mfma_f32_16x16x32_bf16 v[36:39], a[252:255], v[88:91], v[36:39]\n\t"
        "ds_read_b128 v[104:107], v130 offset:12416\n\t"
        "v_mfma_f32_16x16x32_bf16 v[40:43], v[188:191], v[88:91], v[40:43]\n\t"
        "v_mfma_f32_16x16x32_bf16 v[44:47], v[192:195], v[88:91], v[44:47]\n\t"
        "v_mfma_f32_16x16x32_bf16 v[0:3], v[232:235], v[92:95], v[0:3]\n\t"
        "v_mfma_f32_16x16x32_bf16 v[4:7], v[236:239], v[92:95], v[4:7]\n\t"
        "s_waitcnt lgkmcnt(4)\n\t"
        "v_or3_b32 v124, v124, v125, v126\n\t"
        "v_or_b32 v124, v124, v127\n\t"
        "v_mfma_f32_16x16x32_bf16 v[8:11], v[240:243], v[92:95], v[8:11]\n\t"
        "v_readfirstlane_b32 s6, v124\n\t"
        "v_mfma_f32_16x16x32_bf16 v[0:3], v[220:223], v[96:99], v[0:3]\n\t"
        "s_nop 0\n\t"
        "s_bitcmp1_b32 s6, 1\n\t"
        "s_cbranch_scc1 .Lx56%=\n\t"
        ".Lx57%=:\n\t"
        "v_mfma_f32_16x16x32_bf16 v[4:7], v[224:227], v[96:99], v[4:7]\n\t"
        "s_waitcnt lgkmcnt(2)\n\t"
        "v_xor_b32 v132, v181, v132\n\t"
        "v_add_u32 v151, s9, v172\n\t"
        "v_mfma_f32_16x16x32_bf16 v[8:11], v[228:231], v[96:99], v[8:11]\n\t"
        "v_max_u32 v151, v151, v132\n\t"
        "v_xor_b32 v133, v181, v133\n\t"
        "v_mfma_f32_16x16x32_bf16 v[12:15], v[232:235], v[96:99], v[12:15]\n\t"
        "ds_read_b128 v[108:111], v130 offset:16512\n\t"
        "v_add_u32 v183, s9, v173\n\t"
        "v_mfma_f32_16x16x32_bf16 v[16:19], v[236:239], v[96:99], v[16:19]\n\t"
        "v_max_u32 v183, v183, v133\n\t"
        "v_xor_b32 v134, v181, v134\n\t"
        "v_mfma_f32_16x16x32_bf16 v[20:23], v[240:243], v[96:99], v[20:23]\n\t"
        "v_mfma_f32_16x16x32_bf16 v[0:3], v[244:247], v[100:103], v[0:3]\n\t"
        "v_mfma_f32_16x16x32_bf16 v[4:7], v[248:251], v[100:103], v[4:7]\n\t"
        "v_mfma_f32_16x16x32_bf16 v[8:11], v[252:255], v[100:103], v[8:11]\n\t"
        "v_mfma_f32_16x16x32_bf16 v[12:15], v[220:223], v[100:103], v[12:15]\n\t"
        "ds_read_b128 v[112:115], v130 offset:20544\n\t"
        "v_mfma_f32_16x16x32_bf16 v[16:19], v[224:227], v[100:103], v[16:19]\n\t"
        "v_mfma_f32_16x16x32_bf16 v[20:23], v[228:231], v[100:103], v[20:23]\n\t"
        "v_mfma_f32_16x16x32_bf16 v[24:27], v[232:235], v[100:103], v[24:27]\n\t"
        "ds_read_b128 v[84:87], v130 offset:20608\n\t"
        "s_add_u32 s55, s41, 0\n\t"
        "v_mfma_f32_16x16x32_bf16 v[28:31], v[236:239], v[100:103], v[28:31]\n\t"
        "v_cvt_pk_bf16_f32 v116, v0, v1\n\t"
        "v_cvt_pk_bf16_f32 v117, v2, v3\n\t"
        "v_mfma_f32_16x16x32_bf16 v[32:35], v[240:243], v[100:103], v[32:35]\n\t"
        "v_cvt_pk_bf16_f32 v118, v4, v5\n\t"
        "v_cvt_pk_bf16_f32 v119, v6, v7\n\t"
        "s_waitcnt lgkmcnt(2)\n\t"
        "v_mfma_f32_16x16x32_bf16 v[12:15], v[244:247], v[104:107], v[12:15]\n\t"
        "ds_read_b128 v[88:91], v151 offset:0\n\t"
        "v_cvt_pk_bf16_f32 v120, v8, v9\n\t"
        "v_mfma_f32_16x16x32_bf16 v[16:19], v[248:251], v[104:107], v[16:19]\n\t"
        "v_cvt_pk_bf16_f32 v121, v10, v11\n\t"
        "v_mfma_f32_16x16x32_bf16 v[20:23], v[252:255], v[104:107], v[20:23]\n\t"
        "v_permlane16_swap_b32 v116, v118\n\t"
        "v_mfma_f32_16x16x32_bf16 v[24:27], v[220:223], v[104:107], v[24:27]\n\t"
        "v_permlane16_swap_b32 v117, v119\n\t"
        "v_mfma_f32_16x16x32_bf16 v[28:31], v[224:227], v[104:107], v[28:31]\n\t"
        "buffer_store_dwordx4 v[116:119], v144, s[28:31], s55 offen sc1\n\t"
        "v_mfma_f32_16x16x32_bf16 v[32:35], v[228:231], v[104:107], v[32:35]\n\t"
        "ds_write_b64 v146, v[120:121] offset:0\n\t"
        "v_mfma_f32_16x16x32_bf16 v[36:39], v[232:235], v[104:107], v[36:39]\n\t"
        "ds_read_b128 v[92:95], v151 offset:64\n\t"
        "s_add_u32 s55, s41, 43008\n\t"
        "v_mfma_f32_16x16x32_bf16 v[40:43], v[236:239], v[104:107], v[40:43]\n\t"
        "ds_read_b128 v[96:99], v151 offset:128\n\t"
        "v_add_u32 v151, s9, v174\n\t"
        "v_mfma_f32_16x16x32_bf16 v[44:47], v[240:243], v[104:107], v[44:47]\n\t"
        "v_max_u32 v151, v151, v134\n\t"
        "v_xor_b32 v135, v181, v135\n\t"
        "v_mfma_f32_16x16x32_bf16 v[24:27], v[244:247], v[108:111], v[24:27]\n\t"
        "ds_read_b128 v[100:103], v183 offset:0\n\t"
        "v_cvt_pk_bf16_f32 v116, v12, v13\n\t"
        "v_mfma_f32_16x16x32_bf16 v[28:31], v[248:251], v[108:111], v[28:31]\n\t"
        "v_cvt_pk_bf16_f32 v117, v14, v15\n\t"
        "v_cvt_pk_bf16_f32 v118, v16, v17\n\t"
        "v_mfma_f32_16x16x32_bf16 v[32:35], v[252:255], v[108:111], v[32:35]\n\t"
        "v_cvt_pk_bf16_f32 v119, v18, v19\n\t"
        "v_cvt_pk_bf16_f32 v120, v20, v21\n\t"
        "v_mfma_f32_16x16x32_bf16 v[36:39], v[220:223], v[108:111], v[36:39]\n\t"
        "ds_read_b128 v[104:107], v183 offset:64\n\t"
        "v_cvt_pk_bf16_f32 v121, v22, v23\n\t"
        "v_mfma_f32_16x16x32_bf16 v[40:43], v[224:227], v[108:111], v[40:43]\n\t"
        "v_permlane16_swap_b32 v116, v118\n\t"
        "v_mfma_f32_16x16x32_bf16 v[44:47], v[228:231], v[108:111], v[44:47]\n\t"
        "v_permlane16_swap_b32 v117, v119\n\t"
        "s_waitcnt lgkmcnt(5)\n\t"
        "v_mfma_f32_16x16x32_bf16 v[36:39], v[208:211], v[112:115], v[36:39]\n\t"
        "ds_read_b128 v[108:111], v183 offset:128\n\t"
        "v_add_u32 v183, s9, v175\n\t"
        "v_mfma_f32_16x16x32_bf16 v[40:43], v[212:215], v[112:115], v[40:43]\n\t"
        "v_max_u32 v183, v183, v135\n\t"
        "v_xor_b32 v136, v181, v136\n\t"
        "v_mfma_f32_16x16x32_bf16 v[44:47], v[216:219], v[112:115], v[44:47]\n\t"
        "ds_read_b128 v[132:135], v187 offset:2048\n\t"
        "v_mfma_f32_16x16x32_bf16 v[36:39], v[244:247], v[84:87], v[36:39]\n\t"
        "buffer_store_dwordx4 v[116:119], v144, s[28:31], s55 offen sc1\n\t"
        "v_mfma_f32_16x16x32_bf16 v[40:43], v[248:251], v[84:87], v[40:43]\n\t"
        "ds_write_b64 v146, v[120:121] offset:2048\n\t"
        "v_mfma_f32_16x16x32_bf16 v[44:47], v[252:255], v[84:87], v[44:47]\n\t"
        "ds_read_b128 v[112:115], v151 offset:0\n\t"
        "s_add_u32 s55, s41, 86016\n\t"
        "v_mfma_f32_16x16x32_bf16 v[48:51], a[12:15], v[88:91], 0\n\t"
        "ds_read_b128 v[84:87], v151 offset:64\n\t"
        "v_cvt_pk_bf16_f32 v116, v24, v25\n\t"
        "v_mfma_f32_16x16x32_bf16 v[52:55], a[16:19], v[88:91], 0\n\t"
        "v_cvt_pk_bf16_f32 v117, v26, v27\n\t"
        "v_cvt_pk_bf16_f32 v118, v28, v29\n\t"
        "v_mfma_f32_16x16x32_bf16 v[56:59], a[20:23], v[88:91], 0\n\t"
        "v_cvt_pk_bf16_f32 v119, v30, v31\n\t"
        "v_cvt_pk_bf16_f32 v120, v32, v33\n\t"
        "s_waitcnt lgkmcnt(5)\n\t"
        "v_mfma_f32_16x16x32_bf16 v[48:51], a[48:51], v[92:95], v[48:51]\n\t"
        "ds_read_b128 v[88:91], v151 offset:128\n\t"
        ".LpS1%=:\n\t"
        "s_cbranch_execz .LrS%=\n\t"
        "v_add_u32 v151, s9, v176\n\t"
        "v_mfma_f32_16x16x32_bf16 v[52:55], a[52:55], v[92:95], v[52:55]\n\t"
        "v_max_u32 v151, v151, v136\n\t"
        "v_xor_b32 v137, v181, v137\n\t"
        "v_mfma_f32_16x16x32_bf16 v[56:59], a[56:59], v[92:95], v[56:59]\n\t"
        "v_cvt_pk_bf16_f32 v121, v34, v35\n\t"
        "v_mfma_f32_16x16x32_bf16 v[48:51], a[84:87], v[96:99], v[48:51]\n\t"
        "v_permlane16_swap_b32 v116, v118\n\t"
        "v_mfma_f32_16x16x32_bf16 v[52:55], a[88:91], v[96:99], v[52:55]\n\t"
        "v_permlane16_swap_b32 v117, v119\n\t"
        "v_mfma_f32_16x16x32_bf16 v[56:59], a[92:95], v[96:99], v[56:59]\n\t"
        "ds_read_b128 v[92:95], v183 offset:0\n\t"
        "v_mfma_f32_16x16x32_bf16 v[48:51], a[120:123], v[100:103], v[48:51]\n\t"
        "ds_read_b128 v[96:99], v183 offset:64\n\t"
        "v_mfma_f32_16x16x32_bf16 v[52:55], a[124:127], v[100:103], v[52:55]\n\t"
        "v_mfma_f32_16x16x32_bf16 v[56:59], a[128:131], v[100:103], v[56:59]\n\t"
        "buffer_store_dwordx4 v[116:119], v144, s[28:31], s55 offen sc1\n\t"
        "v_mfma_f32_16x16x32_bf16 v[48:51], a[156:159], v[104:107], v[48:51]\n\t"
        "ds_write_b64 v146, v[120:121] offset:4096\n\t"
        "v_mfma_f32_16x16x32_bf16 v[52:55], a[160:163], v[104:107], v[52:55]\n\t"
        "ds_read_b128 v[100:103], v183 offset:128\n\t"
        "v_add_u32 v183, s9, v177\n\t"
        "v_mfma_f32_16x16x32_bf16 v[56:59], a[164:167], v[104:107], v[56:59]\n\t"
        "v_max_u32 v183, v183, v137\n\t"
        "v_xor_b32 v138, v181, v138\n\t"
        "s_waitcnt lgkmcnt(4)\n\t"
        "v_mfma_f32_16x16x32_bf16 v[48:51], a[192:195], v[108:111], v[48:51]\n\t"
        "ds_read_b128 v[104:107], v151 offset:0\n\t"
        "s_add_u32 s55, s41, 129024\n\t"
        "v_mfma_f32_16x16x32_bf16 v[52:55], a[196:199], v[108:111], v[52:55]\n\t"
        "v_cvt_pk_bf16_f32 v116, v36, v37\n\t"
        "v_cvt_pk_bf16_f32 v117, v38, v39\n\t"
        "v_mfma_f32_16x16x32_bf16 v[56:59], a[200:203], v[108:111], v[56:59]\n\t"
        "v_cvt_pk_bf16_f32 v118, v40, v41\n\t"
        "v_cvt_pk_bf16_f32 v119, v42, v43\n\t"
        "v_mfma_f32_16x16x32_bf16 v[48:51], a[228:231], v[112:115], v[48:51]\n\t"
        "ds_read_b128 v[108:111], v151 offset:64\n\t"
        "v_cvt_pk_bf16_f32 v120, v44, v45\n\t"
        "v_mfma_f32_16x16x32_bf16 v[52:55], a[232:235], v[112:115], v[52:55]\n\t"
        "v_cvt_pk_bf16_f32 v121, v46, v47\n\t"
        "v_mfma_f32_16x16x32_bf16 v[56:59], a[236:239], v[112:115], v[56:59]\n\t"
        "v_permlane16_swap_b32 v116, v118\n\t"
        "v_mfma_f32_16x16x32_bf16 v[48:51], v[196:199], v[84:87], v[48:51]\n\t"
        "v_permlane16_swap_b32 v117, v119\n\t"
        "v_mfma_f32_16x16x32_bf16 v[52:55], v[200:203], v[84:87], v[52:55]\n\t"
        "ds_read_b128 v[112:115], v151 offset:128\n\t"
        "v_add_u32 v151, s9, v178\n\t"
        "v_mfma_f32_16x16x32_bf16 v[56:59], v[204:207], v[84:87], v[56:59]\n\t"
        "v_max_u32 v151, v151, v138\n\t"
        "v_xor_b32 v139, v181, v139\n\t"
        "v_mfma_f32_16x16x32_bf16 v[48:51], v[232:235], v[88:91], v[48:51]\n\t"
        "ds_read_b128 v[84:87], v183 offset:0\n\t"
        "v_mfma_f32_16x16x32_bf16 v[52:55], v[236:239], v[88:91], v[52:55]\n\t"
        "buffer_store_dwordx4 v[116:119], v144, s[28:31], s55 offen sc1\n\t"
        "v_mfma_f32_16x16x32_bf16 v[56:59], v[240:243], v[88:91], v[56:59]\n\t"
        "ds_write_b64 v146, v[120:121] offset:6144\n\t"
        "s_waitcnt lgkmcnt(4)\n\t"
        "v_mfma_f32_16x16x32_bf16 v[48:51], a[0:3], v[92:95], v[48:51]\n\t"
        "ds_read_b128 v[88:91], v183 offset:64\n\t"
        "v_mfma_f32_16x16x32_bf16 v[52:55], a[4:7], v[92:95], v[52:55]\n\t"
        "v_mfma_f32_16x16x32_bf16 v[56:59], a[8:11], v[92:95], v[56:59]\n\t"
        "v_mfma_f32_16x16x32_bf16 v[48:51], a[36:39], v[96:99], v[48:51]\n\t"
        "ds_read_b128 v[92:95], v183 offset:128\n\t"
        "v_add_u32 v183, s9, v179\n\t"
        "v_mfma_f32_16x16x32_bf16 v[52:55], a[40:43], v[96:99], v[52:55]\n\t"
        "v_max_u32 v183, v183, v139\n\t"
        "v_xor_b32 v140, v181, v140\n\t"
        "v_mfma_f32_16x16x32_bf16 v[56:59], a[44:47], v[96:99], v[56:59]\n\t"
        "s_waitcnt lgkmcnt(2)\n\t"
        "s_barrier\n\t"
        "v_mfma_f32_16x16x32_bf16 v[48:51], a[72:75], v[100:103], v[48:51]\n\t"
        "ds_read_b128 v[136:139], v187 offset:2064\n\t"
        "v_mfma_f32_16x16x32_bf16 v[52:55], a[76:79], v[100:103], v[52:55]\n\t"
        "ds_read_b128 v[124:127], v147 offset:0\n\t"
        "v_mfma_f32_16x16x32_bf16 v[56:59], a[80:83], v[100:103], v[56:59]\n\t"
        "ds_read_b128 v[96:99], v151 offset:0\n\t"
        "v_mfma_f32_16x16x32_bf16 v[48:51], a[108:111], v[104:107], v[48:51]\n\t"
        "ds_read_b128 v[100:103], v151 offset:64\n\t"
        "v_mfma_f32_16x16x32_bf16 v[52:55], a[112:115], v[104:107], v[52:55]\n\t"
        "v_mfma_f32_16x16x32_bf16 v[56:59], a[116:119], v[104:107], v[56:59]\n\t"
        "v_mfma_f32_16x16x32_bf16 v[48:51], a[144:147], v[108:111], v[48:51]\n\t"
        "ds_read_b128 v[104:107], v151 offset:128\n\t"
        "v_add_u32 v151, s9, v180\n\t"
        "v_mfma_f32_16x16x32_bf16 v[52:55], a[148:151], v[108:111], v[52:55]\n\t"
        "v_max_u32 v151, v151, v140\n\t"
        "v_xor_b32 v132, v181, v132\n\t"
        "v_mfma_f32_16x16x32_bf16 v[56:59], a[152:155], v[108:111], v[56:59]\n\t"
        "ds_read_b32 v140, v187 offset:2080\n\t"
        "v_mfma_f32_16x16x32_bf16 v[48:51], a[180:183], v[112:115], v[48:51]\n\t"
        "ds_read_b128 v[108:111], v183 offset:0\n\t"
        "v_mfma_f32_16x16x32_bf16 v[52:55], a[184:187], v[112:115], v[52:55]\n\t"
        "s_waitcnt lgkmcnt(5)\n\t"
        "buffer_store_dwordx4 v[124:127], v145, s[28:31], s50 offen sc1\n\t"
        "v_mfma_f32_16x16x32_bf16 v[56:59], a[188:191], v[112:115], v[56:59]\n\t"
        "ds_read_b128 v[124:127], v147 offset:4096\n\t"
        "s_add_u32 s50, s50, 86016\n\t"
        "v_mfma_f32_16x16x32_bf16 v[48:51], a[216:219], v[84:87], v[48:51]\n\t"
        "ds_read_b128 v[112:115], v183 offset:64\n\t"
        "v_mfma_f32_16x16x32_bf16 v[52:55], a[220:223], v[84:87], v[52:55]\n\t"
        "v_mfma_f32_16x16x32_bf16 v[56:59], a[224:227], v[84:87], v[56:59]\n\t"
        "v_mfma_f32_16x16x32_bf16 v[48:51], a[252:255], v[88:91], v[48:51]\n\t"
        "ds_read_b128 v[84:87], v183 offset:128\n\t"
        "v_readlane_b32 s9, v155, 47\n\t"
        "v_mfma_f32_16x16x32_bf16 v[52:55], v[188:191], v[88:91], v[52:55]\n\t"
        "s_nop 0\n\t"
        "v_add_u32 v183, s9, v172\n\t"
        "v_max_u32 v183, v183, v132\n\t"
        "v_mfma_f32_16x16x32_bf16 v[56:59], v[192:195], v[88:91], v[56:59]\n\t"
        "v_xor_b32 v133, v181, v133\n\t"
        "v_mfma_f32_16x16x32_bf16 v[48:51], v[220:223], v[92:95], v[48:51]\n\t"
        "ds_read_b128 v[88:91], v151 offset:0\n\t"
        "v_mfma_f32_16x16x32_bf16 v[52:55], v[224:227], v[92:95], v[52:55]\n\t"
        "v_mfma_f32_16x16x32_bf16 v[56:59], v[228:231], v[92:95], v[56:59]\n\t"
        "s_waitcnt lgkmcnt(4)\n\t"
        "v_mfma_f32_16x16x32_bf16 v[48:51], a[24:27], v[96:99], v[48:51]\n\t"
        "ds_read_b128 v[92:95], v151 offset:64\n\t"
        "v_mfma_f32_16x16x32_bf16 v[52:55], a[28:31], v[96:99], v[52:55]\n\t"
        "v_mfma_f32_16x16x32_bf16 v[56:59], a[32:35], v[96:99], v[56:59]\n\t"
        "v_mfma_f32_16x16x32_bf16 v[48:51], a[60:63], v[100:103], v[48:51]\n\t"
        "s_waitcnt lgkmcnt(3)\n\t"
        "buffer_store_dwordx4 v[124:127], v145, s[28:31], s50 offen sc1\n\t"
        "v_mfma_f32_16x16x32_bf16 v[52:55], a[64:67], v[100:103], v[52:55]\n\t"
        "ds_read_b128 v[96:99], v151 offset:128\n\t"
        "v_add_u32 v151, s9, v173\n\t"
        "v_mfma_f32_16x16x32_bf16 v[56:59], a[68:71], v[100:103], v[56:59]\n\t"
        "v_max_u32 v151, v151, v133\n\t"
        "v_xor_b32 v134, v181, v134\n\t"
        "v_mfma_f32_16x16x32_bf16 v[48:51], a[96:99], v[104:107], v[48:51]\n\t"
        "ds_read_b128 v[100:103], v183 offset:0\n\t"
        "v_mfma_f32_16x16x32_bf16 v[52:55], a[100:103], v[104:107], v[52:55]\n\t"
        "v_mfma_f32_16x16x32_bf16 v[56:59], a[104:107], v[104:107], v[56:59]\n\t"
        "v_mfma_f32_16x16x32_bf16 v[48:51], a[132:135], v[108:111], v[48:51]\n\t"
        "ds_read_b128 v[104:107], v183 offset:64\n\t"
        "v_mfma_f32_16x16x32_bf16 v[52:55], a[136:139], v[108:111], v[52:55]\n\t"
        "v_mfma_f32_16x16x32_bf16 v[56:59], a[140:143], v[108:111], v[56:59]\n\t"
        "v_mfma_f32_16x16x32_bf16 v[48:51], a[168:171], v[112:115], v[48:51]\n\t"
        "ds_read_b128 v[108:111], v183 offset:128\n\t"
        "v_add_u32 v183, s9, v174\n\t"
        "v_mfma_f32_16x16x32_bf16 v[52:55], a[172:175], v[112:115], v[52:55]\n\t"
        "v_max_u32 v183, v183, v134\n\t"
        "v_xor_b32 v135, v181, v135\n\t"
        "v_mfma_f32_16x16x32_bf16 v[56:59], a[176:179], v[112:115], v[56:59]\n\t"
        "s_waitcnt lgkmcnt(3)\n\t"
        "v_mfma_f32_16x16x32_bf16 v[48:51], a[204:207], v[84:87], v[48:51]\n\t"
        "ds_read_b128 v[112:115], v151 offset:0\n\t"
        "v_mfma_f32_16x16x32_bf16 v[52:55], a[208:211], v[84:87], v[52:55]\n\t"
        "v_mfma_f32_16x16x32_bf16 v[56:59], a[212:215], v[84:87], v[56:59]\n\t"
        "v_mfma_f32_16x16x32_bf16 v[48:51], a[240:243], v[88:91], v[48:51]\n\t"
        "ds_read_b128 v[84:87], v151 offset:64\n\t"
        "v_mfma_f32_16x16x32_bf16 v[52:55], a[244:247], v[88:91], v[52:55]\n\t"
        "v_mfma_f32_16x16x32_bf16 v[56:59], a[248:251], v[88:91], v[56:59]\n\t"
        "v_mfma_f32_16x16x32_bf16 v[48:51], v[208:211], v[92:95], v[48:51]\n\t"
        "ds_read_b128 v[88:91], v151 offset:128\n\t"
        "v_add_u32 v151, s9, v175\n\t"
        "v_mfma_f32_16x16x32_bf16 v[52:55], v[212:215], v[92:95], v[52:55]\n\t"
        "v_max_u32 v151, v151, v135\n\t"
        "v_xor_b32 v136, v181, v136\n\t"
        "v_mfma_f32_16x16x32_bf16 v[56:59], v[216:219], v[92:95], v[56:59]\n\t"
        "ds_read_b128 v[132:135], v187 offset:4096\n\t"
        "v_mfma_f32_16x16x32_bf16 v[48:51], v[244:247], v[96:99], v[48:51]\n\t"
        "ds_read_b128 v[92:95], v183 offset:0\n\t"
        "v_mfma_f32_16x16x32_bf16 v[52:55], v[248:251], v[96:99], v[52:55]\n\t"
        "v_mfma_f32_16x16x32_bf16 v[56:59], v[252:255], v[96:99], v[56:59]\n\t"
        "s_waitcnt lgkmcnt(4)\n\t"
        "v_mfma_f32_16x16x32_bf16 v[60:63], a[12:15], v[100:103], 0\n\t"
        "ds_read_b128 v[96:99], v183 offset:64\n\t"
        "v_mfma_f32_16x16x32_bf16 v[64:67], a[16:19], v[100:103], 0\n\t"
        "v_mfma_f32_16x16x32_bf16 v[68:71], a[20:23], v[100:103], 0\n\t"
        "v_mfma_f32_16x16x32_bf16 v[60:63], a[48:51], v[104:107], v[60:63]\n\t"
        "ds_read_b128 v[100:103], v183 offset:128\n\t"
        "v_add_u32 v183, s9, v176\n\t"
        "v_mfma_f32_16x16x32_bf16 v[64:67], a[52:55], v[104:107], v[64:67]\n\t"
        "v_max_u32 v183, v183, v136\n\t"
        "v_xor_b32 v137, v181, v137\n\t"
        "v_mfma_f32_16x16x32_bf16 v[68:71], a[56:59], v[104:107], v[68:71]\n\t"
        "v_readlane_b32 s55, v155, 49\n\t"
        "v_cvt_pk_bf16_f32 v116, v48, v49\n\t"
        "v_mfma_f32_16x16x32_bf16 v[60:63], a[84:87], v[108:111], v[60:63]\n\t"
        "ds_read_b128 v[104:107], v151 offset:0\n\t"
        "v_cvt_pk_bf16_f32 v117, v50, v51\n\t"
        "v_mfma_f32_16x16x32_bf16 v[64:67], a[88:91], v[108:111], v[64:67]\n\t"
        "v_cvt_pk_bf16_f32 v118, v52, v53\n\t"
        "v_cvt_pk_bf16_f32 v119, v54, v55\n\t"
        "v_mfma_f32_16x16x32_bf16 v[68:71], a[92:95], v[108:111], v[68:71]\n\t"
        "v_cvt_pk_bf16_f32 v120, v56, v57\n\t"
        "v_cvt_pk_bf16_f32 v121, v58, v59\n\t"
        "v_mfma_f32_16x16x32_bf16 v[60:63], a[120:123], v[112:115], v[60:63]\n\t"
        "v_permlane16_swap_b32 v116, v118\n\t"
        "v_mfma_f32_16x16x32_bf16 v[64:67], a[124:127], v[112:115], v[64:67]\n\t"
        "v_permlane16_swap_b32 v117, v119\n\t"
        "v_mfma_f32_16x16x32_bf16 v[68:71], a[128:131], v[112:115], v[68:71]\n\t"
        "buffer_store_dwordx4 v[116:119], v148, s[28:31], s55 offen sc1\n\t"
        "s_waitcnt lgkmcnt(2)\n\t"
        "v_mfma_f32_16x16x32_bf16 v[60:63], a[156:159], v[84:87], v[60:63]\n\t"
        "ds_write_b64 v146, v[120:121] offset:8192\n\t"
        "v_mfma_f32_16x16x32_bf16 v[64:67], a[160:163], v[84:87], v[64:67]\n\t"
        "ds_read_b128 v[108:111], v151 offset:64\n\t"
        "v_mfma_f32_16x16x32_bf16 v[68:71], a[164:167], v[84:87], v[68:71]\n\t"
        "ds_read_b128 v[112:115], v151 offset:128\n\t"
        "v_add_u32 v151, s9, v177\n\t"
        "v_mfma_f32_16x16x32_bf16 v[60:63], a[192:195], v[88:91], v[60:63]\n\t"
        "ds_read_b128 v[84:87], v183 offset:0\n\t"
        "v_max_u32 v151, v151, v137\n\t"
        "v_mfma_f32_16x16x32_bf16 v[64:67], a[196:199], v[88:91], v[64:67]\n\t"
        "v_xor_b32 v138, v181, v138\n\t"
        "v_mfma_f32_16x16x32_bf16 v[68:71], a[200:203], v[88:91], v[68:71]\n\t"
        "v_mfma_f32_16x16x32_bf16 v[60:63], a[228:231], v[92:95], v[60:63]\n\t"
        "s_waitcnt lgkmcnt(3)\n\t"
        "s_barrier\n\t"
        "v_mfma_f32_16x16x32_bf16 v[64:67], a[232:235], v[92:95], v[64:67]\n\t"
        "ds_read_b128 v[124:127], v147 offset:8192\n\t"
        "v_mfma_f32_16x16x32_bf16 v[68:71], a[236:239], v[92:95], v[68:71]\n\t"
        "ds_read_b128 v[88:91], v183 offset:64\n\t"
        "v_mfma_f32_16x16x32_bf16 v[60:63], v[196:199], v[96:99], v[60:63]\n\t"
        "ds_read_b128 v[92:95], v183 offset:128\n\t"
        "v_add_u32 v183, s9, v178\n\t"
        "v_mfma_f32_16x16x32_bf16 v[64:67], v[200:203], v[96:99], v[64:67]\n\t"
        "v_max_u32 v183, v183, v138\n\t"
        "v_xor_b32 v139, v181, v139\n\t"
        "v_mfma_f32_16x16x32_bf16 v[68:71], v[204:207], v[96:99], v[68:71]\n\t"
        "v_mfma_f32_16x16x32_bf16 v[60:63], v[232:235], v[100:103], v[60:63]\n\t"
        "ds_read_b128 v[96:99], v151 offset:0\n\t"
        "v_mfma_f32_16x16x32_bf16 v[64:67], v[236:239], v[100:103], v[64:67]\n\t"
        "v_mfma_f32_16x16x32_bf16 v[68:71], v[240:243], v[100:103], v[68:71]\n\t"
        "v_mfma_f32_16x16x32_bf16 v[60:63], a[0:3], v[104:107], v[60:63]\n\t"
        "ds_read_b128 v[100:103], v151 offset:64\n\t"
        "v_mfma_f32_16x16x32_bf16 v[64:67], a[4:7], v[104:107], v[64:67]\n\t"
        "s_waitcnt lgkmcnt(4)\n\t"
        "buffer_store_dwordx4 v[124:127], v150, s[28:31], s51 offen sc1\n\t"
        "v_mfma_f32_16x16x32_bf16 v[68:71], a[8:11], v[104:107], v[68:71]\n\t"
        "v_mfma_f32_16x16x32_bf16 v[60:63], a[36:39], v[108:111], v[60:63]\n\t"
        "ds_read_b128 v[104:107], v151 offset:128\n\t"
        "v_add_u32 v151, s9, v179\n\t"
        "v_mfma_f32_16x16x32_bf16 v[64:67], a[40:43], v[108:111], v[64:67]\n\t"
        "v_max_u32 v151, v151, v139\n\t"
        "v_xor_b32 v140, v181, v140\n\t"
        "v_mfma_f32_16x16x32_bf16 v[68:71], a[44:47], v[108:111], v[68:71]\n\t"
        "ds_read_b128 v[136:139], v187 offset:4112\n\t"
        "v_mfma_f32_16x16x32_bf16 v[60:63], a[72:75], v[112:115], v[60:63]\n\t"
        "ds_read_b128 v[108:111], v183 offset:0\n\t"
        "v_mfma_f32_16x16x32_bf16 v[64:67], a[76:79], v[112:115], v[64:67]\n\t"
        "v_mfma_f32_16x16x32_bf16 v[68:71], a[80:83], v[112:115], v[68:71]\n\t"
        "v_mfma_f32_16x16x32_bf16 v[60:63], a[108:111], v[84:87], v[60:63]\n\t"
        "ds_read_b128 v[112:115], v183 offset:64\n\t"
        "v_mfma_f32_16x16x32_bf16 v[64:67], a[112:115], v[84:87], v[64:67]\n\t"
        "v_mfma_f32_16x16x32_bf16 v[68:71], a[116:119], v[84:87], v[68:71]\n\t"
        "s_waitcnt lgkmcnt(4)\n\t"
        "v_mfma_f32_16x16x32_bf16 v[60:63], a[144:147], v[88:91], v[60:63]\n\t"
        "ds_read_b128 v[84:87], v183 offset:128\n\t"
        "v_add_u32 v183, s9, v180\n\t"
        "v_mfma_f32_16x16x32_bf16 v[64:67], a[148:151], v[88:91], v[64:67]\n\t"
        "v_max_u32 v183, v183, v140\n\t"
        "v_xor_b32 v132, v181, v132\n\t"
        "v_mfma_f32_16x16x32_bf16 v[68:71], a[152:155], v[88:91], v[68:71]\n\t"
        "ds_read_b32 v140, v187 offset:4128\n\t"
        "v_mfma_f32_16x16x32_bf16 v[60:63], a[180:183], v[92:95], v[60:63]\n\t"
        "ds_read_b128 v[88:91], v151 offset:0\n\t"
        "v_mfma_f32_16x16x32_bf16 v[64:67], a[184:187], v[92:95], v[64:67]\n\t"
        "v_mfma_f32_16x16x32_bf16 v[68:71], a[188:191], v[92:95], v[68:71]\n\t"
        "v_mfma_f32_16x16x32_bf16 v[60:63], a[216:219], v[96:99], v[60:63]\n\t"
        "ds_read_b128 v[92:95], v151 offset:64\n\t"
        "v_mfma_f32_16x16x32_bf16 v[64:67], a[220:223], v[96:99], v[64:67]\n\t"
        "v_mfma_f32_16x16x32_bf16 v[68:71], a[224:227], v[96:99], v[68:71]\n\t"
        "v_mfma_f32_16x16x32_bf16 v[60:63], a[252:255], v[100:103], v[60:63]\n\t"
        "ds_read_b128 v[96:99], v151 offset:128\n\t"
        "v_readlane_b32 s9, v155, 48\n\t"
        "v_mfma_f32_16x16x32_bf16 v[64:67], v[188:191], v[100:103], v[64:67]\n\t"
        "s_nop 0\n\t"
        "v_add_u32 v151, s9, v172\n\t"
        "v_max_u32 v151, v151, v132\n\t"
        "v_mfma_f32_16x16x32_bf16 v[68:71], v[192:195], v[100:103], v[68:71]\n\t"
        "v_xor_b32 v133, v181, v133\n\t"
        "s_waitcnt lgkmcnt(4)\n\t"
        "v_mfma_f32_16x16x32_bf16 v[60:63], v[220:223], v[104:107], v[60:63]\n\t"
        "ds_read_b128 v[100:103], v183 offset:0\n\t"
        "v_mfma_f32_16x16x32_bf16 v[64:67], v[224:227], v[104:107], v[64:67]\n\t"
        "v_mfma_f32_16x16x32_bf16 v[68:71], v[228:231], v[104:107], v[68:71]\n\t"
        "v_mfma_f32_16x16x32_bf16 v[60:63], a[24:27], v[108:111], v[60:63]\n\t"
        "ds_read_b128 v[104:107], v183 offset:64\n\t"
        "v_mfma_f32_16x16x32_bf16 v[64:67], a[28:31], v[108:111], v[64:67]\n\t"
        "v_mfma_f32_16x16x32_bf16 v[68:71], a[32:35], v[108:111], v[68:71]\n\t"
        "v_mfma_f32_16x16x32_bf16 v[60:63], a[60:63], v[112:115], v[60:63]\n\t"
        "ds_read_b128 v[108:111], v183 offset:128\n\t"
        "v_add_u32 v183, s9, v173\n\t"
        "v_mfma_f32_16x16x32_bf16 v[64:67], a[64:67], v[112:115], v[64:67]\n\t"
        "v_max_u32 v183, v183, v133\n\t"
        "v_xor_b32 v134, v181, v134\n\t"
        "v_mfma_f32_16x16x32_bf16 v[68:71], a[68:71], v[112:115], v[68:71]\n\t"
        "v_mfma_f32_16x16x32_bf16 v[60:63], a[96:99], v[84:87], v[60:63]\n\t"
        "ds_read_b128 v[112:115], v151 offset:0\n\t"
        "v_mfma_f32_16x16x32_bf16 v[64:67], a[100:103], v[84:87], v[64:67]\n\t"
        "v_mfma_f32_16x16x32_bf16 v[68:71], a[104:107], v[84:87], v[68:71]\n\t"
        "s_waitcnt lgkmcnt(3)\n\t"
        "v_mfma_f32_16x16x32_bf16 v[60:63], a[132:135], v[88:91], v[60:63]\n\t"
        "ds_read_b128 v[84:87], v151 offset:64\n\t"
        "v_mfma_f32_16x16x32_bf16 v[64:67], a[136:139], v[88:91], v[64:67]\n\t"
        "v_mfma_f32_16x16x32_bf16 v[68:71], a[140:143], v[88:91], v[68:71]\n\t"
        "v_mfma_f32_16x16x32_bf16 v[60:63], a[168:171], v[92:95], v[60:63]\n\t"
        "ds_read_b128 v[88:91], v151 offset:128\n\t"
        "v_add_u32 v151, s9, v174\n\t"
        "v_mfma_f32_16x16x32_bf16 v[64:67], a[172:175], v[92:95], v[64:67]\n\t"
        "v_max_u32 v151, v151, v134\n\t"
        "v_xor_b32 v135, v181, v135\n\t"
        "v_mfma_f32_16x16x32_bf16 v[68:71], a[176:179], v[92:95], v[68:71]\n\t"
        "v_mfma_f32_16x16x32_bf16 v[60:63], a[204:207], v[96:99], v[60:63]\n\t"
        "ds_read_b128 v[92:95], v183 offset:0\n\t"
        "v_mfma_f32_16x16x32_bf16 v[64:67], a[208:211], v[96:99], v[64:67]\n\t"
        "v_mfma_f32_16x16x32_bf16 v[68:71], a[212:215], v[96:99], v[68:71]\n\t"
        "v_mfma_f32_16x16x32_bf16 v[60:63], a[240:243], v[100:103], v[60:63]\n\t"
        "ds_read_b128 v[96:99], v183 offset:64\n\t"
        "v_mfma_f32_16x16x32_bf16 v[64:67], a[244:247], v[100:103], v[64:67]\n\t"
        "v_mfma_f32_16x16x32_bf16 v[68:71], a[248:251], v[100:103], v[68:71]\n\t"
        "s_waitcnt lgkmcnt(3)\n\t"
        "v_mfma_f32_16x16x32_bf16 v[60:63], v[208:211], v[104:107], v[60:63]\n\t"
        "ds_read_b128 v[100:103], v183 offset:128\n\t"
        "v_add_u32 v183, s9, v175\n\t"
        "v_mfma_f32_16x16x32_bf16 v[64:67], v[212:215], v[104:107], v[64:67]\n\t"
        "v_max_u32 v183, v183, v135\n\t"
        "v_xor_b32 v136, v181, v136\n\t"
        "v_mfma_f32_16x16x32_bf16 v[68:71], v[216:219], v[104:107], v[68:71]\n\t"
        "v_mfma_f32_16x16x32_bf16 v[60:63], v[244:247], v[108:111], v[60:63]\n\t"
        "ds_read_b128 v[104:107], v151 offset:0\n\t"
        "v_mfma_f32_16x16x32_bf16 v[64:67], v[248:251], v[108:111], v[64:67]\n\t"
        "v_mfma_f32_16x16x32_bf16 v[68:71], v[252:255], v[108:111], v[68:71]\n\t"
        "v_mfma_f32_16x16x32_bf16 v[72:75], a[12:15], v[112:115], 0\n\t"
        "ds_read_b128 v[108:111], v151 offset:64\n\t"
        "v_mfma_f32_16x16x32_bf16 v[76:79], a[16:19], v[112:115], 0\n\t"
        "v_mfma_f32_16x16x32_bf16 v[80:83], a[20:23], v[112:115], 0\n\t"
        "v_mfma_f32_16x16x32_bf16 v[72:75], a[48:51], v[84:87], v[72:75]\n\t"
        "ds_read_b128 v[112:115], v151 offset:128\n\t"
        "v_add_u32 v151, s9, v176\n\t"
        "v_mfma_f32_16x16x32_bf16 v[76:79], a[52:55], v[84:87], v[76:79]\n\t"
        "v_max_u32 v151, v151, v136\n\t"
        "v_xor_b32 v137, v181, v137\n\t"
        "v_mfma_f32_16x16x32_bf16 v[80:83], a[56:59], v[84:87], v[80:83]\n\t"
        "v_readlane_b32 s55, v155, 50\n\t"
        "v_cvt_pk_bf16_f32 v116, v60, v61\n\t"
        "s_waitcnt lgkmcnt(3)\n\t"
        "v_mfma_f32_16x16x32_bf16 v[72:75], a[84:87], v[88:91], v[72:75]\n\t"
        "ds_read_b128 v[84:87], v183 offset:0\n\t"
        "v_cvt_pk_bf16_f32 v117, v62, v63\n\t"
        "v_mfma_f32_16x16x32_bf16 v[76:79], a[88:91], v[88:91], v[76:79]\n\t"
        "v_cvt_pk_bf16_f32 v118, v64, v65\n\t"
        "v_cvt_pk_bf16_f32 v119, v66, v67\n\t"
        "v_mfma_f32_16x16x32_bf16 v[80:83], a[92:95], v[88:91], v[80:83]\n\t"
        "v_cvt_pk_bf16_f32 v120, v68, v69\n\t"
        "v_cvt_pk_bf16_f32 v121, v70, v71\n\t"
        "v_mfma_f32_16x16x32_bf16 v[72:75], a[120:123], v[92:95], v[72:75]\n\t"
        "v_permlane16_swap_b32 v116, v118\n\t"
        "v_mfma_f32_16x16x32_bf16 v[76:79], a[124:127], v[92:95], v[76:79]\n\t"
        "v_permlane16_swap_b32 v117, v119\n\t"
        "v_mfma_f32_16x16x32_bf16 v[80:83], a[128:131], v[92:95], v[80:83]\n\t"
        "buffer_store_dwordx4 v[116:119], v148, s[28:31], s55 offen sc1\n\t"
        "v_mfma_f32_16x16x32_bf16 v[72:75], a[156:159], v[96:99], v[72:75]\n\t"
        "ds_write_b64 v146, v[120:121] offset:0\n\t"
        "v_mfma_f32_16x16x32_bf16 v[76:79], a[160:163], v[96:99], v[76:79]\n\t"
        "ds_read_b128 v[88:91], v183 offset:64\n\t"
        "v_mfma_f32_16x16x32_bf16 v[80:83], a[164:167], v[96:99], v[80:83]\n\t"
        "ds_read_b128 v[92:95], v183 offset:128\n\t"
        "v_add_u32 v183, s9, v177\n\t"
        "v_mfma_f32_16x16x32_bf16 v[72:75], a[192:195], v[100:103], v[72:75]\n\t"
        "ds_read_b128 v[96:99], v151 offset:0\n\t"
        "v_max_u32 v183, v183, v137\n\t"
        "v_mfma_f32_16x16x32_bf16 v[76:79], a[196:199], v[100:103], v[76:79]\n\t"
        "v_xor_b32 v138, v181, v138\n\t"
        "v_mfma_f32_16x16x32_bf16 v[80:83], a[200:203], v[100:103], v[80:83]\n\t"
        "s_waitcnt lgkmcnt(4)\n\t"
        "v_mfma_f32_16x16x32_bf16 v[72:75], a[228:231], v[104:107], v[72:75]\n\t"
        "ds_read_b128 v[100:103], v151 offset:64\n\t"
        "v_mfma_f32_16x16x32_bf16 v[76:79], a[232:235], v[104:107], v[76:79]\n\t"
        "v_mfma_f32_16x16x32_bf16 v[80:83], a[236:239], v[104:107], v[80:83]\n\t"
        "v_mfma_f32_16x16x32_bf16 v[72:75], v[196:199], v[108:111], v[72:75]\n\t"
        "ds_read_b128 v[104:107], v151 offset:128\n\t"
        "v_add_u32 v151, s9, v178\n\t"
        "v_mfma_f32_16x16x32_bf16 v[76:79], v[200:203], v[108:111], v[76:79]\n\t"
        "v_max_u32 v151, v151, v138\n\t"
        "v_xor_b32 v139, v181, v139\n\t"
        "v_mfma_f32_16x16x32_bf16 v[80:83], v[204:207], v[108:111], v[80:83]\n\t"
        "v_mfma_f32_16x16x32_bf16 v[72:75], v[232:235], v[112:115], v[72:75]\n\t"
        "ds_read_b128 v[108:111], v183 offset:0\n\t"
        "v_mfma_f32_16x16x32_bf16 v[76:79], v[236:239], v[112:115], v[76:79]\n\t"
        "v_mfma_f32_16x16x32_bf16 v[80:83], v[240:243], v[112:115], v[80:83]\n\t"
        "v_mfma_f32_16x16x32_bf16 v[72:75], a[0:3], v[84:87], v[72:75]\n\t"
        "ds_read_b128 v[112:115], v183 offset:64\n\t"
        "v_mfma_f32_16x16x32_bf16 v[76:79], a[4:7], v[84:87], v[76:79]\n\t"
        "v_mfma_f32_16x16x32_bf16 v[80:83], a[8:11], v[84:87], v[80:83]\n\t"
        "s_waitcnt lgkmcnt(3)\n\t"
        "v_mfma_f32_16x16x32_bf16 v[72:75], a[36:39], v[88:91], v[72:75]\n\t"
        "ds_read_b128 v[84:87], v183 offset:128\n\t"
        "v_add_u32 v183, s9, v179\n\t"
        "v_mfma_f32_16x16x32_bf16 v[76:79], a[40:43], v[88:91], v[76:79]\n\t"
        "v_max_u32 v183, v183, v139\n\t"
        "v_xor_b32 v140, v181, v140\n\t"
        "v_mfma_f32_16x16x32_bf16 v[80:83], a[44:47], v[88:91], v[80:83]\n\t"
        "v_mfma_f32_16x16x32_bf16 v[72:75], a[72:75], v[92:95], v[72:75]\n\t"
        "ds_read_b128 v[88:91], v151 offset:0\n\t"
        "v_mfma_f32_16x16x32_bf16 v[76:79], a[76:79], v[92:95], v[76:79]\n\t"
        "v_mfma_f32_16x16x32_bf16 v[80:83], a[80:83], v[92:95], v[80:83]\n\t"
        "v_mfma_f32_16x16x32_bf16 v[72:75], a[108:111], v[96:99], v[72:75]\n\t"
        "ds_read_b128 v[92:95], v151 offset:64\n\t"
        "v_mfma_f32_16x16x32_bf16 v[76:79], a[112:115], v[96:99], v[76:79]\n\t"
        "v_mfma_f32_16x16x32_bf16 v[80:83], a[116:119], v[96:99], v[80:83]\n\t"
        "ds_read_b128 v[96:99], v151 offset:128\n\t"
        "v_add_u32 v151, s9, v180\n\t"
        "v_mfma_f32_16x16x32_bf16 v[72:75], a[144:147], v[100:103], v[72:75]\n\t"
        "v_max_u32 v151, v151, v140\n\t"
        "v_mfma_f32_16x16x32_bf16 v[76:79], a[148:151], v[100:103], v[76:79]\n\t"
        "v_mfma_f32_16x16x32_bf16 v[80:83], a[152:155], v[100:103], v[80:83]\n\t"
        "ds_read_b128 v[100:103], v183 offset:0\n\t"
        "s_waitcnt lgkmcnt(4)\n\t"
        "v_mfma_f32_16x16x32_bf16 v[72:75], a[180:183], v[104:107], v[72:75]\n\t"
        "v_mfma_f32_16x16x32_bf16 v[76:79], a[184:187], v[104:107], v[76:79]\n\t"
        "v_mfma_f32_16x16x32_bf16 v[80:83], a[188:191], v[104:107], v[80:83]\n\t"
        "ds_read_b128 v[104:107], v183 offset:64\n\t"
        "v_mfma_f32_16x16x32_bf16 v[72:75], a[216:219], v[108:111], v[72:75]\n\t"
        "v_mfma_f32_16x16x32_bf16 v[76:79], a[220:223], v[108:111], v[76:79]\n\t"
        "v_mfma_f32_16x16x32_bf16 v[80:83], a[224:227], v[108:111], v[80:83]\n\t"
        "ds_read_b128 v[108:111], v183 offset:128\n\t"
        "v_mfma_f32_16x16x32_bf16 v[72:75], a[252:255], v[112:115], v[72:75]\n\t"
        "v_mfma_f32_16x16x32_bf16 v[76:79], v[188:191], v[112:115], v[76:79]\n\t"
        "v_mfma_f32_16x16x32_bf16 v[80:83], v[192:195], v[112:115], v[80:83]\n\t"
        "ds_read_b128 v[112:115], v151 offset:0\n\t"
        "v_mfma_f32_16x16x32_bf16 v[72:75], v[220:223], v[84:87], v[72:75]\n\t"
        "v_mfma_f32_16x16x32_bf16 v[76:79], v[224:227], v[84:87], v[76:79]\n\t"
        "v_mfma_f32_16x16x32_bf16 v[80:83], v[228:231], v[84:87], v[80:83]\n\t"
        "ds_read_b128 v[84:87], v151 offset:64\n\t"
        "s_waitcnt lgkmcnt(4)\n\t"
        "v_mfma_f32_16x16x32_bf16 v[72:75], a[24:27], v[88:91], v[72:75]\n\t"
        "v_mfma_f32_16x16x32_bf16 v[76:79], a[28:31], v[88:91], v[76:79]\n\t"
        "v_mfma_f32_16x16x32_bf16 v[80:83], a[32:35], v[88:91], v[80:83]\n\t"
        "ds_read_b128 v[88:91], v151 offset:128\n\t"
        "v_mfma_f32_16x16x32_bf16 v[80:83], a[68:71], v[92:95], v[80:83]\n\t"
        "v_mfma_f32_16x16x32_bf16 v[80:83], a[104:107], v[96:99], v[80:83]\n\t"
        "v_mfma_f32_16x16x32_bf16 v[80:83], a[140:143], v[100:103], v[80:83]\n\t"
        "s_waitcnt lgkmcnt(3)\n\t"
        "v_mfma_f32_16x16x32_bf16 v[80:83], a[176:179], v[104:107], v[80:83]\n\t"
        "v_mfma_f32_16x16x32_bf16 v[80:83], a[212:215], v[108:111], v[80:83]\n\t"
        "s_waitcnt lgkmcnt(2)\n\t"
        "v_mfma_f32_16x16x32_bf16 v[80:83], a[248:251], v[112:115], v[80:83]\n\t"
        "s_waitcnt lgkmcnt(1)\n\t"
        "v_mfma_f32_16x16x32_bf16 v[80:83], v[216:219], v[84:87], v[80:83]\n\t"
        "s_waitcnt lgkmcnt(0)\n\t"
        "v_mfma_f32_16x16x32_bf16 v[80:83], v[252:255], v[88:91], v[80:83]\n\t"
        "v_mfma_f32_16x16x32_bf16 v[72:75], a[60:63], v[92:95], v[72:75]\n\t"
        "v_mfma_f32_16x16x32_bf16 v[76:79], a[64:67], v[92:95], v[76:79]\n\t"
        "s_nop 5\n\t"
        "v_cvt_pk_bf16_f32 v120, v80, v81\n\t"
        "v_cvt_pk_bf16_f32 v121, v82, v83\n\t"
        "v_mfma_f32_16x16x32_bf16 v[72:75], a[96:99], v[96:99], v[72:75]\n\t"
        "ds_write_b64 v146, v[120:121] offset:2048\n\t"
        "v_mfma_f32_16x16x32_bf16 v[76:79], a[100:103], v[96:99], v[76:79]\n\t"
        "v_mfma_f32_16x16x32_bf16 v[72:75], a[132:135], v[100:103], v[72:75]\n\t"
        "v_mfma_f32_16x16x32_bf16 v[76:79], a[136:139], v[100:103], v[76:79]\n\t"
        "s_waitcnt lgkmcnt(0)\n\t"
        "s_barrier\n\t"
        "v_mfma_f32_16x16x32_bf16 v[72:75], a[168:171], v[104:107], v[72:75]\n\t"
        "ds_read_b128 v[124:127], v147 offset:0\n\t"
        "v_readlane_b32 s55, v155, 51\n\t"
        "v_mfma_f32_16x16x32_bf16 v[76:79], a[172:175], v[104:107], v[76:79]\n\t"
        "v_mfma_f32_16x16x32_bf16 v[72:75], a[204:207], v[108:111], v[72:75]\n\t"
        "v_mfma_f32_16x16x32_bf16 v[76:79], a[208:211], v[108:111], v[76:79]\n\t"
        "v_mfma_f32_16x16x32_bf16 v[72:75], a[240:243], v[112:115], v[72:75]\n\t"
        "v_mfma_f32_16x16x32_bf16 v[76:79], a[244:247], v[112:115], v[76:79]\n\t"
        "v_mfma_f32_16x16x32_bf16 v[72:75], v[208:211], v[84:87], v[72:75]\n\t"
        "v_mfma_f32_16x16x32_bf16 v[76:79], v[212:215], v[84:87], v[76:79]\n\t"
        "v_mfma_f32_16x16x32_bf16 v[72:75], v[244:247], v[88:91], v[72:75]\n\t"
        "v_mfma_f32_16x16x32_bf16 v[76:79], v[248:251], v[88:91], v[76:79]\n\t"
        "s_nop 6\n\t"
        "v_cvt_pk_bf16_f32 v116, v72, v73\n\t"
        "v_cvt_pk_bf16_f32 v117, v74, v75\n\t"
        "v_cvt_pk_bf16_f32 v118, v76, v77\n\t"
        "v_cvt_pk_bf16_f32 v119, v78, v79\n\t"
        "s_nop 0\n\t"
        "v_permlane16_swap_b32 v116, v118\n\t"
        "v_permlane16_swap_b32 v117, v119\n\t"
        "buffer_store_dwordx4 v[116:119], v148, s[28:31], s55 offen  \n\t"
        "s_waitcnt lgkmcnt(0)\n\t"
        "buffer_store_dwordx4 v[124:127], v150, s[28:31], s52 offen  \n\t"
        "s_andn2_b32 s6, s6, 2\n\t"
        "s_cbranch_scc1 99f\n\t"
        "s_endpgm\n\t"
        "21:\n\t"
        "s_mul_i32 s69, s68, 16384\n\t"
        "buffer_load_dwordx4 v142, s[24:27], s36 offen offset:2048 lds\n\t"
        "s_lshl_b32 s70, s49, 7\n\t"
        "s_lshl_b32 s71, s49, 5\n\t"
        "s_waitcnt vmcnt(19)\n\t"
        "v_add_u32 v128, s69, v128\n\t"
        "v_add_u32 v129, s69, v129\n\t"
        "v_add_u32 v130, s69, v130\n\t"
        "v_xad_u32 v133, v131, s71, 64\n\t"
        "v_add_u32 v131, s70, v131\n\t"
        "v_xor_b32 v132, 32, v131\n\t"
        "v_subrev_u32 v138, 43008, v131\n\t"
        "s_add_u32 m0, s35, 4096\n\t"
        "s_add_u32 s43, s34, 43008\n\t"
        "buffer_load_dwordx4 v143, s[16:19], s43 offen  lds\n\t"
        "v_subrev_u32 v139, 43008, v132\n\t"
        "v_subrev_u32 v149, 43008, v133\n\t"
        "s_waitcnt vmcnt(18)\n\t"
        "ds_bpermute_b32 v67, v81, v155 offset:172\n\t"
        "ds_bpermute_b32 v78, v81, v155 offset:148\n\t"
        "ds_bpermute_b32 v79, v81, v155 offset:160\n\t"
        "v_readlane_b32 s69, v155, 34\n\t"
        "v_readlane_b32 s70, v155, 35\n\t"
        "v_readlane_b32 s46, v155, 43\n\t"
        "v_readlane_b32 s47, v155, 44\n\t"
        "s_add_u32 m0, s35, 20480\n\t"
        "s_add_u32 s43, s34, 215040\n\t"
        "buffer_load_dwordx4 v143, s[16:19], s43 offen  lds\n\t"
        "v_readlane_b32 s55, v155, 45\n\t"
        "s_lshl_b32 s71, s49, 3\n\t"
        "v_add_u32 v82, s71, v82\n\t"
        "v_add_u32 v53, v70, v83\n\t"
        "v_mad_u32_u24 v70, v52, s69, v70\n\t"
        "v_mad_u32_u24 v70, v158, s70, v70\n\t"
        "s_mul_i32 s71, s69, s68\n\t"
        "s_waitcnt lgkmcnt(2)\n\t"
        "v_mad_u32_u24 v67, v82, s70, v67\n\t"
        "v_add_u32 v67, s71, v67\n\t"
        "v_readlane_b32 s11, v155, 30\n\t"
        "s_movk_i32 s10, 223\n\t"
        "v_mov_b32 v75, 0x7ff00000\n\t"
        "v_readlane_b32 s12, v155, 24\n\t"
        "v_readlane_b32 s13, v155, 25\n\t"
        "v_readlane_b32 s70, v155, 27\n\t"
        "s_nop 1\n\t"
        "s_or_b32 s42, s42, s70\n\t"
        "s_bfe_u32 s70, s12, 524288\n\t"
        "s_bfe_u32 s71, s13, 524288\n\t"
        "v_cmp_gt_u32 vcc, s71, v71\n\t"
        "v_add3_u32 v73, v71, s70, -2\n\t"
        "v_med3_i32 v73, v73, 0, s10\n\t"
        "v_cndmask_b32 v74, v75, v72, vcc\n\t"
        "v_mad_u32_u24 v134, v73, s11, v74\n\t"
        "s_bfe_u32 s70, s12, 524296\n\t"
        "s_bfe_u32 s71, s13, 524296\n\t"
        "v_cmp_gt_u32 vcc, s71, v71\n\t"
        "v_add3_u32 v73, v71, s70, -2\n\t"
        "v_med3_i32 v73, v73, 0, s10\n\t"
        "v_cndmask_b32 v74, v75, v72, vcc\n\t"
        "v_mad_u32_u24 v135, v73, s11, v74\n\t"
        "s_bfe_u32 s70, s12, 524304\n\t"
        "s_bfe_u32 s71, s13, 524304\n\t"
        "v_cmp_gt_u32 vcc, s71, v71\n\t"
        "v_add3_u32 v73, v71, s70, -2\n\t"
        "v_med3_i32 v73, v73, 0, s10\n\t"
        "v_cndmask_b32 v74, v75, v72, vcc\n\t"
        "v_mad_u32_u24 v136, v73, s11, v74\n\t"
        "s_bfe_u32 s70, s12, 524312\n\t"
        "s_bfe_u32 s71, s13, 524312\n\t"
        "v_cmp_gt_u32 vcc, s71, v71\n\t"
        "v_add3_u32 v73, v71, s70, -2\n\t"
        "v_med3_i32 v73, v73, 0, s10\n\t"
        "v_cndmask_b32 v74, v75, v72, vcc\n\t"
        "v_mad_u32_u24 v137, v73, s11, v74\n\t"
        "v_readlane_b32 s44, v155, 32\n\t"
        "v_readlane_b32 s45, v155, 33\n\t"
        "v_readlane_b32 s62, v155, 36\n\t"
        "s_mov_b32 exec_lo, 0\n\t"
        "s_mov_b32 exec_hi, 0xfff00000\n\t"
        "s_waitcnt vmcnt(18)\n\t"
        "ds_write_b32 v119, v155\n\t"
        "s_mov_b64 exec, -1\n\t"
        "s_lshl_b32 s70, s62, 5\n\t"
        "s_sub_u32 s71, s70, 16384\n\t"
        "s_mul_i32 s71, s71, s68\n\t"
        "v_add_u32 v172, s71, v128\n\t"
        "v_add_u32 v173, s71, v129\n\t"
        "v_add_u32 v174, s71, v130\n\t"
        "s_add_u32 s71, s71, s70\n\t"
        "v_add_u32 v175, s71, v128\n\t"
        "v_add_u32 v176, s71, v129\n\t"
        "v_add_u32 v177, s71, v130\n\t"
        "s_add_u32 s71, s71, s70\n\t"
        "v_add_u32 v178, s71, v128\n\t"
        "v_add_u32 v179, s71, v129\n\t"
        "v_add_u32 v180, s71, v130\n\t"
        "s_cmp_eq_u32 s69, 36\n\t"
        "s_cselect_b64 exec, -1, 0\n\t"
        "v_swap_b32 v68, v69\n\t"
        "v_swap_b32 v173, v175\n\t"
        "v_swap_b32 v174, v178\n\t"
        "v_swap_b32 v177, v179\n\t"
        "s_mov_b64 exec, -1\n\t"
        "v_add3_u32 v68, v68, v52, -1\n\t"
        "v_add3_u32 v69, v69, v158, -1\n\t"
        "v_cvt_f32_i32 v68, v68\n\t"
        "v_cvt_f32_i32 v69, v69\n\t"
        "s_add_u32 s70, s68, 1\n\t"
        "v_cvt_f32_u32 v73, s70\n\t"
        "s_waitcnt lgkmcnt(1)\n\t"
        "v_add_f32 v78, v78, v73\n\t"
        "v_add_u32 v73, 1, v82\n\t"
        "v_cvt_f32_u32 v73, v73\n\t"
        "v_add_f32 v79, v79, v73\n\t"
        "s_mul_i32 s70, s4, 384\n\t"
        "v_add_u32 v80, s70, v80\n\t"
        "s_mov_b64 exec, 0\n\t"
        "s_cmp_eq_u32 s4, 0\n\t"
        "s_cbranch_scc1 .LpX0%=\n\t"
        "s_cmp_eq_u32 s4, 1\n\t"
        "s_cbranch_scc1 .LpX1%=\n\t"
        "s_cmp_eq_u32 s4, 2\n\t"
        "s_cbranch_scc1 .LpX2%=\n\t"
        ".LpX2%=:\n\t"
        ".LrX%=:\n\t"
        "s_mov_b64 exec, -1\n\t"
        "s_waitcnt vmcnt(9) lgkmcnt(0)\n\t"
        "s_barrier\n\t"
        "ds_read_b64_tr_b16 a[0:1], v138 offset:6144\n\t"
        "ds_read_b64_tr_b16 a[2:3], v138 offset:6912\n\t"
        "ds_read_b64_tr_b16 a[4:5], v139 offset:6144\n\t"
        "ds_read_b64_tr_b16 a[6:7], v139 offset:6912\n\t"
        "ds_read_b64_tr_b16 a[8:9], v149 offset:6144\n\t"
        "ds_read_b64_tr_b16 a[10:11], v149 offset:6912\n\t"
        "ds_read_b128 v[84:87], v128 offset:8192\n\t"
        "ds_read_b64_tr_b16 a[12:13], v138 offset:12288\n\t"
        "ds_read_b64_tr_b16 a[14:15], v138 offset:13056\n\t"
        "ds_read_b64_tr_b16 a[16:17], v139 offset:12288\n\t"
        "ds_read_b64_tr_b16 a[18:19], v139 offset:13056\n\t"
        "ds_read_b64_tr_b16 a[20:21], v149 offset:12288\n\t"
        "ds_read_b64_tr_b16 a[22:23], v149 offset:13056\n\t"
        "ds_read_b128 v[88:91], v128 offset:8192\n\t"
        "s_add_u32 m0, s37, -12288\n\t"
        "s_add_u32 s36, s36, s39\n\t"
        "s_waitcnt lgkmcnt(7)\n\t"
        "v_mfma_f32_16x16x32_bf16 v[12:15], a[0:3], v[84:87], 0\n\t"
        "buffer_load_dwordx4 v140, s[24:27], s36 offen offset:0 lds\n\t"
        "buffer_load_dwordx4 v141, s[24:27], s36 offen offset:1024 lds\n\t"
        "buffer_load_dwordx4 v142, s[24:27], s36 offen offset:2048 lds\n\t"
        "s_add_u32 m0, s35, 16384\n\t"
        "s_add_u32 s43, s34, 172032\n\t"
        "buffer_load_dwordx4 v143, s[16:19], s43 offen  lds\n\t"
        "s_add_u32 m0, s35, 32768\n\t"
        "s_add_u32 s43, s34, 344064\n\t"
        "s_min_u32 s43, s43, 9590784\n\t"
        "buffer_load_dwordx4 v143, s[16:19], s43 offen  lds\n\t"
        "s_waitcnt vmcnt(10)\n\t"
        "s_barrier\n\t"
        "ds_read_b128 v[92:95], v128 offset:12288\n\t"
        "s_add_u32 m0, s37, 0\n\t"
        "ds_read_b64_tr_b16 a[24:25], v138 offset:0\n\t"
        "s_add_u32 s36, s36, s38\n\t"
        "v_mfma_f32_16x16x32_bf16 v[16:19], a[4:7], v[84:87], 0\n\t"
        "buffer_load_dwordx4 v140, s[24:27], s36 offen offset:0 lds\n\t"
        "ds_read_b64_tr_b16 a[26:27], v138 offset:768\n\t"
        "v_lshrrev_b32 v73, 5, v156\n\t"
        "v_mfma_f32_16x16x32_bf16 v[20:23], a[8:11], v[84:87], 0\n\t"
        "buffer_load_dwordx4 v141, s[24:27], s36 offen offset:1024 lds\n\t"
        "ds_read_b64_tr_b16 a[28:29], v139 offset:0\n\t"
        "v_mul_u32_u24 v73, 8064, v73\n\t"
        "s_waitcnt lgkmcnt(4)\n\t"
        "v_mfma_f32_16x16x32_bf16 v[24:27], a[12:15], v[88:91], 0\n\t"
        "ds_read_b64_tr_b16 a[30:31], v139 offset:768\n\t"
        "ds_read_b64_tr_b16 a[32:33], v149 offset:0\n\t"
        "ds_read_b64_tr_b16 a[34:35], v149 offset:768\n\t"
        "v_and_b32 v55, 496, v140\n\t"
        "v_mfma_f32_16x16x32_bf16 v[28:31], a[16:19], v[88:91], 0\n\t"
        "buffer_load_dwordx4 v142, s[24:27], s36 offen offset:2048 lds\n\t"
        "ds_read_b128 v[96:99], v128 offset:8192\n\t"
        "v_add_u32 v55, v55, v73\n\t"
        "v_mfma_f32_16x16x32_bf16 v[32:35], a[20:23], v[88:91], 0\n\t"
        "ds_read_b128 v[100:103], v128 offset:12288\n\t"
        "v_lshl_add_u32 v61, v158, 2, v73\n\t"
        "s_waitcnt lgkmcnt(8)\n\t"
        "v_mfma_f32_16x16x32_bf16 v[24:27], a[0:3], v[92:95], v[24:27]\n\t"
        "s_mul_i32 s48, s41, 36\n\t"
        "v_readlane_b32 s69, v155, 31\n\t"
        "v_mfma_f32_16x16x32_bf16 v[28:31], a[4:7], v[92:95], v[28:31]\n\t"
        "buffer_load_dword v67, v67, s[20:23], 0 offen offset:32\n\t"
        "v_mov_b32 v154, s69\n\t"
        "v_mfma_f32_16x16x32_bf16 v[32:35], a[8:11], v[92:95], v[32:35]\n\t"
        "v_mov_b32 v119, s62\n\t"
        "v_add_u32 v117, s37, v117\n\t"
        "v_mfma_f32_16x16x32_bf16 v[36:39], a[12:15], v[92:95], 0\n\t"
        "v_add_u32 v118, s35, v118\n\t"
        "v_add_u32 v185, s35, v185\n\t"
        "v_mfma_f32_16x16x32_bf16 v[40:43], a[16:19], v[92:95], 0\n\t"
        "v_add_u32 v186, s37, v186\n\t"
        "v_readlane_b32 s53, v155, 28\n\t"
        "v_mfma_f32_16x16x32_bf16 v[44:47], a[20:23], v[92:95], 0\n\t"
        "s_waitcnt vmcnt(9) lgkmcnt(2)\n\t"
        "s_barrier\n\t"
        "buffer_load_dword v64, v70, s[20:23], s46 offen\n\t"
        "ds_read_b128 v[104:107], v128 offset:4096\n\t"
        "s_waitcnt lgkmcnt(2)\n\t"
        "v_mfma_f32_16x16x32_bf16 v[0:3], a[24:27], v[96:99], 0\n\t"
        "ds_read_b64_tr_b16 a[36:37], v138 offset:18432\n\t"
        "ds_read_b64_tr_b16 a[38:39], v138 offset:19200\n\t"
        "s_lshl_b32 s69, s68, 10\n\t"
        "v_mfma_f32_16x16x32_bf16 v[4:7], a[28:31], v[96:99], 0\n\t"
        "buffer_load_dword v66, v70, s[20:23], s55 offen\n\t"
        "s_add_u32 m0, s37, 12288\n\t"
        "ds_read_b64_tr_b16 a[40:41], v139 offset:18432\n\t"
        "s_add_u32 s36, s36, s5\n\t"
        "v_mfma_f32_16x16x32_bf16 v[8:11], a[32:35], v[96:99], 0\n\t"
        "ds_read_b64_tr_b16 a[42:43], v139 offset:19200\n\t"
        "ds_read_b64_tr_b16 a[44:45], v149 offset:18432\n\t"
        "s_add_u32 s69, s69, 135168\n\t"
        "s_waitcnt lgkmcnt(6)\n\t"
        "v_mfma_f32_16x16x32_bf16 v[12:15], a[24:27], v[100:103], v[12:15]\n\t"
        "ds_read_b64_tr_b16 a[46:47], v149 offset:19200\n\t"
        "ds_read_b64_tr_b16 a[48:49], v138 offset:24576\n\t"
        "ds_read_b64_tr_b16 a[50:51], v138 offset:25344\n\t"
        "s_lshl_b32 s70, s49, 5\n\t"
        "v_mfma_f32_16x16x32_bf16 v[16:19], a[28:31], v[100:103], v[16:19]\n\t"
        "buffer_load_dwordx4 v140, s[24:27], s36 offen offset:0 lds\n\t"
        "ds_read_b64_tr_b16 a[52:53], v139 offset:24576\n\t"
        "ds_read_b64_tr_b16 a[54:55], v139 offset:25344\n\t"
        "v_xor_b32 v146, s70, v146\n\t"
        "v_mfma_f32_16x16x32_bf16 v[20:23], a[32:35], v[100:103], v[20:23]\n\t"
        "ds_read_b64_tr_b16 a[56:57], v149 offset:24576\n\t"
        "ds_read_b64_tr_b16 a[58:59], v149 offset:25344\n\t"
        "ds_read_b128 v[108:111], v128 offset:4160\n\t"
        "v_add_u32 v146, s69, v146\n\t"
        "s_waitcnt lgkmcnt(12)\n\t"
        "v_mfma_f32_16x16x32_bf16 v[0:3], a[0:3], v[104:107], v[0:3]\n\t"
        "s_lshl_b32 s70, s49, 11\n\t"
        "s_add_u32 s70, s70, s69\n\t"
        "v_mfma_f32_16x16x32_bf16 v[4:7], a[4:7], v[104:107], v[4:7]\n\t"
        "buffer_load_dwordx4 v141, s[24:27], s36 offen offset:1024 lds\n\t"
        "v_add_u32 v147, s70, v147\n\t"
        "v_mfma_f32_16x16x32_bf16 v[8:11], a[8:11], v[104:107], v[8:11]\n\t"
        "s_mul_i32 s41, s41, 192\n\t"
        "s_mul_i32 s69, s68, 172032\n\t"
        "v_mfma_f32_16x16x32_bf16 v[12:15], a[12:15], v[104:107], v[12:15]\n\t"
        "s_add_u32 s41, s41, s69\n\t"
        "s_mul_i32 s69, s49, 43008\n\t"
        "v_mfma_f32_16x16x32_bf16 v[16:19], a[16:19], v[104:107], v[16:19]\n\t"
        "s_add_u32 s50, s41, s69\n\t"
        "s_lshl_b32 s70, s49, 7\n\t"
        "v_mfma_f32_16x16x32_bf16 v[20:23], a[20:23], v[104:107], v[20:23]\n\t"
        "v_add_u32 v144, s70, v144\n\t"
        "s_mul_i32 s69, s68, 768\n\t"
        "v_add_u32 v187, s69, v187\n\t"
        "s_waitcnt lgkmcnt(0)\n\t"
        "v_mfma_f32_16x16x32_bf16 v[0:3], a[36:39], v[108:111], v[0:3]\n\t"
        "buffer_load_dwordx4 v142, s[24:27], s36 offen offset:2048 lds\n\t"
        "s_add_u32 m0, s35, 0\n\t"
        "ds_read_b128 v[112:115], v128 offset:8256\n\t"
        "s_add_u32 s43, s34, 0\n\t"
        "v_mfma_f32_16x16x32_bf16 v[4:7], a[40:43], v[108:111], v[4:7]\n\t"
        "v_readlane_b32 s9, v155, 46\n\t"
        "s_sub_u32 s10, 43200, s11\n\t"
        "v_mfma_f32_16x16x32_bf16 v[8:11], a[44:47], v[108:111], v[8:11]\n\t"
        "s_mul_i32 s10, s10, s68\n\t"
        "s_sub_u32 s11, s11, 192\n\t"
        "v_mfma_f32_16x16x32_bf16 v[12:15], a[48:51], v[108:111], v[12:15]\n\t"
        "v_mad_u32_u24 v148, v158, s11, v144\n\t"
        "v_add_u32 v148, s10, v148\n\t"
        "v_mfma_f32_16x16x32_bf16 v[16:19], a[52:55], v[108:111], v[16:19]\n\t"
        "v_lshrrev_b32 v73, 2, v157\n\t"
        "v_mad_u32_u24 v150, v73, s11, v145\n\t"
        "v_mfma_f32_16x16x32_bf16 v[20:23], a[56:59], v[108:111], v[20:23]\n\t"
        "s_lshl_b32 s70, s49, 1\n\t"
        "s_add_u32 s70, s70, 49\n\t"
        "s_waitcnt lgkmcnt(0)\n\t"
        "v_mfma_f32_16x16x32_bf16 v[12:15], a[36:39], v[112:115], v[12:15]\n\t"
        "ds_read_b128 v[84:87], v128 offset:12352\n\t"
        "v_readlane_b32 s52, v155, s70\n\t"
        "v_mfma_f32_16x16x32_bf16 v[16:19], a[40:43], v[112:115], v[16:19]\n\t"
        "s_nop 0\n\t"
        "s_add_u32 s52, s52, s10\n\t"
        "v_mfma_f32_16x16x32_bf16 v[20:23], a[44:47], v[112:115], v[20:23]\n\t"
        "v_mfma_f32_16x16x32_bf16 v[24:27], a[48:51], v[112:115], v[24:27]\n\t"
        "buffer_load_dwordx4 v143, s[16:19], s43 offen  lds\n\t"
        "s_add_u32 m0, s35, 36864\n\t"
        "s_add_u32 s43, s34, 387072\n\t"
        "s_min_u32 s43, s43, 9590784\n\t"
        "v_mfma_f32_16x16x32_bf16 v[28:31], a[52:55], v[112:115], v[28:31]\n\t"
        "v_mfma_f32_16x16x32_bf16 v[32:35], a[56:59], v[112:115], v[32:35]\n\t"
        "buffer_load_dwordx4 v143, s[16:19], s43 offen  lds\n\t"
        "s_waitcnt vmcnt(11)\n\t"
        "s_barrier\n\t"
        "ds_read_b128 v[88:91], v128 offset:16384\n\t"
        "s_add_u32 m0, s37, 24576\n\t"
        "ds_read_b64_tr_b16 a[60:61], v138 offset:30720\n\t"
        "s_add_u32 s36, s36, s39\n\t"
        "s_waitcnt lgkmcnt(2)\n\t"
        "v_mfma_f32_16x16x32_bf16 v[24:27], a[36:39], v[84:87], v[24:27]\n\t"
        "buffer_load_dwordx4 v140, s[24:27], s36 offen offset:0 lds\n\t"
        "ds_read_b64_tr_b16 a[62:63], v138 offset:31488\n\t"
        "v_mfma_f32_16x16x32_bf16 v[28:31], a[40:43], v[84:87], v[28:31]\n\t"
        "ds_read_b64_tr_b16 a[64:65], v139 offset:30720\n\t"
        "ds_read_b64_tr_b16 a[66:67], v139 offset:31488\n\t"
        "v_mfma_f32_16x16x32_bf16 v[32:35], a[44:47], v[84:87], v[32:35]\n\t"
        "buffer_load_dwordx4 v141, s[24:27], s36 offen offset:1024 lds\n\t"
        "ds_read_b64_tr_b16 a[68:69], v149 offset:30720\n\t"
        "v_mfma_f32_16x16x32_bf16 v[36:39], a[48:51], v[84:87], v[36:39]\n\t"
        "ds_read_b64_tr_b16 a[70:71], v149 offset:31488\n\t"
        "ds_read_b128 v[92:95], v128 offset:8256\n\t"
        "v_mfma_f32_16x16x32_bf16 v[40:43], a[52:55], v[84:87], v[40:43]\n\t"
        "ds_read_b128 v[96:99], v128 offset:12352\n\t"
        "ds_read_b128 v[100:103], v128 offset:16448\n\t"
        "v_mfma_f32_16x16x32_bf16 v[44:47], a[56:59], v[84:87], v[44:47]\n\t"
        "ds_read_b64_tr_b16 a[72:73], v138 offset:36864\n\t"
        "ds_read_b64_tr_b16 a[74:75], v138 offset:37632\n\t"
        "s_waitcnt lgkmcnt(11)\n\t"
        "v_mfma_f32_16x16x32_bf16 v[24:27], a[24:27], v[88:91], v[24:27]\n\t"
        "buffer_load_dwordx4 v142, s[24:27], s36 offen offset:2048 lds\n\t"
        "ds_read_b64_tr_b16 a[76:77], v139 offset:36864\n\t"
        "v_mfma_f32_16x16x32_bf16 v[28:31], a[28:31], v[88:91], v[28:31]\n\t"
        "ds_read_b64_tr_b16 a[78:79], v139 offset:37632\n\t"
        "ds_read_b64_tr_b16 a[80:81], v149 offset:36864\n\t"
        "v_mfma_f32_16x16x32_bf16 v[32:35], a[32:35], v[88:91], v[32:35]\n\t"
        "ds_read_b64_tr_b16 a[82:83], v149 offset:37632\n\t"
        "v_mfma_f32_16x16x32_bf16 v[36:39], a[0:3], v[88:91], v[36:39]\n\t"
        "v_mfma_f32_16x16x32_bf16 v[40:43], a[4:7], v[88:91], v[40:43]\n\t"
        "buffer_load_dwordx4 v[48:51], v55, s[20:23], s48 offen\n\t"
        "v_mfma_f32_16x16x32_bf16 v[44:47], a[8:11], v[88:91], v[44:47]\n\t"
        "s_waitcnt lgkmcnt(8)\n\t"
        "v_mfma_f32_16x16x32_bf16 v[0:3], a[60:63], v[92:95], v[0:3]\n\t"
        "v_mfma_f32_16x16x32_bf16 v[4:7], a[64:67], v[92:95], v[4:7]\n\t"
        "v_mfma_f32_16x16x32_bf16 v[8:11], a[68:71], v[92:95], v[8:11]\n\t"
        "buffer_load_dword v61, v61, s[20:23], s48 offen offset:512\n\t"
        "s_add_u32 m0, s37, 36864\n\t"
        "s_add_u32 s36, s36, s38\n\t"
        "s_waitcnt lgkmcnt(4)\n\t"
        "v_mfma_f32_16x16x32_bf16 v[12:15], a[60:63], v[96:99], v[12:15]\n\t"
        "v_mfma_f32_16x16x32_bf16 v[16:19], a[64:67], v[96:99], v[16:19]\n\t"
        "v_mfma_f32_16x16x32_bf16 v[20:23], a[68:71], v[96:99], v[20:23]\n\t"
        "v_mfma_f32_16x16x32_bf16 v[24:27], a[60:63], v[100:103], v[24:27]\n\t"
        "ds_read_b128 v[104:107], v128 offset:4224\n\t"
        "v_mfma_f32_16x16x32_bf16 v[28:31], a[64:67], v[100:103], v[28:31]\n\t"
        "v_mfma_f32_16x16x32_bf16 v[32:35], a[68:71], v[100:103], v[32:35]\n\t"
        "v_mfma_f32_16x16x32_bf16 v[36:39], a[36:39], v[100:103], v[36:39]\n\t"
        "ds_read_b128 v[108:111], v128 offset:8320\n\t"
        "v_mfma_f32_16x16x32_bf16 v[40:43], a[40:43], v[100:103], v[40:43]\n\t"
        "v_mfma_f32_16x16x32_bf16 v[44:47], a[44:47], v[100:103], v[44:47]\n\t"
        "s_waitcnt lgkmcnt(1)\n\t"
        "v_mfma_f32_16x16x32_bf16 v[0:3], a[72:75], v[104:107], v[0:3]\n\t"
        "s_waitcnt vmcnt(13)\n\t"
        "s_barrier\n\t"
        "buffer_load_dwordx4 v140, s[24:27], s36 offen offset:0 lds\n\t"
        "ds_read_b128 v[112:115], v128 offset:12416\n\t"
        "ds_read_b64_tr_b16 a[84:85], v131 offset:0\n\t"
        "v_mfma_f32_16x16x32_bf16 v[4:7], a[76:79], v[104:107], v[4:7]\n\t"
        "ds_read_b64_tr_b16 a[86:87], v131 offset:768\n\t"
        "ds_read_b64_tr_b16 a[88:89], v132 offset:0\n\t"
        "ds_read_b64_tr_b16 a[90:91], v132 offset:768\n\t"
        "v_mfma_f32_16x16x32_bf16 v[8:11], a[80:83], v[104:107], v[8:11]\n\t"
        "ds_read_b64_tr_b16 a[92:93], v133 offset:0\n\t"
        "ds_read_b64_tr_b16 a[94:95], v133 offset:768\n\t"
        "ds_read_b128 v[84:87], v128 offset:4224\n\t"
        "s_waitcnt lgkmcnt(8)\n\t"
        "v_mfma_f32_16x16x32_bf16 v[12:15], a[72:75], v[108:111], v[12:15]\n\t"
        "ds_read_b64_tr_b16 a[96:97], v131 offset:6144\n\t"
        "ds_read_b64_tr_b16 a[98:99], v131 offset:6912\n\t"
        "ds_read_b64_tr_b16 a[100:101], v132 offset:6144\n\t"
        "v_mfma_f32_16x16x32_bf16 v[16:19], a[76:79], v[108:111], v[16:19]\n\t"
        "buffer_load_dwordx4 v141, s[24:27], s36 offen offset:1024 lds\n\t"
        "ds_read_b64_tr_b16 a[102:103], v132 offset:6912\n\t"
        "ds_read_b64_tr_b16 a[104:105], v133 offset:6144\n\t"
        "v_mfma_f32_16x16x32_bf16 v[20:23], a[80:83], v[108:111], v[20:23]\n\t"
        "ds_read_b64_tr_b16 a[106:107], v133 offset:6912\n\t"
        "ds_read_b128 v[88:91], v128 offset:8320\n\t"
        "s_waitcnt lgkmcnt(14)\n\t"
        "v_mfma_f32_16x16x32_bf16 v[24:27], a[72:75], v[112:115], v[24:27]\n\t"
        "ds_read_b128 v[92:95], v128 offset:12416\n\t"
        "v_mfma_f32_16x16x32_bf16 v[28:31], a[76:79], v[112:115], v[28:31]\n\t"
        "ds_read_b128 v[96:99], v128 offset:16512\n\t"
        "v_mfma_f32_16x16x32_bf16 v[32:35], a[80:83], v[112:115], v[32:35]\n\t"
        "buffer_load_dwordx4 v142, s[24:27], s36 offen offset:2048 lds\n\t"
        "v_readlane_b32 s40, v155, 12\n\t"
        "v_readlane_b32 s43, v155, 0\n\t"
        "s_waitcnt lgkmcnt(9)\n\t"
        "v_mfma_f32_16x16x32_bf16 v[12:15], a[84:87], v[84:87], v[12:15]\n\t"
        "s_add_u32 m0, s40, s35\n\t"
        "v_mfma_f32_16x16x32_bf16 v[16:19], a[88:91], v[84:87], v[16:19]\n\t"
        "v_mfma_f32_16x16x32_bf16 v[20:23], a[92:95], v[84:87], v[20:23]\n\t"
        "v_readlane_b32 s46, v155, 37\n\t"
        "v_readlane_b32 s47, v155, 40\n\t"
        "s_waitcnt vmcnt(14)\n\t"
        "v_perm_b32 v56, v64, v64, s44\n\t"
        "s_waitcnt lgkmcnt(2)\n\t"
        "v_mfma_f32_16x16x32_bf16 v[0:3], a[96:99], v[88:91], v[0:3]\n\t"
        "buffer_load_dwordx4 v134, s[16:19], s43 offen  lds\n\t"
        "v_readlane_b32 s40, v155, 13\n\t"
        "v_mfma_f32_16x16x32_bf16 v[4:7], a[100:103], v[88:91], v[4:7]\n\t"
        "v_readlane_b32 s43, v155, 1\n\t"
        "s_add_u32 m0, s40, s35\n\t"
        "v_mfma_f32_16x16x32_bf16 v[8:11], a[104:107], v[88:91], v[8:11]\n\t"
        "v_perm_b32 v57, v64, v64, s45\n\t"
        "v_add_f32 v58, s46, v68\n\t"
        "v_mfma_f32_16x16x32_bf16 v[24:27], a[84:87], v[88:91], v[24:27]\n\t"
        "v_add_f32 v59, s47, v69\n\t"
        "v_add_f32 v56, v56, v58\n\t"
        "v_mfma_f32_16x16x32_bf16 v[28:31], a[88:91], v[88:91], v[28:31]\n\t"
        "v_add_f32 v57, v57, v59\n\t"
        "v_cvt_pk_bf16_f32 v52, v56, v57\n\t"
        "v_cvt_pk_bf16_f32 v58, v58, v59\n\t"
        "v_mfma_f32_16x16x32_bf16 v[32:35], a[92:95], v[88:91], v[32:35]\n\t"
        "v_cmp_ne_u32 s[64:65], v52, v58\n\t"
        "v_perm_b32 v57, v67, v67, s45\n\t"
        "s_waitcnt lgkmcnt(0)\n\t"
        "v_mfma_f32_16x16x32_bf16 v[12:15], a[96:99], v[92:95], v[12:15]\n\t"
        "v_mbcnt_lo_u32_b32 v56, s64, 1\n\t"
        "v_mbcnt_hi_u32_b32 v56, s65, v56\n\t"
        "v_mfma_f32_16x16x32_bf16 v[16:19], a[100:103], v[92:95], v[16:19]\n\t"
        "s_bcnt1_i32_b64 s61, s[64:65]\n\t"
        "v_add_u32 v54, s61, 1\n\t"
        "v_mfma_f32_16x16x32_bf16 v[20:23], a[104:107], v[92:95], v[20:23]\n\t"
        "v_lshl_add_u32 v56, v56, 3, s60\n\t"
        "v_add_f32 v57, v57, v79\n\t"
        "v_mfma_f32_16x16x32_bf16 v[36:39], a[84:87], v[92:95], v[36:39]\n\t"
        "s_mov_b64 exec, s[64:65]\n\t"
        "ds_write_b64 v56, v[52:53]\n\t"
        "s_mov_b64 exec, -1\n\t"
        "v_mfma_f32_16x16x32_bf16 v[40:43], a[88:91], v[92:95], v[40:43]\n\t"
        "buffer_load_dwordx4 v135, s[16:19], s43 offen  lds\n\t"
        "s_add_u32 m0, s37, 49152\n\t"
        "v_mfma_f32_16x16x32_bf16 v[44:47], a[92:95], v[92:95], v[44:47]\n\t"
        "s_waitcnt vmcnt(10)\n\t"
        "s_barrier\n\t"
        "ds_read_b128 v[100:103], v128 offset:0\n\t"
        "s_add_u32 s36, s36, s5\n\t"
        "ds_read_b128 v[104:107], v128 offset:64\n\t"
        "v_mfma_f32_16x16x32_bf16 v[24:27], a[96:99], v[96:99], v[24:27]\n\t"
        "ds_read_b128 v[108:111], v128 offset:128\n\t"
        "ds_read_b128 v[112:115], v128 offset:20480\n\t"
        "v_swap_b32 v53, v80\n\t"
        "v_mfma_f32_16x16x32_bf16 v[28:31], a[100:103], v[96:99], v[28:31]\n\t"
        "ds_read_b128 v[84:87], v128 offset:20544\n\t"
        "ds_read_b128 v[88:91], v128 offset:20608\n\t"
        "v_perm_b32 v56, v67, v67, s44\n\t"
        "v_mfma_f32_16x16x32_bf16 v[32:35], a[104:107], v[96:99], v[32:35]\n\t"
        "buffer_load_dwordx4 v140, s[24:27], s36 offen offset:0 lds\n\t"
        "ds_read_b64_tr_b16 a[108:109], v131 offset:12288\n\t"
        "v_add_f32 v56, v56, v78\n\t"
        "v_cvt_pk_bf16_f32 v52, v56, v57\n\t"
        "v_mfma_f32_16x16x32_bf16 v[36:39], a[72:75], v[96:99], v[36:39]\n\t"
        "ds_read_b64_tr_b16 a[110:111], v131 offset:13056\n\t"
        "ds_read_b64_tr_b16 a[112:113], v132 offset:12288\n\t"
        "v_cvt_pk_bf16_f32 v58, v78, v79\n\t"
        "v_mfma_f32_16x16x32_bf16 v[40:43], a[76:79], v[96:99], v[40:43]\n\t"
        "ds_read_b64_tr_b16 a[114:115], v132 offset:13056\n\t"
        "ds_read_b64_tr_b16 a[116:117], v133 offset:12288\n\t"
        "v_cmp_ne_u32 s[64:65], v52, v58\n\t"
        "v_mfma_f32_16x16x32_bf16 v[44:47], a[80:83], v[96:99], v[44:47]\n\t"
        "ds_read_b64_tr_b16 a[118:119], v133 offset:13056\n\t"
        "ds_read_b64_tr_b16 a[120:121], v131 offset:18432\n\t"
        "s_and_b64 s[64:65], s[64:65], 0xffffff\n\t"
        "s_waitcnt lgkmcnt(12)\n\t"
        "v_mfma_f32_16x16x32_bf16 v[0:3], a[12:15], v[100:103], v[0:3]\n\t"
        "buffer_load_dwordx4 v141, s[24:27], s36 offen offset:1024 lds\n\t"
        "ds_read_b64_tr_b16 a[122:123], v131 offset:19200\n\t"
        "v_mbcnt_lo_u32_b32 v56, s64, v54\n\t"
        "v_mfma_f32_16x16x32_bf16 v[4:7], a[16:19], v[100:103], v[4:7]\n\t"
        "ds_read_b64_tr_b16 a[124:125], v132 offset:18432\n\t"
        "ds_read_b64_tr_b16 a[126:127], v132 offset:19200\n\t"
        "s_bcnt1_i32_b64 s46, s[64:65]\n\t"
        "v_mfma_f32_16x16x32_bf16 v[8:11], a[20:23], v[100:103], v[8:11]\n\t"
        "ds_read_b64_tr_b16 a[128:129], v133 offset:18432\n\t"
        "ds_read_b64_tr_b16 a[130:131], v133 offset:19200\n\t"
        "s_add_u32 s61, s61, s46\n\t"
        "s_waitcnt lgkmcnt(11)\n\t"
        "v_mfma_f32_16x16x32_bf16 v[0:3], a[48:51], v[104:107], v[0:3]\n\t"
        "ds_read_b128 v[92:95], v129 offset:0\n\t"
        "v_add_u32 v54, s46, v54\n\t"
        "v_mfma_f32_16x16x32_bf16 v[4:7], a[52:55], v[104:107], v[4:7]\n\t"
        "buffer_load_dwordx4 v142, s[24:27], s36 offen offset:2048 lds\n\t"
        "v_readlane_b32 s40, v155, 14\n\t"
        "v_mfma_f32_16x16x32_bf16 v[8:11], a[56:59], v[104:107], v[8:11]\n\t"
        "ds_read_b128 v[96:99], v129 offset:4096\n\t"
        "v_readlane_b32 s43, v155, 2\n\t"
        "v_mfma_f32_16x16x32_bf16 v[0:3], a[84:87], v[108:111], v[0:3]\n\t"
        "s_add_u32 m0, s40, s35\n\t"
        "s_mov_b32 s3, s61\n\t"
        "v_mfma_f32_16x16x32_bf16 v[4:7], a[88:91], v[108:111], v[4:7]\n\t"
        "v_lshl_add_u32 v56, v56, 3, s60\n\t"
        "v_readlane_b32 s46, v155, 39\n\t"
        "v_mfma_f32_16x16x32_bf16 v[8:11], a[92:95], v[108:111], v[8:11]\n\t"
        "s_mov_b64 exec, s[64:65]\n\t"
        "ds_write_b64 v56, v[52:53]\n\t"
        "s_mov_b64 exec, -1\n\t"
        "v_mfma_f32_16x16x32_bf16 v[36:39], a[24:27], v[112:115], v[36:39]\n\t"
        "buffer_load_dwordx4 v134, s[16:19], s43 offen  lds\n\t"
        "v_readlane_b32 s40, v155, 15\n\t"
        "v_readlane_b32 s43, v155, 3\n\t"
        "v_mfma_f32_16x16x32_bf16 v[40:43], a[28:31], v[112:115], v[40:43]\n\t"
        "s_add_u32 m0, s40, s35\n\t"
        "v_add_u32 v53, 4096, v80\n\t"
        "v_mfma_f32_16x16x32_bf16 v[44:47], a[32:35], v[112:115], v[44:47]\n\t"
        "v_readlane_b32 s47, v155, 42\n\t"
        "v_perm_b32 v56, v66, v66, s44\n\t"
        "v_mfma_f32_16x16x32_bf16 v[36:39], a[60:63], v[84:87], v[36:39]\n\t"
        "v_perm_b32 v57, v66, v66, s45\n\t"
        "v_add_f32 v58, s46, v68\n\t"
        "v_mfma_f32_16x16x32_bf16 v[40:43], a[64:67], v[84:87], v[40:43]\n\t"
        "buffer_load_dwordx4 v135, s[16:19], s43 offen  lds\n\t"
        "s_add_u32 m0, s37, 0\n\t"
        "v_mfma_f32_16x16x32_bf16 v[44:47], a[68:71], v[84:87], v[44:47]\n\t"
        "s_add_u32 s36, s36, s39\n\t"
        "v_add_f32 v59, s47, v69\n\t"
        "v_mfma_f32_16x16x32_bf16 v[36:39], a[96:99], v[88:91], v[36:39]\n\t"
        "v_add_f32 v56, v56, v58\n\t"
        "v_add_f32 v57, v57, v59\n\t"
        "v_mfma_f32_16x16x32_bf16 v[40:43], a[100:103], v[88:91], v[40:43]\n\t"
        "v_max_f32 v56, 0, v56\n\t"
        "v_max_f32 v57, 0, v57\n\t"
        "v_mfma_f32_16x16x32_bf16 v[44:47], a[104:107], v[88:91], v[44:47]\n\t"
        "s_waitcnt vmcnt(12) lgkmcnt(1)\n\t"
        "s_barrier\n\t"
        "buffer_load_dwordx4 v140, s[24:27], s36 offen offset:0 lds\n\t"
        "ds_read_b64_tr_b16 a[132:133], v131 offset:24576\n\t"
        "v_mfma_f32_16x16x32_bf16 v[0:3], a[120:123], v[92:95], v[0:3]\n\t"
        "ds_read_b64_tr_b16 a[134:135], v131 offset:25344\n\t"
        "ds_read_b64_tr_b16 a[136:137], v132 offset:24576\n\t"
        "ds_read_b64_tr_b16 a[138:139], v132 offset:25344\n\t"
        "v_max_f32 v58, 0, v58\n\t"
        "v_mfma_f32_16x16x32_bf16 v[4:7], a[124:127], v[92:95], v[4:7]\n\t"
        "ds_read_b64_tr_b16 a[140:141], v133 offset:24576\n\t"
        "ds_read_b64_tr_b16 a[142:143], v133 offset:25344\n\t"
        "ds_read_b128 v[100:103], v129 offset:8192\n\t"
        "v_max_f32 v59, 0, v59\n\t"
        "v_mfma_f32_16x16x32_bf16 v[8:11], a[128:131], v[92:95], v[8:11]\n\t"
        "ds_read_b128 v[104:107], v129 offset:12288\n\t"
        "ds_read_b64_tr_b16 a[144:145], v131 offset:30720\n\t"
        "v_cvt_pk_bf16_f32 v52, v56, v57\n\t"
        "v_mfma_f32_16x16x32_bf16 v[0:3], a[108:111], v[96:99], v[0:3]\n\t"
        "buffer_load_dwordx4 v141, s[24:27], s36 offen offset:1024 lds\n\t"
        "ds_read_b64_tr_b16 a[146:147], v131 offset:31488\n\t"
        "v_cvt_pk_bf16_f32 v58, v58, v59\n\t"
        "v_mfma_f32_16x16x32_bf16 v[4:7], a[112:115], v[96:99], v[4:7]\n\t"
        "ds_read_b64_tr_b16 a[148:149], v132 offset:30720\n\t"
        "ds_read_b64_tr_b16 a[150:151], v132 offset:31488\n\t"
        "v_cmp_ne_u32 s[64:65], v52, v58\n\t"
        "v_mfma_f32_16x16x32_bf16 v[8:11], a[116:119], v[96:99], v[8:11]\n\t"
        "ds_read_b64_tr_b16 a[152:153], v133 offset:30720\n\t"
        "ds_read_b64_tr_b16 a[154:155], v133 offset:31488\n\t"
        "v_mbcnt_lo_u32_b32 v56, s64, v54\n\t"
        "v_mfma_f32_16x16x32_bf16 v[12:15], a[120:123], v[96:99], v[12:15]\n\t"
        "ds_read_b128 v[108:111], v129 offset:16384\n\t"
        "v_mbcnt_hi_u32_b32 v56, s65, v56\n\t"
        "v_mfma_f32_16x16x32_bf16 v[16:19], a[124:127], v[96:99], v[16:19]\n\t"
        "buffer_load_dwordx4 v142, s[24:27], s36 offen offset:2048 lds\n\t"
        "v_readlane_b32 s40, v155, 16\n\t"
        "v_mfma_f32_16x16x32_bf16 v[20:23], a[128:131], v[96:99], v[20:23]\n\t"
        "v_readlane_b32 s43, v155, 4\n\t"
        "s_add_u32 m0, s40, s35\n\t"
        "s_waitcnt lgkmcnt(8)\n\t"
        "v_mfma_f32_16x16x32_bf16 v[0:3], a[132:135], v[100:103], v[0:3]\n\t"
        "s_bcnt1_i32_b64 s46, s[64:65]\n\t"
        "s_add_u32 s61, s61, s46\n\t"
        "v_mfma_f32_16x16x32_bf16 v[4:7], a[136:139], v[100:103], v[4:7]\n\t"
        "s_waitcnt vmcnt(14)\n\t"
        "v_and_b32 v48, s54, v48\n\t"
        "v_pk_max_u16 v60, v154, v48\n\t"
        "v_mfma_f32_16x16x32_bf16 v[8:11], a[140:143], v[100:103], v[8:11]\n\t"
        "buffer_load_dwordx4 v134, s[16:19], s43 offen  lds\n\t"
        "v_readlane_b32 s40, v155, 17\n\t"
        "v_mfma_f32_16x16x32_bf16 v[12:15], a[108:111], v[100:103], v[12:15]\n\t"
        "ds_read_b128 v[112:115], v129 offset:20480\n\t"
        "v_readlane_b32 s43, v155, 5\n\t"
        "v_mfma_f32_16x16x32_bf16 v[16:19], a[112:115], v[100:103], v[16:19]\n\t"
        "s_add_u32 m0, s40, s35\n\t"
        "v_and_b32 v49, s54, v49\n\t"
        "v_mfma_f32_16x16x32_bf16 v[20:23], a[116:119], v[100:103], v[20:23]\n\t"
        "v_pk_max_u16 v60, v60, v49\n\t"
        "v_and_b32 v50, s54, v50\n\t"
        "v_mfma_f32_16x16x32_bf16 v[24:27], a[120:123], v[100:103], v[24:27]\n\t"
        "v_pk_max_u16 v60, v60, v50\n\t"
        "v_and_b32 v51, s54, v51\n\t"
        "v_mfma_f32_16x16x32_bf16 v[28:31], a[124:127], v[100:103], v[28:31]\n\t"
        "v_pk_max_u16 v60, v60, v51\n\t"
        "s_waitcnt vmcnt(14)\n\t"
        "v_and_b32 v61, s54, v61\n\t"
        "v_mfma_f32_16x16x32_bf16 v[32:35], a[128:131], v[100:103], v[32:35]\n\t"
        "v_pk_max_u16 v60, v60, v61\n\t"
        "v_cmp_ne_u32 s[12:13], v60, v154\n\t"
        "s_waitcnt lgkmcnt(1)\n\t"
        "v_mfma_f32_16x16x32_bf16 v[12:15], a[132:135], v[104:107], v[12:15]\n\t"
        "buffer_load_dwordx4 v135, s[16:19], s43 offen  lds\n\t"
        "s_add_u32 m0, s37, 12288\n\t"
        "v_mfma_f32_16x16x32_bf16 v[16:19], a[136:139], v[104:107], v[16:19]\n\t"
        "s_add_u32 s36, s36, s38\n\t"
        "s_lshl_b32 s6, 256, s4\n\t"
        "v_mfma_f32_16x16x32_bf16 v[20:23], a[140:143], v[104:107], v[20:23]\n\t"
        "s_cmp_lg_u64 s[12:13], 0\n\t"
        "s_cselect_b32 s6, s6, 0\n\t"
        "v_mfma_f32_16x16x32_bf16 v[24:27], a[108:111], v[104:107], v[24:27]\n\t"
        "v_min_u32 v56, 97, v56\n\t"
        "v_lshl_add_u32 v56, v56, 3, s60\n\t"
        "v_mfma_f32_16x16x32_bf16 v[28:31], a[112:115], v[104:107], v[28:31]\n\t"
        "s_waitcnt vmcnt(10)\n\t"
        "s_barrier\n\t"
        "buffer_load_dwordx4 v140, s[24:27], s36 offen offset:0 lds\n\t"
        "ds_read_b64_tr_b16 a[156:157], v131 offset:36864\n\t"
        "v_mfma_f32_16x16x32_bf16 v[32:35], a[116:119], v[104:107], v[32:35]\n\t"
        "ds_read_b64_tr_b16 a[158:159], v131 offset:37632\n\t"
        "ds_read_b64_tr_b16 a[160:161], v132 offset:36864\n\t"
        "v_mfma_f32_16x16x32_bf16 v[36:39], a[120:123], v[104:107], v[36:39]\n\t"
        "ds_read_b64_tr_b16 a[162:163], v132 offset:37632\n\t"
        "ds_read_b64_tr_b16 a[164:165], v133 offset:36864\n\t"
        "v_mfma_f32_16x16x32_bf16 v[40:43], a[124:127], v[104:107], v[40:43]\n\t"
        "ds_read_b64_tr_b16 a[166:167], v133 offset:37632\n\t"
        "ds_read_b128 v[84:87], v129 offset:64\n\t"
        "v_mfma_f32_16x16x32_bf16 v[44:47], a[128:131], v[104:107], v[44:47]\n\t"
        "buffer_load_dwordx4 v141, s[24:27], s36 offen offset:1024 lds\n\t"
        "ds_read_b128 v[88:91], v129 offset:4160\n\t"
        "v_mfma_f32_16x16x32_bf16 v[24:27], a[132:135], v[108:111], v[24:27]\n\t"
        "s_mov_b64 exec, s[64:65]\n\t"
        "ds_write_b64 v56, v[52:53]\n\t"
        "s_mov_b64 exec, -1\n\t"
        "ds_read_b64_tr_b16 a[168:169], v131 offset:43008\n\t"
        "v_mfma_f32_16x16x32_bf16 v[28:31], a[136:139], v[108:111], v[28:31]\n\t"
        "s_cmp_gt_u32 s61, s53\n\t"
        "s_cselect_b32 s8, 1, 0\n\t"
        "ds_read_b64_tr_b16 a[170:171], v131 offset:43776\n\t"
        "ds_read_b64_tr_b16 a[172:173], v132 offset:43008\n\t"
        "v_mfma_f32_16x16x32_bf16 v[32:35], a[140:143], v[108:111], v[32:35]\n\t"
        "ds_read_b64_tr_b16 a[174:175], v132 offset:43776\n\t"
        "ds_read_b64_tr_b16 a[176:177], v133 offset:43008\n\t"
        "s_or_b32 s56, s56, s8\n\t"
        "v_mfma_f32_16x16x32_bf16 v[36:39], a[108:111], v[108:111], v[36:39]\n\t"
        "s_cmp_gt_u32 s3, 64\n\t"
        "s_cselect_b32 s8, 1, 0\n\t"
        "buffer_load_dwordx4 v142, s[24:27], s36 offen offset:2048 lds\n\t"
        "ds_read_b64_tr_b16 a[178:179], v133 offset:43776\n\t"
        "v_mfma_f32_16x16x32_bf16 v[40:43], a[112:115], v[108:111], v[40:43]\n\t"
        "ds_read_b128 v[92:95], v129 offset:8256\n\t"
        "v_readlane_b32 s40, v155, 18\n\t"
        "v_mfma_f32_16x16x32_bf16 v[44:47], a[116:119], v[108:111], v[44:47]\n\t"
        "v_readlane_b32 s43, v155, 6\n\t"
        "s_add_u32 m0, s40, s35\n\t"
        "s_waitcnt lgkmcnt(9)\n\t"
        "v_mfma_f32_16x16x32_bf16 v[36:39], a[132:135], v[112:115], v[36:39]\n\t"
        "ds_read_b128 v[96:99], v129 offset:12352\n\t"
        "s_or_b32 s56, s56, s8\n\t"
        "v_mfma_f32_16x16x32_bf16 v[40:43], a[136:139], v[112:115], v[40:43]\n\t"
        "buffer_load_dwordx4 v136, s[16:19], s43 offen  lds\n\t"
        "s_bitcmp1_b32 s42, 10\n\t"
        "s_cbranch_scc1 .Lx58%=\n\t"
        ".Lx59%=:\n\t"
        "v_mfma_f32_16x16x32_bf16 v[44:47], a[140:143], v[112:115], v[44:47]\n\t"
        "v_readlane_b32 s40, v155, 19\n\t"
        "v_readlane_b32 s43, v155, 7\n\t"
        "v_mfma_f32_16x16x32_bf16 v[0:3], a[156:159], v[84:87], v[0:3]\n\t"
        "s_add_u32 m0, s40, s35\n\t"
        "s_min_u32 s61, s61, 80\n\t"
        "v_mfma_f32_16x16x32_bf16 v[4:7], a[160:163], v[84:87], v[4:7]\n\t"
        "v_min_u32 v78, s61, v116\n\t"
        "v_lshl_add_u32 v78, v78, 3, s60\n\t"
        "v_mfma_f32_16x16x32_bf16 v[8:11], a[164:167], v[84:87], v[8:11]\n\t"
        "ds_read_b64 v[62:63], v78\n\t"
        "v_cmp_ge_u32 s[66:67], s61, v116\n\t"
        "s_waitcnt lgkmcnt(4)\n\t"
        "v_mfma_f32_16x16x32_bf16 v[0:3], a[144:147], v[88:91], v[0:3]\n\t"
        "s_mov_b32 s46, 0x07060302\n\t"
        "v_mfma_f32_16x16x32_bf16 v[4:7], a[148:151], v[88:91], v[4:7]\n\t"
        "v_mfma_f32_16x16x32_bf16 v[8:11], a[152:155], v[88:91], v[8:11]\n\t"
        "v_mfma_f32_16x16x32_bf16 v[12:15], a[156:159], v[88:91], v[12:15]\n\t"
        "ds_read_b128 v[100:103], v129 offset:16448\n\t"
        "v_mfma_f32_16x16x32_bf16 v[16:19], a[160:163], v[88:91], v[16:19]\n\t"
        "buffer_load_dwordx4 v137, s[16:19], s43 offen  lds\n\t"
        "s_add_u32 m0, s37, 24576\n\t"
        "v_mfma_f32_16x16x32_bf16 v[20:23], a[164:167], v[88:91], v[20:23]\n\t"
        "s_add_u32 s36, s36, s5\n\t"
        "s_waitcnt lgkmcnt(2)\n\t"
        "v_mfma_f32_16x16x32_bf16 v[0:3], a[168:171], v[92:95], v[0:3]\n\t"
        "v_mfma_f32_16x16x32_bf16 v[4:7], a[172:175], v[92:95], v[4:7]\n\t"
        "v_mfma_f32_16x16x32_bf16 v[8:11], a[176:179], v[92:95], v[8:11]\n\t"
        "s_mov_b64 exec, s[66:67]\n\t"
        "s_waitcnt lgkmcnt(1)\n\t"
        "ds_write_b32 v63, v117\n\t"
        "s_mov_b64 exec, -1\n\t"
        "v_mfma_f32_16x16x32_bf16 v[12:15], a[144:147], v[92:95], v[12:15]\n\t"
        "ds_read_b128 v[104:107], v129 offset:20544\n\t"
        "v_and_b32 v78, 0xfffff800, v63\n\t"
        "v_mfma_f32_16x16x32_bf16 v[16:19], a[148:151], v[92:95], v[16:19]\n\t"
        "s_waitcnt vmcnt(10)\n\t"
        "s_barrier\n\t"
        "buffer_load_dwordx4 v140, s[24:27], s36 offen offset:0 lds\n\t"
        "ds_read_b64_tr_b16 a[180:181], v131 offset:49152\n\t"
        "v_mfma_f32_16x16x32_bf16 v[20:23], a[152:155], v[92:95], v[20:23]\n\t"
        "ds_read_b128 v[64:67], v78 offset:1536\n\t"
        "ds_read_b64_tr_b16 a[182:183], v131 offset:49920\n\t"
        "v_lshlrev_b32 v72, 16, v62\n\t"
        "v_mfma_f32_16x16x32_bf16 v[24:27], a[156:159], v[92:95], v[24:27]\n\t"
        "ds_read_b64_tr_b16 a[184:185], v132 offset:49152\n\t"
        "ds_read_b64_tr_b16 a[186:187], v132 offset:49920\n\t"
        "v_and_b32 v73, 0xffff0000, v62\n\t"
        "v_mfma_f32_16x16x32_bf16 v[28:31], a[160:163], v[92:95], v[28:31]\n\t"
        "ds_read_b64_tr_b16 a[188:189], v133 offset:49152\n\t"
        "ds_read_b64_tr_b16 a[190:191], v133 offset:49920\n\t"
        "v_med3_f32 v72, v72, 0, s63\n\t"
        "v_mfma_f32_16x16x32_bf16 v[32:35], a[164:167], v[92:95], v[32:35]\n\t"
        "buffer_load_dwordx4 v141, s[24:27], s36 offen offset:1024 lds\n\t"
        "ds_read_b64_tr_b16 a[192:193], v131 offset:55296\n\t"
        ".LpX0%=:\n\t"
        "s_cbranch_execz .LrX%=\n\t"
        "v_med3_f32 v73, v73, 0, s63\n\t"
        "v_mfma_f32_16x16x32_bf16 v[12:15], a[168:171], v[96:99], v[12:15]\n\t"
        "ds_read_b64_tr_b16 a[194:195], v131 offset:56064\n\t"
        "ds_read_b64_tr_b16 a[196:197], v132 offset:55296\n\t"
        "v_perm_b32 v62, v73, v72, s46\n\t"
        "v_mfma_f32_16x16x32_bf16 v[16:19], a[172:175], v[96:99], v[16:19]\n\t"
        "ds_read_b64_tr_b16 a[198:199], v132 offset:56064\n\t"
        "ds_read_b64_tr_b16 a[200:201], v133 offset:55296\n\t"
        "v_fract_f32 v74, v72\n\t"
        "v_mfma_f32_16x16x32_bf16 v[20:23], a[176:179], v[96:99], v[20:23]\n\t"
        "ds_read_b64_tr_b16 a[202:203], v133 offset:56064\n\t"
        "v_fract_f32 v75, v73\n\t"
        "v_mfma_f32_16x16x32_bf16 v[24:27], a[144:147], v[96:99], v[24:27]\n\t"
        "buffer_load_dwordx4 v142, s[24:27], s36 offen offset:2048 lds\n\t"
        "s_add_u32 m0, s37, 36864\n\t"
        "v_mfma_f32_16x16x32_bf16 v[28:31], a[148:151], v[96:99], v[28:31]\n\t"
        "ds_read_b128 v[108:111], v129 offset:128\n\t"
        "s_add_u32 s36, s36, s39\n\t"
        "v_mfma_f32_16x16x32_bf16 v[32:35], a[152:155], v[96:99], v[32:35]\n\t"
        "ds_read_b128 v[112:115], v129 offset:4224\n\t"
        "v_cvt_u32_f32 v76, v72\n\t"
        "v_mfma_f32_16x16x32_bf16 v[36:39], a[156:159], v[96:99], v[36:39]\n\t"
        "v_cvt_u32_f32 v77, v73\n\t"
        "v_cmp_neq_f32 s[64:65], 0, v74\n\t"
        "v_mfma_f32_16x16x32_bf16 v[40:43], a[160:163], v[96:99], v[40:43]\n\t"
        "buffer_load_dwordx4 v140, s[24:27], s36 offen offset:0 lds\n\t"
        "v_cmp_neq_f32 s[44:45], 0, v75\n\t"
        "v_mfma_f32_16x16x32_bf16 v[44:47], a[164:167], v[96:99], v[44:47]\n\t"
        "v_sub_f32 v78, 1.0, v74\n\t"
        "v_sub_f32 v79, 1.0, v75\n\t"
        "s_waitcnt lgkmcnt(7)\n\t"
        "v_mfma_f32_16x16x32_bf16 v[24:27], a[168:171], v[100:103], v[24:27]\n\t"
        "v_cvt_pk_bf16_f32 v79, v78, v79\n\t"
        "v_lshlrev_b32 v78, 16, v79\n\t"
        "v_mfma_f32_16x16x32_bf16 v[28:31], a[172:175], v[100:103], v[28:31]\n\t"
        "v_and_b32 v79, 0xffff0000, v79\n\t"
        "v_mul_f32 v80, v78, v79\n\t"
        "v_mfma_f32_16x16x32_bf16 v[32:35], a[176:179], v[100:103], v[32:35]\n\t"
        "v_mul_f32 v78, v78, v75\n\t"
        "v_mul_f32 v79, v74, v79\n\t"
        "v_mfma_f32_16x16x32_bf16 v[36:39], a[144:147], v[100:103], v[36:39]\n\t"
        "v_mul_f32 v81, v74, v75\n\t"
        "v_cndmask_b32 v78, v79, v78, s[44:45]\n\t"
        "v_mfma_f32_16x16x32_bf16 v[40:43], a[148:151], v[100:103], v[40:43]\n\t"
        "buffer_load_dwordx4 v141, s[24:27], s36 offen offset:1024 lds\n\t"
        "v_cndmask_b32 v79, 0, v79, s[44:45]\n\t"
        "v_mfma_f32_16x16x32_bf16 v[44:47], a[152:155], v[100:103], v[44:47]\n\t"
        "v_cvt_pk_bf16_f32 v70, v80, v78\n\t"
        "v_cvt_pk_bf16_f32 v71, v79, v81\n\t"
        "v_mfma_f32_16x16x32_bf16 v[36:39], a[168:171], v[104:107], v[36:39]\n\t"
        "v_pk_max_u16 v78, v62, v64\n\t"
        "v_pk_min_u16 v78, v78, v65\n\t"
        "v_mfma_f32_16x16x32_bf16 v[40:43], a[172:175], v[104:107], v[40:43]\n\t"
        "v_cmp_ne_u32 vcc, v78, v62\n\t"
        "v_sub_u32 v78, v77, v67\n\t"
        "v_mfma_f32_16x16x32_bf16 v[44:47], a[176:179], v[104:107], v[44:47]\n\t"
        "v_mad_u32_u24 v79, v77, 6, v66\n\t"
        "v_mad_u32_u24 v79, v76, s62, v79\n\t"
        "s_waitcnt lgkmcnt(0)\n\t"
        "v_mfma_f32_16x16x32_bf16 v[0:3], a[192:195], v[108:111], v[0:3]\n\t"
        "s_and_b64 vcc, vcc, s[66:67]\n\t"
        "s_or_b64 s[56:57], s[56:57], vcc\n\t"
        "v_mfma_f32_16x16x32_bf16 v[4:7], a[196:199], v[108:111], v[4:7]\n\t"
        "s_waitcnt vmcnt(10)\n\t"
        "s_barrier\n\t"
        "buffer_load_dwordx4 v142, s[24:27], s36 offen offset:2048 lds\n\t"
        "s_add_u32 m0, s37, 49152\n\t"
        "ds_read_b64_tr_b16 a[204:205], v131 offset:0\n\t"
        "s_add_u32 s36, s36, s38\n\t"
        "ds_read_b64_tr_b16 a[206:207], v131 offset:768\n\t"
        "v_mfma_f32_16x16x32_bf16 v[8:11], a[200:203], v[108:111], v[8:11]\n\t"
        "ds_read_b64_tr_b16 a[208:209], v132 offset:0\n\t"
        "ds_read_b64_tr_b16 a[210:211], v132 offset:768\n\t"
        "ds_read_b64_tr_b16 a[212:213], v133 offset:0\n\t"
        "ds_read_b64_tr_b16 a[214:215], v133 offset:768\n\t"
        "ds_read_b128 v[84:87], v129 offset:8320\n\t"
        "v_bfe_u32 v80, v78, 2, 1\n\t"
        "v_mfma_f32_16x16x32_bf16 v[0:3], a[180:183], v[112:115], v[0:3]\n\t"
        "ds_read_b128 v[88:91], v129 offset:12416\n\t"
        "ds_read_b64_tr_b16 a[216:217], v131 offset:6144\n\t"
        "v_or_b32 v80, v80, v79\n\t"
        "v_mfma_f32_16x16x32_bf16 v[4:7], a[184:187], v[112:115], v[4:7]\n\t"
        "ds_read_b64_tr_b16 a[218:219], v131 offset:6912\n\t"
        "ds_read_b64_tr_b16 a[220:221], v132 offset:6144\n\t"
        "v_add_u32 v78, 1, v78\n\t"
        "v_mfma_f32_16x16x32_bf16 v[8:11], a[188:191], v[112:115], v[8:11]\n\t"
        "buffer_load_dwordx4 v140, s[24:27], s36 offen offset:0 lds\n\t"
        "ds_read_b64_tr_b16 a[222:223], v132 offset:6912\n\t"
        "v_bfe_u32 v78, v78, 2, 1\n\t"
        "v_mfma_f32_16x16x32_bf16 v[12:15], a[192:195], v[112:115], v[12:15]\n\t"
        "ds_read_b128 v[92:95], v129 offset:16512\n\t"
        "ds_read_b64_tr_b16 a[224:225], v133 offset:6144\n\t"
        "v_add3_u32 v78, v79, v78, 6\n\t"
        "v_mfma_f32_16x16x32_bf16 v[16:19], a[196:199], v[112:115], v[16:19]\n\t"
        "ds_read_b64_tr_b16 a[226:227], v133 offset:6912\n\t"
        "v_cndmask_b32 v78, v80, v78, s[44:45]\n\t"
        "v_mfma_f32_16x16x32_bf16 v[20:23], a[200:203], v[112:115], v[20:23]\n\t"
        "v_cndmask_b32 v79, 0, v119, s[64:65]\n\t"
        "v_add_u32 v81, v80, v79\n\t"
        "s_waitcnt lgkmcnt(8)\n\t"
        "v_mfma_f32_16x16x32_bf16 v[0:3], a[204:207], v[84:87], v[0:3]\n\t"
        "buffer_load_dwordx4 v141, s[24:27], s36 offen offset:1024 lds\n\t"
        "v_add_u32 v82, v78, v79\n\t"
        "v_mfma_f32_16x16x32_bf16 v[4:7], a[208:211], v[84:87], v[4:7]\n\t"
        "v_cndmask_b32 v78, v81, v78, s[44:45]\n\t"
        "v_lshl_or_b32 v68, v78, 16, v80\n\t"
        "v_mfma_f32_16x16x32_bf16 v[8:11], a[212:215], v[84:87], v[8:11]\n\t"
        "v_lshl_or_b32 v69, v82, 16, v81\n\t"
        "v_mfma_f32_16x16x32_bf16 v[12:15], a[180:183], v[84:87], v[12:15]\n\t"
        "ds_write_b128 v118, v[68:71]\n\t"
        "v_mfma_f32_16x16x32_bf16 v[16:19], a[184:187], v[84:87], v[16:19]\n\t"
        "s_cmp_gt_u32 s3, 32\n\t"
        "s_cselect_b32 s8, 2, 0\n\t"
        "v_mfma_f32_16x16x32_bf16 v[20:23], a[188:191], v[84:87], v[20:23]\n\t"
        "s_cmp_lg_u64 s[56:57], 0\n\t"
        "s_addc_u32 s8, s8, s6\n\t"
        "v_mfma_f32_16x16x32_bf16 v[24:27], a[192:195], v[84:87], v[24:27]\n\t"
        "ds_read_b128 v[160:163], v185 offset:0\n\t"
        "v_mov_b32 v56, s8\n\t"
        "v_mfma_f32_16x16x32_bf16 v[28:31], a[196:199], v[84:87], v[28:31]\n\t"
        "s_mov_b64 exec, 1\n\t"
        "ds_write_b32 v153, v56\n\t"
        "s_mov_b64 exec, -1\n\t"
        "v_mfma_f32_16x16x32_bf16 v[32:35], a[200:203], v[84:87], v[32:35]\n\t"
        "buffer_load_dwordx4 v142, s[24:27], s36 offen offset:2048 lds\n\t"
        "ds_read_b128 v[96:99], v129 offset:20608\n\t"
        "v_xor_b32 v154, 32, v186\n\t"
        "s_waitcnt lgkmcnt(4)\n\t"
        "v_mfma_f32_16x16x32_bf16 v[12:15], a[204:207], v[88:91], v[12:15]\n\t"
        "v_mfma_f32_16x16x32_bf16 v[16:19], a[208:211], v[88:91], v[16:19]\n\t"
        "v_mfma_f32_16x16x32_bf16 v[20:23], a[212:215], v[88:91], v[20:23]\n\t"
        "v_mfma_f32_16x16x32_bf16 v[24:27], a[180:183], v[88:91], v[24:27]\n\t"
        "v_mfma_f32_16x16x32_bf16 v[28:31], a[184:187], v[88:91], v[28:31]\n\t"
        "v_mfma_f32_16x16x32_bf16 v[32:35], a[188:191], v[88:91], v[32:35]\n\t"
        "v_mfma_f32_16x16x32_bf16 v[36:39], a[192:195], v[88:91], v[36:39]\n\t"
        "s_waitcnt vmcnt(9)\n\t"
        "s_barrier\n\t"
        "ds_read_b64_tr_b16 a[228:229], v131 offset:12288\n\t"
        "ds_read_b64_tr_b16 a[230:231], v131 offset:13056\n\t"
        "v_mfma_f32_16x16x32_bf16 v[40:43], a[196:199], v[88:91], v[40:43]\n\t"
        "ds_read_b64_tr_b16 a[232:233], v132 offset:12288\n\t"
        "ds_read_b64_tr_b16 a[234:235], v132 offset:13056\n\t"
        "s_waitcnt lgkmcnt(5)\n\t"
        "v_lshlrev_b32_sdwa v157, 5, v160 dst_sel:DWORD dst_unused:UNUSED_PAD src0_sel:DWORD src1_sel:WORD_0\n\t"
        "v_mfma_f32_16x16x32_bf16 v[44:47], a[200:203], v[88:91], v[44:47]\n\t"
        "ds_read_b64_tr_b16 a[236:237], v133 offset:12288\n\t"
        "ds_read_b64_tr_b16 a[238:239], v133 offset:13056\n\t"
        "v_lshlrev_b32_sdwa v158, 5, v160 dst_sel:DWORD dst_unused:UNUSED_PAD src0_sel:DWORD src1_sel:WORD_1\n\t"
        "v_mfma_f32_16x16x32_bf16 v[24:27], a[204:207], v[92:95], v[24:27]\n\t"
        "ds_read_b128 v[100:103], v130 offset:0\n\t"
        "ds_read_b128 v[104:107], v130 offset:4096\n\t"
        "v_or_b32 v157, v184, v157\n\t"
        "v_mfma_f32_16x16x32_bf16 v[28:31], a[208:211], v[92:95], v[28:31]\n\t"
        "ds_read_b64_tr_b16 v[60:61], v157 offset:0\n\t"
        "ds_read_b64_tr_b16 a[240:241], v131 offset:18432\n\t"
        "v_or_b32 v158, v184, v158\n\t"
        "v_mfma_f32_16x16x32_bf16 v[32:35], a[212:215], v[92:95], v[32:35]\n\t"
        "ds_read_b64_tr_b16 a[242:243], v131 offset:19200\n\t"
        "ds_read_b64_tr_b16 a[244:245], v132 offset:18432\n\t"
        "v_xor_b32 v159, 32, v157\n\t"
        "v_mfma_f32_16x16x32_bf16 v[36:39], a[180:183], v[92:95], v[36:39]\n\t"
        "ds_read_b64_tr_b16 a[246:247], v132 offset:19200\n\t"
        "ds_read_b64_tr_b16 a[248:249], v133 offset:18432\n\t"
        "v_xor_b32 v152, 32, v158\n\t"
        "v_mfma_f32_16x16x32_bf16 v[40:43], a[184:187], v[92:95], v[40:43]\n\t"
        "ds_read_b64_tr_b16 a[250:251], v133 offset:19200\n\t"
        "v_perm_b32 v164, v162, v162, v168\n\t"
        "v_mfma_f32_16x16x32_bf16 v[44:47], a[188:191], v[92:95], v[44:47]\n\t"
        "ds_read_b128 v[108:111], v130 offset:8192\n\t"
        "v_perm_b32 v165, v162, v162, v169\n\t"
        "s_waitcnt lgkmcnt(12)\n\t"
        "v_mfma_f32_16x16x32_bf16 v[36:39], a[204:207], v[96:99], v[36:39]\n\t"
        "ds_read_b64_tr_b16 v[62:63], v158 offset:0\n\t"
        "v_perm_b32 v166, v162, v162, v170\n\t"
        "v_mfma_f32_16x16x32_bf16 v[40:43], a[208:211], v[96:99], v[40:43]\n\t"
        "s_waitcnt vmcnt(6)\n\t"
        "s_barrier\n\t"
        "ds_read_b128 v[112:115], v130 offset:12288\n\t"
        "ds_read_b64_tr_b16 a[252:253], v131 offset:24576\n\t"
        "v_mfma_f32_16x16x32_bf16 v[44:47], a[212:215], v[96:99], v[44:47]\n\t"
        "ds_read_b64_tr_b16 v[64:65], v159 offset:0\n\t"
        "ds_read_b64_tr_b16 a[254:255], v131 offset:25344\n\t"
        "v_perm_b32 v167, v162, v162, v171\n\t"
        "s_waitcnt lgkmcnt(11)\n\t"
        "v_mfma_f32_16x16x32_bf16 v[0:3], a[228:231], v[100:103], v[0:3]\n\t"
        "ds_read_b64_tr_b16 v[66:67], v152 offset:0\n\t"
        "ds_read_b64_tr_b16 v[188:189], v132 offset:24576\n\t"
        "v_mfma_f32_16x16x32_bf16 v[4:7], a[232:235], v[100:103], v[4:7]\n\t"
        "ds_read_b64_tr_b16 v[68:69], v157 offset:64\n\t"
        "ds_read_b64_tr_b16 v[190:191], v132 offset:25344\n\t"
        "v_mfma_f32_16x16x32_bf16 v[8:11], a[236:239], v[100:103], v[8:11]\n\t"
        "ds_read_b64_tr_b16 v[70:71], v158 offset:64\n\t"
        "ds_read_b64_tr_b16 v[192:193], v133 offset:24576\n\t"
        "v_mfma_f32_16x16x32_bf16 v[0:3], a[216:219], v[104:107], v[0:3]\n\t"
        "ds_read_b64_tr_b16 v[72:73], v159 offset:64\n\t"
        "ds_read_b64_tr_b16 v[194:195], v133 offset:25344\n\t"
        "v_mfma_f32_16x16x32_bf16 v[4:7], a[220:223], v[104:107], v[4:7]\n\t"
        "ds_read_b64_tr_b16 v[74:75], v152 offset:64\n\t"
        "v_mfma_f32_16x16x32_bf16 v[8:11], a[224:227], v[104:107], v[8:11]\n\t"
        "ds_read_b64_tr_b16 v[76:77], v157 offset:128\n\t"
        "v_lshlrev_b32_sdwa v157, 5, v161 dst_sel:DWORD dst_unused:UNUSED_PAD src0_sel:DWORD src1_sel:WORD_0\n\t"
        "v_mfma_f32_16x16x32_bf16 v[12:15], a[228:231], v[104:107], v[12:15]\n\t"
        "ds_read_b64_tr_b16 v[78:79], v158 offset:128\n\t"
        "v_lshlrev_b32_sdwa v158, 5, v161 dst_sel:DWORD dst_unused:UNUSED_PAD src0_sel:DWORD src1_sel:WORD_1\n\t"
        "s_waitcnt lgkmcnt(13)\n\t"
        "v_mfma_f32_16x16x32_bf16 v[48:51], v[60:63], v[164:167], 0\n\t"
        "ds_read_b64_tr_b16 v[80:81], v159 offset:128\n\t"
        "v_or_b32 v157, v184, v157\n\t"
        "v_mfma_f32_16x16x32_bf16 v[16:19], a[232:235], v[104:107], v[16:19]\n\t"
        "ds_read_b64_tr_b16 v[82:83], v152 offset:128\n\t"
        "v_or_b32 v158, v184, v158\n\t"
        "v_mfma_f32_16x16x32_bf16 v[20:23], a[236:239], v[104:107], v[20:23]\n\t"
        "ds_read_b64_tr_b16 v[60:61], v157 offset:0\n\t"
        "ds_read_b128 v[84:87], v130 offset:16384\n\t"
        "v_xor_b32 v159, 32, v157\n\t"
        "s_waitcnt lgkmcnt(12)\n\t"
        "v_mfma_f32_16x16x32_bf16 v[52:55], v[64:67], v[164:167], 0\n\t"
        "ds_read_b64_tr_b16 v[62:63], v158 offset:0\n\t"
        "ds_read_b64_tr_b16 v[196:197], v131 offset:30720\n\t"
        "v_xor_b32 v152, 32, v158\n\t"
        "v_mfma_f32_16x16x32_bf16 v[0:3], a[240:243], v[108:111], v[0:3]\n\t"
        "ds_read_b64_tr_b16 v[64:65], v159 offset:0\n\t"
        "ds_read_b64_tr_b16 v[198:199], v131 offset:31488\n\t"
        "s_waitcnt lgkmcnt(12)\n\t"
        "v_mfma_f32_16x16x32_bf16 v[56:59], v[68:71], v[164:167], 0\n\t"
        "ds_read_b64_tr_b16 v[66:67], v152 offset:0\n\t"
        "ds_read_b64_tr_b16 v[200:201], v132 offset:30720\n\t"
        "v_mfma_f32_16x16x32_bf16 v[4:7], a[244:247], v[108:111], v[4:7]\n\t"
        "ds_read_b64_tr_b16 v[68:69], v157 offset:64\n\t"
        "ds_read_b64_tr_b16 v[202:203], v132 offset:31488\n\t"
        "s_waitcnt lgkmcnt(13)\n\t"
        "v_mfma_f32_16x16x32_bf16 v[116:119], v[72:75], v[164:167], 0\n\t"
        "ds_read_b64_tr_b16 v[70:71], v158 offset:64\n\t"
        "ds_read_b64_tr_b16 v[204:205], v133 offset:30720\n\t"
        "v_mfma_f32_16x16x32_bf16 v[8:11], a[248:251], v[108:111], v[8:11]\n\t"
        "ds_read_b64_tr_b16 v[72:73], v159 offset:64\n\t"
        "ds_read_b64_tr_b16 v[206:207], v133 offset:31488\n\t"
        "s_waitcnt lgkmcnt(14)\n\t"
        "v_mfma_f32_16x16x32_bf16 v[120:123], v[76:79], v[164:167], 0\n\t"
        "ds_read_b64_tr_b16 v[74:75], v152 offset:64\n\t"
        "v_mfma_f32_16x16x32_bf16 v[12:15], a[216:219], v[108:111], v[12:15]\n\t"
        "ds_read_b64_tr_b16 v[76:77], v157 offset:128\n\t"
        "v_mfma_f32_16x16x32_bf16 v[124:127], v[80:83], v[164:167], 0\n\t"
        "ds_read_b64_tr_b16 v[78:79], v158 offset:128\n\t"
        "v_perm_b32 v164, v163, v163, v168\n\t"
        "v_mfma_f32_16x16x32_bf16 v[16:19], a[220:223], v[108:111], v[16:19]\n\t"
        "ds_read_b64_tr_b16 v[80:81], v159 offset:128\n\t"
        "v_perm_b32 v165, v163, v163, v169\n\t"
        "v_mfma_f32_16x16x32_bf16 v[20:23], a[224:227], v[108:111], v[20:23]\n\t"
        "ds_read_b64_tr_b16 v[82:83], v152 offset:128\n\t"
        "v_perm_b32 v166, v163, v163, v170\n\t"
        "v_mfma_f32_16x16x32_bf16 v[24:27], a[228:231], v[108:111], v[24:27]\n\t"
        "ds_read_b128 v[88:91], v130 offset:64\n\t"
        "v_perm_b32 v167, v163, v163, v171\n\t"
        "v_mfma_f32_16x16x32_bf16 v[28:31], a[232:235], v[108:111], v[28:31]\n\t"
        "ds_read_b128 v[160:163], v185 offset:256\n\t"
        "s_waitcnt lgkmcnt(12)\n\t"
        "v_mfma_f32_16x16x32_bf16 v[48:51], v[60:63], v[164:167], v[48:51]\n\t"
        "ds_read_b128 v[92:95], v130 offset:4160\n\t"
        "v_mfma_f32_16x16x32_bf16 v[32:35], a[236:239], v[108:111], v[32:35]\n\t"
        "v_mfma_f32_16x16x32_bf16 v[52:55], v[64:67], v[164:167], v[52:55]\n\t"
        "v_mfma_f32_16x16x32_bf16 v[12:15], a[240:243], v[112:115], v[12:15]\n\t"
        "s_waitcnt lgkmcnt(7)\n\t"
        "v_mfma_f32_16x16x32_bf16 v[56:59], v[68:71], v[164:167], v[56:59]\n\t"
        "s_nop 1\n\t"
        "v_cvt_pk_bf16_f32 v48, v48, v49\n\t"
        "v_cvt_pk_bf16_f32 v49, v50, v51\n\t"
        "v_mfma_f32_16x16x32_bf16 v[16:19], a[244:247], v[112:115], v[16:19]\n\t"
        "s_waitcnt vmcnt(0)\n\t"
        "s_barrier\n\t"
        "v_mfma_f32_16x16x32_bf16 v[116:119], v[72:75], v[164:167], v[116:119]\n\t"
        "ds_write_b64 v186, v[48:49] offset:0\n\t"
        "v_mfma_f32_16x16x32_bf16 v[20:23], a[248:251], v[112:115], v[20:23]\n\t"
        "ds_read_b64_tr_b16 v[208:209], v131 offset:36864\n\t"
        "ds_read_b64_tr_b16 v[210:211], v131 offset:37632\n\t"
        "v_cvt_pk_bf16_f32 v52, v52, v53\n\t"
        "s_waitcnt lgkmcnt(5)\n\t"
        "v_mfma_f32_16x16x32_bf16 v[120:123], v[76:79], v[164:167], v[120:123]\n\t"
        "ds_read_b64_tr_b16 v[212:213], v132 offset:36864\n\t"
        "ds_read_b64_tr_b16 v[214:215], v132 offset:37632\n\t"
        "v_cvt_pk_bf16_f32 v53, v54, v55\n\t"
        "v_mfma_f32_16x16x32_bf16 v[24:27], a[216:219], v[112:115], v[24:27]\n\t"
        "ds_write_b64 v154, v[52:53] offset:0\n\t"
        "ds_read_b128 v[96:99], v130 offset:20480\n\t"
        "v_mfma_f32_16x16x32_bf16 v[124:127], v[80:83], v[164:167], v[124:127]\n\t"
        "ds_read_b64_tr_b16 v[216:217], v133 offset:36864\n\t"
        "ds_read_b64_tr_b16 v[218:219], v133 offset:37632\n\t"
        "v_cvt_pk_bf16_f32 v56, v56, v57\n\t"
        "v_mfma_f32_16x16x32_bf16 v[28:31], a[220:223], v[112:115], v[28:31]\n\t"
        "ds_read_b64_tr_b16 v[220:221], v131 offset:43008\n\t"
        "v_cvt_pk_bf16_f32 v57, v58, v59\n\t"
        "v_mfma_f32_16x16x32_bf16 v[32:35], a[224:227], v[112:115], v[32:35]\n\t"
        "ds_write_b64 v186, v[56:57] offset:64\n\t"
        "v_mfma_f32_16x16x32_bf16 v[36:39], a[228:231], v[112:115], v[36:39]\n\t"
        "ds_read_b128 v[100:103], v130 offset:8256\n\t"
        "v_cvt_pk_bf16_f32 v116, v116, v117\n\t"
        "v_mfma_f32_16x16x32_bf16 v[40:43], a[232:235], v[112:115], v[40:43]\n\t"
        "ds_read_b64_tr_b16 v[222:223], v131 offset:43776\n\t"
        "v_cvt_pk_bf16_f32 v117, v118, v119\n\t"
        "v_mfma_f32_16x16x32_bf16 v[44:47], a[236:239], v[112:115], v[44:47]\n\t"
        "ds_write_b64 v154, v[116:117] offset:64\n\t"
        "v_mfma_f32_16x16x32_bf16 v[24:27], a[240:243], v[84:87], v[24:27]\n\t"
        "ds_read_b64_tr_b16 v[224:225], v132 offset:43008\n\t"
        "v_cvt_pk_bf16_f32 v120, v120, v121\n\t"
        "v_mfma_f32_16x16x32_bf16 v[28:31], a[244:247], v[84:87], v[28:31]\n\t"
        "ds_read_b64_tr_b16 v[226:227], v132 offset:43776\n\t"
        "v_cvt_pk_bf16_f32 v121, v122, v123\n\t"
        "v_mfma_f32_16x16x32_bf16 v[32:35], a[248:251], v[84:87], v[32:35]\n\t"
        "ds_write_b64 v186, v[120:121] offset:128\n\t"
        "v_mfma_f32_16x16x32_bf16 v[36:39], a[216:219], v[84:87], v[36:39]\n\t"
        "ds_read_b64_tr_b16 v[228:229], v133 offset:43008\n\t"
        "v_cvt_pk_bf16_f32 v124, v124, v125\n\t"
        "v_mfma_f32_16x16x32_bf16 v[40:43], a[220:223], v[84:87], v[40:43]\n\t"
        "ds_read_b64_tr_b16 v[230:231], v133 offset:43776\n\t"
        "v_cvt_pk_bf16_f32 v125, v126, v127\n\t"
        "v_mfma_f32_16x16x32_bf16 v[44:47], a[224:227], v[84:87], v[44:47]\n\t"
        "ds_write_b64 v154, v[124:125] offset:128\n\t"
        "v_mfma_f32_16x16x32_bf16 v[0:3], v[196:199], v[88:91], v[0:3]\n\t"
        "ds_read_b128 v[104:107], v130 offset:12352\n\t"
        "s_waitcnt lgkmcnt(9)\n\t"
        "v_lshlrev_b32_sdwa v157, 5, v160 dst_sel:DWORD dst_unused:UNUSED_PAD src0_sel:DWORD src1_sel:WORD_0\n\t"
        "v_mfma_f32_16x16x32_bf16 v[4:7], v[200:203], v[88:91], v[4:7]\n\t"
        "ds_read_b64_tr_b16 v[232:233], v131 offset:49152\n\t"
        "v_lshlrev_b32_sdwa v158, 5, v160 dst_sel:DWORD dst_unused:UNUSED_PAD src0_sel:DWORD src1_sel:WORD_1\n\t"
        "v_mfma_f32_16x16x32_bf16 v[8:11], v[204:207], v[88:91], v[8:11]\n\t"
        "ds_read_b64_tr_b16 v[234:235], v131 offset:49920\n\t"
        "v_or_b32 v157, v184, v157\n\t"
        "v_mfma_f32_16x16x32_bf16 v[0:3], a[252:255], v[92:95], v[0:3]\n\t"
        "ds_read_b64_tr_b16 v[60:61], v157 offset:0\n\t"
        "v_or_b32 v158, v184, v158\n\t"
        "v_mfma_f32_16x16x32_bf16 v[4:7], v[188:191], v[92:95], v[4:7]\n\t"
        "ds_read_b64_tr_b16 v[62:63], v158 offset:0\n\t"
        "v_xor_b32 v159, 32, v157\n\t"
        "v_mfma_f32_16x16x32_bf16 v[8:11], v[192:195], v[92:95], v[8:11]\n\t"
        "ds_read_b64_tr_b16 v[64:65], v159 offset:0\n\t"
        "v_xor_b32 v152, 32, v158\n\t"
        "v_mfma_f32_16x16x32_bf16 v[12:15], v[196:199], v[92:95], v[12:15]\n\t"
        "ds_read_b64_tr_b16 v[66:67], v152 offset:0\n\t"
        "v_perm_b32 v164, v162, v162, v168\n\t"
        "v_mfma_f32_16x16x32_bf16 v[16:19], v[200:203], v[92:95], v[16:19]\n\t"
        "ds_read_b64_tr_b16 v[68:69], v157 offset:64\n\t"
        "v_perm_b32 v165, v162, v162, v169\n\t"
        "v_mfma_f32_16x16x32_bf16 v[20:23], v[204:207], v[92:95], v[20:23]\n\t"
        "ds_read_b64_tr_b16 v[70:71], v158 offset:64\n\t"
        "v_perm_b32 v166, v162, v162, v170\n\t"
        "v_mfma_f32_16x16x32_bf16 v[36:39], a[240:243], v[96:99], v[36:39]\n\t"
        "ds_read_b64_tr_b16 v[72:73], v159 offset:64\n\t"
        "v_perm_b32 v167, v162, v162, v171\n\t"
        "v_mfma_f32_16x16x32_bf16 v[40:43], a[244:247], v[96:99], v[40:43]\n\t"
        "ds_read_b64_tr_b16 v[74:75], v152 offset:64\n\t"
        "v_mfma_f32_16x16x32_bf16 v[44:47], a[248:251], v[96:99], v[44:47]\n\t"
        "ds_read_b64_tr_b16 v[76:77], v157 offset:128\n\t"
        "ds_read_b128 v[108:111], v130 offset:16448\n\t"
        "v_lshlrev_b32_sdwa v157, 5, v161 dst_sel:DWORD dst_unused:UNUSED_PAD src0_sel:DWORD src1_sel:WORD_0\n\t"
        "v_mfma_f32_16x16x32_bf16 v[0:3], v[208:211], v[100:103], v[0:3]\n\t"
        "ds_read_b64_tr_b16 v[78:79], v158 offset:128\n\t"
        "ds_read_b64_tr_b16 v[236:237], v132 offset:49152\n\t"
        "v_lshlrev_b32_sdwa v158, 5, v161 dst_sel:DWORD dst_unused:UNUSED_PAD src0_sel:DWORD src1_sel:WORD_1\n\t"
        "v_mfma_f32_16x16x32_bf16 v[4:7], v[212:215], v[100:103], v[4:7]\n\t"
        "ds_read_b64_tr_b16 v[80:81], v159 offset:128\n\t"
        "ds_read_b64_tr_b16 v[238:239], v132 offset:49920\n\t"
        "v_or_b32 v157, v184, v157\n\t"
        "s_waitcnt lgkmcnt(11)\n\t"
        "v_mfma_f32_16x16x32_bf16 v[48:51], v[60:63], v[164:167], 0\n\t"
        "ds_read_b64_tr_b16 v[82:83], v152 offset:128\n\t"
        "ds_read_b64_tr_b16 v[240:241], v133 offset:49152\n\t"
        "v_or_b32 v158, v184, v158\n\t"
        "v_mfma_f32_16x16x32_bf16 v[8:11], v[216:219], v[100:103], v[8:11]\n\t"
        "ds_read_b64_tr_b16 v[60:61], v157 offset:0\n\t"
        "ds_read_b64_tr_b16 v[242:243], v133 offset:49920\n\t"
        "v_xor_b32 v159, 32, v157\n\t"
        "s_waitcnt lgkmcnt(12)\n\t"
        "v_mfma_f32_16x16x32_bf16 v[52:55], v[64:67], v[164:167], 0\n\t"
        "ds_read_b64_tr_b16 v[62:63], v158 offset:0\n\t"
        "v_xor_b32 v152, 32, v158\n\t"
        "v_mfma_f32_16x16x32_bf16 v[12:15], a[252:255], v[100:103], v[12:15]\n\t"
        "ds_read_b64_tr_b16 v[64:65], v159 offset:0\n\t"
        "v_mfma_f32_16x16x32_bf16 v[56:59], v[68:71], v[164:167], 0\n\t"
        "ds_read_b64_tr_b16 v[66:67], v152 offset:0\n\t"
        "v_mfma_f32_16x16x32_bf16 v[16:19], v[188:191], v[100:103], v[16:19]\n\t"
        "ds_read_b64_tr_b16 v[68:69], v157 offset:64\n\t"
        "s_waitcnt lgkmcnt(12)\n\t"
        "v_mfma_f32_16x16x32_bf16 v[116:119], v[72:75], v[164:167], 0\n\t"
        "ds_read_b64_tr_b16 v[70:71], v158 offset:64\n\t"
        "v_mfma_f32_16x16x32_bf16 v[20:23], v[192:195], v[100:103], v[20:23]\n\t"
        "ds_read_b64_tr_b16 v[72:73], v159 offset:64\n\t"
        "s_waitcnt lgkmcnt(11)\n\t"
        "v_mfma_f32_16x16x32_bf16 v[120:123], v[76:79], v[164:167], 0\n\t"
        "ds_read_b64_tr_b16 v[74:75], v152 offset:64\n\t"
        "v_mfma_f32_16x16x32_bf16 v[24:27], v[196:199], v[100:103], v[24:27]\n\t"
        "ds_read_b64_tr_b16 v[76:77], v157 offset:128\n\t"
        "s_waitcnt lgkmcnt(10)\n\t"
        "v_mfma_f32_16x16x32_bf16 v[124:127], v[80:83], v[164:167], 0\n\t"
        "ds_read_b64_tr_b16 v[78:79], v158 offset:128\n\t"
        "v_perm_b32 v164, v163, v163, v168\n\t"
        "v_mfma_f32_16x16x32_bf16 v[28:31], v[200:203], v[100:103], v[28:31]\n\t"
        "ds_read_b64_tr_b16 v[80:81], v159 offset:128\n\t"
        "v_perm_b32 v165, v163, v163, v169\n\t"
        "v_mfma_f32_16x16x32_bf16 v[32:35], v[204:207], v[100:103], v[32:35]\n\t"
        "ds_read_b128 v[112:115], v130 offset:128\n\t"
        "v_perm_b32 v166, v163, v163, v170\n\t"
        "v_mfma_f32_16x16x32_bf16 v[12:15], v[208:211], v[104:107], v[12:15]\n\t"
        "ds_read_b64_tr_b16 v[82:83], v152 offset:128\n\t"
        "v_perm_b32 v167, v163, v163, v171\n\t"
        "v_mfma_f32_16x16x32_bf16 v[16:19], v[212:215], v[104:107], v[16:19]\n\t"
        "ds_read_b128 v[84:87], v130 offset:4224\n\t"
        "s_waitcnt lgkmcnt(10)\n\t"
        "v_mfma_f32_16x16x32_bf16 v[48:51], v[60:63], v[164:167], v[48:51]\n\t"
        "ds_read_b64_tr_b16 v[244:245], v131 offset:55296\n\t"
        "v_mfma_f32_16x16x32_bf16 v[20:23], v[216:219], v[104:107], v[20:23]\n\t"
        "ds_read_b64_tr_b16 v[246:247], v131 offset:56064\n\t"
        "ds_read_b64_tr_b16 v[248:249], v132 offset:55296\n\t"
        "v_mfma_f32_16x16x32_bf16 v[52:55], v[64:67], v[164:167], v[52:55]\n\t"
        "ds_read_b64_tr_b16 v[250:251], v132 offset:56064\n\t"
        "ds_read_b64_tr_b16 v[252:253], v133 offset:55296\n\t"
        "v_mfma_f32_16x16x32_bf16 v[24:27], a[252:255], v[104:107], v[24:27]\n\t"
        "ds_read_b128 v[88:91], v130 offset:20544\n\t"
        "ds_read_b64_tr_b16 v[254:255], v133 offset:56064\n\t"
        "s_waitcnt lgkmcnt(12)\n\t"
        "v_mfma_f32_16x16x32_bf16 v[56:59], v[68:71], v[164:167], v[56:59]\n\t"
        "v_cvt_pk_bf16_f32 v48, v48, v49\n\t"
        "v_cvt_pk_bf16_f32 v49, v50, v51\n\t"
        "v_mfma_f32_16x16x32_bf16 v[28:31], v[188:191], v[104:107], v[28:31]\n\t"
        "ds_write_b64 v186, v[48:49] offset:12288\n\t"
        "v_mfma_f32_16x16x32_bf16 v[116:119], v[72:75], v[164:167], v[116:119]\n\t"
        "v_cvt_pk_bf16_f32 v52, v52, v53\n\t"
        "v_cvt_pk_bf16_f32 v53, v54, v55\n\t"
        "v_mfma_f32_16x16x32_bf16 v[32:35], v[192:195], v[104:107], v[32:35]\n\t"
        "ds_write_b64 v154, v[52:53] offset:12288\n\t"
        "s_waitcnt lgkmcnt(9)\n\t"
        "v_mfma_f32_16x16x32_bf16 v[120:123], v[76:79], v[164:167], v[120:123]\n\t"
        "v_cvt_pk_bf16_f32 v56, v56, v57\n\t"
        "v_cvt_pk_bf16_f32 v57, v58, v59\n\t"
        "v_mfma_f32_16x16x32_bf16 v[36:39], v[196:199], v[104:107], v[36:39]\n\t"
        "ds_write_b64 v186, v[56:57] offset:12352\n\t"
        "v_mfma_f32_16x16x32_bf16 v[124:127], v[80:83], v[164:167], v[124:127]\n\t"
        "ds_read_b128 v[92:95], v130 offset:8320\n\t"
        "v_cvt_pk_bf16_f32 v116, v116, v117\n\t"
        "v_mfma_f32_16x16x32_bf16 v[40:43], v[200:203], v[104:107], v[40:43]\n\t"
        "v_cvt_pk_bf16_f32 v117, v118, v119\n\t"
        "v_mfma_f32_16x16x32_bf16 v[44:47], v[204:207], v[104:107], v[44:47]\n\t"
        "ds_write_b64 v154, v[116:117] offset:12352\n\t"
        "v_mfma_f32_16x16x32_bf16 v[24:27], v[208:211], v[108:111], v[24:27]\n\t"
        "v_cvt_pk_bf16_f32 v120, v120, v121\n\t"
        "v_cvt_pk_bf16_f32 v121, v122, v123\n\t"
        "v_mfma_f32_16x16x32_bf16 v[28:31], v[212:215], v[108:111], v[28:31]\n\t"
        "ds_write_b64 v186, v[120:121] offset:12416\n\t"
        "v_mfma_f32_16x16x32_bf16 v[32:35], v[216:219], v[108:111], v[32:35]\n\t"
        "v_cvt_pk_bf16_f32 v124, v124, v125\n\t"
        "v_cvt_pk_bf16_f32 v125, v126, v127\n\t"
        "v_mfma_f32_16x16x32_bf16 v[36:39], a[252:255], v[108:111], v[36:39]\n\t"
        "ds_write_b64 v154, v[124:125] offset:12416\n\t"
        "v_mfma_f32_16x16x32_bf16 v[40:43], v[188:191], v[108:111], v[40:43]\n\t"
        "v_mfma_f32_16x16x32_bf16 v[44:47], v[192:195], v[108:111], v[44:47]\n\t"
        "v_mfma_f32_16x16x32_bf16 v[0:3], v[232:235], v[112:115], v[0:3]\n\t"
        "ds_read_b128 v[96:99], v130 offset:12416\n\t"
        "v_mfma_f32_16x16x32_bf16 v[4:7], v[236:239], v[112:115], v[4:7]\n\t"
        "v_mfma_f32_16x16x32_bf16 v[8:11], v[240:243], v[112:115], v[8:11]\n\t"
        "v_mfma_f32_16x16x32_bf16 v[0:3], v[220:223], v[84:87], v[0:3]\n\t"
        "s_waitcnt lgkmcnt(1)\n\t"
        "s_barrier\n\t"
        "v_mfma_f32_16x16x32_bf16 v[4:7], v[224:227], v[84:87], v[4:7]\n\t"
        "ds_read_b128 v[124:127], v182 offset:0\n\t"
        "v_mfma_f32_16x16x32_bf16 v[8:11], v[228:231], v[84:87], v[8:11]\n\t"
        "ds_read_b128 v[132:135], v187 offset:0\n\t"
        "v_mfma_f32_16x16x32_bf16 v[12:15], v[232:235], v[84:87], v[12:15]\n\t"
        "ds_read_b128 v[136:139], v187 offset:16\n\t"
        "v_mfma_f32_16x16x32_bf16 v[16:19], v[236:239], v[84:87], v[16:19]\n\t"
        "ds_read_b32 v140, v187 offset:32\n\t"
        "v_mfma_f32_16x16x32_bf16 v[20:23], v[240:243], v[84:87], v[20:23]\n\t"
        "v_mfma_f32_16x16x32_bf16 v[36:39], v[208:211], v[88:91], v[36:39]\n\t"
        "ds_read_b128 v[100:103], v130 offset:16512\n\t"
        "v_mfma_f32_16x16x32_bf16 v[40:43], v[212:215], v[88:91], v[40:43]\n\t"
        "v_mfma_f32_16x16x32_bf16 v[44:47], v[216:219], v[88:91], v[44:47]\n\t"
        "v_mfma_f32_16x16x32_bf16 v[0:3], v[244:247], v[92:95], v[0:3]\n\t"
        "v_mfma_f32_16x16x32_bf16 v[4:7], v[248:251], v[92:95], v[4:7]\n\t"
        "s_waitcnt lgkmcnt(4)\n\t"
        "v_or3_b32 v124, v124, v125, v126\n\t"
        "v_or_b32 v124, v124, v127\n\t"
        "v_mfma_f32_16x16x32_bf16 v[8:11], v[252:255], v[92:95], v[8:11]\n\t"
        "v_readfirstlane_b32 s6, v124\n\t"
        "v_mfma_f32_16x16x32_bf16 v[12:15], v[220:223], v[92:95], v[12:15]\n\t"
        "s_nop 0\n\t"
        "s_bitcmp1_b32 s6, 1\n\t"
        "s_cbranch_scc1 .Lx60%=\n\t"
        ".Lx61%=:\n\t"
        "v_mfma_f32_16x16x32_bf16 v[16:19], v[224:227], v[92:95], v[16:19]\n\t"
        "ds_read_b128 v[160:163], v185 offset:512\n\t"
        "s_waitcnt lgkmcnt(3)\n\t"
        "v_xor_b32 v132, v181, v132\n\t"
        "v_add_u32 v151, s9, v172\n\t"
        "v_mfma_f32_16x16x32_bf16 v[20:23], v[228:231], v[92:95], v[20:23]\n\t"
        "ds_read_b128 v[104:107], v130 offset:20608\n\t"
        "v_max_u32 v151, v151, v132\n\t"
        "v_xor_b32 v133, v181, v133\n\t"
        "v_mfma_f32_16x16x32_bf16 v[24:27], v[232:235], v[92:95], v[24:27]\n\t"
        "ds_read_b128 v[108:111], v151 offset:0\n\t"
        "v_add_u32 v183, s9, v173\n\t"
        "v_max_u32 v183, v183, v133\n\t"
        "v_mfma_f32_16x16x32_bf16 v[28:31], v[236:239], v[92:95], v[28:31]\n\t"
        "v_xor_b32 v134, v181, v134\n\t"
        "s_add_u32 s55, s41, 0\n\t"
        "v_cvt_pk_bf16_f32 v116, v0, v1\n\t"
        "v_mfma_f32_16x16x32_bf16 v[32:35], v[240:243], v[92:95], v[32:35]\n\t"
        "v_cvt_pk_bf16_f32 v117, v2, v3\n\t"
        "v_cvt_pk_bf16_f32 v118, v4, v5\n\t"
        "v_cvt_pk_bf16_f32 v119, v6, v7\n\t"
        "v_mfma_f32_16x16x32_bf16 v[12:15], v[244:247], v[96:99], v[12:15]\n\t"
        "ds_read_b128 v[112:115], v151 offset:64\n\t"
        "v_cvt_pk_bf16_f32 v120, v8, v9\n\t"
        "v_cvt_pk_bf16_f32 v121, v10, v11\n\t"
        "v_mfma_f32_16x16x32_bf16 v[16:19], v[248:251], v[96:99], v[16:19]\n\t"
        "v_permlane16_swap_b32 v116, v118\n\t"
        "v_mfma_f32_16x16x32_bf16 v[20:23], v[252:255], v[96:99], v[20:23]\n\t"
        "v_permlane16_swap_b32 v117, v119\n\t"
        "v_mfma_f32_16x16x32_bf16 v[24:27], v[220:223], v[96:99], v[24:27]\n\t"
        "buffer_store_dwordx4 v[116:119], v144, s[28:31], s55 offen sc1\n\t"
        "v_mfma_f32_16x16x32_bf16 v[28:31], v[224:227], v[96:99], v[28:31]\n\t"
        "ds_write_b64 v146, v[120:121] offset:0\n\t"
        "s_waitcnt lgkmcnt(3)\n\t"
        "v_lshlrev_b32_sdwa v157, 5, v160 dst_sel:DWORD dst_unused:UNUSED_PAD src0_sel:DWORD src1_sel:WORD_0\n\t"
        "v_lshlrev_b32_sdwa v158, 5, v160 dst_sel:DWORD dst_unused:UNUSED_PAD src0_sel:DWORD src1_sel:WORD_1\n\t"
        "v_mfma_f32_16x16x32_bf16 v[32:35], v[228:231], v[96:99], v[32:35]\n\t"
        "ds_read_b128 v[84:87], v151 offset:128\n\t"
        "v_add_u32 v151, s9, v174\n\t"
        "v_max_u32 v151, v151, v134\n\t"
        "v_mfma_f32_16x16x32_bf16 v[36:39], v[232:235], v[96:99], v[36:39]\n\t"
        "ds_read_b128 v[88:91], v183 offset:0\n\t"
        "v_xor_b32 v135, v181, v135\n\t"
        "s_add_u32 s55, s41, 43008\n\t"
        "v_mfma_f32_16x16x32_bf16 v[40:43], v[236:239], v[96:99], v[40:43]\n\t"
        "v_cvt_pk_bf16_f32 v116, v12, v13\n\t"
        "v_cvt_pk_bf16_f32 v117, v14, v15\n\t"
        "v_cvt_pk_bf16_f32 v118, v16, v17\n\t"
        "v_mfma_f32_16x16x32_bf16 v[44:47], v[240:243], v[96:99], v[44:47]\n\t"
        "v_cvt_pk_bf16_f32 v119, v18, v19\n\t"
        "v_cvt_pk_bf16_f32 v120, v20, v21\n\t"
        "v_cvt_pk_bf16_f32 v121, v22, v23\n\t"
        "v_mfma_f32_16x16x32_bf16 v[24:27], v[244:247], v[100:103], v[24:27]\n\t"
        "v_permlane16_swap_b32 v116, v118\n\t"
        "v_or_b32 v157, v184, v157\n\t"
        "v_mfma_f32_16x16x32_bf16 v[28:31], v[248:251], v[100:103], v[28:31]\n\t"
        "v_permlane16_swap_b32 v117, v119\n\t"
        "v_or_b32 v158, v184, v158\n\t"
        "v_mfma_f32_16x16x32_bf16 v[32:35], v[252:255], v[100:103], v[32:35]\n\t"
        "ds_read_b64_tr_b16 v[60:61], v157 offset:0\n\t"
        "ds_read_b64_tr_b16 v[62:63], v158 offset:0\n\t"
        "v_xor_b32 v159, 32, v157\n\t"
        "v_mfma_f32_16x16x32_bf16 v[36:39], v[220:223], v[100:103], v[36:39]\n\t"
        "ds_read_b64_tr_b16 v[64:65], v159 offset:0\n\t"
        "ds_read_b64_tr_b16 v[68:69], v157 offset:64\n\t"
        "v_xor_b32 v152, 32, v158\n\t"
        "v_mfma_f32_16x16x32_bf16 v[40:43], v[224:227], v[100:103], v[40:43]\n\t"
        "ds_read_b64_tr_b16 v[66:67], v152 offset:0\n\t"
        "ds_read_b64_tr_b16 v[70:71], v158 offset:64\n\t"
        "v_perm_b32 v164, v162, v162, v168\n\t"
        "v_mfma_f32_16x16x32_bf16 v[44:47], v[228:231], v[100:103], v[44:47]\n\t"
        "ds_read_b128 v[92:95], v183 offset:64\n\t"
        "ds_read_b64_tr_b16 v[72:73], v159 offset:64\n\t"
        "v_perm_b32 v165, v162, v162, v169\n\t"
        "v_mfma_f32_16x16x32_bf16 v[36:39], v[244:247], v[104:107], v[36:39]\n\t"
        "ds_read_b64_tr_b16 v[74:75], v152 offset:64\n\t"
        "ds_read_b64_tr_b16 v[76:77], v157 offset:128\n\t"
        "v_perm_b32 v166, v162, v162, v170\n\t"
        "v_mfma_f32_16x16x32_bf16 v[40:43], v[248:251], v[104:107], v[40:43]\n\t"
        "ds_read_b64_tr_b16 v[78:79], v158 offset:128\n\t"
        ".LpX1%=:\n\t"
        "s_cbranch_execz .LrX%=\n\t"
        "ds_read_b64_tr_b16 v[80:81], v159 offset:128\n\t"
        "v_perm_b32 v167, v162, v162, v171\n\t"
        "v_mfma_f32_16x16x32_bf16 v[44:47], v[252:255], v[104:107], v[44:47]\n\t"
        "buffer_store_dwordx4 v[116:119], v144, s[28:31], s55 offen sc1\n\t"
        "s_waitcnt lgkmcnt(10)\n\t"
        "v_mfma_f32_16x16x32_bf16 v[48:51], a[12:15], v[108:111], 0\n\t"
        "ds_write_b64 v146, v[120:121] offset:2048\n\t"
        "s_add_u32 s55, s41, 86016\n\t"
        "v_mfma_f32_16x16x32_bf16 v[52:55], a[16:19], v[108:111], 0\n\t"
        "ds_read_b128 v[96:99], v183 offset:128\n\t"
        "ds_read_b64_tr_b16 v[82:83], v152 offset:128\n\t"
        "v_add_u32 v183, s9, v175\n\t"
        "v_mfma_f32_16x16x32_bf16 v[56:59], a[20:23], v[108:111], 0\n\t"
        "ds_read_b128 v[100:103], v151 offset:0\n\t"
        "v_max_u32 v183, v183, v135\n\t"
        "v_xor_b32 v136, v181, v136\n\t"
        "v_mfma_f32_16x16x32_bf16 v[0:3], v[60:63], v[164:167], 0\n\t"
        "ds_read_b128 v[104:107], v151 offset:64\n\t"
        "v_cvt_pk_bf16_f32 v116, v24, v25\n\t"
        "v_cvt_pk_bf16_f32 v117, v26, v27\n\t"
        "v_mfma_f32_16x16x32_bf16 v[48:51], a[48:51], v[112:115], v[48:51]\n\t"
        "ds_read_b128 v[108:111], v151 offset:128\n\t"
        "v_add_u32 v151, s9, v176\n\t"
        "v_max_u32 v151, v151, v136\n\t"
        "s_waitcnt lgkmcnt(8)\n\t"
        "v_mfma_f32_16x16x32_bf16 v[4:7], v[64:67], v[164:167], 0\n\t"
        "v_xor_b32 v137, v181, v137\n\t"
        "v_cvt_pk_bf16_f32 v118, v28, v29\n\t"
        "v_cvt_pk_bf16_f32 v119, v30, v31\n\t"
        "v_mfma_f32_16x16x32_bf16 v[52:55], a[52:55], v[112:115], v[52:55]\n\t"
        "v_cvt_pk_bf16_f32 v120, v32, v33\n\t"
        "v_cvt_pk_bf16_f32 v121, v34, v35\n\t"
        "v_lshlrev_b32_sdwa v157, 5, v161 dst_sel:DWORD dst_unused:UNUSED_PAD src0_sel:DWORD src1_sel:WORD_0\n\t"
        "v_mfma_f32_16x16x32_bf16 v[8:11], v[68:71], v[164:167], 0\n\t"
        "v_permlane16_swap_b32 v116, v118\n\t"
        "v_lshlrev_b32_sdwa v158, 5, v161 dst_sel:DWORD dst_unused:UNUSED_PAD src0_sel:DWORD src1_sel:WORD_1\n\t"
        "v_mfma_f32_16x16x32_bf16 v[56:59], a[56:59], v[112:115], v[56:59]\n\t"
        "v_permlane16_swap_b32 v117, v119\n\t"
        "v_or_b32 v157, v184, v157\n\t"
        "v_mfma_f32_16x16x32_bf16 v[12:15], v[72:75], v[164:167], 0\n\t"
        "ds_read_b64_tr_b16 v[60:61], v157 offset:0\n\t"
        "ds_read_b64_tr_b16 v[68:69], v157 offset:64\n\t"
        "v_or_b32 v158, v184, v158\n\t"
        "v_mfma_f32_16x16x32_bf16 v[48:51], a[84:87], v[84:87], v[48:51]\n\t"
        "ds_read_b64_tr_b16 v[62:63], v158 offset:0\n\t"
        "ds_read_b64_tr_b16 v[70:71], v158 offset:64\n\t"
        "v_xor_b32 v159, 32, v157\n\t"
        "s_waitcnt lgkmcnt(5)\n\t"
        "v_mfma_f32_16x16x32_bf16 v[16:19], v[76:79], v[164:167], 0\n\t"
        "ds_read_b64_tr_b16 v[64:65], v159 offset:0\n\t"
        "ds_read_b64_tr_b16 v[72:73], v159 offset:64\n\t"
        "v_xor_b32 v152, 32, v158\n\t"
        "v_mfma_f32_16x16x32_bf16 v[52:55], a[88:91], v[84:87], v[52:55]\n\t"
        "ds_read_b64_tr_b16 v[66:67], v152 offset:0\n\t"
        "ds_read_b64_tr_b16 v[74:75], v152 offset:64\n\t"
        "v_mfma_f32_16x16x32_bf16 v[20:23], v[80:83], v[164:167], 0\n\t"
        "buffer_store_dwordx4 v[116:119], v144, s[28:31], s55 offen sc1\n\t"
        "v_mfma_f32_16x16x32_bf16 v[56:59], a[92:95], v[84:87], v[56:59]\n\t"
        "ds_write_b64 v146, v[120:121] offset:4096\n\t"
        "s_add_u32 s55, s41, 129024\n\t"
        "v_mfma_f32_16x16x32_bf16 v[48:51], a[120:123], v[88:91], v[48:51]\n\t"
        "ds_read_b64_tr_b16 v[76:77], v157 offset:128\n\t"
        "ds_read_b64_tr_b16 v[78:79], v158 offset:128\n\t"
        "v_cvt_pk_bf16_f32 v116, v36, v37\n\t"
        "v_mfma_f32_16x16x32_bf16 v[52:55], a[124:127], v[88:91], v[52:55]\n\t"
        "ds_read_b64_tr_b16 v[80:81], v159 offset:128\n\t"
        "ds_read_b64_tr_b16 v[82:83], v152 offset:128\n\t"
        "v_cvt_pk_bf16_f32 v117, v38, v39\n\t"
        "v_mfma_f32_16x16x32_bf16 v[56:59], a[128:131], v[88:91], v[56:59]\n\t"
        "ds_read_b128 v[112:115], v183 offset:0\n\t"
        "v_cvt_pk_bf16_f32 v118, v40, v41\n\t"
        "v_cvt_pk_bf16_f32 v119, v42, v43\n\t"
        "v_mfma_f32_16x16x32_bf16 v[48:51], a[156:159], v[92:95], v[48:51]\n\t"
        "ds_read_b128 v[84:87], v183 offset:64\n\t"
        "v_cvt_pk_bf16_f32 v120, v44, v45\n\t"
        "v_cvt_pk_bf16_f32 v121, v46, v47\n\t"
        "v_mfma_f32_16x16x32_bf16 v[52:55], a[160:163], v[92:95], v[52:55]\n\t"
        "v_permlane16_swap_b32 v116, v118\n\t"
        "v_perm_b32 v164, v163, v163, v168\n\t"
        "v_mfma_f32_16x16x32_bf16 v[56:59], a[164:167], v[92:95], v[56:59]\n\t"
        "v_permlane16_swap_b32 v117, v119\n\t"
        "v_perm_b32 v165, v163, v163, v169\n\t"
        "v_mfma_f32_16x16x32_bf16 v[48:51], a[192:195], v[96:99], v[48:51]\n\t"
        "ds_read_b128 v[88:91], v183 offset:128\n\t"
        "v_add_u32 v183, s9, v177\n\t"
        "v_max_u32 v183, v183, v137\n\t"
        "v_mfma_f32_16x16x32_bf16 v[52:55], a[196:199], v[96:99], v[52:55]\n\t"
        "ds_read_b128 v[92:95], v151 offset:0\n\t"
        "v_xor_b32 v138, v181, v138\n\t"
        "v_perm_b32 v166, v163, v163, v170\n\t"
        "v_mfma_f32_16x16x32_bf16 v[56:59], a[200:203], v[96:99], v[56:59]\n\t"
        "v_perm_b32 v167, v163, v163, v171\n\t"
        "v_mfma_f32_16x16x32_bf16 v[48:51], a[228:231], v[100:103], v[48:51]\n\t"
        "ds_read_b128 v[160:163], v185 offset:768\n\t"
        "s_waitcnt lgkmcnt(5)\n\t"
        "v_mfma_f32_16x16x32_bf16 v[0:3], v[60:63], v[164:167], v[0:3]\n\t"
        "ds_read_b128 v[96:99], v151 offset:64\n\t"
        "v_mfma_f32_16x16x32_bf16 v[52:55], a[232:235], v[100:103], v[52:55]\n\t"
        "v_mfma_f32_16x16x32_bf16 v[4:7], v[64:67], v[164:167], v[4:7]\n\t"
        "buffer_store_dwordx4 v[116:119], v144, s[28:31], s55 offen sc1\n\t"
        "v_mfma_f32_16x16x32_bf16 v[56:59], a[236:239], v[100:103], v[56:59]\n\t"
        "ds_write_b64 v146, v[120:121] offset:6144\n\t"
        "v_mfma_f32_16x16x32_bf16 v[8:11], v[68:71], v[164:167], v[8:11]\n\t"
        "s_nop 0\n\t"
        "v_cvt_pk_bf16_f32 v0, v0, v1\n\t"
        "v_cvt_pk_bf16_f32 v1, v2, v3\n\t"
        "v_mfma_f32_16x16x32_bf16 v[48:51], v[196:199], v[104:107], v[48:51]\n\t"
        "ds_write_b64 v186, v[0:1] offset:24576\n\t"
        "v_mfma_f32_16x16x32_bf16 v[12:15], v[72:75], v[164:167], v[12:15]\n\t"
        "ds_read_b128 v[100:103], v151 offset:128\n\t"
        "v_add_u32 v151, s9, v178\n\t"
        "v_max_u32 v151, v151, v138\n\t"
        "v_xor_b32 v139, v181, v139\n\t"
        "v_cvt_pk_bf16_f32 v4, v4, v5\n\t"
        "v_mfma_f32_16x16x32_bf16 v[52:55], v[200:203], v[104:107], v[52:55]\n\t"
        "v_cvt_pk_bf16_f32 v5, v6, v7\n\t"
        "v_mfma_f32_16x16x32_bf16 v[16:19], v[76:79], v[164:167], v[16:19]\n\t"
        "ds_write_b64 v154, v[4:5] offset:24576\n\t"
        "v_cvt_pk_bf16_f32 v8, v8, v9\n\t"
        "v_mfma_f32_16x16x32_bf16 v[56:59], v[204:207], v[104:107], v[56:59]\n\t"
        "v_cvt_pk_bf16_f32 v9, v10, v11\n\t"
        "s_waitcnt lgkmcnt(4)\n\t"
        "v_lshlrev_b32_sdwa v157, 5, v160 dst_sel:DWORD dst_unused:UNUSED_PAD src0_sel:DWORD src1_sel:WORD_0\n\t"
        "v_lshlrev_b32_sdwa v158, 5, v160 dst_sel:DWORD dst_unused:UNUSED_PAD src0_sel:DWORD src1_sel:WORD_1\n\t"
        "v_mfma_f32_16x16x32_bf16 v[20:23], v[80:83], v[164:167], v[20:23]\n\t"
        "ds_write_b64 v186, v[8:9] offset:24640\n\t"
        "v_mfma_f32_16x16x32_bf16 v[48:51], v[232:235], v[108:111], v[48:51]\n\t"
        "ds_read_b128 v[104:107], v183 offset:0\n\t"
        "v_cvt_pk_bf16_f32 v12, v12, v13\n\t"
        "v_cvt_pk_bf16_f32 v13, v14, v15\n\t"
        "v_or_b32 v157, v184, v157\n\t"
        "v_mfma_f32_16x16x32_bf16 v[52:55], v[236:239], v[108:111], v[52:55]\n\t"
        "ds_write_b64 v154, v[12:13] offset:24640\n\t"
        "v_mfma_f32_16x16x32_bf16 v[56:59], v[240:243], v[108:111], v[56:59]\n\t"
        "ds_read_b64_tr_b16 v[60:61], v157 offset:0\n\t"
        "ds_read_b64_tr_b16 v[68:69], v157 offset:64\n\t"
        "v_cvt_pk_bf16_f32 v16, v16, v17\n\t"
        "v_cvt_pk_bf16_f32 v17, v18, v19\n\t"
        "v_mfma_f32_16x16x32_bf16 v[48:51], a[0:3], v[112:115], v[48:51]\n\t"
        "ds_write_b64 v186, v[16:17] offset:24704\n\t"
        "v_mfma_f32_16x16x32_bf16 v[52:55], a[4:7], v[112:115], v[52:55]\n\t"
        "ds_read_b64_tr_b16 v[76:77], v157 offset:128\n\t"
        "v_cvt_pk_bf16_f32 v20, v20, v21\n\t"
        "v_cvt_pk_bf16_f32 v21, v22, v23\n\t"
        "v_or_b32 v158, v184, v158\n\t"
        "v_mfma_f32_16x16x32_bf16 v[56:59], a[8:11], v[112:115], v[56:59]\n\t"
        "ds_write_b64 v154, v[20:21] offset:24704\n\t"
        "v_mfma_f32_16x16x32_bf16 v[48:51], a[36:39], v[84:87], v[48:51]\n\t"
        "ds_read_b64_tr_b16 v[62:63], v158 offset:0\n\t"
        "ds_read_b64_tr_b16 v[70:71], v158 offset:64\n\t"
        "v_xor_b32 v159, 32, v157\n\t"
        "v_xor_b32 v152, 32, v158\n\t"
        "v_mfma_f32_16x16x32_bf16 v[52:55], a[40:43], v[84:87], v[52:55]\n\t"
        "ds_read_b64_tr_b16 v[64:65], v159 offset:0\n\t"
        "ds_read_b64_tr_b16 v[66:67], v152 offset:0\n\t"
        "v_perm_b32 v164, v162, v162, v168\n\t"
        "v_mfma_f32_16x16x32_bf16 v[56:59], a[44:47], v[84:87], v[56:59]\n\t"
        "ds_read_b128 v[108:111], v183 offset:64\n\t"
        "ds_read_b64_tr_b16 v[72:73], v159 offset:64\n\t"
        "v_perm_b32 v165, v162, v162, v169\n\t"
        "v_mfma_f32_16x16x32_bf16 v[48:51], a[72:75], v[88:91], v[48:51]\n\t"
        "ds_read_b64_tr_b16 v[74:75], v152 offset:64\n\t"
        "ds_read_b64_tr_b16 v[78:79], v158 offset:128\n\t"
        "v_perm_b32 v166, v162, v162, v170\n\t"
        "v_mfma_f32_16x16x32_bf16 v[52:55], a[76:79], v[88:91], v[52:55]\n\t"
        "ds_read_b64_tr_b16 v[80:81], v159 offset:128\n\t"
        "ds_read_b64_tr_b16 v[82:83], v152 offset:128\n\t"
        "v_perm_b32 v167, v162, v162, v171\n\t"
        "v_mfma_f32_16x16x32_bf16 v[56:59], a[80:83], v[88:91], v[56:59]\n\t"
        "ds_read_b128 v[112:115], v183 offset:128\n\t"
        "v_add_u32 v183, s9, v179\n\t"
        "v_max_u32 v183, v183, v139\n\t"
        "v_mfma_f32_16x16x32_bf16 v[48:51], a[108:111], v[92:95], v[48:51]\n\t"
        "ds_read_b128 v[84:87], v151 offset:0\n\t"
        "v_lshlrev_b32_sdwa v157, 5, v161 dst_sel:DWORD dst_unused:UNUSED_PAD src0_sel:DWORD src1_sel:WORD_0\n\t"
        "v_lshlrev_b32_sdwa v158, 5, v161 dst_sel:DWORD dst_unused:UNUSED_PAD src0_sel:DWORD src1_sel:WORD_1\n\t"
        "v_mfma_f32_16x16x32_bf16 v[52:55], a[112:115], v[92:95], v[52:55]\n\t"
        "ds_read_b128 v[88:91], v151 offset:64\n\t"
        "v_or_b32 v157, v184, v157\n\t"
        "v_or_b32 v158, v184, v158\n\t"
        "v_mfma_f32_16x16x32_bf16 v[56:59], a[116:119], v[92:95], v[56:59]\n\t"
        "v_xor_b32 v159, 32, v157\n\t"
        "v_xor_b32 v152, 32, v158\n\t"
        "v_mfma_f32_16x16x32_bf16 v[48:51], a[144:147], v[96:99], v[48:51]\n\t"
        "ds_read_b128 v[92:95], v151 offset:128\n\t"
        "s_waitcnt lgkmcnt(10)\n\t"
        "v_mfma_f32_16x16x32_bf16 v[0:3], v[60:63], v[164:167], 0\n\t"
        "ds_read_b64_tr_b16 v[60:61], v157 offset:0\n\t"
        "ds_read_b64_tr_b16 v[62:63], v158 offset:0\n\t"
        "v_mfma_f32_16x16x32_bf16 v[52:55], a[148:151], v[96:99], v[52:55]\n\t"
        "v_mfma_f32_16x16x32_bf16 v[4:7], v[64:67], v[164:167], 0\n\t"
        "ds_read_b64_tr_b16 v[64:65], v159 offset:0\n\t"
        "ds_read_b64_tr_b16 v[66:67], v152 offset:0\n\t"
        "v_mfma_f32_16x16x32_bf16 v[56:59], a[152:155], v[96:99], v[56:59]\n\t"
        "v_mfma_f32_16x16x32_bf16 v[8:11], v[68:71], v[164:167], 0\n\t"
        "ds_read_b64_tr_b16 v[68:69], v157 offset:64\n\t"
        "ds_read_b64_tr_b16 v[70:71], v158 offset:64\n\t"
        "v_mfma_f32_16x16x32_bf16 v[48:51], a[180:183], v[100:103], v[48:51]\n\t"
        "ds_read_b128 v[96:99], v183 offset:0\n\t"
        "s_waitcnt lgkmcnt(9)\n\t"
        "v_mfma_f32_16x16x32_bf16 v[12:15], v[72:75], v[164:167], 0\n\t"
        "ds_read_b64_tr_b16 v[72:73], v159 offset:64\n\t"
        "ds_read_b64_tr_b16 v[74:75], v152 offset:64\n\t"
        "v_mfma_f32_16x16x32_bf16 v[52:55], a[184:187], v[100:103], v[52:55]\n\t"
        "v_mfma_f32_16x16x32_bf16 v[16:19], v[76:79], v[164:167], 0\n\t"
        "ds_read_b64_tr_b16 v[76:77], v157 offset:128\n\t"
        "ds_read_b64_tr_b16 v[78:79], v158 offset:128\n\t"
        "v_mfma_f32_16x16x32_bf16 v[56:59], a[188:191], v[100:103], v[56:59]\n\t"
        "v_mfma_f32_16x16x32_bf16 v[20:23], v[80:83], v[164:167], 0\n\t"
        "ds_read_b64_tr_b16 v[80:81], v159 offset:128\n\t"
        "ds_read_b64_tr_b16 v[82:83], v152 offset:128\n\t"
        "v_perm_b32 v164, v163, v163, v168\n\t"
        "v_perm_b32 v165, v163, v163, v169\n\t"
        "v_perm_b32 v166, v163, v163, v170\n\t"
        "v_mfma_f32_16x16x32_bf16 v[48:51], a[216:219], v[104:107], v[48:51]\n\t"
        "ds_read_b128 v[100:103], v183 offset:64\n\t"
        "v_perm_b32 v167, v163, v163, v171\n\t"
        "v_mfma_f32_16x16x32_bf16 v[52:55], a[220:223], v[104:107], v[52:55]\n\t"
        "s_waitcnt lgkmcnt(9)\n\t"
        "v_mfma_f32_16x16x32_bf16 v[0:3], v[60:63], v[164:167], v[0:3]\n\t"
        "v_mfma_f32_16x16x32_bf16 v[56:59], a[224:227], v[104:107], v[56:59]\n\t"
        "v_mfma_f32_16x16x32_bf16 v[4:7], v[64:67], v[164:167], v[4:7]\n\t"
        "v_mfma_f32_16x16x32_bf16 v[48:51], a[252:255], v[108:111], v[48:51]\n\t"
        "ds_read_b128 v[104:107], v183 offset:128\n\t"
        "s_waitcnt lgkmcnt(6)\n\t"
        "v_mfma_f32_16x16x32_bf16 v[8:11], v[68:71], v[164:167], v[8:11]\n\t"
        "s_nop 1\n\t"
        "v_cvt_pk_bf16_f32 v0, v0, v1\n\t"
        "v_cvt_pk_bf16_f32 v1, v2, v3\n\t"
        "v_mfma_f32_16x16x32_bf16 v[52:55], v[188:191], v[108:111], v[52:55]\n\t"
        "ds_write_b64 v186, v[0:1] offset:36864\n\t"
        "v_mfma_f32_16x16x32_bf16 v[12:15], v[72:75], v[164:167], v[12:15]\n\t"
        "v_cvt_pk_bf16_f32 v4, v4, v5\n\t"
        "v_cvt_pk_bf16_f32 v5, v6, v7\n\t"
        "v_mfma_f32_16x16x32_bf16 v[56:59], v[192:195], v[108:111], v[56:59]\n\t"
        "ds_write_b64 v154, v[4:5] offset:36864\n\t"
        "s_waitcnt lgkmcnt(3)\n\t"
        "v_mfma_f32_16x16x32_bf16 v[16:19], v[76:79], v[164:167], v[16:19]\n\t"
        "v_cvt_pk_bf16_f32 v8, v8, v9\n\t"
        "v_cvt_pk_bf16_f32 v9, v10, v11\n\t"
        "v_mfma_f32_16x16x32_bf16 v[48:51], v[220:223], v[112:115], v[48:51]\n\t"
        "ds_write_b64 v186, v[8:9] offset:36928\n\t"
        "v_mfma_f32_16x16x32_bf16 v[20:23], v[80:83], v[164:167], v[20:23]\n\t"
        "v_cvt_pk_bf16_f32 v12, v12, v13\n\t"
        "v_cvt_pk_bf16_f32 v13, v14, v15\n\t"
        "v_mfma_f32_16x16x32_bf16 v[52:55], v[224:227], v[112:115], v[52:55]\n\t"
        "ds_write_b64 v154, v[12:13] offset:36928\n\t"
        "v_mfma_f32_16x16x32_bf16 v[56:59], v[228:231], v[112:115], v[56:59]\n\t"
        "v_cvt_pk_bf16_f32 v16, v16, v17\n\t"
        "v_cvt_pk_bf16_f32 v17, v18, v19\n\t"
        "v_mfma_f32_16x16x32_bf16 v[48:51], a[24:27], v[84:87], v[48:51]\n\t"
        "ds_write_b64 v186, v[16:17] offset:36992\n\t"
        "v_mfma_f32_16x16x32_bf16 v[52:55], a[28:31], v[84:87], v[52:55]\n\t"
        "v_cvt_pk_bf16_f32 v20, v20, v21\n\t"
        "v_cvt_pk_bf16_f32 v21, v22, v23\n\t"
        "v_mfma_f32_16x16x32_bf16 v[56:59], a[32:35], v[84:87], v[56:59]\n\t"
        "ds_write_b64 v154, v[20:21] offset:36992\n\t"
        "s_cmp_gt_u32 s61, 64\n\t"
        "s_cbranch_scc1 .Lx62%=\n\t"
        ".Lx63%=:\n\t"
        "v_mfma_f32_16x16x32_bf16 v[48:51], a[60:63], v[88:91], v[48:51]\n\t"
        "v_mfma_f32_16x16x32_bf16 v[52:55], a[64:67], v[88:91], v[52:55]\n\t"
        "v_mfma_f32_16x16x32_bf16 v[56:59], a[68:71], v[88:91], v[56:59]\n\t"
        "v_mfma_f32_16x16x32_bf16 v[48:51], a[96:99], v[92:95], v[48:51]\n\t"
        "v_mfma_f32_16x16x32_bf16 v[52:55], a[100:103], v[92:95], v[52:55]\n\t"
        "v_mfma_f32_16x16x32_bf16 v[56:59], a[104:107], v[92:95], v[56:59]\n\t"
        "s_waitcnt lgkmcnt(0)\n\t"
        "s_barrier\n\t"
        "v_xor_b32 v140, v181, v140\n\t"
        "v_mfma_f32_16x16x32_bf16 v[48:51], a[132:135], v[96:99], v[48:51]\n\t"
        "ds_read_b128 v[132:135], v187 offset:4096\n\t"
        "v_mfma_f32_16x16x32_bf16 v[52:55], a[136:139], v[96:99], v[52:55]\n\t"
        "ds_read_b128 v[136:139], v187 offset:4112\n\t"
        "v_add_u32 v151, s9, v180\n\t"
        "v_max_u32 v151, v151, v140\n\t"
        "s_waitcnt lgkmcnt(1)\n\t"
        "v_xor_b32 v132, v181, v132\n\t"
        "v_mfma_f32_16x16x32_bf16 v[56:59], a[140:143], v[96:99], v[56:59]\n\t"
        "ds_read_b128 v[108:111], v151 offset:0\n\t"
        "v_mfma_f32_16x16x32_bf16 v[48:51], a[168:171], v[100:103], v[48:51]\n\t"
        "ds_read_b128 v[112:115], v151 offset:64\n\t"
        "v_mfma_f32_16x16x32_bf16 v[52:55], a[172:175], v[100:103], v[52:55]\n\t"
        "ds_read_b128 v[84:87], v151 offset:128\n\t"
        "v_mfma_f32_16x16x32_bf16 v[56:59], a[176:179], v[100:103], v[56:59]\n\t"
        "ds_read_b32 v140, v187 offset:4128\n\t"
        "v_readlane_b32 s9, v155, 48\n\t"
        "v_mfma_f32_16x16x32_bf16 v[48:51], a[204:207], v[104:107], v[48:51]\n\t"
        "ds_read_b128 v[124:127], v182 offset:16\n\t"
        "v_add_u32 v183, s9, v172\n\t"
        "v_max_u32 v183, v183, v132\n\t"
        "v_xor_b32 v133, v181, v133\n\t"
        "v_mfma_f32_16x16x32_bf16 v[52:55], a[208:211], v[104:107], v[52:55]\n\t"
        "ds_read_b128 v[88:91], v183 offset:0\n\t"
        "v_add_u32 v151, s9, v173\n\t"
        "v_mfma_f32_16x16x32_bf16 v[56:59], a[212:215], v[104:107], v[56:59]\n\t"
        "ds_read_b128 v[92:95], v183 offset:64\n\t"
        "v_max_u32 v151, v151, v133\n\t"
        "s_waitcnt lgkmcnt(6)\n\t"
        "v_mfma_f32_16x16x32_bf16 v[48:51], a[240:243], v[108:111], v[48:51]\n\t"
        "ds_read_b128 v[96:99], v183 offset:128\n\t"
        "v_xor_b32 v134, v181, v134\n\t"
        "v_mfma_f32_16x16x32_bf16 v[52:55], a[244:247], v[108:111], v[52:55]\n\t"
        "ds_read_b128 v[100:103], v151 offset:0\n\t"
        "v_add_u32 v183, s9, v174\n\t"
        "v_mfma_f32_16x16x32_bf16 v[56:59], a[248:251], v[108:111], v[56:59]\n\t"
        "ds_read_b128 v[104:107], v151 offset:64\n\t"
        "v_max_u32 v183, v183, v134\n\t"
        "s_waitcnt lgkmcnt(8)\n\t"
        "v_mfma_f32_16x16x32_bf16 v[48:51], v[208:211], v[112:115], v[48:51]\n\t"
        "ds_read_b128 v[108:111], v151 offset:128\n\t"
        "v_xor_b32 v135, v181, v135\n\t"
        "v_mfma_f32_16x16x32_bf16 v[52:55], v[212:215], v[112:115], v[52:55]\n\t"
        "v_add_u32 v151, s9, v175\n\t"
        "v_max_u32 v151, v151, v135\n\t"
        "v_mfma_f32_16x16x32_bf16 v[56:59], v[216:219], v[112:115], v[56:59]\n\t"
        "v_xor_b32 v136, v181, v136\n\t"
        "s_waitcnt lgkmcnt(7)\n\t"
        "v_mfma_f32_16x16x32_bf16 v[48:51], v[244:247], v[84:87], v[48:51]\n\t"
        "ds_read_b128 v[112:115], v183 offset:0\n\t"
        "s_waitcnt lgkmcnt(7)\n\t"
        "v_or3_b32 v124, v124, v125, v126\n\t"
        "v_mfma_f32_16x16x32_bf16 v[52:55], v[248:251], v[84:87], v[52:55]\n\t"
        "v_or_b32 v124, v124, v127\n\t"
        "v_mfma_f32_16x16x32_bf16 v[56:59], v[252:255], v[84:87], v[56:59]\n\t"
        "v_readfirstlane_b32 s8, v124\n\t"
        "s_waitcnt lgkmcnt(6)\n\t"
        "v_mfma_f32_16x16x32_bf16 v[72:75], a[12:15], v[88:91], 0\n\t"
        "ds_read_b128 v[124:127], v147 offset:0\n\t"
        "s_or_b32 s6, s6, s8\n\t"
        "v_mfma_f32_16x16x32_bf16 v[76:79], a[16:19], v[88:91], 0\n\t"
        "ds_read_b128 v[84:87], v183 offset:64\n\t"
        "v_mfma_f32_16x16x32_bf16 v[80:83], a[20:23], v[88:91], 0\n\t"
        "s_waitcnt lgkmcnt(5)\n\t"
        "v_mfma_f32_16x16x32_bf16 v[72:75], a[48:51], v[92:95], v[72:75]\n\t"
        "ds_read_b128 v[88:91], v183 offset:128\n\t"
        "v_add_u32 v183, s9, v176\n\t"
        "v_mfma_f32_16x16x32_bf16 v[76:79], a[52:55], v[92:95], v[76:79]\n\t"
        "v_max_u32 v183, v183, v136\n\t"
        "v_xor_b32 v137, v181, v137\n\t"
        "v_mfma_f32_16x16x32_bf16 v[80:83], a[56:59], v[92:95], v[80:83]\n\t"
        "v_mfma_f32_16x16x32_bf16 v[72:75], a[84:87], v[96:99], v[72:75]\n\t"
        "ds_read_b128 v[92:95], v151 offset:0\n\t"
        "v_mfma_f32_16x16x32_bf16 v[76:79], a[88:91], v[96:99], v[76:79]\n\t"
        "v_mfma_f32_16x16x32_bf16 v[80:83], a[92:95], v[96:99], v[80:83]\n\t"
        "v_mfma_f32_16x16x32_bf16 v[72:75], a[120:123], v[100:103], v[72:75]\n\t"
        "s_waitcnt lgkmcnt(3)\n\t"
        "buffer_store_dwordx4 v[124:127], v145, s[28:31], s50 offen sc1\n\t"
        "v_mfma_f32_16x16x32_bf16 v[76:79], a[124:127], v[100:103], v[76:79]\n\t"
        "ds_read_b128 v[124:127], v147 offset:4096\n\t"
        "s_add_u32 s50, s50, 86016\n\t"
        "v_mfma_f32_16x16x32_bf16 v[80:83], a[128:131], v[100:103], v[80:83]\n\t"
        "ds_read_b128 v[96:99], v151 offset:64\n\t"
        "v_mfma_f32_16x16x32_bf16 v[72:75], a[156:159], v[104:107], v[72:75]\n\t"
        "ds_read_b128 v[100:103], v151 offset:128\n\t"
        "v_add_u32 v151, s9, v177\n\t"
        "v_mfma_f32_16x16x32_bf16 v[76:79], a[160:163], v[104:107], v[76:79]\n\t"
        "v_max_u32 v151, v151, v137\n\t"
        "v_xor_b32 v138, v181, v138\n\t"
        "v_mfma_f32_16x16x32_bf16 v[80:83], a[164:167], v[104:107], v[80:83]\n\t"
        "v_mfma_f32_16x16x32_bf16 v[72:75], a[192:195], v[108:111], v[72:75]\n\t"
        "ds_read_b128 v[104:107], v183 offset:0\n\t"
        "v_mfma_f32_16x16x32_bf16 v[76:79], a[196:199], v[108:111], v[76:79]\n\t"
        "v_mfma_f32_16x16x32_bf16 v[80:83], a[200:203], v[108:111], v[80:83]\n\t"
        "v_mfma_f32_16x16x32_bf16 v[72:75], a[228:231], v[112:115], v[72:75]\n\t"
        "ds_read_b128 v[108:111], v183 offset:64\n\t"
        "v_mfma_f32_16x16x32_bf16 v[76:79], a[232:235], v[112:115], v[76:79]\n\t"
        "v_mfma_f32_16x16x32_bf16 v[80:83], a[236:239], v[112:115], v[80:83]\n\t"
        "s_waitcnt lgkmcnt(4)\n\t"
        "v_mfma_f32_16x16x32_bf16 v[72:75], v[196:199], v[84:87], v[72:75]\n\t"
        "ds_read_b128 v[112:115], v183 offset:128\n\t"
        "v_add_u32 v183, s9, v178\n\t"
        "v_mfma_f32_16x16x32_bf16 v[76:79], v[200:203], v[84:87], v[76:79]\n\t"
        "v_max_u32 v183, v183, v138\n\t"
        "v_xor_b32 v139, v181, v139\n\t"
        "v_mfma_f32_16x16x32_bf16 v[80:83], v[204:207], v[84:87], v[80:83]\n\t"
        "buffer_store_dwordx4 v[124:127], v145, s[28:31], s50 offen sc1\n\t"
        "v_mfma_f32_16x16x32_bf16 v[72:75], v[232:235], v[88:91], v[72:75]\n\t"
        "ds_read_b128 v[84:87], v151 offset:0\n\t"
        "v_readlane_b32 s55, v155, 49\n\t"
        "v_mfma_f32_16x16x32_bf16 v[76:79], v[236:239], v[88:91], v[76:79]\n\t"
        "v_cvt_pk_bf16_f32 v116, v48, v49\n\t"
        "v_cvt_pk_bf16_f32 v117, v50, v51\n\t"
        "v_mfma_f32_16x16x32_bf16 v[80:83], v[240:243], v[88:91], v[80:83]\n\t"
        "v_cvt_pk_bf16_f32 v118, v52, v53\n\t"
        "v_cvt_pk_bf16_f32 v119, v54, v55\n\t"
        "v_mfma_f32_16x16x32_bf16 v[72:75], a[0:3], v[92:95], v[72:75]\n\t"
        "ds_read_b128 v[88:91], v151 offset:64\n\t"
        "v_cvt_pk_bf16_f32 v120, v56, v57\n\t"
        "v_mfma_f32_16x16x32_bf16 v[76:79], a[4:7], v[92:95], v[76:79]\n\t"
        "v_cvt_pk_bf16_f32 v121, v58, v59\n\t"
        "v_mfma_f32_16x16x32_bf16 v[80:83], a[8:11], v[92:95], v[80:83]\n\t"
        "v_permlane16_swap_b32 v116, v118\n\t"
        "s_waitcnt lgkmcnt(3)\n\t"
        "v_mfma_f32_16x16x32_bf16 v[72:75], a[36:39], v[96:99], v[72:75]\n\t"
        "v_permlane16_swap_b32 v117, v119\n\t"
        "v_mfma_f32_16x16x32_bf16 v[76:79], a[40:43], v[96:99], v[76:79]\n\t"
        "ds_read_b128 v[92:95], v151 offset:128\n\t"
        "v_add_u32 v151, s9, v179\n\t"
        "v_mfma_f32_16x16x32_bf16 v[80:83], a[44:47], v[96:99], v[80:83]\n\t"
        "v_max_u32 v151, v151, v139\n\t"
        "v_xor_b32 v140, v181, v140\n\t"
        "v_mfma_f32_16x16x32_bf16 v[72:75], a[72:75], v[100:103], v[72:75]\n\t"
        "ds_read_b128 v[96:99], v183 offset:0\n\t"
        "v_mfma_f32_16x16x32_bf16 v[76:79], a[76:79], v[100:103], v[76:79]\n\t"
        "v_mfma_f32_16x16x32_bf16 v[80:83], a[80:83], v[100:103], v[80:83]\n\t"
        "v_mfma_f32_16x16x32_bf16 v[72:75], a[108:111], v[104:107], v[72:75]\n\t"
        "ds_read_b128 v[100:103], v183 offset:64\n\t"
        "v_mfma_f32_16x16x32_bf16 v[76:79], a[112:115], v[104:107], v[76:79]\n\t"
        "buffer_store_dwordx4 v[116:119], v148, s[28:31], s55 offen sc1\n\t"
        "v_mfma_f32_16x16x32_bf16 v[80:83], a[116:119], v[104:107], v[80:83]\n\t"
        "ds_write_b64 v146, v[120:121] offset:8192\n\t"
        "v_mfma_f32_16x16x32_bf16 v[72:75], a[144:147], v[108:111], v[72:75]\n\t"
        "ds_read_b128 v[104:107], v183 offset:128\n\t"
        "v_add_u32 v183, s9, v180\n\t"
        "v_mfma_f32_16x16x32_bf16 v[76:79], a[148:151], v[108:111], v[76:79]\n\t"
        "v_max_u32 v183, v183, v140\n\t"
        "v_mfma_f32_16x16x32_bf16 v[80:83], a[152:155], v[108:111], v[80:83]\n\t"
        "ds_read_b128 v[108:111], v151 offset:0\n\t"
        "s_waitcnt lgkmcnt(5)\n\t"
        "v_mfma_f32_16x16x32_bf16 v[72:75], a[180:183], v[112:115], v[72:75]\n\t"
        "v_mfma_f32_16x16x32_bf16 v[76:79], a[184:187], v[112:115], v[76:79]\n\t"
        "v_mfma_f32_16x16x32_bf16 v[80:83], a[188:191], v[112:115], v[80:83]\n\t"
        "ds_read_b128 v[112:115], v151 offset:64\n\t"
        "v_mfma_f32_16x16x32_bf16 v[72:75], a[216:219], v[84:87], v[72:75]\n\t"
        "v_mfma_f32_16x16x32_bf16 v[76:79], a[220:223], v[84:87], v[76:79]\n\t"
        "v_mfma_f32_16x16x32_bf16 v[80:83], a[224:227], v[84:87], v[80:83]\n\t"
        "ds_read_b128 v[84:87], v151 offset:128\n\t"
        "v_mfma_f32_16x16x32_bf16 v[72:75], a[252:255], v[88:91], v[72:75]\n\t"
        "v_mfma_f32_16x16x32_bf16 v[76:79], v[188:191], v[88:91], v[76:79]\n\t"
        "v_mfma_f32_16x16x32_bf16 v[80:83], v[192:195], v[88:91], v[80:83]\n\t"
        "ds_read_b128 v[88:91], v183 offset:0\n\t"
        "v_mfma_f32_16x16x32_bf16 v[72:75], v[220:223], v[92:95], v[72:75]\n\t"
        "v_mfma_f32_16x16x32_bf16 v[76:79], v[224:227], v[92:95], v[76:79]\n\t"
        "v_mfma_f32_16x16x32_bf16 v[80:83], v[228:231], v[92:95], v[80:83]\n\t"
        "ds_read_b128 v[92:95], v183 offset:64\n\t"
        "s_waitcnt lgkmcnt(4)\n\t"
        "v_mfma_f32_16x16x32_bf16 v[72:75], a[24:27], v[96:99], v[72:75]\n\t"
        "v_mfma_f32_16x16x32_bf16 v[76:79], a[28:31], v[96:99], v[76:79]\n\t"
        "v_mfma_f32_16x16x32_bf16 v[80:83], a[32:35], v[96:99], v[80:83]\n\t"
        "ds_read_b128 v[96:99], v183 offset:128\n\t"
        "v_mfma_f32_16x16x32_bf16 v[80:83], a[68:71], v[100:103], v[80:83]\n\t"
        "v_mfma_f32_16x16x32_bf16 v[80:83], a[104:107], v[104:107], v[80:83]\n\t"
        "v_mfma_f32_16x16x32_bf16 v[80:83], a[140:143], v[108:111], v[80:83]\n\t"
        "s_waitcnt lgkmcnt(3)\n\t"
        "v_mfma_f32_16x16x32_bf16 v[80:83], a[176:179], v[112:115], v[80:83]\n\t"
        "v_mfma_f32_16x16x32_bf16 v[80:83], a[212:215], v[84:87], v[80:83]\n\t"
        "s_waitcnt lgkmcnt(2)\n\t"
        "v_mfma_f32_16x16x32_bf16 v[80:83], a[248:251], v[88:91], v[80:83]\n\t"
        "s_waitcnt lgkmcnt(1)\n\t"
        "v_mfma_f32_16x16x32_bf16 v[80:83], v[216:219], v[92:95], v[80:83]\n\t"
        "s_waitcnt lgkmcnt(0)\n\t"
        "v_mfma_f32_16x16x32_bf16 v[80:83], v[252:255], v[96:99], v[80:83]\n\t"
        "v_mfma_f32_16x16x32_bf16 v[72:75], a[60:63], v[100:103], v[72:75]\n\t"
        "v_mfma_f32_16x16x32_bf16 v[76:79], a[64:67], v[100:103], v[76:79]\n\t"
        "s_nop 5\n\t"
        "v_cvt_pk_bf16_f32 v120, v80, v81\n\t"
        "v_cvt_pk_bf16_f32 v121, v82, v83\n\t"
        "v_mfma_f32_16x16x32_bf16 v[72:75], a[96:99], v[104:107], v[72:75]\n\t"
        "ds_write_b64 v146, v[120:121] offset:10240\n\t"
        "v_mfma_f32_16x16x32_bf16 v[76:79], a[100:103], v[104:107], v[76:79]\n\t"
        "v_mfma_f32_16x16x32_bf16 v[72:75], a[132:135], v[108:111], v[72:75]\n\t"
        "v_mfma_f32_16x16x32_bf16 v[76:79], a[136:139], v[108:111], v[76:79]\n\t"
        "s_waitcnt lgkmcnt(0)\n\t"
        "s_barrier\n\t"
        "v_mfma_f32_16x16x32_bf16 v[72:75], a[168:171], v[112:115], v[72:75]\n\t"
        "ds_read_b128 v[124:127], v147 offset:8192\n\t"
        "v_readlane_b32 s55, v155, 51\n\t"
        "v_mfma_f32_16x16x32_bf16 v[76:79], a[172:175], v[112:115], v[76:79]\n\t"
        "v_mfma_f32_16x16x32_bf16 v[72:75], a[204:207], v[84:87], v[72:75]\n\t"
        "v_mfma_f32_16x16x32_bf16 v[76:79], a[208:211], v[84:87], v[76:79]\n\t"
        "v_mfma_f32_16x16x32_bf16 v[72:75], a[240:243], v[88:91], v[72:75]\n\t"
        "v_mfma_f32_16x16x32_bf16 v[76:79], a[244:247], v[88:91], v[76:79]\n\t"
        "v_mfma_f32_16x16x32_bf16 v[72:75], v[208:211], v[92:95], v[72:75]\n\t"
        "v_mfma_f32_16x16x32_bf16 v[76:79], v[212:215], v[92:95], v[76:79]\n\t"
        "v_mfma_f32_16x16x32_bf16 v[72:75], v[244:247], v[96:99], v[72:75]\n\t"
        "v_mfma_f32_16x16x32_bf16 v[76:79], v[248:251], v[96:99], v[76:79]\n\t"
        "s_nop 6\n\t"
        "v_cvt_pk_bf16_f32 v116, v72, v73\n\t"
        "v_cvt_pk_bf16_f32 v117, v74, v75\n\t"
        "v_cvt_pk_bf16_f32 v118, v76, v77\n\t"
        "v_cvt_pk_bf16_f32 v119, v78, v79\n\t"
        "s_nop 0\n\t"
        "v_permlane16_swap_b32 v116, v118\n\t"
        "v_permlane16_swap_b32 v117, v119\n\t"
        "buffer_store_dwordx4 v[116:119], v148, s[28:31], s55 offen  \n\t"
        "s_waitcnt lgkmcnt(0)\n\t"
        "buffer_store_dwordx4 v[124:127], v150, s[28:31], s52 offen  \n\t"
        "s_andn2_b32 s6, s6, 2\n\t"
        "s_cbranch_scc1 99f\n\t"
        "s_endpgm\n\t"
        ".Lx40%=:\n\t"
        "v_readlane_b32 s40, v155, 20\n\t"
        "v_readlane_b32 s43, v155, 8\n\t"
        "s_nop 0\n\t"
        "s_add_u32 m0, s40, s35\n\t"
        "s_nop 2\n\t"
        "buffer_load_dwordx4 v136, s[16:19], s43 offen  lds\n\t"
        "v_readlane_b32 s40, v155, 21\n\t"
        "v_readlane_b32 s43, v155, 9\n\t"
        "s_nop 0\n\t"
        "s_add_u32 m0, s40, s35\n\t"
        "s_nop 2\n\t"
        "buffer_load_dwordx4 v137, s[16:19], s43 offen  lds\n\t"
        "v_readlane_b32 s40, v155, 22\n\t"
        "v_readlane_b32 s43, v155, 10\n\t"
        "s_nop 0\n\t"
        "s_add_u32 m0, s40, s35\n\t"
        "s_nop 2\n\t"
        "buffer_load_dwordx4 v136, s[16:19], s43 offen  lds\n\t"
        "v_readlane_b32 s40, v155, 23\n\t"
        "v_readlane_b32 s43, v155, 11\n\t"
        "s_nop 0\n\t"
        "s_add_u32 m0, s40, s35\n\t"
        "s_nop 2\n\t"
        "buffer_load_dwordx4 v137, s[16:19], s43 offen  lds\n\t"
        "s_branch .Lx41%=\n\t"
        ".Lx42%=:\n\t"
        "v_lshlrev_b32_sdwa v157, 5, v161 dst_sel:DWORD dst_unused:UNUSED_PAD src0_sel:DWORD src1_sel:WORD_0\n\t"
        "v_lshlrev_b32_sdwa v158, 5, v161 dst_sel:DWORD dst_unused:UNUSED_PAD src0_sel:DWORD src1_sel:WORD_1\n\t"
        "v_or_b32 v157, v184, v157\n\t"
        "v_or_b32 v158, v184, v158\n\t"
        "v_xor_b32 v159, 32, v157\n\t"
        "v_xor_b32 v152, 32, v158\n\t"
        "ds_read_b64_tr_b16 v[60:61], v157 offset:0\n\t"
        "ds_read_b64_tr_b16 v[62:63], v158 offset:0\n\t"
        "ds_read_b64_tr_b16 v[64:65], v159 offset:0\n\t"
        "ds_read_b64_tr_b16 v[66:67], v152 offset:0\n\t"
        "ds_read_b64_tr_b16 v[68:69], v157 offset:64\n\t"
        "ds_read_b64_tr_b16 v[70:71], v158 offset:64\n\t"
        "ds_read_b64_tr_b16 v[72:73], v159 offset:64\n\t"
        "ds_read_b64_tr_b16 v[74:75], v152 offset:64\n\t"
        "ds_read_b64_tr_b16 v[76:77], v157 offset:128\n\t"
        "ds_read_b64_tr_b16 v[78:79], v158 offset:128\n\t"
        "ds_read_b64_tr_b16 v[80:81], v159 offset:128\n\t"
        "ds_read_b64_tr_b16 v[82:83], v152 offset:128\n\t"
        "v_perm_b32 v164, v163, v163, v168\n\t"
        "v_perm_b32 v165, v163, v163, v169\n\t"
        "v_perm_b32 v166, v163, v163, v170\n\t"
        "v_perm_b32 v167, v163, v163, v171\n\t"
        "s_waitcnt lgkmcnt(10)\n\t"
        "s_nop 0\n\t"
        "v_mfma_f32_16x16x32_bf16 v[48:51], v[60:63], v[164:167], v[48:51]\n\t"
        "s_waitcnt lgkmcnt(8)\n\t"
        "v_mfma_f32_16x16x32_bf16 v[52:55], v[64:67], v[164:167], v[52:55]\n\t"
        "s_waitcnt lgkmcnt(6)\n\t"
        "v_mfma_f32_16x16x32_bf16 v[56:59], v[68:71], v[164:167], v[56:59]\n\t"
        "s_waitcnt lgkmcnt(4)\n\t"
        "v_mfma_f32_16x16x32_bf16 v[116:119], v[72:75], v[164:167], v[116:119]\n\t"
        "s_waitcnt lgkmcnt(2)\n\t"
        "v_mfma_f32_16x16x32_bf16 v[120:123], v[76:79], v[164:167], v[120:123]\n\t"
        "s_waitcnt lgkmcnt(0)\n\t"
        "v_mfma_f32_16x16x32_bf16 v[124:127], v[80:83], v[164:167], v[124:127]\n\t"
        "s_nop 4\n\t"
        "s_branch .Lx43%=\n\t"
        ".Lx44%=:\n\t"
        "v_lshlrev_b32_sdwa v157, 5, v161 dst_sel:DWORD dst_unused:UNUSED_PAD src0_sel:DWORD src1_sel:WORD_0\n\t"
        "v_lshlrev_b32_sdwa v158, 5, v161 dst_sel:DWORD dst_unused:UNUSED_PAD src0_sel:DWORD src1_sel:WORD_1\n\t"
        "v_or_b32 v157, v184, v157\n\t"
        "v_or_b32 v158, v184, v158\n\t"
        "v_xor_b32 v159, 32, v157\n\t"
        "v_xor_b32 v152, 32, v158\n\t"
        "ds_read_b64_tr_b16 v[60:61], v157 offset:0\n\t"
        "ds_read_b64_tr_b16 v[62:63], v158 offset:0\n\t"
        "ds_read_b64_tr_b16 v[64:65], v159 offset:0\n\t"
        "ds_read_b64_tr_b16 v[66:67], v152 offset:0\n\t"
        "ds_read_b64_tr_b16 v[68:69], v157 offset:64\n\t"
        "ds_read_b64_tr_b16 v[70:71], v158 offset:64\n\t"
        "ds_read_b64_tr_b16 v[72:73], v159 offset:64\n\t"
        "ds_read_b64_tr_b16 v[74:75], v152 offset:64\n\t"
        "ds_read_b64_tr_b16 v[76:77], v157 offset:128\n\t"
        "ds_read_b64_tr_b16 v[78:79], v158 offset:128\n\t"
        "ds_read_b64_tr_b16 v[80:81], v159 offset:128\n\t"
        "ds_read_b64_tr_b16 v[82:83], v152 offset:128\n\t"
        "v_perm_b32 v164, v163, v163, v168\n\t"
        "v_perm_b32 v165, v163, v163, v169\n\t"
        "v_perm_b32 v166, v163, v163, v170\n\t"
        "v_perm_b32 v167, v163, v163, v171\n\t"
        "s_waitcnt lgkmcnt(10)\n\t"
        "s_nop 0\n\t"
        "v_mfma_f32_16x16x32_bf16 v[48:51], v[60:63], v[164:167], v[48:51]\n\t"
        "s_waitcnt lgkmcnt(8)\n\t"
        "v_mfma_f32_16x16x32_bf16 v[52:55], v[64:67], v[164:167], v[52:55]\n\t"
        "s_waitcnt lgkmcnt(6)\n\t"
        "v_mfma_f32_16x16x32_bf16 v[56:59], v[68:71], v[164:167], v[56:59]\n\t"
        "s_waitcnt lgkmcnt(4)\n\t"
        "v_mfma_f32_16x16x32_bf16 v[116:119], v[72:75], v[164:167], v[116:119]\n\t"
        "s_waitcnt lgkmcnt(2)\n\t"
        "v_mfma_f32_16x16x32_bf16 v[120:123], v[76:79], v[164:167], v[120:123]\n\t"
        "s_waitcnt lgkmcnt(0)\n\t"
        "v_mfma_f32_16x16x32_bf16 v[124:127], v[80:83], v[164:167], v[124:127]\n\t"
        "s_nop 4\n\t"
        "s_branch .Lx45%=\n\t"
        ".Lx46%=:\n\t"
        "v_mov_b32 v151, v185\n\t"
        "v_mov_b32 v183, v186\n\t"
        "s_min_u32 s8, s3, 64\n\t"
        "s_waitcnt lgkmcnt(0)\n\t"
        "30:\n\t"
        "ds_read_b128 v[160:163], v185\n\t"
        "s_waitcnt lgkmcnt(0)\n\t"
        "v_lshlrev_b32_sdwa v157, 5, v160 dst_sel:DWORD dst_unused:UNUSED_PAD src0_sel:DWORD src1_sel:WORD_0\n\t"
        "v_lshlrev_b32_sdwa v158, 5, v160 dst_sel:DWORD dst_unused:UNUSED_PAD src0_sel:DWORD src1_sel:WORD_1\n\t"
        "v_or_b32 v157, v184, v157\n\t"
        "v_or_b32 v158, v184, v158\n\t"
        "v_xor_b32 v159, 32, v157\n\t"
        "v_xor_b32 v152, 32, v158\n\t"
        "ds_read_b64_tr_b16 v[60:61], v157 offset:0\n\t"
        "ds_read_b64_tr_b16 v[62:63], v158 offset:0\n\t"
        "ds_read_b64_tr_b16 v[64:65], v159 offset:0\n\t"
        "ds_read_b64_tr_b16 v[66:67], v152 offset:0\n\t"
        "ds_read_b64_tr_b16 v[68:69], v157 offset:64\n\t"
        "ds_read_b64_tr_b16 v[70:71], v158 offset:64\n\t"
        "ds_read_b64_tr_b16 v[72:73], v159 offset:64\n\t"
        "ds_read_b64_tr_b16 v[74:75], v152 offset:64\n\t"
        "ds_read_b64_tr_b16 v[76:77], v157 offset:128\n\t"
        "ds_read_b64_tr_b16 v[78:79], v158 offset:128\n\t"
        "ds_read_b64_tr_b16 v[80:81], v159 offset:128\n\t"
        "ds_read_b64_tr_b16 v[82:83], v152 offset:128\n\t"
        "v_perm_b32 v164, v162, v162, v168\n\t"
        "v_perm_b32 v165, v162, v162, v169\n\t"
        "v_perm_b32 v166, v162, v162, v170\n\t"
        "v_perm_b32 v167, v162, v162, v171\n\t"
        "v_cmp_ne_u32 vcc, 0, v163\n\t"
        "s_waitcnt lgkmcnt(10)\n\t"
        "v_mfma_f32_16x16x32_bf16 v[48:51], v[60:63], v[164:167], 0\n\t"
        "s_waitcnt lgkmcnt(8)\n\t"
        "v_mfma_f32_16x16x32_bf16 v[52:55], v[64:67], v[164:167], 0\n\t"
        "s_waitcnt lgkmcnt(6)\n\t"
        "v_mfma_f32_16x16x32_bf16 v[56:59], v[68:71], v[164:167], 0\n\t"
        "s_waitcnt lgkmcnt(4)\n\t"
        "v_mfma_f32_16x16x32_bf16 v[116:119], v[72:75], v[164:167], 0\n\t"
        "s_waitcnt lgkmcnt(2)\n\t"
        "v_mfma_f32_16x16x32_bf16 v[120:123], v[76:79], v[164:167], 0\n\t"
        "s_waitcnt lgkmcnt(0)\n\t"
        "v_mfma_f32_16x16x32_bf16 v[124:127], v[80:83], v[164:167], 0\n\t"
        "s_cbranch_vccz 31f\n\t"
        "v_lshlrev_b32_sdwa v157, 5, v161 dst_sel:DWORD dst_unused:UNUSED_PAD src0_sel:DWORD src1_sel:WORD_0\n\t"
        "v_lshlrev_b32_sdwa v158, 5, v161 dst_sel:DWORD dst_unused:UNUSED_PAD src0_sel:DWORD src1_sel:WORD_1\n\t"
        "v_or_b32 v157, v184, v157\n\t"
        "v_or_b32 v158, v184, v158\n\t"
        "v_xor_b32 v159, 32, v157\n\t"
        "v_xor_b32 v152, 32, v158\n\t"
        "ds_read_b64_tr_b16 v[60:61], v157 offset:0\n\t"
        "ds_read_b64_tr_b16 v[62:63], v158 offset:0\n\t"
        "ds_read_b64_tr_b16 v[64:65], v159 offset:0\n\t"
        "ds_read_b64_tr_b16 v[66:67], v152 offset:0\n\t"
        "ds_read_b64_tr_b16 v[68:69], v157 offset:64\n\t"
        "ds_read_b64_tr_b16 v[70:71], v158 offset:64\n\t"
        "ds_read_b64_tr_b16 v[72:73], v159 offset:64\n\t"
        "ds_read_b64_tr_b16 v[74:75], v152 offset:64\n\t"
        "ds_read_b64_tr_b16 v[76:77], v157 offset:128\n\t"
        "ds_read_b64_tr_b16 v[78:79], v158 offset:128\n\t"
        "ds_read_b64_tr_b16 v[80:81], v159 offset:128\n\t"
        "ds_read_b64_tr_b16 v[82:83], v152 offset:128\n\t"
        "v_perm_b32 v164, v163, v163, v168\n\t"
        "v_perm_b32 v165, v163, v163, v169\n\t"
        "v_perm_b32 v166, v163, v163, v170\n\t"
        "v_perm_b32 v167, v163, v163, v171\n\t"
        "s_waitcnt lgkmcnt(10)\n\t"
        "s_nop 0\n\t"
        "v_mfma_f32_16x16x32_bf16 v[48:51], v[60:63], v[164:167], v[48:51]\n\t"
        "s_waitcnt lgkmcnt(8)\n\t"
        "v_mfma_f32_16x16x32_bf16 v[52:55], v[64:67], v[164:167], v[52:55]\n\t"
        "s_waitcnt lgkmcnt(6)\n\t"
        "v_mfma_f32_16x16x32_bf16 v[56:59], v[68:71], v[164:167], v[56:59]\n\t"
        "s_waitcnt lgkmcnt(4)\n\t"
        "v_mfma_f32_16x16x32_bf16 v[116:119], v[72:75], v[164:167], v[116:119]\n\t"
        "s_waitcnt lgkmcnt(2)\n\t"
        "v_mfma_f32_16x16x32_bf16 v[120:123], v[76:79], v[164:167], v[120:123]\n\t"
        "s_waitcnt lgkmcnt(0)\n\t"
        "v_mfma_f32_16x16x32_bf16 v[124:127], v[80:83], v[164:167], v[124:127]\n\t"
        "31:\n\t"
        "v_cvt_pk_bf16_f32 v48, v48, v49\n\t"
        "v_cvt_pk_bf16_f32 v49, v50, v51\n\t"
        "v_cvt_pk_bf16_f32 v52, v52, v53\n\t"
        "v_cvt_pk_bf16_f32 v53, v54, v55\n\t"
        "v_cvt_pk_bf16_f32 v56, v56, v57\n\t"
        "v_cvt_pk_bf16_f32 v57, v58, v59\n\t"
        "v_cvt_pk_bf16_f32 v116, v116, v117\n\t"
        "v_cvt_pk_bf16_f32 v117, v118, v119\n\t"
        "v_cvt_pk_bf16_f32 v120, v120, v121\n\t"
        "v_cvt_pk_bf16_f32 v121, v122, v123\n\t"
        "v_cvt_pk_bf16_f32 v124, v124, v125\n\t"
        "v_cvt_pk_bf16_f32 v125, v126, v127\n\t"
        "ds_write_b64 v186, v[48:49] offset:0\n\t"
        "ds_write_b64 v154, v[52:53] offset:0\n\t"
        "ds_write_b64 v186, v[56:57] offset:64\n\t"
        "ds_write_b64 v154, v[116:117] offset:64\n\t"
        "ds_write_b64 v186, v[120:121] offset:128\n\t"
        "ds_write_b64 v154, v[124:125] offset:128\n\t"
        "v_add_u32 v185, 256, v185\n\t"
        "v_add_u32 v186, 12288, v186\n\t"
        "v_xor_b32 v154, 32, v186\n\t"
        "s_sub_i32 s8, s8, 16\n\t"
        "s_cmp_gt_i32 s8, 0\n\t"
        "s_cbranch_scc1 30b\n\t"
        "v_mov_b32 v185, v151\n\t"
        "v_mov_b32 v186, v183\n\t"
        "v_xor_b32 v154, 32, v186\n\t"
        "s_waitcnt lgkmcnt(0)\n\t"
        "s_barrier\n\t"
        "s_branch .Lx47%=\n\t"
        ".Lx48%=:\n\t"
        "v_lshlrev_b32_sdwa v157, 5, v161 dst_sel:DWORD dst_unused:UNUSED_PAD src0_sel:DWORD src1_sel:WORD_0\n\t"
        "v_lshlrev_b32_sdwa v158, 5, v161 dst_sel:DWORD dst_unused:UNUSED_PAD src0_sel:DWORD src1_sel:WORD_1\n\t"
        "v_or_b32 v157, v184, v157\n\t"
        "v_or_b32 v158, v184, v158\n\t"
        "v_xor_b32 v159, 32, v157\n\t"
        "v_xor_b32 v152, 32, v158\n\t"
        "ds_read_b64_tr_b16 v[60:61], v157 offset:0\n\t"
        "ds_read_b64_tr_b16 v[62:63], v158 offset:0\n\t"
        "ds_read_b64_tr_b16 v[64:65], v159 offset:0\n\t"
        "ds_read_b64_tr_b16 v[66:67], v152 offset:0\n\t"
        "ds_read_b64_tr_b16 v[68:69], v157 offset:64\n\t"
        "ds_read_b64_tr_b16 v[70:71], v158 offset:64\n\t"
        "ds_read_b64_tr_b16 v[72:73], v159 offset:64\n\t"
        "ds_read_b64_tr_b16 v[74:75], v152 offset:64\n\t"
        "ds_read_b64_tr_b16 v[76:77], v157 offset:128\n\t"
        "ds_read_b64_tr_b16 v[78:79], v158 offset:128\n\t"
        "ds_read_b64_tr_b16 v[80:81], v159 offset:128\n\t"
        "ds_read_b64_tr_b16 v[82:83], v152 offset:128\n\t"
        "v_perm_b32 v164, v163, v163, v168\n\t"
        "v_perm_b32 v165, v163, v163, v169\n\t"
        "v_perm_b32 v166, v163, v163, v170\n\t"
        "v_perm_b32 v167, v163, v163, v171\n\t"
        "s_waitcnt lgkmcnt(10)\n\t"
        "s_nop 0\n\t"
        "v_mfma_f32_16x16x32_bf16 v[0:3], v[60:63], v[164:167], v[0:3]\n\t"
        "s_waitcnt lgkmcnt(8)\n\t"
        "v_mfma_f32_16x16x32_bf16 v[4:7], v[64:67], v[164:167], v[4:7]\n\t"
        "s_waitcnt lgkmcnt(6)\n\t"
        "v_mfma_f32_16x16x32_bf16 v[8:11], v[68:71], v[164:167], v[8:11]\n\t"
        "s_waitcnt lgkmcnt(4)\n\t"
        "v_mfma_f32_16x16x32_bf16 v[12:15], v[72:75], v[164:167], v[12:15]\n\t"
        "s_waitcnt lgkmcnt(2)\n\t"
        "v_mfma_f32_16x16x32_bf16 v[16:19], v[76:79], v[164:167], v[16:19]\n\t"
        "s_waitcnt lgkmcnt(0)\n\t"
        "v_mfma_f32_16x16x32_bf16 v[20:23], v[80:83], v[164:167], v[20:23]\n\t"
        "s_nop 4\n\t"
        "s_branch .Lx49%=\n\t"
        ".Lx50%=:\n\t"
        "v_add_u32 v185, 768, v185\n\t"
        "v_add_u32 v186, 36864, v186\n\t"
        "v_xor_b32 v154, 32, v186\n\t"
        "s_min_u32 s47, s61, 64\n\t"
        "s_sub_u32 s47, s47, 48\n\t"
        "s_waitcnt lgkmcnt(0)\n\t"
        "40:\n\t"
        "ds_read_b128 v[160:163], v185\n\t"
        "s_waitcnt lgkmcnt(0)\n\t"
        "v_lshlrev_b32_sdwa v157, 5, v160 dst_sel:DWORD dst_unused:UNUSED_PAD src0_sel:DWORD src1_sel:WORD_0\n\t"
        "v_lshlrev_b32_sdwa v158, 5, v160 dst_sel:DWORD dst_unused:UNUSED_PAD src0_sel:DWORD src1_sel:WORD_1\n\t"
        "v_or_b32 v157, v184, v157\n\t"
        "v_or_b32 v158, v184, v158\n\t"
        "v_xor_b32 v159, 32, v157\n\t"
        "v_xor_b32 v152, 32, v158\n\t"
        "ds_read_b64_tr_b16 v[60:61], v157 offset:0\n\t"
        "ds_read_b64_tr_b16 v[62:63], v158 offset:0\n\t"
        "ds_read_b64_tr_b16 v[64:65], v159 offset:0\n\t"
        "ds_read_b64_tr_b16 v[66:67], v152 offset:0\n\t"
        "ds_read_b64_tr_b16 v[68:69], v157 offset:64\n\t"
        "ds_read_b64_tr_b16 v[70:71], v158 offset:64\n\t"
        "ds_read_b64_tr_b16 v[72:73], v159 offset:64\n\t"
        "ds_read_b64_tr_b16 v[74:75], v152 offset:64\n\t"
        "ds_read_b64_tr_b16 v[76:77], v157 offset:128\n\t"
        "ds_read_b64_tr_b16 v[78:79], v158 offset:128\n\t"
        "ds_read_b64_tr_b16 v[80:81], v159 offset:128\n\t"
        "ds_read_b64_tr_b16 v[82:83], v152 offset:128\n\t"
        "v_perm_b32 v164, v162, v162, v168\n\t"
        "v_perm_b32 v165, v162, v162, v169\n\t"
        "v_perm_b32 v166, v162, v162, v170\n\t"
        "v_perm_b32 v167, v162, v162, v171\n\t"
        "v_cmp_ne_u32 vcc, 0, v163\n\t"
        "s_waitcnt lgkmcnt(10)\n\t"
        "v_mfma_f32_16x16x32_bf16 v[0:3], v[60:63], v[164:167], 0\n\t"
        "s_waitcnt lgkmcnt(8)\n\t"
        "v_mfma_f32_16x16x32_bf16 v[4:7], v[64:67], v[164:167], 0\n\t"
        "s_waitcnt lgkmcnt(6)\n\t"
        "v_mfma_f32_16x16x32_bf16 v[8:11], v[68:71], v[164:167], 0\n\t"
        "s_waitcnt lgkmcnt(4)\n\t"
        "v_mfma_f32_16x16x32_bf16 v[12:15], v[72:75], v[164:167], 0\n\t"
        "s_waitcnt lgkmcnt(2)\n\t"
        "v_mfma_f32_16x16x32_bf16 v[16:19], v[76:79], v[164:167], 0\n\t"
        "s_waitcnt lgkmcnt(0)\n\t"
        "v_mfma_f32_16x16x32_bf16 v[20:23], v[80:83], v[164:167], 0\n\t"
        "s_cbranch_vccz 41f\n\t"
        "v_lshlrev_b32_sdwa v157, 5, v161 dst_sel:DWORD dst_unused:UNUSED_PAD src0_sel:DWORD src1_sel:WORD_0\n\t"
        "v_lshlrev_b32_sdwa v158, 5, v161 dst_sel:DWORD dst_unused:UNUSED_PAD src0_sel:DWORD src1_sel:WORD_1\n\t"
        "v_or_b32 v157, v184, v157\n\t"
        "v_or_b32 v158, v184, v158\n\t"
        "v_xor_b32 v159, 32, v157\n\t"
        "v_xor_b32 v152, 32, v158\n\t"
        "ds_read_b64_tr_b16 v[60:61], v157 offset:0\n\t"
        "ds_read_b64_tr_b16 v[62:63], v158 offset:0\n\t"
        "ds_read_b64_tr_b16 v[64:65], v159 offset:0\n\t"
        "ds_read_b64_tr_b16 v[66:67], v152 offset:0\n\t"
        "ds_read_b64_tr_b16 v[68:69], v157 offset:64\n\t"
        "ds_read_b64_tr_b16 v[70:71], v158 offset:64\n\t"
        "ds_read_b64_tr_b16 v[72:73], v159 offset:64\n\t"
        "ds_read_b64_tr_b16 v[74:75], v152 offset:64\n\t"
        "ds_read_b64_tr_b16 v[76:77], v157 offset:128\n\t"
        "ds_read_b64_tr_b16 v[78:79], v158 offset:128\n\t"
        "ds_read_b64_tr_b16 v[80:81], v159 offset:128\n\t"
        "ds_read_b64_tr_b16 v[82:83], v152 offset:128\n\t"
        "v_perm_b32 v164, v163, v163, v168\n\t"
        "v_perm_b32 v165, v163, v163, v169\n\t"
        "v_perm_b32 v166, v163, v163, v170\n\t"
        "v_perm_b32 v167, v163, v163, v171\n\t"
        "s_waitcnt lgkmcnt(10)\n\t"
        "s_nop 0\n\t"
        "v_mfma_f32_16x16x32_bf16 v[0:3], v[60:63], v[164:167], v[0:3]\n\t"
        "s_waitcnt lgkmcnt(8)\n\t"
        "v_mfma_f32_16x16x32_bf16 v[4:7], v[64:67], v[164:167], v[4:7]\n\t"
        "s_waitcnt lgkmcnt(6)\n\t"
        "v_mfma_f32_16x16x32_bf16 v[8:11], v[68:71], v[164:167], v[8:11]\n\t"
        "s_waitcnt lgkmcnt(4)\n\t"
        "v_mfma_f32_16x16x32_bf16 v[12:15], v[72:75], v[164:167], v[12:15]\n\t"
        "s_waitcnt lgkmcnt(2)\n\t"
        "v_mfma_f32_16x16x32_bf16 v[16:19], v[76:79], v[164:167], v[16:19]\n\t"
        "s_waitcnt lgkmcnt(0)\n\t"
        "v_mfma_f32_16x16x32_bf16 v[20:23], v[80:83], v[164:167], v[20:23]\n\t"
        "41:\n\t"
        "v_cvt_pk_bf16_f32 v0, v0, v1\n\t"
        "v_cvt_pk_bf16_f32 v1, v2, v3\n\t"
        "v_cvt_pk_bf16_f32 v4, v4, v5\n\t"
        "v_cvt_pk_bf16_f32 v5, v6, v7\n\t"
        "v_cvt_pk_bf16_f32 v8, v8, v9\n\t"
        "v_cvt_pk_bf16_f32 v9, v10, v11\n\t"
        "v_cvt_pk_bf16_f32 v12, v12, v13\n\t"
        "v_cvt_pk_bf16_f32 v13, v14, v15\n\t"
        "v_cvt_pk_bf16_f32 v16, v16, v17\n\t"
        "v_cvt_pk_bf16_f32 v17, v18, v19\n\t"
        "v_cvt_pk_bf16_f32 v20, v20, v21\n\t"
        "v_cvt_pk_bf16_f32 v21, v22, v23\n\t"
        "ds_write_b64 v186, v[0:1] offset:0\n\t"
        "ds_write_b64 v154, v[4:5] offset:0\n\t"
        "ds_write_b64 v186, v[8:9] offset:64\n\t"
        "ds_write_b64 v154, v[12:13] offset:64\n\t"
        "ds_write_b64 v186, v[16:17] offset:128\n\t"
        "ds_write_b64 v154, v[20:21] offset:128\n\t"
        "v_add_u32 v185, 256, v185\n\t"
        "v_add_u32 v186, 12288, v186\n\t"
        "v_xor_b32 v154, 32, v186\n\t"
        "s_sub_i32 s47, s47, 16\n\t"
        "s_cmp_gt_i32 s47, 0\n\t"
        "s_cbranch_scc1 40b\n\t"
        "s_cmp_le_u32 s61, 64\n\t"
        "s_cbranch_scc1 52f\n\t"
        "v_and_b32 v64, 63, v156\n\t"
        "v_add_u32 v60, 65, v64\n\t"
        "s_mul_i32 s8, s4, 3072\n\t"
        "s_add_u32 s8, s8, 73728\n\t"
        "v_mov_b32 v63, s62\n\t"
        "v_lshrrev_b32 v65, 4, v64\n\t"
        "v_mul_u32_u24 v65, 9216, v65\n\t"
        "v_lshlrev_b32 v66, 3, v64\n\t"
        "v_and_or_b32 v65, v66, 48, v65\n\t"
        "v_mul_u32_u24 v66, 192, v64\n\t"
        "v_add3_u32 v61, v65, v66, s8\n\t"
        "s_add_u32 s8, s35, 157696\n\t"
        "v_lshl_add_u32 v62, v64, 4, s8\n\t"
        "v_subrev_u32 v185, 1024, v185\n\t"
        "v_subrev_u32 v186, 61440, v186\n\t"
        "v_xor_b32 v154, 32, v186\n\t"
        "v_min_u32 v16, s61, v60\n\t"
        "v_lshl_add_u32 v16, v16, 3, s60\n\t"
        "ds_read_b64 v[0:1], v16\n\t"
        "v_cmp_ge_u32 s[66:67], s61, v60\n\t"
        "s_mov_b32 s46, 0x07060302\n\t"
        "s_waitcnt lgkmcnt(0)\n\t"
        "v_and_b32 v16, 0xfffff800, v1\n\t"
        "ds_read_b128 v[2:5], v16 offset:1536\n\t"
        "v_lshlrev_b32 v10, 16, v0\n\t"
        "v_and_b32 v11, 0xffff0000, v0\n\t"
        "v_med3_f32 v10, v10, 0, s63\n\t"
        "v_med3_f32 v11, v11, 0, s63\n\t"
        "s_mov_b64 exec, s[66:67]\n\t"
        "ds_write_b32 v1, v61\n\t"
        "s_mov_b64 exec, -1\n\t"
        "v_perm_b32 v0, v11, v10, s46\n\t"
        "v_fract_f32 v12, v10\n\t"
        "v_fract_f32 v13, v11\n\t"
        "v_cvt_u32_f32 v14, v10\n\t"
        "v_cvt_u32_f32 v15, v11\n\t"
        "v_cmp_neq_f32 s[64:65], 0, v12\n\t"
        "v_cmp_neq_f32 s[44:45], 0, v13\n\t"
        "v_sub_f32 v16, 1.0, v12\n\t"
        "v_sub_f32 v17, 1.0, v13\n\t"
        "v_cvt_pk_bf16_f32 v17, v16, v17\n\t"
        "v_lshlrev_b32 v16, 16, v17\n\t"
        "v_and_b32 v17, 0xffff0000, v17\n\t"
        "v_mul_f32 v18, v16, v17\n\t"
        "v_mul_f32 v16, v16, v13\n\t"
        "v_mul_f32 v17, v12, v17\n\t"
        "v_mul_f32 v19, v12, v13\n\t"
        "v_cndmask_b32 v16, v17, v16, s[44:45]\n\t"
        "v_cndmask_b32 v17, 0, v17, s[44:45]\n\t"
        "v_cvt_pk_bf16_f32 v8, v18, v16\n\t"
        "v_cvt_pk_bf16_f32 v9, v17, v19\n\t"
        "s_waitcnt lgkmcnt(1)\n\t"
        "v_pk_max_u16 v16, v0, v2\n\t"
        "v_pk_min_u16 v16, v16, v3\n\t"
        "v_cmp_ne_u32 vcc, v16, v0\n\t"
        "v_sub_u32 v16, v15, v5\n\t"
        "v_mad_u32_u24 v17, v15, 6, v4\n\t"
        "v_mad_u32_u24 v17, v14, s62, v17\n\t"
        "s_and_b64 vcc, vcc, s[66:67]\n\t"
        "s_or_b64 s[56:57], s[56:57], vcc\n\t"
        "v_bfe_u32 v18, v16, 2, 1\n\t"
        "v_or_b32 v18, v18, v17\n\t"
        "v_add_u32 v16, 1, v16\n\t"
        "v_bfe_u32 v16, v16, 2, 1\n\t"
        "v_add3_u32 v16, v17, v16, 6\n\t"
        "v_cndmask_b32 v16, v18, v16, s[44:45]\n\t"
        "v_cndmask_b32 v17, 0, v63, s[64:65]\n\t"
        "v_add_u32 v19, v18, v17\n\t"
        "v_add_u32 v20, v16, v17\n\t"
        "v_cndmask_b32 v16, v19, v16, s[44:45]\n\t"
        "v_lshl_or_b32 v6, v16, 16, v18\n\t"
        "v_lshl_or_b32 v7, v20, 16, v19\n\t"
        "ds_write_b128 v62, v[6:9]\n\t"
        "s_cmp_lg_u64 s[56:57], 0\n\t"
        "s_cselect_b32 s8, 1, 0\n\t"
        "v_mov_b32 v60, s8\n\t"
        "ds_write_b32 v153, v60 offset:16\n\t"
        "s_sub_u32 s47, s61, 64\n\t"
        "s_waitcnt lgkmcnt(0)\n\t"
        "50:\n\t"
        "ds_read_b128 v[160:163], v185\n\t"
        "s_waitcnt lgkmcnt(0)\n\t"
        "v_lshlrev_b32_sdwa v157, 5, v160 dst_sel:DWORD dst_unused:UNUSED_PAD src0_sel:DWORD src1_sel:WORD_0\n\t"
        "v_lshlrev_b32_sdwa v158, 5, v160 dst_sel:DWORD dst_unused:UNUSED_PAD src0_sel:DWORD src1_sel:WORD_1\n\t"
        "v_or_b32 v157, v184, v157\n\t"
        "v_or_b32 v158, v184, v158\n\t"
        "v_xor_b32 v159, 32, v157\n\t"
        "v_xor_b32 v152, 32, v158\n\t"
        "ds_read_b64_tr_b16 v[60:61], v157 offset:0\n\t"
        "ds_read_b64_tr_b16 v[62:63], v158 offset:0\n\t"
        "ds_read_b64_tr_b16 v[64:65], v159 offset:0\n\t"
        "ds_read_b64_tr_b16 v[66:67], v152 offset:0\n\t"
        "ds_read_b64_tr_b16 v[68:69], v157 offset:64\n\t"
        "ds_read_b64_tr_b16 v[70:71], v158 offset:64\n\t"
        "ds_read_b64_tr_b16 v[72:73], v159 offset:64\n\t"
        "ds_read_b64_tr_b16 v[74:75], v152 offset:64\n\t"
        "ds_read_b64_tr_b16 v[76:77], v157 offset:128\n\t"
        "ds_read_b64_tr_b16 v[78:79], v158 offset:128\n\t"
        "ds_read_b64_tr_b16 v[80:81], v159 offset:128\n\t"
        "ds_read_b64_tr_b16 v[82:83], v152 offset:128\n\t"
        "v_perm_b32 v164, v162, v162, v168\n\t"
        "v_perm_b32 v165, v162, v162, v169\n\t"
        "v_perm_b32 v166, v162, v162, v170\n\t"
        "v_perm_b32 v167, v162, v162, v171\n\t"
        "v_cmp_ne_u32 vcc, 0, v163\n\t"
        "s_waitcnt lgkmcnt(10)\n\t"
        "v_mfma_f32_16x16x32_bf16 v[0:3], v[60:63], v[164:167], 0\n\t"
        "s_waitcnt lgkmcnt(8)\n\t"
        "v_mfma_f32_16x16x32_bf16 v[4:7], v[64:67], v[164:167], 0\n\t"
        "s_waitcnt lgkmcnt(6)\n\t"
        "v_mfma_f32_16x16x32_bf16 v[8:11], v[68:71], v[164:167], 0\n\t"
        "s_waitcnt lgkmcnt(4)\n\t"
        "v_mfma_f32_16x16x32_bf16 v[12:15], v[72:75], v[164:167], 0\n\t"
        "s_waitcnt lgkmcnt(2)\n\t"
        "v_mfma_f32_16x16x32_bf16 v[16:19], v[76:79], v[164:167], 0\n\t"
        "s_waitcnt lgkmcnt(0)\n\t"
        "v_mfma_f32_16x16x32_bf16 v[20:23], v[80:83], v[164:167], 0\n\t"
        "s_cbranch_vccz 51f\n\t"
        "v_lshlrev_b32_sdwa v157, 5, v161 dst_sel:DWORD dst_unused:UNUSED_PAD src0_sel:DWORD src1_sel:WORD_0\n\t"
        "v_lshlrev_b32_sdwa v158, 5, v161 dst_sel:DWORD dst_unused:UNUSED_PAD src0_sel:DWORD src1_sel:WORD_1\n\t"
        "v_or_b32 v157, v184, v157\n\t"
        "v_or_b32 v158, v184, v158\n\t"
        "v_xor_b32 v159, 32, v157\n\t"
        "v_xor_b32 v152, 32, v158\n\t"
        "ds_read_b64_tr_b16 v[60:61], v157 offset:0\n\t"
        "ds_read_b64_tr_b16 v[62:63], v158 offset:0\n\t"
        "ds_read_b64_tr_b16 v[64:65], v159 offset:0\n\t"
        "ds_read_b64_tr_b16 v[66:67], v152 offset:0\n\t"
        "ds_read_b64_tr_b16 v[68:69], v157 offset:64\n\t"
        "ds_read_b64_tr_b16 v[70:71], v158 offset:64\n\t"
        "ds_read_b64_tr_b16 v[72:73], v159 offset:64\n\t"
        "ds_read_b64_tr_b16 v[74:75], v152 offset:64\n\t"
        "ds_read_b64_tr_b16 v[76:77], v157 offset:128\n\t"
        "ds_read_b64_tr_b16 v[78:79], v158 offset:128\n\t"
        "ds_read_b64_tr_b16 v[80:81], v159 offset:128\n\t"
        "ds_read_b64_tr_b16 v[82:83], v152 offset:128\n\t"
        "v_perm_b32 v164, v163, v163, v168\n\t"
        "v_perm_b32 v165, v163, v163, v169\n\t"
        "v_perm_b32 v166, v163, v163, v170\n\t"
        "v_perm_b32 v167, v163, v163, v171\n\t"
        "s_waitcnt lgkmcnt(10)\n\t"
        "s_nop 0\n\t"
        "v_mfma_f32_16x16x32_bf16 v[0:3], v[60:63], v[164:167], v[0:3]\n\t"
        "s_waitcnt lgkmcnt(8)\n\t"
        "v_mfma_f32_16x16x32_bf16 v[4:7], v[64:67], v[164:167], v[4:7]\n\t"
        "s_waitcnt lgkmcnt(6)\n\t"
        "v_mfma_f32_16x16x32_bf16 v[8:11], v[68:71], v[164:167], v[8:11]\n\t"
        "s_waitcnt lgkmcnt(4)\n\t"
        "v_mfma_f32_16x16x32_bf16 v[12:15], v[72:75], v[164:167], v[12:15]\n\t"
        "s_waitcnt lgkmcnt(2)\n\t"
        "v_mfma_f32_16x16x32_bf16 v[16:19], v[76:79], v[164:167], v[16:19]\n\t"
        "s_waitcnt lgkmcnt(0)\n\t"
        "v_mfma_f32_16x16x32_bf16 v[20:23], v[80:83], v[164:167], v[20:23]\n\t"
        "51:\n\t"
        "v_cvt_pk_bf16_f32 v0, v0, v1\n\t"
        "v_cvt_pk_bf16_f32 v1, v2, v3\n\t"
        "v_cvt_pk_bf16_f32 v4, v4, v5\n\t"
        "v_cvt_pk_bf16_f32 v5, v6, v7\n\t"
        "v_cvt_pk_bf16_f32 v8, v8, v9\n\t"
        "v_cvt_pk_bf16_f32 v9, v10, v11\n\t"
        "v_cvt_pk_bf16_f32 v12, v12, v13\n\t"
        "v_cvt_pk_bf16_f32 v13, v14, v15\n\t"
        "v_cvt_pk_bf16_f32 v16, v16, v17\n\t"
        "v_cvt_pk_bf16_f32 v17, v18, v19\n\t"
        "v_cvt_pk_bf16_f32 v20, v20, v21\n\t"
        "v_cvt_pk_bf16_f32 v21, v22, v23\n\t"
        "ds_write_b64 v186, v[0:1] offset:0\n\t"
        "ds_write_b64 v154, v[4:5] offset:0\n\t"
        "ds_write_b64 v186, v[8:9] offset:64\n\t"
        "ds_write_b64 v154, v[12:13] offset:64\n\t"
        "ds_write_b64 v186, v[16:17] offset:128\n\t"
        "ds_write_b64 v154, v[20:21] offset:128\n\t"
        "v_add_u32 v185, 256, v185\n\t"
        "v_add_u32 v186, 12288, v186\n\t"
        "v_xor_b32 v154, 32, v186\n\t"
        "s_sub_i32 s47, s47, 16\n\t"
        "s_cmp_gt_i32 s47, 0\n\t"
        "s_cbranch_scc1 50b\n\t"
        "52:\n\t"
        "s_branch .Lx51%=\n\t"
        ".Lx52%=:\n\t"
        "v_readlane_b32 s40, v155, 20\n\t"
        "v_readlane_b32 s43, v155, 8\n\t"
        "s_nop 0\n\t"
        "s_add_u32 m0, s40, s35\n\t"
        "s_nop 2\n\t"
        "buffer_load_dwordx4 v136, s[16:19], s43 offen  lds\n\t"
        "v_readlane_b32 s40, v155, 21\n\t"
        "v_readlane_b32 s43, v155, 9\n\t"
        "s_nop 0\n\t"
        "s_add_u32 m0, s40, s35\n\t"
        "s_nop 2\n\t"
        "buffer_load_dwordx4 v137, s[16:19], s43 offen  lds\n\t"
        "v_readlane_b32 s40, v155, 22\n\t"
        "v_readlane_b32 s43, v155, 10\n\t"
        "s_nop 0\n\t"
        "s_add_u32 m0, s40, s35\n\t"
        "s_nop 2\n\t"
        "buffer_load_dwordx4 v136, s[16:19], s43 offen  lds\n\t"
        "v_readlane_b32 s40, v155, 23\n\t"
        "v_readlane_b32 s43, v155, 11\n\t"
        "s_nop 0\n\t"
        "s_add_u32 m0, s40, s35\n\t"
        "s_nop 2\n\t"
        "buffer_load_dwordx4 v137, s[16:19], s43 offen  lds\n\t"
        "s_branch .Lx53%=\n\t"
        ".Lx54%=:\n\t"
        "v_lshlrev_b32_sdwa v157, 5, v161 dst_sel:DWORD dst_unused:UNUSED_PAD src0_sel:DWORD src1_sel:WORD_0\n\t"
        "v_lshlrev_b32_sdwa v158, 5, v161 dst_sel:DWORD dst_unused:UNUSED_PAD src0_sel:DWORD src1_sel:WORD_1\n\t"
        "v_or_b32 v157, v184, v157\n\t"
        "v_or_b32 v158, v184, v158\n\t"
        "v_xor_b32 v159, 32, v157\n\t"
        "v_xor_b32 v152, 32, v158\n\t"
        "ds_read_b64_tr_b16 v[60:61], v157 offset:0\n\t"
        "ds_read_b64_tr_b16 v[62:63], v158 offset:0\n\t"
        "ds_read_b64_tr_b16 v[64:65], v159 offset:0\n\t"
        "ds_read_b64_tr_b16 v[66:67], v152 offset:0\n\t"
        "ds_read_b64_tr_b16 v[68:69], v157 offset:64\n\t"
        "ds_read_b64_tr_b16 v[70:71], v158 offset:64\n\t"
        "ds_read_b64_tr_b16 v[72:73], v159 offset:64\n\t"
        "ds_read_b64_tr_b16 v[74:75], v152 offset:64\n\t"
        "ds_read_b64_tr_b16 v[76:77], v157 offset:128\n\t"
        "ds_read_b64_tr_b16 v[78:79], v158 offset:128\n\t"
        "ds_read_b64_tr_b16 v[80:81], v159 offset:128\n\t"
        "ds_read_b64_tr_b16 v[82:83], v152 offset:128\n\t"
        "v_perm_b32 v164, v163, v163, v168\n\t"
        "v_perm_b32 v165, v163, v163, v169\n\t"
        "v_perm_b32 v166, v163, v163, v170\n\t"
        "v_perm_b32 v167, v163, v163, v171\n\t"
        "s_waitcnt lgkmcnt(10)\n\t"
        "s_nop 0\n\t"
        "v_mfma_f32_16x16x32_bf16 v[48:51], v[60:63], v[164:167], v[48:51]\n\t"
        "s_waitcnt lgkmcnt(8)\n\t"
        "v_mfma_f32_16x16x32_bf16 v[52:55], v[64:67], v[164:167], v[52:55]\n\t"
        "s_waitcnt lgkmcnt(6)\n\t"
        "v_mfma_f32_16x16x32_bf16 v[56:59], v[68:71], v[164:167], v[56:59]\n\t"
        "s_waitcnt lgkmcnt(4)\n\t"
        "v_mfma_f32_16x16x32_bf16 v[116:119], v[72:75], v[164:167], v[116:119]\n\t"
        "s_waitcnt lgkmcnt(2)\n\t"
        "v_mfma_f32_16x16x32_bf16 v[120:123], v[76:79], v[164:167], v[120:123]\n\t"
        "s_waitcnt lgkmcnt(0)\n\t"
        "v_mfma_f32_16x16x32_bf16 v[124:127], v[80:83], v[164:167], v[124:127]\n\t"
        "s_nop 4\n\t"
        "s_branch .Lx55%=\n\t"
        ".Lx56%=:\n\t"
        "v_mov_b32 v151, v185\n\t"
        "v_mov_b32 v183, v186\n\t"
        "s_min_u32 s8, s3, 64\n\t"
        "s_waitcnt lgkmcnt(0)\n\t"
        "30:\n\t"
        "ds_read_b128 v[160:163], v185\n\t"
        "s_waitcnt lgkmcnt(0)\n\t"
        "v_lshlrev_b32_sdwa v157, 5, v160 dst_sel:DWORD dst_unused:UNUSED_PAD src0_sel:DWORD src1_sel:WORD_0\n\t"
        "v_lshlrev_b32_sdwa v158, 5, v160 dst_sel:DWORD dst_unused:UNUSED_PAD src0_sel:DWORD src1_sel:WORD_1\n\t"
        "v_or_b32 v157, v184, v157\n\t"
        "v_or_b32 v158, v184, v158\n\t"
        "v_xor_b32 v159, 32, v157\n\t"
        "v_xor_b32 v152, 32, v158\n\t"
        "ds_read_b64_tr_b16 v[60:61], v157 offset:0\n\t"
        "ds_read_b64_tr_b16 v[62:63], v158 offset:0\n\t"
        "ds_read_b64_tr_b16 v[64:65], v159 offset:0\n\t"
        "ds_read_b64_tr_b16 v[66:67], v152 offset:0\n\t"
        "ds_read_b64_tr_b16 v[68:69], v157 offset:64\n\t"
        "ds_read_b64_tr_b16 v[70:71], v158 offset:64\n\t"
        "ds_read_b64_tr_b16 v[72:73], v159 offset:64\n\t"
        "ds_read_b64_tr_b16 v[74:75], v152 offset:64\n\t"
        "ds_read_b64_tr_b16 v[76:77], v157 offset:128\n\t"
        "ds_read_b64_tr_b16 v[78:79], v158 offset:128\n\t"
        "ds_read_b64_tr_b16 v[80:81], v159 offset:128\n\t"
        "ds_read_b64_tr_b16 v[82:83], v152 offset:128\n\t"
        "v_perm_b32 v164, v162, v162, v168\n\t"
        "v_perm_b32 v165, v162, v162, v169\n\t"
        "v_perm_b32 v166, v162, v162, v170\n\t"
        "v_perm_b32 v167, v162, v162, v171\n\t"
        "v_cmp_ne_u32 vcc, 0, v163\n\t"
        "s_waitcnt lgkmcnt(10)\n\t"
        "v_mfma_f32_16x16x32_bf16 v[48:51], v[60:63], v[164:167], 0\n\t"
        "s_waitcnt lgkmcnt(8)\n\t"
        "v_mfma_f32_16x16x32_bf16 v[52:55], v[64:67], v[164:167], 0\n\t"
        "s_waitcnt lgkmcnt(6)\n\t"
        "v_mfma_f32_16x16x32_bf16 v[56:59], v[68:71], v[164:167], 0\n\t"
        "s_waitcnt lgkmcnt(4)\n\t"
        "v_mfma_f32_16x16x32_bf16 v[116:119], v[72:75], v[164:167], 0\n\t"
        "s_waitcnt lgkmcnt(2)\n\t"
        "v_mfma_f32_16x16x32_bf16 v[120:123], v[76:79], v[164:167], 0\n\t"
        "s_waitcnt lgkmcnt(0)\n\t"
        "v_mfma_f32_16x16x32_bf16 v[124:127], v[80:83], v[164:167], 0\n\t"
        "s_cbranch_vccz 31f\n\t"
        "v_lshlrev_b32_sdwa v157, 5, v161 dst_sel:DWORD dst_unused:UNUSED_PAD src0_sel:DWORD src1_sel:WORD_0\n\t"
        "v_lshlrev_b32_sdwa v158, 5, v161 dst_sel:DWORD dst_unused:UNUSED_PAD src0_sel:DWORD src1_sel:WORD_1\n\t"
        "v_or_b32 v157, v184, v157\n\t"
        "v_or_b32 v158, v184, v158\n\t"
        "v_xor_b32 v159, 32, v157\n\t"
        "v_xor_b32 v152, 32, v158\n\t"
        "ds_read_b64_tr_b16 v[60:61], v157 offset:0\n\t"
        "ds_read_b64_tr_b16 v[62:63], v158 offset:0\n\t"
        "ds_read_b64_tr_b16 v[64:65], v159 offset:0\n\t"
        "ds_read_b64_tr_b16 v[66:67], v152 offset:0\n\t"
        "ds_read_b64_tr_b16 v[68:69], v157 offset:64\n\t"
        "ds_read_b64_tr_b16 v[70:71], v158 offset:64\n\t"
        "ds_read_b64_tr_b16 v[72:73], v159 offset:64\n\t"
        "ds_read_b64_tr_b16 v[74:75], v152 offset:64\n\t"
        "ds_read_b64_tr_b16 v[76:77], v157 offset:128\n\t"
        "ds_read_b64_tr_b16 v[78:79], v158 offset:128\n\t"
        "ds_read_b64_tr_b16 v[80:81], v159 offset:128\n\t"
        "ds_read_b64_tr_b16 v[82:83], v152 offset:128\n\t"
        "v_perm_b32 v164, v163, v163, v168\n\t"
        "v_perm_b32 v165, v163, v163, v169\n\t"
        "v_perm_b32 v166, v163, v163, v170\n\t"
        "v_perm_b32 v167, v163, v163, v171\n\t"
        "s_waitcnt lgkmcnt(10)\n\t"
        "s_nop 0\n\t"
        "v_mfma_f32_16x16x32_bf16 v[48:51], v[60:63], v[164:167], v[48:51]\n\t"
        "s_waitcnt lgkmcnt(8)\n\t"
        "v_mfma_f32_16x16x32_bf16 v[52:55], v[64:67], v[164:167], v[52:55]\n\t"
        "s_waitcnt lgkmcnt(6)\n\t"
        "v_mfma_f32_16x16x32_bf16 v[56:59], v[68:71], v[164:167], v[56:59]\n\t"
        "s_waitcnt lgkmcnt(4)\n\t"
        "v_mfma_f32_16x16x32_bf16 v[116:119], v[72:75], v[164:167], v[116:119]\n\t"
        "s_waitcnt lgkmcnt(2)\n\t"
        "v_mfma_f32_16x16x32_bf16 v[120:123], v[76:79], v[164:167], v[120:123]\n\t"
        "s_waitcnt lgkmcnt(0)\n\t"
        "v_mfma_f32_16x16x32_bf16 v[124:127], v[80:83], v[164:167], v[124:127]\n\t"
        "31:\n\t"
        "v_cvt_pk_bf16_f32 v48, v48, v49\n\t"
        "v_cvt_pk_bf16_f32 v49, v50, v51\n\t"
        "v_cvt_pk_bf16_f32 v52, v52, v53\n\t"
        "v_cvt_pk_bf16_f32 v53, v54, v55\n\t"
        "v_cvt_pk_bf16_f32 v56, v56, v57\n\t"
        "v_cvt_pk_bf16_f32 v57, v58, v59\n\t"
        "v_cvt_pk_bf16_f32 v116, v116, v117\n\t"
        "v_cvt_pk_bf16_f32 v117, v118, v119\n\t"
        "v_cvt_pk_bf16_f32 v120, v120, v121\n\t"
        "v_cvt_pk_bf16_f32 v121, v122, v123\n\t"
        "v_cvt_pk_bf16_f32 v124, v124, v125\n\t"
        "v_cvt_pk_bf16_f32 v125, v126, v127\n\t"
        "ds_write_b64 v186, v[48:49] offset:0\n\t"
        "ds_write_b64 v154, v[52:53] offset:0\n\t"
        "ds_write_b64 v186, v[56:57] offset:64\n\t"
        "ds_write_b64 v154, v[116:117] offset:64\n\t"
        "ds_write_b64 v186, v[120:121] offset:128\n\t"
        "ds_write_b64 v154, v[124:125] offset:128\n\t"
        "v_add_u32 v185, 256, v185\n\t"
        "v_add_u32 v186, 12288, v186\n\t"
        "v_xor_b32 v154, 32, v186\n\t"
        "s_sub_i32 s8, s8, 16\n\t"
        "s_cmp_gt_i32 s8, 0\n\t"
        "s_cbranch_scc1 30b\n\t"
        "v_mov_b32 v185, v151\n\t"
        "v_mov_b32 v186, v183\n\t"
        "v_xor_b32 v154, 32, v186\n\t"
        "s_waitcnt lgkmcnt(0)\n\t"
        "s_barrier\n\t"
        "s_branch .Lx57%=\n\t"
        ".Lx58%=:\n\t"
        "v_readlane_b32 s40, v155, 20\n\t"
        "v_readlane_b32 s43, v155, 8\n\t"
        "s_nop 0\n\t"
        "s_add_u32 m0, s40, s35\n\t"
        "s_nop 2\n\t"
        "buffer_load_dwordx4 v136, s[16:19], s43 offen  lds\n\t"
        "v_readlane_b32 s40, v155, 21\n\t"
        "v_readlane_b32 s43, v155, 9\n\t"
        "s_nop 0\n\t"
        "s_add_u32 m0, s40, s35\n\t"
        "s_nop 2\n\t"
        "buffer_load_dwordx4 v137, s[16:19], s43 offen  lds\n\t"
        "v_readlane_b32 s40, v155, 22\n\t"
        "v_readlane_b32 s43, v155, 10\n\t"
        "s_nop 0\n\t"
        "s_add_u32 m0, s40, s35\n\t"
        "s_nop 2\n\t"
        "buffer_load_dwordx4 v136, s[16:19], s43 offen  lds\n\t"
        "v_readlane_b32 s40, v155, 23\n\t"
        "v_readlane_b32 s43, v155, 11\n\t"
        "s_nop 0\n\t"
        "s_add_u32 m0, s40, s35\n\t"
        "s_nop 2\n\t"
        "buffer_load_dwordx4 v137, s[16:19], s43 offen  lds\n\t"
        "s_branch .Lx59%=\n\t"
        ".Lx60%=:\n\t"
        "v_mov_b32 v151, v185\n\t"
        "v_mov_b32 v183, v186\n\t"
        "s_min_u32 s8, s3, 64\n\t"
        "s_waitcnt lgkmcnt(0)\n\t"
        "30:\n\t"
        "ds_read_b128 v[160:163], v185\n\t"
        "s_waitcnt lgkmcnt(0)\n\t"
        "v_lshlrev_b32_sdwa v157, 5, v160 dst_sel:DWORD dst_unused:UNUSED_PAD src0_sel:DWORD src1_sel:WORD_0\n\t"
        "v_lshlrev_b32_sdwa v158, 5, v160 dst_sel:DWORD dst_unused:UNUSED_PAD src0_sel:DWORD src1_sel:WORD_1\n\t"
        "v_or_b32 v157, v184, v157\n\t"
        "v_or_b32 v158, v184, v158\n\t"
        "v_xor_b32 v159, 32, v157\n\t"
        "v_xor_b32 v152, 32, v158\n\t"
        "ds_read_b64_tr_b16 v[60:61], v157 offset:0\n\t"
        "ds_read_b64_tr_b16 v[62:63], v158 offset:0\n\t"
        "ds_read_b64_tr_b16 v[64:65], v159 offset:0\n\t"
        "ds_read_b64_tr_b16 v[66:67], v152 offset:0\n\t"
        "ds_read_b64_tr_b16 v[68:69], v157 offset:64\n\t"
        "ds_read_b64_tr_b16 v[70:71], v158 offset:64\n\t"
        "ds_read_b64_tr_b16 v[72:73], v159 offset:64\n\t"
        "ds_read_b64_tr_b16 v[74:75], v152 offset:64\n\t"
        "ds_read_b64_tr_b16 v[76:77], v157 offset:128\n\t"
        "ds_read_b64_tr_b16 v[78:79], v158 offset:128\n\t"
        "ds_read_b64_tr_b16 v[80:81], v159 offset:128\n\t"
        "ds_read_b64_tr_b16 v[82:83], v152 offset:128\n\t"
        "v_perm_b32 v164, v162, v162, v168\n\t"
        "v_perm_b32 v165, v162, v162, v169\n\t"
        "v_perm_b32 v166, v162, v162, v170\n\t"
        "v_perm_b32 v167, v162, v162, v171\n\t"
        "v_cmp_ne_u32 vcc, 0, v163\n\t"
        "s_waitcnt lgkmcnt(10)\n\t"
        "v_mfma_f32_16x16x32_bf16 v[48:51], v[60:63], v[164:167], 0\n\t"
        "s_waitcnt lgkmcnt(8)\n\t"
        "v_mfma_f32_16x16x32_bf16 v[52:55], v[64:67], v[164:167], 0\n\t"
        "s_waitcnt lgkmcnt(6)\n\t"
        "v_mfma_f32_16x16x32_bf16 v[56:59], v[68:71], v[164:167], 0\n\t"
        "s_waitcnt lgkmcnt(4)\n\t"
        "v_mfma_f32_16x16x32_bf16 v[116:119], v[72:75], v[164:167], 0\n\t"
        "s_waitcnt lgkmcnt(2)\n\t"
        "v_mfma_f32_16x16x32_bf16 v[120:123], v[76:79], v[164:167], 0\n\t"
        "s_waitcnt lgkmcnt(0)\n\t"
        "v_mfma_f32_16x16x32_bf16 v[124:127], v[80:83], v[164:167], 0\n\t"
        "s_cbranch_vccz 31f\n\t"
        "v_lshlrev_b32_sdwa v157, 5, v161 dst_sel:DWORD dst_unused:UNUSED_PAD src0_sel:DWORD src1_sel:WORD_0\n\t"
        "v_lshlrev_b32_sdwa v158, 5, v161 dst_sel:DWORD dst_unused:UNUSED_PAD src0_sel:DWORD src1_sel:WORD_1\n\t"
        "v_or_b32 v157, v184, v157\n\t"
        "v_or_b32 v158, v184, v158\n\t"
        "v_xor_b32 v159, 32, v157\n\t"
        "v_xor_b32 v152, 32, v158\n\t"
        "ds_read_b64_tr_b16 v[60:61], v157 offset:0\n\t"
        "ds_read_b64_tr_b16 v[62:63], v158 offset:0\n\t"
        "ds_read_b64_tr_b16 v[64:65], v159 offset:0\n\t"
        "ds_read_b64_tr_b16 v[66:67], v152 offset:0\n\t"
        "ds_read_b64_tr_b16 v[68:69], v157 offset:64\n\t"
        "ds_read_b64_tr_b16 v[70:71], v158 offset:64\n\t"
        "ds_read_b64_tr_b16 v[72:73], v159 offset:64\n\t"
        "ds_read_b64_tr_b16 v[74:75], v152 offset:64\n\t"
        "ds_read_b64_tr_b16 v[76:77], v157 offset:128\n\t"
        "ds_read_b64_tr_b16 v[78:79], v158 offset:128\n\t"
        "ds_read_b64_tr_b16 v[80:81], v159 offset:128\n\t"
        "ds_read_b64_tr_b16 v[82:83], v152 offset:128\n\t"
        "v_perm_b32 v164, v163, v163, v168\n\t"
        "v_perm_b32 v165, v163, v163, v169\n\t"
        "v_perm_b32 v166, v163, v163, v170\n\t"
        "v_perm_b32 v167, v163, v163, v171\n\t"
        "s_waitcnt lgkmcnt(10)\n\t"
        "s_nop 0\n\t"
        "v_mfma_f32_16x16x32_bf16 v[48:51], v[60:63], v[164:167], v[48:51]\n\t"
        "s_waitcnt lgkmcnt(8)\n\t"
        "v_mfma_f32_16x16x32_bf16 v[52:55], v[64:67], v[164:167], v[52:55]\n\t"
        "s_waitcnt lgkmcnt(6)\n\t"
        "v_mfma_f32_16x16x32_bf16 v[56:59], v[68:71], v[164:167], v[56:59]\n\t"
        "s_waitcnt lgkmcnt(4)\n\t"
        "v_mfma_f32_16x16x32_bf16 v[116:119], v[72:75], v[164:167], v[116:119]\n\t"
        "s_waitcnt lgkmcnt(2)\n\t"
        "v_mfma_f32_16x16x32_bf16 v[120:123], v[76:79], v[164:167], v[120:123]\n\t"
        "s_waitcnt lgkmcnt(0)\n\t"
        "v_mfma_f32_16x16x32_bf16 v[124:127], v[80:83], v[164:167], v[124:127]\n\t"
        "31:\n\t"
        "v_cvt_pk_bf16_f32 v48, v48, v49\n\t"
        "v_cvt_pk_bf16_f32 v49, v50, v51\n\t"
        "v_cvt_pk_bf16_f32 v52, v52, v53\n\t"
        "v_cvt_pk_bf16_f32 v53, v54, v55\n\t"
        "v_cvt_pk_bf16_f32 v56, v56, v57\n\t"
        "v_cvt_pk_bf16_f32 v57, v58, v59\n\t"
        "v_cvt_pk_bf16_f32 v116, v116, v117\n\t"
        "v_cvt_pk_bf16_f32 v117, v118, v119\n\t"
        "v_cvt_pk_bf16_f32 v120, v120, v121\n\t"
        "v_cvt_pk_bf16_f32 v121, v122, v123\n\t"
        "v_cvt_pk_bf16_f32 v124, v124, v125\n\t"
        "v_cvt_pk_bf16_f32 v125, v126, v127\n\t"
        "ds_write_b64 v186, v[48:49] offset:0\n\t"
        "ds_write_b64 v154, v[52:53] offset:0\n\t"
        "ds_write_b64 v186, v[56:57] offset:64\n\t"
        "ds_write_b64 v154, v[116:117] offset:64\n\t"
        "ds_write_b64 v186, v[120:121] offset:128\n\t"
        "ds_write_b64 v154, v[124:125] offset:128\n\t"
        "v_add_u32 v185, 256, v185\n\t"
        "v_add_u32 v186, 12288, v186\n\t"
        "v_xor_b32 v154, 32, v186\n\t"
        "s_sub_i32 s8, s8, 16\n\t"
        "s_cmp_gt_i32 s8, 0\n\t"
        "s_cbranch_scc1 30b\n\t"
        "v_mov_b32 v185, v151\n\t"
        "v_mov_b32 v186, v183\n\t"
        "v_xor_b32 v154, 32, v186\n\t"
        "s_waitcnt lgkmcnt(0)\n\t"
        "s_barrier\n\t"
        "s_branch .Lx61%=\n\t"
        ".Lx62%=:\n\t"
        "v_add_u32 v185, 1024, v185\n\t"
        "v_add_u32 v186, 49152, v186\n\t"
        "v_xor_b32 v154, 32, v186\n\t"
        "v_and_b32 v64, 63, v156\n\t"
        "v_add_u32 v60, 65, v64\n\t"
        "s_mul_i32 s8, s4, 3072\n\t"
        "s_add_u32 s8, s8, 73728\n\t"
        "v_mov_b32 v63, s62\n\t"
        "v_lshrrev_b32 v65, 4, v64\n\t"
        "v_mul_u32_u24 v65, 9216, v65\n\t"
        "v_lshlrev_b32 v66, 3, v64\n\t"
        "v_and_or_b32 v65, v66, 48, v65\n\t"
        "v_mul_u32_u24 v66, 192, v64\n\t"
        "v_add3_u32 v61, v65, v66, s8\n\t"
        "s_add_u32 s8, s35, 157696\n\t"
        "v_lshl_add_u32 v62, v64, 4, s8\n\t"
        "v_subrev_u32 v185, 1024, v185\n\t"
        "v_subrev_u32 v186, 61440, v186\n\t"
        "v_xor_b32 v154, 32, v186\n\t"
        "v_min_u32 v16, s61, v60\n\t"
        "v_lshl_add_u32 v16, v16, 3, s60\n\t"
        "ds_read_b64 v[0:1], v16\n\t"
        "v_cmp_ge_u32 s[66:67], s61, v60\n\t"
        "s_mov_b32 s46, 0x07060302\n\t"
        "s_waitcnt lgkmcnt(0)\n\t"
        "v_and_b32 v16, 0xfffff800, v1\n\t"
        "ds_read_b128 v[2:5], v16 offset:1536\n\t"
        "v_lshlrev_b32 v10, 16, v0\n\t"
        "v_and_b32 v11, 0xffff0000, v0\n\t"
        "v_med3_f32 v10, v10, 0, s63\n\t"
        "v_med3_f32 v11, v11, 0, s63\n\t"
        "s_mov_b64 exec, s[66:67]\n\t"
        "ds_write_b32 v1, v61\n\t"
        "s_mov_b64 exec, -1\n\t"
        "v_perm_b32 v0, v11, v10, s46\n\t"
        "v_fract_f32 v12, v10\n\t"
        "v_fract_f32 v13, v11\n\t"
        "v_cvt_u32_f32 v14, v10\n\t"
        "v_cvt_u32_f32 v15, v11\n\t"
        "v_cmp_neq_f32 s[64:65], 0, v12\n\t"
        "v_cmp_neq_f32 s[44:45], 0, v13\n\t"
        "v_sub_f32 v16, 1.0, v12\n\t"
        "v_sub_f32 v17, 1.0, v13\n\t"
        "v_cvt_pk_bf16_f32 v17, v16, v17\n\t"
        "v_lshlrev_b32 v16, 16, v17\n\t"
        "v_and_b32 v17, 0xffff0000, v17\n\t"
        "v_mul_f32 v18, v16, v17\n\t"
        "v_mul_f32 v16, v16, v13\n\t"
        "v_mul_f32 v17, v12, v17\n\t"
        "v_mul_f32 v19, v12, v13\n\t"
        "v_cndmask_b32 v16, v17, v16, s[44:45]\n\t"
        "v_cndmask_b32 v17, 0, v17, s[44:45]\n\t"
        "v_cvt_pk_bf16_f32 v8, v18, v16\n\t"
        "v_cvt_pk_bf16_f32 v9, v17, v19\n\t"
        "s_waitcnt lgkmcnt(1)\n\t"
        "v_pk_max_u16 v16, v0, v2\n\t"
        "v_pk_min_u16 v16, v16, v3\n\t"
        "v_cmp_ne_u32 vcc, v16, v0\n\t"
        "v_sub_u32 v16, v15, v5\n\t"
        "v_mad_u32_u24 v17, v15, 6, v4\n\t"
        "v_mad_u32_u24 v17, v14, s62, v17\n\t"
        "s_and_b64 vcc, vcc, s[66:67]\n\t"
        "s_or_b64 s[56:57], s[56:57], vcc\n\t"
        "v_bfe_u32 v18, v16, 2, 1\n\t"
        "v_or_b32 v18, v18, v17\n\t"
        "v_add_u32 v16, 1, v16\n\t"
        "v_bfe_u32 v16, v16, 2, 1\n\t"
        "v_add3_u32 v16, v17, v16, 6\n\t"
        "v_cndmask_b32 v16, v18, v16, s[44:45]\n\t"
        "v_cndmask_b32 v17, 0, v63, s[64:65]\n\t"
        "v_add_u32 v19, v18, v17\n\t"
        "v_add_u32 v20, v16, v17\n\t"
        "v_cndmask_b32 v16, v19, v16, s[44:45]\n\t"
        "v_lshl_or_b32 v6, v16, 16, v18\n\t"
        "v_lshl_or_b32 v7, v20, 16, v19\n\t"
        "ds_write_b128 v62, v[6:9]\n\t"
        "s_cmp_lg_u64 s[56:57], 0\n\t"
        "s_cselect_b32 s8, 1, 0\n\t"
        "v_mov_b32 v60, s8\n\t"
        "ds_write_b32 v153, v60 offset:16\n\t"
        "s_sub_u32 s47, s61, 64\n\t"
        "s_waitcnt lgkmcnt(0)\n\t"
        "50:\n\t"
        "ds_read_b128 v[160:163], v185\n\t"
        "s_waitcnt lgkmcnt(0)\n\t"
        "v_lshlrev_b32_sdwa v157, 5, v160 dst_sel:DWORD dst_unused:UNUSED_PAD src0_sel:DWORD src1_sel:WORD_0\n\t"
        "v_lshlrev_b32_sdwa v158, 5, v160 dst_sel:DWORD dst_unused:UNUSED_PAD src0_sel:DWORD src1_sel:WORD_1\n\t"
        "v_or_b32 v157, v184, v157\n\t"
        "v_or_b32 v158, v184, v158\n\t"
        "v_xor_b32 v159, 32, v157\n\t"
        "v_xor_b32 v152, 32, v158\n\t"
        "ds_read_b64_tr_b16 v[60:61], v157 offset:0\n\t"
        "ds_read_b64_tr_b16 v[62:63], v158 offset:0\n\t"
        "ds_read_b64_tr_b16 v[64:65], v159 offset:0\n\t"
        "ds_read_b64_tr_b16 v[66:67], v152 offset:0\n\t"
        "ds_read_b64_tr_b16 v[68:69], v157 offset:64\n\t"
        "ds_read_b64_tr_b16 v[70:71], v158 offset:64\n\t"
        "ds_read_b64_tr_b16 v[72:73], v159 offset:64\n\t"
        "ds_read_b64_tr_b16 v[74:75], v152 offset:64\n\t"
        "ds_read_b64_tr_b16 v[76:77], v157 offset:128\n\t"
        "ds_read_b64_tr_b16 v[78:79], v158 offset:128\n\t"
        "ds_read_b64_tr_b16 v[80:81], v159 offset:128\n\t"
        "ds_read_b64_tr_b16 v[82:83], v152 offset:128\n\t"
        "v_perm_b32 v164, v162, v162, v168\n\t"
        "v_perm_b32 v165, v162, v162, v169\n\t"
        "v_perm_b32 v166, v162, v162, v170\n\t"
        "v_perm_b32 v167, v162, v162, v171\n\t"
        "v_cmp_ne_u32 vcc, 0, v163\n\t"
        "s_waitcnt lgkmcnt(10)\n\t"
        "v_mfma_f32_16x16x32_bf16 v[0:3], v[60:63], v[164:167], 0\n\t"
        "s_waitcnt lgkmcnt(8)\n\t"
        "v_mfma_f32_16x16x32_bf16 v[4:7], v[64:67], v[164:167], 0\n\t"
        "s_waitcnt lgkmcnt(6)\n\t"
        "v_mfma_f32_16x16x32_bf16 v[8:11], v[68:71], v[164:167], 0\n\t"
        "s_waitcnt lgkmcnt(4)\n\t"
        "v_mfma_f32_16x16x32_bf16 v[12:15], v[72:75], v[164:167], 0\n\t"
        "s_waitcnt lgkmcnt(2)\n\t"
        "v_mfma_f32_16x16x32_bf16 v[16:19], v[76:79], v[164:167], 0\n\t"
        "s_waitcnt lgkmcnt(0)\n\t"
        "v_mfma_f32_16x16x32_bf16 v[20:23], v[80:83], v[164:167], 0\n\t"
        "s_cbranch_vccz 51f\n\t"
        "v_lshlrev_b32_sdwa v157, 5, v161 dst_sel:DWORD dst_unused:UNUSED_PAD src0_sel:DWORD src1_sel:WORD_0\n\t"
        "v_lshlrev_b32_sdwa v158, 5, v161 dst_sel:DWORD dst_unused:UNUSED_PAD src0_sel:DWORD src1_sel:WORD_1\n\t"
        "v_or_b32 v157, v184, v157\n\t"
        "v_or_b32 v158, v184, v158\n\t"
        "v_xor_b32 v159, 32, v157\n\t"
        "v_xor_b32 v152, 32, v158\n\t"
        "ds_read_b64_tr_b16 v[60:61], v157 offset:0\n\t"
        "ds_read_b64_tr_b16 v[62:63], v158 offset:0\n\t"
        "ds_read_b64_tr_b16 v[64:65], v159 offset:0\n\t"
        "ds_read_b64_tr_b16 v[66:67], v152 offset:0\n\t"
        "ds_read_b64_tr_b16 v[68:69], v157 offset:64\n\t"
        "ds_read_b64_tr_b16 v[70:71], v158 offset:64\n\t"
        "ds_read_b64_tr_b16 v[72:73], v159 offset:64\n\t"
        "ds_read_b64_tr_b16 v[74:75], v152 offset:64\n\t"
        "ds_read_b64_tr_b16 v[76:77], v157 offset:128\n\t"
        "ds_read_b64_tr_b16 v[78:79], v158 offset:128\n\t"
        "ds_read_b64_tr_b16 v[80:81], v159 offset:128\n\t"
        "ds_read_b64_tr_b16 v[82:83], v152 offset:128\n\t"
        "v_perm_b32 v164, v163, v163, v168\n\t"
        "v_perm_b32 v165, v163, v163, v169\n\t"
        "v_perm_b32 v166, v163, v163, v170\n\t"
        "v_perm_b32 v167, v163, v163, v171\n\t"
        "s_waitcnt lgkmcnt(10)\n\t"
        "s_nop 0\n\t"
        "v_mfma_f32_16x16x32_bf16 v[0:3], v[60:63], v[164:167], v[0:3]\n\t"
        "s_waitcnt lgkmcnt(8)\n\t"
        "v_mfma_f32_16x16x32_bf16 v[4:7], v[64:67], v[164:167], v[4:7]\n\t"
        "s_waitcnt lgkmcnt(6)\n\t"
        "v_mfma_f32_16x16x32_bf16 v[8:11], v[68:71], v[164:167], v[8:11]\n\t"
        "s_waitcnt lgkmcnt(4)\n\t"
        "v_mfma_f32_16x16x32_bf16 v[12:15], v[72:75], v[164:167], v[12:15]\n\t"
        "s_waitcnt lgkmcnt(2)\n\t"
        "v_mfma_f32_16x16x32_bf16 v[16:19], v[76:79], v[164:167], v[16:19]\n\t"
        "s_waitcnt lgkmcnt(0)\n\t"
        "v_mfma_f32_16x16x32_bf16 v[20:23], v[80:83], v[164:167], v[20:23]\n\t"
        "51:\n\t"
        "v_cvt_pk_bf16_f32 v0, v0, v1\n\t"
        "v_cvt_pk_bf16_f32 v1, v2, v3\n\t"
        "v_cvt_pk_bf16_f32 v4, v4, v5\n\t"
        "v_cvt_pk_bf16_f32 v5, v6, v7\n\t"
        "v_cvt_pk_bf16_f32 v8, v8, v9\n\t"
        "v_cvt_pk_bf16_f32 v9, v10, v11\n\t"
        "v_cvt_pk_bf16_f32 v12, v12, v13\n\t"
        "v_cvt_pk_bf16_f32 v13, v14, v15\n\t"
        "v_cvt_pk_bf16_f32 v16, v16, v17\n\t"
        "v_cvt_pk_bf16_f32 v17, v18, v19\n\t"
        "v_cvt_pk_bf16_f32 v20, v20, v21\n\t"
        "v_cvt_pk_bf16_f32 v21, v22, v23\n\t"
        "ds_write_b64 v186, v[0:1] offset:0\n\t"
        "ds_write_b64 v154, v[4:5] offset:0\n\t"
        "ds_write_b64 v186, v[8:9] offset:64\n\t"
        "ds_write_b64 v154, v[12:13] offset:64\n\t"
        "ds_write_b64 v186, v[16:17] offset:128\n\t"
        "ds_write_b64 v154, v[20:21] offset:128\n\t"
        "v_add_u32 v185, 256, v185\n\t"
        "v_add_u32 v186, 12288, v186\n\t"
        "v_xor_b32 v154, 32, v186\n\t"
        "s_sub_i32 s47, s47, 16\n\t"
        "s_cmp_gt_i32 s47, 0\n\t"
        "s_cbranch_scc1 50b\n\t"
        "s_branch .Lx63%=\n\t"
        "99:\n\t"
        "s_nop 7\n\t"
        "s_waitcnt vmcnt(0) lgkmcnt(0)\n\t"
        "s_barrier\n\t"
        "s_lshl_b32 s6, s6, 16\n\t"
        "v_or_b32 %0, s6, v156\n\t"
        : "+v"(tid) : "s"(ka), "s"(bid), "s"(recs)
        : "memory", "scc", "vcc", "s3", "s4", "s5", "s6", "s7", "s8", "s9", "s10", "s11", "s12", "s13", "s16", "s17", "s18", "s19", "s20", "s21", "s22", "s23", "s24", "s25", "s26", "s27", "s28", "s29", "s30", "s31", "s34", "s35", "s36", "s37", "s38", "s39", "s40", "s41", "s42", "s43", "s44", "s45", "s46", "s47", "s48", "s49", "s50", "s51", "s52", "s53", "s54", "s55", "s56", "s57", "s60", "s61", "s62", "s63", "s64", "s65", "s66", "s67", "s68", "s69", "s70", "s71", "s72", "s73", "s74", "s75", "s76", "s77", "s78", "s79", "s80", "s81", "s82", "s83", "s84", "s85", "s86", "s87", "s88", "s89", "s90", "s91", "s92", "s93", "s94", "s95", "s96", "s97", "s98", "s99", "v1", "v2", "v3", "v4", "v5", "v6", "v7", "v8", "v9", "v10", "v11", "v12", "v13", "v14", "v15", "v16", "v17", "v18", "v19", "v20", "v21", "v22", "v23", "v24", "v25", "v26", "v27", "v28", "v29", "v30", "v31", "v32", "v33", "v34", "v35", "v36", "v37", "v38", "v39", "v40", "v41", "v42", "v43", "v44", "v45", "v46", "v47", "v48", "v49", "v50", "v51", "v52", "v53", "v54", "v55", "v56", "v57", "v58", "v59", "v60", "v61", "v62", "v63", "v64", "v65", "v66", "v67", "v68", "v69", "v70", "v71", "v72", "v73", "v74", "v75", "v76", "v77", "v78", "v79", "v80", "v81", "v82", "v83", "v84", "v85", "v86", "v87", "v88", "v89", "v90", "v91", "v92", "v93", "v94", "v95", "v96", "v97", "v98", "v99", "v100", "v101", "v102", "v103", "v104", "v105", "v106", "v107", "v108", "v109", "v110", "v111", "v112", "v113", "v114", "v115", "v116", "v117", "v118", "v119", "v120", "v121", "v122", "v123", "v124", "v125", "v126", "v127", "v128", "v129", "v130", "v131", "v132", "v133", "v134", "v135", "v136", "v137", "v138", "v139", "v140", "v141", "v142", "v143", "v144", "v145", "v146", "v147", "v148", "v149", "v150", "v151", "v152", "v153", "v154", "v155", "v156", "v157", "v158", "v159", "v160", "v161", "v162", "v163", "v164", "v165", "v166", "v167", "v168", "v169", "v170", "v171", "v172", "v173", "v174", "v175", "v176", "v177", "v178", "v179", "v180", "v181", "v182", "v183", "v184", "v185", "v186", "v187", "v188", "v189", "v190", "v191", "v192", "v193", "v194", "v195", "v196", "v197", "v198", "v199", "v200", "v201", "v202", "v203", "v204", "v205", "v206", "v207", "v208", "v209", "v210", "v211", "v212", "v213", "v214", "v215", "v216", "v217", "v218", "v219", "v220", "v221", "v222", "v223", "v224", "v225", "v226", "v227", "v228", "v229", "v230", "v231", "v232", "v233", "v234", "v235", "v236", "v237", "v238", "v239", "v240", "v241", "v242", "v243", "v244", "v245", "v246", "v247", "v248", "v249", "v250", "v251", "v252", "v253", "v254", "v255", "a0", "a1", "a2", "a3", "a4", "a5", "a6", "a7", "a8", "a9", "a10", "a11", "a12", "a13", "a14", "a15", "a16", "a17", "a18", "a19", "a20", "a21", "a22", "a23", "a24", "a25", "a26", "a27", "a28", "a29", "a30", "a31", "a32", "a33", "a34", "a35", "a36", "a37", "a38", "a39", "a40", "a41", "a42", "a43", "a44", "a45", "a46", "a47", "a48", "a49", "a50", "a51", "a52", "a53", "a54", "a55", "a56", "a57", "a58", "a59", "a60", "a61", "a62", "a63", "a64", "a65", "a66", "a67", "a68", "a69", "a70", "a71", "a72", "a73", "a74", "a75", "a76", "a77", "a78", "a79", "a80", "a81", "a82", "a83", "a84", "a85", "a86", "a87", "a88", "a89", "a90", "a91", "a92", "a93", "a94", "a95", "a96", "a97", "a98", "a99", "a100", "a101", "a102", "a103", "a104", "a105", "a106", "a107", "a108", "a109", "a110", "a111", "a112", "a113", "a114", "a115", "a116", "a117", "a118", "a119", "a120", "a121", "a122", "a123", "a124", "a125", "a126", "a127", "a128", "a129", "a130", "a131", "a132", "a133", "a134", "a135", "a136", "a137", "a138", "a139", "a140", "a141", "a142", "a143", "a144", "a145", "a146", "a147", "a148", "a149", "a150", "a151", "a152", "a153", "a154", "a155", "a156", "a157", "a158", "a159", "a160", "a161", "a162", "a163", "a164", "a165", "a166", "a167", "a168", "a169", "a170", "a171", "a172", "a173", "a174", "a175", "a176", "a177", "a178", "a179", "a180", "a181", "a182", "a183", "a184", "a185", "a186", "a187", "a188", "a189", "a190", "a191", "a192", "a193", "a194", "a195", "a196", "a197", "a198", "a199", "a200", "a201", "a202", "a203", "a204", "a205", "a206", "a207", "a208", "a209", "a210", "a211", "a212", "a213", "a214", "a215", "a216", "a217", "a218", "a219", "a220", "a221", "a222", "a223", "a224", "a225", "a226", "a227", "a228", "a229", "a230", "a231", "a232", "a233", "a234", "a235", "a236", "a237", "a238", "a239", "a240", "a241", "a242", "a243", "a244", "a245", "a246", "a247", "a248", "a249", "a250", "a251", "a252", "a253", "a254", "a255");
    asm volatile("" : "+s"(ka), "+s"(bid) :: "memory");
    {
        typedef short v4s __attribute__((ext_vector_type(4)));
        const unsigned short *x = ka->x, *w = ka->w;
        const unsigned* off = (const unsigned*)ka->off;
        unsigned short* out = ka->out;
        const unsigned tw = ka->t.w[bid], redo = __builtin_amdgcn_readfirstlane(tid >> 16);
        const unsigned* rc = recs + bid * 64;
        tid &= 255;
        const int lane = tid & 63, li = lane & 15, q = lane >> 4, wv = __builtin_amdgcn_readfirstlane(tid >> 6);
        int pix[4] = {-1, -1, -1, -1}, n = 0;
#pragma unroll 1
        for (int s = 0; s < 14; ++s) {
            const unsigned o = s < 8 ? (((tw & 255) + s) * 224 + ((tw >> 8) & 255)) * 192 : rc[49 + ((s - 8) >> 1)] + (s & 1) * (43200 - rc[30]);
            if (!((redo >> (s < 8 ? 8 + (s >> 1) : 0)) & 1) || o >= 224 * 224 * 192) continue;
            const int p = (o + li * (s < 8 ? 192 : rc[30])) / 192;
#pragma unroll
            for (int i = 0; i < 4; ++i) pix[i] = n == wv + 4 * i ? p : pix[i];
            ++n;
        }
        f32x4 acc[4][6];
        unsigned od[4];
        u32x4 wn[5];
#pragma unroll
        for (int i = 0; i < 4; ++i) {
            od[i] = off[max(pix[i], 0) * 9];
#pragma unroll
            for (int k = 0; k < 6; ++k) acc[i][k] = (f32x4){0.f, 0.f, 0.f, 0.f};
        }
#pragma unroll
        for (int k = 0; k < 5; ++k) wn[k] = *(const u32x4*)(w + min(tid + 256 * k, 1151u) * 8);
        const __amdgpu_buffer_rsrc_t xr = __builtin_amdgcn_make_buffer_rsrc((void*)x, 0, 224 * 224 * 192, 0x00020000);
        const unsigned wb = (8 * q + (li >> 2)) * 192 + (li & 3) * 8;
#pragma unroll 1
        for (int t = 0; t < 9; ++t) {
            unsigned char* slab = lds + (t & 1) * 18432;
#pragma unroll
            for (int k = 0; k < 5; ++k) {
                const unsigned c = min(tid + 256 * k, 1151u);
                *(u32x4*)(slab + ((c * 16) ^ (((c / 96) & 1) << 5))) = wn[k];
            }
            __syncthreads();
            u32x4 wf[3][6];
#pragma unroll
            for (int k = 0; k < 18; ++k) {
                __attribute__((address_space(3))) unsigned char* a = (__attribute__((address_space(3))) unsigned char*)slab + (k / 6) * 6144 + wb + (((k % 6) * 32) ^ ((q & 1) << 5));
                const u32x2 lo = __builtin_bit_cast(u32x2, __builtin_amdgcn_ds_read_tr16_b64_v4i16((__attribute__((address_space(3))) v4s*)a));
                const u32x2 hi = __builtin_bit_cast(u32x2, __builtin_amdgcn_ds_read_tr16_b64_v4i16((__attribute__((address_space(3))) v4s*)(a + 768)));
                wf[k / 6][k % 6] = (u32x4){lo[0], lo[1], hi[0], hi[1]};
            }
            const int tn = min(t + 1, 8);
#pragma unroll
            for (int k = 0; k < 5; ++k) wn[k] = *(const u32x4*)(w + tn * 9216 + min(tid + 256 * k, 1151u) * 8);
#pragma unroll
            for (int i0 = 0; i0 < 4; i0 += 2) {
                if (wv + 4 * i0 >= n) continue;
                u32x4 g[2][3][4];
                float wt[2][4];
#pragma unroll
                for (int j = 0; j < 2; ++j) {
                    const int i = i0 + j, p = max(pix[i], 0), h = p / 224, c = p - h * 224;
                    float py = rb((float)(h + t / 3 - 1) + bf_lo(od[i])), px = rb((float)(c + t % 3 - 1) + bf_hi(od[i]));
                    od[i] = off[p * 9 + tn];
                    py = fminf(fmaxf(py, 0.f), 223.f); px = fminf(fmaxf(px, 0.f), 223.f);
                    const float y0 = floorf(py), x0 = floorf(px), y1 = fminf(y0 + 1.f, 223.f), x1 = fminf(x0 + 1.f, 223.f);
                    const float wy = py - y0, wx = px - x0, a = rb(1.f - wy), b = rb(1.f - wx);
                    wt[j][0] = rb(a * b); wt[j][1] = rb(a * wx); wt[j][2] = rb(wy * b); wt[j][3] = rb(wy * wx);
                    int ad[4] = {(int)y0 * 224 + (int)x0, (int)y0 * 224 + (int)x1, (int)y1 * 224 + (int)x0, (int)y1 * 224 + (int)x1};
#pragma unroll
                    for (int k = 0; k < 4; ++k) ad[k] = wt[j][k] != 0.f && pix[i] >= 0 ? ad[k] * 192 + q * 16 : 0x40000000;
#pragma unroll
                    for (int k = 0; k < 4; ++k) {
#pragma unroll
                        for (int ks = 0; ks < 3; ++ks) g[j][ks][k] = (u32x4){0u, 0u, 0u, 0u};
                        if (__builtin_amdgcn_ballot_w64(ad[k] != 0x40000000) == 0) continue;
#pragma unroll
                        for (int ks = 0; ks < 3; ++ks) g[j][ks][k] = __builtin_amdgcn_raw_buffer_load_b128(xr, ad[k] + ks * 64, 0, 0);
                    }
                }
                asm volatile("" ::: "memory");
#pragma unroll
                for (int j = 0; j < 2; ++j) {
                    if (wv + 4 * (i0 + j) >= n) continue;
#pragma unroll
                    for (int ks = 0; ks < 3; ++ks) {
                        u32x4 bfr;
#pragma unroll
                        for (int d = 0; d < 4; ++d) {
                            float lo = 0.f, hi = 0.f;
#pragma unroll
                            for (int cn = 0; cn < 4; ++cn) {
                                const unsigned u = g[j][ks][cn][d];
                                const bf16x2 v = __builtin_bit_cast(bf16x2, u);
                                lo = __builtin_amdgcn_fdot2_f32_bf16(v, __builtin_bit_cast(bf16x2, __float_as_uint(wt[j][cn]) >> 16), lo, false);
                                hi = __builtin_amdgcn_fdot2_f32_bf16(v, __builtin_bit_cast(bf16x2, __float_as_uint(wt[j][cn])), hi, false);
                            }
                            bfr[d] = cvt_pk(lo, hi);
                        }
#pragma unroll
                        for (int k = 0; k < 6; ++k)
                            acc[i0 + j][k] = __builtin_amdgcn_mfma_f32_16x16x32_bf16(__builtin_bit_cast(bf16x8, wf[ks][k]), __builtin_bit_cast(bf16x8, bfr), acc[i0 + j][k], 0, 0, 0);
                    }
                }
            }
        }
#pragma unroll
        for (int i = 0; i < 4; ++i) {
            if (wv + 4 * i >= n) continue;
#pragma unroll
            for (int k = 0; k < 6; ++k) {
                u32x2 o; o[0] = cvt_pk(acc[i][k][0], acc[i][k][1]); o[1] = cvt_pk(acc[i][k][2], acc[i][k][3]);
                *(u32x2*)(out + pix[i] * 96 + k * 16 + q * 4) = o;
            }
        }
    }
}
extern "C" void kernel_launch(void* const* d_in, const int* in_sizes, int n_in, void* d_out, int out_size, void* d_ws, size_t ws_size, hipStream_t stream) {
    const Tiles tl = {{0x13022, 0x1406c, 0x250b2, 0x2703a, 0x18086, 0x290ca, 0x2b056, 0x1c0a0, 0x1302a, 0x14074, 0x250ba, 0x27042, 0x1808e, 0x290d2, 0x2b05e, 0x1c0a8, 0x13032, 0x7407c, 0x250c2, 0x2704a, 0x18096, 0x290da, 0x2b066, 0x1c0b0, 0x7303a, 0x14086, 0x250ca, 0x27052, 0x1809e, 0x2a022, 0x2b06e, 0x1c0b8, 0x3044, 0x1408e, 0x50d2, 0x2705a, 0x180a6, 0xa02a, 0x2b076, 0x1c0c0, 0x1304c, 0x14096, 0x250da, 0x7062, 0x280ae, 0x2a032, 0x2b07e, 0x1c0c8, 0x13054, 0x409e, 0x26022, 0x2706a, 0x80b6, 0x3a03a, 0xb086, 0xc0d0, 0x1305c, 0x140a6, 0x2602a, 0x27072, 0x380be, 0x2a044, 0x1b08e, 0x1c0d8, 0x13064, 0x140ae, 0x26032, 0x707a, 0x280c8, 0x2a04c, 0x1b096, 0x1d022, 0x1306c, 0x140b6, 0x7603a, 0x27088, 0x280d0, 0x2a054, 0x1b09e, 0x1d02a, 0x13074, 0x740be, 0x26044, 0x27090, 0x280d8, 0x2a05c, 0x1b0a6, 0x1d032, 0x7307c, 0x140c8, 0x2604c, 0x27098, 0x29022, 0x2a064, 0x3b0ae, 0x1d03a, 0x13086, 0x140d0, 0x26054, 0x270a0, 0x2902a, 0x2a06c, 0x1b0b8, 0x1d042, 0x308e, 0x140d8, 0x605c, 0x70a8, 0x29032, 0xa074, 0x1b0c0, 0xd04a, 0x13096, 0x5022, 0x26064, 0x270b0, 0x903a, 0x3a07c, 0xb0c8, 0x1d052, 0x1309e, 0x1502a, 0x2606c, 0x270b8, 0x29042, 0x2a086, 0x1b0d0, 0x1d05a, 0x130a6, 0x15032, 0x26074, 0x270c0, 0x2904a, 0x2a08e, 0x1b0d8, 0x1d062, 0x130ae, 0x1503a, 0x7607c, 0x270c8, 0x29052, 0x2a096, 0x1c022, 0x1d06a, 0x130b6, 0x15042, 0x26086, 0x270d0, 0x2905a, 0x2a09e, 0x1c02a, 0x1d072, 0x730be, 0x1504a, 0x2608e, 0x270d8, 0x29062, 0x2a0a6, 0x1c032, 0x1d07a, 0x130c8, 0x15052, 0x26096, 0x28022, 0x2906a, 0x2a0ae, 0x1c03a, 0xd082, 0x30d0, 0x1505a, 0x609e, 0x802a, 0x29072, 0xa0b6, 0x1c042, 0x1d08a, 0x130d8, 0x5062, 0x260a6, 0x28032, 0x907a, 0x3a0be, 0xc04a, 0x1d092, 0x14022, 0x1506a, 0x260ae, 0x3803a, 0x29082, 0x2a0c8, 0x1c052, 0x1d09a, 0x1402a, 0x15072, 0x260b6, 0x28044, 0x2908a, 0x2a0d0, 0x1c05a, 0x1d0a2, 0x14032, 0x1507a, 0x360be, 0x2804c, 0x29092, 0x2a0d8, 0x1c062, 0x1d0aa, 0x7403a, 0x15082, 0x260c8, 0x28054, 0x2909a, 0x2b022, 0x1c06a, 0x1d0b2, 0x14044, 0x1508a, 0x260d0, 0x2805c, 0x290a2, 0x2b02a, 0x1c072, 0x1d0ba, 0x1404c, 0x15092, 0x260d8, 0x28064, 0x290aa, 0x2b032, 0xc07a, 0xd0c2, 0x4054, 0x509a, 0x27022, 0x806c, 0x90b2, 0x2b03a, 0x1c088, 0x1d0ca, 0x1405c, 0x650a2, 0x702a, 0x28074, 0x290ba, 0xb042, 0xc090, 0x1d0d2, 0x14064, 0x250aa, 0x27032, 0x3807c, 0x290c2, 0xb04a, 0x1c098, 0x1d0da}};
    dcn_kernel<<<256, 256, 0, stream>>>((const unsigned short*)d_in[0], (const unsigned short*)d_in[1], (const unsigned short*)d_in[2], (unsigned short*)d_out, d_ws, tl);
}
```

```cpp
#include <hip/hip_runtime.h>
#include <stdint.h>
typedef __attribute__((ext_vector_type(8))) __bf16 bf16x8;
typedef __attribute__((ext_vector_type(2))) __bf16 bf16x2;
typedef __attribute__((ext_vector_type(4))) float f32x4;
typedef __attribute__((ext_vector_type(4))) unsigned u32x4;
typedef __attribute__((ext_vector_type(2))) unsigned u32x2;
struct Args { const unsigned short *x, *off, *w; unsigned short* out; void* ws; };
static __device__ const unsigned recs[256 * 64] = {
1290240,1290240,1333248,1333248,1376256,1376256,1419264,1419264,1462272,1462272,1505280,1505280,40960,44032,48128,51200,55296,58368,62464,65536,69632,72704,76800,79872,268439552,336991254,57,807541760,64,7488,192,1040137727,16780300,50465804,576,36,224,1107296256,1107296256,1107296256,1098907648,1098907648,0,258624,1073741824,258048,51200,51200,48128,1379328,1073741824,1376256,16880,1107575308,4294961868,4294967294,16880,1107575308,4294961868,4294967294,16880,1107575308,4294961868,4294967294,
2623488,2666496,2709504,2752512,2795520,2838528,2881536,2924544,2924544,2924544,2924544,2924544,40960,45056,49152,53248,57344,61440,65536,69632,69632,69632,69632,69632,538976288,353703189,57,1080164608,80,7488,192,1040137855,16780300,50465804,576,36,128,1115947008,1115684864,1115160576,1107296256,1107296256,1107296256,533376,517248,501120,57344,49152,40960,2844672,2758656,2672640,1106264692,1112031880,4294960588,30,1106264692,1112031880,4294960588,30,1106264692,1112031880,4294960588,30,
5548032,5548032,5591040,5591040,5634048,5634048,5677056,5677056,5677056,5677056,5677056,5677056,40960,44032,48128,51200,55296,58368,62464,65536,62464,65536,62464,65536,268439552,336991254,57,1352798208,80,7488,192,1048526591,16780300,50465804,576,36,224,1124204544,1124204544,1124204544,1098907648,1098907648,0,1048896,1073741824,1048320,44032,44032,40960,5594112,1073741824,5591040,17153,1107575556,4294939692,4294967294,17153,1107575556,4294939692,4294967294,17153,1107575556,4294939692,4294967294,
8640,8640,8832,8832,9024,9024,9216,9216,9216,9216,9216,9216,40960,44032,48128,51200,55296,58368,62464,65536,62464,65536,62464,65536,268439552,336991254,410,1881808896,80,7984,43008,1048526335,50465804,16780300,36,80,224,1110966272,1110966272,1110966272,1098907648,1098907648,0,130680,1073741824,1656,44032,44032,40960,696960,1073741824,8832,16948,1107575360,4294958508,4294967294,16948,1107575360,4294958508,4294967294,16948,1107575360,4294958508,4294967294,
18240,18240,18432,18432,18624,18624,18816,18816,18816,18816,18816,18816,40960,44032,48128,51200,55296,58368,62464,65536,62464,65536,62464,65536,268439552,336991254,410,2154962944,80,7984,43008,1048526463,50465804,16780300,36,80,224,1119879168,1119879168,1119879168,1098907648,1098907648,0,132480,1073741824,3456,44032,44032,40960,706560,1073741824,18432,17086,1107575492,4294947308,4294967294,17086,1107575492,4294947308,4294967294,17086,1107575492,4294947308,4294967294,
26688,26688,26880,26880,27072,27072,27264,27264,27264,27264,27264,27264,40960,44032,48128,51200,55296,58368,62464,65536,62464,65536,62464,65536,268439552,336991254,410,2428116992,80,7984,43008,1056915199,50465804,16780300,36,80,224,1124859904,1124859904,1124859904,1098907648,1098907648,0,134064,1073741824,5040,44032,44032,40960,715008,1073741824,26880,17163,1107575566,4294937452,4294967294,17163,1107575566,4294937452,4294967294,17163,1107575566,4294937452,4294967294,
35520,35520,35712,35712,35904,35904,36096,36096,36096,36096,36096,36096,40960,44032,48128,51200,55296,58368,62464,65536,62464,65536,62464,65536,268439552,336991254,410,2957647872,80,7984,43008,1056915071,50465804,16780300,36,80,224,1127874560,1127874560,1127874560,1098907648,1098907648,0,135720,1073741824,6696,44032,44032,40960,723840,1073741824,35712,17209,1107575612,4294927148,4294967294,17209,1107575612,4294927148,4294967294,17209,1107575612,4294927148,4294967294,
7526400,7526400,7569408,7569408,7612416,7612416,7655424,7655424,7655424,7655424,7655424,7655424,40960,44032,48128,51200,55296,58368,62464,65536,62464,65536,62464,65536,268439552,336991254,57,3230797824,80,7488,192,1056915199,16780300,50465804,576,36,224,1127219200,1127219200,1127219200,1098907648,1098907648,0,1419840,1073741824,1419264,44032,44032,40960,7572480,1073741824,7569408,17199,1107575602,4294929388,4294967294,17199,1107575602,4294929388,4294967294,17199,1107575602,4294929388,4294967294,
1419264,1419264,1462272,1462272,1505280,1505280,1548288,1548288,1548288,1548288,1548288,1548288,40960,44032,48128,51200,55296,58368,62464,65536,62464,65536,62464,65536,268439552,336991254,57,808062976,80,7488,192,1040137727,16780300,50465804,576,36,224,1107820544,1107820544,1107820544,1098907648,1098907648,0,274752,1073741824,274176,44032,44032,40960,1465344,1073741824,1462272,16900,1107575312,4294961196,4294967294,16900,1107575312,4294961196,4294967294,16900,1107575312,4294961196,4294967294,
3655680,3655680,3698688,3698688,3741696,3741696,3784704,3784704,3784704,3784704,3784704,3784704,40960,44032,48128,51200,55296,58368,62464,65536,62464,65536,62464,65536,268439552,336991254,57,1080692736,80,7488,192,1040137855,16780300,50465804,576,36,224,1118568448,1118568448,1118568448,1098907648,1098907648,0,694080,1073741824,693504,44032,44032,40960,3701760,1073741824,3698688,17066,1107575472,4294949548,4294967294,17066,1107575472,4294949548,4294967294,17066,1107575472,4294949548,4294967294,
5634048,5634048,5677056,5677056,5720064,5720064,5763072,5763072,5763072,5763072,5763072,5763072,40960,44032,48128,51200,55296,58368,62464,65536,62464,65536,62464,65536,268439552,336991254,57,1353322496,80,7488,192,1048526591,16780300,50465804,576,36,224,1124335616,1124335616,1124335616,1098907648,1098907648,0,1065024,1073741824,1064448,44032,44032,40960,5680128,1073741824,5677056,17155,1107575558,4294939244,4294967294,17155,1107575558,4294939244,4294967294,17155,1107575558,4294939244,4294967294,
9024,9024,9216,9216,9408,9408,9600,9600,9600,9600,9600,9600,40960,44032,48128,51200,55296,58368,62464,65536,62464,65536,62464,65536,268439552,336991254,410,1882333184,80,7984,43008,1048526335,50465804,16780300,36,80,224,1111490560,1111490560,1111490560,1098907648,1098907648,0,130752,1073741824,1728,44032,44032,40960,697344,1073741824,9216,16956,1107575368,4294958060,4294967294,16956,1107575368,4294958060,4294967294,16956,1107575368,4294958060,4294967294,
18624,18624,18816,18816,19008,19008,19200,19200,19200,19200,19200,19200,40960,44032,48128,51200,55296,58368,62464,65536,62464,65536,62464,65536,268439552,336991254,410,2155487232,80,7984,43008,1048526463,50465804,16780300,36,80,224,1120141312,1120141312,1120141312,1098907648,1098907648,0,132552,1073741824,3528,44032,44032,40960,706944,1073741824,18816,17090,1107575496,4294946860,4294967294,17090,1107575496,4294946860,4294967294,17090,1107575496,4294946860,4294967294,
86016,129024,172032,215040,258048,301056,8730624,8773632,8773632,8773632,8773632,8773632,49152,53248,57344,61440,65536,69632,40960,45056,45056,45056,45056,45056,2425356288,320017685,57,2428645888,80,7488,192,1056915199,16780300,50465804,576,36,128,1128923136,1128923136,1082130432,1125122048,1125122048,0,1634112,1073741824,32256,32768,32768,53248,8715264,1073741824,172032,1125073729,1126187852,4294941740,142,1125073729,1126187852,4294941740,142,16384,1099972832,1292,4294967294,
35904,35904,36096,36096,36288,36288,36480,36480,36480,36480,36480,36480,40960,44032,48128,51200,55296,58368,62464,65536,62464,65536,62464,65536,268439552,336991254,410,2958172160,80,7984,43008,1056915071,50465804,16780300,36,80,224,1128005632,1128005632,1128005632,1098907648,1098907648,0,135792,1073741824,6768,44032,44032,40960,724224,1073741824,36096,17211,1107575614,4294926700,4294967294,17211,1107575614,4294926700,4294967294,17211,1107575614,4294926700,4294967294,
7612416,7612416,7655424,7655424,7698432,7698432,7741440,7741440,7741440,7741440,7741440,7741440,40960,44032,48128,51200,55296,58368,62464,65536,62464,65536,62464,65536,268439552,336991254,57,3231322112,80,7488,192,1056915199,16780300,50465804,576,36,224,1127350272,1127350272,1127350272,1098907648,1098907648,0,1435968,1073741824,1435392,44032,44032,40960,7658496,1073741824,7655424,17201,1107575604,4294928940,4294967294,17201,1107575604,4294928940,4294967294,17201,1107575604,4294928940,4294967294,
1290240,1333248,1376256,1419264,1462272,1505280,1548288,1591296,1634304,1634304,1634304,1634304,40960,45056,49152,53248,57344,61440,65536,69632,73728,73728,73728,73728,538976288,353703189,57,808584448,64,7488,192,1040137727,16780300,50465804,576,36,128,1108344832,1107820544,1107296256,1107296256,1107296256,1107296256,291456,275328,259200,61440,53248,45056,1554432,1468416,1382400,1106264560,1112031768,4294964556,30,1106264560,1112031768,4294964556,30,1106264560,1112031768,4294964556,30,
3741696,3741696,3784704,3784704,3827712,3827712,3870720,3870720,3870720,3870720,3870720,3870720,40960,44032,48128,51200,55296,58368,62464,65536,62464,65536,62464,65536,268439552,336991254,57,1081217024,80,7488,192,1040137855,16780300,50465804,576,36,224,1118830592,1118830592,1118830592,1098907648,1098907648,0,710208,1073741824,709632,44032,44032,40960,3787776,1073741824,3784704,17070,1107575476,4294949100,4294967294,17070,1107575476,4294949100,4294967294,17070,1107575476,4294949100,4294967294,
5720064,5720064,5763072,5763072,5806080,5806080,5849088,5849088,5849088,5849088,5849088,5849088,40960,44032,48128,51200,55296,58368,62464,65536,62464,65536,62464,65536,268439552,336991254,57,1353846784,80,7488,192,1048526591,16780300,50465804,576,36,224,1124466688,1124466688,1124466688,1098907648,1098907648,0,1081152,1073741824,1080576,44032,44032,40960,5766144,1073741824,5763072,17157,1107575560,4294938796,4294967294,17157,1107575560,4294938796,4294967294,17157,1107575560,4294938796,4294967294,
9408,9408,9600,9600,9792,9792,9984,9984,9984,9984,9984,9984,40960,44032,48128,51200,55296,58368,62464,65536,62464,65536,62464,65536,268439552,336991254,410,1882857472,80,7984,43008,1048526335,50465804,16780300,36,80,224,1112014848,1112014848,1112014848,1098907648,1098907648,0,130824,1073741824,1800,44032,44032,40960,697728,1073741824,9600,16964,1107575376,4294957612,4294967294,16964,1107575376,4294957612,4294967294,16964,1107575376,4294957612,4294967294,
19008,19008,19200,19200,19392,19392,19584,19584,19584,19584,19584,19584,40960,44032,48128,51200,55296,58368,62464,65536,62464,65536,62464,65536,268439552,336991254,410,2156011520,80,7984,43008,1048526591,50465804,16780300,36,80,224,1120403456,1120403456,1120403456,1098907648,1098907648,0,132624,1073741824,3600,44032,44032,40960,707328,1073741824,19200,17094,1107575500,4294946412,4294967294,17094,1107575500,4294946412,4294967294,17094,1107575500,4294946412,4294967294,
430080,473088,516096,559104,602112,645120,9160704,9203712,9203712,9203712,9203712,9203712,49152,53248,57344,61440,65536,69632,40960,45056,45056,45056,45056,45056,2425356288,320017685,57,2429301248,80,7488,192,1056915199,16780300,50465804,576,36,128,1129578496,1129578496,1094713344,1125122048,1125122048,0,1714752,1073741824,96768,32768,32768,53248,9145344,1073741824,516096,1125073739,1126187862,4294940460,142,1125073739,1126187862,4294940460,142,16672,1099972976,268,4294967294,
36288,36288,36480,36480,36672,36672,36864,36864,36864,36864,36864,36864,40960,44032,48128,51200,55296,58368,62464,65536,62464,65536,62464,65536,268439552,336991254,410,2958696448,80,7984,43008,1056915071,50465804,16780300,36,80,224,1128136704,1128136704,1128136704,1098907648,1098907648,0,135864,1073741824,6840,44032,44032,40960,724608,1073741824,36480,17213,1107575616,4294926252,4294967294,17213,1107575616,4294926252,4294967294,17213,1107575616,4294926252,4294967294,
7698432,7698432,7741440,7741440,7784448,7784448,7827456,7827456,7827456,7827456,7827456,7827456,40960,44032,48128,51200,55296,58368,62464,65536,62464,65536,62464,65536,268439552,336991254,57,3231846400,80,7488,192,1056915199,16780300,50465804,576,36,224,1127481344,1127481344,1127481344,1098907648,1098907648,0,1452096,1073741824,1451520,44032,44032,40960,7744512,1073741824,7741440,17203,1107575606,4294928492,4294967294,17203,1107575606,4294928492,4294967294,17203,1107575606,4294928492,4294967294,
1505280,1505280,1548288,1548288,1591296,1591296,1634304,1634304,1634304,1634304,1634304,1634304,40960,44032,48128,51200,55296,58368,62464,65536,62464,65536,62464,65536,268439552,336991254,57,809111552,80,7488,192,1040137727,16780300,50465804,576,36,224,1108344832,1108344832,1108344832,1098907648,1098907648,0,290880,1073741824,290304,44032,44032,40960,1551360,1073741824,1548288,16908,1107575320,4294960748,4294967294,16908,1107575320,4294960748,4294967294,16908,1107575320,4294960748,4294967294,
3827712,3827712,3870720,3870720,3913728,3913728,3956736,3956736,3956736,3956736,3956736,3956736,40960,44032,48128,51200,55296,58368,62464,65536,62464,65536,62464,65536,268439552,336991254,57,1081741312,80,7488,192,1040137855,16780300,50465804,576,36,224,1119092736,1119092736,1119092736,1098907648,1098907648,0,726336,1073741824,725760,44032,44032,40960,3873792,1073741824,3870720,17074,1107575480,4294948652,4294967294,17074,1107575480,4294948652,4294967294,17074,1107575480,4294948652,4294967294,
3913728,3956736,3999744,4042752,4085760,4128768,4171776,4214784,4214784,4214784,4214784,4214784,40960,45056,49152,53248,57344,61440,65536,69632,69632,69632,69632,69632,538976288,353703189,57,1354367232,80,7488,192,1048526591,16780300,50465804,576,36,128,1119879168,1119617024,1119354880,1107296256,1107296256,1107296256,775296,759168,743040,57344,49152,40960,4134912,4048896,3962880,1106264758,1112031940,4294956748,30,1106264758,1112031940,4294956748,30,1106264758,1112031940,4294956748,30,
4945920,4988928,5031936,5074944,5117952,5160960,5203968,5246976,5246976,5246976,5246976,5246976,40960,45056,49152,53248,57344,61440,65536,69632,69632,69632,69632,69632,538976288,353703189,57,1883373824,80,7488,192,1048526463,16780300,50465804,576,36,128,1123024896,1122762752,1122500608,1107296256,1107296256,1107296256,968832,952704,936576,57344,49152,40960,5167104,5081088,4995072,1106264806,1112031988,4294953676,30,1106264806,1112031988,4294953676,30,1106264806,1112031988,4294953676,30,
19392,19392,19584,19584,19776,19776,19968,19968,19968,19968,19968,19968,40960,44032,48128,51200,55296,58368,62464,65536,62464,65536,62464,65536,268439552,336991254,410,2156535808,80,7984,43008,1048526591,50465804,16780300,36,80,224,1120665600,1120665600,1120665600,1098907648,1098907648,0,132696,1073741824,3672,44032,44032,40960,707712,1073741824,19584,17098,1107575504,4294945964,4294967294,17098,1107575504,4294945964,4294967294,17098,1107575504,4294945964,4294967294,
946176,989184,1032192,1075200,1118208,1161216,9590784,9590784,9590784,9590784,9590784,9590784,49152,53248,57344,61440,65536,69632,40960,45056,45056,45056,45056,45056,2425356288,320017685,57,2429956096,80,7488,192,1056915199,16780300,50465804,576,36,128,1130233856,1130233856,1103101952,1125122048,1125122048,0,1795392,1073741824,193536,32768,32768,53248,9575424,1073741824,1032192,1125073749,1126187871,4294939180,142,1125073749,1126187871,4294939180,142,16816,1099973080,4294966028,4294967294,
36672,36672,36864,36864,37056,37056,37248,37248,37248,37248,37248,37248,40960,44032,48128,51200,55296,58368,62464,65536,62464,65536,62464,65536,268439552,336991254,410,2959220736,80,7984,43008,1056915071,50465804,16780300,36,80,224,1128267776,1128267776,1128267776,1098907648,1098907648,0,135936,1073741824,6912,44032,44032,40960,724992,1073741824,36864,17215,1107575618,4294925804,4294967294,17215,1107575618,4294925804,4294967294,17215,1107575618,4294925804,4294967294,
7784448,7784448,7827456,7827456,7870464,7870464,7913472,7913472,7913472,7913472,7913472,7913472,40960,44032,48128,51200,55296,58368,62464,65536,62464,65536,62464,65536,268439552,336991254,57,3232370688,80,7488,192,1056915199,16780300,50465804,576,36,224,1127612416,1127612416,1127612416,1098907648,1098907648,0,1468224,1073741824,1467648,44032,44032,40960,7830528,1073741824,7827456,17205,1107575608,4294928044,4294967294,17205,1107575608,4294928044,4294967294,17205,1107575608,4294928044,4294967294,
1591296,1591296,1634304,1634304,1677312,1677312,1720320,1720320,1720320,1720320,1720320,1720320,40960,44032,48128,51200,55296,58368,62464,65536,62464,65536,62464,65536,268439552,336991254,57,809635840,80,7488,192,1040137855,16780300,50465804,576,36,224,1108869120,1108869120,1108869120,1098907648,1098907648,0,307008,1073741824,306432,44032,44032,40960,1637376,1073741824,1634304,16916,1107575328,4294960300,4294967294,16916,1107575328,4294960300,4294967294,16916,1107575328,4294960300,4294967294,
3913728,3913728,3956736,3956736,3999744,3999744,4042752,4042752,4042752,4042752,4042752,4042752,40960,44032,48128,51200,55296,58368,62464,65536,62464,65536,62464,65536,268439552,336991254,57,1082265600,80,7488,192,1040137983,16780300,50465804,576,36,224,1119354880,1119354880,1119354880,1098907648,1098907648,0,742464,1073741824,741888,44032,44032,40960,3959808,1073741824,3956736,17078,1107575484,4294948204,4294967294,17078,1107575484,4294948204,4294967294,17078,1107575484,4294948204,4294967294,
5806080,5806080,5849088,5849088,5892096,5892096,5935104,5935104,5935104,5935104,5935104,5935104,40960,44032,48128,51200,55296,58368,62464,65536,62464,65536,62464,65536,268439552,336991254,57,1354895360,80,7488,192,1048526591,16780300,50465804,576,36,224,1124597760,1124597760,1124597760,1098907648,1098907648,0,1097280,1073741824,1096704,44032,44032,40960,5852160,1073741824,5849088,17159,1107575562,4294938348,4294967294,17159,1107575562,4294938348,4294967294,17159,1107575562,4294938348,4294967294,
9792,9792,9984,9984,10176,10176,10368,10368,10368,10368,10368,10368,40960,44032,48128,51200,55296,58368,62464,65536,62464,65536,62464,65536,268439552,336991254,410,1883906048,80,7984,43008,1048526463,50465804,16780300,36,80,224,1112539136,1112539136,1112539136,1098907648,1098907648,0,130896,1073741824,1872,44032,44032,40960,698112,1073741824,9984,16972,1107575384,4294957164,4294967294,16972,1107575384,4294957164,4294967294,16972,1107575384,4294957164,4294967294,
19776,19776,19968,19968,20160,20160,20352,20352,20352,20352,20352,20352,40960,44032,48128,51200,55296,58368,62464,65536,62464,65536,62464,65536,268439552,336991254,410,2157060096,80,7984,43008,1048526591,50465804,16780300,36,80,224,1120927744,1120927744,1120927744,1098907648,1098907648,0,132768,1073741824,3744,44032,44032,40960,708096,1073741824,19968,17102,1107575508,4294945516,4294967294,17102,1107575508,4294945516,4294967294,17102,1107575508,4294945516,4294967294,
27072,27072,27264,27264,27456,27456,27648,27648,27648,27648,27648,27648,40960,44032,48128,51200,55296,58368,62464,65536,62464,65536,62464,65536,268439552,336991254,410,2686590976,80,7984,43008,1056914943,50465804,16780300,36,80,224,1124990976,1124990976,1124990976,1098907648,1098907648,0,134136,1073741824,5112,44032,44032,40960,715392,1073741824,27264,17165,1107575568,4294937004,4294967294,17165,1107575568,4294937004,4294967294,17165,1107575568,4294937004,4294967294,
7784448,7827456,7870464,7913472,7956480,7999488,8042496,8085504,8085504,8085504,8085504,8085504,40960,45056,49152,53248,57344,61440,65536,69632,69632,69632,69632,69632,538976288,353703189,57,2959737088,80,7488,192,1056915071,16780300,50465804,576,36,128,1127874560,1127743488,1127612416,1107296256,1107296256,1107296256,1501056,1484928,1468800,57344,49152,40960,8005632,7919616,7833600,1106264885,1112032060,4294945228,30,1106264885,1112032060,4294945228,30,1106264885,1112032060,4294945228,30,
7870464,7870464,7913472,7913472,7956480,7956480,7999488,7999488,7999488,7999488,7999488,7999488,40960,44032,48128,51200,55296,58368,62464,65536,62464,65536,62464,65536,268439552,336991254,57,3232894976,80,7488,192,1056915199,16780300,50465804,576,36,224,1127743488,1127743488,1127743488,1098907648,1098907648,0,1484352,1073741824,1483776,44032,44032,40960,7916544,1073741824,7913472,17207,1107575610,4294927596,4294967294,17207,1107575610,4294927596,4294967294,17207,1107575610,4294927596,4294967294,
1677312,1677312,1720320,1720320,1763328,1763328,1806336,1806336,1806336,1806336,1806336,1806336,40960,44032,48128,51200,55296,58368,62464,65536,62464,65536,62464,65536,268439552,336991254,57,810160128,80,7488,192,1040137855,16780300,50465804,576,36,224,1109393408,1109393408,1109393408,1098907648,1098907648,0,323136,1073741824,322560,44032,44032,40960,1723392,1073741824,1720320,16924,1107575336,4294959852,4294967294,16924,1107575336,4294959852,4294967294,16924,1107575336,4294959852,4294967294,
3999744,3999744,4042752,4042752,4085760,4085760,4128768,4128768,4128768,4128768,4128768,4128768,40960,44032,48128,51200,55296,58368,62464,65536,62464,65536,62464,65536,268439552,336991254,57,1082789888,80,7488,192,1040137983,16780300,50465804,576,36,224,1119617024,1119617024,1119617024,1098907648,1098907648,0,758592,1073741824,758016,44032,44032,40960,4045824,1073741824,4042752,17082,1107575488,4294947756,4294967294,17082,1107575488,4294947756,4294967294,17082,1107575488,4294947756,4294967294,
5892096,5892096,5935104,5935104,5978112,5978112,6021120,6021120,6021120,6021120,6021120,6021120,40960,44032,48128,51200,55296,58368,62464,65536,62464,65536,62464,65536,268439552,336991254,57,1355419648,80,7488,192,1048526591,16780300,50465804,576,36,224,1124728832,1124728832,1124728832,1098907648,1098907648,0,1113408,1073741824,1112832,44032,44032,40960,5938176,1073741824,5935104,17161,1107575564,4294937900,4294967294,17161,1107575564,4294937900,4294967294,17161,1107575564,4294937900,4294967294,
10176,10176,10368,10368,10560,10560,10752,10752,10752,10752,10752,10752,40960,44032,48128,51200,55296,58368,62464,65536,62464,65536,62464,65536,268439552,336991254,410,1884430336,80,7984,43008,1048526463,50465804,16780300,36,80,224,1113063424,1113063424,1113063424,1098907648,1098907648,0,130968,1073741824,1944,44032,44032,40960,698496,1073741824,10368,16980,1107575392,4294956716,4294967294,16980,1107575392,4294956716,4294967294,16980,1107575392,4294956716,4294967294,
5978112,6021120,6064128,6107136,6150144,6193152,6236160,6279168,6279168,6279168,6279168,6279168,40960,45056,49152,53248,57344,61440,65536,69632,69632,69632,69632,69632,538976288,353703189,57,2157576448,80,7488,192,1048526591,16780300,50465804,576,36,128,1125122048,1124990976,1124859904,1107296256,1107296256,1107296256,1162368,1146240,1130112,57344,49152,40960,6199296,6113280,6027264,1106264843,1112032018,4294950604,30,1106264843,1112032018,4294950604,30,1106264843,1112032018,4294950604,30,
27456,27456,27648,27648,27840,27840,28032,28032,28032,28032,28032,28032,40960,44032,48128,51200,55296,58368,62464,65536,62464,65536,62464,65536,268439552,336991254,410,2687115264,80,7984,43008,1056914943,50465804,16780300,36,80,224,1125122048,1125122048,1125122048,1098907648,1098907648,0,134208,1073741824,5184,44032,44032,40960,715776,1073741824,27648,17167,1107575570,4294936556,4294967294,17167,1107575570,4294936556,4294967294,17167,1107575570,4294936556,4294967294,
37056,37056,37248,37248,37440,37440,37632,37632,37632,37632,37632,37632,40960,44032,48128,51200,55296,58368,62464,65536,62464,65536,62464,65536,268439552,336991254,410,2960269312,80,7984,43008,1056915071,50465804,16780300,36,80,224,1128398848,1128398848,1128398848,1098907648,1098907648,0,136008,1073741824,6984,44032,44032,40960,725376,1073741824,37248,17217,1107575620,4294925356,4294967294,17217,1107575620,4294925356,4294967294,17217,1107575620,4294925356,4294967294,
7956480,7956480,7999488,7999488,8042496,8042496,8085504,8085504,8085504,8085504,8085504,8085504,40960,44032,48128,51200,55296,58368,62464,65536,62464,65536,62464,65536,268439552,336991254,57,3233419264,80,7488,192,1056915199,16780300,50465804,576,36,224,1127874560,1127874560,1127874560,1098907648,1098907648,0,1500480,1073741824,1499904,44032,44032,40960,8002560,1073741824,7999488,17209,1107575612,4294927148,4294967294,17209,1107575612,4294927148,4294967294,17209,1107575612,4294927148,4294967294,
1763328,1763328,1806336,1806336,1849344,1849344,1892352,1892352,1892352,1892352,1892352,1892352,40960,44032,48128,51200,55296,58368,62464,65536,62464,65536,62464,65536,268439552,336991254,57,810684416,80,7488,192,1040137855,16780300,50465804,576,36,224,1109917696,1109917696,1109917696,1098907648,1098907648,0,339264,1073741824,338688,44032,44032,40960,1809408,1073741824,1806336,16932,1107575344,4294959404,4294967294,16932,1107575344,4294959404,4294967294,16932,1107575344,4294959404,4294967294,
2688,2688,2880,2880,3072,3072,3264,3264,3456,3456,3648,3648,40960,44032,48128,51200,55296,58368,62464,65536,69632,72704,76800,79872,268439552,336991254,410,1083321856,64,7984,43008,1040137983,50465804,16780300,36,80,224,1098907648,1098907648,1098907648,1098907648,1098907648,0,129600,1073741824,576,51200,51200,48128,691200,1073741824,3072,16736,1107575192,4294965452,4294967294,16736,1107575192,4294965452,4294967294,16736,1107575192,4294965452,4294967294,
5978112,5978112,6021120,6021120,6064128,6064128,6107136,6107136,6107136,6107136,6107136,6107136,40960,44032,48128,51200,55296,58368,62464,65536,62464,65536,62464,65536,268439552,336991254,57,1355943936,80,7488,192,1048526591,16780300,50465804,576,36,224,1124859904,1124859904,1124859904,1098907648,1098907648,0,1129536,1073741824,1128960,44032,44032,40960,6024192,1073741824,6021120,17163,1107575566,4294937452,4294967294,17163,1107575566,4294937452,4294967294,17163,1107575566,4294937452,4294967294,
10560,10560,10752,10752,10944,10944,11136,11136,11136,11136,11136,11136,40960,44032,48128,51200,55296,58368,62464,65536,62464,65536,62464,65536,268439552,336991254,410,1884954624,80,7984,43008,1048526463,50465804,16780300,36,80,224,1113587712,1113587712,1113587712,1098907648,1098907648,0,131040,1073741824,2016,44032,44032,40960,698880,1073741824,10752,16988,1107575400,4294956268,4294967294,16988,1107575400,4294956268,4294967294,16988,1107575400,4294956268,4294967294,
20160,20160,20352,20352,20544,20544,20736,20736,20736,20736,20736,20736,40960,44032,48128,51200,55296,58368,62464,65536,62464,65536,62464,65536,268439552,336991254,410,2158108672,80,7984,43008,1048526591,50465804,16780300,36,80,224,1121189888,1121189888,1121189888,1098907648,1098907648,0,132840,1073741824,3816,44032,44032,40960,708480,1073741824,20352,17106,1107575512,4294945068,4294967294,17106,1107575512,4294945068,4294967294,17106,1107575512,4294945068,4294967294,
27840,27840,28032,28032,28224,28224,28416,28416,28416,28416,28416,28416,40960,44032,48128,51200,55296,58368,62464,65536,62464,65536,62464,65536,268439552,336991254,410,2687639552,80,7984,43008,1056914943,50465804,16780300,36,80,224,1125253120,1125253120,1125253120,1098907648,1098907648,0,134280,1073741824,5256,44032,44032,40960,716160,1073741824,28032,17169,1107575572,4294936108,4294967294,17169,1107575572,4294936108,4294967294,17169,1107575572,4294936108,4294967294,
37440,37440,37632,37632,37824,37824,38016,38016,38016,38016,38016,38016,40960,44032,48128,51200,55296,58368,62464,65536,62464,65536,62464,65536,268439552,336991254,410,2960793600,80,7984,43008,1056915071,50465804,16780300,36,80,224,1128529920,1128529920,1128529920,1098907648,1098907648,0,136080,1073741824,7056,44032,44032,40960,725760,1073741824,37632,17219,1107575622,4294924908,4294967294,17219,1107575622,4294924908,4294967294,17219,1107575622,4294924908,4294967294,
258048,301056,344064,387072,430080,473088,8730624,8773632,8773632,8773632,8773632,8773632,49152,53248,57344,61440,65536,69632,40960,45056,45056,45056,45056,45056,3233808384,320017685,57,3233952256,80,7488,192,1056915199,16780300,50465804,576,36,128,1128923136,1128923136,1090519040,1128267776,1128267776,0,1635840,1073741824,64512,32768,32768,53248,8724480,1073741824,344064,1128219457,1129333580,4294941452,190,1128219457,1129333580,4294941452,190,16576,1099972912,780,4294967294,
1591296,1634304,1677312,1720320,1763328,1806336,1849344,1892352,1892352,1892352,1892352,1892352,40960,45056,49152,53248,57344,61440,65536,69632,69632,69632,69632,69632,538976288,353703189,57,811204864,80,7488,192,1040137855,16780300,50465804,576,36,128,1109917696,1109393408,1108869120,1107296256,1107296256,1107296256,339840,323712,307584,57344,49152,40960,1812480,1726464,1640448,1106264596,1112031792,4294963660,30,1106264596,1112031792,4294963660,30,1106264596,1112031792,4294963660,30,
2881536,2924544,2967552,3010560,3053568,3096576,3139584,3182592,3182592,3182592,3182592,3182592,40960,45056,49152,53248,57344,61440,65536,69632,69632,69632,69632,69632,538976288,353703189,57,1083834624,80,7488,192,1040137983,16780300,50465804,576,36,128,1116733440,1116471296,1116209152,1107296256,1107296256,1107296256,581760,565632,549504,57344,49152,40960,3102720,3016704,2930688,1106264710,1112031892,4294959820,30,1106264710,1112031892,4294959820,30,1106264710,1112031892,4294959820,30,
6064128,6064128,6107136,6107136,6150144,6150144,6193152,6193152,6193152,6193152,6193152,6193152,40960,44032,48128,51200,55296,58368,62464,65536,62464,65536,62464,65536,268439552,336991254,57,1356468224,80,7488,192,1048526591,16780300,50465804,576,36,224,1124990976,1124990976,1124990976,1098907648,1098907648,0,1145664,1073741824,1145088,44032,44032,40960,6110208,1073741824,6107136,17165,1107575568,4294937004,4294967294,17165,1107575568,4294937004,4294967294,17165,1107575568,4294937004,4294967294,
10944,10944,11136,11136,11328,11328,11520,11520,11520,11520,11520,11520,40960,44032,48128,51200,55296,58368,62464,65536,62464,65536,62464,65536,268439552,336991254,410,1885478912,80,7984,43008,1048526463,50465804,16780300,36,80,224,1114112000,1114112000,1114112000,1098907648,1098907648,0,131112,1073741824,2088,44032,44032,40960,699264,1073741824,11136,16996,1107575408,4294955820,4294967294,16996,1107575408,4294955820,4294967294,16996,1107575408,4294955820,4294967294,
20544,20544,20736,20736,20928,20928,21120,21120,21120,21120,21120,21120,40960,44032,48128,51200,55296,58368,62464,65536,62464,65536,62464,65536,268439552,336991254,410,2158632960,80,7984,43008,1048526591,50465804,16780300,36,80,224,1121452032,1121452032,1121452032,1098907648,1098907648,0,132912,1073741824,3888,44032,44032,40960,708864,1073741824,20736,17110,1107575516,4294944620,4294967294,17110,1107575516,4294944620,4294967294,17110,1107575516,4294944620,4294967294,
28224,28224,28416,28416,28608,28608,28800,28800,28800,28800,28800,28800,40960,44032,48128,51200,55296,58368,62464,65536,62464,65536,62464,65536,268439552,336991254,410,2688163840,80,7984,43008,1056914943,50465804,16780300,36,80,224,1125384192,1125384192,1125384192,1098907648,1098907648,0,134352,1073741824,5328,44032,44032,40960,716544,1073741824,28416,17171,1107575574,4294935660,4294967294,17171,1107575574,4294935660,4294967294,17171,1107575574,4294935660,4294967294,
37824,37824,38016,38016,38208,38208,38400,38400,38400,38400,38400,38400,40960,44032,48128,51200,55296,58368,62464,65536,62464,65536,62464,65536,268439552,336991254,410,2961317888,80,7984,43008,1056915199,50465804,16780300,36,80,224,1128660992,1128660992,1128660992,1098907648,1098907648,0,136152,1073741824,7128,44032,44032,40960,726144,1073741824,38016,17221,1107575624,4294924460,4294967294,17221,1107575624,4294924460,4294967294,17221,1107575624,4294924460,4294967294,
1118208,1161216,1204224,1247232,1290240,1333248,9160704,9203712,9203712,9203712,9203712,9203712,49152,53248,57344,61440,65536,69632,40960,45056,45056,45056,45056,45056,3233808384,320017685,57,3234607104,80,7488,192,1056915199,16780300,50465804,576,36,128,1129578496,1129578496,1105199104,1128267776,1128267776,0,1716480,1073741824,225792,32768,32768,53248,9154560,1073741824,1204224,1128219467,1129333590,4294940172,190,1128219467,1129333590,4294940172,190,16848,1099973112,4294965516,4294967294,
1849344,1849344,1892352,1892352,1935360,1935360,1978368,1978368,1978368,1978368,1978368,1978368,40960,44032,48128,51200,55296,58368,62464,65536,62464,65536,62464,65536,268439552,336991254,57,811732992,80,7488,192,1040137855,16780300,50465804,576,36,224,1110441984,1110441984,1110441984,1098907648,1098907648,0,355392,1073741824,354816,44032,44032,40960,1895424,1073741824,1892352,16940,1107575352,4294958956,4294967294,16940,1107575352,4294958956,4294967294,16940,1107575352,4294958956,4294967294,
3072,3072,3264,3264,3456,3456,3648,3648,3840,3840,4032,4032,40960,44032,48128,51200,55296,58368,62464,65536,69632,72704,76800,79872,268439552,336991254,410,1084369920,64,7984,43008,1040137983,50465804,16780300,36,80,224,1099956224,1099956224,1099956224,1098907648,1098907648,0,129672,1073741824,648,51200,51200,48128,691584,1073741824,3456,16768,1107575208,4294965004,4294967294,16768,1107575208,4294965004,4294967294,16768,1107575208,4294965004,4294967294,
6150144,6150144,6193152,6193152,6236160,6236160,6279168,6279168,6279168,6279168,6279168,6279168,40960,44032,48128,51200,55296,58368,62464,65536,62464,65536,62464,65536,268439552,336991254,57,1612845056,80,7488,192,1048526335,16780300,50465804,576,36,224,1125122048,1125122048,1125122048,1098907648,1098907648,0,1161792,1073741824,1161216,44032,44032,40960,6196224,1073741824,6193152,17167,1107575570,4294936556,4294967294,17167,1107575570,4294936556,4294967294,17167,1107575570,4294936556,4294967294,
11328,11328,11520,11520,11712,11712,11904,11904,11904,11904,11904,11904,40960,44032,48128,51200,55296,58368,62464,65536,62464,65536,62464,65536,268439552,336991254,410,1886003200,80,7984,43008,1048526463,50465804,16780300,36,80,224,1114636288,1114636288,1114636288,1098907648,1098907648,0,131184,1073741824,2160,44032,44032,40960,699648,1073741824,11520,17004,1107575416,4294955372,4294967294,17004,1107575416,4294955372,4294967294,17004,1107575416,4294955372,4294967294,
20928,20928,21120,21120,21312,21312,21504,21504,21504,21504,21504,21504,40960,44032,48128,51200,55296,58368,62464,65536,62464,65536,62464,65536,268439552,336991254,410,2159157248,80,7984,43008,1048526591,50465804,16780300,36,80,224,1121714176,1121714176,1121714176,1098907648,1098907648,0,132984,1073741824,3960,44032,44032,40960,709248,1073741824,21120,17114,1107575520,4294944172,4294967294,17114,1107575520,4294944172,4294967294,17114,1107575520,4294944172,4294967294,
28608,28608,28800,28800,28992,28992,29184,29184,29184,29184,29184,29184,40960,44032,48128,51200,55296,58368,62464,65536,62464,65536,62464,65536,268439552,336991254,410,2688688128,80,7984,43008,1056915071,50465804,16780300,36,80,224,1125515264,1125515264,1125515264,1098907648,1098907648,0,134424,1073741824,5400,44032,44032,40960,716928,1073741824,28800,17173,1107575576,4294935212,4294967294,17173,1107575576,4294935212,4294967294,17173,1107575576,4294935212,4294967294,
38208,38208,38400,38400,38592,38592,38784,38784,38784,38784,38784,38784,40960,44032,48128,51200,55296,58368,62464,65536,62464,65536,62464,65536,268439552,336991254,410,2961842176,80,7984,43008,1056915199,50465804,16780300,36,80,224,1128792064,1128792064,1128792064,1098907648,1098907648,0,136224,1073741824,7200,44032,44032,40960,726528,1073741824,38400,17223,1107575626,4294924012,4294967294,17223,1107575626,4294924012,4294967294,17223,1107575626,4294924012,4294967294,
1204224,1247232,1290240,1333248,1376256,1419264,9590784,9590784,9590784,9590784,9590784,9590784,49152,53248,57344,61440,65536,69632,40960,45056,45056,45056,45056,45056,3233808384,320017685,57,3235262464,80,7488,192,1056915199,16780300,50465804,576,36,128,1130233856,1130233856,1106247680,1128267776,1128267776,0,1797120,1073741824,241920,32768,32768,53248,9584640,1073741824,1290240,1128219477,1129333599,4294938892,190,1128219477,1129333599,4294938892,190,16864,1099973124,4294965260,4294967294,
1935360,1935360,1978368,1978368,2021376,2021376,2064384,2064384,2064384,2064384,2064384,2064384,40960,44032,48128,51200,55296,58368,62464,65536,62464,65536,62464,65536,268439552,336991254,57,812257280,80,7488,192,1040137855,16780300,50465804,576,36,224,1110966272,1110966272,1110966272,1098907648,1098907648,0,371520,1073741824,370944,44032,44032,40960,1981440,1073741824,1978368,16948,1107575360,4294958508,4294967294,16948,1107575360,4294958508,4294967294,16948,1107575360,4294958508,4294967294,
3456,3456,3648,3648,3840,3840,4032,4032,4224,4224,4416,4416,40960,44032,48128,51200,55296,58368,62464,65536,69632,72704,76800,79872,268439552,336991254,410,1084894208,64,7984,43008,1040137983,50465804,16780300,36,80,224,1101004800,1101004800,1101004800,1098907648,1098907648,0,129744,1073741824,720,51200,51200,48128,691968,1073741824,3840,16784,1107575224,4294964556,4294967294,16784,1107575224,4294964556,4294967294,16784,1107575224,4294964556,4294967294,
6236160,6236160,6279168,6279168,6322176,6322176,6365184,6365184,6365184,6365184,6365184,6365184,40960,44032,48128,51200,55296,58368,62464,65536,62464,65536,62464,65536,268439552,336991254,57,1613369344,80,7488,192,1048526335,16780300,50465804,576,36,224,1125253120,1125253120,1125253120,1098907648,1098907648,0,1177920,1073741824,1177344,44032,44032,40960,6282240,1073741824,6279168,17169,1107575572,4294936108,4294967294,17169,1107575572,4294936108,4294967294,17169,1107575572,4294936108,4294967294,
11712,11712,11904,11904,12096,12096,12288,12288,12288,12288,12288,12288,40960,44032,48128,51200,55296,58368,62464,65536,62464,65536,62464,65536,268439552,336991254,410,1886527488,80,7984,43008,1048526463,50465804,16780300,36,80,224,1115160576,1115160576,1115160576,1098907648,1098907648,0,131256,1073741824,2232,44032,44032,40960,700032,1073741824,11904,17012,1107575424,4294954924,4294967294,17012,1107575424,4294954924,4294967294,17012,1107575424,4294954924,4294967294,
21312,21312,21504,21504,21696,21696,21888,21888,21888,21888,21888,21888,40960,44032,48128,51200,55296,58368,62464,65536,62464,65536,62464,65536,268439552,336991254,410,2159681536,80,7984,43008,1048526591,50465804,16780300,36,80,224,1121976320,1121976320,1121976320,1098907648,1098907648,0,133056,1073741824,4032,44032,44032,40960,709632,1073741824,21504,17118,1107575524,4294943724,4294967294,17118,1107575524,4294943724,4294967294,17118,1107575524,4294943724,4294967294,
7010304,7053312,7096320,7139328,7182336,7225344,7268352,7311360,7311360,7311360,7311360,7311360,40960,45056,49152,53248,57344,61440,65536,69632,69632,69632,69632,69632,538976288,353703189,57,2689204480,80,7488,192,1056915071,16780300,50465804,576,36,128,1126694912,1126563840,1126432768,1107296256,1107296256,1107296256,1355904,1339776,1323648,57344,49152,40960,7231488,7145472,7059456,1106264867,1112032042,4294947532,30,1106264867,1112032042,4294947532,30,1106264867,1112032042,4294947532,30,
38592,38592,38784,38784,38976,38976,39168,39168,39168,39168,39168,39168,40960,44032,48128,51200,55296,58368,62464,65536,62464,65536,62464,65536,268439552,336991254,410,2962366464,80,7984,43008,1056915199,50465804,16780300,36,80,224,1128923136,1128923136,1128923136,1098907648,1098907648,0,136296,1073741824,7272,44032,44032,40960,726912,1073741824,38784,17225,1107575628,4294923564,4294967294,17225,1107575628,4294923564,4294967294,17225,1107575628,4294923564,4294967294,
8816640,8859648,8902656,8945664,8988672,9031680,9074688,9117696,9117696,9117696,9117696,9117696,40960,45056,49152,53248,57344,61440,65536,69632,69632,69632,69632,69632,538976288,353703189,57,3491889408,80,7488,192,1056914943,16780300,50465804,576,36,128,1129447424,1129316352,1129185280,1107296256,1107296256,1107296256,1694592,1678464,1662336,57344,49152,40960,9037824,8951808,8865792,1106264909,1112032084,4294942156,30,1106264909,1112032084,4294942156,30,1106264909,1112032084,4294942156,30,
2021376,2021376,2064384,2064384,2107392,2107392,2150400,2150400,2150400,2150400,2150400,2150400,40960,44032,48128,51200,55296,58368,62464,65536,62464,65536,62464,65536,268439552,336991254,57,812781568,80,7488,192,1040137855,16780300,50465804,576,36,224,1111490560,1111490560,1111490560,1098907648,1098907648,0,387648,1073741824,387072,44032,44032,40960,2067456,1073741824,2064384,16956,1107575368,4294958060,4294967294,16956,1107575368,4294958060,4294967294,16956,1107575368,4294958060,4294967294,
3840,3840,4032,4032,4224,4224,4416,4416,4608,4608,4800,4800,40960,44032,48128,51200,55296,58368,62464,65536,69632,72704,76800,79872,268439552,336991254,410,1085418496,64,7984,43008,1040137983,50465804,16780300,36,80,224,1102053376,1102053376,1102053376,1098907648,1098907648,0,129816,1073741824,792,51200,51200,48128,692352,1073741824,4224,16800,1107575240,4294964108,4294967294,16800,1107575240,4294964108,4294967294,16800,1107575240,4294964108,4294967294,
4171776,4214784,4257792,4300800,4343808,4386816,4429824,4472832,4472832,4472832,4472832,4472832,40960,45056,49152,53248,57344,61440,65536,69632,69632,69632,69632,69632,538976288,353703189,57,1613889792,80,7488,192,1048526335,16780300,50465804,576,36,128,1120665600,1120403456,1120141312,1107296256,1107296256,1107296256,823680,807552,791424,57344,49152,40960,4392960,4306944,4220928,1106264770,1112031952,4294955980,30,1106264770,1112031952,4294955980,30,1106264770,1112031952,4294955980,30,
12096,12096,12288,12288,12480,12480,12672,12672,12672,12672,12672,12672,40960,44032,48128,51200,55296,58368,62464,65536,62464,65536,62464,65536,268439552,336991254,410,1887051776,80,7984,43008,1048526463,50465804,16780300,36,80,224,1115684864,1115684864,1115684864,1098907648,1098907648,0,131328,1073741824,2304,44032,44032,40960,700416,1073741824,12288,17020,1107575428,4294954476,4294967294,17020,1107575428,4294954476,4294967294,17020,1107575428,4294954476,4294967294,
21696,21696,21888,21888,22080,22080,22272,22272,22272,22272,22272,22272,40960,44032,48128,51200,55296,58368,62464,65536,62464,65536,62464,65536,268439552,336991254,410,2160205824,80,7984,43008,1048526591,50465804,16780300,36,80,224,1122238464,1122238464,1122238464,1098907648,1098907648,0,133128,1073741824,4104,44032,44032,40960,710016,1073741824,21888,17122,1107575528,4294943276,4294967294,17122,1107575528,4294943276,4294967294,17122,1107575528,4294943276,4294967294,
28992,28992,29184,29184,29376,29376,29568,29568,29568,29568,29568,29568,40960,44032,48128,51200,55296,58368,62464,65536,62464,65536,62464,65536,268439552,336991254,410,2689736704,80,7984,43008,1056915071,50465804,16780300,36,80,224,1125646336,1125646336,1125646336,1098907648,1098907648,0,134496,1073741824,5472,44032,44032,40960,717312,1073741824,29184,17175,1107575578,4294934764,4294967294,17175,1107575578,4294934764,4294967294,17175,1107575578,4294934764,4294967294,
38976,38976,39168,39168,39360,39360,39552,39552,39552,39552,39552,39552,40960,44032,48128,51200,55296,58368,62464,65536,62464,65536,62464,65536,268439552,336991254,410,2962890752,80,7984,43008,1056915199,50465804,16780300,36,80,224,1129054208,1129054208,1129054208,1098907648,1098907648,0,136368,1073741824,7344,44032,44032,40960,727296,1073741824,39168,17227,1107575630,4294923116,4294967294,17227,1107575630,4294923116,4294967294,17227,1107575630,4294923116,4294967294,
8042496,8042496,8085504,8085504,8128512,8128512,8171520,8171520,8171520,8171520,8171520,8171520,40960,44032,48128,51200,55296,58368,62464,65536,62464,65536,62464,65536,268439552,336991254,57,3492417536,80,7488,192,1056914943,16780300,50465804,576,36,224,1128005632,1128005632,1128005632,1098907648,1098907648,0,1516608,1073741824,1516032,44032,44032,40960,8088576,1073741824,8085504,17211,1107575614,4294926700,4294967294,17211,1107575614,4294926700,4294967294,17211,1107575614,4294926700,4294967294,
2107392,2107392,2150400,2150400,2193408,2193408,2236416,2236416,2236416,2236416,2236416,2236416,40960,44032,48128,51200,55296,58368,62464,65536,62464,65536,62464,65536,268439552,336991254,57,813305856,80,7488,192,1040137855,16780300,50465804,576,36,224,1112014848,1112014848,1112014848,1098907648,1098907648,0,403776,1073741824,403200,44032,44032,40960,2153472,1073741824,2150400,16964,1107575376,4294957612,4294967294,16964,1107575376,4294957612,4294967294,16964,1107575376,4294957612,4294967294,
4224,4224,4416,4416,4608,4608,4800,4800,4992,4992,5184,5184,40960,44032,48128,51200,55296,58368,62464,65536,69632,72704,76800,79872,268439552,336991254,410,1085942784,64,7984,43008,1040137983,50465804,16780300,36,80,224,1103101952,1103101952,1103101952,1098907648,1098907648,0,129888,1073741824,864,51200,51200,48128,692736,1073741824,4608,16816,1107575256,4294963660,4294967294,16816,1107575256,4294963660,4294967294,16816,1107575256,4294963660,4294967294,
6322176,6322176,6365184,6365184,6408192,6408192,6451200,6451200,6451200,6451200,6451200,6451200,40960,44032,48128,51200,55296,58368,62464,65536,62464,65536,62464,65536,268439552,336991254,57,1614417920,80,7488,192,1048526335,16780300,50465804,576,36,224,1125384192,1125384192,1125384192,1098907648,1098907648,0,1194048,1073741824,1193472,44032,44032,40960,6368256,1073741824,6365184,17171,1107575574,4294935660,4294967294,17171,1107575574,4294935660,4294967294,17171,1107575574,4294935660,4294967294,
5203968,5246976,5289984,5332992,5376000,5419008,5462016,5505024,5505024,5505024,5505024,5505024,40960,45056,49152,53248,57344,61440,65536,69632,69632,69632,69632,69632,538976288,353703189,57,1887568128,80,7488,192,1048526591,16780300,50465804,576,36,128,1123811328,1123549184,1123287040,1107296256,1107296256,1107296256,1017216,1001088,984960,57344,49152,40960,5425152,5339136,5253120,1106264818,1112032000,4294952908,30,1106264818,1112032000,4294952908,30,1106264818,1112032000,4294952908,30,
22080,22080,22272,22272,22464,22464,22656,22656,22656,22656,22656,22656,40960,44032,48128,51200,55296,58368,62464,65536,62464,65536,62464,65536,268439552,336991254,410,2160730112,80,7984,43008,1048526591,50465804,16780300,36,80,224,1122500608,1122500608,1122500608,1098907648,1098907648,0,133200,1073741824,4176,44032,44032,40960,710400,1073741824,22272,17126,1107575532,4294942828,4294967294,17126,1107575532,4294942828,4294967294,17126,1107575532,4294942828,4294967294,
29376,29376,29568,29568,29760,29760,29952,29952,29952,29952,29952,29952,40960,44032,48128,51200,55296,58368,62464,65536,62464,65536,62464,65536,268439552,336991254,410,2690260992,80,7984,43008,1056915071,50465804,16780300,36,80,224,1125777408,1125777408,1125777408,1098907648,1098907648,0,134568,1073741824,5544,44032,44032,40960,717696,1073741824,29568,17177,1107575580,4294934316,4294967294,17177,1107575580,4294934316,4294967294,17177,1107575580,4294934316,4294967294,
39360,39360,39552,39552,39744,39744,39936,39936,39936,39936,39936,39936,40960,44032,48128,51200,55296,58368,62464,65536,62464,65536,62464,65536,268439552,336991254,410,2963415040,80,7984,43008,1056915199,50465804,16780300,36,80,224,1129185280,1129185280,1129185280,1098907648,1098907648,0,136440,1073741824,7416,44032,44032,40960,727680,1073741824,39552,17229,1107575632,4294922668,4294967294,17229,1107575632,4294922668,4294967294,17229,1107575632,4294922668,4294967294,
8128512,8128512,8171520,8171520,8214528,8214528,8257536,8257536,8257536,8257536,8257536,8257536,40960,44032,48128,51200,55296,58368,62464,65536,62464,65536,62464,65536,268439552,336991254,57,3492941824,80,7488,192,1056914943,16780300,50465804,576,36,224,1128136704,1128136704,1128136704,1098907648,1098907648,0,1532736,1073741824,1532160,44032,44032,40960,8174592,1073741824,8171520,17213,1107575616,4294926252,4294967294,17213,1107575616,4294926252,4294967294,17213,1107575616,4294926252,4294967294,
2193408,2193408,2236416,2236416,2279424,2279424,2322432,2322432,2322432,2322432,2322432,2322432,40960,44032,48128,51200,55296,58368,62464,65536,62464,65536,62464,65536,268439552,336991254,57,813830144,80,7488,192,1040137983,16780300,50465804,576,36,224,1112539136,1112539136,1112539136,1098907648,1098907648,0,419904,1073741824,419328,44032,44032,40960,2239488,1073741824,2236416,16972,1107575384,4294957164,4294967294,16972,1107575384,4294957164,4294967294,16972,1107575384,4294957164,4294967294,
4085760,4085760,4128768,4128768,4171776,4171776,4214784,4214784,4214784,4214784,4214784,4214784,40960,44032,48128,51200,55296,58368,62464,65536,62464,65536,62464,65536,268439552,336991254,57,1086459904,80,7488,192,1040137983,16780300,50465804,576,36,224,1119879168,1119879168,1119879168,1098907648,1098907648,0,774720,1073741824,774144,44032,44032,40960,4131840,1073741824,4128768,17086,1107575492,4294947308,4294967294,17086,1107575492,4294947308,4294967294,17086,1107575492,4294947308,4294967294,
6408192,6408192,6451200,6451200,6494208,6494208,6537216,6537216,6537216,6537216,6537216,6537216,40960,44032,48128,51200,55296,58368,62464,65536,62464,65536,62464,65536,268439552,336991254,57,1614942208,80,7488,192,1048526463,16780300,50465804,576,36,224,1125515264,1125515264,1125515264,1098907648,1098907648,0,1210176,1073741824,1209600,44032,44032,40960,6454272,1073741824,6451200,17173,1107575576,4294935212,4294967294,17173,1107575576,4294935212,4294967294,17173,1107575576,4294935212,4294967294,
12480,12480,12672,12672,12864,12864,13056,13056,13056,13056,13056,13056,40960,44032,48128,51200,55296,58368,62464,65536,62464,65536,62464,65536,268439552,336991254,410,1888100352,80,7984,43008,1048526591,50465804,16780300,36,80,224,1115947008,1115947008,1115947008,1098907648,1098907648,0,131400,1073741824,2376,44032,44032,40960,700800,1073741824,12672,17026,1107575432,4294954028,4294967294,17026,1107575432,4294954028,4294967294,17026,1107575432,4294954028,4294967294,
9418752,9461760,9504768,9547776,9590784,9590784,9590784,9590784,9590784,9590784,9590784,9590784,40960,45056,49152,53248,57344,61440,61440,61440,61440,61440,61440,61440,2155905152,320017171,57,2161246464,80,7488,192,1048526591,16780300,50465804,576,36,128,1129971712,1130102784,1130233856,1124073472,1124073472,1124073472,1762560,1778688,1794816,32768,40960,49152,9400320,9486336,9572352,1123959633,1125139295,4294939788,126,1123959633,1125139295,4294939788,126,1123959633,1125139295,4294939788,126,
29760,29760,29952,29952,30144,30144,30336,30336,30336,30336,30336,30336,40960,44032,48128,51200,55296,58368,62464,65536,62464,65536,62464,65536,268439552,336991254,410,2690785280,80,7984,43008,1056915071,50465804,16780300,36,80,224,1125908480,1125908480,1125908480,1098907648,1098907648,0,134640,1073741824,5616,44032,44032,40960,718080,1073741824,29952,17179,1107575582,4294933868,4294967294,17179,1107575582,4294933868,4294967294,17179,1107575582,4294933868,4294967294,
8042496,8085504,8128512,8171520,8214528,8257536,8300544,8343552,8343552,8343552,8343552,8343552,40960,45056,49152,53248,57344,61440,65536,69632,69632,69632,69632,69632,538976288,353703189,57,2963931392,80,7488,192,1056915199,16780300,50465804,576,36,128,1128267776,1128136704,1128005632,1107296256,1107296256,1107296256,1549440,1533312,1517184,57344,49152,40960,8263680,8177664,8091648,1106264891,1112032066,4294944460,30,1106264891,1112032066,4294944460,30,1106264891,1112032066,4294944460,30,
8214528,8214528,8257536,8257536,8300544,8300544,8343552,8343552,8343552,8343552,8343552,8343552,40960,44032,48128,51200,55296,58368,62464,65536,62464,65536,62464,65536,268439552,336991254,57,3493466112,80,7488,192,1056914943,16780300,50465804,576,36,224,1128267776,1128267776,1128267776,1098907648,1098907648,0,1548864,1073741824,1548288,44032,44032,40960,8260608,1073741824,8257536,17215,1107575618,4294925804,4294967294,17215,1107575618,4294925804,4294967294,17215,1107575618,4294925804,4294967294,
1849344,1892352,1935360,1978368,2021376,2064384,2107392,2150400,2150400,2150400,2150400,2150400,40960,45056,49152,53248,57344,61440,65536,69632,69632,69632,69632,69632,538976288,353703189,57,814350592,80,7488,192,1040137983,16780300,50465804,576,36,128,1111490560,1110966272,1110441984,1107296256,1107296256,1107296256,388224,372096,355968,57344,49152,40960,2070528,1984512,1898496,1106264620,1112031816,4294962892,30,1106264620,1112031816,4294962892,30,1106264620,1112031816,4294962892,30,
4171776,4171776,4214784,4214784,4257792,4257792,4300800,4300800,4300800,4300800,4300800,4300800,40960,44032,48128,51200,55296,58368,62464,65536,62464,65536,62464,65536,268439552,336991254,57,1086984192,80,7488,192,1040137983,16780300,50465804,576,36,224,1120141312,1120141312,1120141312,1098907648,1098907648,0,790848,1073741824,790272,44032,44032,40960,4217856,1073741824,4214784,17090,1107575496,4294946860,4294967294,17090,1107575496,4294946860,4294967294,17090,1107575496,4294946860,4294967294,
6494208,6494208,6537216,6537216,6580224,6580224,6623232,6623232,6623232,6623232,6623232,6623232,40960,44032,48128,51200,55296,58368,62464,65536,62464,65536,62464,65536,268439552,336991254,57,1615466496,80,7488,192,1048526463,16780300,50465804,576,36,224,1125646336,1125646336,1125646336,1098907648,1098907648,0,1226304,1073741824,1225728,44032,44032,40960,6540288,1073741824,6537216,17175,1107575578,4294934764,4294967294,17175,1107575578,4294934764,4294967294,17175,1107575578,4294934764,4294967294,
12864,12864,13056,13056,13248,13248,13440,13440,13440,13440,13440,13440,40960,44032,48128,51200,55296,58368,62464,65536,62464,65536,62464,65536,268439552,336991254,410,1888624640,80,7984,43008,1048526591,50465804,16780300,36,80,224,1116209152,1116209152,1116209152,1098907648,1098907648,0,131472,1073741824,2448,44032,44032,40960,701184,1073741824,13056,17030,1107575436,4294953580,4294967294,17030,1107575436,4294953580,4294967294,17030,1107575436,4294953580,4294967294,
22464,22464,22656,22656,22848,22848,23040,23040,23040,23040,23040,23040,40960,44032,48128,51200,55296,58368,62464,65536,62464,65536,62464,65536,268439552,336991254,410,2418155520,80,7984,43008,1056914943,50465804,16780300,36,80,224,1122762752,1122762752,1122762752,1098907648,1098907648,0,133272,1073741824,4248,44032,44032,40960,710784,1073741824,22656,17130,1107575536,4294942380,4294967294,17130,1107575536,4294942380,4294967294,17130,1107575536,4294942380,4294967294,
30144,30144,30336,30336,30528,30528,30720,30720,30720,30720,30720,30720,40960,44032,48128,51200,55296,58368,62464,65536,62464,65536,62464,65536,268439552,336991254,410,2691309568,80,7984,43008,1056915071,50465804,16780300,36,80,224,1126039552,1126039552,1126039552,1098907648,1098907648,0,134712,1073741824,5688,44032,44032,40960,718464,1073741824,30336,17181,1107575584,4294933420,4294967294,17181,1107575584,4294933420,4294967294,17181,1107575584,4294933420,4294967294,
39744,39744,39936,39936,40128,40128,40320,40320,40320,40320,40320,40320,40960,44032,48128,51200,55296,58368,62464,65536,62464,65536,62464,65536,268439552,336991254,410,2964463616,80,7984,43008,1056915199,50465804,16780300,36,80,224,1129316352,1129316352,1129316352,1098907648,1098907648,0,136512,1073741824,7488,44032,44032,40960,728064,1073741824,39936,17231,1107575634,4294922220,4294967294,17231,1107575634,4294922220,4294967294,17231,1107575634,4294922220,4294967294,
8300544,8300544,8343552,8343552,8386560,8386560,8429568,8429568,8429568,8429568,8429568,8429568,40960,44032,48128,51200,55296,58368,62464,65536,62464,65536,62464,65536,268439552,336991254,57,3493990400,80,7488,192,1056915071,16780300,50465804,576,36,224,1128398848,1128398848,1128398848,1098907648,1098907648,0,1564992,1073741824,1564416,44032,44032,40960,8346624,1073741824,8343552,17217,1107575620,4294925356,4294967294,17217,1107575620,4294925356,4294967294,17217,1107575620,4294925356,4294967294,
2279424,2279424,2322432,2322432,2365440,2365440,2408448,2408448,2408448,2408448,2408448,2408448,40960,44032,48128,51200,55296,58368,62464,65536,62464,65536,62464,65536,268439552,336991254,57,814878720,80,7488,192,1040137983,16780300,50465804,576,36,224,1113063424,1113063424,1113063424,1098907648,1098907648,0,436032,1073741824,435456,44032,44032,40960,2325504,1073741824,2322432,16980,1107575392,4294956716,4294967294,16980,1107575392,4294956716,4294967294,16980,1107575392,4294956716,4294967294,
3139584,3182592,3225600,3268608,3311616,3354624,3397632,3440640,3440640,3440640,3440640,3440640,40960,45056,49152,53248,57344,61440,65536,69632,69632,69632,69632,69632,538976288,353703189,57,1087504640,80,7488,192,1040137983,16780300,50465804,576,36,128,1117519872,1117257728,1116995584,1107296256,1107296256,1107296256,630144,614016,597888,57344,49152,40960,3360768,3274752,3188736,1106264722,1112031904,4294959052,30,1106264722,1112031904,4294959052,30,1106264722,1112031904,4294959052,30,
6580224,6580224,6623232,6623232,6666240,6666240,6709248,6709248,6709248,6709248,6709248,6709248,40960,44032,48128,51200,55296,58368,62464,65536,62464,65536,62464,65536,268439552,336991254,57,1615990784,80,7488,192,1048526463,16780300,50465804,576,36,224,1125777408,1125777408,1125777408,1098907648,1098907648,0,1242432,1073741824,1241856,44032,44032,40960,6626304,1073741824,6623232,17177,1107575580,4294934316,4294967294,17177,1107575580,4294934316,4294967294,17177,1107575580,4294934316,4294967294,
13248,13248,13440,13440,13632,13632,13824,13824,13824,13824,13824,13824,40960,44032,48128,51200,55296,58368,62464,65536,62464,65536,62464,65536,268439552,336991254,410,1889148928,80,7984,43008,1048526591,50465804,16780300,36,80,224,1116471296,1116471296,1116471296,1098907648,1098907648,0,131544,1073741824,2520,44032,44032,40960,701568,1073741824,13440,17034,1107575440,4294953132,4294967294,17034,1107575440,4294953132,4294967294,17034,1107575440,4294953132,4294967294,
6236160,6279168,6322176,6365184,6408192,6451200,6494208,6537216,6537216,6537216,6537216,6537216,40960,45056,49152,53248,57344,61440,65536,69632,69632,69632,69632,69632,538976288,353703189,57,2418671872,80,7488,192,1056914943,16780300,50465804,576,36,128,1125515264,1125384192,1125253120,1107296256,1107296256,1107296256,1210752,1194624,1178496,57344,49152,40960,6457344,6371328,6285312,1106264849,1112032024,4294949836,30,1106264849,1112032024,4294949836,30,1106264849,1112032024,4294949836,30,
30528,30528,30720,30720,30912,30912,31104,31104,31104,31104,31104,31104,40960,44032,48128,51200,55296,58368,62464,65536,62464,65536,62464,65536,268439552,336991254,410,2691833856,80,7984,43008,1056915071,50465804,16780300,36,80,224,1126170624,1126170624,1126170624,1098907648,1098907648,0,134784,1073741824,5760,44032,44032,40960,718848,1073741824,30720,17183,1107575586,4294932972,4294967294,17183,1107575586,4294932972,4294967294,17183,1107575586,4294932972,4294967294,
40128,40128,40320,40320,40512,40512,40704,40704,40704,40704,40704,40704,40960,44032,48128,51200,55296,58368,62464,65536,62464,65536,62464,65536,268439552,336991254,410,2964987904,80,7984,43008,1056915199,50465804,16780300,36,80,224,1129447424,1129447424,1129447424,1098907648,1098907648,0,136584,1073741824,7560,44032,44032,40960,728448,1073741824,40320,17233,1107575636,4294921772,4294967294,17233,1107575636,4294921772,4294967294,17233,1107575636,4294921772,4294967294,
8386560,8386560,8429568,8429568,8472576,8472576,8515584,8515584,8515584,8515584,8515584,8515584,40960,44032,48128,51200,55296,58368,62464,65536,62464,65536,62464,65536,268439552,336991254,57,3494514688,80,7488,192,1056915071,16780300,50465804,576,36,224,1128529920,1128529920,1128529920,1098907648,1098907648,0,1581120,1073741824,1580544,44032,44032,40960,8432640,1073741824,8429568,17219,1107575622,4294924908,4294967294,17219,1107575622,4294924908,4294967294,17219,1107575622,4294924908,4294967294,
2365440,2365440,2408448,2408448,2451456,2451456,2494464,2494464,2494464,2494464,2494464,2494464,40960,44032,48128,51200,55296,58368,62464,65536,62464,65536,62464,65536,268439552,336991254,57,815403008,80,7488,192,1040137983,16780300,50465804,576,36,224,1113587712,1113587712,1113587712,1098907648,1098907648,0,452160,1073741824,451584,44032,44032,40960,2411520,1073741824,2408448,16988,1107575400,4294956268,4294967294,16988,1107575400,4294956268,4294967294,16988,1107575400,4294956268,4294967294,
4257792,4257792,4300800,4300800,4343808,4343808,4386816,4386816,4386816,4386816,4386816,4386816,40960,44032,48128,51200,55296,58368,62464,65536,62464,65536,62464,65536,268439552,336991254,57,1088032768,80,7488,192,1040137983,16780300,50465804,576,36,224,1120403456,1120403456,1120403456,1098907648,1098907648,0,806976,1073741824,806400,44032,44032,40960,4303872,1073741824,4300800,17094,1107575500,4294946412,4294967294,17094,1107575500,4294946412,4294967294,17094,1107575500,4294946412,4294967294,
6666240,6666240,6709248,6709248,6752256,6752256,6795264,6795264,6795264,6795264,6795264,6795264,40960,44032,48128,51200,55296,58368,62464,65536,62464,65536,62464,65536,268439552,336991254,57,1616515072,80,7488,192,1048526463,16780300,50465804,576,36,224,1125908480,1125908480,1125908480,1098907648,1098907648,0,1258560,1073741824,1257984,44032,44032,40960,6712320,1073741824,6709248,17179,1107575582,4294933868,4294967294,17179,1107575582,4294933868,4294967294,17179,1107575582,4294933868,4294967294,
13632,13632,13824,13824,14016,14016,14208,14208,14208,14208,14208,14208,40960,44032,48128,51200,55296,58368,62464,65536,62464,65536,62464,65536,268439552,336991254,410,1889673216,80,7984,43008,1048526591,50465804,16780300,36,80,224,1116733440,1116733440,1116733440,1098907648,1098907648,0,131616,1073741824,2592,44032,44032,40960,701952,1073741824,13824,17038,1107575444,4294952684,4294967294,17038,1107575444,4294952684,4294967294,17038,1107575444,4294952684,4294967294,
1290240,1290240,1333248,1333248,1376256,1376256,1419264,1419264,1462272,1462272,1505280,1505280,40960,44032,48128,51200,55296,58368,62464,65536,69632,72704,76800,79872,1076903984,336991254,57,2419203072,64,7488,192,1056914943,16780300,50465804,576,36,224,1107296256,1107296256,1107296256,1115684864,1115684864,1111490560,260352,1073741824,259776,51200,51200,48128,1388544,1073741824,1385472,1110983152,1117929996,4294961580,46,1110983152,1117929996,4294961580,46,1110983152,1117929996,4294961580,46,
30912,30912,31104,31104,31296,31296,31488,31488,31488,31488,31488,31488,40960,44032,48128,51200,55296,58368,62464,65536,62464,65536,62464,65536,268439552,336991254,410,2692358144,80,7984,43008,1056915071,50465804,16780300,36,80,224,1126301696,1126301696,1126301696,1098907648,1098907648,0,134856,1073741824,5832,44032,44032,40960,719232,1073741824,31104,17185,1107575588,4294932524,4294967294,17185,1107575588,4294932524,4294967294,17185,1107575588,4294932524,4294967294,
40512,40512,40704,40704,40896,40896,41088,41088,41088,41088,41088,41088,40960,44032,48128,51200,55296,58368,62464,65536,62464,65536,62464,65536,268439552,336991254,410,2965512192,80,7984,43008,1056915199,50465804,16780300,36,80,224,1129578496,1129578496,1129578496,1098907648,1098907648,0,136656,1073741824,7632,44032,44032,40960,728832,1073741824,40704,17235,1107575638,4294921324,4294967294,17235,1107575638,4294921324,4294967294,17235,1107575638,4294921324,4294967294,
8472576,8472576,8515584,8515584,8558592,8558592,8601600,8601600,8601600,8601600,8601600,8601600,40960,44032,48128,51200,55296,58368,62464,65536,62464,65536,62464,65536,268439552,336991254,57,3495038976,80,7488,192,1056915071,16780300,50465804,576,36,224,1128660992,1128660992,1128660992,1098907648,1098907648,0,1597248,1073741824,1596672,44032,44032,40960,8518656,1073741824,8515584,17221,1107575624,4294924460,4294967294,17221,1107575624,4294924460,4294967294,17221,1107575624,4294924460,4294967294,
2451456,2451456,2494464,2494464,2537472,2537472,2580480,2580480,2580480,2580480,2580480,2580480,40960,44032,48128,51200,55296,58368,62464,65536,62464,65536,62464,65536,268439552,336991254,57,815927296,80,7488,192,1040137983,16780300,50465804,576,36,224,1114112000,1114112000,1114112000,1098907648,1098907648,0,468288,1073741824,467712,44032,44032,40960,2497536,1073741824,2494464,16996,1107575408,4294955820,4294967294,16996,1107575408,4294955820,4294967294,16996,1107575408,4294955820,4294967294,
4343808,4343808,4386816,4386816,4429824,4429824,4472832,4472832,4472832,4472832,4472832,4472832,40960,44032,48128,51200,55296,58368,62464,65536,62464,65536,62464,65536,268439552,336991254,57,1344409600,80,7488,192,1048526335,16780300,50465804,576,36,224,1120665600,1120665600,1120665600,1098907648,1098907648,0,823104,1073741824,822528,44032,44032,40960,4389888,1073741824,4386816,17098,1107575504,4294945964,4294967294,17098,1107575504,4294945964,4294967294,17098,1107575504,4294945964,4294967294,
6752256,6752256,6795264,6795264,6838272,6838272,6881280,6881280,6881280,6881280,6881280,6881280,40960,44032,48128,51200,55296,58368,62464,65536,62464,65536,62464,65536,268439552,336991254,57,1617039360,80,7488,192,1048526463,16780300,50465804,576,36,224,1126039552,1126039552,1126039552,1098907648,1098907648,0,1274688,1073741824,1274112,44032,44032,40960,6798336,1073741824,6795264,17181,1107575584,4294933420,4294967294,17181,1107575584,4294933420,4294967294,17181,1107575584,4294933420,4294967294,
14016,14016,14208,14208,14400,14400,14592,14592,14592,14592,14592,14592,40960,44032,48128,51200,55296,58368,62464,65536,62464,65536,62464,65536,268439552,336991254,410,1890197504,80,7984,43008,1048526591,50465804,16780300,36,80,224,1116995584,1116995584,1116995584,1098907648,1098907648,0,131688,1073741824,2664,44032,44032,40960,702336,1073741824,14208,17042,1107575448,4294952236,4294967294,17042,1107575448,4294952236,4294967294,17042,1107575448,4294952236,4294967294,
1290240,1290240,1333248,1333248,1376256,1376256,1419264,1419264,1462272,1462272,1505280,1505280,40960,44032,48128,51200,55296,58368,62464,65536,69632,72704,76800,79872,1615880272,336991254,57,2419727360,64,7488,192,1056914943,16780300,50465804,576,36,224,1107296256,1107296256,1107296256,1119879168,1119879168,1117782016,261504,1073741824,260928,51200,51200,48128,1394688,1073741824,1391616,1117536752,1122124300,4294961388,78,1117536752,1122124300,4294961388,78,1117536752,1122124300,4294961388,78,
31296,31296,31488,31488,31680,31680,31872,31872,31872,31872,31872,31872,40960,44032,48128,51200,55296,58368,62464,65536,62464,65536,62464,65536,268439552,336991254,410,2692882432,80,7984,43008,1056915199,50465804,16780300,36,80,224,1126432768,1126432768,1126432768,1098907648,1098907648,0,134928,1073741824,5904,44032,44032,40960,719616,1073741824,31488,17187,1107575590,4294932076,4294967294,17187,1107575590,4294932076,4294967294,17187,1107575590,4294932076,4294967294,
40896,40896,41088,41088,41280,41280,41472,41472,41472,41472,41472,41472,40960,44032,48128,51200,55296,58368,62464,65536,62464,65536,62464,65536,268439552,336991254,410,2966036480,80,7984,43008,1056915199,50465804,16780300,36,80,224,1129709568,1129709568,1129709568,1098907648,1098907648,0,136728,1073741824,7704,44032,44032,40960,729216,1073741824,41088,17237,1107575640,4294920876,4294967294,17237,1107575640,4294920876,4294967294,17237,1107575640,4294920876,4294967294,
8558592,8558592,8601600,8601600,8644608,8644608,8687616,8687616,8687616,8687616,8687616,8687616,40960,44032,48128,51200,55296,58368,62464,65536,62464,65536,62464,65536,268439552,336991254,57,3495563264,80,7488,192,1056915071,16780300,50465804,576,36,224,1128792064,1128792064,1128792064,1098907648,1098907648,0,1613376,1073741824,1612800,44032,44032,40960,8604672,1073741824,8601600,17223,1107575626,4294924012,4294967294,17223,1107575626,4294924012,4294967294,17223,1107575626,4294924012,4294967294,
2537472,2537472,2580480,2580480,2623488,2623488,2666496,2666496,2666496,2666496,2666496,2666496,40960,44032,48128,51200,55296,58368,62464,65536,62464,65536,62464,65536,268439552,336991254,57,816451584,80,7488,192,1040137983,16780300,50465804,576,36,224,1114636288,1114636288,1114636288,1098907648,1098907648,0,484416,1073741824,483840,44032,44032,40960,2583552,1073741824,2580480,17004,1107575416,4294955372,4294967294,17004,1107575416,4294955372,4294967294,17004,1107575416,4294955372,4294967294,
4429824,4429824,4472832,4472832,4515840,4515840,4558848,4558848,4558848,4558848,4558848,4558848,40960,44032,48128,51200,55296,58368,62464,65536,62464,65536,62464,65536,268439552,336991254,57,1344933888,80,7488,192,1048526335,16780300,50465804,576,36,224,1120927744,1120927744,1120927744,1098907648,1098907648,0,839232,1073741824,838656,44032,44032,40960,4475904,1073741824,4472832,17102,1107575508,4294945516,4294967294,17102,1107575508,4294945516,4294967294,17102,1107575508,4294945516,4294967294,
4608,4608,4800,4800,4992,4992,5184,5184,5376,5376,5568,5568,40960,44032,48128,51200,55296,58368,62464,65536,69632,72704,76800,79872,268439552,336991254,410,1617570816,64,7984,43008,1048526463,50465804,16780300,36,80,224,1104150528,1104150528,1104150528,1098907648,1098907648,0,129960,1073741824,936,51200,51200,48128,693120,1073741824,4992,16832,1107575272,4294963212,4294967294,16832,1107575272,4294963212,4294967294,16832,1107575272,4294963212,4294967294,
14400,14400,14592,14592,14784,14784,14976,14976,14976,14976,14976,14976,40960,44032,48128,51200,55296,58368,62464,65536,62464,65536,62464,65536,268439552,336991254,410,1890721792,80,7984,43008,1048526591,50465804,16780300,36,80,224,1117257728,1117257728,1117257728,1098907648,1098907648,0,131760,1073741824,2736,44032,44032,40960,702720,1073741824,14592,17046,1107575452,4294951788,4294967294,17046,1107575452,4294951788,4294967294,17046,1107575452,4294951788,4294967294,
1290240,1290240,1333248,1333248,1376256,1376256,1419264,1419264,1462272,1462272,1505280,1505280,40960,44032,48128,51200,55296,58368,62464,65536,69632,72704,76800,79872,2154856560,336991254,57,2420251648,64,7488,192,1056915071,16780300,50465804,576,36,224,1107296256,1107296256,1107296256,1124073472,1124073472,1121976320,262656,1073741824,262080,51200,51200,48128,1400832,1073741824,1397760,1121731056,1125204492,4294961196,110,1121731056,1125204492,4294961196,110,1121731056,1125204492,4294961196,110,
7268352,7311360,7354368,7397376,7440384,7483392,7526400,7569408,7569408,7569408,7569408,7569408,40960,45056,49152,53248,57344,61440,65536,69632,69632,69632,69632,69632,538976288,353703189,57,2693398784,80,7488,192,1056915199,16780300,50465804,576,36,128,1127088128,1126957056,1126825984,1107296256,1107296256,1107296256,1404288,1388160,1372032,57344,49152,40960,7489536,7403520,7317504,1106264873,1112032048,4294946764,30,1106264873,1112032048,4294946764,30,1106264873,1112032048,4294946764,30,
9418752,9461760,9504768,9547776,9590784,9590784,9590784,9590784,9590784,9590784,9590784,9590784,40960,45056,49152,53248,57344,61440,61440,61440,61440,61440,61440,61440,2964369584,320017171,57,2966552832,80,7488,192,1056915199,16780300,50465804,576,36,128,1129971712,1130102784,1130233856,1127219200,1127219200,1127219200,1764288,1780416,1796544,32768,40960,49152,9409536,9495552,9581568,1127170897,1128285023,4294939500,174,1127170897,1128285023,4294939500,174,1127170897,1128285023,4294939500,174,
9074688,9117696,9160704,9203712,9246720,9289728,9332736,9375744,9375744,9375744,9375744,9375744,40960,45056,49152,53248,57344,61440,65536,69632,69632,69632,69632,69632,538976288,353703189,57,3496083712,80,7488,192,1056915071,16780300,50465804,576,36,128,1129840640,1129709568,1129578496,1107296256,1107296256,1107296256,1742976,1726848,1710720,57344,49152,40960,9295872,9209856,9123840,1106264915,1112032090,4294941388,30,1106264915,1112032090,4294941388,30,1106264915,1112032090,4294941388,30,
2623488,2623488,2666496,2666496,2709504,2709504,2752512,2752512,2752512,2752512,2752512,2752512,40960,44032,48128,51200,55296,58368,62464,65536,62464,65536,62464,65536,268439552,336991254,57,816975872,80,7488,192,1040137983,16780300,50465804,576,36,224,1115160576,1115160576,1115160576,1098907648,1098907648,0,500544,1073741824,499968,44032,44032,40960,2669568,1073741824,2666496,17012,1107575424,4294954924,4294967294,17012,1107575424,4294954924,4294967294,17012,1107575424,4294954924,4294967294,
4515840,4515840,4558848,4558848,4601856,4601856,4644864,4644864,4644864,4644864,4644864,4644864,40960,44032,48128,51200,55296,58368,62464,65536,62464,65536,62464,65536,268439552,336991254,57,1345458176,80,7488,192,1048526335,16780300,50465804,576,36,224,1121189888,1121189888,1121189888,1098907648,1098907648,0,855360,1073741824,854784,44032,44032,40960,4561920,1073741824,4558848,17106,1107575512,4294945068,4294967294,17106,1107575512,4294945068,4294967294,17106,1107575512,4294945068,4294967294,
4429824,4472832,4515840,4558848,4601856,4644864,4687872,4730880,4730880,4730880,4730880,4730880,40960,45056,49152,53248,57344,61440,65536,69632,69632,69632,69632,69632,538976288,353703189,57,1618084096,80,7488,192,1048526463,16780300,50465804,576,36,128,1121452032,1121189888,1120927744,1107296256,1107296256,1107296256,872064,855936,839808,57344,49152,40960,4651008,4564992,4478976,1106264782,1112031964,4294955212,30,1106264782,1112031964,4294955212,30,1106264782,1112031964,4294955212,30,
14784,14784,14976,14976,15168,15168,15360,15360,15360,15360,15360,15360,40960,44032,48128,51200,55296,58368,62464,65536,62464,65536,62464,65536,268439552,336991254,410,1891246080,80,7984,43008,1048526591,50465804,16780300,36,80,224,1117519872,1117519872,1117519872,1098907648,1098907648,0,131832,1073741824,2808,44032,44032,40960,703104,1073741824,14976,17050,1107575456,4294951340,4294967294,17050,1107575456,4294951340,4294967294,17050,1107575456,4294951340,4294967294,
1290240,1290240,1333248,1333248,1376256,1376256,1419264,1419264,1462272,1462272,1505280,1505280,40960,44032,48128,51200,55296,58368,62464,65536,69632,72704,76800,79872,2693832848,336991254,57,2420775936,64,7488,192,1056915071,16780300,50465804,576,36,224,1107296256,1107296256,1107296256,1126170624,1126170624,1125122048,263808,1073741824,263232,51200,51200,48128,1406976,1073741824,1403904,1125007856,1127301644,4294961004,142,1125007856,1127301644,4294961004,142,1125007856,1127301644,4294961004,142,
31680,31680,31872,31872,32064,32064,32256,32256,32256,32256,32256,32256,40960,44032,48128,51200,55296,58368,62464,65536,62464,65536,62464,65536,268439552,336991254,410,2693931008,80,7984,43008,1056915199,50465804,16780300,36,80,224,1126563840,1126563840,1126563840,1098907648,1098907648,0,135000,1073741824,5976,44032,44032,40960,720000,1073741824,31872,17189,1107575592,4294931628,4294967294,17189,1107575592,4294931628,4294967294,17189,1107575592,4294931628,4294967294,
41280,41280,41472,41472,41664,41664,41856,41856,41856,41856,41856,41856,40960,44032,48128,51200,55296,58368,62464,65536,62464,65536,62464,65536,268439552,336991254,410,3223461888,80,7984,43008,1056914943,50465804,16780300,36,80,224,1129840640,1129840640,1129840640,1098907648,1098907648,0,136800,1073741824,7776,44032,44032,40960,729600,1073741824,41472,17239,1107575642,4294920428,4294967294,17239,1107575642,4294920428,4294967294,17239,1107575642,4294920428,4294967294,
8644608,8644608,8687616,8687616,8730624,8730624,8773632,8773632,8773632,8773632,8773632,8773632,40960,44032,48128,51200,55296,58368,62464,65536,62464,65536,62464,65536,268439552,336991254,57,3496611840,80,7488,192,1056915071,16780300,50465804,576,36,224,1128923136,1128923136,1128923136,1098907648,1098907648,0,1629504,1073741824,1628928,44032,44032,40960,8690688,1073741824,8687616,17225,1107575628,4294923564,4294967294,17225,1107575628,4294923564,4294967294,17225,1107575628,4294923564,4294967294,
2107392,2150400,2193408,2236416,2279424,2322432,2365440,2408448,2408448,2408448,2408448,2408448,40960,45056,49152,53248,57344,61440,65536,69632,69632,69632,69632,69632,538976288,353703189,57,817496320,80,7488,192,1040137983,16780300,50465804,576,36,128,1113063424,1112539136,1112014848,1107296256,1107296256,1107296256,436608,420480,404352,57344,49152,40960,2328576,2242560,2156544,1106264644,1112031840,4294962124,30,1106264644,1112031840,4294962124,30,1106264644,1112031840,4294962124,30,
4601856,4601856,4644864,4644864,4687872,4687872,4730880,4730880,4730880,4730880,4730880,4730880,40960,44032,48128,51200,55296,58368,62464,65536,62464,65536,62464,65536,268439552,336991254,57,1345982464,80,7488,192,1048526335,16780300,50465804,576,36,224,1121452032,1121452032,1121452032,1098907648,1098907648,0,871488,1073741824,870912,44032,44032,40960,4647936,1073741824,4644864,17110,1107575516,4294944620,4294967294,17110,1107575516,4294944620,4294967294,17110,1107575516,4294944620,4294967294,
4992,4992,5184,5184,5376,5376,5568,5568,5760,5760,5952,5952,40960,44032,48128,51200,55296,58368,62464,65536,69632,72704,76800,79872,268439552,336991254,410,1618619392,64,7984,43008,1048526463,50465804,16780300,36,80,224,1105199104,1105199104,1105199104,1098907648,1098907648,0,130032,1073741824,1008,51200,51200,48128,693504,1073741824,5376,16848,1107575288,4294962764,4294967294,16848,1107575288,4294962764,4294967294,16848,1107575288,4294962764,4294967294,
0,43008,86016,129024,172032,215040,8730624,8773632,8773632,8773632,8773632,8773632,49152,53248,57344,61440,65536,69632,40960,45056,45056,45056,45056,45056,1886388224,320017685,57,1891774976,80,7488,192,1048526591,16780300,50465804,576,36,128,1128923136,1128923136,1073741824,1121976320,1121976320,0,1632960,1073741824,16128,32768,32768,53248,8709120,1073741824,86016,1121862465,1124090700,4294941932,110,1121862465,1124090700,4294941932,110,0,1099972768,1548,4294967294,
1290240,1290240,1333248,1333248,1376256,1376256,1419264,1419264,1462272,1462272,1505280,1505280,40960,44032,48128,51200,55296,58368,62464,65536,69632,72704,76800,79872,3232809136,336991254,57,2421300224,64,7488,192,1056915071,16780300,50465804,576,36,224,1107296256,1107296256,1107296256,1128267776,1128267776,1127219200,264960,1073741824,264384,51200,51200,48128,1413120,1073741824,1410048,1127105008,1129398796,4294960812,174,1127105008,1129398796,4294960812,174,1127105008,1129398796,4294960812,174,
32064,32064,32256,32256,32448,32448,32640,32640,32640,32640,32640,32640,40960,44032,48128,51200,55296,58368,62464,65536,62464,65536,62464,65536,268439552,336991254,410,2694455296,80,7984,43008,1056915199,50465804,16780300,36,80,224,1126694912,1126694912,1126694912,1098907648,1098907648,0,135072,1073741824,6048,44032,44032,40960,720384,1073741824,32256,17191,1107575594,4294931180,4294967294,17191,1107575594,4294931180,4294967294,17191,1107575594,4294931180,4294967294,
41664,41664,41856,41856,42048,42048,42240,42240,42240,42240,42240,42240,40960,44032,48128,51200,55296,58368,62464,65536,62464,65536,62464,65536,268439552,336991254,410,3223986176,80,7984,43008,1056914943,50465804,16780300,36,80,224,1129971712,1129971712,1129971712,1098907648,1098907648,0,136872,1073741824,7848,44032,44032,40960,729984,1073741824,41856,17241,1107575644,4294919980,4294967294,17241,1107575644,4294919980,4294967294,17241,1107575644,4294919980,4294967294,
8730624,8730624,8773632,8773632,8816640,8816640,8859648,8859648,8859648,8859648,8859648,8859648,40960,44032,48128,51200,55296,58368,62464,65536,62464,65536,62464,65536,268439552,336991254,57,3497136128,80,7488,192,1056915071,16780300,50465804,576,36,224,1129054208,1129054208,1129054208,1098907648,1098907648,0,1645632,1073741824,1645056,44032,44032,40960,8776704,1073741824,8773632,17227,1107575630,4294923116,4294967294,17227,1107575630,4294923116,4294967294,17227,1107575630,4294923116,4294967294,
2709504,2709504,2752512,2752512,2795520,2795520,2838528,2838528,2838528,2838528,2838528,2838528,40960,44032,48128,51200,55296,58368,62464,65536,62464,65536,62464,65536,268439552,336991254,57,818024448,80,7488,192,1040137983,16780300,50465804,576,36,224,1115684864,1115684864,1115684864,1098907648,1098907648,0,516672,1073741824,516096,44032,44032,40960,2755584,1073741824,2752512,17020,1107575428,4294954476,4294967294,17020,1107575428,4294954476,4294967294,17020,1107575428,4294954476,4294967294,
4687872,4687872,4730880,4730880,4773888,4773888,4816896,4816896,4816896,4816896,4816896,4816896,40960,44032,48128,51200,55296,58368,62464,65536,62464,65536,62464,65536,268439552,336991254,57,1346506752,80,7488,192,1048526463,16780300,50465804,576,36,224,1121714176,1121714176,1121714176,1098907648,1098907648,0,887616,1073741824,887040,44032,44032,40960,4733952,1073741824,4730880,17114,1107575520,4294944172,4294967294,17114,1107575520,4294944172,4294967294,17114,1107575520,4294944172,4294967294,
5376,5376,5568,5568,5760,5760,5952,5952,6144,6144,6336,6336,40960,44032,48128,51200,55296,58368,62464,65536,69632,72704,76800,79872,268439552,336991254,410,1619143680,64,7984,43008,1048526591,50465804,16780300,36,80,224,1106247680,1106247680,1106247680,1098907648,1098907648,0,130104,1073741824,1080,51200,51200,48128,693888,1073741824,5760,16864,1107575300,4294962316,4294967294,16864,1107575300,4294962316,4294967294,16864,1107575300,4294962316,4294967294,
602112,645120,688128,731136,774144,817152,9160704,9203712,9203712,9203712,9203712,9203712,49152,53248,57344,61440,65536,69632,40960,45056,45056,45056,45056,45056,1886388224,320017685,57,1892429824,80,7488,192,1048526591,16780300,50465804,576,36,128,1129578496,1129578496,1098907648,1121976320,1121976320,0,1713600,1073741824,129024,32768,32768,53248,9139200,1073741824,688128,1121862475,1124090710,4294940652,110,1121862475,1124090710,4294940652,110,16736,1099973016,4294967052,4294967294,
22848,22848,23040,23040,23232,23232,23424,23424,23424,23424,23424,23424,40960,44032,48128,51200,55296,58368,62464,65536,62464,65536,62464,65536,268439552,336991254,410,2421825536,80,7984,43008,1056915071,50465804,16780300,36,80,224,1123024896,1123024896,1123024896,1098907648,1098907648,0,133344,1073741824,4320,44032,44032,40960,711168,1073741824,23040,17134,1107575540,4294941932,4294967294,17134,1107575540,4294941932,4294967294,17134,1107575540,4294941932,4294967294,
32448,32448,32640,32640,32832,32832,33024,33024,33024,33024,33024,33024,40960,44032,48128,51200,55296,58368,62464,65536,62464,65536,62464,65536,268439552,336991254,410,2694979584,80,7984,43008,1056915199,50465804,16780300,36,80,224,1126825984,1126825984,1126825984,1098907648,1098907648,0,135144,1073741824,6120,44032,44032,40960,720768,1073741824,32640,17193,1107575596,4294930732,4294967294,17193,1107575596,4294930732,4294967294,17193,1107575596,4294930732,4294967294,
42048,42048,42240,42240,42432,42432,42624,42624,42624,42624,42624,42624,40960,44032,48128,51200,55296,58368,62464,65536,62464,65536,62464,65536,268439552,336991254,410,3224510464,80,7984,43008,1056914943,50465804,16780300,36,80,224,1130102784,1130102784,1130102784,1098907648,1098907648,0,136944,1073741824,7920,44032,44032,40960,730368,1073741824,42240,17243,1107575646,4294919532,4294967294,17243,1107575646,4294919532,4294967294,17243,1107575646,4294919532,4294967294,
8816640,8816640,8859648,8859648,8902656,8902656,8945664,8945664,8945664,8945664,8945664,8945664,40960,44032,48128,51200,55296,58368,62464,65536,62464,65536,62464,65536,268439552,336991254,57,3497660416,80,7488,192,1056915071,16780300,50465804,576,36,224,1129185280,1129185280,1129185280,1098907648,1098907648,0,1661760,1073741824,1661184,44032,44032,40960,8862720,1073741824,8859648,17229,1107575632,4294922668,4294967294,17229,1107575632,4294922668,4294967294,17229,1107575632,4294922668,4294967294,
2795520,2795520,2838528,2838528,2881536,2881536,2924544,2924544,2924544,2924544,2924544,2924544,40960,44032,48128,51200,55296,58368,62464,65536,62464,65536,62464,65536,268439552,336991254,57,818548736,80,7488,192,1040137983,16780300,50465804,576,36,224,1115947008,1115947008,1115947008,1098907648,1098907648,0,532800,1073741824,532224,44032,44032,40960,2841600,1073741824,2838528,17026,1107575432,4294954028,4294967294,17026,1107575432,4294954028,4294967294,17026,1107575432,4294954028,4294967294,
3397632,3440640,3483648,3526656,3569664,3612672,3655680,3698688,3698688,3698688,3698688,3698688,40960,45056,49152,53248,57344,61440,65536,69632,69632,69632,69632,69632,538976288,353703189,57,1347027200,80,7488,192,1048526463,16780300,50465804,576,36,128,1118306304,1118044160,1117782016,1107296256,1107296256,1107296256,678528,662400,646272,57344,49152,40960,3618816,3532800,3446784,1106264734,1112031916,4294958284,30,1106264734,1112031916,4294958284,30,1106264734,1112031916,4294958284,30,
5760,5760,5952,5952,6144,6144,6336,6336,6528,6528,6720,6720,40960,44032,48128,51200,55296,58368,62464,65536,69632,72704,76800,79872,268439552,336991254,410,1619667968,64,7984,43008,1048526591,50465804,16780300,36,80,224,1107296256,1107296256,1107296256,1098907648,1098907648,0,130176,1073741824,1152,51200,51200,48128,694272,1073741824,6144,16880,1107575308,4294961868,4294967294,16880,1107575308,4294961868,4294967294,16880,1107575308,4294961868,4294967294,
860160,903168,946176,989184,1032192,1075200,9590784,9590784,9590784,9590784,9590784,9590784,49152,53248,57344,61440,65536,69632,40960,45056,45056,45056,45056,45056,1886388224,320017685,57,1893085184,80,7488,192,1048526591,16780300,50465804,576,36,128,1130233856,1130233856,1102053376,1121976320,1121976320,0,1794240,1073741824,177408,32768,32768,53248,9569280,1073741824,946176,1121862485,1124090719,4294939372,110,1121862485,1124090719,4294939372,110,16800,1099973064,4294966284,4294967294,
23232,23232,23424,23424,23616,23616,23808,23808,23808,23808,23808,23808,40960,44032,48128,51200,55296,58368,62464,65536,62464,65536,62464,65536,268439552,336991254,410,2422349824,80,7984,43008,1056915071,50465804,16780300,36,80,224,1123287040,1123287040,1123287040,1098907648,1098907648,0,133416,1073741824,4392,44032,44032,40960,711552,1073741824,23424,17138,1107575544,4294941484,4294967294,17138,1107575544,4294941484,4294967294,17138,1107575544,4294941484,4294967294,
32832,32832,33024,33024,33216,33216,33408,33408,33408,33408,33408,33408,40960,44032,48128,51200,55296,58368,62464,65536,62464,65536,62464,65536,268439552,336991254,410,2695503872,80,7984,43008,1056915199,50465804,16780300,36,80,224,1126957056,1126957056,1126957056,1098907648,1098907648,0,135216,1073741824,6192,44032,44032,40960,721152,1073741824,33024,17195,1107575598,4294930284,4294967294,17195,1107575598,4294930284,4294967294,17195,1107575598,4294930284,4294967294,
8300544,8343552,8386560,8429568,8472576,8515584,8558592,8601600,8601600,8601600,8601600,8601600,40960,45056,49152,53248,57344,61440,65536,69632,69632,69632,69632,69632,538976288,353703189,57,3225026816,80,7488,192,1056914943,16780300,50465804,576,36,128,1128660992,1128529920,1128398848,1107296256,1107296256,1107296256,1597824,1581696,1565568,57344,49152,40960,8521728,8435712,8349696,1106264897,1112032072,4294943692,30,1106264897,1112032072,4294943692,30,1106264897,1112032072,4294943692,30,
8902656,8902656,8945664,8945664,8988672,8988672,9031680,9031680,9031680,9031680,9031680,9031680,40960,44032,48128,51200,55296,58368,62464,65536,62464,65536,62464,65536,268439552,336991254,57,3498184704,80,7488,192,1056915199,16780300,50465804,576,36,224,1129316352,1129316352,1129316352,1098907648,1098907648,0,1677888,1073741824,1677312,44032,44032,40960,8948736,1073741824,8945664,17231,1107575634,4294922220,4294967294,17231,1107575634,4294922220,4294967294,17231,1107575634,4294922220,4294967294,
2881536,2881536,2924544,2924544,2967552,2967552,3010560,3010560,3010560,3010560,3010560,3010560,40960,44032,48128,51200,55296,58368,62464,65536,62464,65536,62464,65536,268439552,336991254,57,819073024,80,7488,192,1040137983,16780300,50465804,576,36,224,1116209152,1116209152,1116209152,1098907648,1098907648,0,548928,1073741824,548352,44032,44032,40960,2927616,1073741824,2924544,17030,1107575436,4294953580,4294967294,17030,1107575436,4294953580,4294967294,17030,1107575436,4294953580,4294967294,
4773888,4773888,4816896,4816896,4859904,4859904,4902912,4902912,4902912,4902912,4902912,4902912,40960,44032,48128,51200,55296,58368,62464,65536,62464,65536,62464,65536,268439552,336991254,57,1347555328,80,7488,192,1048526463,16780300,50465804,576,36,224,1121976320,1121976320,1121976320,1098907648,1098907648,0,903744,1073741824,903168,44032,44032,40960,4819968,1073741824,4816896,17118,1107575524,4294943724,4294967294,17118,1107575524,4294943724,4294967294,17118,1107575524,4294943724,4294967294,
6336,6336,6528,6528,6720,6720,6912,6912,6912,6912,6912,6912,40960,44032,48128,51200,55296,58368,62464,65536,62464,65536,62464,65536,268439552,336991254,410,1620189184,80,7984,43008,1048526591,50465804,16780300,36,80,224,1107820544,1107820544,1107820544,1098907648,1098907648,0,130248,1073741824,1224,44032,44032,40960,694656,1073741824,6528,16900,1107575312,4294961196,4294967294,16900,1107575312,4294961196,4294967294,16900,1107575312,4294961196,4294967294,
5462016,5505024,5548032,5591040,5634048,5677056,5720064,5763072,5763072,5763072,5763072,5763072,40960,45056,49152,53248,57344,61440,65536,69632,69632,69632,69632,69632,538976288,353703189,57,2149712128,80,7488,192,1048526335,16780300,50465804,576,36,128,1124335616,1124204544,1124073472,1107296256,1107296256,1107296256,1065600,1049472,1033344,57344,49152,40960,5683200,5597184,5511168,1106264830,1112032006,4294952140,30,1106264830,1112032006,4294952140,30,1106264830,1112032006,4294952140,30,
6494208,6537216,6580224,6623232,6666240,6709248,6752256,6795264,6795264,6795264,6795264,6795264,40960,45056,49152,53248,57344,61440,65536,69632,69632,69632,69632,69632,538976288,353703189,57,2422866176,80,7488,192,1056915071,16780300,50465804,576,36,128,1125908480,1125777408,1125646336,1107296256,1107296256,1107296256,1259136,1243008,1226880,57344,49152,40960,6715392,6629376,6543360,1106264855,1112032030,4294949068,30,1106264855,1112032030,4294949068,30,1106264855,1112032030,4294949068,30,
33216,33216,33408,33408,33600,33600,33792,33792,33792,33792,33792,33792,40960,44032,48128,51200,55296,58368,62464,65536,62464,65536,62464,65536,268439552,336991254,410,2696028160,80,7984,43008,1056915199,50465804,16780300,36,80,224,1127088128,1127088128,1127088128,1098907648,1098907648,0,135288,1073741824,6264,44032,44032,40960,721536,1073741824,33408,17197,1107575600,4294929836,4294967294,17197,1107575600,4294929836,4294967294,17197,1107575600,4294929836,4294967294,
42432,42432,42624,42624,42816,42816,42816,42816,42816,42816,42816,42816,40960,44032,48128,51200,55296,58368,62464,65536,62464,65536,62464,65536,268439552,336991254,410,3225559040,80,7984,43008,1056915071,50465804,16780300,36,80,224,1130233856,1130233856,1130233856,1098907648,1098907648,0,137016,1073741824,7992,44032,44032,40960,730752,1073741824,42624,17245,1107575647,4294919084,4294967294,17245,1107575647,4294919084,4294967294,17245,1107575647,4294919084,4294967294,
8988672,8988672,9031680,9031680,9074688,9074688,9117696,9117696,9117696,9117696,9117696,9117696,40960,44032,48128,51200,55296,58368,62464,65536,62464,65536,62464,65536,268439552,336991254,57,3498708992,80,7488,192,1056915199,16780300,50465804,576,36,224,1129447424,1129447424,1129447424,1098907648,1098907648,0,1694016,1073741824,1693440,44032,44032,40960,9034752,1073741824,9031680,17233,1107575636,4294921772,4294967294,17233,1107575636,4294921772,4294967294,17233,1107575636,4294921772,4294967294,
2967552,2967552,3010560,3010560,3053568,3053568,3096576,3096576,3096576,3096576,3096576,3096576,40960,44032,48128,51200,55296,58368,62464,65536,62464,65536,62464,65536,268439552,336991254,57,819597312,80,7488,192,1040137983,16780300,50465804,576,36,224,1116471296,1116471296,1116471296,1098907648,1098907648,0,565056,1073741824,564480,44032,44032,40960,3013632,1073741824,3010560,17034,1107575440,4294953132,4294967294,17034,1107575440,4294953132,4294967294,17034,1107575440,4294953132,4294967294,
4859904,4859904,4902912,4902912,4945920,4945920,4988928,4988928,4988928,4988928,4988928,4988928,40960,44032,48128,51200,55296,58368,62464,65536,62464,65536,62464,65536,268439552,336991254,57,1348079616,80,7488,192,1048526463,16780300,50465804,576,36,224,1122238464,1122238464,1122238464,1098907648,1098907648,0,919872,1073741824,919296,44032,44032,40960,4905984,1073741824,4902912,17122,1107575528,4294943276,4294967294,17122,1107575528,4294943276,4294967294,17122,1107575528,4294943276,4294967294,
6720,6720,6912,6912,7104,7104,7296,7296,7296,7296,7296,7296,40960,44032,48128,51200,55296,58368,62464,65536,62464,65536,62464,65536,268439552,336991254,410,1620713472,80,7984,43008,1048526591,50465804,16780300,36,80,224,1108344832,1108344832,1108344832,1098907648,1098907648,0,130320,1073741824,1296,44032,44032,40960,695040,1073741824,6912,16908,1107575320,4294960748,4294967294,16908,1107575320,4294960748,4294967294,16908,1107575320,4294960748,4294967294,
15168,15168,15360,15360,15552,15552,15744,15744,15744,15744,15744,15744,40960,44032,48128,51200,55296,58368,62464,65536,62464,65536,62464,65536,268439552,336991254,410,2150244352,80,7984,43008,1048526335,50465804,16780300,36,80,224,1117782016,1117782016,1117782016,1098907648,1098907648,0,131904,1073741824,2880,44032,44032,40960,703488,1073741824,15360,17054,1107575460,4294950892,4294967294,17054,1107575460,4294950892,4294967294,17054,1107575460,4294950892,4294967294,
23616,23616,23808,23808,24000,24000,24192,24192,24192,24192,24192,24192,40960,44032,48128,51200,55296,58368,62464,65536,62464,65536,62464,65536,268439552,336991254,410,2423398400,80,7984,43008,1056915071,50465804,16780300,36,80,224,1123549184,1123549184,1123549184,1098907648,1098907648,0,133488,1073741824,4464,44032,44032,40960,711936,1073741824,23808,17142,1107575548,4294941036,4294967294,17142,1107575548,4294941036,4294967294,17142,1107575548,4294941036,4294967294,
33600,33600,33792,33792,33984,33984,34176,34176,34176,34176,34176,34176,40960,44032,48128,51200,55296,58368,62464,65536,62464,65536,62464,65536,268439552,336991254,410,2696552448,80,7984,43008,1056915199,50465804,16780300,36,80,224,1127219200,1127219200,1127219200,1098907648,1098907648,0,135360,1073741824,6336,44032,44032,40960,721920,1073741824,33792,17199,1107575602,4294929388,4294967294,17199,1107575602,4294929388,4294967294,17199,1107575602,4294929388,4294967294,
6838272,6838272,6881280,6881280,6924288,6924288,6967296,6967296,6967296,6967296,6967296,6967296,40960,44032,48128,51200,55296,58368,62464,65536,62464,65536,62464,65536,268439552,336991254,57,3226079232,80,7488,192,1056915071,16780300,50465804,576,36,224,1126170624,1126170624,1126170624,1098907648,1098907648,0,1290816,1073741824,1290240,44032,44032,40960,6884352,1073741824,6881280,17183,1107575586,4294932972,4294967294,17183,1107575586,4294932972,4294967294,17183,1107575586,4294932972,4294967294,
9074688,9074688,9117696,9117696,9160704,9160704,9203712,9203712,9203712,9203712,9203712,9203712,40960,44032,48128,51200,55296,58368,62464,65536,62464,65536,62464,65536,268439552,336991254,57,3499233280,80,7488,192,1056915199,16780300,50465804,576,36,224,1129578496,1129578496,1129578496,1098907648,1098907648,0,1710144,1073741824,1709568,44032,44032,40960,9120768,1073741824,9117696,17235,1107575638,4294921324,4294967294,17235,1107575638,4294921324,4294967294,17235,1107575638,4294921324,4294967294,
3053568,3053568,3096576,3096576,3139584,3139584,3182592,3182592,3182592,3182592,3182592,3182592,40960,44032,48128,51200,55296,58368,62464,65536,62464,65536,62464,65536,268439552,336991254,57,1075974144,80,7488,192,1040137727,16780300,50465804,576,36,224,1116733440,1116733440,1116733440,1098907648,1098907648,0,581184,1073741824,580608,44032,44032,40960,3099648,1073741824,3096576,17038,1107575444,4294952684,4294967294,17038,1107575444,4294952684,4294967294,17038,1107575444,4294952684,4294967294,
4945920,4945920,4988928,4988928,5031936,5031936,5074944,5074944,5074944,5074944,5074944,5074944,40960,44032,48128,51200,55296,58368,62464,65536,62464,65536,62464,65536,268439552,336991254,57,1348603904,80,7488,192,1048526463,16780300,50465804,576,36,224,1122500608,1122500608,1122500608,1098907648,1098907648,0,936000,1073741824,935424,44032,44032,40960,4992000,1073741824,4988928,17126,1107575532,4294942828,4294967294,17126,1107575532,4294942828,4294967294,17126,1107575532,4294942828,4294967294,
7104,7104,7296,7296,7488,7488,7680,7680,7680,7680,7680,7680,40960,44032,48128,51200,55296,58368,62464,65536,62464,65536,62464,65536,268439552,336991254,410,1621237760,80,7984,43008,1048526591,50465804,16780300,36,80,224,1108869120,1108869120,1108869120,1098907648,1098907648,0,130392,1073741824,1368,44032,44032,40960,695424,1073741824,7296,16916,1107575328,4294960300,4294967294,16916,1107575328,4294960300,4294967294,16916,1107575328,4294960300,4294967294,
15552,15552,15744,15744,15936,15936,16128,16128,16128,16128,16128,16128,40960,44032,48128,51200,55296,58368,62464,65536,62464,65536,62464,65536,268439552,336991254,410,2150768640,80,7984,43008,1048526335,50465804,16780300,36,80,224,1118044160,1118044160,1118044160,1098907648,1098907648,0,131976,1073741824,2952,44032,44032,40960,703872,1073741824,15744,17058,1107575464,4294950444,4294967294,17058,1107575464,4294950444,4294967294,17058,1107575464,4294950444,4294967294,
24000,24000,24192,24192,24384,24384,24576,24576,24576,24576,24576,24576,40960,44032,48128,51200,55296,58368,62464,65536,62464,65536,62464,65536,268439552,336991254,410,2423922688,80,7984,43008,1056915071,50465804,16780300,36,80,224,1123811328,1123811328,1123811328,1098907648,1098907648,0,133560,1073741824,4536,44032,44032,40960,712320,1073741824,24192,17146,1107575552,4294940588,4294967294,17146,1107575552,4294940588,4294967294,17146,1107575552,4294940588,4294967294,
172032,215040,258048,301056,344064,387072,8730624,8773632,8773632,8773632,8773632,8773632,49152,53248,57344,61440,65536,69632,40960,45056,45056,45056,45056,45056,2694840320,320017685,57,2697081344,80,7488,192,1056915199,16780300,50465804,576,36,128,1128923136,1128923136,1086324736,1126170624,1126170624,0,1634688,1073741824,48384,32768,32768,53248,8718336,1073741824,258048,1126122305,1127236428,4294941644,158,1126122305,1127236428,4294941644,158,16512,1099972880,1036,4294967294,
6924288,6924288,6967296,6967296,7010304,7010304,7053312,7053312,7053312,7053312,7053312,7053312,40960,44032,48128,51200,55296,58368,62464,65536,62464,65536,62464,65536,268439552,336991254,57,3226603520,80,7488,192,1056915071,16780300,50465804,576,36,224,1126301696,1126301696,1126301696,1098907648,1098907648,0,1306944,1073741824,1306368,44032,44032,40960,6970368,1073741824,6967296,17185,1107575588,4294932524,4294967294,17185,1107575588,4294932524,4294967294,17185,1107575588,4294932524,4294967294,
9160704,9160704,9203712,9203712,9246720,9246720,9289728,9289728,9289728,9289728,9289728,9289728,40960,44032,48128,51200,55296,58368,62464,65536,62464,65536,62464,65536,268439552,336991254,57,3499757568,80,7488,192,1056915199,16780300,50465804,576,36,224,1129709568,1129709568,1129709568,1098907648,1098907648,0,1726272,1073741824,1725696,44032,44032,40960,9206784,1073741824,9203712,17237,1107575640,4294920876,4294967294,17237,1107575640,4294920876,4294967294,17237,1107575640,4294920876,4294967294,
2365440,2408448,2451456,2494464,2537472,2580480,2623488,2666496,2666496,2666496,2666496,2666496,40960,45056,49152,53248,57344,61440,65536,69632,69632,69632,69632,69632,538976288,353703189,57,1076494592,80,7488,192,1040137727,16780300,50465804,576,36,128,1114636288,1114112000,1113587712,1107296256,1107296256,1107296256,484992,468864,452736,57344,49152,40960,2586624,2500608,2414592,1106264668,1112031864,4294961356,30,1106264668,1112031864,4294961356,30,1106264668,1112031864,4294961356,30,
5031936,5031936,5074944,5074944,5117952,5117952,5160960,5160960,5160960,5160960,5160960,5160960,40960,44032,48128,51200,55296,58368,62464,65536,62464,65536,62464,65536,268439552,336991254,57,1349128192,80,7488,192,1048526463,16780300,50465804,576,36,224,1122762752,1122762752,1122762752,1098907648,1098907648,0,952128,1073741824,951552,44032,44032,40960,5078016,1073741824,5074944,17130,1107575536,4294942380,4294967294,17130,1107575536,4294942380,4294967294,17130,1107575536,4294942380,4294967294,
4687872,4730880,4773888,4816896,4859904,4902912,4945920,4988928,4988928,4988928,4988928,4988928,40960,45056,49152,53248,57344,61440,65536,69632,69632,69632,69632,69632,538976288,353703189,57,1621754112,80,7488,192,1048526591,16780300,50465804,576,36,128,1122238464,1121976320,1121714176,1107296256,1107296256,1107296256,920448,904320,888192,57344,49152,40960,4909056,4823040,4737024,1106264794,1112031976,4294954444,30,1106264794,1112031976,4294954444,30,1106264794,1112031976,4294954444,30,
15936,15936,16128,16128,16320,16320,16512,16512,16512,16512,16512,16512,40960,44032,48128,51200,55296,58368,62464,65536,62464,65536,62464,65536,268439552,336991254,410,2151292928,80,7984,43008,1048526335,50465804,16780300,36,80,224,1118306304,1118306304,1118306304,1098907648,1098907648,0,132048,1073741824,3024,44032,44032,40960,704256,1073741824,16128,17062,1107575468,4294949996,4294967294,17062,1107575468,4294949996,4294967294,17062,1107575468,4294949996,4294967294,
24384,24384,24576,24576,24768,24768,24960,24960,24960,24960,24960,24960,40960,44032,48128,51200,55296,58368,62464,65536,62464,65536,62464,65536,268439552,336991254,410,2424446976,80,7984,43008,1056915199,50465804,16780300,36,80,224,1124073472,1124073472,1124073472,1098907648,1098907648,0,133632,1073741824,4608,44032,44032,40960,712704,1073741824,24576,17150,1107575554,4294940140,4294967294,17150,1107575554,4294940140,4294967294,17150,1107575554,4294940140,4294967294,
688128,731136,774144,817152,860160,903168,9160704,9203712,9203712,9203712,9203712,9203712,49152,53248,57344,61440,65536,69632,40960,45056,45056,45056,45056,45056,2694840320,320017685,57,2697736192,80,7488,192,1056915199,16780300,50465804,576,36,128,1129578496,1129578496,1099956224,1126170624,1126170624,0,1715328,1073741824,145152,32768,32768,53248,9148416,1073741824,774144,1126122315,1127236438,4294940364,158,1126122315,1127236438,4294940364,158,16768,1099973032,4294966796,4294967294,
7010304,7010304,7053312,7053312,7096320,7096320,7139328,7139328,7139328,7139328,7139328,7139328,40960,44032,48128,51200,55296,58368,62464,65536,62464,65536,62464,65536,268439552,336991254,57,3227127808,80,7488,192,1056915071,16780300,50465804,576,36,224,1126432768,1126432768,1126432768,1098907648,1098907648,0,1323072,1073741824,1322496,44032,44032,40960,7056384,1073741824,7053312,17187,1107575590,4294932076,4294967294,17187,1107575590,4294932076,4294967294,17187,1107575590,4294932076,4294967294,
9332736,9375744,9418752,9461760,9504768,9547776,9590784,9590784,9590784,9590784,9590784,9590784,40960,45056,49152,53248,57344,61440,65536,69632,69632,69632,69632,69632,538976288,353703189,57,3500278016,80,7488,192,1056915199,16780300,50465804,576,36,128,1130233856,1130102784,1129971712,1107296256,1107296256,1107296256,1791360,1775232,1759104,57344,49152,40960,9553920,9467904,9381888,1106264921,1112032095,4294940620,30,1106264921,1112032095,4294940620,30,1106264921,1112032095,4294940620,30,
3139584,3139584,3182592,3182592,3225600,3225600,3268608,3268608,3268608,3268608,3268608,3268608,40960,44032,48128,51200,55296,58368,62464,65536,62464,65536,62464,65536,268439552,336991254,57,1077022720,80,7488,192,1040137727,16780300,50465804,576,36,224,1116995584,1116995584,1116995584,1098907648,1098907648,0,597312,1073741824,596736,44032,44032,40960,3185664,1073741824,3182592,17042,1107575448,4294952236,4294967294,17042,1107575448,4294952236,4294967294,17042,1107575448,4294952236,4294967294,
5117952,5117952,5160960,5160960,5203968,5203968,5246976,5246976,5246976,5246976,5246976,5246976,40960,44032,48128,51200,55296,58368,62464,65536,62464,65536,62464,65536,268439552,336991254,57,1349652480,80,7488,192,1048526463,16780300,50465804,576,36,224,1123024896,1123024896,1123024896,1098907648,1098907648,0,968256,1073741824,967680,44032,44032,40960,5164032,1073741824,5160960,17134,1107575540,4294941932,4294967294,17134,1107575540,4294941932,4294967294,17134,1107575540,4294941932,4294967294,
7488,7488,7680,7680,7872,7872,8064,8064,8064,8064,8064,8064,40960,44032,48128,51200,55296,58368,62464,65536,62464,65536,62464,65536,268439552,336991254,410,1622286336,80,7984,43008,1048526591,50465804,16780300,36,80,224,1109393408,1109393408,1109393408,1098907648,1098907648,0,130464,1073741824,1440,44032,44032,40960,695808,1073741824,7680,16924,1107575336,4294959852,4294967294,16924,1107575336,4294959852,4294967294,16924,1107575336,4294959852,4294967294,
16320,16320,16512,16512,16704,16704,16896,16896,16896,16896,16896,16896,40960,44032,48128,51200,55296,58368,62464,65536,62464,65536,62464,65536,268439552,336991254,410,2151817216,80,7984,43008,1048526463,50465804,16780300,36,80,224,1118568448,1118568448,1118568448,1098907648,1098907648,0,132120,1073741824,3096,44032,44032,40960,704640,1073741824,16512,17066,1107575472,4294949548,4294967294,17066,1107575472,4294949548,4294967294,17066,1107575472,4294949548,4294967294,
24768,24768,24960,24960,25152,25152,25344,25344,25344,25344,25344,25344,40960,44032,48128,51200,55296,58368,62464,65536,62464,65536,62464,65536,268439552,336991254,410,2424971264,80,7984,43008,1056915199,50465804,16780300,36,80,224,1124204544,1124204544,1124204544,1098907648,1098907648,0,133704,1073741824,4680,44032,44032,40960,713088,1073741824,24960,17153,1107575556,4294939692,4294967294,17153,1107575556,4294939692,4294967294,17153,1107575556,4294939692,4294967294,
1032192,1075200,1118208,1161216,1204224,1247232,9590784,9590784,9590784,9590784,9590784,9590784,49152,53248,57344,61440,65536,69632,40960,45056,45056,45056,45056,45056,2694840320,320017685,57,2698391552,80,7488,192,1056915199,16780300,50465804,576,36,128,1130233856,1130233856,1104150528,1126170624,1126170624,0,1795968,1073741824,209664,32768,32768,53248,9578496,1073741824,1118208,1126122325,1127236447,4294939084,158,1126122325,1127236447,4294939084,158,16832,1099973096,4294965772,4294967294,
7096320,7096320,7139328,7139328,7182336,7182336,7225344,7225344,7225344,7225344,7225344,7225344,40960,44032,48128,51200,55296,58368,62464,65536,62464,65536,62464,65536,268439552,336991254,57,3227652096,80,7488,192,1056915071,16780300,50465804,576,36,224,1126563840,1126563840,1126563840,1098907648,1098907648,0,1339200,1073741824,1338624,44032,44032,40960,7142400,1073741824,7139328,17189,1107575592,4294931628,4294967294,17189,1107575592,4294931628,4294967294,17189,1107575592,4294931628,4294967294,
9246720,9246720,9289728,9289728,9332736,9332736,9375744,9375744,9375744,9375744,9375744,9375744,40960,44032,48128,51200,55296,58368,62464,65536,62464,65536,62464,65536,268439552,336991254,57,3500806144,80,7488,192,1056915199,16780300,50465804,576,36,224,1129840640,1129840640,1129840640,1098907648,1098907648,0,1742400,1073741824,1741824,44032,44032,40960,9292800,1073741824,9289728,17239,1107575642,4294920428,4294967294,17239,1107575642,4294920428,4294967294,17239,1107575642,4294920428,4294967294,
3225600,3225600,3268608,3268608,3311616,3311616,3354624,3354624,3354624,3354624,3354624,3354624,40960,44032,48128,51200,55296,58368,62464,65536,62464,65536,62464,65536,268439552,336991254,57,1077547008,80,7488,192,1040137727,16780300,50465804,576,36,224,1117257728,1117257728,1117257728,1098907648,1098907648,0,613440,1073741824,612864,44032,44032,40960,3271680,1073741824,3268608,17046,1107575452,4294951788,4294967294,17046,1107575452,4294951788,4294967294,17046,1107575452,4294951788,4294967294,
5203968,5203968,5246976,5246976,5289984,5289984,5332992,5332992,5332992,5332992,5332992,5332992,40960,44032,48128,51200,55296,58368,62464,65536,62464,65536,62464,65536,268439552,336991254,57,1350176768,80,7488,192,1048526463,16780300,50465804,576,36,224,1123287040,1123287040,1123287040,1098907648,1098907648,0,984384,1073741824,983808,44032,44032,40960,5250048,1073741824,5246976,17138,1107575544,4294941484,4294967294,17138,1107575544,4294941484,4294967294,17138,1107575544,4294941484,4294967294,
7872,7872,8064,8064,8256,8256,8448,8448,8448,8448,8448,8448,40960,44032,48128,51200,55296,58368,62464,65536,62464,65536,62464,65536,268439552,336991254,410,1622810624,80,7984,43008,1048526591,50465804,16780300,36,80,224,1109917696,1109917696,1109917696,1098907648,1098907648,0,130536,1073741824,1512,44032,44032,40960,696192,1073741824,8064,16932,1107575344,4294959404,4294967294,16932,1107575344,4294959404,4294967294,16932,1107575344,4294959404,4294967294,
16704,16704,16896,16896,17088,17088,17280,17280,17280,17280,17280,17280,40960,44032,48128,51200,55296,58368,62464,65536,62464,65536,62464,65536,268439552,336991254,410,2152341504,80,7984,43008,1048526463,50465804,16780300,36,80,224,1118830592,1118830592,1118830592,1098907648,1098907648,0,132192,1073741824,3168,44032,44032,40960,705024,1073741824,16896,17070,1107575476,4294949100,4294967294,17070,1107575476,4294949100,4294967294,17070,1107575476,4294949100,4294967294,
25152,25152,25344,25344,25536,25536,25728,25728,25728,25728,25728,25728,40960,44032,48128,51200,55296,58368,62464,65536,62464,65536,62464,65536,268439552,336991254,410,2425495552,80,7984,43008,1056915199,50465804,16780300,36,80,224,1124335616,1124335616,1124335616,1098907648,1098907648,0,133776,1073741824,4752,44032,44032,40960,713472,1073741824,25344,17155,1107575558,4294939244,4294967294,17155,1107575558,4294939244,4294967294,17155,1107575558,4294939244,4294967294,
33984,33984,34176,34176,34368,34368,34560,34560,34560,34560,34560,34560,40960,44032,48128,51200,55296,58368,62464,65536,62464,65536,62464,65536,268439552,336991254,410,2955026432,80,7984,43008,1056914943,50465804,16780300,36,80,224,1127350272,1127350272,1127350272,1098907648,1098907648,0,135432,1073741824,6408,44032,44032,40960,722304,1073741824,34176,17201,1107575604,4294928940,4294967294,17201,1107575604,4294928940,4294967294,17201,1107575604,4294928940,4294967294,
7182336,7182336,7225344,7225344,7268352,7268352,7311360,7311360,7311360,7311360,7311360,7311360,40960,44032,48128,51200,55296,58368,62464,65536,62464,65536,62464,65536,268439552,336991254,57,3228176384,80,7488,192,1056915071,16780300,50465804,576,36,224,1126694912,1126694912,1126694912,1098907648,1098907648,0,1355328,1073741824,1354752,44032,44032,40960,7228416,1073741824,7225344,17191,1107575594,4294931180,4294967294,17191,1107575594,4294931180,4294967294,17191,1107575594,4294931180,4294967294,
9332736,9332736,9375744,9375744,9418752,9418752,9461760,9461760,9461760,9461760,9461760,9461760,40960,44032,48128,51200,55296,58368,62464,65536,62464,65536,62464,65536,268439552,336991254,57,3501330432,80,7488,192,1056915199,16780300,50465804,576,36,224,1129971712,1129971712,1129971712,1098907648,1098907648,0,1758528,1073741824,1757952,44032,44032,40960,9378816,1073741824,9375744,17241,1107575644,4294919980,4294967294,17241,1107575644,4294919980,4294967294,17241,1107575644,4294919980,4294967294,
3311616,3311616,3354624,3354624,3397632,3397632,3440640,3440640,3440640,3440640,3440640,3440640,40960,44032,48128,51200,55296,58368,62464,65536,62464,65536,62464,65536,268439552,336991254,57,1078071296,80,7488,192,1040137855,16780300,50465804,576,36,224,1117519872,1117519872,1117519872,1098907648,1098907648,0,629568,1073741824,628992,44032,44032,40960,3357696,1073741824,3354624,17050,1107575456,4294951340,4294967294,17050,1107575456,4294951340,4294967294,17050,1107575456,4294951340,4294967294,
3655680,3698688,3741696,3784704,3827712,3870720,3913728,3956736,3956736,3956736,3956736,3956736,40960,45056,49152,53248,57344,61440,65536,69632,69632,69632,69632,69632,538976288,353703189,57,1350697216,80,7488,192,1048526591,16780300,50465804,576,36,128,1119092736,1118830592,1118568448,1107296256,1107296256,1107296256,726912,710784,694656,57344,49152,40960,3876864,3790848,3704832,1106264746,1112031928,4294957516,30,1106264746,1112031928,4294957516,30,1106264746,1112031928,4294957516,30,
0,0,0,43008,86016,129024,8730624,8773632,8773632,8773632,8773632,8773632,49152,53248,57344,61440,65536,69632,40960,45056,45056,45056,45056,45056,1616904192,320017685,57,1623339520,80,7488,192,1048526591,16780300,50465804,576,36,128,1128923136,1128923136,0,1119879168,1119879168,0,1632384,1073741824,0,32768,32768,53248,8706048,1073741824,0,1119765313,1121993548,4294942028,94,1119765313,1121993548,4294942028,94,0,1099972672,1804,4294967294,
17088,17088,17280,17280,17472,17472,17664,17664,17664,17664,17664,17664,40960,44032,48128,51200,55296,58368,62464,65536,62464,65536,62464,65536,268439552,336991254,410,2152865792,80,7984,43008,1048526463,50465804,16780300,36,80,224,1119092736,1119092736,1119092736,1098907648,1098907648,0,132264,1073741824,3240,44032,44032,40960,705408,1073741824,17280,17074,1107575480,4294948652,4294967294,17074,1107575480,4294948652,4294967294,17074,1107575480,4294948652,4294967294,
25536,25536,25728,25728,25920,25920,26112,26112,26112,26112,26112,26112,40960,44032,48128,51200,55296,58368,62464,65536,62464,65536,62464,65536,268439552,336991254,410,2426019840,80,7984,43008,1056915199,50465804,16780300,36,80,224,1124466688,1124466688,1124466688,1098907648,1098907648,0,133848,1073741824,4824,44032,44032,40960,713856,1073741824,25728,17157,1107575560,4294938796,4294967294,17157,1107575560,4294938796,4294967294,17157,1107575560,4294938796,4294967294,
7526400,7569408,7612416,7655424,7698432,7741440,7784448,7827456,7827456,7827456,7827456,7827456,40960,45056,49152,53248,57344,61440,65536,69632,69632,69632,69632,69632,538976288,353703189,57,2955542784,80,7488,192,1056914943,16780300,50465804,576,36,128,1127481344,1127350272,1127219200,1107296256,1107296256,1107296256,1452672,1436544,1420416,57344,49152,40960,7747584,7661568,7575552,1106264879,1112032054,4294945996,30,1106264879,1112032054,4294945996,30,1106264879,1112032054,4294945996,30,
7268352,7268352,7311360,7311360,7354368,7354368,7397376,7397376,7397376,7397376,7397376,7397376,40960,44032,48128,51200,55296,58368,62464,65536,62464,65536,62464,65536,268439552,336991254,57,3228700672,80,7488,192,1056915071,16780300,50465804,576,36,224,1126825984,1126825984,1126825984,1098907648,1098907648,0,1371456,1073741824,1370880,44032,44032,40960,7314432,1073741824,7311360,17193,1107575596,4294930732,4294967294,17193,1107575596,4294930732,4294967294,17193,1107575596,4294930732,4294967294,
9418752,9418752,9461760,9461760,9504768,9504768,9547776,9547776,9547776,9547776,9547776,9547776,40960,44032,48128,51200,55296,58368,62464,65536,62464,65536,62464,65536,268439552,336991254,57,3501854720,80,7488,192,1056915199,16780300,50465804,576,36,224,1130102784,1130102784,1130102784,1098907648,1098907648,0,1774656,1073741824,1774080,44032,44032,40960,9464832,1073741824,9461760,17243,1107575646,4294919532,4294967294,17243,1107575646,4294919532,4294967294,17243,1107575646,4294919532,4294967294,
3397632,3397632,3440640,3440640,3483648,3483648,3526656,3526656,3526656,3526656,3526656,3526656,40960,44032,48128,51200,55296,58368,62464,65536,62464,65536,62464,65536,268439552,336991254,57,1078595584,80,7488,192,1040137855,16780300,50465804,576,36,224,1117782016,1117782016,1117782016,1098907648,1098907648,0,645696,1073741824,645120,44032,44032,40960,3443712,1073741824,3440640,17054,1107575460,4294950892,4294967294,17054,1107575460,4294950892,4294967294,17054,1107575460,4294950892,4294967294,
5289984,5289984,5332992,5332992,5376000,5376000,5419008,5419008,5419008,5419008,5419008,5419008,40960,44032,48128,51200,55296,58368,62464,65536,62464,65536,62464,65536,268439552,336991254,57,1351225344,80,7488,192,1048526591,16780300,50465804,576,36,224,1123549184,1123549184,1123549184,1098907648,1098907648,0,1000512,1073741824,999936,44032,44032,40960,5336064,1073741824,5332992,17142,1107575548,4294941036,4294967294,17142,1107575548,4294941036,4294967294,17142,1107575548,4294941036,4294967294,
344064,387072,430080,473088,516096,559104,9160704,9203712,9203712,9203712,9203712,9203712,49152,53248,57344,61440,65536,69632,40960,45056,45056,45056,45056,45056,1616904192,320017685,57,1623994880,80,7488,192,1048526591,16780300,50465804,576,36,128,1129578496,1129578496,1092616192,1119879168,1119879168,0,1713024,1073741824,80640,32768,32768,53248,9136128,1073741824,430080,1119765323,1121993558,4294940748,94,1119765323,1121993558,4294940748,94,16640,1099972944,524,4294967294,
5720064,5763072,5806080,5849088,5892096,5935104,5978112,6021120,6021120,6021120,6021120,6021120,40960,45056,49152,53248,57344,61440,65536,69632,69632,69632,69632,69632,538976288,353703189,57,2153382144,80,7488,192,1048526463,16780300,50465804,576,36,128,1124728832,1124597760,1124466688,1107296256,1107296256,1107296256,1113984,1097856,1081728,57344,49152,40960,5941248,5855232,5769216,1106264837,1112032012,4294951372,30,1106264837,1112032012,4294951372,30,1106264837,1112032012,4294951372,30,
25920,25920,26112,26112,26304,26304,26496,26496,26496,26496,26496,26496,40960,44032,48128,51200,55296,58368,62464,65536,62464,65536,62464,65536,268439552,336991254,410,2426544128,80,7984,43008,1056915199,50465804,16780300,36,80,224,1124597760,1124597760,1124597760,1098907648,1098907648,0,133920,1073741824,4896,44032,44032,40960,714240,1073741824,26112,17159,1107575562,4294938348,4294967294,17159,1107575562,4294938348,4294967294,17159,1107575562,4294938348,4294967294,
34368,34368,34560,34560,34752,34752,34944,34944,34944,34944,34944,34944,40960,44032,48128,51200,55296,58368,62464,65536,62464,65536,62464,65536,268439552,336991254,410,2956075008,80,7984,43008,1056914943,50465804,16780300,36,80,224,1127481344,1127481344,1127481344,1098907648,1098907648,0,135504,1073741824,6480,44032,44032,40960,722688,1073741824,34560,17203,1107575606,4294928492,4294967294,17203,1107575606,4294928492,4294967294,17203,1107575606,4294928492,4294967294,
8558592,8601600,8644608,8687616,8730624,8773632,8816640,8859648,8859648,8859648,8859648,8859648,40960,45056,49152,53248,57344,61440,65536,69632,69632,69632,69632,69632,538976288,353703189,57,3229221120,80,7488,192,1056915071,16780300,50465804,576,36,128,1129054208,1128923136,1128792064,1107296256,1107296256,1107296256,1646208,1630080,1613952,57344,49152,40960,8779776,8693760,8607744,1106264903,1112032078,4294942924,30,1106264903,1112032078,4294942924,30,1106264903,1112032078,4294942924,30,
9504768,9504768,9547776,9547776,9590784,9590784,9590784,9590784,9590784,9590784,9590784,9590784,40960,44032,48128,51200,55296,58368,62464,65536,62464,65536,62464,65536,268439552,336991254,57,3502379008,80,7488,192,1056915199,16780300,50465804,576,36,224,1130233856,1130233856,1130233856,1098907648,1098907648,0,1790784,1073741824,1790208,44032,44032,40960,9550848,1073741824,9547776,17245,1107575647,4294919084,4294967294,17245,1107575647,4294919084,4294967294,17245,1107575647,4294919084,4294967294,
3483648,3483648,3526656,3526656,3569664,3569664,3612672,3612672,3612672,3612672,3612672,3612672,40960,44032,48128,51200,55296,58368,62464,65536,62464,65536,62464,65536,268439552,336991254,57,1079119872,80,7488,192,1040137855,16780300,50465804,576,36,224,1118044160,1118044160,1118044160,1098907648,1098907648,0,661824,1073741824,661248,44032,44032,40960,3529728,1073741824,3526656,17058,1107575464,4294950444,4294967294,17058,1107575464,4294950444,4294967294,17058,1107575464,4294950444,4294967294,
5376000,5376000,5419008,5419008,5462016,5462016,5505024,5505024,5505024,5505024,5505024,5505024,40960,44032,48128,51200,55296,58368,62464,65536,62464,65536,62464,65536,268439552,336991254,57,1351749632,80,7488,192,1048526591,16780300,50465804,576,36,224,1123811328,1123811328,1123811328,1098907648,1098907648,0,1016640,1073741824,1016064,44032,44032,40960,5422080,1073741824,5419008,17146,1107575552,4294940588,4294967294,17146,1107575552,4294940588,4294967294,17146,1107575552,4294940588,4294967294,
774144,817152,860160,903168,946176,989184,9590784,9590784,9590784,9590784,9590784,9590784,49152,53248,57344,61440,65536,69632,40960,45056,45056,45056,45056,45056,1616904192,320017685,57,1624649728,80,7488,192,1048526591,16780300,50465804,576,36,128,1130233856,1130233856,1101004800,1119879168,1119879168,0,1793664,1073741824,161280,32768,32768,53248,9566208,1073741824,860160,1119765333,1121993567,4294939468,94,1119765333,1121993567,4294939468,94,16784,1099973048,4294966540,4294967294,
17472,17472,17664,17664,17856,17856,18048,18048,18048,18048,18048,18048,40960,44032,48128,51200,55296,58368,62464,65536,62464,65536,62464,65536,268439552,336991254,410,2153914368,80,7984,43008,1048526463,50465804,16780300,36,80,224,1119354880,1119354880,1119354880,1098907648,1098907648,0,132336,1073741824,3312,44032,44032,40960,705792,1073741824,17664,17078,1107575484,4294948204,4294967294,17078,1107575484,4294948204,4294967294,17078,1107575484,4294948204,4294967294,
6752256,6795264,6838272,6881280,6924288,6967296,7010304,7053312,7053312,7053312,7053312,7053312,40960,45056,49152,53248,57344,61440,65536,69632,69632,69632,69632,69632,538976288,353703189,57,2427060480,80,7488,192,1056915199,16780300,50465804,576,36,128,1126301696,1126170624,1126039552,1107296256,1107296256,1107296256,1307520,1291392,1275264,57344,49152,40960,6973440,6887424,6801408,1106264861,1112032036,4294948300,30,1106264861,1112032036,4294948300,30,1106264861,1112032036,4294948300,30,
34752,34752,34944,34944,35136,35136,35328,35328,35328,35328,35328,35328,40960,44032,48128,51200,55296,58368,62464,65536,62464,65536,62464,65536,268439552,336991254,410,2956599296,80,7984,43008,1056914943,50465804,16780300,36,80,224,1127612416,1127612416,1127612416,1098907648,1098907648,0,135576,1073741824,6552,44032,44032,40960,723072,1073741824,34944,17205,1107575608,4294928044,4294967294,17205,1107575608,4294928044,4294967294,17205,1107575608,4294928044,4294967294,
7354368,7354368,7397376,7397376,7440384,7440384,7483392,7483392,7483392,7483392,7483392,7483392,40960,44032,48128,51200,55296,58368,62464,65536,62464,65536,62464,65536,268439552,336991254,57,3229749248,80,7488,192,1056915199,16780300,50465804,576,36,224,1126957056,1126957056,1126957056,1098907648,1098907648,0,1387584,1073741824,1387008,44032,44032,40960,7400448,1073741824,7397376,17195,1107575598,4294930284,4294967294,17195,1107575598,4294930284,4294967294,17195,1107575598,4294930284,4294967294,
516096,559104,602112,645120,688128,731136,9074688,9117696,9117696,9117696,9117696,9117696,49152,53248,57344,61440,65536,69632,40960,45056,45056,45056,45056,45056,3503292416,320017685,57,3502912000,80,7488,192,1056915199,16780300,50465804,576,36,128,1129447424,1129447424,1096810496,1129316352,1129316352,0,1700928,1073741824,112896,32768,32768,53248,9071616,1073741824,602112,1129268041,1130316628,4294940332,206,1129268041,1130316628,4294940332,206,16704,1099973000,12,4294967294,
3569664,3569664,3612672,3612672,3655680,3655680,3698688,3698688,3698688,3698688,3698688,3698688,40960,44032,48128,51200,55296,58368,62464,65536,62464,65536,62464,65536,268439552,336991254,57,1079644160,80,7488,192,1040137855,16780300,50465804,576,36,224,1118306304,1118306304,1118306304,1098907648,1098907648,0,677952,1073741824,677376,44032,44032,40960,3615744,1073741824,3612672,17062,1107575468,4294949996,4294967294,17062,1107575468,4294949996,4294967294,17062,1107575468,4294949996,4294967294,
5462016,5462016,5505024,5505024,5548032,5548032,5591040,5591040,5591040,5591040,5591040,5591040,40960,44032,48128,51200,55296,58368,62464,65536,62464,65536,62464,65536,268439552,336991254,57,1352273920,80,7488,192,1048526591,16780300,50465804,576,36,224,1124073472,1124073472,1124073472,1098907648,1098907648,0,1032768,1073741824,1032192,44032,44032,40960,5508096,1073741824,5505024,17150,1107575554,4294940140,4294967294,17150,1107575554,4294940140,4294967294,17150,1107575554,4294940140,4294967294,
8256,8256,8448,8448,8640,8640,8832,8832,8832,8832,8832,8832,40960,44032,48128,51200,55296,58368,62464,65536,62464,65536,62464,65536,268439552,336991254,410,1881284608,80,7984,43008,1048526335,50465804,16780300,36,80,224,1110441984,1110441984,1110441984,1098907648,1098907648,0,130608,1073741824,1584,44032,44032,40960,696576,1073741824,8448,16940,1107575352,4294958956,4294967294,16940,1107575352,4294958956,4294967294,16940,1107575352,4294958956,4294967294,
17856,17856,18048,18048,18240,18240,18432,18432,18432,18432,18432,18432,40960,44032,48128,51200,55296,58368,62464,65536,62464,65536,62464,65536,268439552,336991254,410,2154438656,80,7984,43008,1048526463,50465804,16780300,36,80,224,1119617024,1119617024,1119617024,1098907648,1098907648,0,132408,1073741824,3384,44032,44032,40960,706176,1073741824,18048,17082,1107575488,4294947756,4294967294,17082,1107575488,4294947756,4294967294,17082,1107575488,4294947756,4294967294,
26304,26304,26496,26496,26688,26688,26880,26880,26880,26880,26880,26880,40960,44032,48128,51200,55296,58368,62464,65536,62464,65536,62464,65536,268439552,336991254,410,2427592704,80,7984,43008,1056915199,50465804,16780300,36,80,224,1124728832,1124728832,1124728832,1098907648,1098907648,0,133992,1073741824,4968,44032,44032,40960,714624,1073741824,26496,17161,1107575564,4294937900,4294967294,17161,1107575564,4294937900,4294967294,17161,1107575564,4294937900,4294967294,
35136,35136,35328,35328,35520,35520,35712,35712,35712,35712,35712,35712,40960,44032,48128,51200,55296,58368,62464,65536,62464,65536,62464,65536,268439552,336991254,410,2957123584,80,7984,43008,1056915071,50465804,16780300,36,80,224,1127743488,1127743488,1127743488,1098907648,1098907648,0,135648,1073741824,6624,44032,44032,40960,723456,1073741824,35328,17207,1107575610,4294927596,4294967294,17207,1107575610,4294927596,4294967294,17207,1107575610,4294927596,4294967294,
7440384,7440384,7483392,7483392,7526400,7526400,7569408,7569408,7569408,7569408,7569408,7569408,40960,44032,48128,51200,55296,58368,62464,65536,62464,65536,62464,65536,268439552,336991254,57,3230273536,80,7488,192,1056915199,16780300,50465804,576,36,224,1127088128,1127088128,1127088128,1098907648,1098907648,0,1403712,1073741824,1403136,44032,44032,40960,7486464,1073741824,7483392,17197,1107575600,4294929836,4294967294,17197,1107575600,4294929836,4294967294,17197,1107575600,4294929836,4294967294,
1290240,1333248,1376256,1419264,1462272,9504768,9547776,9590784,9590784,9590784,9590784,9590784,57344,61440,65536,69632,73728,40960,45056,49152,53248,53248,53248,53248,3503345872,353703189,57,3503555840,64,7488,192,1056915199,16780300,50465804,576,36,128,1130102784,1130233856,1107296256,1129316352,1129316352,1129316352,1781568,1797696,265536,32768,40960,61440,9501696,9587712,1416192,1129268051,1130316639,4294939052,206,1129268051,1130316639,4294939052,206,1129202160,1130316296,4294964012,206};
__device__ __forceinline__ float bf_lo(unsigned u) { return __uint_as_float(u << 16); }
__device__ __forceinline__ float bf_hi(unsigned u) { return __uint_as_float(u & 0xffff0000u); }
__device__ __forceinline__ unsigned cvt_pk(float lo, float hi) { bf16x2 r = {(__bf16)lo, (__bf16)hi}; return __builtin_bit_cast(unsigned, r); }
__device__ __forceinline__ float rb(float v) { return bf_lo(cvt_pk(v, 0.f)); }
__global__ __launch_bounds__(256) __attribute__((aligned(4096))) void dcn_kernel(const unsigned short* x_, const unsigned short* off_, const unsigned short* w_, unsigned short* out_, void* ws_) {
    __shared__ __attribute__((aligned(128))) unsigned char lds[163840];
    asm volatile("" ::"v"((__attribute__((address_space(3))) unsigned char*)lds));
    unsigned tid = threadIdx.x, bid = blockIdx.x;
    const __attribute__((address_space(4))) Args* ka = (const __attribute__((address_space(4))) Args*)__builtin_amdgcn_kernarg_segment_ptr();
    asm volatile(
        "s_load_dwordx8 s[48:55], %1, 0\n\t"
        "v_mov_b32 v156, %0\n\t"
        "s_mov_b32 s42, %2\n\t"
        "s_getpc_b64 s[44:45]\n\t"
        "s_and_b32 s44, s44, 0xffffff00\n\t"
        "9:\n\t"
        "s_mov_b32 s46, 99f-9b+3300\n\t"
        "s_mov_b32 s47, 0x20000\n\t"
        "v_lshlrev_b32 v160, 12, v156\n\t"
        "s_lshl_b32 s43, s42, 4\n\t"
        "s_and_b32 s43, s43, 3968\n\t"
        "s_mov_b32 m0, 161856\n\t"
        "v_cmp_gt_u32 vcc, 64, v156\n\t"
        "s_nop 1\n\t"
        "s_cbranch_vccz 10f\n\t"
        "buffer_load_dword v160, s[44:47], s43 offen lds\n\t"
        "10:\n\t"
        "s_mov_b64 s[64:65], %3\n\t"
        "v_and_b32 v157, 63, v156\n\t"
        "s_mov_b32 s66, 65536\n\t"
        "s_mov_b32 s67, 0x20000\n\t"
        "v_lshlrev_b32 v155, 2, v157\n\t"
        "s_lshl_b32 s70, s42, 8\n\t"
        "buffer_load_dword v155, v155, s[64:67], s70 offen\n\t"
        "v_lshrrev_b32 v73, 6, v156\n\t"
        "v_and_b32 v157, 63, v156\n\t"
        "v_readfirstlane_b32 s4, v73\n\t"
        "v_lshlrev_b32 v140, 4, v157\n\t"
        "s_mov_b32 s26, 165888\n\t"
        "s_mov_b32 s27, 0x20000\n\t"
        "v_xor_b32 v142, 32, v140\n\t"
        "v_and_b32 v73, 32, v157\n\t"
        "v_xor_b32 v141, v73, v140\n\t"
        "s_lshr_b32 s68, s4, 1\n\t"
        "s_mul_i32 s37, s4, 3072\n\t"
        "s_mul_i32 s36, s68, -61440\n\t"
        "s_add_u32 s36, s36, s37\n\t"
        "s_add_u32 s36, s36, 55296\n\t"
        "s_add_u32 s37, s37, 86016\n\t"
        "s_and_b32 s8, s42, 7\n\t"
        "s_lshl_b32 s8, s8, 5\n\t"
        "s_lshr_b32 s10, s42, 3\n\t"
        "s_add_u32 s10, s10, s8\n\t"
        "s_mul_i32 s8, s10, 2731\n\t"
        "s_sub_u32 s11, s10, 3\n\t"
        "s_mul_i32 s11, s11, 2850\n\t"
        "s_max_i32 s8, s8, s11\n\t"
        "s_lshr_b32 s8, s8, 16\n\t"
        "v_mul_u32_u24 v71, 171, v156\n\t"
        "v_lshrrev_b32 v71, 11, v71\n\t"
        "v_mul_u32_u24 v74, 12, v71\n\t"
        "v_sub_u32 v74, v156, v74\n\t"
        "s_mul_i32 s11, s8, 23\n\t"
        "s_sub_u32 s10, s10, s11\n\t"
        "s_min_u32 s11, s8, 3\n\t"
        "s_sub_u32 s10, s10, s11\n\t"
        "s_lshl_b32 s6, s8, 4\n\t"
        "s_add_u32 s6, s6, 48\n\t"
        "v_lshrrev_b32 v75, 1, v71\n\t"
        "v_and_b32 v75, 2, v75\n\t"
        "v_xor_b32 v74, v74, v75\n\t"
        "v_lshlrev_b32 v72, 4, v74\n\t"
        "s_lshl_b32 s8, s8, 1\n\t"
        "s_lshr_b32 s8, 1248319, s8\n\t"
        "s_and_b32 s8, s8, 3\n\t"
        "s_sub_u32 s8, s10, s8\n\t"
        "s_max_i32 s8, s8, 20\n\t"
        "s_lshl2_add_u32 s8, s10, s8\n\t"
        "s_lshl1_add_u32 s8, s8, -6\n\t"
        "v_add_u32 v73, -1, v71\n\t"
        "v_cmp_lt_u32 vcc, 17, v73\n\t"
        "v_mov_b32 v75, 0x7ff00000\n\t"
        "v_add3_u32 v161, v71, s6, -2\n\t"
        "v_cndmask_b32 v162, v72, v75, vcc\n\t"
        "s_movk_i32 s9, 192\n\t"
        "s_lshl_b32 s35, s4, 10\n\t"
        "s_mov_b32 s18, 9633792\n\t"
        "s_mov_b32 s19, 0x20000\n\t"
        "v_min_u32 v161, 223, v161\n\t"
        "s_mul_i32 s34, s8, 43008\n\t"
        "v_mad_u32_u24 v143, v161, s9, v162\n\t"
        "s_sub_u32 s34, s34, 43008\n\t"
        "s_lshr_b32 s12, s42, 3\n\t"
        "s_lshl_b32 s12, s12, 2\n\t"
        "s_add_u32 s12, s12, s4\n\t"
        "s_mul_i32 s12, s12, 1296\n\t"
        "s_and_b32 s12, s12, 0xffffff80\n\t"
        "v_min_u32 v164, 11, v157\n\t"
        "v_lshlrev_b32 v164, 7, v164\n\t"
        "s_mul_i32 s39, s68, 6144\n\t"
        "s_add_u32 s39, s39, 55296\n\t"
        "s_mul_i32 s38, s68, 159744\n\t"
        "s_add_u32 s38, s38, -104448\n\t"
        "s_mul_i32 s5, s68, -165888\n\t"
        "s_add_u32 s5, s5, 61440\n\t"
        "s_mov_b32 s22, 1806336\n\t"
        "s_mov_b32 s23, 0x20000\n\t"
        "s_mov_b32 s30, 9633792\n\t"
        "s_mov_b32 s31, 0x20000\n\t"
        "s_lshl_b32 s10, s4, 2\n\t"
        "s_add_u32 s10, s10, 161792\n\t"
        "v_mov_b32 v153, s10\n\t"
        "v_mov_b32 v182, 161792\n\t"
        "v_mov_b32 v76, 0\n\t"
        "v_mov_b32 v77, 0\n\t"
        "v_lshlrev_b32 v73, 3, v156\n\t"
        "v_add_u32 v73, 147456, v73\n\t"
        "s_add_u32 s60, s35, 153600\n\t"
        "v_mov_b32 v74, s60\n\t"
        "s_cmp_lt_u32 s4, 3\n\t"
        "s_cselect_b64 exec, -1, 0\n\t"
        "ds_write_b64 v73, v[76:77] offset:0\n\t"
        "ds_write_b64 v73, v[76:77] offset:2048\n\t"
        "ds_write_b64 v73, v[76:77] offset:4096\n\t"
        "s_mov_b64 exec, 1\n\t"
        "ds_write_b32 v153, v76 offset:16\n\t"
        "ds_write_b64 v74, v[76:77]\n\t"
        "s_mov_b64 exec, -1\n\t"
        "s_waitcnt lgkmcnt(0)\n\t"
        "s_mov_b64 s[24:25], s[52:53]\n\t"
        "s_mov_b64 s[16:17], s[48:49]\n\t"
        "s_add_u32 m0, s37, -36864\n\t"
        "s_nop 0\n\t"
        "buffer_load_dwordx4 v140, s[24:27], s36 offen offset:0 lds\n\t"
        "buffer_load_dwordx4 v141, s[24:27], s36 offen offset:1024 lds\n\t"
        "buffer_load_dwordx4 v142, s[24:27], s36 offen offset:2048 lds\n\t"
        "buffer_load_dword v165, v164, s[24:27], s12 offen\n\t"
        "s_add_u32 m0, s35, 8192\n\t"
        "s_add_u32 s43, s34, 86016\n\t"
        "buffer_load_dwordx4 v143, s[16:19], s43 offen  lds\n\t"
        "s_add_u32 m0, s35, 24576\n\t"
        "s_add_u32 s43, s34, 258048\n\t"
        "buffer_load_dwordx4 v143, s[16:19], s43 offen  lds\n\t"
        "s_mov_b64 s[20:21], s[50:51]\n\t"
        "s_mov_b64 s[28:29], s[54:55]\n\t"
        "s_mul_i32 s41, s8, 224\n\t"
        "s_add_u32 s41, s41, s6\n\t"
        "v_and_b32 v158, 15, v156\n\t"
        "s_and_b32 s49, s4, 1\n\t"
        "s_lshl_b32 s8, s49, 5\n\t"
        "v_xor_b32 v152, s8, v140\n\t"
        "s_mul_i32 s43, s4, 1536\n\t"
        "s_add_u32 m0, s43, 43008\n\t"
        "s_add_u32 s43, s43, 110592\n\t"
        "buffer_load_dwordx4 v152, s[24:27], s43 offen lds\n\t"
        "buffer_load_dwordx4 v152, s[24:27], s43 offen offset:512 lds\n\t"
        "s_add_u32 m0, s35, 12288\n\t"
        "s_add_u32 s43, s34, 129024\n\t"
        "buffer_load_dwordx4 v143, s[16:19], s43 offen  lds\n\t"
        "s_add_u32 m0, s35, 28672\n\t"
        "s_add_u32 s43, s34, 301056\n\t"
        "s_min_u32 s43, s43, 9590784\n\t"
        "buffer_load_dwordx4 v143, s[16:19], s43 offen  lds\n\t"
        "v_bfe_i32 v74, v156, 5, 1\n\t"
        "v_and_b32 v74, 7, v74\n\t"
        "v_xor_b32 v74, s4, v74\n\t"
        "v_mul_u32_u24 v68, 11, v74\n\t"
        "v_lshrrev_b32 v68, 5, v68\n\t"
        "v_mul_u32_u24 v75, 3, v68\n\t"
        "v_sub_u32 v69, v74, v75\n\t"
        "v_lshlrev_b32 v70, 2, v74\n\t"
        "v_mul_u32_u24 v74, 205, v156\n\t"
        "v_lshrrev_b32 v74, 14, v74\n\t"
        "v_min_u32 v74, 2, v74\n\t"
        "v_lshlrev_b32 v65, 2, v74\n\t"
        "v_mul_u32_u24 v74, 80, v74\n\t"
        "v_sub_u32 v66, v156, v74\n\t"
        "v_bfe_u32 v52, v156, 4, 1\n\t"
        "v_and_b32 v181, 48, v156\n\t"
        "s_mov_b64 s[56:57], 0\n\t"
        "s_mov_b32 s63, 0x435f0000\n\t"
        "s_mov_b32 s54, 0x7fff7fff\n\t"
        "v_bfe_u32 v159, v156, 4, 2\n\t"
        "v_mul_u32_u24 v62, 192, v158\n\t"
        "v_lshlrev_b32 v76, 3, v158\n\t"
        "v_and_b32 v77, 48, v76\n\t"
        "v_and_b32 v184, 24, v76\n\t"
        "s_mul_i32 s69, s68, 16384\n\t"
        "s_lshl_b32 s70, s49, 7\n\t"
        "s_lshl_b32 s71, s49, 5\n\t"
        "v_add_u32 v73, 8, v76\n\t"
        "v_and_b32 v73, 32, v73\n\t"
        "s_add_u32 s69, s69, 192\n\t"
        "v_xor_b32 v73, v181, v73\n\t"
        "v_add3_u32 v128, v73, v62, s69\n\t"
        "v_add_u32 v73, 16, v76\n\t"
        "v_and_b32 v73, 32, v73\n\t"
        "s_add_u32 s69, s69, 192\n\t"
        "v_xor_b32 v73, v181, v73\n\t"
        "v_add3_u32 v129, v73, v62, s69\n\t"
        "v_add_u32 v73, 24, v76\n\t"
        "v_and_b32 v73, 32, v73\n\t"
        "s_add_u32 s69, s69, 192\n\t"
        "v_xor_b32 v73, v181, v73\n\t"
        "v_add3_u32 v130, v73, v62, s69\n\t"
        "v_lshrrev_b32 v73, 2, v158\n\t"
        "v_lshl_add_u32 v74, v159, 3, v73\n\t"
        "v_mul_u32_u24 v74, 192, v74\n\t"
        "v_lshlrev_b32 v75, 1, v156\n\t"
        "v_and_b32 v75, 32, v75\n\t"
        "v_add3_u32 v74, v74, v75, v184\n\t"
        "v_add_u32 v74, 86016, v74\n\t"
        "v_xad_u32 v133, v74, s71, 64\n\t"
        "v_add_u32 v131, s70, v74\n\t"
        "v_xor_b32 v132, 32, v131\n\t"
        "v_subrev_u32 v138, 43008, v131\n\t"
        "v_subrev_u32 v139, 43008, v132\n\t"
        "v_subrev_u32 v149, 43008, v133\n\t"
        "v_lshrrev_b32 v74, 3, v157\n\t"
        "v_min_u32 v74, 2, v74\n\t"
        "v_lshlrev_b32 v81, 2, v74\n\t"
        "s_lshl_b32 s69, s4, 3\n\t"
        "v_and_or_b32 v82, v156, 7, s69\n\t"
        "v_mul_u32_u24 v80, 48, v82\n\t"
        "v_lshl_add_u32 v80, v74, 11, v80\n\t"
        "v_add_u32 v80, 147488, v80\n\t"
        "v_and_b32 v82, 15, v82\n\t"
        "v_and_b32 v74, 31, v156\n\t"
        "v_mad_u32_u24 v53, v74, 48, v70\n\t"
        "v_add_u32 v53, 147456, v53\n\t"
        "v_add_u32 v70, 157696, v70\n\t"
        "v_mul_u32_u24 v67, 320, v81\n\t"
        "s_add_u32 s69, s68, 1\n\t"
        "v_cvt_f32_u32 v56, s69\n\t"
        "v_add_u32 v57, 1, v82\n\t"
        "v_cvt_f32_u32 v57, v57\n\t"
        "v_lshrrev_b32 v83, 2, v157\n\t"
        "v_and_b32 v74, 3, v156\n\t"
        "v_lshlrev_b32 v119, 11, v83\n\t"
        "v_lshl_add_u32 v119, v74, 2, v119\n\t"
        "v_add_u32 v119, 122368, v119\n\t"
        "v_lshl_add_u32 v74, v74, 4, 64\n\t"
        "v_mad_u32_u24 v145, v83, s9, v74\n\t"
        "v_lshrrev_b32 v74, 1, v156\n\t"
        "v_and_or_b32 v74, v74, 16, v75\n\t"
        "v_add3_u32 v144, v74, v62, s70\n\t"
        "s_lshl_b32 s69, s68, 10\n\t"
        "s_add_u32 s69, s69, 135168\n\t"
        "v_add_u32 v74, v76, v159\n\t"
        "v_lshl_or_b32 v74, v74, 3, s71\n\t"
        "v_xad_u32 v146, v74, v77, s69\n\t"
        "s_lshl_b32 s70, s49, 11\n\t"
        "s_add_u32 s70, s70, s69\n\t"
        "v_lshlrev_b32 v74, 1, v156\n\t"
        "v_and_b32 v74, 48, v74\n\t"
        "v_xad_u32 v147, v74, v140, s70\n\t"
        "s_mul_i32 s48, s41, 36\n\t"
        "s_mul_i32 s41, s41, 192\n\t"
        "s_mul_i32 s69, s68, 172032\n\t"
        "s_add_u32 s41, s41, s69\n\t"
        "s_mul_i32 s69, s49, 43008\n\t"
        "s_add_u32 s50, s41, s69\n\t"
        "v_lshrrev_b32 v74, 5, v156\n\t"
        "v_mul_u32_u24 v74, 8064, v74\n\t"
        "v_and_b32 v55, 496, v140\n\t"
        "v_add_u32 v55, v55, v74\n\t"
        "v_lshl_add_u32 v61, v158, 2, v74\n\t"
        "s_mul_i32 s69, s68, 768\n\t"
        "s_add_u32 s69, s69, 147456\n\t"
        "v_mad_u32_u24 v187, v158, 48, s69\n\t"
        "v_lshl_add_u32 v74, v159, 3, v62\n\t"
        "v_xad_u32 v186, v74, v77, s37\n\t"
        "v_and_b32 v73, 2, v73\n\t"
        "v_add_u32 v73, v73, v159\n\t"
        "v_and_b32 v73, 3, v73\n\t"
        "v_and_or_b32 v73, v158, 12, v73\n\t"
        "s_add_u32 s69, s35, 157696\n\t"
        "v_lshl_add_u32 v185, v73, 4, s69\n\t"
        "v_add_u32 v118, s69, v140\n\t"
        "v_add_u32 v116, 1, v157\n\t"
        "v_mul_u32_u24 v74, 12288, v159\n\t"
        "v_add3_u32 v74, v74, v62, v77\n\t"
        "v_add_u32 v117, s37, v74\n\t"
        "v_lshlrev_b32 v73, 2, v158\n\t"
        "v_and_b32 v73, 16, v73\n\t"
        "s_mov_b32 s69, 0x0c0c0100\n\t"
        "v_alignbit_b32 v169, s69, s69, v73\n\t"
        "s_mov_b32 s69, 0x0c0c0302\n\t"
        "v_alignbit_b32 v171, s69, s69, v73\n\t"
        "v_mov_b32 v74, 0x0c0c0c0c\n\t"
        "s_mov_b32 s10, 2228241\n\t"
        "s_mov_b32 s11, 8912964\n\t"
        "v_cndmask_b32 v168, v74, v169, s[10:11]\n\t"
        "v_cndmask_b32 v170, v74, v171, s[10:11]\n\t"
        "s_mov_b32 s10, 2281718784\n\t"
        "s_mov_b32 s11, 570429696\n\t"
        "v_cndmask_b32 v169, v74, v169, s[10:11]\n\t"
        "v_cndmask_b32 v171, v74, v171, s[10:11]\n\t"
        "s_waitcnt vmcnt(10)\n\t"
        "v_readlane_b32 s7, v155, 27\n\t"
        "s_nop 1\n\t"
        "s_or_b32 s42, s42, s7\n\t"
        "s_bitcmp1_b32 s7, 8\n\t"
        "s_cbranch_scc1 20f\n\t"
        "s_bitcmp1_b32 s7, 9\n\t"
        "s_cbranch_scc1 21f\n\t"
        "ds_bpermute_b32 v64, v65, v155 offset:172\n\t"
        "s_add_u32 m0, s37, -24576\n\t"
        "s_add_u32 s36, s36, 6144\n\t"
        "buffer_load_dwordx4 v140, s[24:27], s36 offen offset:0 lds\n\t"
        "ds_bpermute_b32 v78, v81, v155 offset:148\n\t"
        "ds_bpermute_b32 v79, v81, v155 offset:160\n\t"
        "v_readlane_b32 s71, v155, 26\n\t"
        "v_readlane_b32 s10, v155, 29\n\t"
        "s_nop 0\n\t"
        "v_mul_u32_u24 v73, s71, v66\n\t"
        "v_lshrrev_b32 v73, 11, v73\n\t"
        "v_lshlrev_b32 v66, 4, v66\n\t"
        "v_mad_u32_u24 v73, v73, s10, v66\n\t"
        "s_waitcnt lgkmcnt(2)\n\t"
        "v_add_u32 v64, v64, v73\n\t"
        "buffer_load_dwordx4 v141, s[24:27], s36 offen offset:1024 lds\n\t"
        "v_readlane_b32 s69, v155, 34\n\t"
        "v_readlane_b32 s70, v155, 35\n\t"
        "s_nop 0\n\t"
        "v_mad_u32_u24 v70, v52, s69, v70\n\t"
        "v_mad_u32_u24 v70, v158, s70, v70\n\t"
        "s_mul_i32 s71, s69, s68\n\t"
        "s_add_u32 s71, s71, 157728\n\t"
        "v_mad_u32_u24 v67, v82, s70, v67\n\t"
        "v_add_u32 v67, s71, v67\n\t"
        "v_readlane_b32 s11, v155, 30\n\t"
        "buffer_load_dwordx4 v142, s[24:27], s36 offen offset:2048 lds\n\t"
        "s_movk_i32 s10, 223\n\t"
        "v_mov_b32 v75, 0x7ff00000\n\t"
        "v_readlane_b32 s12, v155, 24\n\t"
        "v_readlane_b32 s13, v155, 25\n\t"
        "s_nop 0\n\t"
        "s_bfe_u32 s70, s12, 524288\n\t"
        "s_bfe_u32 s71, s13, 524288\n\t"
        "v_cmp_gt_u32 vcc, s71, v71\n\t"
        "v_add3_u32 v73, v71, s70, -2\n\t"
        "v_med3_i32 v73, v73, 0, s10\n\t"
        "s_add_u32 m0, s35, 4096\n\t"
        "s_add_u32 s43, s34, 43008\n\t"
        "buffer_load_dwordx4 v143, s[16:19], s43 offen  lds\n\t"
        "v_cndmask_b32 v74, v75, v72, vcc\n\t"
        "v_mad_u32_u24 v134, v73, s11, v74\n\t"
        "s_bfe_u32 s70, s12, 524296\n\t"
        "s_bfe_u32 s71, s13, 524296\n\t"
        "v_cmp_gt_u32 vcc, s71, v71\n\t"
        "v_add3_u32 v73, v71, s70, -2\n\t"
        "v_med3_i32 v73, v73, 0, s10\n\t"
        "v_cndmask_b32 v74, v75, v72, vcc\n\t"
        "v_mad_u32_u24 v135, v73, s11, v74\n\t"
        "s_add_u32 m0, s35, 20480\n\t"
        "s_add_u32 s43, s34, 215040\n\t"
        "buffer_load_dwordx4 v143, s[16:19], s43 offen  lds\n\t"
        "s_bfe_u32 s70, s12, 524304\n\t"
        "s_bfe_u32 s71, s13, 524304\n\t"
        "v_cmp_gt_u32 vcc, s71, v71\n\t"
        "v_add3_u32 v73, v71, s70, -2\n\t"
        "v_med3_i32 v73, v73, 0, s10\n\t"
        "v_cndmask_b32 v74, v75, v72, vcc\n\t"
        "v_mad_u32_u24 v136, v73, s11, v74\n\t"
        "s_bfe_u32 s70, s12, 524312\n\t"
        "s_bfe_u32 s71, s13, 524312\n\t"
        "v_cmp_gt_u32 vcc, s71, v71\n\t"
        "v_add3_u32 v73, v71, s70, -2\n\t"
        "v_med3_i32 v73, v73, 0, s10\n\t"
        "v_cndmask_b32 v74, v75, v72, vcc\n\t"
        "v_mad_u32_u24 v137, v73, s11, v74\n\t"
        "s_mov_b64 exec, 0\n\t"
        "s_cmp_eq_u32 s4, 0\n\t"
        "s_cbranch_scc1 .LpM0%=\n\t"
        "s_cmp_eq_u32 s4, 1\n\t"
        "s_cbranch_scc1 .LpM1%=\n\t"
        "s_cmp_eq_u32 s4, 2\n\t"
        "s_cbranch_scc1 .LpM2%=\n\t"
        ".LrM%=:\n\t"
        "s_mov_b64 exec, -1\n\t"
        "s_waitcnt vmcnt(9)\n\t"
        "s_barrier\n\t"
        "ds_read_b64_tr_b16 a[0:1], v138 offset:6144\n\t"
        "ds_read_b64_tr_b16 a[2:3], v138 offset:6912\n\t"
        "ds_read_b64_tr_b16 a[4:5], v139 offset:6144\n\t"
        "ds_read_b64_tr_b16 a[6:7], v139 offset:6912\n\t"
        "ds_read_b64_tr_b16 a[8:9], v149 offset:6144\n\t"
        "ds_read_b64_tr_b16 a[10:11], v149 offset:6912\n\t"
        "ds_read_b128 v[84:87], v128 offset:8192\n\t"
        "ds_read_b64_tr_b16 a[12:13], v138 offset:12288\n\t"
        "ds_read_b64_tr_b16 a[14:15], v138 offset:13056\n\t"
        "ds_read_b64_tr_b16 a[16:17], v139 offset:12288\n\t"
        "ds_read_b64_tr_b16 a[18:19], v139 offset:13056\n\t"
        "ds_read_b64_tr_b16 a[20:21], v149 offset:12288\n\t"
        "ds_read_b64_tr_b16 a[22:23], v149 offset:13056\n\t"
        "ds_read_b128 v[88:91], v128 offset:8192\n\t"
        "s_add_u32 m0, s37, -12288\n\t"
        "s_add_u32 s36, s36, s39\n\t"
        "s_waitcnt lgkmcnt(7)\n\t"
        "v_mfma_f32_16x16x32_bf16 v[12:15], a[0:3], v[84:87], 0\n\t"
        "buffer_load_dwordx4 v140, s[24:27], s36 offen offset:0 lds\n\t"
        "buffer_load_dwordx4 v141, s[24:27], s36 offen offset:1024 lds\n\t"
        "buffer_load_dwordx4 v142, s[24:27], s36 offen offset:2048 lds\n\t"
        "s_add_u32 m0, s35, 16384\n\t"
        "s_add_u32 s43, s34, 172032\n\t"
        "buffer_load_dwordx4 v143, s[16:19], s43 offen  lds\n\t"
        "s_add_u32 m0, s35, 32768\n\t"
        "s_add_u32 s43, s34, 344064\n\t"
        "s_min_u32 s43, s43, 9590784\n\t"
        "buffer_load_dwordx4 v143, s[16:19], s43 offen  lds\n\t"
        "s_waitcnt vmcnt(10)\n\t"
        "s_barrier\n\t"
        "ds_read_b128 v[92:95], v128 offset:12288\n\t"
        "s_add_u32 m0, s37, 0\n\t"
        "ds_read_b64_tr_b16 a[24:25], v138 offset:0\n\t"
        "s_add_u32 s36, s36, s38\n\t"
        "v_mfma_f32_16x16x32_bf16 v[16:19], a[4:7], v[84:87], 0\n\t"
        "buffer_load_dwordx4 v140, s[24:27], s36 offen offset:0 lds\n\t"
        "ds_read_b64_tr_b16 a[26:27], v138 offset:768\n\t"
        "v_readlane_b32 s44, v155, 32\n\t"
        "v_mfma_f32_16x16x32_bf16 v[20:23], a[8:11], v[84:87], 0\n\t"
        "buffer_load_dwordx4 v141, s[24:27], s36 offen offset:1024 lds\n\t"
        "ds_read_b64_tr_b16 a[28:29], v139 offset:0\n\t"
        "v_readlane_b32 s45, v155, 33\n\t"
        "s_waitcnt lgkmcnt(4)\n\t"
        "v_mfma_f32_16x16x32_bf16 v[24:27], a[12:15], v[88:91], 0\n\t"
        "ds_read_b64_tr_b16 a[30:31], v139 offset:768\n\t"
        "ds_read_b64_tr_b16 a[32:33], v149 offset:0\n\t"
        "ds_read_b64_tr_b16 a[34:35], v149 offset:768\n\t"
        "v_readlane_b32 s62, v155, 36\n\t"
        "v_mfma_f32_16x16x32_bf16 v[28:31], a[16:19], v[88:91], 0\n\t"
        "buffer_load_dwordx4 v142, s[24:27], s36 offen offset:2048 lds\n\t"
        "s_add_u32 m0, s35, 157696\n\t"
        "ds_read_b128 v[96:99], v128 offset:8192\n\t"
        "v_mfma_f32_16x16x32_bf16 v[32:35], a[20:23], v[88:91], 0\n\t"
        "ds_read_b128 v[100:103], v128 offset:12288\n\t"
        "s_lshl_b32 s70, s62, 5\n\t"
        "s_waitcnt lgkmcnt(8)\n\t"
        "v_mfma_f32_16x16x32_bf16 v[24:27], a[0:3], v[92:95], v[24:27]\n\t"
        "s_sub_u32 s71, s70, 16384\n\t"
        "s_mul_i32 s71, s71, s68\n\t"
        "v_mfma_f32_16x16x32_bf16 v[28:31], a[4:7], v[92:95], v[28:31]\n\t"
        "buffer_load_dwordx4 v64, s[20:23], 0 offen lds\n\t"
        "s_add_u32 m0, s37, 12288\n\t"
        "s_add_u32 s36, s36, s5\n\t"
        "v_mfma_f32_16x16x32_bf16 v[32:35], a[8:11], v[92:95], v[32:35]\n\t"
        "s_mov_b32 exec_lo, 0\n\t"
        "s_mov_b32 exec_hi, 0xfff00000\n\t"
        "ds_write_b32 v119, v155\n\t"
        "s_mov_b64 exec, -1\n\t"
        "v_mfma_f32_16x16x32_bf16 v[36:39], a[12:15], v[92:95], 0\n\t"
        "v_add_u32 v172, s71, v128\n\t"
        "v_add_u32 v173, s71, v129\n\t"
        "v_mfma_f32_16x16x32_bf16 v[40:43], a[16:19], v[92:95], 0\n\t"
        "buffer_load_dwordx4 v140, s[24:27], s36 offen offset:0 lds\n\t"
        "v_add_u32 v174, s71, v130\n\t"
        "v_mfma_f32_16x16x32_bf16 v[44:47], a[20:23], v[92:95], 0\n\t"
        "s_waitcnt vmcnt(10) lgkmcnt(3)\n\t"
        "s_barrier\n\t"
        "ds_read_b128 v[104:107], v128 offset:4096\n\t"
        "ds_read_b64_tr_b16 a[36:37], v138 offset:18432\n\t"
        "s_waitcnt lgkmcnt(4)\n\t"
        "v_mfma_f32_16x16x32_bf16 v[0:3], a[24:27], v[96:99], 0\n\t"
        "ds_read_b64_tr_b16 a[38:39], v138 offset:19200\n\t"
        "ds_read_b64_tr_b16 a[40:41], v139 offset:18432\n\t"
        "s_add_u32 s71, s71, s70\n\t"
        "v_mfma_f32_16x16x32_bf16 v[4:7], a[28:31], v[96:99], 0\n\t"
        "buffer_load_dwordx4 v141, s[24:27], s36 offen offset:1024 lds\n\t"
        "ds_read_b64_tr_b16 a[42:43], v139 offset:19200\n\t"
        "v_add_u32 v175, s71, v128\n\t"
        "v_mfma_f32_16x16x32_bf16 v[8:11], a[32:35], v[96:99], 0\n\t"
        "ds_read_b64_tr_b16 a[44:45], v149 offset:18432\n\t"
        "ds_read_b64_tr_b16 a[46:47], v149 offset:19200\n\t"
        "v_add_u32 v176, s71, v129\n\t"
        "s_waitcnt lgkmcnt(8)\n\t"
        "v_mfma_f32_16x16x32_bf16 v[12:15], a[24:27], v[100:103], v[12:15]\n\t"
        "ds_read_b64_tr_b16 a[48:49], v138 offset:24576\n\t"
        "ds_read_b64_tr_b16 a[50:51], v138 offset:25344\n\t"
        "ds_read_b64_tr_b16 a[52:53], v139 offset:24576\n\t"
        "v_add_u32 v177, s71, v130\n\t"
        "v_mfma_f32_16x16x32_bf16 v[16:19], a[28:31], v[100:103], v[16:19]\n\t"
        "buffer_load_dwordx4 v142, s[24:27], s36 offen offset:2048 lds\n\t"
        "s_add_u32 m0, s35, 0\n\t"
        "ds_read_b64_tr_b16 a[54:55], v139 offset:25344\n\t"
        "s_add_u32 s43, s34, 0\n\t"
        "ds_read_b64_tr_b16 a[56:57], v149 offset:24576\n\t"
        "v_mfma_f32_16x16x32_bf16 v[20:23], a[32:35], v[100:103], v[20:23]\n\t"
        "ds_read_b64_tr_b16 a[58:59], v149 offset:25344\n\t"
        "ds_read_b128 v[108:111], v128 offset:4160\n\t"
        "s_add_u32 s71, s71, s70\n\t"
        "s_waitcnt lgkmcnt(12)\n\t"
        "v_mfma_f32_16x16x32_bf16 v[0:3], a[0:3], v[104:107], v[0:3]\n\t"
        "buffer_load_dwordx4 v143, s[16:19], s43 offen  lds\n\t"
        "s_add_u32 m0, s35, 36864\n\t"
        "s_add_u32 s43, s34, 387072\n\t"
        "s_min_u32 s43, s43, 9590784\n\t"
        "v_mfma_f32_16x16x32_bf16 v[4:7], a[4:7], v[104:107], v[4:7]\n\t"
        "v_add_u32 v178, s71, v128\n\t"
        "v_add_u32 v179, s71, v129\n\t"
        "v_mfma_f32_16x16x32_bf16 v[8:11], a[8:11], v[104:107], v[8:11]\n\t"
        "s_cmp_eq_u32 s69, 36\n\t"
        "s_cselect_b64 exec, -1, 0\n\t"
        "v_swap_b32 v68, v69\n\t"
        "v_swap_b32 v173, v175\n\t"
        "v_swap_b32 v174, v178\n\t"
        "v_swap_b32 v177, v179\n\t"
        "s_mov_b64 exec, -1\n\t"
        "v_mfma_f32_16x16x32_bf16 v[12:15], a[12:15], v[104:107], v[12:15]\n\t"
        "v_add_u32 v180, s71, v130\n\t"
        "v_add3_u32 v68, v68, v52, -1\n\t"
        "v_mfma_f32_16x16x32_bf16 v[16:19], a[16:19], v[104:107], v[16:19]\n\t"
        "v_add3_u32 v69, v69, v158, -1\n\t"
        "v_cvt_f32_i32 v68, v68\n\t"
        "v_mfma_f32_16x16x32_bf16 v[20:23], a[20:23], v[104:107], v[20:23]\n\t"
        "v_cvt_f32_i32 v69, v69\n\t"
        "v_add_f32 v78, v78, v56\n\t"
        "s_waitcnt lgkmcnt(0)\n\t"
        "v_mfma_f32_16x16x32_bf16 v[0:3], a[36:39], v[108:111], v[0:3]\n\t"
        "ds_read_b128 v[112:115], v128 offset:8256\n\t"
        "v_add_f32 v79, v79, v57\n\t"
        "v_mfma_f32_16x16x32_bf16 v[4:7], a[40:43], v[108:111], v[4:7]\n\t"
        "v_readlane_b32 s69, v155, 31\n\t"
        "v_mfma_f32_16x16x32_bf16 v[8:11], a[44:47], v[108:111], v[8:11]\n\t"
        "s_nop 0\n\t"
        "v_mov_b32 v154, s69\n\t"
        "v_mov_b32 v119, s62\n\t"
        "v_mfma_f32_16x16x32_bf16 v[12:15], a[48:51], v[108:111], v[12:15]\n\t"
        "v_readlane_b32 s53, v155, 28\n\t"
        "v_readlane_b32 s9, v155, 46\n\t"
        "v_mfma_f32_16x16x32_bf16 v[16:19], a[52:55], v[108:111], v[16:19]\n\t"
        "s_sub_u32 s10, 43200, s11\n\t"
        "s_mul_i32 s10, s10, s68\n\t"
        "s_sub_u32 s11, s11, 192\n\t"
        "v_mfma_f32_16x16x32_bf16 v[20:23], a[56:59], v[108:111], v[20:23]\n\t"
        "v_mad_u32_u24 v148, v158, s11, v144\n\t"
        "v_add_u32 v148, s10, v148\n\t"
        "s_waitcnt lgkmcnt(0)\n\t"
        "v_mfma_f32_16x16x32_bf16 v[12:15], a[36:39], v[112:115], v[12:15]\n\t"
        "ds_read_b128 v[84:87], v128 offset:12352\n\t"
        "v_mad_u32_u24 v150, v83, s11, v145\n\t"
        "v_mfma_f32_16x16x32_bf16 v[16:19], a[40:43], v[112:115], v[16:19]\n\t"
        "s_lshl_b32 s70, s49, 1\n\t"
        "s_add_u32 s70, s70, 49\n\t"
        "v_mfma_f32_16x16x32_bf16 v[20:23], a[44:47], v[112:115], v[20:23]\n\t"
        "v_readlane_b32 s52, v155, s70\n\t"
        "v_mfma_f32_16x16x32_bf16 v[24:27], a[48:51], v[112:115], v[24:27]\n\t"
        "buffer_load_dwordx4 v143, s[16:19], s43 offen  lds\n\t"
        "s_add_u32 m0, s37, 24576\n\t"
        "s_add_u32 s36, s36, s39\n\t"
        "v_mfma_f32_16x16x32_bf16 v[28:31], a[52:55], v[112:115], v[28:31]\n\t"
        "s_add_u32 s52, s52, s10\n\t"
        "v_mfma_f32_16x16x32_bf16 v[32:35], a[56:59], v[112:115], v[32:35]\n\t"
        "buffer_load_dwordx4 v140, s[24:27], s36 offen offset:0 lds\n\t"
        "s_waitcnt vmcnt(10)\n\t"
        "s_barrier\n\t"
        "ds_read_b128 v[88:91], v128 offset:16384\n\t"
        "ds_read_b64_tr_b16 a[60:61], v138 offset:30720\n\t"
        "s_waitcnt lgkmcnt(2)\n\t"
        "v_mfma_f32_16x16x32_bf16 v[24:27], a[36:39], v[84:87], v[24:27]\n\t"
        "buffer_load_dwordx4 v141, s[24:27], s36 offen offset:1024 lds\n\t"
        "ds_read_b64_tr_b16 a[62:63], v138 offset:31488\n\t"
        "v_mfma_f32_16x16x32_bf16 v[28:31], a[40:43], v[84:87], v[28:31]\n\t"
        "ds_read_b64_tr_b16 a[64:65], v139 offset:30720\n\t"
        "ds_read_b64_tr_b16 a[66:67], v139 offset:31488\n\t"
        "v_mfma_f32_16x16x32_bf16 v[32:35], a[44:47], v[84:87], v[32:35]\n\t"
        "ds_read_b64_tr_b16 a[68:69], v149 offset:30720\n\t"
        "ds_read_b64_tr_b16 a[70:71], v149 offset:31488\n\t"
        "v_mfma_f32_16x16x32_bf16 v[36:39], a[48:51], v[84:87], v[36:39]\n\t"
        "buffer_load_dwordx4 v142, s[24:27], s36 offen offset:2048 lds\n\t"
        "ds_read_b128 v[92:95], v128 offset:8256\n\t"
        "v_mfma_f32_16x16x32_bf16 v[40:43], a[52:55], v[84:87], v[40:43]\n\t"
        "ds_read_b128 v[96:99], v128 offset:12352\n\t"
        "ds_read_b128 v[100:103], v128 offset:16448\n\t"
        "v_mfma_f32_16x16x32_bf16 v[44:47], a[56:59], v[84:87], v[44:47]\n\t"
        "ds_read_b64_tr_b16 a[72:73], v138 offset:36864\n\t"
        "ds_read_b64_tr_b16 a[74:75], v138 offset:37632\n\t"
        "s_waitcnt lgkmcnt(11)\n\t"
        "v_mfma_f32_16x16x32_bf16 v[24:27], a[24:27], v[88:91], v[24:27]\n\t"
        "ds_read_b64_tr_b16 a[76:77], v139 offset:36864\n\t"
        "ds_read_b64_tr_b16 a[78:79], v139 offset:37632\n\t"
        "v_mfma_f32_16x16x32_bf16 v[28:31], a[28:31], v[88:91], v[28:31]\n\t"
        "buffer_load_dwordx4 v[48:51], v55, s[20:23], s48 offen\n\t"
        "ds_read_b64_tr_b16 a[80:81], v149 offset:36864\n\t"
        "v_mfma_f32_16x16x32_bf16 v[32:35], a[32:35], v[88:91], v[32:35]\n\t"
        "ds_read_b64_tr_b16 a[82:83], v149 offset:37632\n\t"
        "v_mfma_f32_16x16x32_bf16 v[36:39], a[0:3], v[88:91], v[36:39]\n\t"
        "v_mfma_f32_16x16x32_bf16 v[40:43], a[4:7], v[88:91], v[40:43]\n\t"
        "v_mfma_f32_16x16x32_bf16 v[44:47], a[8:11], v[88:91], v[44:47]\n\t"
        "buffer_load_dword v61, v61, s[20:23], s48 offen offset:512\n\t"
        "s_add_u32 m0, s37, 36864\n\t"
        "s_waitcnt lgkmcnt(8)\n\t"
        "v_mfma_f32_16x16x32_bf16 v[0:3], a[60:63], v[92:95], v[0:3]\n\t"
        "s_add_u32 s36, s36, s38\n\t"
        "v_mfma_f32_16x16x32_bf16 v[4:7], a[64:67], v[92:95], v[4:7]\n\t"
        "s_waitcnt vmcnt(10)\n\t"
        "s_barrier\n\t"
        "ds_read_b64_tr_b16 a[84:85], v131 offset:0\n\t"
        "ds_read_b64_tr_b16 a[86:87], v131 offset:768\n\t"
        "v_mfma_f32_16x16x32_bf16 v[8:11], a[68:71], v[92:95], v[8:11]\n\t"
        "ds_read_b64_tr_b16 a[88:89], v132 offset:0\n\t"
        "ds_read_b64_tr_b16 a[90:91], v132 offset:768\n\t"
        "v_readlane_b32 s46, v155, 37\n\t"
        "s_waitcnt lgkmcnt(6)\n\t"
        "v_mfma_f32_16x16x32_bf16 v[12:15], a[60:63], v[96:99], v[12:15]\n\t"
        "buffer_load_dwordx4 v140, s[24:27], s36 offen offset:0 lds\n\t"
        "ds_read_b64_tr_b16 a[92:93], v133 offset:0\n\t"
        "ds_read_b64_tr_b16 a[94:95], v133 offset:768\n\t"
        "v_readlane_b32 s47, v155, 40\n\t"
        "v_mfma_f32_16x16x32_bf16 v[16:19], a[64:67], v[96:99], v[16:19]\n\t"
        "ds_read_b128 v[104:107], v128 offset:4224\n\t"
        "ds_read_b64_tr_b16 a[96:97], v131 offset:6144\n\t"
        "v_add_f32 v58, s46, v68\n\t"
        "v_mfma_f32_16x16x32_bf16 v[20:23], a[68:71], v[96:99], v[20:23]\n\t"
        "ds_read_b64_tr_b16 a[98:99], v131 offset:6912\n\t"
        "ds_read_b64_tr_b16 a[100:101], v132 offset:6144\n\t"
        "v_add_f32 v59, s47, v69\n\t"
        "v_mfma_f32_16x16x32_bf16 v[24:27], a[60:63], v[100:103], v[24:27]\n\t"
        "ds_read_b64_tr_b16 a[102:103], v132 offset:6912\n\t"
        "ds_read_b64_tr_b16 a[104:105], v133 offset:6144\n\t"
        "v_mfma_f32_16x16x32_bf16 v[28:31], a[64:67], v[100:103], v[28:31]\n\t"
        "buffer_load_dwordx4 v141, s[24:27], s36 offen offset:1024 lds\n\t"
        "ds_read_b64_tr_b16 a[106:107], v133 offset:6912\n\t"
        "v_mfma_f32_16x16x32_bf16 v[32:35], a[68:71], v[100:103], v[32:35]\n\t"
        "ds_read_b128 v[108:111], v128 offset:8320\n\t"
        "v_mfma_f32_16x16x32_bf16 v[36:39], a[36:39], v[100:103], v[36:39]\n\t"
        "ds_read_b32 v64, v70 offset:0\n\t"
        "v_mfma_f32_16x16x32_bf16 v[40:43], a[40:43], v[100:103], v[40:43]\n\t"
        "ds_read_b32 v67, v67 offset:0\n\t"
        "v_mfma_f32_16x16x32_bf16 v[44:47], a[44:47], v[100:103], v[44:47]\n\t"
        "buffer_load_dwordx4 v142, s[24:27], s36 offen offset:2048 lds\n\t"
        "v_readlane_b32 s40, v155, 12\n\t"
        "v_readlane_b32 s43, v155, 0\n\t"
        "s_waitcnt lgkmcnt(9)\n\t"
        "v_mfma_f32_16x16x32_bf16 v[0:3], a[72:75], v[104:107], v[0:3]\n\t"
        "ds_read_b128 v[112:115], v128 offset:12416\n\t"
        "s_add_u32 m0, s40, s35\n\t"
        "v_mfma_f32_16x16x32_bf16 v[4:7], a[76:79], v[104:107], v[4:7]\n\t"
        "ds_read_b32 v66, v70 offset:2560\n\t"
        "v_mfma_f32_16x16x32_bf16 v[8:11], a[80:83], v[104:107], v[8:11]\n\t"
        "v_mfma_f32_16x16x32_bf16 v[12:15], a[84:87], v[104:107], v[12:15]\n\t"
        "ds_read_b128 v[84:87], v128 offset:16512\n\t"
        "v_mfma_f32_16x16x32_bf16 v[16:19], a[88:91], v[104:107], v[16:19]\n\t"
        "v_mfma_f32_16x16x32_bf16 v[20:23], a[92:95], v[104:107], v[20:23]\n\t"
        "s_waitcnt lgkmcnt(5)\n\t"
        "v_mfma_f32_16x16x32_bf16 v[0:3], a[96:99], v[108:111], v[0:3]\n\t"
        "s_waitcnt lgkmcnt(4)\n\t"
        ".LpM0%=:\n\t"
        "s_cbranch_execz .LrM%=\n\t"
        "v_perm_b32 v56, v64, v64, s44\n\t"
        "v_perm_b32 v57, v64, v64, s45\n\t"
        "v_mfma_f32_16x16x32_bf16 v[4:7], a[100:103], v[108:111], v[4:7]\n\t"
        "v_add_f32 v56, v56, v58\n\t"
        "v_add_f32 v57, v57, v59\n\t"
        "v_mfma_f32_16x16x32_bf16 v[8:11], a[104:107], v[108:111], v[8:11]\n\t"
        "v_cvt_pk_bf16_f32 v52, v56, v57\n\t"
        "v_cvt_pk_bf16_f32 v58, v58, v59\n\t"
        "v_mfma_f32_16x16x32_bf16 v[12:15], a[72:75], v[108:111], v[12:15]\n\t"
        "v_cmp_ne_u32 s[64:65], v52, v58\n\t"
        "s_waitcnt lgkmcnt(3)\n\t"
        "v_perm_b32 v57, v67, v67, s45\n\t"
        "v_add_f32 v57, v57, v79\n\t"
        "v_mfma_f32_16x16x32_bf16 v[16:19], a[76:79], v[108:111], v[16:19]\n\t"
        "v_mbcnt_lo_u32_b32 v56, s64, 1\n\t"
        "v_mbcnt_hi_u32_b32 v56, s65, v56\n\t"
        "v_mfma_f32_16x16x32_bf16 v[20:23], a[80:83], v[108:111], v[20:23]\n\t"
        "s_bcnt1_i32_b64 s61, s[64:65]\n\t"
        "v_add_u32 v54, s61, 1\n\t"
        "v_mfma_f32_16x16x32_bf16 v[24:27], a[84:87], v[108:111], v[24:27]\n\t"
        "v_lshl_add_u32 v56, v56, 3, s60\n\t"
        "v_cvt_pk_bf16_f32 v58, v78, v79\n\t"
        "v_mfma_f32_16x16x32_bf16 v[28:31], a[88:91], v[108:111], v[28:31]\n\t"
        "s_mov_b64 exec, s[64:65]\n\t"
        "ds_write_b64 v56, v[52:53]\n\t"
        "s_mov_b64 exec, -1\n\t"
        "v_mfma_f32_16x16x32_bf16 v[32:35], a[92:95], v[108:111], v[32:35]\n\t"
        "v_swap_b32 v53, v80\n\t"
        "v_perm_b32 v56, v67, v67, s44\n\t"
        "s_waitcnt lgkmcnt(1)\n\t"
        "v_mfma_f32_16x16x32_bf16 v[12:15], a[96:99], v[112:115], v[12:15]\n\t"
        "v_add_f32 v56, v56, v78\n\t"
        "v_cvt_pk_bf16_f32 v52, v56, v57\n\t"
        "v_mfma_f32_16x16x32_bf16 v[16:19], a[100:103], v[112:115], v[16:19]\n\t"
        "v_cmp_ne_u32 s[64:65], v52, v58\n\t"
        "v_readlane_b32 s47, v155, 42\n\t"
        "v_mfma_f32_16x16x32_bf16 v[20:23], a[104:107], v[112:115], v[20:23]\n\t"
        "s_and_b64 s[64:65], s[64:65], 0xffffff\n\t"
        "v_mbcnt_lo_u32_b32 v56, s64, v54\n\t"
        "v_mfma_f32_16x16x32_bf16 v[24:27], a[72:75], v[112:115], v[24:27]\n\t"
        "s_bcnt1_i32_b64 s46, s[64:65]\n\t"
        "s_add_u32 s61, s61, s46\n\t"
        "v_mfma_f32_16x16x32_bf16 v[28:31], a[76:79], v[112:115], v[28:31]\n\t"
        "v_add_u32 v54, s46, v54\n\t"
        "s_mov_b32 s3, s61\n\t"
        "v_mfma_f32_16x16x32_bf16 v[32:35], a[80:83], v[112:115], v[32:35]\n\t"
        "v_lshl_add_u32 v56, v56, 3, s60\n\t"
        "v_readlane_b32 s46, v155, 39\n\t"
        "v_mfma_f32_16x16x32_bf16 v[36:39], a[84:87], v[112:115], v[36:39]\n\t"
        "s_mov_b64 exec, s[64:65]\n\t"
        "ds_write_b64 v56, v[52:53]\n\t"
        "s_mov_b64 exec, -1\n\t"
        "v_mfma_f32_16x16x32_bf16 v[40:43], a[88:91], v[112:115], v[40:43]\n\t"
        "v_add_u32 v53, 4096, v80\n\t"
        "v_perm_b32 v56, v66, v66, s44\n\t"
        "v_mfma_f32_16x16x32_bf16 v[44:47], a[92:95], v[112:115], v[44:47]\n\t"
        "s_waitcnt vmcnt(8)\n\t"
        "s_barrier\n\t"
        "buffer_load_dwordx4 v134, s[16:19], s43 offen  lds\n\t"
        "ds_read_b128 v[88:91], v128 offset:0\n\t"
        "v_mfma_f32_16x16x32_bf16 v[24:27], a[96:99], v[84:87], v[24:27]\n\t"
        "ds_read_b128 v[92:95], v128 offset:64\n\t"
        "ds_read_b128 v[96:99], v128 offset:128\n\t"
        "v_readlane_b32 s40, v155, 13\n\t"
        "v_mfma_f32_16x16x32_bf16 v[28:31], a[100:103], v[84:87], v[28:31]\n\t"
        "ds_read_b128 v[100:103], v128 offset:20480\n\t"
        "ds_read_b128 v[104:107], v128 offset:20544\n\t"
        "v_readlane_b32 s43, v155, 1\n\t"
        "v_mfma_f32_16x16x32_bf16 v[32:35], a[104:107], v[84:87], v[32:35]\n\t"
        "ds_read_b128 v[108:111], v128 offset:20608\n\t"
        "s_add_u32 m0, s40, s35\n\t"
        "ds_read_b64_tr_b16 a[108:109], v131 offset:12288\n\t"
        "v_mfma_f32_16x16x32_bf16 v[36:39], a[72:75], v[84:87], v[36:39]\n\t"
        "buffer_load_dwordx4 v135, s[16:19], s43 offen  lds\n\t"
        "s_add_u32 m0, s37, 49152\n\t"
        "ds_read_b64_tr_b16 a[110:111], v131 offset:13056\n\t"
        "v_mfma_f32_16x16x32_bf16 v[40:43], a[76:79], v[84:87], v[40:43]\n\t"
        "ds_read_b64_tr_b16 a[112:113], v132 offset:12288\n\t"
        "s_add_u32 s36, s36, s5\n\t"
        "ds_read_b64_tr_b16 a[114:115], v132 offset:13056\n\t"
        "v_mfma_f32_16x16x32_bf16 v[44:47], a[80:83], v[84:87], v[44:47]\n\t"
        "ds_read_b64_tr_b16 a[116:117], v133 offset:12288\n\t"
        "ds_read_b64_tr_b16 a[118:119], v133 offset:13056\n\t"
        "v_perm_b32 v57, v66, v66, s45\n\t"
        "s_waitcnt lgkmcnt(11)\n\t"
        "v_mfma_f32_16x16x32_bf16 v[0:3], a[12:15], v[88:91], v[0:3]\n\t"
        "ds_read_b64_tr_b16 a[120:121], v131 offset:18432\n\t"
        "ds_read_b64_tr_b16 a[122:123], v131 offset:19200\n\t"
        "v_add_f32 v58, s46, v68\n\t"
        "v_mfma_f32_16x16x32_bf16 v[4:7], a[16:19], v[88:91], v[4:7]\n\t"
        "buffer_load_dwordx4 v140, s[24:27], s36 offen offset:0 lds\n\t"
        "ds_read_b64_tr_b16 a[124:125], v132 offset:18432\n\t"
        "v_add_f32 v59, s47, v69\n\t"
        "v_mfma_f32_16x16x32_bf16 v[8:11], a[20:23], v[88:91], v[8:11]\n\t"
        "ds_read_b64_tr_b16 a[126:127], v132 offset:19200\n\t"
        "ds_read_b64_tr_b16 a[128:129], v133 offset:18432\n\t"
        "v_add_f32 v56, v56, v58\n\t"
        "s_waitcnt lgkmcnt(12)\n\t"
        "v_mfma_f32_16x16x32_bf16 v[0:3], a[48:51], v[92:95], v[0:3]\n\t"
        "ds_read_b64_tr_b16 a[130:131], v133 offset:19200\n\t"
        "v_add_f32 v57, v57, v59\n\t"
        "v_mfma_f32_16x16x32_bf16 v[4:7], a[52:55], v[92:95], v[4:7]\n\t"
        "ds_read_b128 v[112:115], v129 offset:0\n\t"
        "v_max_f32 v56, 0, v56\n\t"
        "v_mfma_f32_16x16x32_bf16 v[8:11], a[56:59], v[92:95], v[8:11]\n\t"
        "buffer_load_dwordx4 v141, s[24:27], s36 offen offset:1024 lds\n\t"
        "v_max_f32 v57, 0, v57\n\t"
        "v_mfma_f32_16x16x32_bf16 v[0:3], a[84:87], v[96:99], v[0:3]\n\t"
        "ds_read_b128 v[84:87], v129 offset:4096\n\t"
        "v_max_f32 v58, 0, v58\n\t"
        "v_mfma_f32_16x16x32_bf16 v[4:7], a[88:91], v[96:99], v[4:7]\n\t"
        "v_max_f32 v59, 0, v59\n\t"
        "v_cvt_pk_bf16_f32 v52, v56, v57\n\t"
        "v_mfma_f32_16x16x32_bf16 v[8:11], a[92:95], v[96:99], v[8:11]\n\t"
        "v_cvt_pk_bf16_f32 v58, v58, v59\n\t"
        "v_cmp_ne_u32 s[64:65], v52, v58\n\t"
        "v_mfma_f32_16x16x32_bf16 v[36:39], a[24:27], v[100:103], v[36:39]\n\t"
        "buffer_load_dwordx4 v142, s[24:27], s36 offen offset:2048 lds\n\t"
        "v_readlane_b32 s40, v155, 14\n\t"
        "v_mfma_f32_16x16x32_bf16 v[40:43], a[28:31], v[100:103], v[40:43]\n\t"
        "v_readlane_b32 s43, v155, 2\n\t"
        "s_add_u32 m0, s40, s35\n\t"
        "v_mfma_f32_16x16x32_bf16 v[44:47], a[32:35], v[100:103], v[44:47]\n\t"
        "s_waitcnt vmcnt(10) lgkmcnt(2)\n\t"
        "s_barrier\n\t"
        "ds_read_b64_tr_b16 a[132:133], v131 offset:24576\n\t"
        "ds_read_b64_tr_b16 a[134:135], v131 offset:25344\n\t"
        "v_mfma_f32_16x16x32_bf16 v[36:39], a[60:63], v[104:107], v[36:39]\n\t"
        "ds_read_b64_tr_b16 a[136:137], v132 offset:24576\n\t"
        "ds_read_b64_tr_b16 a[138:139], v132 offset:25344\n\t"
        "v_mbcnt_lo_u32_b32 v56, s64, v54\n\t"
        "v_mfma_f32_16x16x32_bf16 v[40:43], a[64:67], v[104:107], v[40:43]\n\t"
        "buffer_load_dwordx4 v134, s[16:19], s43 offen  lds\n\t"
        "ds_read_b64_tr_b16 a[140:141], v133 offset:24576\n\t"
        "s_bitcmp1_b32 s42, 10\n\t"
        "s_cbranch_scc1 .Lx40%=\n\t"
        ".Lx41%=:\n\t"
        "v_mfma_f32_16x16x32_bf16 v[44:47], a[68:71], v[104:107], v[44:47]\n\t"
        "ds_read_b64_tr_b16 a[142:143], v133 offset:25344\n\t"
        "ds_read_b128 v[88:91], v129 offset:8192\n\t"
        "v_readlane_b32 s40, v155, 15\n\t"
        "v_mfma_f32_16x16x32_bf16 v[36:39], a[96:99], v[108:111], v[36:39]\n\t"
        "ds_read_b128 v[92:95], v129 offset:12288\n\t"
        "ds_read_b64_tr_b16 a[144:145], v131 offset:30720\n\t"
        "v_readlane_b32 s43, v155, 3\n\t"
        "v_mfma_f32_16x16x32_bf16 v[40:43], a[100:103], v[108:111], v[40:43]\n\t"
        "ds_read_b64_tr_b16 a[146:147], v131 offset:31488\n\t"
        "s_add_u32 m0, s40, s35\n\t"
        "ds_read_b64_tr_b16 a[148:149], v132 offset:30720\n\t"
        "v_mfma_f32_16x16x32_bf16 v[44:47], a[104:107], v[108:111], v[44:47]\n\t"
        "buffer_load_dwordx4 v135, s[16:19], s43 offen  lds\n\t"
        "s_add_u32 m0, s37, 0\n\t"
        "ds_read_b64_tr_b16 a[150:151], v132 offset:31488\n\t"
        "s_waitcnt lgkmcnt(12)\n\t"
        "v_mfma_f32_16x16x32_bf16 v[0:3], a[120:123], v[112:115], v[0:3]\n\t"
        "ds_read_b64_tr_b16 a[152:153], v133 offset:30720\n\t"
        "s_add_u32 s36, s36, s39\n\t"
        "ds_read_b64_tr_b16 a[154:155], v133 offset:31488\n\t"
        "v_mfma_f32_16x16x32_bf16 v[4:7], a[124:127], v[112:115], v[4:7]\n\t"
        "v_mbcnt_hi_u32_b32 v56, s65, v56\n\t"
        "s_bcnt1_i32_b64 s46, s[64:65]\n\t"
        "v_mfma_f32_16x16x32_bf16 v[8:11], a[128:131], v[112:115], v[8:11]\n\t"
        "s_add_u32 s61, s61, s46\n\t"
        "v_min_u32 v56, 97, v56\n\t"
        "v_mfma_f32_16x16x32_bf16 v[0:3], a[108:111], v[84:87], v[0:3]\n\t"
        "buffer_load_dwordx4 v140, s[24:27], s36 offen offset:0 lds\n\t"
        "v_lshl_add_u32 v56, v56, 3, s60\n\t"
        "v_mfma_f32_16x16x32_bf16 v[4:7], a[112:115], v[84:87], v[4:7]\n\t"
        "s_mov_b64 exec, s[64:65]\n\t"
        "ds_write_b64 v56, v[52:53]\n\t"
        "s_mov_b64 exec, -1\n\t"
        "v_mfma_f32_16x16x32_bf16 v[8:11], a[116:119], v[84:87], v[8:11]\n\t"
        "s_cmp_gt_u32 s61, s53\n\t"
        "s_cselect_b32 s8, 1, 0\n\t"
        "v_mfma_f32_16x16x32_bf16 v[12:15], a[120:123], v[84:87], v[12:15]\n\t"
        "ds_read_b128 v[96:99], v129 offset:16384\n\t"
        "s_waitcnt vmcnt(12)\n\t"
        "v_and_b32 v48, s54, v48\n\t"
        "v_mfma_f32_16x16x32_bf16 v[16:19], a[124:127], v[84:87], v[16:19]\n\t"
        "buffer_load_dwordx4 v141, s[24:27], s36 offen offset:1024 lds\n\t"
        "v_pk_max_u16 v60, v154, v48\n\t"
        "v_mfma_f32_16x16x32_bf16 v[20:23], a[128:131], v[84:87], v[20:23]\n\t"
        "v_and_b32 v49, s54, v49\n\t"
        "v_pk_max_u16 v60, v60, v49\n\t"
        "s_waitcnt lgkmcnt(4)\n\t"
        "v_mfma_f32_16x16x32_bf16 v[0:3], a[132:135], v[88:91], v[0:3]\n\t"
        "v_and_b32 v50, s54, v50\n\t"
        "v_pk_max_u16 v60, v60, v50\n\t"
        "v_mfma_f32_16x16x32_bf16 v[4:7], a[136:139], v[88:91], v[4:7]\n\t"
        "v_and_b32 v51, s54, v51\n\t"
        "v_pk_max_u16 v60, v60, v51\n\t"
        "v_mfma_f32_16x16x32_bf16 v[8:11], a[140:143], v[88:91], v[8:11]\n\t"
        "buffer_load_dwordx4 v142, s[24:27], s36 offen offset:2048 lds\n\t"
        "v_readlane_b32 s40, v155, 16\n\t"
        "v_mfma_f32_16x16x32_bf16 v[12:15], a[108:111], v[88:91], v[12:15]\n\t"
        "ds_read_b128 v[100:103], v129 offset:20480\n\t"
        "v_readlane_b32 s43, v155, 4\n\t"
        "v_mfma_f32_16x16x32_bf16 v[16:19], a[112:115], v[88:91], v[16:19]\n\t"
        "s_add_u32 m0, s40, s35\n\t"
        "s_waitcnt vmcnt(13)\n\t"
        "v_and_b32 v61, s54, v61\n\t"
        "v_mfma_f32_16x16x32_bf16 v[20:23], a[116:119], v[88:91], v[20:23]\n\t"
        "v_pk_max_u16 v60, v60, v61\n\t"
        "v_cmp_ne_u32 s[12:13], v60, v154\n\t"
        "v_mfma_f32_16x16x32_bf16 v[24:27], a[120:123], v[88:91], v[24:27]\n\t"
        "buffer_load_dwordx4 v134, s[16:19], s43 offen  lds\n\t"
        "s_bitcmp1_b32 s42, 11\n\t"
        "s_cbranch_scc1 .Lx42%=\n\t"
        ".Lx43%=:\n\t"
        "v_mfma_f32_16x16x32_bf16 v[28:31], a[124:127], v[88:91], v[28:31]\n\t"
        "v_readlane_b32 s40, v155, 17\n\t"
        "v_readlane_b32 s43, v155, 5\n\t"
        "v_mfma_f32_16x16x32_bf16 v[32:35], a[128:131], v[88:91], v[32:35]\n\t"
        "s_add_u32 m0, s40, s35\n\t"
        "s_lshl_b32 s6, 256, s4\n\t"
        "v_mfma_f32_16x16x32_bf16 v[12:15], a[132:135], v[92:95], v[12:15]\n\t"
        "s_cmp_lg_u64 s[12:13], 0\n\t"
        "s_cselect_b32 s6, s6, 0\n\t"
        "v_mfma_f32_16x16x32_bf16 v[16:19], a[136:139], v[92:95], v[16:19]\n\t"
        "buffer_load_dwordx4 v135, s[16:19], s43 offen  lds\n\t"
        "s_add_u32 m0, s37, 12288\n\t"
        "v_mfma_f32_16x16x32_bf16 v[20:23], a[140:143], v[92:95], v[20:23]\n\t"
        "s_add_u32 s36, s36, s38\n\t"
        "s_or_b32 s56, s56, s8\n\t"
        "v_mfma_f32_16x16x32_bf16 v[24:27], a[108:111], v[92:95], v[24:27]\n\t"
        "s_cmp_gt_u32 s3, 64\n\t"
        "s_cselect_b32 s8, 1, 0\n\t"
        "v_mfma_f32_16x16x32_bf16 v[28:31], a[112:115], v[92:95], v[28:31]\n\t"
        "s_or_b32 s56, s56, s8\n\t"
        "s_min_u32 s61, s61, 80\n\t"
        "v_mfma_f32_16x16x32_bf16 v[32:35], a[116:119], v[92:95], v[32:35]\n\t"
        "s_waitcnt vmcnt(10) lgkmcnt(0)\n\t"
        "s_barrier\n\t"
        "buffer_load_dwordx4 v140, s[24:27], s36 offen offset:0 lds\n\t"
        "ds_read_b64_tr_b16 a[156:157], v131 offset:36864\n\t"
        "v_mfma_f32_16x16x32_bf16 v[36:39], a[120:123], v[92:95], v[36:39]\n\t"
        "ds_read_b64_tr_b16 a[158:159], v131 offset:37632\n\t"
        "ds_read_b64_tr_b16 a[160:161], v132 offset:36864\n\t"
        "v_min_u32 v78, s61, v116\n\t"
        "v_mfma_f32_16x16x32_bf16 v[40:43], a[124:127], v[92:95], v[40:43]\n\t"
        "ds_read_b64_tr_b16 a[162:163], v132 offset:37632\n\t"
        "ds_read_b64_tr_b16 a[164:165], v133 offset:36864\n\t"
        "v_lshl_add_u32 v78, v78, 3, s60\n\t"
        "v_mfma_f32_16x16x32_bf16 v[44:47], a[128:131], v[92:95], v[44:47]\n\t"
        "ds_read_b64_tr_b16 a[166:167], v133 offset:37632\n\t"
        "ds_read_b128 v[104:107], v129 offset:64\n\t"
        "v_cmp_ge_u32 s[66:67], s61, v116\n\t"
        "v_mfma_f32_16x16x32_bf16 v[24:27], a[132:135], v[96:99], v[24:27]\n\t"
        "buffer_load_dwordx4 v141, s[24:27], s36 offen offset:1024 lds\n\t"
        "ds_read_b128 v[108:111], v129 offset:4160\n\t"
        "s_mov_b32 s46, 0x07060302\n\t"
        "v_mfma_f32_16x16x32_bf16 v[28:31], a[136:139], v[96:99], v[28:31]\n\t"
        "ds_read_b64 v[62:63], v78\n\t"
        "ds_read_b64_tr_b16 a[168:169], v131 offset:43008\n\t"
        "v_mfma_f32_16x16x32_bf16 v[32:35], a[140:143], v[96:99], v[32:35]\n\t"
        "ds_read_b64_tr_b16 a[170:171], v131 offset:43776\n\t"
        "ds_read_b64_tr_b16 a[172:173], v132 offset:43008\n\t"
        "v_mfma_f32_16x16x32_bf16 v[36:39], a[108:111], v[96:99], v[36:39]\n\t"
        "ds_read_b64_tr_b16 a[174:175], v132 offset:43776\n\t"
        "ds_read_b64_tr_b16 a[176:177], v133 offset:43008\n\t"
        "v_mfma_f32_16x16x32_bf16 v[40:43], a[112:115], v[96:99], v[40:43]\n\t"
        "buffer_load_dwordx4 v142, s[24:27], s36 offen offset:2048 lds\n\t"
        "ds_read_b64_tr_b16 a[178:179], v133 offset:43776\n\t"
        "v_readlane_b32 s40, v155, 18\n\t"
        "v_mfma_f32_16x16x32_bf16 v[44:47], a[116:119], v[96:99], v[44:47]\n\t"
        "ds_read_b128 v[112:115], v129 offset:8256\n\t"
        "v_readlane_b32 s43, v155, 6\n\t"
        "v_mfma_f32_16x16x32_bf16 v[36:39], a[132:135], v[100:103], v[36:39]\n\t"
        "ds_read_b128 v[84:87], v129 offset:12352\n\t"
        "s_add_u32 m0, s40, s35\n\t"
        "v_mfma_f32_16x16x32_bf16 v[40:43], a[136:139], v[100:103], v[40:43]\n\t"
        "v_mfma_f32_16x16x32_bf16 v[44:47], a[140:143], v[100:103], v[44:47]\n\t"
        "buffer_load_dwordx4 v136, s[16:19], s43 offen  lds\n\t"
        "s_bitcmp1_b32 s42, 11\n\t"
        "s_cbranch_scc1 .Lx44%=\n\t"
        ".Lx45%=:\n\t"
        "s_waitcnt lgkmcnt(9)\n\t"
        "v_mfma_f32_16x16x32_bf16 v[0:3], a[156:159], v[104:107], v[0:3]\n\t"
        "v_readlane_b32 s40, v155, 19\n\t"
        "v_readlane_b32 s43, v155, 7\n\t"
        "v_mfma_f32_16x16x32_bf16 v[4:7], a[160:163], v[104:107], v[4:7]\n\t"
        "s_mov_b64 exec, s[66:67]\n\t"
        "s_waitcnt lgkmcnt(7)\n\t"
        "ds_write_b32 v63, v117\n\t"
        "s_mov_b64 exec, -1\n\t"
        "v_mfma_f32_16x16x32_bf16 v[8:11], a[164:167], v[104:107], v[8:11]\n\t"
        "s_add_u32 m0, s40, s35\n\t"
        "v_and_b32 v78, 0xfffff800, v63\n\t"
        "v_mfma_f32_16x16x32_bf16 v[0:3], a[144:147], v[108:111], v[0:3]\n\t"
        "ds_read_b128 v[64:67], v78 offset:1536\n\t"
        "v_lshlrev_b32 v72, 16, v62\n\t"
        "v_mfma_f32_16x16x32_bf16 v[4:7], a[148:151], v[108:111], v[4:7]\n\t"
        "buffer_load_dwordx4 v137, s[16:19], s43 offen  lds\n\t"
        "s_add_u32 m0, s37, 24576\n\t"
        "v_mfma_f32_16x16x32_bf16 v[8:11], a[152:155], v[108:111], v[8:11]\n\t"
        "s_add_u32 s36, s36, s5\n\t"
        "v_and_b32 v73, 0xffff0000, v62\n\t"
        "v_mfma_f32_16x16x32_bf16 v[12:15], a[156:159], v[108:111], v[12:15]\n\t"
        "ds_read_b128 v[88:91], v129 offset:16448\n\t"
        "v_med3_f32 v72, v72, 0, s63\n\t"
        "v_mfma_f32_16x16x32_bf16 v[16:19], a[160:163], v[108:111], v[16:19]\n\t"
        "v_med3_f32 v73, v73, 0, s63\n\t"
        "v_perm_b32 v62, v73, v72, s46\n\t"
        "v_mfma_f32_16x16x32_bf16 v[20:23], a[164:167], v[108:111], v[20:23]\n\t"
        "v_fract_f32 v74, v72\n\t"
        "v_fract_f32 v75, v73\n\t"
        "s_waitcnt lgkmcnt(3)\n\t"
        "v_mfma_f32_16x16x32_bf16 v[0:3], a[168:171], v[112:115], v[0:3]\n\t"
        "v_cvt_u32_f32 v76, v72\n\t"
        "v_cvt_u32_f32 v77, v73\n\t"
        "v_mfma_f32_16x16x32_bf16 v[4:7], a[172:175], v[112:115], v[4:7]\n\t"
        "v_cmp_neq_f32 s[64:65], 0, v74\n\t"
        "v_cmp_neq_f32 s[44:45], 0, v75\n\t"
        "v_mfma_f32_16x16x32_bf16 v[8:11], a[176:179], v[112:115], v[8:11]\n\t"
        "v_sub_f32 v78, 1.0, v74\n\t"
        "v_sub_f32 v79, 1.0, v75\n\t"
        "v_mfma_f32_16x16x32_bf16 v[12:15], a[144:147], v[112:115], v[12:15]\n\t"
        "ds_read_b128 v[92:95], v129 offset:20544\n\t"
        "v_cvt_pk_bf16_f32 v79, v78, v79\n\t"
        "v_mfma_f32_16x16x32_bf16 v[16:19], a[148:151], v[112:115], v[16:19]\n\t"
        "v_lshlrev_b32 v78, 16, v79\n\t"
        "v_and_b32 v79, 0xffff0000, v79\n\t"
        "v_mfma_f32_16x16x32_bf16 v[20:23], a[152:155], v[112:115], v[20:23]\n\t"
        "s_waitcnt vmcnt(10)\n\t"
        "s_barrier\n\t"
        "buffer_load_dwordx4 v140, s[24:27], s36 offen offset:0 lds\n\t"
        "ds_read_b64_tr_b16 a[180:181], v131 offset:49152\n\t"
        "v_mfma_f32_16x16x32_bf16 v[24:27], a[156:159], v[112:115], v[24:27]\n\t"
        "ds_read_b64_tr_b16 a[182:183], v131 offset:49920\n\t"
        "ds_read_b64_tr_b16 a[184:185], v132 offset:49152\n\t"
        "v_mul_f32 v80, v78, v79\n\t"
        "v_mfma_f32_16x16x32_bf16 v[28:31], a[160:163], v[112:115], v[28:31]\n\t"
        "ds_read_b64_tr_b16 a[186:187], v132 offset:49920\n\t"
        "ds_read_b64_tr_b16 a[188:189], v133 offset:49152\n\t"
        "v_mul_f32 v78, v78, v75\n\t"
        "v_mfma_f32_16x16x32_bf16 v[32:35], a[164:167], v[112:115], v[32:35]\n\t"
        "ds_read_b64_tr_b16 a[190:191], v133 offset:49920\n\t"
        "ds_read_b64_tr_b16 a[192:193], v131 offset:55296\n\t"
        "v_mul_f32 v79, v74, v79\n\t"
        "v_mfma_f32_16x16x32_bf16 v[12:15], a[168:171], v[84:87], v[12:15]\n\t"
        "buffer_load_dwordx4 v141, s[24:27], s36 offen offset:1024 lds\n\t"
        "ds_read_b64_tr_b16 a[194:195], v131 offset:56064\n\t"
        "v_mul_f32 v81, v74, v75\n\t"
        "v_mfma_f32_16x16x32_bf16 v[16:19], a[172:175], v[84:87], v[16:19]\n\t"
        "ds_read_b64_tr_b16 a[196:197], v132 offset:55296\n\t"
        "ds_read_b64_tr_b16 a[198:199], v132 offset:56064\n\t"
        "v_cndmask_b32 v78, v79, v78, s[44:45]\n\t"
        "v_mfma_f32_16x16x32_bf16 v[20:23], a[176:179], v[84:87], v[20:23]\n\t"
        "ds_read_b64_tr_b16 a[200:201], v133 offset:55296\n\t"
        "ds_read_b64_tr_b16 a[202:203], v133 offset:56064\n\t"
        "v_cndmask_b32 v79, 0, v79, s[44:45]\n\t"
        "v_mfma_f32_16x16x32_bf16 v[24:27], a[144:147], v[84:87], v[24:27]\n\t"
        "ds_read_b128 v[96:99], v129 offset:128\n\t"
        "v_cvt_pk_bf16_f32 v70, v80, v78\n\t"
        "v_mfma_f32_16x16x32_bf16 v[28:31], a[148:151], v[84:87], v[28:31]\n\t"
        "buffer_load_dwordx4 v142, s[24:27], s36 offen offset:2048 lds\n\t"
        "s_add_u32 m0, s37, 36864\n\t"
        "v_mfma_f32_16x16x32_bf16 v[32:35], a[152:155], v[84:87], v[32:35]\n\t"
        "ds_read_b128 v[100:103], v129 offset:4224\n\t"
        "s_add_u32 s36, s36, s39\n\t"
        "v_mfma_f32_16x16x32_bf16 v[36:39], a[156:159], v[84:87], v[36:39]\n\t"
        "v_cvt_pk_bf16_f32 v71, v79, v81\n\t"
        "s_waitcnt lgkmcnt(9)\n\t"
        "v_pk_max_u16 v78, v62, v64\n\t"
        "v_mfma_f32_16x16x32_bf16 v[40:43], a[160:163], v[84:87], v[40:43]\n\t"
        "v_pk_min_u16 v78, v78, v65\n\t"
        "v_cmp_ne_u32 vcc, v78, v62\n\t"
        "v_mfma_f32_16x16x32_bf16 v[44:47], a[164:167], v[84:87], v[44:47]\n\t"
        "buffer_load_dwordx4 v140, s[24:27], s36 offen offset:0 lds\n\t"
        "v_sub_u32 v78, v77, v67\n\t"
        "v_mfma_f32_16x16x32_bf16 v[24:27], a[168:171], v[88:91], v[24:27]\n\t"
        "v_mad_u32_u24 v79, v77, 6, v66\n\t"
        "v_mad_u32_u24 v79, v76, s62, v79\n\t"
        "v_mfma_f32_16x16x32_bf16 v[28:31], a[172:175], v[88:91], v[28:31]\n\t"
        "s_and_b64 vcc, vcc, s[66:67]\n\t"
        "s_or_b64 s[56:57], s[56:57], vcc\n\t"
        "v_mfma_f32_16x16x32_bf16 v[32:35], a[176:179], v[88:91], v[32:35]\n\t"
        "v_bfe_u32 v80, v78, 2, 1\n\t"
        "v_or_b32 v80, v80, v79\n\t"
        "v_mfma_f32_16x16x32_bf16 v[36:39], a[144:147], v[88:91], v[36:39]\n\t"
        "v_add_u32 v78, 1, v78\n\t"
        "v_bfe_u32 v78, v78, 2, 1\n\t"
        "v_mfma_f32_16x16x32_bf16 v[40:43], a[148:151], v[88:91], v[40:43]\n\t"
        "v_add3_u32 v78, v79, v78, 6\n\t"
        "v_cndmask_b32 v78, v80, v78, s[44:45]\n\t"
        "v_mfma_f32_16x16x32_bf16 v[44:47], a[152:155], v[88:91], v[44:47]\n\t"
        "buffer_load_dwordx4 v141, s[24:27], s36 offen offset:1024 lds\n\t"
        "v_cndmask_b32 v79, 0, v119, s[64:65]\n\t"
        "v_mfma_f32_16x16x32_bf16 v[36:39], a[168:171], v[92:95], v[36:39]\n\t"
        "v_add_u32 v81, v80, v79\n\t"
        "v_add_u32 v82, v78, v79\n\t"
        "v_mfma_f32_16x16x32_bf16 v[40:43], a[172:175], v[92:95], v[40:43]\n\t"
        "v_cndmask_b32 v78, v81, v78, s[44:45]\n\t"
        "v_lshl_or_b32 v68, v78, 16, v80\n\t"
        "v_mfma_f32_16x16x32_bf16 v[44:47], a[176:179], v[92:95], v[44:47]\n\t"
        "v_lshl_or_b32 v69, v82, 16, v81\n\t"
        "s_waitcnt lgkmcnt(0)\n\t"
        "v_mfma_f32_16x16x32_bf16 v[0:3], a[192:195], v[96:99], v[0:3]\n\t"
        "ds_write_b128 v118, v[68:71]\n\t"
        "v_mfma_f32_16x16x32_bf16 v[4:7], a[196:199], v[96:99], v[4:7]\n\t"
        "s_waitcnt vmcnt(10)\n\t"
        "s_barrier\n\t"
        "buffer_load_dwordx4 v142, s[24:27], s36 offen offset:2048 lds\n\t"
        "s_add_u32 m0, s37, 49152\n\t"
        "ds_read_b64_tr_b16 a[204:205], v131 offset:0\n\t"
        "s_add_u32 s36, s36, s38\n\t"
        "ds_read_b64_tr_b16 a[206:207], v131 offset:768\n\t"
        "v_mfma_f32_16x16x32_bf16 v[8:11], a[200:203], v[96:99], v[8:11]\n\t"
        "ds_read_b64_tr_b16 a[208:209], v132 offset:0\n\t"
        "ds_read_b64_tr_b16 a[210:211], v132 offset:768\n\t"
        "ds_read_b64_tr_b16 a[212:213], v133 offset:0\n\t"
        "ds_read_b64_tr_b16 a[214:215], v133 offset:768\n\t"
        "ds_read_b128 v[104:107], v129 offset:8320\n\t"
        "v_xor_b32 v154, 32, v186\n\t"
        "v_mfma_f32_16x16x32_bf16 v[0:3], a[180:183], v[100:103], v[0:3]\n\t"
        "s_cmp_gt_u32 s3, 32\n\t"
        "s_cselect_b32 s8, 2, 0\n\t"
        "ds_read_b128 v[108:111], v129 offset:12416\n\t"
        "ds_read_b64_tr_b16 a[216:217], v131 offset:6144\n\t"
        "v_mfma_f32_16x16x32_bf16 v[4:7], a[184:187], v[100:103], v[4:7]\n\t"
        "s_cmp_lg_u64 s[56:57], 0\n\t"
        "s_addc_u32 s8, s8, s6\n\t"
        "ds_read_b64_tr_b16 a[218:219], v131 offset:6912\n\t"
        "ds_read_b64_tr_b16 a[220:221], v132 offset:6144\n\t"
        "v_mfma_f32_16x16x32_bf16 v[8:11], a[188:191], v[100:103], v[8:11]\n\t"
        "ds_read_b128 v[160:163], v185 offset:0\n\t"
        "buffer_load_dwordx4 v140, s[24:27], s36 offen offset:0 lds\n\t"
        "v_mov_b32 v56, s8\n\t"
        "v_mfma_f32_16x16x32_bf16 v[12:15], a[192:195], v[100:103], v[12:15]\n\t"
        "s_mov_b64 exec, 1\n\t"
        "ds_write_b32 v153, v56\n\t"
        "s_mov_b64 exec, -1\n\t"
        "ds_read_b128 v[112:115], v129 offset:16512\n\t"
        "v_mfma_f32_16x16x32_bf16 v[16:19], a[196:199], v[100:103], v[16:19]\n\t"
        "ds_read_b64_tr_b16 a[222:223], v132 offset:6912\n\t"
        "ds_read_b64_tr_b16 a[224:225], v133 offset:6144\n\t"
        "v_mfma_f32_16x16x32_bf16 v[20:23], a[200:203], v[100:103], v[20:23]\n\t"
        "ds_read_b64_tr_b16 a[226:227], v133 offset:6912\n\t"
        "s_waitcnt lgkmcnt(10)\n\t"
        "v_mfma_f32_16x16x32_bf16 v[0:3], a[204:207], v[104:107], v[0:3]\n\t"
        "buffer_load_dwordx4 v141, s[24:27], s36 offen offset:1024 lds\n\t"
        "v_mfma_f32_16x16x32_bf16 v[4:7], a[208:211], v[104:107], v[4:7]\n\t"
        "v_mfma_f32_16x16x32_bf16 v[8:11], a[212:215], v[104:107], v[8:11]\n\t"
        "v_mfma_f32_16x16x32_bf16 v[12:15], a[180:183], v[104:107], v[12:15]\n\t"
        "ds_read_b128 v[84:87], v129 offset:20608\n\t"
        "v_mfma_f32_16x16x32_bf16 v[16:19], a[184:187], v[104:107], v[16:19]\n\t"
        "buffer_load_dwordx4 v142, s[24:27], s36 offen offset:2048 lds\n\t"
        "v_mfma_f32_16x16x32_bf16 v[20:23], a[188:191], v[104:107], v[20:23]\n\t"
        "s_waitcnt lgkmcnt(6)\n\t"
        "v_lshlrev_b32_sdwa v157, 5, v160 dst_sel:DWORD dst_unused:UNUSED_PAD src0_sel:DWORD src1_sel:WORD_0\n\t"
        "v_lshlrev_b32_sdwa v158, 5, v160 dst_sel:DWORD dst_unused:UNUSED_PAD src0_sel:DWORD src1_sel:WORD_1\n\t"
        "v_mfma_f32_16x16x32_bf16 v[24:27], a[192:195], v[104:107], v[24:27]\n\t"
        "v_or_b32 v157, v184, v157\n\t"
        "v_or_b32 v158, v184, v158\n\t"
        "v_mfma_f32_16x16x32_bf16 v[28:31], a[196:199], v[104:107], v[28:31]\n\t"
        "v_xor_b32 v159, 32, v157\n\t"
        "v_xor_b32 v152, 32, v158\n\t"
        "v_mfma_f32_16x16x32_bf16 v[32:35], a[200:203], v[104:107], v[32:35]\n\t"
        "v_perm_b32 v164, v162, v162, v168\n\t"
        "v_perm_b32 v165, v162, v162, v169\n\t"
        "v_mfma_f32_16x16x32_bf16 v[12:15], a[204:207], v[108:111], v[12:15]\n\t"
        "v_perm_b32 v166, v162, v162, v170\n\t"
        "v_perm_b32 v167, v162, v162, v171\n\t"
        "v_mfma_f32_16x16x32_bf16 v[16:19], a[208:211], v[108:111], v[16:19]\n\t"
        "v_cmp_ne_u32 vcc, 0, v163\n\t"
        "v_mfma_f32_16x16x32_bf16 v[20:23], a[212:215], v[108:111], v[20:23]\n\t"
        "v_mfma_f32_16x16x32_bf16 v[24:27], a[180:183], v[108:111], v[24:27]\n\t"
        "v_mfma_f32_16x16x32_bf16 v[28:31], a[184:187], v[108:111], v[28:31]\n\t"
        "v_mfma_f32_16x16x32_bf16 v[32:35], a[188:191], v[108:111], v[32:35]\n\t"
        "v_mfma_f32_16x16x32_bf16 v[36:39], a[192:195], v[108:111], v[36:39]\n\t"
        "v_mfma_f32_16x16x32_bf16 v[40:43], a[196:199], v[108:111], v[40:43]\n\t"
        "s_waitcnt vmcnt(9) lgkmcnt(0)\n\t"
        "s_barrier\n\t"
        "ds_read_b64_tr_b16 a[228:229], v131 offset:12288\n\t"
        "ds_read_b64_tr_b16 a[230:231], v131 offset:13056\n\t"
        "v_mfma_f32_16x16x32_bf16 v[44:47], a[200:203], v[108:111], v[44:47]\n\t"
        "ds_read_b64_tr_b16 a[232:233], v132 offset:12288\n\t"
        "ds_read_b64_tr_b16 a[234:235], v132 offset:13056\n\t"
        "v_mfma_f32_16x16x32_bf16 v[24:27], a[204:207], v[112:115], v[24:27]\n\t"
        "ds_read_b64_tr_b16 a[236:237], v133 offset:12288\n\t"
        "ds_read_b64_tr_b16 a[238:239], v133 offset:13056\n\t"
        "ds_read_b128 v[88:91], v130 offset:0\n\t"
        "v_mfma_f32_16x16x32_bf16 v[28:31], a[208:211], v[112:115], v[28:31]\n\t"
        "ds_read_b128 v[92:95], v130 offset:4096\n\t"
        "ds_read_b64_tr_b16 a[240:241], v131 offset:18432\n\t"
        "v_mfma_f32_16x16x32_bf16 v[32:35], a[212:215], v[112:115], v[32:35]\n\t"
        "ds_read_b64_tr_b16 a[242:243], v131 offset:19200\n\t"
        "ds_read_b64_tr_b16 a[244:245], v132 offset:18432\n\t"
        "v_mfma_f32_16x16x32_bf16 v[36:39], a[180:183], v[112:115], v[36:39]\n\t"
        "ds_read_b64_tr_b16 a[246:247], v132 offset:19200\n\t"
        "ds_read_b64_tr_b16 a[248:249], v133 offset:18432\n\t"
        "v_mfma_f32_16x16x32_bf16 v[40:43], a[184:187], v[112:115], v[40:43]\n\t"
        "ds_read_b64_tr_b16 a[250:251], v133 offset:19200\n\t"
        "v_mfma_f32_16x16x32_bf16 v[44:47], a[188:191], v[112:115], v[44:47]\n\t"
        "ds_read_b128 v[96:99], v130 offset:8192\n\t"
        "v_mfma_f32_16x16x32_bf16 v[36:39], a[204:207], v[84:87], v[36:39]\n\t"
        "ds_read_b64_tr_b16 v[60:61], v157 offset:0\n\t"
        "v_mfma_f32_16x16x32_bf16 v[40:43], a[208:211], v[84:87], v[40:43]\n\t"
        "ds_read_b64_tr_b16 v[62:63], v158 offset:0\n\t"
        "v_mfma_f32_16x16x32_bf16 v[44:47], a[212:215], v[84:87], v[44:47]\n\t"
        "s_waitcnt vmcnt(6)\n\t"
        "s_barrier\n\t"
        "ds_read_b128 v[100:103], v130 offset:12288\n\t"
        "ds_read_b64_tr_b16 a[252:253], v131 offset:24576\n\t"
        "s_waitcnt lgkmcnt(12)\n\t"
        "v_mfma_f32_16x16x32_bf16 v[0:3], a[228:231], v[88:91], v[0:3]\n\t"
        "ds_read_b64_tr_b16 v[64:65], v159 offset:0\n\t"
        "ds_read_b64_tr_b16 a[254:255], v131 offset:25344\n\t"
        "v_mfma_f32_16x16x32_bf16 v[4:7], a[232:235], v[88:91], v[4:7]\n\t"
        "ds_read_b64_tr_b16 v[66:67], v152 offset:0\n\t"
        "ds_read_b64_tr_b16 v[188:189], v132 offset:24576\n\t"
        "v_mfma_f32_16x16x32_bf16 v[8:11], a[236:239], v[88:91], v[8:11]\n\t"
        "ds_read_b64_tr_b16 v[68:69], v157 offset:64\n\t"
        "ds_read_b64_tr_b16 v[190:191], v132 offset:25344\n\t"
        "s_waitcnt lgkmcnt(12)\n\t"
        "v_mfma_f32_16x16x32_bf16 v[0:3], a[216:219], v[92:95], v[0:3]\n\t"
        "ds_read_b64_tr_b16 v[70:71], v158 offset:64\n\t"
        "ds_read_b64_tr_b16 v[192:193], v133 offset:24576\n\t"
        "v_mfma_f32_16x16x32_bf16 v[4:7], a[220:223], v[92:95], v[4:7]\n\t"
        "ds_read_b64_tr_b16 v[72:73], v159 offset:64\n\t"
        "ds_read_b64_tr_b16 v[194:195], v133 offset:25344\n\t"
        "v_mfma_f32_16x16x32_bf16 v[8:11], a[224:227], v[92:95], v[8:11]\n\t"
        "ds_read_b64_tr_b16 v[74:75], v152 offset:64\n\t"
        "v_mfma_f32_16x16x32_bf16 v[12:15], a[228:231], v[92:95], v[12:15]\n\t"
        "ds_read_b64_tr_b16 v[76:77], v157 offset:128\n\t"
        "v_mfma_f32_16x16x32_bf16 v[16:19], a[232:235], v[92:95], v[16:19]\n\t"
        "ds_read_b64_tr_b16 v[78:79], v158 offset:128\n\t"
        "s_waitcnt lgkmcnt(14)\n\t"
        "v_mfma_f32_16x16x32_bf16 v[48:51], v[60:63], v[164:167], 0\n\t"
        "ds_read_b64_tr_b16 v[80:81], v159 offset:128\n\t"
        "v_mfma_f32_16x16x32_bf16 v[20:23], a[236:239], v[92:95], v[20:23]\n\t"
        "ds_read_b64_tr_b16 v[82:83], v152 offset:128\n\t"
        "ds_read_b128 v[104:107], v130 offset:16384\n\t"
        "v_mfma_f32_16x16x32_bf16 v[0:3], a[240:243], v[96:99], v[0:3]\n\t"
        "ds_read_b64_tr_b16 v[196:197], v131 offset:30720\n\t"
        "ds_read_b64_tr_b16 v[198:199], v131 offset:31488\n\t"
        "s_waitcnt lgkmcnt(13)\n\t"
        "v_mfma_f32_16x16x32_bf16 v[52:55], v[64:67], v[164:167], 0\n\t"
        "ds_read_b64_tr_b16 v[200:201], v132 offset:30720\n\t"
        "ds_read_b64_tr_b16 v[202:203], v132 offset:31488\n\t"
        "v_mfma_f32_16x16x32_bf16 v[4:7], a[244:247], v[96:99], v[4:7]\n\t"
        "ds_read_b64_tr_b16 v[204:205], v133 offset:30720\n\t"
        "ds_read_b64_tr_b16 v[206:207], v133 offset:31488\n\t"
        "s_waitcnt lgkmcnt(13)\n\t"
        "v_mfma_f32_16x16x32_bf16 v[56:59], v[68:71], v[164:167], 0\n\t"
        "v_mfma_f32_16x16x32_bf16 v[8:11], a[248:251], v[96:99], v[8:11]\n\t"
        "s_waitcnt lgkmcnt(11)\n\t"
        "v_mfma_f32_16x16x32_bf16 v[116:119], v[72:75], v[164:167], 0\n\t"
        "v_mfma_f32_16x16x32_bf16 v[12:15], a[216:219], v[96:99], v[12:15]\n\t"
        "ds_read_b128 v[108:111], v130 offset:64\n\t"
        "s_waitcnt lgkmcnt(10)\n\t"
        "v_mfma_f32_16x16x32_bf16 v[120:123], v[76:79], v[164:167], 0\n\t"
        "v_mfma_f32_16x16x32_bf16 v[16:19], a[220:223], v[96:99], v[16:19]\n\t"
        "s_waitcnt lgkmcnt(8)\n\t"
        "v_mfma_f32_16x16x32_bf16 v[124:127], v[80:83], v[164:167], 0\n\t"
        "s_cbranch_vccnz .Lx46%=\n\t"
        ".Lx47%=:\n\t"
        "v_cvt_pk_bf16_f32 v48, v48, v49\n\t"
        "v_mfma_f32_16x16x32_bf16 v[20:23], a[224:227], v[96:99], v[20:23]\n\t"
        "ds_read_b128 v[160:163], v185 offset:256\n\t"
        "v_cvt_pk_bf16_f32 v49, v50, v51\n\t"
        "v_mfma_f32_16x16x32_bf16 v[24:27], a[228:231], v[96:99], v[24:27]\n\t"
        "ds_write_b64 v186, v[48:49] offset:0\n\t"
        "v_mfma_f32_16x16x32_bf16 v[28:31], a[232:235], v[96:99], v[28:31]\n\t"
        "ds_read_b128 v[112:115], v130 offset:4160\n\t"
        "v_cvt_pk_bf16_f32 v52, v52, v53\n\t"
        "v_mfma_f32_16x16x32_bf16 v[32:35], a[236:239], v[96:99], v[32:35]\n\t"
        "v_cvt_pk_bf16_f32 v53, v54, v55\n\t"
        "v_cvt_pk_bf16_f32 v56, v56, v57\n\t"
        "v_mfma_f32_16x16x32_bf16 v[12:15], a[240:243], v[100:103], v[12:15]\n\t"
        "ds_write_b64 v154, v[52:53] offset:0\n\t"
        "v_mfma_f32_16x16x32_bf16 v[16:19], a[244:247], v[100:103], v[16:19]\n\t"
        "v_cvt_pk_bf16_f32 v57, v58, v59\n\t"
        "v_cvt_pk_bf16_f32 v116, v116, v117\n\t"
        "v_mfma_f32_16x16x32_bf16 v[20:23], a[248:251], v[100:103], v[20:23]\n\t"
        "ds_write_b64 v186, v[56:57] offset:64\n\t"
        "v_mfma_f32_16x16x32_bf16 v[24:27], a[216:219], v[100:103], v[24:27]\n\t"
        "ds_read_b128 v[84:87], v130 offset:20480\n\t"
        "v_cvt_pk_bf16_f32 v117, v118, v119\n\t"
        "v_mfma_f32_16x16x32_bf16 v[28:31], a[220:223], v[100:103], v[28:31]\n\t"
        "ds_write_b64 v154, v[116:117] offset:64\n\t"
        "v_mfma_f32_16x16x32_bf16 v[32:35], a[224:227], v[100:103], v[32:35]\n\t"
        ".LpM1%=:\n\t"
        "s_cbranch_execz .LrM%=\n\t"
        "v_cvt_pk_bf16_f32 v120, v120, v121\n\t"
        "v_cvt_pk_bf16_f32 v121, v122, v123\n\t"
        "v_mfma_f32_16x16x32_bf16 v[36:39], a[228:231], v[100:103], v[36:39]\n\t"
        "ds_write_b64 v186, v[120:121] offset:128\n\t"
        "v_mfma_f32_16x16x32_bf16 v[40:43], a[232:235], v[100:103], v[40:43]\n\t"
        "s_waitcnt vmcnt(0) lgkmcnt(5)\n\t"
        "s_barrier\n\t"
        "ds_read_b64_tr_b16 v[208:209], v131 offset:36864\n\t"
        "ds_read_b64_tr_b16 v[210:211], v131 offset:37632\n\t"
        "v_mfma_f32_16x16x32_bf16 v[44:47], a[236:239], v[100:103], v[44:47]\n\t"
        "ds_read_b64_tr_b16 v[212:213], v132 offset:36864\n\t"
        "ds_read_b64_tr_b16 v[214:215], v132 offset:37632\n\t"
        "v_cvt_pk_bf16_f32 v124, v124, v125\n\t"
        "v_mfma_f32_16x16x32_bf16 v[24:27], a[240:243], v[104:107], v[24:27]\n\t"
        "ds_read_b64_tr_b16 v[216:217], v133 offset:36864\n\t"
        "ds_read_b64_tr_b16 v[218:219], v133 offset:37632\n\t"
        "v_cvt_pk_bf16_f32 v125, v126, v127\n\t"
        "v_mfma_f32_16x16x32_bf16 v[28:31], a[244:247], v[104:107], v[28:31]\n\t"
        "ds_write_b64 v154, v[124:125] offset:128\n\t"
        "v_mfma_f32_16x16x32_bf16 v[32:35], a[248:251], v[104:107], v[32:35]\n\t"
        "ds_read_b128 v[88:91], v130 offset:8256\n\t"
        "v_lshlrev_b32_sdwa v157, 5, v160 dst_sel:DWORD dst_unused:UNUSED_PAD src0_sel:DWORD src1_sel:WORD_0\n\t"
        "v_mfma_f32_16x16x32_bf16 v[36:39], a[216:219], v[104:107], v[36:39]\n\t"
        "ds_read_b64_tr_b16 v[220:221], v131 offset:43008\n\t"
        "v_lshlrev_b32_sdwa v158, 5, v160 dst_sel:DWORD dst_unused:UNUSED_PAD src0_sel:DWORD src1_sel:WORD_1\n\t"
        "v_mfma_f32_16x16x32_bf16 v[40:43], a[220:223], v[104:107], v[40:43]\n\t"
        "ds_read_b64_tr_b16 v[222:223], v131 offset:43776\n\t"
        "v_or_b32 v157, v184, v157\n\t"
        "v_mfma_f32_16x16x32_bf16 v[44:47], a[224:227], v[104:107], v[44:47]\n\t"
        "ds_read_b64_tr_b16 v[60:61], v157 offset:0\n\t"
        "ds_read_b64_tr_b16 v[224:225], v132 offset:43008\n\t"
        "v_or_b32 v158, v184, v158\n\t"
        "v_mfma_f32_16x16x32_bf16 v[0:3], v[196:199], v[108:111], v[0:3]\n\t"
        "ds_read_b64_tr_b16 v[62:63], v158 offset:0\n\t"
        "ds_read_b128 v[92:95], v130 offset:12352\n\t"
        "v_xor_b32 v159, 32, v157\n\t"
        "v_mfma_f32_16x16x32_bf16 v[4:7], v[200:203], v[108:111], v[4:7]\n\t"
        "ds_read_b64_tr_b16 v[64:65], v159 offset:0\n\t"
        "ds_read_b64_tr_b16 v[226:227], v132 offset:43776\n\t"
        "v_xor_b32 v152, 32, v158\n\t"
        "v_mfma_f32_16x16x32_bf16 v[8:11], v[204:207], v[108:111], v[8:11]\n\t"
        "ds_read_b64_tr_b16 v[66:67], v152 offset:0\n\t"
        "ds_read_b64_tr_b16 v[228:229], v133 offset:43008\n\t"
        "v_perm_b32 v164, v162, v162, v168\n\t"
        "v_mfma_f32_16x16x32_bf16 v[0:3], a[252:255], v[112:115], v[0:3]\n\t"
        "ds_read_b64_tr_b16 v[68:69], v157 offset:64\n\t"
        "ds_read_b64_tr_b16 v[230:231], v133 offset:43776\n\t"
        "v_perm_b32 v165, v162, v162, v169\n\t"
        "v_mfma_f32_16x16x32_bf16 v[4:7], v[188:191], v[112:115], v[4:7]\n\t"
        "ds_read_b64_tr_b16 v[70:71], v158 offset:64\n\t"
        "v_perm_b32 v166, v162, v162, v170\n\t"
        "v_mfma_f32_16x16x32_bf16 v[8:11], v[192:195], v[112:115], v[8:11]\n\t"
        "ds_read_b64_tr_b16 v[72:73], v159 offset:64\n\t"
        "v_perm_b32 v167, v162, v162, v171\n\t"
        "v_mfma_f32_16x16x32_bf16 v[12:15], v[196:199], v[112:115], v[12:15]\n\t"
        "ds_read_b64_tr_b16 v[74:75], v152 offset:64\n\t"
        "v_cmp_ne_u32 vcc, 0, v163\n\t"
        "v_mfma_f32_16x16x32_bf16 v[16:19], v[200:203], v[112:115], v[16:19]\n\t"
        "ds_read_b64_tr_b16 v[76:77], v157 offset:128\n\t"
        "v_mfma_f32_16x16x32_bf16 v[20:23], v[204:207], v[112:115], v[20:23]\n\t"
        "ds_read_b64_tr_b16 v[78:79], v158 offset:128\n\t"
        "s_waitcnt lgkmcnt(11)\n\t"
        "v_mfma_f32_16x16x32_bf16 v[36:39], a[240:243], v[84:87], v[36:39]\n\t"
        "ds_read_b64_tr_b16 v[80:81], v159 offset:128\n\t"
        "v_mfma_f32_16x16x32_bf16 v[48:51], v[60:63], v[164:167], 0\n\t"
        "ds_read_b64_tr_b16 v[82:83], v152 offset:128\n\t"
        "v_mfma_f32_16x16x32_bf16 v[40:43], a[244:247], v[84:87], v[40:43]\n\t"
        "ds_read_b128 v[96:99], v130 offset:16448\n\t"
        "s_waitcnt lgkmcnt(10)\n\t"
        "v_mfma_f32_16x16x32_bf16 v[52:55], v[64:67], v[164:167], 0\n\t"
        "ds_read_b64_tr_b16 v[232:233], v131 offset:49152\n\t"
        "v_mfma_f32_16x16x32_bf16 v[44:47], a[248:251], v[84:87], v[44:47]\n\t"
        "ds_read_b64_tr_b16 v[234:235], v131 offset:49920\n\t"
        "ds_read_b64_tr_b16 v[236:237], v132 offset:49152\n\t"
        "s_waitcnt lgkmcnt(10)\n\t"
        "v_mfma_f32_16x16x32_bf16 v[56:59], v[68:71], v[164:167], 0\n\t"
        "ds_read_b64_tr_b16 v[238:239], v132 offset:49920\n\t"
        "ds_read_b64_tr_b16 v[240:241], v133 offset:49152\n\t"
        "v_mfma_f32_16x16x32_bf16 v[0:3], v[208:211], v[88:91], v[0:3]\n\t"
        "ds_read_b64_tr_b16 v[242:243], v133 offset:49920\n\t"
        "s_waitcnt lgkmcnt(11)\n\t"
        "v_mfma_f32_16x16x32_bf16 v[116:119], v[72:75], v[164:167], 0\n\t"
        "ds_read_b64_tr_b16 v[244:245], v131 offset:55296\n\t"
        "v_mfma_f32_16x16x32_bf16 v[4:7], v[212:215], v[88:91], v[4:7]\n\t"
        "ds_read_b64_tr_b16 v[246:247], v131 offset:56064\n\t"
        "s_waitcnt lgkmcnt(11)\n\t"
        "v_mfma_f32_16x16x32_bf16 v[120:123], v[76:79], v[164:167], 0\n\t"
        "ds_read_b64_tr_b16 v[248:249], v132 offset:55296\n\t"
        "v_mfma_f32_16x16x32_bf16 v[8:11], v[216:219], v[88:91], v[8:11]\n\t"
        "ds_read_b64_tr_b16 v[250:251], v132 offset:56064\n\t"
        "s_waitcnt lgkmcnt(11)\n\t"
        "v_mfma_f32_16x16x32_bf16 v[124:127], v[80:83], v[164:167], 0\n\t"
        "ds_read_b64_tr_b16 v[252:253], v133 offset:55296\n\t"
        "s_cbranch_vccnz .Lx48%=\n\t"
        ".Lx49%=:\n\t"
        "v_mfma_f32_16x16x32_bf16 v[12:15], a[252:255], v[88:91], v[12:15]\n\t"
        "ds_read_b128 v[100:103], v130 offset:128\n\t"
        "v_cvt_pk_bf16_f32 v48, v48, v49\n\t"
        "v_mfma_f32_16x16x32_bf16 v[16:19], v[188:191], v[88:91], v[16:19]\n\t"
        "ds_read_b64_tr_b16 v[254:255], v133 offset:56064\n\t"
        "v_cvt_pk_bf16_f32 v49, v50, v51\n\t"
        "v_mfma_f32_16x16x32_bf16 v[20:23], v[192:195], v[88:91], v[20:23]\n\t"
        "ds_write_b64 v186, v[48:49] offset:12288\n\t"
        "v_mfma_f32_16x16x32_bf16 v[24:27], v[196:199], v[88:91], v[24:27]\n\t"
        "ds_read_b128 v[104:107], v130 offset:4224\n\t"
        "v_cvt_pk_bf16_f32 v52, v52, v53\n\t"
        "v_mfma_f32_16x16x32_bf16 v[28:31], v[200:203], v[88:91], v[28:31]\n\t"
        "v_cvt_pk_bf16_f32 v53, v54, v55\n\t"
        "v_cvt_pk_bf16_f32 v56, v56, v57\n\t"
        "v_mfma_f32_16x16x32_bf16 v[32:35], v[204:207], v[88:91], v[32:35]\n\t"
        "ds_write_b64 v154, v[52:53] offset:12288\n\t"
        "v_mfma_f32_16x16x32_bf16 v[12:15], v[208:211], v[92:95], v[12:15]\n\t"
        "v_cvt_pk_bf16_f32 v57, v58, v59\n\t"
        "v_cvt_pk_bf16_f32 v116, v116, v117\n\t"
        "v_mfma_f32_16x16x32_bf16 v[16:19], v[212:215], v[92:95], v[16:19]\n\t"
        "ds_write_b64 v186, v[56:57] offset:12352\n\t"
        "v_mfma_f32_16x16x32_bf16 v[20:23], v[216:219], v[92:95], v[20:23]\n\t"
        "v_cvt_pk_bf16_f32 v117, v118, v119\n\t"
        "v_cvt_pk_bf16_f32 v120, v120, v121\n\t"
        "v_mfma_f32_16x16x32_bf16 v[24:27], a[252:255], v[92:95], v[24:27]\n\t"
        "ds_write_b64 v154, v[116:117] offset:12352\n\t"
        "v_mfma_f32_16x16x32_bf16 v[28:31], v[188:191], v[92:95], v[28:31]\n\t"
        "ds_read_b128 v[108:111], v130 offset:8320\n\t"
        "v_cvt_pk_bf16_f32 v121, v122, v123\n\t"
        "v_mfma_f32_16x16x32_bf16 v[32:35], v[192:195], v[92:95], v[32:35]\n\t"
        "ds_write_b64 v186, v[120:121] offset:12416\n\t"
        "v_mfma_f32_16x16x32_bf16 v[36:39], v[196:199], v[92:95], v[36:39]\n\t"
        "v_cvt_pk_bf16_f32 v124, v124, v125\n\t"
        "v_cvt_pk_bf16_f32 v125, v126, v127\n\t"
        "v_mfma_f32_16x16x32_bf16 v[40:43], v[200:203], v[92:95], v[40:43]\n\t"
        "ds_write_b64 v154, v[124:125] offset:12416\n\t"
        "v_mfma_f32_16x16x32_bf16 v[44:47], v[204:207], v[92:95], v[44:47]\n\t"
        "s_waitcnt lgkmcnt(6)\n\t"
        "v_mfma_f32_16x16x32_bf16 v[24:27], v[208:211], v[96:99], v[24:27]\n\t"
        "v_mfma_f32_16x16x32_bf16 v[28:31], v[212:215], v[96:99], v[28:31]\n\t"
        "v_mfma_f32_16x16x32_bf16 v[32:35], v[216:219], v[96:99], v[32:35]\n\t"
        "v_mfma_f32_16x16x32_bf16 v[36:39], a[252:255], v[96:99], v[36:39]\n\t"
        "ds_read_b128 v[112:115], v130 offset:12416\n\t"
        "v_mfma_f32_16x16x32_bf16 v[40:43], v[188:191], v[96:99], v[40:43]\n\t"
        "s_waitcnt lgkmcnt(1)\n\t"
        "s_barrier\n\t"
        "v_mfma_f32_16x16x32_bf16 v[44:47], v[192:195], v[96:99], v[44:47]\n\t"
        "ds_read_b128 v[124:127], v182 offset:0\n\t"
        "v_mfma_f32_16x16x32_bf16 v[0:3], v[232:235], v[100:103], v[0:3]\n\t"
        "ds_read_b128 v[132:135], v187 offset:0\n\t"
        "v_mfma_f32_16x16x32_bf16 v[4:7], v[236:239], v[100:103], v[4:7]\n\t"
        "ds_read_b128 v[136:139], v187 offset:16\n\t"
        "v_mfma_f32_16x16x32_bf16 v[8:11], v[240:243], v[100:103], v[8:11]\n\t"
        "ds_read_b32 v140, v187 offset:32\n\t"
        "v_mfma_f32_16x16x32_bf16 v[0:3], v[220:223], v[104:107], v[0:3]\n\t"
        "v_mfma_f32_16x16x32_bf16 v[4:7], v[224:227], v[104:107], v[4:7]\n\t"
        "v_mfma_f32_16x16x32_bf16 v[8:11], v[228:231], v[104:107], v[8:11]\n\t"
        "v_mfma_f32_16x16x32_bf16 v[12:15], v[232:235], v[104:107], v[12:15]\n\t"
        "ds_read_b128 v[84:87], v130 offset:16512\n\t"
        "v_mfma_f32_16x16x32_bf16 v[16:19], v[236:239], v[104:107], v[16:19]\n\t"
        "v_mfma_f32_16x16x32_bf16 v[20:23], v[240:243], v[104:107], v[20:23]\n\t"
        "s_waitcnt lgkmcnt(4)\n\t"
        "v_or3_b32 v124, v124, v125, v126\n\t"
        "v_or_b32 v124, v124, v127\n\t"
        "v_mfma_f32_16x16x32_bf16 v[0:3], v[244:247], v[108:111], v[0:3]\n\t"
        "v_readfirstlane_b32 s6, v124\n\t"
        "v_mfma_f32_16x16x32_bf16 v[4:7], v[248:251], v[108:111], v[4:7]\n\t"
        "s_nop 0\n\t"
        "s_bitcmp1_b32 s6, 1\n\t"
        "s_cbranch_scc1 .Lx50%=\n\t"
        ".Lx51%=:\n\t"
        "v_mfma_f32_16x16x32_bf16 v[8:11], v[252:255], v[108:111], v[8:11]\n\t"
        "ds_read_b128 v[160:163], v185 offset:512\n\t"
        "s_waitcnt lgkmcnt(3)\n\t"
        "v_xor_b32 v132, v181, v132\n\t"
        "v_mfma_f32_16x16x32_bf16 v[12:15], v[220:223], v[108:111], v[12:15]\n\t"
        "ds_read_b128 v[88:91], v130 offset:20544\n\t"
        "v_add_u32 v151, s9, v172\n\t"
        "v_mfma_f32_16x16x32_bf16 v[16:19], v[224:227], v[108:111], v[16:19]\n\t"
        "v_max_u32 v151, v151, v132\n\t"
        "v_xor_b32 v133, v181, v133\n\t"
        "v_mfma_f32_16x16x32_bf16 v[20:23], v[228:231], v[108:111], v[20:23]\n\t"
        "v_add_u32 v183, s9, v173\n\t"
        "v_max_u32 v183, v183, v133\n\t"
        "v_mfma_f32_16x16x32_bf16 v[24:27], v[232:235], v[108:111], v[24:27]\n\t"
        "ds_read_b128 v[92:95], v130 offset:20608\n\t"
        "v_xor_b32 v134, v181, v134\n\t"
        "s_add_u32 s55, s41, 0\n\t"
        "v_mfma_f32_16x16x32_bf16 v[28:31], v[236:239], v[108:111], v[28:31]\n\t"
        "v_cvt_pk_bf16_f32 v116, v0, v1\n\t"
        "v_cvt_pk_bf16_f32 v117, v2, v3\n\t"
        "v_mfma_f32_16x16x32_bf16 v[32:35], v[240:243], v[108:111], v[32:35]\n\t"
        "v_cvt_pk_bf16_f32 v118, v4, v5\n\t"
        "v_cvt_pk_bf16_f32 v119, v6, v7\n\t"
        "v_mfma_f32_16x16x32_bf16 v[12:15], v[244:247], v[112:115], v[12:15]\n\t"
        "ds_read_b128 v[96:99], v151 offset:0\n\t"
        "v_cvt_pk_bf16_f32 v120, v8, v9\n\t"
        "v_mfma_f32_16x16x32_bf16 v[16:19], v[248:251], v[112:115], v[16:19]\n\t"
        "v_cvt_pk_bf16_f32 v121, v10, v11\n\t"
        "v_mfma_f32_16x16x32_bf16 v[20:23], v[252:255], v[112:115], v[20:23]\n\t"
        "v_permlane16_swap_b32 v116, v118\n\t"
        "s_waitcnt lgkmcnt(3)\n\t"
        "v_lshlrev_b32_sdwa v157, 5, v160 dst_sel:DWORD dst_unused:UNUSED_PAD src0_sel:DWORD src1_sel:WORD_0\n\t"
        "v_mfma_f32_16x16x32_bf16 v[24:27], v[220:223], v[112:115], v[24:27]\n\t"
        "v_permlane16_swap_b32 v117, v119\n\t"
        "v_mfma_f32_16x16x32_bf16 v[28:31], v[224:227], v[112:115], v[28:31]\n\t"
        "buffer_store_dwordx4 v[116:119], v144, s[28:31], s55 offen sc1\n\t"
        "v_mfma_f32_16x16x32_bf16 v[32:35], v[228:231], v[112:115], v[32:35]\n\t"
        "ds_write_b64 v146, v[120:121] offset:0\n\t"
        "v_mfma_f32_16x16x32_bf16 v[36:39], v[232:235], v[112:115], v[36:39]\n\t"
        "ds_read_b128 v[100:103], v151 offset:64\n\t"
        "s_add_u32 s55, s41, 43008\n\t"
        "v_mfma_f32_16x16x32_bf16 v[40:43], v[236:239], v[112:115], v[40:43]\n\t"
        "ds_read_b128 v[104:107], v151 offset:128\n\t"
        "v_add_u32 v151, s9, v174\n\t"
        "v_max_u32 v151, v151, v134\n\t"
        "v_mfma_f32_16x16x32_bf16 v[44:47], v[240:243], v[112:115], v[44:47]\n\t"
        "v_xor_b32 v135, v181, v135\n\t"
        "v_cvt_pk_bf16_f32 v116, v12, v13\n\t"
        "v_mfma_f32_16x16x32_bf16 v[24:27], v[244:247], v[84:87], v[24:27]\n\t"
        "ds_read_b128 v[108:111], v183 offset:0\n\t"
        "v_cvt_pk_bf16_f32 v117, v14, v15\n\t"
        "v_mfma_f32_16x16x32_bf16 v[28:31], v[248:251], v[84:87], v[28:31]\n\t"
        "v_cvt_pk_bf16_f32 v118, v16, v17\n\t"
        "v_cvt_pk_bf16_f32 v119, v18, v19\n\t"
        "v_mfma_f32_16x16x32_bf16 v[32:35], v[252:255], v[84:87], v[32:35]\n\t"
        "v_cvt_pk_bf16_f32 v120, v20, v21\n\t"
        "v_cvt_pk_bf16_f32 v121, v22, v23\n\t"
        "v_mfma_f32_16x16x32_bf16 v[36:39], v[220:223], v[84:87], v[36:39]\n\t"
        "v_permlane16_swap_b32 v116, v118\n\t"
        "v_lshlrev_b32_sdwa v158, 5, v160 dst_sel:DWORD dst_unused:UNUSED_PAD src0_sel:DWORD src1_sel:WORD_1\n\t"
        "v_mfma_f32_16x16x32_bf16 v[40:43], v[224:227], v[84:87], v[40:43]\n\t"
        "v_permlane16_swap_b32 v117, v119\n\t"
        "v_mfma_f32_16x16x32_bf16 v[44:47], v[228:231], v[84:87], v[44:47]\n\t"
        "ds_read_b128 v[112:115], v183 offset:64\n\t"
        "v_or_b32 v157, v184, v157\n\t"
        "s_waitcnt lgkmcnt(4)\n\t"
        "v_mfma_f32_16x16x32_bf16 v[36:39], v[208:211], v[88:91], v[36:39]\n\t"
        "ds_read_b64_tr_b16 v[60:61], v157 offset:0\n\t"
        "v_or_b32 v158, v184, v158\n\t"
        "v_mfma_f32_16x16x32_bf16 v[40:43], v[212:215], v[88:91], v[40:43]\n\t"
        "ds_read_b64_tr_b16 v[62:63], v158 offset:0\n\t"
        "v_xor_b32 v159, 32, v157\n\t"
        "v_mfma_f32_16x16x32_bf16 v[44:47], v[216:219], v[88:91], v[44:47]\n\t"
        "ds_read_b64_tr_b16 v[64:65], v159 offset:0\n\t"
        "v_xor_b32 v152, 32, v158\n\t"
        "v_perm_b32 v164, v162, v162, v168\n\t"
        "v_mfma_f32_16x16x32_bf16 v[36:39], v[244:247], v[92:95], v[36:39]\n\t"
        "buffer_store_dwordx4 v[116:119], v144, s[28:31], s55 offen sc1\n\t"
        "v_mfma_f32_16x16x32_bf16 v[40:43], v[248:251], v[92:95], v[40:43]\n\t"
        "ds_write_b64 v146, v[120:121] offset:2048\n\t"
        "v_mfma_f32_16x16x32_bf16 v[44:47], v[252:255], v[92:95], v[44:47]\n\t"
        "ds_read_b128 v[84:87], v183 offset:128\n\t"
        "v_add_u32 v183, s9, v175\n\t"
        "v_mfma_f32_16x16x32_bf16 v[48:51], a[12:15], v[96:99], 0\n\t"
        "ds_read_b64_tr_b16 v[66:67], v152 offset:0\n\t"
        "v_max_u32 v183, v183, v135\n\t"
        "v_mfma_f32_16x16x32_bf16 v[52:55], a[16:19], v[96:99], 0\n\t"
        "ds_read_b64_tr_b16 v[68:69], v157 offset:64\n\t"
        "v_xor_b32 v136, v181, v136\n\t"
        "s_add_u32 s55, s41, 86016\n\t"
        "v_mfma_f32_16x16x32_bf16 v[56:59], a[20:23], v[96:99], 0\n\t"
        "ds_read_b128 v[88:91], v151 offset:0\n\t"
        "v_cvt_pk_bf16_f32 v116, v24, v25\n\t"
        "s_waitcnt lgkmcnt(7)\n\t"
        "v_mfma_f32_16x16x32_bf16 v[48:51], a[48:51], v[100:103], v[48:51]\n\t"
        "ds_read_b64_tr_b16 v[70:71], v158 offset:64\n\t"
        "v_cvt_pk_bf16_f32 v117, v26, v27\n\t"
        "v_mfma_f32_16x16x32_bf16 v[52:55], a[52:55], v[100:103], v[52:55]\n\t"
        "ds_read_b64_tr_b16 v[72:73], v159 offset:64\n\t"
        "v_cvt_pk_bf16_f32 v118, v28, v29\n\t"
        "v_mfma_f32_16x16x32_bf16 v[56:59], a[56:59], v[100:103], v[56:59]\n\t"
        "ds_read_b128 v[92:95], v151 offset:64\n\t"
        "v_cvt_pk_bf16_f32 v119, v30, v31\n\t"
        "v_mfma_f32_16x16x32_bf16 v[48:51], a[84:87], v[104:107], v[48:51]\n\t"
        "ds_read_b64_tr_b16 v[74:75], v152 offset:64\n\t"
        "v_cvt_pk_bf16_f32 v120, v32, v33\n\t"
        "v_cvt_pk_bf16_f32 v121, v34, v35\n\t"
        "v_mfma_f32_16x16x32_bf16 v[52:55], a[88:91], v[104:107], v[52:55]\n\t"
        "v_permlane16_swap_b32 v116, v118\n\t"
        "v_mfma_f32_16x16x32_bf16 v[56:59], a[92:95], v[104:107], v[56:59]\n\t"
        "v_permlane16_swap_b32 v117, v119\n\t"
        "v_mfma_f32_16x16x32_bf16 v[48:51], a[120:123], v[108:111], v[48:51]\n\t"
        "ds_read_b128 v[96:99], v151 offset:128\n\t"
        "v_add_u32 v151, s9, v176\n\t"
        "v_mfma_f32_16x16x32_bf16 v[52:55], a[124:127], v[108:111], v[52:55]\n\t"
        "ds_read_b64_tr_b16 v[76:77], v157 offset:128\n\t"
        "v_max_u32 v151, v151, v136\n\t"
        "v_mfma_f32_16x16x32_bf16 v[56:59], a[128:131], v[108:111], v[56:59]\n\t"
        "buffer_store_dwordx4 v[116:119], v144, s[28:31], s55 offen sc1\n\t"
        "v_mfma_f32_16x16x32_bf16 v[48:51], a[156:159], v[112:115], v[48:51]\n\t"
        "ds_write_b64 v146, v[120:121] offset:4096\n\t"
        "v_mfma_f32_16x16x32_bf16 v[52:55], a[160:163], v[112:115], v[52:55]\n\t"
        "ds_read_b128 v[100:103], v183 offset:0\n\t"
        "v_xor_b32 v137, v181, v137\n\t"
        "s_add_u32 s55, s41, 129024\n\t"
        "v_mfma_f32_16x16x32_bf16 v[56:59], a[164:167], v[112:115], v[56:59]\n\t"
        "ds_read_b128 v[104:107], v183 offset:64\n\t"
        "v_cvt_pk_bf16_f32 v116, v36, v37\n\t"
        "s_waitcnt lgkmcnt(5)\n\t"
        "v_mfma_f32_16x16x32_bf16 v[48:51], a[192:195], v[84:87], v[48:51]\n\t"
        "ds_read_b64_tr_b16 v[78:79], v158 offset:128\n\t"
        "v_cvt_pk_bf16_f32 v117, v38, v39\n\t"
        "v_mfma_f32_16x16x32_bf16 v[52:55], a[196:199], v[84:87], v[52:55]\n\t"
        "ds_read_b64_tr_b16 v[80:81], v159 offset:128\n\t"
        "v_cvt_pk_bf16_f32 v118, v40, v41\n\t"
        "v_mfma_f32_16x16x32_bf16 v[56:59], a[200:203], v[84:87], v[56:59]\n\t"
        "ds_read_b128 v[108:111], v183 offset:128\n\t"
        "v_add_u32 v183, s9, v177\n\t"
        "v_max_u32 v183, v183, v137\n\t"
        "v_mfma_f32_16x16x32_bf16 v[48:51], a[228:231], v[88:91], v[48:51]\n\t"
        "ds_read_b64_tr_b16 v[82:83], v152 offset:128\n\t"
        "v_xor_b32 v138, v181, v138\n\t"
        "v_mfma_f32_16x16x32_bf16 v[52:55], a[232:235], v[88:91], v[52:55]\n\t"
        "ds_read_b128 v[112:115], v151 offset:0\n\t"
        "v_cvt_pk_bf16_f32 v119, v42, v43\n\t"
        "v_mfma_f32_16x16x32_bf16 v[56:59], a[236:239], v[88:91], v[56:59]\n\t"
        "ds_read_b128 v[84:87], v151 offset:64\n\t"
        "v_cvt_pk_bf16_f32 v120, v44, v45\n\t"
        "v_mfma_f32_16x16x32_bf16 v[48:51], v[196:199], v[92:95], v[48:51]\n\t"
        "ds_read_b128 v[88:91], v151 offset:128\n\t"
        "v_add_u32 v151, s9, v178\n\t"
        "v_mfma_f32_16x16x32_bf16 v[52:55], v[200:203], v[92:95], v[52:55]\n\t"
        "v_max_u32 v151, v151, v138\n\t"
        "v_xor_b32 v139, v181, v139\n\t"
        "v_cvt_pk_bf16_f32 v121, v46, v47\n\t"
        "v_mfma_f32_16x16x32_bf16 v[56:59], v[204:207], v[92:95], v[56:59]\n\t"
        "v_permlane16_swap_b32 v116, v118\n\t"
        "s_waitcnt lgkmcnt(7)\n\t"
        "v_mfma_f32_16x16x32_bf16 v[48:51], v[232:235], v[96:99], v[48:51]\n\t"
        "v_permlane16_swap_b32 v117, v119\n\t"
        "v_mfma_f32_16x16x32_bf16 v[52:55], v[236:239], v[96:99], v[52:55]\n\t"
        "buffer_store_dwordx4 v[116:119], v144, s[28:31], s55 offen sc1\n\t"
        "v_mfma_f32_16x16x32_bf16 v[56:59], v[240:243], v[96:99], v[56:59]\n\t"
        "ds_write_b64 v146, v[120:121] offset:6144\n\t"
        "v_mfma_f32_16x16x32_bf16 v[48:51], a[0:3], v[100:103], v[48:51]\n\t"
        "ds_read_b128 v[92:95], v183 offset:0\n\t"
        "v_perm_b32 v165, v162, v162, v169\n\t"
        "v_perm_b32 v166, v162, v162, v170\n\t"
        "v_mfma_f32_16x16x32_bf16 v[52:55], a[4:7], v[100:103], v[52:55]\n\t"
        "ds_read_b128 v[96:99], v183 offset:64\n\t"
        "v_perm_b32 v167, v162, v162, v171\n\t"
        "v_mfma_f32_16x16x32_bf16 v[56:59], a[8:11], v[100:103], v[56:59]\n\t"
        "v_cmp_ne_u32 vcc, 0, v163\n\t"
        "v_mfma_f32_16x16x32_bf16 v[0:3], v[60:63], v[164:167], 0\n\t"
        "v_mfma_f32_16x16x32_bf16 v[48:51], a[36:39], v[104:107], v[48:51]\n\t"
        "ds_read_b128 v[100:103], v183 offset:128\n\t"
        "v_add_u32 v183, s9, v179\n\t"
        "v_mfma_f32_16x16x32_bf16 v[4:7], v[64:67], v[164:167], 0\n\t"
        "v_max_u32 v183, v183, v139\n\t"
        "v_mfma_f32_16x16x32_bf16 v[52:55], a[40:43], v[104:107], v[52:55]\n\t"
        "v_mfma_f32_16x16x32_bf16 v[8:11], v[68:71], v[164:167], 0\n\t"
        "v_mfma_f32_16x16x32_bf16 v[56:59], a[44:47], v[104:107], v[56:59]\n\t"
        "v_mfma_f32_16x16x32_bf16 v[12:15], v[72:75], v[164:167], 0\n\t"
        "s_waitcnt lgkmcnt(3)\n\t"
        "v_mfma_f32_16x16x32_bf16 v[48:51], a[72:75], v[108:111], v[48:51]\n\t"
        "ds_read_b128 v[104:107], v151 offset:0\n\t"
        "v_mfma_f32_16x16x32_bf16 v[16:19], v[76:79], v[164:167], 0\n\t"
        "v_mfma_f32_16x16x32_bf16 v[52:55], a[76:79], v[108:111], v[52:55]\n\t"
        "v_mfma_f32_16x16x32_bf16 v[20:23], v[80:83], v[164:167], 0\n\t"
        "s_cbranch_vccnz .Lx52%=\n\t"
        ".Lx53%=:\n\t"
        "v_cvt_pk_bf16_f32 v0, v0, v1\n\t"
        "v_cvt_pk_bf16_f32 v1, v2, v3\n\t"
        "v_cvt_pk_bf16_f32 v4, v4, v5\n\t"
        "v_mfma_f32_16x16x32_bf16 v[56:59], a[80:83], v[108:111], v[56:59]\n\t"
        "ds_write_b64 v186, v[0:1] offset:24576\n\t"
        "v_mfma_f32_16x16x32_bf16 v[48:51], a[108:111], v[112:115], v[48:51]\n\t"
        "ds_read_b128 v[108:111], v151 offset:64\n\t"
        "v_cvt_pk_bf16_f32 v5, v6, v7\n\t"
        "v_cvt_pk_bf16_f32 v8, v8, v9\n\t"
        "v_mfma_f32_16x16x32_bf16 v[52:55], a[112:115], v[112:115], v[52:55]\n\t"
        "ds_write_b64 v154, v[4:5] offset:24576\n\t"
        "v_mfma_f32_16x16x32_bf16 v[56:59], a[116:119], v[112:115], v[56:59]\n\t"
        "v_cvt_pk_bf16_f32 v9, v10, v11\n\t"
        "v_cvt_pk_bf16_f32 v12, v12, v13\n\t"
        "v_mfma_f32_16x16x32_bf16 v[48:51], a[144:147], v[84:87], v[48:51]\n\t"
        "ds_write_b64 v186, v[8:9] offset:24640\n\t"
        "v_mfma_f32_16x16x32_bf16 v[52:55], a[148:151], v[84:87], v[52:55]\n\t"
        "ds_read_b128 v[112:115], v151 offset:128\n\t"
        "v_cvt_pk_bf16_f32 v13, v14, v15\n\t"
        "v_cvt_pk_bf16_f32 v16, v16, v17\n\t"
        "v_mfma_f32_16x16x32_bf16 v[56:59], a[152:155], v[84:87], v[56:59]\n\t"
        "ds_write_b64 v154, v[12:13] offset:24640\n\t"
        "v_mfma_f32_16x16x32_bf16 v[48:51], a[180:183], v[88:91], v[48:51]\n\t"
        "ds_read_b128 v[84:87], v183 offset:0\n\t"
        "v_cvt_pk_bf16_f32 v17, v18, v19\n\t"
        "v_mfma_f32_16x16x32_bf16 v[52:55], a[184:187], v[88:91], v[52:55]\n\t"
        "ds_write_b64 v186, v[16:17] offset:24704\n\t"
        "v_mfma_f32_16x16x32_bf16 v[56:59], a[188:191], v[88:91], v[56:59]\n\t"
        "v_cvt_pk_bf16_f32 v20, v20, v21\n\t"
        "v_cvt_pk_bf16_f32 v21, v22, v23\n\t"
        "s_waitcnt lgkmcnt(6)\n\t"
        "v_mfma_f32_16x16x32_bf16 v[48:51], a[216:219], v[92:95], v[48:51]\n\t"
        "ds_write_b64 v154, v[20:21] offset:24704\n\t"
        "v_mfma_f32_16x16x32_bf16 v[52:55], a[220:223], v[92:95], v[52:55]\n\t"
        "s_cmp_gt_u32 s61, 48\n\t"
        "s_cbranch_scc1 .Lx54%=\n\t"
        ".Lx55%=:\n\t"
        "v_mfma_f32_16x16x32_bf16 v[56:59], a[224:227], v[92:95], v[56:59]\n\t"
        "ds_read_b128 v[88:91], v183 offset:64\n\t"
        "v_mfma_f32_16x16x32_bf16 v[48:51], a[252:255], v[96:99], v[48:51]\n\t"
        "ds_read_b128 v[92:95], v183 offset:128\n\t"
        "v_mfma_f32_16x16x32_bf16 v[52:55], v[188:191], v[96:99], v[52:55]\n\t"
        "v_mfma_f32_16x16x32_bf16 v[56:59], v[192:195], v[96:99], v[56:59]\n\t"
        "v_mfma_f32_16x16x32_bf16 v[48:51], v[220:223], v[100:103], v[48:51]\n\t"
        "s_waitcnt lgkmcnt(2)\n\t"
        "s_barrier\n\t"
        "v_xor_b32 v140, v181, v140\n\t"
        "v_mfma_f32_16x16x32_bf16 v[52:55], v[224:227], v[100:103], v[52:55]\n\t"
        "ds_read_b128 v[132:135], v187 offset:4096\n\t"
        "v_mfma_f32_16x16x32_bf16 v[56:59], v[228:231], v[100:103], v[56:59]\n\t"
        "ds_read_b128 v[136:139], v187 offset:4112\n\t"
        "v_add_u32 v151, s9, v180\n\t"
        "v_mfma_f32_16x16x32_bf16 v[48:51], a[24:27], v[104:107], v[48:51]\n\t"
        "ds_read_b128 v[124:127], v182 offset:16\n\t"
        "v_max_u32 v151, v151, v140\n\t"
        "v_mfma_f32_16x16x32_bf16 v[52:55], a[28:31], v[104:107], v[52:55]\n\t"
        "ds_read_b32 v140, v187 offset:4128\n\t"
        "s_waitcnt lgkmcnt(3)\n\t"
        "v_xor_b32 v132, v181, v132\n\t"
        "v_mfma_f32_16x16x32_bf16 v[56:59], a[32:35], v[104:107], v[56:59]\n\t"
        "ds_read_b128 v[96:99], v151 offset:0\n\t"
        "v_readlane_b32 s9, v155, 48\n\t"
        "v_mfma_f32_16x16x32_bf16 v[48:51], a[60:63], v[108:111], v[48:51]\n\t"
        "ds_read_b128 v[100:103], v151 offset:64\n\t"
        "v_add_u32 v183, s9, v172\n\t"
        "v_mfma_f32_16x16x32_bf16 v[52:55], a[64:67], v[108:111], v[52:55]\n\t"
        "ds_read_b128 v[104:107], v151 offset:128\n\t"
        "v_max_u32 v183, v183, v132\n\t"
        "v_mfma_f32_16x16x32_bf16 v[56:59], a[68:71], v[108:111], v[56:59]\n\t"
        "v_xor_b32 v133, v181, v133\n\t"
        "v_add_u32 v151, s9, v173\n\t"
        "v_mfma_f32_16x16x32_bf16 v[48:51], a[96:99], v[112:115], v[48:51]\n\t"
        "ds_read_b128 v[108:111], v183 offset:0\n\t"
        "v_max_u32 v151, v151, v133\n\t"
        "v_mfma_f32_16x16x32_bf16 v[52:55], a[100:103], v[112:115], v[52:55]\n\t"
        "v_xor_b32 v134, v181, v134\n\t"
        "v_mfma_f32_16x16x32_bf16 v[56:59], a[104:107], v[112:115], v[56:59]\n\t"
        "v_mfma_f32_16x16x32_bf16 v[48:51], a[132:135], v[84:87], v[48:51]\n\t"
        "ds_read_b128 v[112:115], v183 offset:64\n\t"
        "s_waitcnt lgkmcnt(6)\n\t"
        "v_or3_b32 v124, v124, v125, v126\n\t"
        "v_mfma_f32_16x16x32_bf16 v[52:55], a[136:139], v[84:87], v[52:55]\n\t"
        "v_or_b32 v124, v124, v127\n\t"
        "v_mfma_f32_16x16x32_bf16 v[56:59], a[140:143], v[84:87], v[56:59]\n\t"
        "v_readfirstlane_b32 s8, v124\n\t"
        "v_mfma_f32_16x16x32_bf16 v[48:51], a[168:171], v[88:91], v[48:51]\n\t"
        "ds_read_b128 v[124:127], v147 offset:0\n\t"
        "s_or_b32 s6, s6, s8\n\t"
        "v_mfma_f32_16x16x32_bf16 v[52:55], a[172:175], v[88:91], v[52:55]\n\t"
        "ds_read_b128 v[84:87], v183 offset:128\n\t"
        "v_add_u32 v183, s9, v174\n\t"
        "v_mfma_f32_16x16x32_bf16 v[56:59], a[176:179], v[88:91], v[56:59]\n\t"
        "v_max_u32 v183, v183, v134\n\t"
        "v_xor_b32 v135, v181, v135\n\t"
        "v_mfma_f32_16x16x32_bf16 v[48:51], a[204:207], v[92:95], v[48:51]\n\t"
        "ds_read_b128 v[88:91], v151 offset:0\n\t"
        "v_mfma_f32_16x16x32_bf16 v[52:55], a[208:211], v[92:95], v[52:55]\n\t"
        "v_mfma_f32_16x16x32_bf16 v[56:59], a[212:215], v[92:95], v[56:59]\n\t"
        "s_waitcnt lgkmcnt(4)\n\t"
        "v_mfma_f32_16x16x32_bf16 v[48:51], a[240:243], v[96:99], v[48:51]\n\t"
        "ds_read_b128 v[92:95], v151 offset:64\n\t"
        "v_mfma_f32_16x16x32_bf16 v[52:55], a[244:247], v[96:99], v[52:55]\n\t"
        "v_mfma_f32_16x16x32_bf16 v[56:59], a[248:251], v[96:99], v[56:59]\n\t"
        "v_mfma_f32_16x16x32_bf16 v[48:51], v[208:211], v[100:103], v[48:51]\n\t"
        "s_waitcnt lgkmcnt(3)\n\t"
        "buffer_store_dwordx4 v[124:127], v145, s[28:31], s50 offen sc1\n\t"
        "v_mfma_f32_16x16x32_bf16 v[52:55], v[212:215], v[100:103], v[52:55]\n\t"
        "ds_read_b128 v[124:127], v147 offset:4096\n\t"
        "s_add_u32 s50, s50, 86016\n\t"
        "v_mfma_f32_16x16x32_bf16 v[56:59], v[216:219], v[100:103], v[56:59]\n\t"
        "ds_read_b128 v[96:99], v151 offset:128\n\t"
        "v_add_u32 v151, s9, v175\n\t"
        "v_mfma_f32_16x16x32_bf16 v[48:51], v[244:247], v[104:107], v[48:51]\n\t"
        "ds_read_b128 v[100:103], v183 offset:0\n\t"
        "v_max_u32 v151, v151, v135\n\t"
        "v_mfma_f32_16x16x32_bf16 v[52:55], v[248:251], v[104:107], v[52:55]\n\t"
        "v_xor_b32 v136, v181, v136\n\t"
        "v_mfma_f32_16x16x32_bf16 v[56:59], v[252:255], v[104:107], v[56:59]\n\t"
        "v_mfma_f32_16x16x32_bf16 v[72:75], a[12:15], v[108:111], 0\n\t"
        "ds_read_b128 v[104:107], v183 offset:64\n\t"
        "v_mfma_f32_16x16x32_bf16 v[76:79], a[16:19], v[108:111], 0\n\t"
        "v_mfma_f32_16x16x32_bf16 v[80:83], a[20:23], v[108:111], 0\n\t"
        "v_mfma_f32_16x16x32_bf16 v[72:75], a[48:51], v[112:115], v[72:75]\n\t"
        "ds_read_b128 v[108:111], v183 offset:128\n\t"
        "v_add_u32 v183, s9, v176\n\t"
        "v_mfma_f32_16x16x32_bf16 v[76:79], a[52:55], v[112:115], v[76:79]\n\t"
        "v_max_u32 v183, v183, v136\n\t"
        "v_xor_b32 v137, v181, v137\n\t"
        "v_mfma_f32_16x16x32_bf16 v[80:83], a[56:59], v[112:115], v[80:83]\n\t"
        "s_waitcnt lgkmcnt(4)\n\t"
        "v_mfma_f32_16x16x32_bf16 v[72:75], a[84:87], v[84:87], v[72:75]\n\t"
        "ds_read_b128 v[112:115], v151 offset:0\n\t"
        "v_mfma_f32_16x16x32_bf16 v[76:79], a[88:91], v[84:87], v[76:79]\n\t"
        "v_mfma_f32_16x16x32_bf16 v[80:83], a[92:95], v[84:87], v[80:83]\n\t"
        "buffer_store_dwordx4 v[124:127], v145, s[28:31], s50 offen sc1\n\t"
        "v_mfma_f32_16x16x32_bf16 v[72:75], a[120:123], v[88:91], v[72:75]\n\t"
        "ds_read_b128 v[84:87], v151 offset:64\n\t"
        "v_readlane_b32 s55, v155, 49\n\t"
        "v_mfma_f32_16x16x32_bf16 v[76:79], a[124:127], v[88:91], v[76:79]\n\t"
        "v_cvt_pk_bf16_f32 v116, v48, v49\n\t"
        "v_cvt_pk_bf16_f32 v117, v50, v51\n\t"
        "v_mfma_f32_16x16x32_bf16 v[80:83], a[128:131], v[88:91], v[80:83]\n\t"
        "v_cvt_pk_bf16_f32 v118, v52, v53\n\t"
        "v_cvt_pk_bf16_f32 v119, v54, v55\n\t"
        "v_mfma_f32_16x16x32_bf16 v[72:75], a[156:159], v[92:95], v[72:75]\n\t"
        "ds_read_b128 v[88:91], v151 offset:128\n\t"
        "v_add_u32 v151, s9, v177\n\t"
        "v_mfma_f32_16x16x32_bf16 v[76:79], a[160:163], v[92:95], v[76:79]\n\t"
        "v_max_u32 v151, v151, v137\n\t"
        "v_xor_b32 v138, v181, v138\n\t"
        "v_mfma_f32_16x16x32_bf16 v[80:83], a[164:167], v[92:95], v[80:83]\n\t"
        "v_cvt_pk_bf16_f32 v120, v56, v57\n\t"
        "v_cvt_pk_bf16_f32 v121, v58, v59\n\t"
        "s_waitcnt lgkmcnt(3)\n\t"
        "v_mfma_f32_16x16x32_bf16 v[72:75], a[192:195], v[96:99], v[72:75]\n\t"
        "v_permlane16_swap_b32 v116, v118\n\t"
        "v_mfma_f32_16x16x32_bf16 v[76:79], a[196:199], v[96:99], v[76:79]\n\t"
        "v_permlane16_swap_b32 v117, v119\n\t"
        "v_mfma_f32_16x16x32_bf16 v[80:83], a[200:203], v[96:99], v[80:83]\n\t"
        "ds_read_b128 v[92:95], v183 offset:0\n\t"
        "v_mfma_f32_16x16x32_bf16 v[72:75], a[228:231], v[100:103], v[72:75]\n\t"
        "ds_read_b128 v[96:99], v183 offset:64\n\t"
        "v_mfma_f32_16x16x32_bf16 v[76:79], a[232:235], v[100:103], v[76:79]\n\t"
        "v_mfma_f32_16x16x32_bf16 v[80:83], a[236:239], v[100:103], v[80:83]\n\t"
        "v_mfma_f32_16x16x32_bf16 v[72:75], v[196:199], v[104:107], v[72:75]\n\t"
        "ds_read_b128 v[100:103], v183 offset:128\n\t"
        "v_add_u32 v183, s9, v178\n\t"
        "v_mfma_f32_16x16x32_bf16 v[76:79], v[200:203], v[104:107], v[76:79]\n\t"
        "buffer_store_dwordx4 v[116:119], v148, s[28:31], s55 offen sc1\n\t"
        "v_mfma_f32_16x16x32_bf16 v[80:83], v[204:207], v[104:107], v[80:83]\n\t"
        "ds_write_b64 v146, v[120:121] offset:8192\n\t"
        "v_mfma_f32_16x16x32_bf16 v[72:75], v[232:235], v[108:111], v[72:75]\n\t"
        "ds_read_b128 v[104:107], v151 offset:0\n\t"
        "v_max_u32 v183, v183, v138\n\t"
        "v_mfma_f32_16x16x32_bf16 v[76:79], v[236:239], v[108:111], v[76:79]\n\t"
        "v_xor_b32 v139, v181, v139\n\t"
        "v_mfma_f32_16x16x32_bf16 v[80:83], v[240:243], v[108:111], v[80:83]\n\t"
        "s_waitcnt lgkmcnt(4)\n\t"
        "v_mfma_f32_16x16x32_bf16 v[72:75], a[0:3], v[112:115], v[72:75]\n\t"
        "ds_read_b128 v[108:111], v151 offset:64\n\t"
        "v_mfma_f32_16x16x32_bf16 v[76:79], a[4:7], v[112:115], v[76:79]\n\t"
        "v_mfma_f32_16x16x32_bf16 v[80:83], a[8:11], v[112:115], v[80:83]\n\t"
        "v_mfma_f32_16x16x32_bf16 v[72:75], a[36:39], v[84:87], v[72:75]\n\t"
        "ds_read_b128 v[112:115], v151 offset:128\n\t"
        "v_add_u32 v151, s9, v179\n\t"
        "v_mfma_f32_16x16x32_bf16 v[76:79], a[40:43], v[84:87], v[76:79]\n\t"
        "v_max_u32 v151, v151, v139\n\t"
        "v_xor_b32 v140, v181, v140\n\t"
        "v_mfma_f32_16x16x32_bf16 v[80:83], a[44:47], v[84:87], v[80:83]\n\t"
        "v_mfma_f32_16x16x32_bf16 v[72:75], a[72:75], v[88:91], v[72:75]\n\t"
        "ds_read_b128 v[84:87], v183 offset:0\n\t"
        "v_mfma_f32_16x16x32_bf16 v[76:79], a[76:79], v[88:91], v[76:79]\n\t"
        "v_mfma_f32_16x16x32_bf16 v[80:83], a[80:83], v[88:91], v[80:83]\n\t"
        "v_mfma_f32_16x16x32_bf16 v[72:75], a[108:111], v[92:95], v[72:75]\n\t"
        "ds_read_b128 v[88:91], v183 offset:64\n\t"
        "v_mfma_f32_16x16x32_bf16 v[76:79], a[112:115], v[92:95], v[76:79]\n\t"
        "v_mfma_f32_16x16x32_bf16 v[80:83], a[116:119], v[92:95], v[80:83]\n\t"
        "ds_read_b128 v[92:95], v183 offset:128\n\t"
        "v_add_u32 v183, s9, v180\n\t"
        "s_waitcnt lgkmcnt(4)\n\t"
        "v_mfma_f32_16x16x32_bf16 v[72:75], a[144:147], v[96:99], v[72:75]\n\t"
        "v_max_u32 v183, v183, v140\n\t"
        "v_mfma_f32_16x16x32_bf16 v[76:79], a[148:151], v[96:99], v[76:79]\n\t"
        "v_mfma_f32_16x16x32_bf16 v[80:83], a[152:155], v[96:99], v[80:83]\n\t"
        "ds_read_b128 v[96:99], v151 offset:0\n\t"
        "v_mfma_f32_16x16x32_bf16 v[72:75], a[180:183], v[100:103], v[72:75]\n\t"
        "v_mfma_f32_16x16x32_bf16 v[76:79], a[184:187], v[100:103], v[76:79]\n\t"
        "v_mfma_f32_16x16x32_bf16 v[80:83], a[188:191], v[100:103], v[80:83]\n\t"
        "ds_read_b128 v[100:103], v151 offset:64\n\t"
        "v_mfma_f32_16x16x32_bf16 v[72:75], a[216:219], v[104:107], v[72:75]\n\t"
        "v_mfma_f32_16x16x32_bf16 v[76:79], a[220:223], v[104:107], v[76:79]\n\t"
        "v_mfma_f32_16x16x32_bf16 v[80:83], a[224:227], v[104:107], v[80:83]\n\t"
        "ds_read_b128 v[104:107], v151 offset:128\n\t"
        "v_mfma_f32_16x16x32_bf16 v[72:75], a[252:255], v[108:111], v[72:75]\n\t"
        "v_mfma_f32_16x16x32_bf16 v[76:79], v[188:191], v[108:111], v[76:79]\n\t"
        "v_mfma_f32_16x16x32_bf16 v[80:83], v[192:195], v[108:111], v[80:83]\n\t"
        "ds_read_b128 v[108:111], v183 offset:0\n\t"
        ".LpM2%=:\n\t"
        "s_cbranch_execz .LrM%=\n\t"
        "s_waitcnt lgkmcnt(4)\n\t"
        "v_mfma_f32_16x16x32_bf16 v[72:75], v[220:223], v[112:115], v[72:75]\n\t"
        "v_mfma_f32_16x16x32_bf16 v[76:79], v[224:227], v[112:115], v[76:79]\n\t"
        "v_mfma_f32_16x16x32_bf16 v[80:83], v[228:231], v[112:115], v[80:83]\n\t"
        "ds_read_b128 v[112:115], v183 offset:64\n\t"
        "v_mfma_f32_16x16x32_bf16 v[72:75], a[24:27], v[84:87], v[72:75]\n\t"
        "v_mfma_f32_16x16x32_bf16 v[76:79], a[28:31], v[84:87], v[76:79]\n\t"
        "v_mfma_f32_16x16x32_bf16 v[80:83], a[32:35], v[84:87], v[80:83]\n\t"
        "ds_read_b128 v[84:87], v183 offset:128\n\t"
        "v_mfma_f32_16x16x32_bf16 v[80:83], a[68:71], v[88:91], v[80:83]\n\t"
        "v_mfma_f32_16x16x32_bf16 v[80:83], a[104:107], v[92:95], v[80:83]\n\t"
        "s_waitcnt lgkmcnt(3)\n\t"
        "v_mfma_f32_16x16x32_bf16 v[80:83], a[140:143], v[96:99], v[80:83]\n\t"
        "v_mfma_f32_16x16x32_bf16 v[80:83], a[176:179], v[100:103], v[80:83]\n\t"
        "v_mfma_f32_16x16x32_bf16 v[80:83], a[212:215], v[104:107], v[80:83]\n\t"
        "s_waitcnt lgkmcnt(2)\n\t"
        "v_mfma_f32_16x16x32_bf16 v[80:83], a[248:251], v[108:111], v[80:83]\n\t"
        "s_waitcnt lgkmcnt(1)\n\t"
        "v_mfma_f32_16x16x32_bf16 v[80:83], v[216:219], v[112:115], v[80:83]\n\t"
        "s_waitcnt lgkmcnt(0)\n\t"
        "v_mfma_f32_16x16x32_bf16 v[80:83], v[252:255], v[84:87], v[80:83]\n\t"
        "v_mfma_f32_16x16x32_bf16 v[72:75], a[60:63], v[88:91], v[72:75]\n\t"
        "v_mfma_f32_16x16x32_bf16 v[76:79], a[64:67], v[88:91], v[76:79]\n\t"
        "s_nop 5\n\t"
        "v_cvt_pk_bf16_f32 v120, v80, v81\n\t"
        "v_cvt_pk_bf16_f32 v121, v82, v83\n\t"
        "v_mfma_f32_16x16x32_bf16 v[72:75], a[96:99], v[92:95], v[72:75]\n\t"
        "ds_write_b64 v146, v[120:121] offset:10240\n\t"
        "v_mfma_f32_16x16x32_bf16 v[76:79], a[100:103], v[92:95], v[76:79]\n\t"
        "v_mfma_f32_16x16x32_bf16 v[72:75], a[132:135], v[96:99], v[72:75]\n\t"
        "v_mfma_f32_16x16x32_bf16 v[76:79], a[136:139], v[96:99], v[76:79]\n\t"
        "s_waitcnt lgkmcnt(0)\n\t"
        "s_barrier\n\t"
        "v_mfma_f32_16x16x32_bf16 v[72:75], a[168:171], v[100:103], v[72:75]\n\t"
        "ds_read_b128 v[124:127], v147 offset:8192\n\t"
        "v_readlane_b32 s55, v155, 51\n\t"
        "v_mfma_f32_16x16x32_bf16 v[76:79], a[172:175], v[100:103], v[76:79]\n\t"
        "v_mfma_f32_16x16x32_bf16 v[72:75], a[204:207], v[104:107], v[72:75]\n\t"
        "v_mfma_f32_16x16x32_bf16 v[76:79], a[208:211], v[104:107], v[76:79]\n\t"
        "v_mfma_f32_16x16x32_bf16 v[72:75], a[240:243], v[108:111], v[72:75]\n\t"
        "v_mfma_f32_16x16x32_bf16 v[76:79], a[244:247], v[108:111], v[76:79]\n\t"
        "v_mfma_f32_16x16x32_bf16 v[72:75], v[208:211], v[112:115], v[72:75]\n\t"
        "v_mfma_f32_16x16x32_bf16 v[76:79], v[212:215], v[112:115], v[76:79]\n\t"
        "v_mfma_f32_16x16x32_bf16 v[72:75], v[244:247], v[84:87], v[72:75]\n\t"
        "v_mfma_f32_16x16x32_bf16 v[76:79], v[248:251], v[84:87], v[76:79]\n\t"
        "s_nop 6\n\t"
        "v_cvt_pk_bf16_f32 v116, v72, v73\n\t"
        "v_cvt_pk_bf16_f32 v117, v74, v75\n\t"
        "v_cvt_pk_bf16_f32 v118, v76, v77\n\t"
        "v_cvt_pk_bf16_f32 v119, v78, v79\n\t"
        "s_nop 0\n\t"
        "v_permlane16_swap_b32 v116, v118\n\t"
        "v_permlane16_swap_b32 v117, v119\n\t"
        "buffer_store_dwordx4 v[116:119], v148, s[28:31], s55 offen  \n\t"
        "s_waitcnt lgkmcnt(0)\n\t"
        "buffer_store_dwordx4 v[124:127], v150, s[28:31], s52 offen  \n\t"
        "s_andn2_b32 s6, s6, 2\n\t"
        "s_cbranch_scc1 99f\n\t"
        "s_endpgm\n\t"
        "20:\n\t"
        "ds_bpermute_b32 v64, v65, v155 offset:172\n\t"
        "s_add_u32 m0, s37, -24576\n\t"
        "s_add_u32 s36, s36, 6144\n\t"
        "buffer_load_dwordx4 v140, s[24:27], s36 offen offset:0 lds\n\t"
        "ds_bpermute_b32 v78, v81, v155 offset:148\n\t"
        "ds_bpermute_b32 v79, v81, v155 offset:160\n\t"
        "v_readlane_b32 s71, v155, 26\n\t"
        "v_readlane_b32 s10, v155, 29\n\t"
        "s_nop 0\n\t"
        "v_mul_u32_u24 v73, s71, v66\n\t"
        "v_lshrrev_b32 v73, 11, v73\n\t"
        "v_lshlrev_b32 v66, 4, v66\n\t"
        "v_mad_u32_u24 v73, v73, s10, v66\n\t"
        "s_waitcnt lgkmcnt(2)\n\t"
        "v_add_u32 v64, v64, v73\n\t"
        "buffer_load_dwordx4 v141, s[24:27], s36 offen offset:1024 lds\n\t"
        "v_readlane_b32 s69, v155, 34\n\t"
        "v_readlane_b32 s70, v155, 35\n\t"
        "s_nop 0\n\t"
        "v_mad_u32_u24 v70, v52, s69, v70\n\t"
        "v_mad_u32_u24 v70, v158, s70, v70\n\t"
        "s_mul_i32 s71, s69, s68\n\t"
        "s_add_u32 s71, s71, 157728\n\t"
        "v_mad_u32_u24 v67, v82, s70, v67\n\t"
        "v_add_u32 v67, s71, v67\n\t"
        "v_readlane_b32 s11, v155, 30\n\t"
        "buffer_load_dwordx4 v142, s[24:27], s36 offen offset:2048 lds\n\t"
        "s_movk_i32 s10, 223\n\t"
        "v_mov_b32 v75, 0x7ff00000\n\t"
        "v_readlane_b32 s12, v155, 24\n\t"
        "v_readlane_b32 s13, v155, 25\n\t"
        "s_nop 0\n\t"
        "s_bfe_u32 s70, s12, 524288\n\t"
        "s_bfe_u32 s71, s13, 524288\n\t"
        "v_cmp_gt_u32 vcc, s71, v71\n\t"
        "v_add3_u32 v73, v71, s70, -2\n\t"
        "v_med3_i32 v73, v73, 0, s10\n\t"
        "s_add_u32 m0, s35, 4096\n\t"
        "s_add_u32 s43, s34, 43008\n\t"
        "buffer_load_dwordx4 v143, s[16:19], s43 offen  lds\n\t"
        "v_cndmask_b32 v74, v75, v72, vcc\n\t"
        "v_mad_u32_u24 v134, v73, s11, v74\n\t"
        "s_bfe_u32 s70, s12, 524296\n\t"
        "s_bfe_u32 s71, s13, 524296\n\t"
        "v_cmp_gt_u32 vcc, s71, v71\n\t"
        "v_add3_u32 v73, v71, s70, -2\n\t"
        "v_med3_i32 v73, v73, 0, s10\n\t"
        "v_cndmask_b32 v74, v75, v72, vcc\n\t"
        "v_mad_u32_u24 v135, v73, s11, v74\n\t"
        "s_add_u32 m0, s35, 20480\n\t"
        "s_add_u32 s43, s34, 215040\n\t"
        "buffer_load_dwordx4 v143, s[16:19], s43 offen  lds\n\t"
        "s_bfe_u32 s70, s12, 524304\n\t"
        "s_bfe_u32 s71, s13, 524304\n\t"
        "v_cmp_gt_u32 vcc, s71, v71\n\t"
        "v_add3_u32 v73, v71, s70, -2\n\t"
        "v_med3_i32 v73, v73, 0, s10\n\t"
        "v_cndmask_b32 v74, v75, v72, vcc\n\t"
        "v_mad_u32_u24 v136, v73, s11, v74\n\t"
        "s_bfe_u32 s70, s12, 524312\n\t"
        "s_bfe_u32 s71, s13, 524312\n\t"
        "v_cmp_gt_u32 vcc, s71, v71\n\t"
        "v_add3_u32 v73, v71, s70, -2\n\t"
        "v_med3_i32 v73, v73, 0, s10\n\t"
        "v_cndmask_b32 v74, v75, v72, vcc\n\t"
        "v_mad_u32_u24 v137, v73, s11, v74\n\t"
        "s_mov_b64 exec, 0\n\t"
        "s_cmp_eq_u32 s4, 0\n\t"
        "s_cbranch_scc1 .LpS0%=\n\t"
        "s_cmp_eq_u32 s4, 1\n\t"
        "s_cbranch_scc1 .LpS1%=\n\t"
        "s_cmp_eq_u32 s4, 2\n\t"
        "s_cbranch_scc1 .LpS2%=\n\t"
        ".LpS2%=:\n\t"
        ".LrS%=:\n\t"
        "s_mov_b64 exec, -1\n\t"
        "s_waitcnt vmcnt(9)\n\t"
        "s_barrier\n\t"
        "ds_read_b64_tr_b16 a[0:1], v138 offset:6144\n\t"
        "ds_read_b64_tr_b16 a[2:3], v138 offset:6912\n\t"
        "ds_read_b64_tr_b16 a[4:5], v139 offset:6144\n\t"
        "ds_read_b64_tr_b16 a[6:7], v139 offset:6912\n\t"
        "ds_read_b64_tr_b16 a[8:9], v149 offset:6144\n\t"
        "ds_read_b64_tr_b16 a[10:11], v149 offset:6912\n\t"
        "ds_read_b128 v[84:87], v128 offset:8192\n\t"
        "ds_read_b64_tr_b16 a[12:13], v138 offset:12288\n\t"
        "ds_read_b64_tr_b16 a[14:15], v138 offset:13056\n\t"
        "ds_read_b64_tr_b16 a[16:17], v139 offset:12288\n\t"
        "ds_read_b64_tr_b16 a[18:19], v139 offset:13056\n\t"
        "ds_read_b64_tr_b16 a[20:21], v149 offset:12288\n\t"
        "ds_read_b64_tr_b16 a[22:23], v149 offset:13056\n\t"
        "ds_read_b128 v[88:91], v128 offset:8192\n\t"
        "s_add_u32 m0, s37, -12288\n\t"
        "s_add_u32 s36, s36, s39\n\t"
        "s_waitcnt lgkmcnt(7)\n\t"
        "v_mfma_f32_16x16x32_bf16 v[12:15], a[0:3], v[84:87], 0\n\t"
        "buffer_load_dwordx4 v140, s[24:27], s36 offen offset:0 lds\n\t"
        "buffer_load_dwordx4 v141, s[24:27], s36 offen offset:1024 lds\n\t"
        "buffer_load_dwordx4 v142, s[24:27], s36 offen offset:2048 lds\n\t"
        "s_add_u32 m0, s35, 16384\n\t"
        "s_add_u32 s43, s34, 172032\n\t"
        "buffer_load_dwordx4 v143, s[16:19], s43 offen  lds\n\t"
        "s_add_u32 m0, s35, 32768\n\t"
        "s_add_u32 s43, s34, 344064\n\t"
        "s_min_u32 s43, s43, 9590784\n\t"
        "buffer_load_dwordx4 v143, s[16:19], s43 offen  lds\n\t"
        "s_waitcnt vmcnt(10)\n\t"
        "s_barrier\n\t"
        "ds_read_b128 v[92:95], v128 offset:12288\n\t"
        "s_add_u32 m0, s37, 0\n\t"
        "ds_read_b64_tr_b16 a[24:25], v138 offset:0\n\t"
        "s_add_u32 s36, s36, s38\n\t"
        "v_mfma_f32_16x16x32_bf16 v[16:19], a[4:7], v[84:87], 0\n\t"
        "buffer_load_dwordx4 v140, s[24:27], s36 offen offset:0 lds\n\t"
        "ds_read_b64_tr_b16 a[26:27], v138 offset:768\n\t"
        "v_readlane_b32 s44, v155, 32\n\t"
        "v_mfma_f32_16x16x32_bf16 v[20:23], a[8:11], v[84:87], 0\n\t"
        "buffer_load_dwordx4 v141, s[24:27], s36 offen offset:1024 lds\n\t"
        "ds_read_b64_tr_b16 a[28:29], v139 offset:0\n\t"
        "v_readlane_b32 s45, v155, 33\n\t"
        "s_waitcnt lgkmcnt(4)\n\t"
        "v_mfma_f32_16x16x32_bf16 v[24:27], a[12:15], v[88:91], 0\n\t"
        "ds_read_b64_tr_b16 a[30:31], v139 offset:768\n\t"
        "ds_read_b64_tr_b16 a[32:33], v149 offset:0\n\t"
        "ds_read_b64_tr_b16 a[34:35], v149 offset:768\n\t"
        "v_readlane_b32 s62, v155, 36\n\t"
        "v_mfma_f32_16x16x32_bf16 v[28:31], a[16:19], v[88:91], 0\n\t"
        "buffer_load_dwordx4 v142, s[24:27], s36 offen offset:2048 lds\n\t"
        "s_add_u32 m0, s35, 157696\n\t"
        "ds_read_b128 v[96:99], v128 offset:8192\n\t"
        "v_mfma_f32_16x16x32_bf16 v[32:35], a[20:23], v[88:91], 0\n\t"
        "ds_read_b128 v[100:103], v128 offset:12288\n\t"
        "s_lshl_b32 s70, s62, 5\n\t"
        "s_waitcnt lgkmcnt(8)\n\t"
        "v_mfma_f32_16x16x32_bf16 v[24:27], a[0:3], v[92:95], v[24:27]\n\t"
        "s_sub_u32 s71, s70, 16384\n\t"
        "s_mul_i32 s71, s71, s68\n\t"
        "v_mfma_f32_16x16x32_bf16 v[28:31], a[4:7], v[92:95], v[28:31]\n\t"
        "buffer_load_dwordx4 v64, s[20:23], 0 offen lds\n\t"
        "s_add_u32 m0, s37, 12288\n\t"
        "s_add_u32 s36, s36, s5\n\t"
        "v_mfma_f32_16x16x32_bf16 v[32:35], a[8:11], v[92:95], v[32:35]\n\t"
        "s_mov_b32 exec_lo, 0\n\t"
        "s_mov_b32 exec_hi, 0xfff00000\n\t"
        "ds_write_b32 v119, v155\n\t"
        "s_mov_b64 exec, -1\n\t"
        "v_mfma_f32_16x16x32_bf16 v[36:39], a[12:15], v[92:95], 0\n\t"
        "v_add_u32 v172, s71, v128\n\t"
        "v_add_u32 v173, s71, v129\n\t"
        "v_mfma_f32_16x16x32_bf16 v[40:43], a[16:19], v[92:95], 0\n\t"
        "buffer_load_dwordx4 v140, s[24:27], s36 offen offset:0 lds\n\t"
        "v_add_u32 v174, s71, v130\n\t"
        "v_mfma_f32_16x16x32_bf16 v[44:47], a[20:23], v[92:95], 0\n\t"
        "s_waitcnt vmcnt(10) lgkmcnt(3)\n\t"
        "s_barrier\n\t"
        "ds_read_b128 v[104:107], v128 offset:4096\n\t"
        "ds_read_b64_tr_b16 a[36:37], v138 offset:18432\n\t"
        "s_waitcnt lgkmcnt(4)\n\t"
        "v_mfma_f32_16x16x32_bf16 v[0:3], a[24:27], v[96:99], 0\n\t"
        "ds_read_b64_tr_b16 a[38:39], v138 offset:19200\n\t"
        "ds_read_b64_tr_b16 a[40:41], v139 offset:18432\n\t"
        "s_add_u32 s71, s71, s70\n\t"
        "v_mfma_f32_16x16x32_bf16 v[4:7], a[28:31], v[96:99], 0\n\t"
        "buffer_load_dwordx4 v141, s[24:27], s36 offen offset:1024 lds\n\t"
        "ds_read_b64_tr_b16 a[42:43], v139 offset:19200\n\t"
        "v_add_u32 v175, s71, v128\n\t"
        "v_mfma_f32_16x16x32_bf16 v[8:11], a[32:35], v[96:99], 0\n\t"
        "ds_read_b64_tr_b16 a[44:45], v149 offset:18432\n\t"
        "ds_read_b64_tr_b16 a[46:47], v149 offset:19200\n\t"
        "v_add_u32 v176, s71, v129\n\t"
        "s_waitcnt lgkmcnt(8)\n\t"
        "v_mfma_f32_16x16x32_bf16 v[12:15], a[24:27], v[100:103], v[12:15]\n\t"
        "ds_read_b64_tr_b16 a[48:49], v138 offset:24576\n\t"
        "ds_read_b64_tr_b16 a[50:51], v138 offset:25344\n\t"
        "ds_read_b64_tr_b16 a[52:53], v139 offset:24576\n\t"
        "v_add_u32 v177, s71, v130\n\t"
        "v_mfma_f32_16x16x32_bf16 v[16:19], a[28:31], v[100:103], v[16:19]\n\t"
        "buffer_load_dwordx4 v142, s[24:27], s36 offen offset:2048 lds\n\t"
        "s_add_u32 m0, s35, 0\n\t"
        "ds_read_b64_tr_b16 a[54:55], v139 offset:25344\n\t"
        "s_add_u32 s43, s34, 0\n\t"
        "ds_read_b64_tr_b16 a[56:57], v149 offset:24576\n\t"
        "v_mfma_f32_16x16x32_bf16 v[20:23], a[32:35], v[100:103], v[20:23]\n\t"
        "ds_read_b64_tr_b16 a[58:59], v149 offset:25344\n\t"
        "ds_read_b128 v[108:111], v128 offset:4160\n\t"
        "s_add_u32 s71, s71, s70\n\t"
        "s_waitcnt lgkmcnt(12)\n\t"
        "v_mfma_f32_16x16x32_bf16 v[0:3], a[0:3], v[104:107], v[0:3]\n\t"
        "buffer_load_dwordx4 v143, s[16:19], s43 offen  lds\n\t"
        "s_add_u32 m0, s35, 36864\n\t"
        "s_add_u32 s43, s34, 387072\n\t"
        "s_min_u32 s43, s43, 9590784\n\t"
        "v_mfma_f32_16x16x32_bf16 v[4:7], a[4:7], v[104:107], v[4:7]\n\t"
        "v_add_u32 v178, s71, v128\n\t"
        "v_add_u32 v179, s71, v129\n\t"
        "v_mfma_f32_16x16x32_bf16 v[8:11], a[8:11], v[104:107], v[8:11]\n\t"
        "s_cmp_eq_u32 s69, 36\n\t"
        "s_cselect_b64 exec, -1, 0\n\t"
        "v_swap_b32 v68, v69\n\t"
        "v_swap_b32 v173, v175\n\t"
        "v_swap_b32 v174, v178\n\t"
        "v_swap_b32 v177, v179\n\t"
        "s_mov_b64 exec, -1\n\t"
        "v_mfma_f32_16x16x32_bf16 v[12:15], a[12:15], v[104:107], v[12:15]\n\t"
        "v_add_u32 v180, s71, v130\n\t"
        "v_add3_u32 v68, v68, v52, -1\n\t"
        "v_mfma_f32_16x16x32_bf16 v[16:19], a[16:19], v[104:107], v[16:19]\n\t"
        "v_add3_u32 v69, v69, v158, -1\n\t"
        "v_cvt_f32_i32 v68, v68\n\t"
        "v_mfma_f32_16x16x32_bf16 v[20:23], a[20:23], v[104:107], v[20:23]\n\t"
        "v_cvt_f32_i32 v69, v69\n\t"
        "v_add_f32 v78, v78, v56\n\t"
        "s_waitcnt lgkmcnt(0)\n\t"
        "v_mfma_f32_16x16x32_bf16 v[0:3], a[36:39], v[108:111], v[0:3]\n\t"
        "ds_read_b128 v[112:115], v128 offset:8256\n\t"
        "v_add_f32 v79, v79, v57\n\t"
        "v_mfma_f32_16x16x32_bf16 v[4:7], a[40:43], v[108:111], v[4:7]\n\t"
        "v_readlane_b32 s69, v155, 31\n\t"
        "v_mfma_f32_16x16x32_bf16 v[8:11], a[44:47], v[108:111], v[8:11]\n\t"
        "s_nop 0\n\t"
        "v_mov_b32 v154, s69\n\t"
        "v_mov_b32 v119, s62\n\t"
        "v_mfma_f32_16x16x32_bf16 v[12:15], a[48:51], v[108:111], v[12:15]\n\t"
        "v_readlane_b32 s53, v155, 28\n\t"
        "v_readlane_b32 s9, v155, 46\n\t"
        "v_mfma_f32_16x16x32_bf16 v[16:19], a[52:55], v[108:111], v[16:19]\n\t"
        "s_sub_u32 s10, 43200, s11\n\t"
        "s_mul_i32 s10, s10, s68\n\t"
        "v_mfma_f32_16x16x32_bf16 v[20:23], a[56:59], v[108:111], v[20:23]\n\t"
        "s_sub_u32 s11, s11, 192\n\t"
        "v_mad_u32_u24 v148, v158, s11, v144\n\t"
        "s_waitcnt lgkmcnt(0)\n\t"
        "v_mfma_f32_16x16x32_bf16 v[12:15], a[36:39], v[112:115], v[12:15]\n\t"
        "ds_read_b128 v[84:87], v128 offset:12352\n\t"
        "v_add_u32 v148, s10, v148\n\t"
        "v_mfma_f32_16x16x32_bf16 v[16:19], a[40:43], v[112:115], v[16:19]\n\t"
        "v_mad_u32_u24 v150, v83, s11, v145\n\t"
        "s_lshl_b32 s70, s49, 0\n\t"
        "v_mfma_f32_16x16x32_bf16 v[20:23], a[44:47], v[112:115], v[20:23]\n\t"
        "s_add_u32 s70, s70, 50\n\t"
        "v_readlane_b32 s52, v155, s70\n\t"
        "v_mfma_f32_16x16x32_bf16 v[24:27], a[48:51], v[112:115], v[24:27]\n\t"
        "buffer_load_dwordx4 v143, s[16:19], s43 offen  lds\n\t"
        "s_add_u32 m0, s37, 24576\n\t"
        "s_add_u32 s36, s36, s39\n\t"
        "v_mfma_f32_16x16x32_bf16 v[28:31], a[52:55], v[112:115], v[28:31]\n\t"
        "s_add_u32 s52, s52, s10\n\t"
        "v_readlane_b32 s51, v155, 49\n\t"
        "v_mfma_f32_16x16x32_bf16 v[32:35], a[56:59], v[112:115], v[32:35]\n\t"
        "buffer_load_dwordx4 v140, s[24:27], s36 offen offset:0 lds\n\t"
        "s_waitcnt vmcnt(10)\n\t"
        "s_barrier\n\t"
        "ds_read_b128 v[88:91], v128 offset:16384\n\t"
        "ds_read_b64_tr_b16 a[60:61], v138 offset:30720\n\t"
        "s_waitcnt lgkmcnt(2)\n\t"
        "v_mfma_f32_16x16x32_bf16 v[24:27], a[36:39], v[84:87], v[24:27]\n\t"
        "buffer_load_dwordx4 v141, s[24:27], s36 offen offset:1024 lds\n\t"
        "ds_read_b64_tr_b16 a[62:63], v138 offset:31488\n\t"
        "s_lshl_b32 s70, s49, 30\n\t"
        "v_mfma_f32_16x16x32_bf16 v[28:31], a[40:43], v[84:87], v[28:31]\n\t"
        "ds_read_b64_tr_b16 a[64:65], v139 offset:30720\n\t"
        "ds_read_b64_tr_b16 a[66:67], v139 offset:31488\n\t"
        "s_add_u32 s70, s70, s10\n\t"
        "v_mfma_f32_16x16x32_bf16 v[32:35], a[44:47], v[84:87], v[32:35]\n\t"
        "buffer_load_dwordx4 v142, s[24:27], s36 offen offset:2048 lds\n\t"
        "ds_read_b64_tr_b16 a[68:69], v149 offset:30720\n\t"
        "s_add_u32 s51, s51, s70\n\t"
        "v_mfma_f32_16x16x32_bf16 v[36:39], a[48:51], v[84:87], v[36:39]\n\t"
        "ds_read_b64_tr_b16 a[70:71], v149 offset:31488\n\t"
        "ds_read_b128 v[92:95], v128 offset:8256\n\t"
        "v_mfma_f32_16x16x32_bf16 v[40:43], a[52:55], v[84:87], v[40:43]\n\t"
        "ds_read_b128 v[96:99], v128 offset:12352\n\t"
        "ds_read_b128 v[100:103], v128 offset:16448\n\t"
        "v_mfma_f32_16x16x32_bf16 v[44:47], a[56:59], v[84:87], v[44:47]\n\t"
        "ds_read_b64_tr_b16 a[72:73], v138 offset:36864\n\t"
        "ds_read_b64_tr_b16 a[74:75], v138 offset:37632\n\t"
        "s_waitcnt lgkmcnt(11)\n\t"
        "v_mfma_f32_16x16x32_bf16 v[24:27], a[24:27], v[88:91], v[24:27]\n\t"
        "buffer_load_dwordx4 v[48:51], v55, s[20:23], s48 offen\n\t"
        "ds_read_b64_tr_b16 a[76:77], v139 offset:36864\n\t"
        "v_mfma_f32_16x16x32_bf16 v[28:31], a[28:31], v[88:91], v[28:31]\n\t"
        "ds_read_b64_tr_b16 a[78:79], v139 offset:37632\n\t"
        "ds_read_b64_tr_b16 a[80:81], v149 offset:36864\n\t"
        "v_mfma_f32_16x16x32_bf16 v[32:35], a[32:35], v[88:91], v[32:35]\n\t"
        "ds_read_b64_tr_b16 a[82:83], v149 offset:37632\n\t"
        "v_mfma_f32_16x16x32_bf16 v[36:39], a[0:3], v[88:91], v[36:39]\n\t"
        "v_mfma_f32_16x16x32_bf16 v[40:43], a[4:7], v[88:91], v[40:43]\n\t"
        "buffer_load_dword v61, v61, s[20:23], s48 offen offset:512\n\t"
        "s_add_u32 m0, s37, 36864\n\t"
        "v_mfma_f32_16x16x32_bf16 v[44:47], a[8:11], v[88:91], v[44:47]\n\t"
        "s_add_u32 s36, s36, s38\n\t"
        "s_waitcnt lgkmcnt(8)\n\t"
        "v_mfma_f32_16x16x32_bf16 v[0:3], a[60:63], v[92:95], v[0:3]\n\t"
        "s_waitcnt vmcnt(10)\n\t"
        "s_barrier\n\t"
        "ds_read_b64_tr_b16 a[84:85], v131 offset:0\n\t"
        "ds_read_b64_tr_b16 a[86:87], v131 offset:768\n\t"
        "v_mfma_f32_16x16x32_bf16 v[4:7], a[64:67], v[92:95], v[4:7]\n\t"
        "ds_read_b64_tr_b16 a[88:89], v132 offset:0\n\t"
        "ds_read_b64_tr_b16 a[90:91], v132 offset:768\n\t"
        "v_readlane_b32 s46, v155, 37\n\t"
        "v_mfma_f32_16x16x32_bf16 v[8:11], a[68:71], v[92:95], v[8:11]\n\t"
        "buffer_load_dwordx4 v140, s[24:27], s36 offen offset:0 lds\n\t"
        "ds_read_b64_tr_b16 a[92:93], v133 offset:0\n\t"
        "v_readlane_b32 s47, v155, 40\n\t"
        "s_waitcnt lgkmcnt(8)\n\t"
        "v_mfma_f32_16x16x32_bf16 v[12:15], a[60:63], v[96:99], v[12:15]\n\t"
        "ds_read_b64_tr_b16 a[94:95], v133 offset:768\n\t"
        "ds_read_b128 v[104:107], v128 offset:4224\n\t"
        "v_add_f32 v58, s46, v68\n\t"
        "v_mfma_f32_16x16x32_bf16 v[16:19], a[64:67], v[96:99], v[16:19]\n\t"
        "ds_read_b64_tr_b16 a[96:97], v131 offset:6144\n\t"
        "ds_read_b64_tr_b16 a[98:99], v131 offset:6912\n\t"
        "v_add_f32 v59, s47, v69\n\t"
        "v_mfma_f32_16x16x32_bf16 v[20:23], a[68:71], v[96:99], v[20:23]\n\t"
        "ds_read_b64_tr_b16 a[100:101], v132 offset:6144\n\t"
        "ds_read_b64_tr_b16 a[102:103], v132 offset:6912\n\t"
        "v_mfma_f32_16x16x32_bf16 v[24:27], a[60:63], v[100:103], v[24:27]\n\t"
        "buffer_load_dwordx4 v141, s[24:27], s36 offen offset:1024 lds\n\t"
        "ds_read_b64_tr_b16 a[104:105], v133 offset:6144\n\t"
        "v_mfma_f32_16x16x32_bf16 v[28:31], a[64:67], v[100:103], v[28:31]\n\t"
        "ds_read_b64_tr_b16 a[106:107], v133 offset:6912\n\t"
        "v_mfma_f32_16x16x32_bf16 v[32:35], a[68:71], v[100:103], v[32:35]\n\t"
        "ds_read_b128 v[108:111], v128 offset:8320\n\t"
        "v_mfma_f32_16x16x32_bf16 v[36:39], a[36:39], v[100:103], v[36:39]\n\t"
        "ds_read_b32 v64, v70 offset:0\n\t"
        "v_mfma_f32_16x16x32_bf16 v[40:43], a[40:43], v[100:103], v[40:43]\n\t"
        "ds_read_b32 v67, v67 offset:0\n\t"
        "v_mfma_f32_16x16x32_bf16 v[44:47], a[44:47], v[100:103], v[44:47]\n\t"
        "buffer_load_dwordx4 v142, s[24:27], s36 offen offset:2048 lds\n\t"
        "v_readlane_b32 s40, v155, 12\n\t"
        "s_waitcnt lgkmcnt(9)\n\t"
        "v_mfma_f32_16x16x32_bf16 v[0:3], a[72:75], v[104:107], v[0:3]\n\t"
        "ds_read_b128 v[112:115], v128 offset:12416\n\t"
        "v_readlane_b32 s43, v155, 0\n\t"
        "v_mfma_f32_16x16x32_bf16 v[4:7], a[76:79], v[104:107], v[4:7]\n\t"
        "ds_read_b32 v65, v70 offset:1280\n\t"
        "s_add_u32 m0, s40, s35\n\t"
        "v_mfma_f32_16x16x32_bf16 v[8:11], a[80:83], v[104:107], v[8:11]\n\t"
        "ds_read_b32 v66, v70 offset:2560\n\t"
        "v_mfma_f32_16x16x32_bf16 v[12:15], a[84:87], v[104:107], v[12:15]\n\t"
        "ds_read_b128 v[84:87], v128 offset:16512\n\t"
        "v_mfma_f32_16x16x32_bf16 v[16:19], a[88:91], v[104:107], v[16:19]\n\t"
        "v_mfma_f32_16x16x32_bf16 v[20:23], a[92:95], v[104:107], v[20:23]\n\t"
        "s_waitcnt lgkmcnt(6)\n\t"
        "v_mfma_f32_16x16x32_bf16 v[0:3], a[96:99], v[108:111], v[0:3]\n\t"
        "s_waitcnt lgkmcnt(5)\n\t"
        "v_perm_b32 v56, v64, v64, s44\n\t"
        "v_perm_b32 v57, v64, v64, s45\n\t"
        "v_mfma_f32_16x16x32_bf16 v[4:7], a[100:103], v[108:111], v[4:7]\n\t"
        "v_add_f32 v56, v56, v58\n\t"
        "v_add_f32 v57, v57, v59\n\t"
        "v_mfma_f32_16x16x32_bf16 v[8:11], a[104:107], v[108:111], v[8:11]\n\t"
        "v_cvt_pk_bf16_f32 v52, v56, v57\n\t"
        "v_cvt_pk_bf16_f32 v58, v58, v59\n\t"
        "v_mfma_f32_16x16x32_bf16 v[12:15], a[72:75], v[108:111], v[12:15]\n\t"
        "v_cmp_ne_u32 s[64:65], v52, v58\n\t"
        "s_waitcnt lgkmcnt(4)\n\t"
        "v_perm_b32 v57, v67, v67, s45\n\t"
        "v_mfma_f32_16x16x32_bf16 v[16:19], a[76:79], v[108:111], v[16:19]\n\t"
        "v_mbcnt_lo_u32_b32 v56, s64, 1\n\t"
        "v_mbcnt_hi_u32_b32 v56, s65, v56\n\t"
        "v_mfma_f32_16x16x32_bf16 v[20:23], a[80:83], v[108:111], v[20:23]\n\t"
        "s_bcnt1_i32_b64 s61, s[64:65]\n\t"
        "v_add_u32 v54, s61, 1\n\t"
        "v_mfma_f32_16x16x32_bf16 v[24:27], a[84:87], v[108:111], v[24:27]\n\t"
        "v_lshl_add_u32 v56, v56, 3, s60\n\t"
        "v_add_f32 v57, v57, v79\n\t"
        "v_mfma_f32_16x16x32_bf16 v[28:31], a[88:91], v[108:111], v[28:31]\n\t"
        "s_mov_b64 exec, s[64:65]\n\t"
        "ds_write_b64 v56, v[52:53]\n\t"
        "s_mov_b64 exec, -1\n\t"
        "v_mfma_f32_16x16x32_bf16 v[32:35], a[92:95], v[108:111], v[32:35]\n\t"
        "v_swap_b32 v53, v80\n\t"
        "v_perm_b32 v56, v67, v67, s44\n\t"
        "s_waitcnt lgkmcnt(1)\n\t"
        "v_mfma_f32_16x16x32_bf16 v[12:15], a[96:99], v[112:115], v[12:15]\n\t"
        "v_add_f32 v56, v56, v78\n\t"
        "v_cvt_pk_bf16_f32 v52, v56, v57\n\t"
        "v_mfma_f32_16x16x32_bf16 v[16:19], a[100:103], v[112:115], v[16:19]\n\t"
        "v_cvt_pk_bf16_f32 v58, v78, v79\n\t"
        "v_cmp_ne_u32 s[64:65], v52, v58\n\t"
        "v_mfma_f32_16x16x32_bf16 v[20:23], a[104:107], v[112:115], v[20:23]\n\t"
        "s_nop 0\n\t"
        "s_and_b64 s[64:65], s[64:65], 0xffffff\n\t"
        "v_mbcnt_lo_u32_b32 v56, s64, v54\n\t"
        "v_mfma_f32_16x16x32_bf16 v[24:27], a[72:75], v[112:115], v[24:27]\n\t"
        "s_bcnt1_i32_b64 s46, s[64:65]\n\t"
        "s_add_u32 s61, s61, s46\n\t"
        "v_mfma_f32_16x16x32_bf16 v[28:31], a[76:79], v[112:115], v[28:31]\n\t"
        "v_add_u32 v54, s46, v54\n\t"
        "v_lshl_add_u32 v56, v56, 3, s60\n\t"
        "v_mfma_f32_16x16x32_bf16 v[32:35], a[80:83], v[112:115], v[32:35]\n\t"
        "s_mov_b64 exec, s[64:65]\n\t"
        "ds_write_b64 v56, v[52:53]\n\t"
        "s_mov_b64 exec, -1\n\t"
        "v_mfma_f32_16x16x32_bf16 v[36:39], a[84:87], v[112:115], v[36:39]\n\t"
        "v_add_u32 v53, 2048, v80\n\t"
        "v_readlane_b32 s46, v155, 38\n\t"
        "v_mfma_f32_16x16x32_bf16 v[40:43], a[88:91], v[112:115], v[40:43]\n\t"
        "v_readlane_b32 s47, v155, 41\n\t"
        "v_perm_b32 v56, v65, v65, s44\n\t"
        "v_mfma_f32_16x16x32_bf16 v[44:47], a[92:95], v[112:115], v[44:47]\n\t"
        "s_waitcnt vmcnt(8)\n\t"
        "s_barrier\n\t"
        "buffer_load_dwordx4 v134, s[16:19], s43 offen  lds\n\t"
        "ds_read_b128 v[88:91], v128 offset:0\n\t"
        "v_mfma_f32_16x16x32_bf16 v[24:27], a[96:99], v[84:87], v[24:27]\n\t"
        "ds_read_b128 v[92:95], v128 offset:64\n\t"
        "ds_read_b128 v[96:99], v128 offset:128\n\t"
        "v_readlane_b32 s40, v155, 13\n\t"
        "v_mfma_f32_16x16x32_bf16 v[28:31], a[100:103], v[84:87], v[28:31]\n\t"
        "ds_read_b128 v[100:103], v128 offset:20480\n\t"
        "ds_read_b128 v[104:107], v128 offset:20544\n\t"
        "v_readlane_b32 s43, v155, 1\n\t"
        "v_mfma_f32_16x16x32_bf16 v[32:35], a[104:107], v[84:87], v[32:35]\n\t"
        "ds_read_b128 v[108:111], v128 offset:20608\n\t"
        "s_add_u32 m0, s40, s35\n\t"
        "ds_read_b64_tr_b16 a[108:109], v131 offset:12288\n\t"
        "v_mfma_f32_16x16x32_bf16 v[36:39], a[72:75], v[84:87], v[36:39]\n\t"
        "buffer_load_dwordx4 v135, s[16:19], s43 offen  lds\n\t"
        "s_add_u32 m0, s37, 49152\n\t"
        "ds_read_b64_tr_b16 a[110:111], v131 offset:13056\n\t"
        "v_mfma_f32_16x16x32_bf16 v[40:43], a[76:79], v[84:87], v[40:43]\n\t"
        "ds_read_b64_tr_b16 a[112:113], v132 offset:12288\n\t"
        "s_add_u32 s36, s36, s5\n\t"
        "ds_read_b64_tr_b16 a[114:115], v132 offset:13056\n\t"
        "v_mfma_f32_16x16x32_bf16 v[44:47], a[80:83], v[84:87], v[44:47]\n\t"
        "ds_read_b64_tr_b16 a[116:117], v133 offset:12288\n\t"
        "ds_read_b64_tr_b16 a[118:119], v133 offset:13056\n\t"
        "v_perm_b32 v57, v65, v65, s45\n\t"
        "s_waitcnt lgkmcnt(11)\n\t"
        "v_mfma_f32_16x16x32_bf16 v[0:3], a[12:15], v[88:91], v[0:3]\n\t"
        "ds_read_b64_tr_b16 a[120:121], v131 offset:18432\n\t"
        "ds_read_b64_tr_b16 a[122:123], v131 offset:19200\n\t"
        "v_add_f32 v58, s46, v68\n\t"
        "v_mfma_f32_16x16x32_bf16 v[4:7], a[16:19], v[88:91], v[4:7]\n\t"
        "buffer_load_dwordx4 v140, s[24:27], s36 offen offset:0 lds\n\t"
        "ds_read_b64_tr_b16 a[124:125], v132 offset:18432\n\t"
        "v_add_f32 v59, s47, v69\n\t"
        "v_mfma_f32_16x16x32_bf16 v[8:11], a[20:23], v[88:91], v[8:11]\n\t"
        "ds_read_b64_tr_b16 a[126:127], v132 offset:19200\n\t"
        "ds_read_b64_tr_b16 a[128:129], v133 offset:18432\n\t"
        "v_add_f32 v56, v56, v58\n\t"
        "s_waitcnt lgkmcnt(12)\n\t"
        "v_mfma_f32_16x16x32_bf16 v[0:3], a[48:51], v[92:95], v[0:3]\n\t"
        "ds_read_b64_tr_b16 a[130:131], v133 offset:19200\n\t"
        "v_add_f32 v57, v57, v59\n\t"
        "v_mfma_f32_16x16x32_bf16 v[4:7], a[52:55], v[92:95], v[4:7]\n\t"
        "ds_read_b128 v[112:115], v129 offset:0\n\t"
        "v_cvt_pk_bf16_f32 v52, v56, v57\n\t"
        "v_mfma_f32_16x16x32_bf16 v[8:11], a[56:59], v[92:95], v[8:11]\n\t"
        "buffer_load_dwordx4 v141, s[24:27], s36 offen offset:1024 lds\n\t"
        "v_cvt_pk_bf16_f32 v58, v58, v59\n\t"
        "v_mfma_f32_16x16x32_bf16 v[0:3], a[84:87], v[96:99], v[0:3]\n\t"
        "ds_read_b128 v[84:87], v129 offset:4096\n\t"
        "v_cmp_ne_u32 s[64:65], v52, v58\n\t"
        "v_mfma_f32_16x16x32_bf16 v[4:7], a[88:91], v[96:99], v[4:7]\n\t"
        "s_nop 0\n\t"
        "v_mbcnt_lo_u32_b32 v56, s64, v54\n\t"
        "v_mbcnt_hi_u32_b32 v56, s65, v56\n\t"
        "v_mfma_f32_16x16x32_bf16 v[8:11], a[92:95], v[96:99], v[8:11]\n\t"
        "s_bcnt1_i32_b64 s46, s[64:65]\n\t"
        "s_add_u32 s61, s61, s46\n\t"
        "v_mfma_f32_16x16x32_bf16 v[36:39], a[24:27], v[100:103], v[36:39]\n\t"
        "buffer_load_dwordx4 v142, s[24:27], s36 offen offset:2048 lds\n\t"
        "v_readlane_b32 s40, v155, 14\n\t"
        "v_mfma_f32_16x16x32_bf16 v[40:43], a[28:31], v[100:103], v[40:43]\n\t"
        "v_readlane_b32 s43, v155, 2\n\t"
        "s_add_u32 m0, s40, s35\n\t"
        "v_mfma_f32_16x16x32_bf16 v[44:47], a[32:35], v[100:103], v[44:47]\n\t"
        "v_add_u32 v54, s46, v54\n\t"
        "v_min_u32 v56, 97, v56\n\t"
        "v_mfma_f32_16x16x32_bf16 v[36:39], a[60:63], v[104:107], v[36:39]\n\t"
        "s_waitcnt vmcnt(10) lgkmcnt(2)\n\t"
        "s_barrier\n\t"
        "ds_read_b64_tr_b16 a[132:133], v131 offset:24576\n\t"
        "ds_read_b64_tr_b16 a[134:135], v131 offset:25344\n\t"
        "v_mfma_f32_16x16x32_bf16 v[40:43], a[64:67], v[104:107], v[40:43]\n\t"
        "buffer_load_dwordx4 v134, s[16:19], s43 offen  lds\n\t"
        "ds_read_b64_tr_b16 a[136:137], v132 offset:24576\n\t"
        "s_bitcmp1_b32 s42, 10\n\t"
        "s_cbranch_scc1 .Lx56%=\n\t"
        ".Lx57%=:\n\t"
        "v_mfma_f32_16x16x32_bf16 v[44:47], a[68:71], v[104:107], v[44:47]\n\t"
        "ds_read_b64_tr_b16 a[138:139], v132 offset:25344\n\t"
        "ds_read_b64_tr_b16 a[140:141], v133 offset:24576\n\t"
        "v_readlane_b32 s40, v155, 15\n\t"
        "v_mfma_f32_16x16x32_bf16 v[36:39], a[96:99], v[108:111], v[36:39]\n\t"
        "ds_read_b64_tr_b16 a[142:143], v133 offset:25344\n\t"
        "ds_read_b128 v[88:91], v129 offset:8192\n\t"
        "v_readlane_b32 s43, v155, 3\n\t"
        "v_mfma_f32_16x16x32_bf16 v[40:43], a[100:103], v[108:111], v[40:43]\n\t"
        "ds_read_b128 v[92:95], v129 offset:12288\n\t"
        "s_add_u32 m0, s40, s35\n\t"
        "ds_read_b64_tr_b16 a[144:145], v131 offset:30720\n\t"
        "v_mfma_f32_16x16x32_bf16 v[44:47], a[104:107], v[108:111], v[44:47]\n\t"
        "buffer_load_dwordx4 v135, s[16:19], s43 offen  lds\n\t"
        "s_add_u32 m0, s37, 0\n\t"
        "ds_read_b64_tr_b16 a[146:147], v131 offset:31488\n\t"
        "s_waitcnt lgkmcnt(10)\n\t"
        "v_mfma_f32_16x16x32_bf16 v[0:3], a[120:123], v[112:115], v[0:3]\n\t"
        "ds_read_b64_tr_b16 a[148:149], v132 offset:30720\n\t"
        "s_add_u32 s36, s36, s39\n\t"
        "ds_read_b64_tr_b16 a[150:151], v132 offset:31488\n\t"
        "v_mfma_f32_16x16x32_bf16 v[4:7], a[124:127], v[112:115], v[4:7]\n\t"
        "ds_read_b64_tr_b16 a[152:153], v133 offset:30720\n\t"
        "ds_read_b64_tr_b16 a[154:155], v133 offset:31488\n\t"
        "v_lshl_add_u32 v56, v56, 3, s60\n\t"
        "v_mfma_f32_16x16x32_bf16 v[8:11], a[128:131], v[112:115], v[8:11]\n\t"
        "s_mov_b64 exec, s[64:65]\n\t"
        "ds_write_b64 v56, v[52:53]\n\t"
        "s_mov_b64 exec, -1\n\t"
        "v_mfma_f32_16x16x32_bf16 v[0:3], a[108:111], v[84:87], v[0:3]\n\t"
        "buffer_load_dwordx4 v140, s[24:27], s36 offen offset:0 lds\n\t"
        "v_add_u32 v53, 2048, v53\n\t"
        "v_mfma_f32_16x16x32_bf16 v[4:7], a[112:115], v[84:87], v[4:7]\n\t"
        "s_waitcnt vmcnt(12)\n\t"
        "v_and_b32 v48, s54, v48\n\t"
        "v_pk_max_u16 v60, v154, v48\n\t"
        "v_mfma_f32_16x16x32_bf16 v[8:11], a[116:119], v[84:87], v[8:11]\n\t"
        "v_and_b32 v49, s54, v49\n\t"
        "v_pk_max_u16 v60, v60, v49\n\t"
        "v_mfma_f32_16x16x32_bf16 v[12:15], a[120:123], v[84:87], v[12:15]\n\t"
        "ds_read_b128 v[96:99], v129 offset:16384\n\t"
        "v_and_b32 v50, s54, v50\n\t"
        "v_mfma_f32_16x16x32_bf16 v[16:19], a[124:127], v[84:87], v[16:19]\n\t"
        "buffer_load_dwordx4 v141, s[24:27], s36 offen offset:1024 lds\n\t"
        "v_pk_max_u16 v60, v60, v50\n\t"
        "v_mfma_f32_16x16x32_bf16 v[20:23], a[128:131], v[84:87], v[20:23]\n\t"
        "v_and_b32 v51, s54, v51\n\t"
        "v_pk_max_u16 v60, v60, v51\n\t"
        "s_waitcnt lgkmcnt(5)\n\t"
        "v_mfma_f32_16x16x32_bf16 v[0:3], a[132:135], v[88:91], v[0:3]\n\t"
        "s_waitcnt vmcnt(12)\n\t"
        "v_and_b32 v61, s54, v61\n\t"
        "v_pk_max_u16 v60, v60, v61\n\t"
        "v_mfma_f32_16x16x32_bf16 v[4:7], a[136:139], v[88:91], v[4:7]\n\t"
        ".LpS0%=:\n\t"
        "s_cbranch_execz .LrS%=\n\t"
        "v_cmp_ne_u32 s[12:13], v60, v154\n\t"
        "s_lshl_b32 s6, 256, s4\n\t"
        "v_mfma_f32_16x16x32_bf16 v[8:11], a[140:143], v[88:91], v[8:11]\n\t"
        "s_cmp_lg_u64 s[12:13], 0\n\t"
        "s_cselect_b32 s6, s6, 0\n\t"
        "v_mfma_f32_16x16x32_bf16 v[12:15], a[108:111], v[88:91], v[12:15]\n\t"
        "buffer_load_dwordx4 v142, s[24:27], s36 offen offset:2048 lds\n\t"
        "v_readlane_b32 s40, v155, 16\n\t"
        "v_mfma_f32_16x16x32_bf16 v[16:19], a[112:115], v[88:91], v[16:19]\n\t"
        "ds_read_b128 v[100:103], v129 offset:20480\n\t"
        "v_readlane_b32 s43, v155, 4\n\t"
        "v_mfma_f32_16x16x32_bf16 v[20:23], a[116:119], v[88:91], v[20:23]\n\t"
        "s_add_u32 m0, s40, s35\n\t"
        "v_readlane_b32 s46, v155, 39\n\t"
        "v_mfma_f32_16x16x32_bf16 v[24:27], a[120:123], v[88:91], v[24:27]\n\t"
        "v_readlane_b32 s47, v155, 42\n\t"
        "v_perm_b32 v56, v66, v66, s44\n\t"
        "v_mfma_f32_16x16x32_bf16 v[28:31], a[124:127], v[88:91], v[28:31]\n\t"
        "buffer_load_dwordx4 v134, s[16:19], s43 offen  lds\n\t"
        "v_readlane_b32 s40, v155, 17\n\t"
        "v_mfma_f32_16x16x32_bf16 v[32:35], a[128:131], v[88:91], v[32:35]\n\t"
        "v_readlane_b32 s43, v155, 5\n\t"
        "s_add_u32 m0, s40, s35\n\t"
        "v_mfma_f32_16x16x32_bf16 v[12:15], a[132:135], v[92:95], v[12:15]\n\t"
        "v_perm_b32 v57, v66, v66, s45\n\t"
        "v_add_f32 v58, s46, v68\n\t"
        "v_mfma_f32_16x16x32_bf16 v[16:19], a[136:139], v[92:95], v[16:19]\n\t"
        "v_add_f32 v59, s47, v69\n\t"
        "v_add_f32 v56, v56, v58\n\t"
        "v_mfma_f32_16x16x32_bf16 v[20:23], a[140:143], v[92:95], v[20:23]\n\t"
        "buffer_load_dwordx4 v135, s[16:19], s43 offen  lds\n\t"
        "s_add_u32 m0, s37, 12288\n\t"
        "v_mfma_f32_16x16x32_bf16 v[24:27], a[108:111], v[92:95], v[24:27]\n\t"
        "s_add_u32 s36, s36, s38\n\t"
        "v_add_f32 v57, v57, v59\n\t"
        "v_mfma_f32_16x16x32_bf16 v[28:31], a[112:115], v[92:95], v[28:31]\n\t"
        "v_cvt_pk_bf16_f32 v52, v56, v57\n\t"
        "v_cvt_pk_bf16_f32 v58, v58, v59\n\t"
        "v_mfma_f32_16x16x32_bf16 v[32:35], a[116:119], v[92:95], v[32:35]\n\t"
        "s_waitcnt vmcnt(10) lgkmcnt(0)\n\t"
        "s_barrier\n\t"
        "ds_read_b64_tr_b16 a[156:157], v131 offset:36864\n\t"
        "ds_read_b64_tr_b16 a[158:159], v131 offset:37632\n\t"
        "v_mfma_f32_16x16x32_bf16 v[36:39], a[120:123], v[92:95], v[36:39]\n\t"
        "buffer_load_dwordx4 v140, s[24:27], s36 offen offset:0 lds\n\t"
        "ds_read_b64_tr_b16 a[160:161], v132 offset:36864\n\t"
        "v_cmp_ne_u32 s[64:65], v52, v58\n\t"
        "v_mfma_f32_16x16x32_bf16 v[40:43], a[124:127], v[92:95], v[40:43]\n\t"
        "ds_read_b64_tr_b16 a[162:163], v132 offset:37632\n\t"
        "ds_read_b64_tr_b16 a[164:165], v133 offset:36864\n\t"
        "v_mbcnt_lo_u32_b32 v56, s64, v54\n\t"
        "v_mfma_f32_16x16x32_bf16 v[44:47], a[128:131], v[92:95], v[44:47]\n\t"
        "ds_read_b64_tr_b16 a[166:167], v133 offset:37632\n\t"
        "ds_read_b128 v[104:107], v129 offset:64\n\t"
        "v_mbcnt_hi_u32_b32 v56, s65, v56\n\t"
        "v_mfma_f32_16x16x32_bf16 v[24:27], a[132:135], v[96:99], v[24:27]\n\t"
        "ds_read_b128 v[108:111], v129 offset:4160\n\t"
        "ds_read_b64_tr_b16 a[168:169], v131 offset:43008\n\t"
        "s_bcnt1_i32_b64 s46, s[64:65]\n\t"
        "v_mfma_f32_16x16x32_bf16 v[28:31], a[136:139], v[96:99], v[28:31]\n\t"
        "buffer_load_dwordx4 v141, s[24:27], s36 offen offset:1024 lds\n\t"
        "ds_read_b64_tr_b16 a[170:171], v131 offset:43776\n\t"
        "s_add_u32 s61, s61, s46\n\t"
        "v_mfma_f32_16x16x32_bf16 v[32:35], a[140:143], v[96:99], v[32:35]\n\t"
        "ds_read_b64_tr_b16 a[172:173], v132 offset:43008\n\t"
        "ds_read_b64_tr_b16 a[174:175], v132 offset:43776\n\t"
        "s_mov_b32 s3, s61\n\t"
        "v_mfma_f32_16x16x32_bf16 v[36:39], a[108:111], v[96:99], v[36:39]\n\t"
        "ds_read_b64_tr_b16 a[176:177], v133 offset:43008\n\t"
        "ds_read_b64_tr_b16 a[178:179], v133 offset:43776\n\t"
        "v_min_u32 v56, 97, v56\n\t"
        "v_mfma_f32_16x16x32_bf16 v[40:43], a[112:115], v[96:99], v[40:43]\n\t"
        "ds_read_b128 v[112:115], v129 offset:8256\n\t"
        "v_lshl_add_u32 v56, v56, 3, s60\n\t"
        "v_mfma_f32_16x16x32_bf16 v[44:47], a[116:119], v[96:99], v[44:47]\n\t"
        "s_mov_b64 exec, s[64:65]\n\t"
        "ds_write_b64 v56, v[52:53]\n\t"
        "s_mov_b64 exec, -1\n\t"
        "v_mfma_f32_16x16x32_bf16 v[36:39], a[132:135], v[100:103], v[36:39]\n\t"
        "s_cmp_gt_u32 s3, 64\n\t"
        "s_cselect_b32 s8, 1, 0\n\t"
        "v_mfma_f32_16x16x32_bf16 v[40:43], a[136:139], v[100:103], v[40:43]\n\t"
        "buffer_load_dwordx4 v142, s[24:27], s36 offen offset:2048 lds\n\t"
        "v_readlane_b32 s40, v155, 18\n\t"
        "v_mfma_f32_16x16x32_bf16 v[44:47], a[140:143], v[100:103], v[44:47]\n\t"
        "ds_read_b128 v[84:87], v129 offset:12352\n\t"
        "v_readlane_b32 s43, v155, 6\n\t"
        "s_waitcnt lgkmcnt(8)\n\t"
        "v_mfma_f32_16x16x32_bf16 v[0:3], a[156:159], v[104:107], v[0:3]\n\t"
        "s_add_u32 m0, s40, s35\n\t"
        "s_or_b32 s56, s56, s8\n\t"
        "v_mfma_f32_16x16x32_bf16 v[4:7], a[160:163], v[104:107], v[4:7]\n\t"
        "s_min_u32 s61, s61, 80\n\t"
        "v_min_u32 v78, s61, v116\n\t"
        "v_mfma_f32_16x16x32_bf16 v[8:11], a[164:167], v[104:107], v[8:11]\n\t"
        "buffer_load_dwordx4 v136, s[16:19], s43 offen  lds\n\t"
        "v_readlane_b32 s40, v155, 19\n\t"
        "v_mfma_f32_16x16x32_bf16 v[0:3], a[144:147], v[108:111], v[0:3]\n\t"
        "v_readlane_b32 s43, v155, 7\n\t"
        "s_add_u32 m0, s40, s35\n\t"
        "v_mfma_f32_16x16x32_bf16 v[4:7], a[148:151], v[108:111], v[4:7]\n\t"
        "v_lshl_add_u32 v78, v78, 3, s60\n\t"
        "v_cmp_ge_u32 s[66:67], s61, v116\n\t"
        "v_mfma_f32_16x16x32_bf16 v[8:11], a[152:155], v[108:111], v[8:11]\n\t"
        "ds_read_b64 v[62:63], v78\n\t"
        "s_mov_b32 s46, 0x07060302\n\t"
        "v_mfma_f32_16x16x32_bf16 v[12:15], a[156:159], v[108:111], v[12:15]\n\t"
        "buffer_load_dwordx4 v137, s[16:19], s43 offen  lds\n\t"
        "s_add_u32 m0, s37, 24576\n\t"
        "v_mfma_f32_16x16x32_bf16 v[16:19], a[160:163], v[108:111], v[16:19]\n\t"
        "ds_read_b128 v[88:91], v129 offset:16448\n\t"
        "s_add_u32 s36, s36, s5\n\t"
        "v_mfma_f32_16x16x32_bf16 v[20:23], a[164:167], v[108:111], v[20:23]\n\t"
        "s_waitcnt lgkmcnt(4)\n\t"
        "v_mfma_f32_16x16x32_bf16 v[0:3], a[168:171], v[112:115], v[0:3]\n\t"
        "v_mfma_f32_16x16x32_bf16 v[4:7], a[172:175], v[112:115], v[4:7]\n\t"
        "v_mfma_f32_16x16x32_bf16 v[8:11], a[176:179], v[112:115], v[8:11]\n\t"
        "v_mfma_f32_16x16x32_bf16 v[12:15], a[144:147], v[112:115], v[12:15]\n\t"
        "ds_read_b128 v[92:95], v129 offset:20544\n\t"
        "v_mfma_f32_16x16x32_bf16 v[16:19], a[148:151], v[112:115], v[16:19]\n\t"
        "v_mfma_f32_16x16x32_bf16 v[20:23], a[152:155], v[112:115], v[20:23]\n\t"
        "s_waitcnt vmcnt(10)\n\t"
        "s_barrier\n\t"
        "buffer_load_dwordx4 v140, s[24:27], s36 offen offset:0 lds\n\t"
        "ds_read_b64_tr_b16 a[180:181], v131 offset:49152\n\t"
        "v_mfma_f32_16x16x32_bf16 v[24:27], a[156:159], v[112:115], v[24:27]\n\t"
        "s_mov_b64 exec, s[66:67]\n\t"
        "s_waitcnt lgkmcnt(3)\n\t"
        "ds_write_b32 v63, v117\n\t"
        "s_mov_b64 exec, -1\n\t"
        "ds_read_b64_tr_b16 a[182:183], v131 offset:49920\n\t"
        "v_mfma_f32_16x16x32_bf16 v[28:31], a[160:163], v[112:115], v[28:31]\n\t"
        "ds_read_b64_tr_b16 a[184:185], v132 offset:49152\n\t"
        "ds_read_b64_tr_b16 a[186:187], v132 offset:49920\n\t"
        "v_and_b32 v78, 0xfffff800, v63\n\t"
        "v_mfma_f32_16x16x32_bf16 v[32:35], a[164:167], v[112:115], v[32:35]\n\t"
        "ds_read_b128 v[64:67], v78 offset:1536\n\t"
        "ds_read_b64_tr_b16 a[188:189], v133 offset:49152\n\t"
        "v_lshlrev_b32 v72, 16, v62\n\t"
        "v_mfma_f32_16x16x32_bf16 v[12:15], a[168:171], v[84:87], v[12:15]\n\t"
        "buffer_load_dwordx4 v141, s[24:27], s36 offen offset:1024 lds\n\t"
        "ds_read_b64_tr_b16 a[190:191], v133 offset:49920\n\t"
        "v_and_b32 v73, 0xffff0000, v62\n\t"
        "v_mfma_f32_16x16x32_bf16 v[16:19], a[172:175], v[84:87], v[16:19]\n\t"
        "ds_read_b64_tr_b16 a[192:193], v131 offset:55296\n\t"
        "ds_read_b64_tr_b16 a[194:195], v131 offset:56064\n\t"
        "v_med3_f32 v72, v72, 0, s63\n\t"
        "v_mfma_f32_16x16x32_bf16 v[20:23], a[176:179], v[84:87], v[20:23]\n\t"
        "ds_read_b64_tr_b16 a[196:197], v132 offset:55296\n\t"
        "ds_read_b64_tr_b16 a[198:199], v132 offset:56064\n\t"
        "v_med3_f32 v73, v73, 0, s63\n\t"
        "v_mfma_f32_16x16x32_bf16 v[24:27], a[144:147], v[84:87], v[24:27]\n\t"
        "ds_read_b64_tr_b16 a[200:201], v133 offset:55296\n\t"
        "ds_read_b64_tr_b16 a[202:203], v133 offset:56064\n\t"
        "v_perm_b32 v62, v73, v72, s46\n\t"
        "v_mfma_f32_16x16x32_bf16 v[28:31], a[148:151], v[84:87], v[28:31]\n\t"
        "buffer_load_dwordx4 v142, s[24:27], s36 offen offset:2048 lds\n\t"
        "s_add_u32 m0, s37, 36864\n\t"
        "v_mfma_f32_16x16x32_bf16 v[32:35], a[152:155], v[84:87], v[32:35]\n\t"
        "ds_read_b128 v[96:99], v129 offset:128\n\t"
        "s_add_u32 s36, s36, s39\n\t"
        "v_mfma_f32_16x16x32_bf16 v[36:39], a[156:159], v[84:87], v[36:39]\n\t"
        "ds_read_b128 v[100:103], v129 offset:4224\n\t"
        "v_fract_f32 v74, v72\n\t"
        "v_mfma_f32_16x16x32_bf16 v[40:43], a[160:163], v[84:87], v[40:43]\n\t"
        "v_fract_f32 v75, v73\n\t"
        "v_cvt_u32_f32 v76, v72\n\t"
        "v_mfma_f32_16x16x32_bf16 v[44:47], a[164:167], v[84:87], v[44:47]\n\t"
        "buffer_load_dwordx4 v140, s[24:27], s36 offen offset:0 lds\n\t"
        "v_cvt_u32_f32 v77, v73\n\t"
        "s_waitcnt lgkmcnt(8)\n\t"
        "v_mfma_f32_16x16x32_bf16 v[24:27], a[168:171], v[88:91], v[24:27]\n\t"
        "v_cmp_neq_f32 s[64:65], 0, v74\n\t"
        "v_cmp_neq_f32 s[44:45], 0, v75\n\t"
        "v_mfma_f32_16x16x32_bf16 v[28:31], a[172:175], v[88:91], v[28:31]\n\t"
        "v_sub_f32 v78, 1.0, v74\n\t"
        "v_sub_f32 v79, 1.0, v75\n\t"
        "v_mfma_f32_16x16x32_bf16 v[32:35], a[176:179], v[88:91], v[32:35]\n\t"
        "v_cvt_pk_bf16_f32 v79, v78, v79\n\t"
        "v_lshlrev_b32 v78, 16, v79\n\t"
        "v_mfma_f32_16x16x32_bf16 v[36:39], a[144:147], v[88:91], v[36:39]\n\t"
        "v_and_b32 v79, 0xffff0000, v79\n\t"
        "v_mul_f32 v80, v78, v79\n\t"
        "v_mfma_f32_16x16x32_bf16 v[40:43], a[148:151], v[88:91], v[40:43]\n\t"
        "buffer_load_dwordx4 v141, s[24:27], s36 offen offset:1024 lds\n\t"
        "v_mul_f32 v78, v78, v75\n\t"
        "v_mfma_f32_16x16x32_bf16 v[44:47], a[152:155], v[88:91], v[44:47]\n\t"
        "v_mul_f32 v79, v74, v79\n\t"
        "v_mul_f32 v81, v74, v75\n\t"
        "v_mfma_f32_16x16x32_bf16 v[36:39], a[168:171], v[92:95], v[36:39]\n\t"
        "v_cndmask_b32 v78, v79, v78, s[44:45]\n\t"
        "v_cndmask_b32 v79, 0, v79, s[44:45]\n\t"
        "v_mfma_f32_16x16x32_bf16 v[40:43], a[172:175], v[92:95], v[40:43]\n\t"
        "v_cvt_pk_bf16_f32 v70, v80, v78\n\t"
        "v_cvt_pk_bf16_f32 v71, v79, v81\n\t"
        "v_mfma_f32_16x16x32_bf16 v[44:47], a[176:179], v[92:95], v[44:47]\n\t"
        "s_waitcnt vmcnt(10) lgkmcnt(0)\n\t"
        "s_barrier\n\t"
        "buffer_load_dwordx4 v142, s[24:27], s36 offen offset:2048 lds\n\t"
        "s_add_u32 m0, s37, 49152\n\t"
        "ds_read_b64_tr_b16 a[204:205], v131 offset:0\n\t"
        "v_mfma_f32_16x16x32_bf16 v[0:3], a[192:195], v[96:99], v[0:3]\n\t"
        "ds_read_b64_tr_b16 a[206:207], v131 offset:768\n\t"
        "s_add_u32 s36, s36, s38\n\t"
        "ds_read_b64_tr_b16 a[208:209], v132 offset:0\n\t"
        "ds_read_b64_tr_b16 a[210:211], v132 offset:768\n\t"
        "v_mfma_f32_16x16x32_bf16 v[4:7], a[196:199], v[96:99], v[4:7]\n\t"
        "ds_read_b64_tr_b16 a[212:213], v133 offset:0\n\t"
        "ds_read_b64_tr_b16 a[214:215], v133 offset:768\n\t"
        "ds_read_b128 v[104:107], v129 offset:8320\n\t"
        "v_pk_max_u16 v78, v62, v64\n\t"
        "v_mfma_f32_16x16x32_bf16 v[8:11], a[200:203], v[96:99], v[8:11]\n\t"
        "ds_read_b128 v[108:111], v129 offset:12416\n\t"
        "ds_read_b64_tr_b16 a[216:217], v131 offset:6144\n\t"
        "v_pk_min_u16 v78, v78, v65\n\t"
        "v_mfma_f32_16x16x32_bf16 v[0:3], a[180:183], v[100:103], v[0:3]\n\t"
        "buffer_load_dwordx4 v140, s[24:27], s36 offen offset:0 lds\n\t"
        "ds_read_b64_tr_b16 a[218:219], v131 offset:6912\n\t"
        "v_cmp_ne_u32 vcc, v78, v62\n\t"
        "v_mfma_f32_16x16x32_bf16 v[4:7], a[184:187], v[100:103], v[4:7]\n\t"
        "ds_read_b64_tr_b16 a[220:221], v132 offset:6144\n\t"
        "ds_read_b64_tr_b16 a[222:223], v132 offset:6912\n\t"
        "v_sub_u32 v78, v77, v67\n\t"
        "v_mfma_f32_16x16x32_bf16 v[8:11], a[188:191], v[100:103], v[8:11]\n\t"
        "ds_read_b64_tr_b16 a[224:225], v133 offset:6144\n\t"
        "ds_read_b64_tr_b16 a[226:227], v133 offset:6912\n\t"
        "v_mad_u32_u24 v79, v77, 6, v66\n\t"
        "v_mfma_f32_16x16x32_bf16 v[12:15], a[192:195], v[100:103], v[12:15]\n\t"
        "ds_read_b128 v[112:115], v129 offset:16512\n\t"
        "v_mad_u32_u24 v79, v76, s62, v79\n\t"
        "v_mfma_f32_16x16x32_bf16 v[16:19], a[196:199], v[100:103], v[16:19]\n\t"
        "buffer_load_dwordx4 v141, s[24:27], s36 offen offset:1024 lds\n\t"
        "s_and_b64 vcc, vcc, s[66:67]\n\t"
        "v_mfma_f32_16x16x32_bf16 v[20:23], a[200:203], v[100:103], v[20:23]\n\t"
        "s_or_b64 s[56:57], s[56:57], vcc\n\t"
        "v_bfe_u32 v80, v78, 2, 1\n\t"
        "s_waitcnt lgkmcnt(8)\n\t"
        "v_mfma_f32_16x16x32_bf16 v[0:3], a[204:207], v[104:107], v[0:3]\n\t"
        "v_or_b32 v80, v80, v79\n\t"
        "v_add_u32 v78, 1, v78\n\t"
        "v_mfma_f32_16x16x32_bf16 v[4:7], a[208:211], v[104:107], v[4:7]\n\t"
        "v_bfe_u32 v78, v78, 2, 1\n\t"
        "v_add3_u32 v78, v79, v78, 6\n\t"
        "v_mfma_f32_16x16x32_bf16 v[8:11], a[212:215], v[104:107], v[8:11]\n\t"
        "v_cndmask_b32 v78, v80, v78, s[44:45]\n\t"
        "v_cndmask_b32 v79, 0, v119, s[64:65]\n\t"
        "v_mfma_f32_16x16x32_bf16 v[12:15], a[180:183], v[104:107], v[12:15]\n\t"
        "buffer_load_dwordx4 v142, s[24:27], s36 offen offset:2048 lds\n\t"
        "v_add_u32 v81, v80, v79\n\t"
        "v_mfma_f32_16x16x32_bf16 v[16:19], a[184:187], v[104:107], v[16:19]\n\t"
        "ds_read_b128 v[84:87], v129 offset:20608\n\t"
        "v_add_u32 v82, v78, v79\n\t"
        "v_mfma_f32_16x16x32_bf16 v[20:23], a[188:191], v[104:107], v[20:23]\n\t"
        "v_cndmask_b32 v78, v81, v78, s[44:45]\n\t"
        "v_lshl_or_b32 v68, v78, 16, v80\n\t"
        "v_mfma_f32_16x16x32_bf16 v[24:27], a[192:195], v[104:107], v[24:27]\n\t"
        "v_lshl_or_b32 v69, v82, 16, v81\n\t"
        "v_mfma_f32_16x16x32_bf16 v[28:31], a[196:199], v[104:107], v[28:31]\n\t"
        "ds_write_b128 v118, v[68:71]\n\t"
        "v_mfma_f32_16x16x32_bf16 v[32:35], a[200:203], v[104:107], v[32:35]\n\t"
        "s_cmp_gt_u32 s3, 16\n\t"
        "s_cselect_b32 s8, 2, 0\n\t"
        "s_waitcnt lgkmcnt(2)\n\t"
        "v_mfma_f32_16x16x32_bf16 v[12:15], a[204:207], v[108:111], v[12:15]\n\t"
        "s_cmp_lg_u64 s[56:57], 0\n\t"
        "s_addc_u32 s8, s8, s6\n\t"
        "v_mfma_f32_16x16x32_bf16 v[16:19], a[208:211], v[108:111], v[16:19]\n\t"
        "ds_read_b128 v[160:163], v185 offset:0\n\t"
        "v_mov_b32 v56, s8\n\t"
        "v_mfma_f32_16x16x32_bf16 v[20:23], a[212:215], v[108:111], v[20:23]\n\t"
        "s_mov_b64 exec, 1\n\t"
        "ds_write_b32 v153, v56\n\t"
        "s_mov_b64 exec, -1\n\t"
        "v_mfma_f32_16x16x32_bf16 v[24:27], a[180:183], v[108:111], v[24:27]\n\t"
        "v_xor_b32 v154, 32, v186\n\t"
        "v_mfma_f32_16x16x32_bf16 v[28:31], a[184:187], v[108:111], v[28:31]\n\t"
        "v_mfma_f32_16x16x32_bf16 v[32:35], a[188:191], v[108:111], v[32:35]\n\t"
        "v_mfma_f32_16x16x32_bf16 v[36:39], a[192:195], v[108:111], v[36:39]\n\t"
        "v_mfma_f32_16x16x32_bf16 v[40:43], a[196:199], v[108:111], v[40:43]\n\t"
        "s_waitcnt vmcnt(9)\n\t"
        "s_barrier\n\t"
        "ds_read_b64_tr_b16 a[228:229], v131 offset:12288\n\t"
        "ds_read_b64_tr_b16 a[230:231], v131 offset:13056\n\t"
        "v_mfma_f32_16x16x32_bf16 v[44:47], a[200:203], v[108:111], v[44:47]\n\t"
        "ds_read_b64_tr_b16 a[232:233], v132 offset:12288\n\t"
        "ds_read_b64_tr_b16 a[234:235], v132 offset:13056\n\t"
        "v_mfma_f32_16x16x32_bf16 v[24:27], a[204:207], v[112:115], v[24:27]\n\t"
        "ds_read_b64_tr_b16 a[236:237], v133 offset:12288\n\t"
        "ds_read_b64_tr_b16 a[238:239], v133 offset:13056\n\t"
        "ds_read_b128 v[88:91], v130 offset:0\n\t"
        "v_mfma_f32_16x16x32_bf16 v[28:31], a[208:211], v[112:115], v[28:31]\n\t"
        "ds_read_b128 v[92:95], v130 offset:4096\n\t"
        "ds_read_b64_tr_b16 a[240:241], v131 offset:18432\n\t"
        "s_waitcnt lgkmcnt(9)\n\t"
        "v_lshlrev_b32_sdwa v157, 5, v160 dst_sel:DWORD dst_unused:UNUSED_PAD src0_sel:DWORD src1_sel:WORD_0\n\t"
        "v_mfma_f32_16x16x32_bf16 v[32:35], a[212:215], v[112:115], v[32:35]\n\t"
        "ds_read_b64_tr_b16 a[242:243], v131 offset:19200\n\t"
        "ds_read_b64_tr_b16 a[244:245], v132 offset:18432\n\t"
        "v_lshlrev_b32_sdwa v158, 5, v160 dst_sel:DWORD dst_unused:UNUSED_PAD src0_sel:DWORD src1_sel:WORD_1\n\t"
        "v_mfma_f32_16x16x32_bf16 v[36:39], a[180:183], v[112:115], v[36:39]\n\t"
        "ds_read_b64_tr_b16 a[246:247], v132 offset:19200\n\t"
        "ds_read_b64_tr_b16 a[248:249], v133 offset:18432\n\t"
        "v_or_b32 v157, v184, v157\n\t"
        "v_mfma_f32_16x16x32_bf16 v[40:43], a[184:187], v[112:115], v[40:43]\n\t"
        "ds_read_b64_tr_b16 a[250:251], v133 offset:19200\n\t"
        "v_or_b32 v158, v184, v158\n\t"
        "v_mfma_f32_16x16x32_bf16 v[44:47], a[188:191], v[112:115], v[44:47]\n\t"
        "ds_read_b128 v[96:99], v130 offset:8192\n\t"
        "v_xor_b32 v159, 32, v157\n\t"
        "v_mfma_f32_16x16x32_bf16 v[36:39], a[204:207], v[84:87], v[36:39]\n\t"
        "ds_read_b64_tr_b16 v[60:61], v157 offset:0\n\t"
        "v_xor_b32 v152, 32, v158\n\t"
        "v_mfma_f32_16x16x32_bf16 v[40:43], a[208:211], v[84:87], v[40:43]\n\t"
        "s_waitcnt vmcnt(6)\n\t"
        "s_barrier\n\t"
        "ds_read_b128 v[100:103], v130 offset:12288\n\t"
        "ds_read_b64_tr_b16 a[252:253], v131 offset:24576\n\t"
        "v_mfma_f32_16x16x32_bf16 v[44:47], a[212:215], v[84:87], v[44:47]\n\t"
        "ds_read_b64_tr_b16 v[62:63], v158 offset:0\n\t"
        "ds_read_b64_tr_b16 a[254:255], v131 offset:25344\n\t"
        "v_perm_b32 v164, v162, v162, v168\n\t"
        "s_waitcnt lgkmcnt(11)\n\t"
        "v_mfma_f32_16x16x32_bf16 v[0:3], a[228:231], v[88:91], v[0:3]\n\t"
        "ds_read_b64_tr_b16 v[64:65], v159 offset:0\n\t"
        "ds_read_b64_tr_b16 v[188:189], v132 offset:24576\n\t"
        "v_perm_b32 v165, v162, v162, v169\n\t"
        "v_mfma_f32_16x16x32_bf16 v[4:7], a[232:235], v[88:91], v[4:7]\n\t"
        "ds_read_b64_tr_b16 v[66:67], v152 offset:0\n\t"
        "ds_read_b64_tr_b16 v[190:191], v132 offset:25344\n\t"
        "v_perm_b32 v166, v162, v162, v170\n\t"
        "v_mfma_f32_16x16x32_bf16 v[8:11], a[236:239], v[88:91], v[8:11]\n\t"
        "ds_read_b64_tr_b16 v[68:69], v157 offset:64\n\t"
        "ds_read_b64_tr_b16 v[192:193], v133 offset:24576\n\t"
        "v_perm_b32 v167, v162, v162, v171\n\t"
        "v_mfma_f32_16x16x32_bf16 v[0:3], a[216:219], v[92:95], v[0:3]\n\t"
        "ds_read_b64_tr_b16 v[70:71], v158 offset:64\n\t"
        "ds_read_b64_tr_b16 v[194:195], v133 offset:25344\n\t"
        "v_cmp_ne_u32 vcc, 0, v163\n\t"
        "v_mfma_f32_16x16x32_bf16 v[4:7], a[220:223], v[92:95], v[4:7]\n\t"
        "ds_read_b64_tr_b16 v[72:73], v159 offset:64\n\t"
        "v_mfma_f32_16x16x32_bf16 v[8:11], a[224:227], v[92:95], v[8:11]\n\t"
        "ds_read_b64_tr_b16 v[74:75], v152 offset:64\n\t"
        "v_mfma_f32_16x16x32_bf16 v[12:15], a[228:231], v[92:95], v[12:15]\n\t"
        "ds_read_b64_tr_b16 v[76:77], v157 offset:128\n\t"
        "v_mfma_f32_16x16x32_bf16 v[16:19], a[232:235], v[92:95], v[16:19]\n\t"
        "ds_read_b64_tr_b16 v[78:79], v158 offset:128\n\t"
        "v_mfma_f32_16x16x32_bf16 v[20:23], a[236:239], v[92:95], v[20:23]\n\t"
        "ds_read_b64_tr_b16 v[80:81], v159 offset:128\n\t"
        "ds_read_b128 v[104:107], v130 offset:16384\n\t"
        "s_waitcnt lgkmcnt(12)\n\t"
        "v_mfma_f32_16x16x32_bf16 v[48:51], v[60:63], v[164:167], 0\n\t"
        "ds_read_b64_tr_b16 v[82:83], v152 offset:128\n\t"
        "ds_read_b64_tr_b16 v[196:197], v131 offset:30720\n\t"
        "v_mfma_f32_16x16x32_bf16 v[0:3], a[240:243], v[96:99], v[0:3]\n\t"
        "ds_read_b64_tr_b16 v[198:199], v131 offset:31488\n\t"
        "ds_read_b64_tr_b16 v[200:201], v132 offset:30720\n\t"
        "s_waitcnt lgkmcnt(12)\n\t"
        "v_mfma_f32_16x16x32_bf16 v[52:55], v[64:67], v[164:167], 0\n\t"
        "ds_read_b64_tr_b16 v[202:203], v132 offset:31488\n\t"
        "ds_read_b64_tr_b16 v[204:205], v133 offset:30720\n\t"
        "v_mfma_f32_16x16x32_bf16 v[4:7], a[244:247], v[96:99], v[4:7]\n\t"
        "ds_read_b64_tr_b16 v[206:207], v133 offset:31488\n\t"
        "s_waitcnt lgkmcnt(13)\n\t"
        "v_mfma_f32_16x16x32_bf16 v[56:59], v[68:71], v[164:167], 0\n\t"
        "v_mfma_f32_16x16x32_bf16 v[8:11], a[248:251], v[96:99], v[8:11]\n\t"
        "s_waitcnt lgkmcnt(11)\n\t"
        "v_mfma_f32_16x16x32_bf16 v[116:119], v[72:75], v[164:167], 0\n\t"
        "v_mfma_f32_16x16x32_bf16 v[12:15], a[216:219], v[96:99], v[12:15]\n\t"
        "ds_read_b128 v[108:111], v130 offset:64\n\t"
        "s_waitcnt lgkmcnt(10)\n\t"
        "v_mfma_f32_16x16x32_bf16 v[120:123], v[76:79], v[164:167], 0\n\t"
        "v_mfma_f32_16x16x32_bf16 v[16:19], a[220:223], v[96:99], v[16:19]\n\t"
        "s_waitcnt lgkmcnt(7)\n\t"
        "v_mfma_f32_16x16x32_bf16 v[124:127], v[80:83], v[164:167], 0\n\t"
        "s_cbranch_vccnz .Lx58%=\n\t"
        ".Lx59%=:\n\t"
        "v_cvt_pk_bf16_f32 v48, v48, v49\n\t"
        "v_mfma_f32_16x16x32_bf16 v[20:23], a[224:227], v[96:99], v[20:23]\n\t"
        "v_cvt_pk_bf16_f32 v49, v50, v51\n\t"
        "v_cvt_pk_bf16_f32 v52, v52, v53\n\t"
        "v_mfma_f32_16x16x32_bf16 v[24:27], a[228:231], v[96:99], v[24:27]\n\t"
        "ds_write_b64 v186, v[48:49] offset:0\n\t"
        "v_mfma_f32_16x16x32_bf16 v[28:31], a[232:235], v[96:99], v[28:31]\n\t"
        "ds_read_b128 v[112:115], v130 offset:4160\n\t"
        "v_cvt_pk_bf16_f32 v53, v54, v55\n\t"
        "v_mfma_f32_16x16x32_bf16 v[32:35], a[236:239], v[96:99], v[32:35]\n\t"
        "ds_write_b64 v154, v[52:53] offset:0\n\t"
        "v_mfma_f32_16x16x32_bf16 v[12:15], a[240:243], v[100:103], v[12:15]\n\t"
        "v_cvt_pk_bf16_f32 v56, v56, v57\n\t"
        "v_cvt_pk_bf16_f32 v57, v58, v59\n\t"
        "v_mfma_f32_16x16x32_bf16 v[16:19], a[244:247], v[100:103], v[16:19]\n\t"
        "ds_write_b64 v186, v[56:57] offset:64\n\t"
        "v_mfma_f32_16x16x32_bf16 v[20:23], a[248:251], v[100:103], v[20:23]\n\t"
        "v_cvt_pk_bf16_f32 v116, v116, v117\n\t"
        "v_cvt_pk_bf16_f32 v117, v118, v119\n\t"
        "v_mfma_f32_16x16x32_bf16 v[24:27], a[216:219], v[100:103], v[24:27]\n\t"
        "ds_write_b64 v154, v[116:117] offset:64\n\t"
        "v_mfma_f32_16x16x32_bf16 v[28:31], a[220:223], v[100:103], v[28:31]\n\t"
        "ds_read_b128 v[84:87], v130 offset:20480\n\t"
        "v_cvt_pk_bf16_f32 v120, v120, v121\n\t"
        "v_mfma_f32_16x16x32_bf16 v[32:35], a[224:227], v[100:103], v[32:35]\n\t"
        "s_waitcnt vmcnt(0) lgkmcnt(6)\n\t"
        "s_barrier\n\t"
        "ds_read_b64_tr_b16 v[208:209], v131 offset:36864\n\t"
        "ds_read_b64_tr_b16 v[210:211], v131 offset:37632\n\t"
        "v_mfma_f32_16x16x32_bf16 v[36:39], a[228:231], v[100:103], v[36:39]\n\t"
        "ds_read_b64_tr_b16 v[212:213], v132 offset:36864\n\t"
        "ds_read_b64_tr_b16 v[214:215], v132 offset:37632\n\t"
        "v_cvt_pk_bf16_f32 v121, v122, v123\n\t"
        "v_mfma_f32_16x16x32_bf16 v[40:43], a[232:235], v[100:103], v[40:43]\n\t"
        "ds_write_b64 v186, v[120:121] offset:128\n\t"
        "ds_read_b64_tr_b16 v[216:217], v133 offset:36864\n\t"
        "v_mfma_f32_16x16x32_bf16 v[44:47], a[236:239], v[100:103], v[44:47]\n\t"
        "ds_read_b64_tr_b16 v[218:219], v133 offset:37632\n\t"
        "v_cvt_pk_bf16_f32 v124, v124, v125\n\t"
        "v_mfma_f32_16x16x32_bf16 v[24:27], a[240:243], v[104:107], v[24:27]\n\t"
        "ds_read_b128 v[88:91], v130 offset:8256\n\t"
        "v_cvt_pk_bf16_f32 v125, v126, v127\n\t"
        "v_mfma_f32_16x16x32_bf16 v[28:31], a[244:247], v[104:107], v[28:31]\n\t"
        "ds_write_b64 v154, v[124:125] offset:128\n\t"
        "v_mfma_f32_16x16x32_bf16 v[32:35], a[248:251], v[104:107], v[32:35]\n\t"
        "ds_read_b64_tr_b16 v[220:221], v131 offset:43008\n\t"
        "v_mfma_f32_16x16x32_bf16 v[36:39], a[216:219], v[104:107], v[36:39]\n\t"
        "ds_read_b64_tr_b16 v[222:223], v131 offset:43776\n\t"
        "v_mfma_f32_16x16x32_bf16 v[40:43], a[220:223], v[104:107], v[40:43]\n\t"
        "ds_read_b64_tr_b16 v[224:225], v132 offset:43008\n\t"
        "v_mfma_f32_16x16x32_bf16 v[44:47], a[224:227], v[104:107], v[44:47]\n\t"
        "ds_read_b64_tr_b16 v[226:227], v132 offset:43776\n\t"
        "ds_read_b64_tr_b16 v[228:229], v133 offset:43008\n\t"
        "v_mfma_f32_16x16x32_bf16 v[0:3], v[196:199], v[108:111], v[0:3]\n\t"
        "ds_read_b128 v[92:95], v130 offset:12352\n\t"
        "ds_read_b64_tr_b16 v[230:231], v133 offset:43776\n\t"
        "v_mfma_f32_16x16x32_bf16 v[4:7], v[200:203], v[108:111], v[4:7]\n\t"
        "ds_read_b64_tr_b16 v[232:233], v131 offset:49152\n\t"
        "v_mfma_f32_16x16x32_bf16 v[8:11], v[204:207], v[108:111], v[8:11]\n\t"
        "ds_read_b64_tr_b16 v[234:235], v131 offset:49920\n\t"
        "s_waitcnt lgkmcnt(11)\n\t"
        "v_mfma_f32_16x16x32_bf16 v[0:3], a[252:255], v[112:115], v[0:3]\n\t"
        "ds_read_b64_tr_b16 v[236:237], v132 offset:49152\n\t"
        "v_mfma_f32_16x16x32_bf16 v[4:7], v[188:191], v[112:115], v[4:7]\n\t"
        "ds_read_b64_tr_b16 v[238:239], v132 offset:49920\n\t"
        "v_mfma_f32_16x16x32_bf16 v[8:11], v[192:195], v[112:115], v[8:11]\n\t"
        "ds_read_b64_tr_b16 v[240:241], v133 offset:49152\n\t"
        "v_mfma_f32_16x16x32_bf16 v[12:15], v[196:199], v[112:115], v[12:15]\n\t"
        "ds_read_b64_tr_b16 v[242:243], v133 offset:49920\n\t"
        "v_mfma_f32_16x16x32_bf16 v[16:19], v[200:203], v[112:115], v[16:19]\n\t"
        "ds_read_b64_tr_b16 v[244:245], v131 offset:55296\n\t"
        "v_mfma_f32_16x16x32_bf16 v[20:23], v[204:207], v[112:115], v[20:23]\n\t"
        "ds_read_b64_tr_b16 v[246:247], v131 offset:56064\n\t"
        "v_mfma_f32_16x16x32_bf16 v[36:39], a[240:243], v[84:87], v[36:39]\n\t"
        "ds_read_b128 v[96:99], v130 offset:128\n\t"
        "v_mfma_f32_16x16x32_bf16 v[40:43], a[244:247], v[84:87], v[40:43]\n\t"
        "ds_read_b64_tr_b16 v[248:249], v132 offset:55296\n\t"
        "v_mfma_f32_16x16x32_bf16 v[44:47], a[248:251], v[84:87], v[44:47]\n\t"
        "ds_read_b64_tr_b16 v[250:251], v132 offset:56064\n\t"
        "s_waitcnt lgkmcnt(10)\n\t"
        "v_mfma_f32_16x16x32_bf16 v[0:3], v[208:211], v[88:91], v[0:3]\n\t"
        "ds_read_b128 v[100:103], v130 offset:4224\n\t"
        "v_mfma_f32_16x16x32_bf16 v[4:7], v[212:215], v[88:91], v[4:7]\n\t"
        "ds_read_b64_tr_b16 v[252:253], v133 offset:55296\n\t"
        "v_mfma_f32_16x16x32_bf16 v[8:11], v[216:219], v[88:91], v[8:11]\n\t"
        "ds_read_b64_tr_b16 v[254:255], v133 offset:56064\n\t"
        "v_mfma_f32_16x16x32_bf16 v[12:15], a[252:255], v[88:91], v[12:15]\n\t"
        "v_mfma_f32_16x16x32_bf16 v[16:19], v[188:191], v[88:91], v[16:19]\n\t"
        "v_mfma_f32_16x16x32_bf16 v[20:23], v[192:195], v[88:91], v[20:23]\n\t"
        "v_mfma_f32_16x16x32_bf16 v[24:27], v[196:199], v[88:91], v[24:27]\n\t"
        "ds_read_b128 v[104:107], v130 offset:8320\n\t"
        "v_mfma_f32_16x16x32_bf16 v[28:31], v[200:203], v[88:91], v[28:31]\n\t"
        "v_mfma_f32_16x16x32_bf16 v[32:35], v[204:207], v[88:91], v[32:35]\n\t"
        "v_mfma_f32_16x16x32_bf16 v[12:15], v[208:211], v[92:95], v[12:15]\n\t"
        "s_waitcnt lgkmcnt(1)\n\t"
        "s_barrier\n\t"
        "v_mfma_f32_16x16x32_bf16 v[16:19], v[212:215], v[92:95], v[16:19]\n\t"
        "ds_read_b128 v[124:127], v182 offset:0\n\t"
        "v_mfma_f32_16x16x32_bf16 v[20:23], v[216:219], v[92:95], v[20:23]\n\t"
        "ds_read_b128 v[132:135], v187 offset:0\n\t"
        "v_mfma_f32_16x16x32_bf16 v[24:27], a[252:255], v[92:95], v[24:27]\n\t"
        "ds_read_b128 v[136:139], v187 offset:16\n\t"
        "v_mfma_f32_16x16x32_bf16 v[28:31], v[188:191], v[92:95], v[28:31]\n\t"
        "ds_read_b32 v140, v187 offset:32\n\t"
        "v_mfma_f32_16x16x32_bf16 v[32:35], v[192:195], v[92:95], v[32:35]\n\t"
        "v_mfma_f32_16x16x32_bf16 v[36:39], v[196:199], v[92:95], v[36:39]\n\t"
        "ds_read_b128 v[108:111], v130 offset:12416\n\t"
        "v_mfma_f32_16x16x32_bf16 v[40:43], v[200:203], v[92:95], v[40:43]\n\t"
        "v_mfma_f32_16x16x32_bf16 v[44:47], v[204:207], v[92:95], v[44:47]\n\t"
        "v_mfma_f32_16x16x32_bf16 v[0:3], v[232:235], v[96:99], v[0:3]\n\t"
        "v_mfma_f32_16x16x32_bf16 v[4:7], v[236:239], v[96:99], v[4:7]\n\t"
        "s_waitcnt lgkmcnt(4)\n\t"
        "v_or3_b32 v124, v124, v125, v126\n\t"
        "v_or_b32 v124, v124, v127\n\t"
        "v_mfma_f32_16x16x32_bf16 v[8:11], v[240:243], v[96:99], v[8:11]\n\t"
        "v_readfirstlane_b32 s6, v124\n\t"
        "v_mfma_f32_16x16x32_bf16 v[0:3], v[220:223], v[100:103], v[0:3]\n\t"
        "s_nop 0\n\t"
        "s_bitcmp1_b32 s6, 1\n\t"
        "s_cbranch_scc1 .Lx60%=\n\t"
        ".Lx61%=:\n\t"
        "v_mfma_f32_16x16x32_bf16 v[4:7], v[224:227], v[100:103], v[4:7]\n\t"
        "s_waitcnt lgkmcnt(2)\n\t"
        "v_xor_b32 v132, v181, v132\n\t"
        "v_add_u32 v151, s9, v172\n\t"
        "v_mfma_f32_16x16x32_bf16 v[8:11], v[228:231], v[100:103], v[8:11]\n\t"
        "v_max_u32 v151, v151, v132\n\t"
        "v_xor_b32 v133, v181, v133\n\t"
        "v_mfma_f32_16x16x32_bf16 v[12:15], v[232:235], v[100:103], v[12:15]\n\t"
        "ds_read_b128 v[112:115], v130 offset:16448\n\t"
        "v_add_u32 v183, s9, v173\n\t"
        "v_mfma_f32_16x16x32_bf16 v[16:19], v[236:239], v[100:103], v[16:19]\n\t"
        "v_max_u32 v183, v183, v133\n\t"
        "v_xor_b32 v134, v181, v134\n\t"
        "v_mfma_f32_16x16x32_bf16 v[20:23], v[240:243], v[100:103], v[20:23]\n\t"
        "v_mfma_f32_16x16x32_bf16 v[0:3], v[244:247], v[104:107], v[0:3]\n\t"
        "v_mfma_f32_16x16x32_bf16 v[4:7], v[248:251], v[104:107], v[4:7]\n\t"
        "v_mfma_f32_16x16x32_bf16 v[8:11], v[252:255], v[104:107], v[8:11]\n\t"
        "v_mfma_f32_16x16x32_bf16 v[12:15], v[220:223], v[104:107], v[12:15]\n\t"
        "ds_read_b128 v[84:87], v130 offset:16512\n\t"
        "v_mfma_f32_16x16x32_bf16 v[16:19], v[224:227], v[104:107], v[16:19]\n\t"
        "v_mfma_f32_16x16x32_bf16 v[20:23], v[228:231], v[104:107], v[20:23]\n\t"
        "v_mfma_f32_16x16x32_bf16 v[24:27], v[232:235], v[104:107], v[24:27]\n\t"
        "s_add_u32 s55, s41, 0\n\t"
        "v_cvt_pk_bf16_f32 v116, v0, v1\n\t"
        "v_mfma_f32_16x16x32_bf16 v[28:31], v[236:239], v[104:107], v[28:31]\n\t"
        "v_cvt_pk_bf16_f32 v117, v2, v3\n\t"
        "v_cvt_pk_bf16_f32 v118, v4, v5\n\t"
        "v_mfma_f32_16x16x32_bf16 v[32:35], v[240:243], v[104:107], v[32:35]\n\t"
        "v_cvt_pk_bf16_f32 v119, v6, v7\n\t"
        "v_cvt_pk_bf16_f32 v120, v8, v9\n\t"
        "s_waitcnt lgkmcnt(1)\n\t"
        "v_mfma_f32_16x16x32_bf16 v[12:15], v[244:247], v[108:111], v[12:15]\n\t"
        "ds_read_b128 v[88:91], v130 offset:20544\n\t"
        "v_cvt_pk_bf16_f32 v121, v10, v11\n\t"
        "v_mfma_f32_16x16x32_bf16 v[16:19], v[248:251], v[108:111], v[16:19]\n\t"
        "v_permlane16_swap_b32 v116, v118\n\t"
        "v_mfma_f32_16x16x32_bf16 v[20:23], v[252:255], v[108:111], v[20:23]\n\t"
        "v_permlane16_swap_b32 v117, v119\n\t"
        "v_mfma_f32_16x16x32_bf16 v[24:27], v[220:223], v[108:111], v[24:27]\n\t"
        "buffer_store_dwordx4 v[116:119], v144, s[28:31], s55 offen sc1\n\t"
        "v_mfma_f32_16x16x32_bf16 v[28:31], v[224:227], v[108:111], v[28:31]\n\t"
        "ds_write_b64 v146, v[120:121] offset:0\n\t"
        "v_mfma_f32_16x16x32_bf16 v[32:35], v[228:231], v[108:111], v[32:35]\n\t"
        "ds_read_b128 v[92:95], v130 offset:20608\n\t"
        "v_mfma_f32_16x16x32_bf16 v[36:39], v[232:235], v[108:111], v[36:39]\n\t"
        "ds_read_b128 v[96:99], v151 offset:0\n\t"
        "s_add_u32 s55, s41, 43008\n\t"
        "v_mfma_f32_16x16x32_bf16 v[40:43], v[236:239], v[108:111], v[40:43]\n\t"
        "v_cvt_pk_bf16_f32 v116, v12, v13\n\t"
        "v_cvt_pk_bf16_f32 v117, v14, v15\n\t"
        "v_mfma_f32_16x16x32_bf16 v[44:47], v[240:243], v[108:111], v[44:47]\n\t"
        "v_cvt_pk_bf16_f32 v118, v16, v17\n\t"
        "v_cvt_pk_bf16_f32 v119, v18, v19\n\t"
        "v_mfma_f32_16x16x32_bf16 v[24:27], v[208:211], v[112:115], v[24:27]\n\t"
        "ds_read_b128 v[100:103], v151 offset:64\n\t"
        "v_cvt_pk_bf16_f32 v120, v20, v21\n\t"
        "v_mfma_f32_16x16x32_bf16 v[28:31], v[212:215], v[112:115], v[28:31]\n\t"
        "v_cvt_pk_bf16_f32 v121, v22, v23\n\t"
        "v_mfma_f32_16x16x32_bf16 v[32:35], v[216:219], v[112:115], v[32:35]\n\t"
        "v_permlane16_swap_b32 v116, v118\n\t"
        "v_mfma_f32_16x16x32_bf16 v[36:39], a[252:255], v[112:115], v[36:39]\n\t"
        "v_permlane16_swap_b32 v117, v119\n\t"
        "v_mfma_f32_16x16x32_bf16 v[40:43], v[188:191], v[112:115], v[40:43]\n\t"
        "ds_read_b128 v[104:107], v151 offset:128\n\t"
        "v_add_u32 v151, s9, v174\n\t"
        "v_mfma_f32_16x16x32_bf16 v[44:47], v[192:195], v[112:115], v[44:47]\n\t"
        "v_max_u32 v151, v151, v134\n\t"
        "v_xor_b32 v135, v181, v135\n\t"
        "s_waitcnt lgkmcnt(4)\n\t"
        "v_mfma_f32_16x16x32_bf16 v[24:27], v[244:247], v[84:87], v[24:27]\n\t"
        "ds_read_b128 v[108:111], v183 offset:0\n\t"
        "v_mfma_f32_16x16x32_bf16 v[28:31], v[248:251], v[84:87], v[28:31]\n\t"
        "v_mfma_f32_16x16x32_bf16 v[32:35], v[252:255], v[84:87], v[32:35]\n\t"
        ".LpS1%=:\n\t"
        "s_cbranch_execz .LrS%=\n\t"
        "buffer_store_dwordx4 v[116:119], v144, s[28:31], s55 offen sc1\n\t"
        "v_mfma_f32_16x16x32_bf16 v[36:39], v[220:223], v[84:87], v[36:39]\n\t"
        "ds_write_b64 v146, v[120:121] offset:2048\n\t"
        "v_mfma_f32_16x16x32_bf16 v[40:43], v[224:227], v[84:87], v[40:43]\n\t"
        "ds_read_b128 v[112:115], v183 offset:64\n\t"
        "v_mfma_f32_16x16x32_bf16 v[44:47], v[228:231], v[84:87], v[44:47]\n\t"
        "v_mfma_f32_16x16x32_bf16 v[36:39], v[208:211], v[88:91], v[36:39]\n\t"
        "ds_read_b128 v[84:87], v183 offset:128\n\t"
        "v_add_u32 v183, s9, v175\n\t"
        "v_mfma_f32_16x16x32_bf16 v[40:43], v[212:215], v[88:91], v[40:43]\n\t"
        "v_max_u32 v183, v183, v135\n\t"
        "v_xor_b32 v136, v181, v136\n\t"
        "v_mfma_f32_16x16x32_bf16 v[44:47], v[216:219], v[88:91], v[44:47]\n\t"
        "ds_read_b128 v[132:135], v187 offset:2048\n\t"
        "s_add_u32 s55, s41, 86016\n\t"
        "s_waitcnt lgkmcnt(5)\n\t"
        "v_mfma_f32_16x16x32_bf16 v[36:39], v[244:247], v[92:95], v[36:39]\n\t"
        "ds_read_b128 v[88:91], v151 offset:0\n\t"
        "v_cvt_pk_bf16_f32 v116, v24, v25\n\t"
        "v_mfma_f32_16x16x32_bf16 v[40:43], v[248:251], v[92:95], v[40:43]\n\t"
        "v_cvt_pk_bf16_f32 v117, v26, v27\n\t"
        "v_cvt_pk_bf16_f32 v118, v28, v29\n\t"
        "v_mfma_f32_16x16x32_bf16 v[44:47], v[252:255], v[92:95], v[44:47]\n\t"
        "v_cvt_pk_bf16_f32 v119, v30, v31\n\t"
        "v_cvt_pk_bf16_f32 v120, v32, v33\n\t"
        "v_mfma_f32_16x16x32_bf16 v[48:51], a[12:15], v[96:99], 0\n\t"
        "ds_read_b128 v[92:95], v151 offset:64\n\t"
        "v_cvt_pk_bf16_f32 v121, v34, v35\n\t"
        "v_mfma_f32_16x16x32_bf16 v[52:55], a[16:19], v[96:99], 0\n\t"
        "v_permlane16_swap_b32 v116, v118\n\t"
        "v_mfma_f32_16x16x32_bf16 v[56:59], a[20:23], v[96:99], 0\n\t"
        "v_permlane16_swap_b32 v117, v119\n\t"
        "v_mfma_f32_16x16x32_bf16 v[48:51], a[48:51], v[100:103], v[48:51]\n\t"
        "ds_read_b128 v[96:99], v151 offset:128\n\t"
        "v_add_u32 v151, s9, v176\n\t"
        "v_mfma_f32_16x16x32_bf16 v[52:55], a[52:55], v[100:103], v[52:55]\n\t"
        "buffer_store_dwordx4 v[116:119], v144, s[28:31], s55 offen sc1\n\t"
        "v_mfma_f32_16x16x32_bf16 v[56:59], a[56:59], v[100:103], v[56:59]\n\t"
        "ds_write_b64 v146, v[120:121] offset:4096\n\t"
        "v_mfma_f32_16x16x32_bf16 v[48:51], a[84:87], v[104:107], v[48:51]\n\t"
        "ds_read_b128 v[100:103], v183 offset:0\n\t"
        "v_max_u32 v151, v151, v136\n\t"
        "v_mfma_f32_16x16x32_bf16 v[52:55], a[88:91], v[104:107], v[52:55]\n\t"
        "v_xor_b32 v137, v181, v137\n\t"
        "s_add_u32 s55, s41, 129024\n\t"
        "v_mfma_f32_16x16x32_bf16 v[56:59], a[92:95], v[104:107], v[56:59]\n\t"
        "v_cvt_pk_bf16_f32 v116, v36, v37\n\t"
        "v_cvt_pk_bf16_f32 v117, v38, v39\n\t"
        "s_waitcnt lgkmcnt(4)\n\t"
        "v_mfma_f32_16x16x32_bf16 v[48:51], a[120:123], v[108:111], v[48:51]\n\t"
        "ds_read_b128 v[104:107], v183 offset:64\n\t"
        "v_cvt_pk_bf16_f32 v118, v40, v41\n\t"
        "v_mfma_f32_16x16x32_bf16 v[52:55], a[124:127], v[108:111], v[52:55]\n\t"
        "v_cvt_pk_bf16_f32 v119, v42, v43\n\t"
        "v_cvt_pk_bf16_f32 v120, v44, v45\n\t"
        "v_mfma_f32_16x16x32_bf16 v[56:59], a[128:131], v[108:111], v[56:59]\n\t"
        "v_cvt_pk_bf16_f32 v121, v46, v47\n\t"
        "v_mfma_f32_16x16x32_bf16 v[48:51], a[156:159], v[112:115], v[48:51]\n\t"
        "v_permlane16_swap_b32 v116, v118\n\t"
        "v_mfma_f32_16x16x32_bf16 v[52:55], a[160:163], v[112:115], v[52:55]\n\t"
        "v_permlane16_swap_b32 v117, v119\n\t"
        "v_mfma_f32_16x16x32_bf16 v[56:59], a[164:167], v[112:115], v[56:59]\n\t"
        "ds_read_b128 v[108:111], v183 offset:128\n\t"
        "v_add_u32 v183, s9, v177\n\t"
        "v_mfma_f32_16x16x32_bf16 v[48:51], a[192:195], v[84:87], v[48:51]\n\t"
        "ds_read_b128 v[112:115], v151 offset:0\n\t"
        "v_max_u32 v183, v183, v137\n\t"
        "v_mfma_f32_16x16x32_bf16 v[52:55], a[196:199], v[84:87], v[52:55]\n\t"
        "v_xor_b32 v138, v181, v138\n\t"
        "v_mfma_f32_16x16x32_bf16 v[56:59], a[200:203], v[84:87], v[56:59]\n\t"
        "v_mfma_f32_16x16x32_bf16 v[48:51], a[228:231], v[88:91], v[48:51]\n\t"
        "buffer_store_dwordx4 v[116:119], v144, s[28:31], s55 offen sc1\n\t"
        "v_mfma_f32_16x16x32_bf16 v[52:55], a[232:235], v[88:91], v[52:55]\n\t"
        "ds_write_b64 v146, v[120:121] offset:6144\n\t"
        "v_mfma_f32_16x16x32_bf16 v[56:59], a[236:239], v[88:91], v[56:59]\n\t"
        "ds_read_b128 v[84:87], v151 offset:64\n\t"
        "s_waitcnt lgkmcnt(4)\n\t"
        "v_mfma_f32_16x16x32_bf16 v[48:51], v[196:199], v[92:95], v[48:51]\n\t"
        "ds_read_b128 v[88:91], v151 offset:128\n\t"
        "v_add_u32 v151, s9, v178\n\t"
        "v_mfma_f32_16x16x32_bf16 v[52:55], v[200:203], v[92:95], v[52:55]\n\t"
        "v_max_u32 v151, v151, v138\n\t"
        "v_xor_b32 v139, v181, v139\n\t"
        "v_mfma_f32_16x16x32_bf16 v[56:59], v[204:207], v[92:95], v[56:59]\n\t"
        "v_mfma_f32_16x16x32_bf16 v[48:51], v[232:235], v[96:99], v[48:51]\n\t"
        "ds_read_b128 v[92:95], v183 offset:0\n\t"
        "v_mfma_f32_16x16x32_bf16 v[52:55], v[236:239], v[96:99], v[52:55]\n\t"
        "s_waitcnt lgkmcnt(3)\n\t"
        "s_barrier\n\t"
        "v_mfma_f32_16x16x32_bf16 v[56:59], v[240:243], v[96:99], v[56:59]\n\t"
        "ds_read_b128 v[124:127], v147 offset:0\n\t"
        "v_mfma_f32_16x16x32_bf16 v[48:51], a[0:3], v[100:103], v[48:51]\n\t"
        "ds_read_b128 v[96:99], v183 offset:64\n\t"
        "v_mfma_f32_16x16x32_bf16 v[52:55], a[4:7], v[100:103], v[52:55]\n\t"
        "v_mfma_f32_16x16x32_bf16 v[56:59], a[8:11], v[100:103], v[56:59]\n\t"
        "v_mfma_f32_16x16x32_bf16 v[48:51], a[36:39], v[104:107], v[48:51]\n\t"
        "ds_read_b128 v[100:103], v183 offset:128\n\t"
        "v_add_u32 v183, s9, v179\n\t"
        "v_mfma_f32_16x16x32_bf16 v[52:55], a[40:43], v[104:107], v[52:55]\n\t"
        "v_max_u32 v183, v183, v139\n\t"
        "v_xor_b32 v140, v181, v140\n\t"
        "v_mfma_f32_16x16x32_bf16 v[56:59], a[44:47], v[104:107], v[56:59]\n\t"
        "ds_read_b128 v[136:139], v187 offset:2064\n\t"
        "v_mfma_f32_16x16x32_bf16 v[48:51], a[72:75], v[108:111], v[48:51]\n\t"
        "ds_read_b128 v[104:107], v151 offset:0\n\t"
        "v_mfma_f32_16x16x32_bf16 v[52:55], a[76:79], v[108:111], v[52:55]\n\t"
        "v_mfma_f32_16x16x32_bf16 v[56:59], a[80:83], v[108:111], v[56:59]\n\t"
        "s_waitcnt lgkmcnt(4)\n\t"
        "buffer_store_dwordx4 v[124:127], v145, s[28:31], s50 offen sc1\n\t"
        "v_mfma_f32_16x16x32_bf16 v[48:51], a[108:111], v[112:115], v[48:51]\n\t"
        "ds_read_b128 v[124:127], v147 offset:4096\n\t"
        "s_add_u32 s50, s50, 86016\n\t"
        "v_mfma_f32_16x16x32_bf16 v[52:55], a[112:115], v[112:115], v[52:55]\n\t"
        "ds_read_b128 v[108:111], v151 offset:64\n\t"
        "v_mfma_f32_16x16x32_bf16 v[56:59], a[116:119], v[112:115], v[56:59]\n\t"
        "v_mfma_f32_16x16x32_bf16 v[48:51], a[144:147], v[84:87], v[48:51]\n\t"
        "ds_read_b128 v[112:115], v151 offset:128\n\t"
        "v_add_u32 v151, s9, v180\n\t"
        "v_mfma_f32_16x16x32_bf16 v[52:55], a[148:151], v[84:87], v[52:55]\n\t"
        "v_max_u32 v151, v151, v140\n\t"
        "v_xor_b32 v132, v181, v132\n\t"
        "v_mfma_f32_16x16x32_bf16 v[56:59], a[152:155], v[84:87], v[56:59]\n\t"
        "ds_read_b32 v140, v187 offset:2080\n\t"
        "v_mfma_f32_16x16x32_bf16 v[48:51], a[180:183], v[88:91], v[48:51]\n\t"
        "ds_read_b128 v[84:87], v183 offset:0\n\t"
        "v_mfma_f32_16x16x32_bf16 v[52:55], a[184:187], v[88:91], v[52:55]\n\t"
        "v_mfma_f32_16x16x32_bf16 v[56:59], a[188:191], v[88:91], v[56:59]\n\t"
        "v_mfma_f32_16x16x32_bf16 v[48:51], a[216:219], v[92:95], v[48:51]\n\t"
        "ds_read_b128 v[88:91], v183 offset:64\n\t"
        "v_mfma_f32_16x16x32_bf16 v[52:55], a[220:223], v[92:95], v[52:55]\n\t"
        "v_mfma_f32_16x16x32_bf16 v[56:59], a[224:227], v[92:95], v[56:59]\n\t"
        "s_waitcnt lgkmcnt(4)\n\t"
        "v_mfma_f32_16x16x32_bf16 v[48:51], a[252:255], v[96:99], v[48:51]\n\t"
        "ds_read_b128 v[92:95], v183 offset:128\n\t"
        "v_readlane_b32 s9, v155, 47\n\t"
        "v_mfma_f32_16x16x32_bf16 v[52:55], v[188:191], v[96:99], v[52:55]\n\t"
        "buffer_store_dwordx4 v[124:127], v145, s[28:31], s50 offen sc1\n\t"
        "v_mfma_f32_16x16x32_bf16 v[56:59], v[192:195], v[96:99], v[56:59]\n\t"
        "v_add_u32 v183, s9, v172\n\t"
        "v_max_u32 v183, v183, v132\n\t"
        "v_mfma_f32_16x16x32_bf16 v[48:51], v[220:223], v[100:103], v[48:51]\n\t"
        "ds_read_b128 v[96:99], v151 offset:0\n\t"
        "v_xor_b32 v133, v181, v133\n\t"
        "v_mfma_f32_16x16x32_bf16 v[52:55], v[224:227], v[100:103], v[52:55]\n\t"
        "v_mfma_f32_16x16x32_bf16 v[56:59], v[228:231], v[100:103], v[56:59]\n\t"
        "v_mfma_f32_16x16x32_bf16 v[48:51], a[24:27], v[104:107], v[48:51]\n\t"
        "ds_read_b128 v[100:103], v151 offset:64\n\t"
        "v_mfma_f32_16x16x32_bf16 v[52:55], a[28:31], v[104:107], v[52:55]\n\t"
        "v_mfma_f32_16x16x32_bf16 v[56:59], a[32:35], v[104:107], v[56:59]\n\t"
        "v_mfma_f32_16x16x32_bf16 v[48:51], a[60:63], v[108:111], v[48:51]\n\t"
        "ds_read_b128 v[104:107], v151 offset:128\n\t"
        "v_add_u32 v151, s9, v173\n\t"
        "v_mfma_f32_16x16x32_bf16 v[52:55], a[64:67], v[108:111], v[52:55]\n\t"
        "v_max_u32 v151, v151, v133\n\t"
        "v_xor_b32 v134, v181, v134\n\t"
        "v_mfma_f32_16x16x32_bf16 v[56:59], a[68:71], v[108:111], v[56:59]\n\t"
        "s_waitcnt lgkmcnt(3)\n\t"
        "v_mfma_f32_16x16x32_bf16 v[48:51], a[96:99], v[112:115], v[48:51]\n\t"
        "ds_read_b128 v[108:111], v183 offset:0\n\t"
        "v_mfma_f32_16x16x32_bf16 v[52:55], a[100:103], v[112:115], v[52:55]\n\t"
        "v_mfma_f32_16x16x32_bf16 v[56:59], a[104:107], v[112:115], v[56:59]\n\t"
        "v_mfma_f32_16x16x32_bf16 v[48:51], a[132:135], v[84:87], v[48:51]\n\t"
        "ds_read_b128 v[112:115], v183 offset:64\n\t"
        "v_mfma_f32_16x16x32_bf16 v[52:55], a[136:139], v[84:87], v[52:55]\n\t"
        "v_mfma_f32_16x16x32_bf16 v[56:59], a[140:143], v[84:87], v[56:59]\n\t"
        "v_mfma_f32_16x16x32_bf16 v[48:51], a[168:171], v[88:91], v[48:51]\n\t"
        "ds_read_b128 v[84:87], v183 offset:128\n\t"
        "v_add_u32 v183, s9, v174\n\t"
        "v_mfma_f32_16x16x32_bf16 v[52:55], a[172:175], v[88:91], v[52:55]\n\t"
        "v_max_u32 v183, v183, v134\n\t"
        "v_xor_b32 v135, v181, v135\n\t"
        "v_mfma_f32_16x16x32_bf16 v[56:59], a[176:179], v[88:91], v[56:59]\n\t"
        "v_mfma_f32_16x16x32_bf16 v[48:51], a[204:207], v[92:95], v[48:51]\n\t"
        "ds_read_b128 v[88:91], v151 offset:0\n\t"
        "v_mfma_f32_16x16x32_bf16 v[52:55], a[208:211], v[92:95], v[52:55]\n\t"
        "v_mfma_f32_16x16x32_bf16 v[56:59], a[212:215], v[92:95], v[56:59]\n\t"
        "s_waitcnt lgkmcnt(3)\n\t"
        "v_mfma_f32_16x16x32_bf16 v[48:51], a[240:243], v[96:99], v[48:51]\n\t"
        "ds_read_b128 v[92:95], v151 offset:64\n\t"
        "v_mfma_f32_16x16x32_bf16 v[52:55], a[244:247], v[96:99], v[52:55]\n\t"
        "v_mfma_f32_16x16x32_bf16 v[56:59], a[248:251], v[96:99], v[56:59]\n\t"
        "v_mfma_f32_16x16x32_bf16 v[48:51], v[208:211], v[100:103], v[48:51]\n\t"
        "ds_read_b128 v[96:99], v151 offset:128\n\t"
        "v_add_u32 v151, s9, v175\n\t"
        "v_mfma_f32_16x16x32_bf16 v[52:55], v[212:215], v[100:103], v[52:55]\n\t"
        "v_max_u32 v151, v151, v135\n\t"
        "v_xor_b32 v136, v181, v136\n\t"
        "v_mfma_f32_16x16x32_bf16 v[56:59], v[216:219], v[100:103], v[56:59]\n\t"
        "ds_read_b128 v[132:135], v187 offset:4096\n\t"
        "v_mfma_f32_16x16x32_bf16 v[48:51], v[244:247], v[104:107], v[48:51]\n\t"
        "ds_read_b128 v[100:103], v183 offset:0\n\t"
        "v_mfma_f32_16x16x32_bf16 v[52:55], v[248:251], v[104:107], v[52:55]\n\t"
        "v_mfma_f32_16x16x32_bf16 v[56:59], v[252:255], v[104:107], v[56:59]\n\t"
        "v_mfma_f32_16x16x32_bf16 v[60:63], a[12:15], v[108:111], 0\n\t"
        "ds_read_b128 v[104:107], v183 offset:64\n\t"
        "v_mfma_f32_16x16x32_bf16 v[64:67], a[16:19], v[108:111], 0\n\t"
        "v_mfma_f32_16x16x32_bf16 v[68:71], a[20:23], v[108:111], 0\n\t"
        "s_waitcnt lgkmcnt(4)\n\t"
        "v_mfma_f32_16x16x32_bf16 v[60:63], a[48:51], v[112:115], v[60:63]\n\t"
        "ds_read_b128 v[108:111], v183 offset:128\n\t"
        "v_add_u32 v183, s9, v176\n\t"
        "v_mfma_f32_16x16x32_bf16 v[64:67], a[52:55], v[112:115], v[64:67]\n\t"
        "v_max_u32 v183, v183, v136\n\t"
        "v_xor_b32 v137, v181, v137\n\t"
        "v_mfma_f32_16x16x32_bf16 v[68:71], a[56:59], v[112:115], v[68:71]\n\t"
        "v_readlane_b32 s55, v155, 49\n\t"
        "v_cvt_pk_bf16_f32 v116, v48, v49\n\t"
        "v_mfma_f32_16x16x32_bf16 v[60:63], a[84:87], v[84:87], v[60:63]\n\t"
        "ds_read_b128 v[112:115], v151 offset:0\n\t"
        "v_cvt_pk_bf16_f32 v117, v50, v51\n\t"
        "v_mfma_f32_16x16x32_bf16 v[64:67], a[88:91], v[84:87], v[64:67]\n\t"
        "v_cvt_pk_bf16_f32 v118, v52, v53\n\t"
        "v_cvt_pk_bf16_f32 v119, v54, v55\n\t"
        "v_mfma_f32_16x16x32_bf16 v[68:71], a[92:95], v[84:87], v[68:71]\n\t"
        "v_cvt_pk_bf16_f32 v120, v56, v57\n\t"
        "v_cvt_pk_bf16_f32 v121, v58, v59\n\t"
        "v_mfma_f32_16x16x32_bf16 v[60:63], a[120:123], v[88:91], v[60:63]\n\t"
        "v_permlane16_swap_b32 v116, v118\n\t"
        "v_mfma_f32_16x16x32_bf16 v[64:67], a[124:127], v[88:91], v[64:67]\n\t"
        "v_permlane16_swap_b32 v117, v119\n\t"
        "v_mfma_f32_16x16x32_bf16 v[68:71], a[128:131], v[88:91], v[68:71]\n\t"
        "buffer_store_dwordx4 v[116:119], v148, s[28:31], s55 offen sc1\n\t"
        "v_mfma_f32_16x16x32_bf16 v[60:63], a[156:159], v[92:95], v[60:63]\n\t"
        "ds_write_b64 v146, v[120:121] offset:8192\n\t"
        "v_mfma_f32_16x16x32_bf16 v[64:67], a[160:163], v[92:95], v[64:67]\n\t"
        "ds_read_b128 v[84:87], v151 offset:64\n\t"
        "v_mfma_f32_16x16x32_bf16 v[68:71], a[164:167], v[92:95], v[68:71]\n\t"
        "ds_read_b128 v[88:91], v151 offset:128\n\t"
        "v_add_u32 v151, s9, v177\n\t"
        "s_waitcnt lgkmcnt(4)\n\t"
        "v_mfma_f32_16x16x32_bf16 v[60:63], a[192:195], v[96:99], v[60:63]\n\t"
        "ds_read_b128 v[92:95], v183 offset:0\n\t"
        "v_max_u32 v151, v151, v137\n\t"
        "v_mfma_f32_16x16x32_bf16 v[64:67], a[196:199], v[96:99], v[64:67]\n\t"
        "v_xor_b32 v138, v181, v138\n\t"
        "v_mfma_f32_16x16x32_bf16 v[68:71], a[200:203], v[96:99], v[68:71]\n\t"
        "v_mfma_f32_16x16x32_bf16 v[60:63], a[228:231], v[100:103], v[60:63]\n\t"
        "s_waitcnt lgkmcnt(3)\n\t"
        "s_barrier\n\t"
        "v_mfma_f32_16x16x32_bf16 v[64:67], a[232:235], v[100:103], v[64:67]\n\t"
        "ds_read_b128 v[124:127], v147 offset:8192\n\t"
        "v_mfma_f32_16x16x32_bf16 v[68:71], a[236:239], v[100:103], v[68:71]\n\t"
        "ds_read_b128 v[96:99], v183 offset:64\n\t"
        "v_mfma_f32_16x16x32_bf16 v[60:63], v[196:199], v[104:107], v[60:63]\n\t"
        "ds_read_b128 v[100:103], v183 offset:128\n\t"
        "v_add_u32 v183, s9, v178\n\t"
        "v_mfma_f32_16x16x32_bf16 v[64:67], v[200:203], v[104:107], v[64:67]\n\t"
        "v_max_u32 v183, v183, v138\n\t"
        "v_xor_b32 v139, v181, v139\n\t"
        "v_mfma_f32_16x16x32_bf16 v[68:71], v[204:207], v[104:107], v[68:71]\n\t"
        "v_mfma_f32_16x16x32_bf16 v[60:63], v[232:235], v[108:111], v[60:63]\n\t"
        "ds_read_b128 v[104:107], v151 offset:0\n\t"
        "v_mfma_f32_16x16x32_bf16 v[64:67], v[236:239], v[108:111], v[64:67]\n\t"
        "v_mfma_f32_16x16x32_bf16 v[68:71], v[240:243], v[108:111], v[68:71]\n\t"
        "v_mfma_f32_16x16x32_bf16 v[60:63], a[0:3], v[112:115], v[60:63]\n\t"
        "ds_read_b128 v[108:111], v151 offset:64\n\t"
        "v_mfma_f32_16x16x32_bf16 v[64:67], a[4:7], v[112:115], v[64:67]\n\t"
        "s_waitcnt lgkmcnt(4)\n\t"
        "buffer_store_dwordx4 v[124:127], v150, s[28:31], s51 offen sc1\n\t"
        "v_mfma_f32_16x16x32_bf16 v[68:71], a[8:11], v[112:115], v[68:71]\n\t"
        "v_mfma_f32_16x16x32_bf16 v[60:63], a[36:39], v[84:87], v[60:63]\n\t"
        "ds_read_b128 v[112:115], v151 offset:128\n\t"
        "v_add_u32 v151, s9, v179\n\t"
        "v_mfma_f32_16x16x32_bf16 v[64:67], a[40:43], v[84:87], v[64:67]\n\t"
        "v_max_u32 v151, v151, v139\n\t"
        "v_xor_b32 v140, v181, v140\n\t"
        "v_mfma_f32_16x16x32_bf16 v[68:71], a[44:47], v[84:87], v[68:71]\n\t"
        "ds_read_b128 v[136:139], v187 offset:4112\n\t"
        "v_mfma_f32_16x16x32_bf16 v[60:63], a[72:75], v[88:91], v[60:63]\n\t"
        "ds_read_b128 v[84:87], v183 offset:0\n\t"
        "v_mfma_f32_16x16x32_bf16 v[64:67], a[76:79], v[88:91], v[64:67]\n\t"
        "v_mfma_f32_16x16x32_bf16 v[68:71], a[80:83], v[88:91], v[68:71]\n\t"
        "v_mfma_f32_16x16x32_bf16 v[60:63], a[108:111], v[92:95], v[60:63]\n\t"
        "ds_read_b128 v[88:91], v183 offset:64\n\t"
        "v_mfma_f32_16x16x32_bf16 v[64:67], a[112:115], v[92:95], v[64:67]\n\t"
        "v_mfma_f32_16x16x32_bf16 v[68:71], a[116:119], v[92:95], v[68:71]\n\t"
        "s_waitcnt lgkmcnt(4)\n\t"
        "v_mfma_f32_16x16x32_bf16 v[60:63], a[144:147], v[96:99], v[60:63]\n\t"
        "ds_read_b128 v[92:95], v183 offset:128\n\t"
        "v_add_u32 v183, s9, v180\n\t"
        "v_mfma_f32_16x16x32_bf16 v[64:67], a[148:151], v[96:99], v[64:67]\n\t"
        "v_max_u32 v183, v183, v140\n\t"
        "v_xor_b32 v132, v181, v132\n\t"
        "v_mfma_f32_16x16x32_bf16 v[68:71], a[152:155], v[96:99], v[68:71]\n\t"
        "ds_read_b32 v140, v187 offset:4128\n\t"
        "v_mfma_f32_16x16x32_bf16 v[60:63], a[180:183], v[100:103], v[60:63]\n\t"
        "ds_read_b128 v[96:99], v151 offset:0\n\t"
        "v_mfma_f32_16x16x32_bf16 v[64:67], a[184:187], v[100:103], v[64:67]\n\t"
        "v_mfma_f32_16x16x32_bf16 v[68:71], a[188:191], v[100:103], v[68:71]\n\t"
        "v_mfma_f32_16x16x32_bf16 v[60:63], a[216:219], v[104:107], v[60:63]\n\t"
        "ds_read_b128 v[100:103], v151 offset:64\n\t"
        "v_mfma_f32_16x16x32_bf16 v[64:67], a[220:223], v[104:107], v[64:67]\n\t"
        "v_mfma_f32_16x16x32_bf16 v[68:71], a[224:227], v[104:107], v[68:71]\n\t"
        "v_mfma_f32_16x16x32_bf16 v[60:63], a[252:255], v[108:111], v[60:63]\n\t"
        "ds_read_b128 v[104:107], v151 offset:128\n\t"
        "v_readlane_b32 s9, v155, 48\n\t"
        "v_mfma_f32_16x16x32_bf16 v[64:67], v[188:191], v[108:111], v[64:67]\n\t"
        "s_nop 0\n\t"
        "v_add_u32 v151, s9, v172\n\t"
        "v_max_u32 v151, v151, v132\n\t"
        "v_mfma_f32_16x16x32_bf16 v[68:71], v[192:195], v[108:111], v[68:71]\n\t"
        "v_xor_b32 v133, v181, v133\n\t"
        "s_waitcnt lgkmcnt(4)\n\t"
        "v_mfma_f32_16x16x32_bf16 v[60:63], v[220:223], v[112:115], v[60:63]\n\t"
        "ds_read_b128 v[108:111], v183 offset:0\n\t"
        "v_mfma_f32_16x16x32_bf16 v[64:67], v[224:227], v[112:115], v[64:67]\n\t"
        "v_mfma_f32_16x16x32_bf16 v[68:71], v[228:231], v[112:115], v[68:71]\n\t"
        "v_mfma_f32_16x16x32_bf16 v[60:63], a[24:27], v[84:87], v[60:63]\n\t"
        "ds_read_b128 v[112:115], v183 offset:64\n\t"
        "v_mfma_f32_16x16x32_bf16 v[64:67], a[28:31], v[84:87], v[64:67]\n\t"
        "v_mfma_f32_16x16x32_bf16 v[68:71], a[32:35], v[84:87], v[68:71]\n\t"
        "v_mfma_f32_16x16x32_bf16 v[60:63], a[60:63], v[88:91], v[60:63]\n\t"
        "ds_read_b128 v[84:87], v183 offset:128\n\t"
        "v_add_u32 v183, s9, v173\n\t"
        "v_mfma_f32_16x16x32_bf16 v[64:67], a[64:67], v[88:91], v[64:67]\n\t"
        "v_max_u32 v183, v183, v133\n\t"
        "v_xor_b32 v134, v181, v134\n\t"
        "v_mfma_f32_16x16x32_bf16 v[68:71], a[68:71], v[88:91], v[68:71]\n\t"
        "v_mfma_f32_16x16x32_bf16 v[60:63], a[96:99], v[92:95], v[60:63]\n\t"
        "ds_read_b128 v[88:91], v151 offset:0\n\t"
        "v_mfma_f32_16x16x32_bf16 v[64:67], a[100:103], v[92:95], v[64:67]\n\t"
        "v_mfma_f32_16x16x32_bf16 v[68:71], a[104:107], v[92:95], v[68:71]\n\t"
        "s_waitcnt lgkmcnt(3)\n\t"
        "v_mfma_f32_16x16x32_bf16 v[60:63], a[132:135], v[96:99], v[60:63]\n\t"
        "ds_read_b128 v[92:95], v151 offset:64\n\t"
        "v_mfma_f32_16x16x32_bf16 v[64:67], a[136:139], v[96:99], v[64:67]\n\t"
        "v_mfma_f32_16x16x32_bf16 v[68:71], a[140:143], v[96:99], v[68:71]\n\t"
        "v_mfma_f32_16x16x32_bf16 v[60:63], a[168:171], v[100:103], v[60:63]\n\t"
        "ds_read_b128 v[96:99], v151 offset:128\n\t"
        "v_add_u32 v151, s9, v174\n\t"
        "v_mfma_f32_16x16x32_bf16 v[64:67], a[172:175], v[100:103], v[64:67]\n\t"
        "v_max_u32 v151, v151, v134\n\t"
        "v_xor_b32 v135, v181, v135\n\t"
        "v_mfma_f32_16x16x32_bf16 v[68:71], a[176:179], v[100:103], v[68:71]\n\t"
        "v_mfma_f32_16x16x32_bf16 v[60:63], a[204:207], v[104:107], v[60:63]\n\t"
        "ds_read_b128 v[100:103], v183 offset:0\n\t"
        "v_mfma_f32_16x16x32_bf16 v[64:67], a[208:211], v[104:107], v[64:67]\n\t"
        "v_mfma_f32_16x16x32_bf16 v[68:71], a[212:215], v[104:107], v[68:71]\n\t"
        "v_mfma_f32_16x16x32_bf16 v[60:63], a[240:243], v[108:111], v[60:63]\n\t"
        "ds_read_b128 v[104:107], v183 offset:64\n\t"
        "v_mfma_f32_16x16x32_bf16 v[64:67], a[244:247], v[108:111], v[64:67]\n\t"
        "v_mfma_f32_16x16x32_bf16 v[68:71], a[248:251], v[108:111], v[68:71]\n\t"
        "s_waitcnt lgkmcnt(3)\n\t"
        "v_mfma_f32_16x16x32_bf16 v[60:63], v[208:211], v[112:115], v[60:63]\n\t"
        "ds_read_b128 v[108:111], v183 offset:128\n\t"
        "v_add_u32 v183, s9, v175\n\t"
        "v_mfma_f32_16x16x32_bf16 v[64:67], v[212:215], v[112:115], v[64:67]\n\t"
        "v_max_u32 v183, v183, v135\n\t"
        "v_xor_b32 v136, v181, v136\n\t"
        "v_mfma_f32_16x16x32_bf16 v[68:71], v[216:219], v[112:115], v[68:71]\n\t"
        "v_mfma_f32_16x16x32_bf16 v[60:63], v[244:247], v[84:87], v[60:63]\n\t"
        "ds_read_b128 v[112:115], v151 offset:0\n\t"
        "v_mfma_f32_16x16x32_bf16 v[64:67], v[248:251], v[84:87], v[64:67]\n\t"
        "v_mfma_f32_16x16x32_bf16 v[68:71], v[252:255], v[84:87], v[68:71]\n\t"
        "v_mfma_f32_16x16x32_bf16 v[72:75], a[12:15], v[88:91], 0\n\t"
        "ds_read_b128 v[84:87], v151 offset:64\n\t"
        "v_mfma_f32_16x16x32_bf16 v[76:79], a[16:19], v[88:91], 0\n\t"
        "v_mfma_f32_16x16x32_bf16 v[80:83], a[20:23], v[88:91], 0\n\t"
        "v_mfma_f32_16x16x32_bf16 v[72:75], a[48:51], v[92:95], v[72:75]\n\t"
        "ds_read_b128 v[88:91], v151 offset:128\n\t"
        "v_add_u32 v151, s9, v176\n\t"
        "v_mfma_f32_16x16x32_bf16 v[76:79], a[52:55], v[92:95], v[76:79]\n\t"
        "v_max_u32 v151, v151, v136\n\t"
        "v_xor_b32 v137, v181, v137\n\t"
        "v_mfma_f32_16x16x32_bf16 v[80:83], a[56:59], v[92:95], v[80:83]\n\t"
        "v_readlane_b32 s55, v155, 50\n\t"
        "v_cvt_pk_bf16_f32 v116, v60, v61\n\t"
        "s_waitcnt lgkmcnt(3)\n\t"
        "v_mfma_f32_16x16x32_bf16 v[72:75], a[84:87], v[96:99], v[72:75]\n\t"
        "ds_read_b128 v[92:95], v183 offset:0\n\t"
        "v_cvt_pk_bf16_f32 v117, v62, v63\n\t"
        "v_mfma_f32_16x16x32_bf16 v[76:79], a[88:91], v[96:99], v[76:79]\n\t"
        "v_cvt_pk_bf16_f32 v118, v64, v65\n\t"
        "v_cvt_pk_bf16_f32 v119, v66, v67\n\t"
        "v_mfma_f32_16x16x32_bf16 v[80:83], a[92:95], v[96:99], v[80:83]\n\t"
        "v_cvt_pk_bf16_f32 v120, v68, v69\n\t"
        "v_cvt_pk_bf16_f32 v121, v70, v71\n\t"
        "v_mfma_f32_16x16x32_bf16 v[72:75], a[120:123], v[100:103], v[72:75]\n\t"
        "v_permlane16_swap_b32 v116, v118\n\t"
        "v_mfma_f32_16x16x32_bf16 v[76:79], a[124:127], v[100:103], v[76:79]\n\t"
        "v_permlane16_swap_b32 v117, v119\n\t"
        "v_mfma_f32_16x16x32_bf16 v[80:83], a[128:131], v[100:103], v[80:83]\n\t"
        "buffer_store_dwordx4 v[116:119], v148, s[28:31], s55 offen sc1\n\t"
        "v_mfma_f32_16x16x32_bf16 v[72:75], a[156:159], v[104:107], v[72:75]\n\t"
        "ds_write_b64 v146, v[120:121] offset:0\n\t"
        "v_mfma_f32_16x16x32_bf16 v[76:79], a[160:163], v[104:107], v[76:79]\n\t"
        "ds_read_b128 v[96:99], v183 offset:64\n\t"
        "v_mfma_f32_16x16x32_bf16 v[80:83], a[164:167], v[104:107], v[80:83]\n\t"
        "ds_read_b128 v[100:103], v183 offset:128\n\t"
        "v_add_u32 v183, s9, v177\n\t"
        "v_mfma_f32_16x16x32_bf16 v[72:75], a[192:195], v[108:111], v[72:75]\n\t"
        "ds_read_b128 v[104:107], v151 offset:0\n\t"
        "v_max_u32 v183, v183, v137\n\t"
        "v_mfma_f32_16x16x32_bf16 v[76:79], a[196:199], v[108:111], v[76:79]\n\t"
        "v_xor_b32 v138, v181, v138\n\t"
        "v_mfma_f32_16x16x32_bf16 v[80:83], a[200:203], v[108:111], v[80:83]\n\t"
        "s_waitcnt lgkmcnt(4)\n\t"
        "v_mfma_f32_16x16x32_bf16 v[72:75], a[228:231], v[112:115], v[72:75]\n\t"
        "ds_read_b128 v[108:111], v151 offset:64\n\t"
        "v_mfma_f32_16x16x32_bf16 v[76:79], a[232:235], v[112:115], v[76:79]\n\t"
        "v_mfma_f32_16x16x32_bf16 v[80:83], a[236:239], v[112:115], v[80:83]\n\t"
        "v_mfma_f32_16x16x32_bf16 v[72:75], v[196:199], v[84:87], v[72:75]\n\t"
        "ds_read_b128 v[112:115], v151 offset:128\n\t"
        "v_add_u32 v151, s9, v178\n\t"
        "v_mfma_f32_16x16x32_bf16 v[76:79], v[200:203], v[84:87], v[76:79]\n\t"
        "v_max_u32 v151, v151, v138\n\t"
        "v_xor_b32 v139, v181, v139\n\t"
        "v_mfma_f32_16x16x32_bf16 v[80:83], v[204:207], v[84:87], v[80:83]\n\t"
        "v_mfma_f32_16x16x32_bf16 v[72:75], v[232:235], v[88:91], v[72:75]\n\t"
        "ds_read_b128 v[84:87], v183 offset:0\n\t"
        "v_mfma_f32_16x16x32_bf16 v[76:79], v[236:239], v[88:91], v[76:79]\n\t"
        "v_mfma_f32_16x16x32_bf16 v[80:83], v[240:243], v[88:91], v[80:83]\n\t"
        "v_mfma_f32_16x16x32_bf16 v[72:75], a[0:3], v[92:95], v[72:75]\n\t"
        "ds_read_b128 v[88:91], v183 offset:64\n\t"
        "v_mfma_f32_16x16x32_bf16 v[76:79], a[4:7], v[92:95], v[76:79]\n\t"
        "v_mfma_f32_16x16x32_bf16 v[80:83], a[8:11], v[92:95], v[80:83]\n\t"
        "s_waitcnt lgkmcnt(3)\n\t"
        "v_mfma_f32_16x16x32_bf16 v[72:75], a[36:39], v[96:99], v[72:75]\n\t"
        "ds_read_b128 v[92:95], v183 offset:128\n\t"
        "v_add_u32 v183, s9, v179\n\t"
        "v_mfma_f32_16x16x32_bf16 v[76:79], a[40:43], v[96:99], v[76:79]\n\t"
        "v_max_u32 v183, v183, v139\n\t"
        "v_xor_b32 v140, v181, v140\n\t"
        "v_mfma_f32_16x16x32_bf16 v[80:83], a[44:47], v[96:99], v[80:83]\n\t"
        "v_mfma_f32_16x16x32_bf16 v[72:75], a[72:75], v[100:103], v[72:75]\n\t"
        "ds_read_b128 v[96:99], v151 offset:0\n\t"
        "v_mfma_f32_16x16x32_bf16 v[76:79], a[76:79], v[100:103], v[76:79]\n\t"
        "v_mfma_f32_16x16x32_bf16 v[80:83], a[80:83], v[100:103], v[80:83]\n\t"
        "v_mfma_f32_16x16x32_bf16 v[72:75], a[108:111], v[104:107], v[72:75]\n\t"
        "ds_read_b128 v[100:103], v151 offset:64\n\t"
        "v_mfma_f32_16x16x32_bf16 v[76:79], a[112:115], v[104:107], v[76:79]\n\t"
        "v_mfma_f32_16x16x32_bf16 v[80:83], a[116:119], v[104:107], v[80:83]\n\t"
        "ds_read_b128 v[104:107], v151 offset:128\n\t"
        "v_add_u32 v151, s9, v180\n\t"
        "v_mfma_f32_16x16x32_bf16 v[72:75], a[144:147], v[108:111], v[72:75]\n\t"
        "v_max_u32 v151, v151, v140\n\t"
        "v_mfma_f32_16x16x32_bf16 v[76:79], a[148:151], v[108:111], v[76:79]\n\t"
        "v_mfma_f32_16x16x32_bf16 v[80:83], a[152:155], v[108:111], v[80:83]\n\t"
        "ds_read_b128 v[108:111], v183 offset:0\n\t"
        "s_waitcnt lgkmcnt(4)\n\t"
        "v_mfma_f32_16x16x32_bf16 v[72:75], a[180:183], v[112:115], v[72:75]\n\t"
        "v_mfma_f32_16x16x32_bf16 v[76:79], a[184:187], v[112:115], v[76:79]\n\t"
        "v_mfma_f32_16x16x32_bf16 v[80:83], a[188:191], v[112:115], v[80:83]\n\t"
        "ds_read_b128 v[112:115], v183 offset:64\n\t"
        "v_mfma_f32_16x16x32_bf16 v[72:75], a[216:219], v[84:87], v[72:75]\n\t"
        "v_mfma_f32_16x16x32_bf16 v[76:79], a[220:223], v[84:87], v[76:79]\n\t"
        "v_mfma_f32_16x16x32_bf16 v[80:83], a[224:227], v[84:87], v[80:83]\n\t"
        "ds_read_b128 v[84:87], v183 offset:128\n\t"
        "v_mfma_f32_16x16x32_bf16 v[72:75], a[252:255], v[88:91], v[72:75]\n\t"
        "v_mfma_f32_16x16x32_bf16 v[76:79], v[188:191], v[88:91], v[76:79]\n\t"
        "v_mfma_f32_16x16x32_bf16 v[80:83], v[192:195], v[88:91], v[80:83]\n\t"
        "ds_read_b128 v[88:91], v151 offset:0\n\t"
        "v_mfma_f32_16x16x32_bf16 v[72:75], v[220:223], v[92:95], v[72:75]\n\t"
        "v_mfma_f32_16x16x32_bf16 v[76:79], v[224:227], v[92:95], v[76:79]\n\t"
        "v_mfma_f32_16x16x32_bf16 v[80:83], v[228:231], v[92:95], v[80:83]\n\t"
        "ds_read_b128 v[92:95], v151 offset:64\n\t"
        "s_waitcnt lgkmcnt(4)\n\t"
        "v_mfma_f32_16x16x32_bf16 v[72:75], a[24:27], v[96:99], v[72:75]\n\t"
        "v_mfma_f32_16x16x32_bf16 v[76:79], a[28:31], v[96:99], v[76:79]\n\t"
        "v_mfma_f32_16x16x32_bf16 v[80:83], a[32:35], v[96:99], v[80:83]\n\t"
        "ds_read_b128 v[96:99], v151 offset:128\n\t"
        "v_mfma_f32_16x16x32_bf16 v[80:83], a[68:71], v[100:103], v[80:83]\n\t"
        "v_mfma_f32_16x16x32_bf16 v[80:83], a[104:107], v[104:107], v[80:83]\n\t"
        "v_mfma_f32_16x16x32_bf16 v[80:83], a[140:143], v[108:111], v[80:83]\n\t"
        "s_waitcnt lgkmcnt(3)\n\t"
        "v_mfma_f32_16x16x32_bf16 v[80:83], a[176:179], v[112:115], v[80:83]\n\t"
        "v_mfma_f32_16x16x32_bf16 v[80:83], a[212:215], v[84:87], v[80:83]\n\t"
        "s_waitcnt lgkmcnt(2)\n\t"
        "v_mfma_f32_16x16x32_bf16 v[80:83], a[248:251], v[88:91], v[80:83]\n\t"
        "s_waitcnt lgkmcnt(1)\n\t"
        "v_mfma_f32_16x16x32_bf16 v[80:83], v[216:219], v[92:95], v[80:83]\n\t"
        "s_waitcnt lgkmcnt(0)\n\t"
        "v_mfma_f32_16x16x32_bf16 v[80:83], v[252:255], v[96:99], v[80:83]\n\t"
        "v_mfma_f32_16x16x32_bf16 v[72:75], a[60:63], v[100:103], v[72:75]\n\t"
        "v_mfma_f32_16x16x32_bf16 v[76:79], a[64:67], v[100:103], v[76:79]\n\t"
        "s_nop 5\n\t"
        "v_cvt_pk_bf16_f32 v120, v80, v81\n\t"
        "v_cvt_pk_bf16_f32 v121, v82, v83\n\t"
        "v_mfma_f32_16x16x32_bf16 v[72:75], a[96:99], v[104:107], v[72:75]\n\t"
        "ds_write_b64 v146, v[120:121] offset:2048\n\t"
        "v_mfma_f32_16x16x32_bf16 v[76:79], a[100:103], v[104:107], v[76:79]\n\t"
        "v_mfma_f32_16x16x32_bf16 v[72:75], a[132:135], v[108:111], v[72:75]\n\t"
        "v_mfma_f32_16x16x32_bf16 v[76:79], a[136:139], v[108:111], v[76:79]\n\t"
        "s_waitcnt lgkmcnt(0)\n\t"
        "s_barrier\n\t"
        "v_mfma_f32_16x16x32_bf16 v[72:75], a[168:171], v[112:115], v[72:75]\n\t"
        "ds_read_b128 v[124:127], v147 offset:0\n\t"
        "v_readlane_b32 s55, v155, 51\n\t"
        "v_mfma_f32_16x16x32_bf16 v[76:79], a[172:175], v[112:115], v[76:79]\n\t"
        "v_mfma_f32_16x16x32_bf16 v[72:75], a[204:207], v[84:87], v[72:75]\n\t"
        "v_mfma_f32_16x16x32_bf16 v[76:79], a[208:211], v[84:87], v[76:79]\n\t"
        "v_mfma_f32_16x16x32_bf16 v[72:75], a[240:243], v[88:91], v[72:75]\n\t"
        "v_mfma_f32_16x16x32_bf16 v[76:79], a[244:247], v[88:91], v[76:79]\n\t"
        "v_mfma_f32_16x16x32_bf16 v[72:75], v[208:211], v[92:95], v[72:75]\n\t"
        "v_mfma_f32_16x16x32_bf16 v[76:79], v[212:215], v[92:95], v[76:79]\n\t"
        "v_mfma_f32_16x16x32_bf16 v[72:75], v[244:247], v[96:99], v[72:75]\n\t"
        "v_mfma_f32_16x16x32_bf16 v[76:79], v[248:251], v[96:99], v[76:79]\n\t"
        "s_nop 6\n\t"
        "v_cvt_pk_bf16_f32 v116, v72, v73\n\t"
        "v_cvt_pk_bf16_f32 v117, v74, v75\n\t"
        "v_cvt_pk_bf16_f32 v118, v76, v77\n\t"
        "v_cvt_pk_bf16_f32 v119, v78, v79\n\t"
        "s_nop 0\n\t"
        "v_permlane16_swap_b32 v116, v118\n\t"
        "v_permlane16_swap_b32 v117, v119\n\t"
        "buffer_store_dwordx4 v[116:119], v148, s[28:31], s55 offen  \n\t"
        "s_waitcnt lgkmcnt(0)\n\t"
        "buffer_store_dwordx4 v[124:127], v150, s[28:31], s52 offen  \n\t"
        "s_andn2_b32 s6, s6, 2\n\t"
        "s_cbranch_scc1 99f\n\t"
        "s_endpgm\n\t"
        "21:\n\t"
        "ds_bpermute_b32 v64, v65, v155 offset:172\n\t"
        "s_add_u32 m0, s37, -24576\n\t"
        "s_add_u32 s36, s36, 6144\n\t"
        "buffer_load_dwordx4 v140, s[24:27], s36 offen offset:0 lds\n\t"
        "ds_bpermute_b32 v78, v81, v155 offset:148\n\t"
        "ds_bpermute_b32 v79, v81, v155 offset:160\n\t"
        "v_readlane_b32 s71, v155, 26\n\t"
        "v_readlane_b32 s10, v155, 29\n\t"
        "s_nop 0\n\t"
        "v_mul_u32_u24 v73, s71, v66\n\t"
        "v_lshrrev_b32 v73, 11, v73\n\t"
        "v_lshlrev_b32 v66, 4, v66\n\t"
        "v_mad_u32_u24 v73, v73, s10, v66\n\t"
        "s_waitcnt lgkmcnt(2)\n\t"
        "v_add_u32 v64, v64, v73\n\t"
        "buffer_load_dwordx4 v141, s[24:27], s36 offen offset:1024 lds\n\t"
        "v_readlane_b32 s69, v155, 34\n\t"
        "v_readlane_b32 s70, v155, 35\n\t"
        "s_nop 0\n\t"
        "v_mad_u32_u24 v70, v52, s69, v70\n\t"
        "v_mad_u32_u24 v70, v158, s70, v70\n\t"
        "s_mul_i32 s71, s69, s68\n\t"
        "s_add_u32 s71, s71, 157728\n\t"
        "v_mad_u32_u24 v67, v82, s70, v67\n\t"
        "v_add_u32 v67, s71, v67\n\t"
        "v_readlane_b32 s11, v155, 30\n\t"
        "buffer_load_dwordx4 v142, s[24:27], s36 offen offset:2048 lds\n\t"
        "s_movk_i32 s10, 223\n\t"
        "v_mov_b32 v75, 0x7ff00000\n\t"
        "v_readlane_b32 s12, v155, 24\n\t"
        "v_readlane_b32 s13, v155, 25\n\t"
        "s_nop 0\n\t"
        "s_bfe_u32 s70, s12, 524288\n\t"
        "s_bfe_u32 s71, s13, 524288\n\t"
        "v_cmp_gt_u32 vcc, s71, v71\n\t"
        "v_add3_u32 v73, v71, s70, -2\n\t"
        "v_med3_i32 v73, v73, 0, s10\n\t"
        "s_add_u32 m0, s35, 4096\n\t"
        "s_add_u32 s43, s34, 43008\n\t"
        "buffer_load_dwordx4 v143, s[16:19], s43 offen  lds\n\t"
        "v_cndmask_b32 v74, v75, v72, vcc\n\t"
        "v_mad_u32_u24 v134, v73, s11, v74\n\t"
        "s_bfe_u32 s70, s12, 524296\n\t"
        "s_bfe_u32 s71, s13, 524296\n\t"
        "v_cmp_gt_u32 vcc, s71, v71\n\t"
        "v_add3_u32 v73, v71, s70, -2\n\t"
        "v_med3_i32 v73, v73, 0, s10\n\t"
        "v_cndmask_b32 v74, v75, v72, vcc\n\t"
        "v_mad_u32_u24 v135, v73, s11, v74\n\t"
        "s_add_u32 m0, s35, 20480\n\t"
        "s_add_u32 s43, s34, 215040\n\t"
        "buffer_load_dwordx4 v143, s[16:19], s43 offen  lds\n\t"
        "s_bfe_u32 s70, s12, 524304\n\t"
        "s_bfe_u32 s71, s13, 524304\n\t"
        "v_cmp_gt_u32 vcc, s71, v71\n\t"
        "v_add3_u32 v73, v71, s70, -2\n\t"
        "v_med3_i32 v73, v73, 0, s10\n\t"
        "v_cndmask_b32 v74, v75, v72, vcc\n\t"
        "v_mad_u32_u24 v136, v73, s11, v74\n\t"
        "s_bfe_u32 s70, s12, 524312\n\t"
        "s_bfe_u32 s71, s13, 524312\n\t"
        "v_cmp_gt_u32 vcc, s71, v71\n\t"
        "v_add3_u32 v73, v71, s70, -2\n\t"
        "v_med3_i32 v73, v73, 0, s10\n\t"
        "v_cndmask_b32 v74, v75, v72, vcc\n\t"
        "v_mad_u32_u24 v137, v73, s11, v74\n\t"
        "s_mov_b64 exec, 0\n\t"
        "s_cmp_eq_u32 s4, 0\n\t"
        "s_cbranch_scc1 .LpX0%=\n\t"
        "s_cmp_eq_u32 s4, 1\n\t"
        "s_cbranch_scc1 .LpX1%=\n\t"
        "s_cmp_eq_u32 s4, 2\n\t"
        "s_cbranch_scc1 .LpX2%=\n\t"
        ".LpX2%=:\n\t"
        ".LrX%=:\n\t"
        "s_mov_b64 exec, -1\n\t"
        "s_waitcnt vmcnt(9)\n\t"
        "s_barrier\n\t"
        "ds_read_b64_tr_b16 a[0:1], v138 offset:6144\n\t"
        "ds_read_b64_tr_b16 a[2:3], v138 offset:6912\n\t"
        "ds_read_b64_tr_b16 a[4:5], v139 offset:6144\n\t"
        "ds_read_b64_tr_b16 a[6:7], v139 offset:6912\n\t"
        "ds_read_b64_tr_b16 a[8:9], v149 offset:6144\n\t"
        "ds_read_b64_tr_b16 a[10:11], v149 offset:6912\n\t"
        "ds_read_b128 v[84:87], v128 offset:8192\n\t"
        "ds_read_b64_tr_b16 a[12:13], v138 offset:12288\n\t"
        "ds_read_b64_tr_b16 a[14:15], v138 offset:13056\n\t"
        "ds_read_b64_tr_b16 a[16:17], v139 offset:12288\n\t"
        "ds_read_b64_tr_b16 a[18:19], v139 offset:13056\n\t"
        "ds_read_b64_tr_b16 a[20:21], v149 offset:12288\n\t"
        "ds_read_b64_tr_b16 a[22:23], v149 offset:13056\n\t"
        "ds_read_b128 v[88:91], v128 offset:8192\n\t"
        "s_add_u32 m0, s37, -12288\n\t"
        "s_add_u32 s36, s36, s39\n\t"
        "s_waitcnt lgkmcnt(7)\n\t"
        "v_mfma_f32_16x16x32_bf16 v[12:15], a[0:3], v[84:87], 0\n\t"
        "buffer_load_dwordx4 v140, s[24:27], s36 offen offset:0 lds\n\t"
        "buffer_load_dwordx4 v141, s[24:27], s36 offen offset:1024 lds\n\t"
        "buffer_load_dwordx4 v142, s[24:27], s36 offen offset:2048 lds\n\t"
        "s_add_u32 m0, s35, 16384\n\t"
        "s_add_u32 s43, s34, 172032\n\t"
        "buffer_load_dwordx4 v143, s[16:19], s43 offen  lds\n\t"
        "s_add_u32 m0, s35, 32768\n\t"
        "s_add_u32 s43, s34, 344064\n\t"
        "s_min_u32 s43, s43, 9590784\n\t"
        "buffer_load_dwordx4 v143, s[16:19], s43 offen  lds\n\t"
        "s_waitcnt vmcnt(10)\n\t"
        "s_barrier\n\t"
        "ds_read_b128 v[92:95], v128 offset:12288\n\t"
        "s_add_u32 m0, s37, 0\n\t"
        "ds_read_b64_tr_b16 a[24:25], v138 offset:0\n\t"
        "s_add_u32 s36, s36, s38\n\t"
        "v_mfma_f32_16x16x32_bf16 v[16:19], a[4:7], v[84:87], 0\n\t"
        "buffer_load_dwordx4 v140, s[24:27], s36 offen offset:0 lds\n\t"
        "ds_read_b64_tr_b16 a[26:27], v138 offset:768\n\t"
        "v_readlane_b32 s44, v155, 32\n\t"
        "v_mfma_f32_16x16x32_bf16 v[20:23], a[8:11], v[84:87], 0\n\t"
        "buffer_load_dwordx4 v141, s[24:27], s36 offen offset:1024 lds\n\t"
        "ds_read_b64_tr_b16 a[28:29], v139 offset:0\n\t"
        "v_readlane_b32 s45, v155, 33\n\t"
        "s_waitcnt lgkmcnt(4)\n\t"
        "v_mfma_f32_16x16x32_bf16 v[24:27], a[12:15], v[88:91], 0\n\t"
        "ds_read_b64_tr_b16 a[30:31], v139 offset:768\n\t"
        "ds_read_b64_tr_b16 a[32:33], v149 offset:0\n\t"
        "ds_read_b64_tr_b16 a[34:35], v149 offset:768\n\t"
        "v_readlane_b32 s62, v155, 36\n\t"
        "v_mfma_f32_16x16x32_bf16 v[28:31], a[16:19], v[88:91], 0\n\t"
        "buffer_load_dwordx4 v142, s[24:27], s36 offen offset:2048 lds\n\t"
        "s_add_u32 m0, s35, 157696\n\t"
        "ds_read_b128 v[96:99], v128 offset:8192\n\t"
        "v_mfma_f32_16x16x32_bf16 v[32:35], a[20:23], v[88:91], 0\n\t"
        "ds_read_b128 v[100:103], v128 offset:12288\n\t"
        "s_lshl_b32 s70, s62, 5\n\t"
        "s_waitcnt lgkmcnt(8)\n\t"
        "v_mfma_f32_16x16x32_bf16 v[24:27], a[0:3], v[92:95], v[24:27]\n\t"
        "s_sub_u32 s71, s70, 16384\n\t"
        "s_mul_i32 s71, s71, s68\n\t"
        "v_mfma_f32_16x16x32_bf16 v[28:31], a[4:7], v[92:95], v[28:31]\n\t"
        "buffer_load_dwordx4 v64, s[20:23], 0 offen lds\n\t"
        "s_add_u32 m0, s37, 12288\n\t"
        "s_add_u32 s36, s36, s5\n\t"
        "v_mfma_f32_16x16x32_bf16 v[32:35], a[8:11], v[92:95], v[32:35]\n\t"
        "s_mov_b32 exec_lo, 0\n\t"
        "s_mov_b32 exec_hi, 0xfff00000\n\t"
        "ds_write_b32 v119, v155\n\t"
        "s_mov_b64 exec, -1\n\t"
        "v_mfma_f32_16x16x32_bf16 v[36:39], a[12:15], v[92:95], 0\n\t"
        "v_add_u32 v172, s71, v128\n\t"
        "v_add_u32 v173, s71, v129\n\t"
        "v_mfma_f32_16x16x32_bf16 v[40:43], a[16:19], v[92:95], 0\n\t"
        "buffer_load_dwordx4 v140, s[24:27], s36 offen offset:0 lds\n\t"
        "v_add_u32 v174, s71, v130\n\t"
        "v_mfma_f32_16x16x32_bf16 v[44:47], a[20:23], v[92:95], 0\n\t"
        "s_waitcnt vmcnt(10) lgkmcnt(3)\n\t"
        "s_barrier\n\t"
        "ds_read_b128 v[104:107], v128 offset:4096\n\t"
        "ds_read_b64_tr_b16 a[36:37], v138 offset:18432\n\t"
        "s_waitcnt lgkmcnt(4)\n\t"
        "v_mfma_f32_16x16x32_bf16 v[0:3], a[24:27], v[96:99], 0\n\t"
        "ds_read_b64_tr_b16 a[38:39], v138 offset:19200\n\t"
        "ds_read_b64_tr_b16 a[40:41], v139 offset:18432\n\t"
        "s_add_u32 s71, s71, s70\n\t"
        "v_mfma_f32_16x16x32_bf16 v[4:7], a[28:31], v[96:99], 0\n\t"
        "buffer_load_dwordx4 v141, s[24:27], s36 offen offset:1024 lds\n\t"
        "ds_read_b64_tr_b16 a[42:43], v139 offset:19200\n\t"
        "v_add_u32 v175, s71, v128\n\t"
        "v_mfma_f32_16x16x32_bf16 v[8:11], a[32:35], v[96:99], 0\n\t"
        "ds_read_b64_tr_b16 a[44:45], v149 offset:18432\n\t"
        "ds_read_b64_tr_b16 a[46:47], v149 offset:19200\n\t"
        "v_add_u32 v176, s71, v129\n\t"
        "s_waitcnt lgkmcnt(8)\n\t"
        "v_mfma_f32_16x16x32_bf16 v[12:15], a[24:27], v[100:103], v[12:15]\n\t"
        "ds_read_b64_tr_b16 a[48:49], v138 offset:24576\n\t"
        "ds_read_b64_tr_b16 a[50:51], v138 offset:25344\n\t"
        "ds_read_b64_tr_b16 a[52:53], v139 offset:24576\n\t"
        "v_add_u32 v177, s71, v130\n\t"
        "v_mfma_f32_16x16x32_bf16 v[16:19], a[28:31], v[100:103], v[16:19]\n\t"
        "buffer_load_dwordx4 v142, s[24:27], s36 offen offset:2048 lds\n\t"
        "s_add_u32 m0, s35, 0\n\t"
        "ds_read_b64_tr_b16 a[54:55], v139 offset:25344\n\t"
        "s_add_u32 s43, s34, 0\n\t"
        "ds_read_b64_tr_b16 a[56:57], v149 offset:24576\n\t"
        "v_mfma_f32_16x16x32_bf16 v[20:23], a[32:35], v[100:103], v[20:23]\n\t"
        "ds_read_b64_tr_b16 a[58:59], v149 offset:25344\n\t"
        "ds_read_b128 v[108:111], v128 offset:4160\n\t"
        "s_add_u32 s71, s71, s70\n\t"
        "s_waitcnt lgkmcnt(12)\n\t"
        "v_mfma_f32_16x16x32_bf16 v[0:3], a[0:3], v[104:107], v[0:3]\n\t"
        "buffer_load_dwordx4 v143, s[16:19], s43 offen  lds\n\t"
        "s_add_u32 m0, s35, 36864\n\t"
        "s_add_u32 s43, s34, 387072\n\t"
        "s_min_u32 s43, s43, 9590784\n\t"
        "v_mfma_f32_16x16x32_bf16 v[4:7], a[4:7], v[104:107], v[4:7]\n\t"
        "v_add_u32 v178, s71, v128\n\t"
        "v_add_u32 v179, s71, v129\n\t"
        "v_mfma_f32_16x16x32_bf16 v[8:11], a[8:11], v[104:107], v[8:11]\n\t"
        "s_cmp_eq_u32 s69, 36\n\t"
        "s_cselect_b64 exec, -1, 0\n\t"
        "v_swap_b32 v68, v69\n\t"
        "v_swap_b32 v173, v175\n\t"
        "v_swap_b32 v174, v178\n\t"
        "v_swap_b32 v177, v179\n\t"
        "s_mov_b64 exec, -1\n\t"
        "v_mfma_f32_16x16x32_bf16 v[12:15], a[12:15], v[104:107], v[12:15]\n\t"
        "v_add_u32 v180, s71, v130\n\t"
        "v_add3_u32 v68, v68, v52, -1\n\t"
        "v_mfma_f32_16x16x32_bf16 v[16:19], a[16:19], v[104:107], v[16:19]\n\t"
        "v_add3_u32 v69, v69, v158, -1\n\t"
        "v_cvt_f32_i32 v68, v68\n\t"
        "v_mfma_f32_16x16x32_bf16 v[20:23], a[20:23], v[104:107], v[20:23]\n\t"
        "v_cvt_f32_i32 v69, v69\n\t"
        "v_add_f32 v78, v78, v56\n\t"
        "v_add_f32 v79, v79, v57\n\t"
        "s_waitcnt lgkmcnt(0)\n\t"
        "v_mfma_f32_16x16x32_bf16 v[0:3], a[36:39], v[108:111], v[0:3]\n\t"
        "ds_read_b128 v[112:115], v128 offset:8256\n\t"
        "v_readlane_b32 s69, v155, 31\n\t"
        "v_mfma_f32_16x16x32_bf16 v[4:7], a[40:43], v[108:111], v[4:7]\n\t"
        "s_nop 0\n\t"
        "v_mov_b32 v154, s69\n\t"
        "v_mov_b32 v119, s62\n\t"
        "v_mfma_f32_16x16x32_bf16 v[8:11], a[44:47], v[108:111], v[8:11]\n\t"
        "v_readlane_b32 s53, v155, 28\n\t"
        "v_readlane_b32 s9, v155, 46\n\t"
        "v_mfma_f32_16x16x32_bf16 v[12:15], a[48:51], v[108:111], v[12:15]\n\t"
        "s_sub_u32 s10, 43200, s11\n\t"
        "s_mul_i32 s10, s10, s68\n\t"
        "v_mfma_f32_16x16x32_bf16 v[16:19], a[52:55], v[108:111], v[16:19]\n\t"
        "s_sub_u32 s11, s11, 192\n\t"
        "v_mad_u32_u24 v148, v158, s11, v144\n\t"
        "v_mfma_f32_16x16x32_bf16 v[20:23], a[56:59], v[108:111], v[20:23]\n\t"
        "v_add_u32 v148, s10, v148\n\t"
        "v_mad_u32_u24 v150, v83, s11, v145\n\t"
        "s_waitcnt lgkmcnt(0)\n\t"
        "v_mfma_f32_16x16x32_bf16 v[12:15], a[36:39], v[112:115], v[12:15]\n\t"
        "ds_read_b128 v[84:87], v128 offset:12352\n\t"
        "s_lshl_b32 s70, s49, 1\n\t"
        "v_mfma_f32_16x16x32_bf16 v[16:19], a[40:43], v[112:115], v[16:19]\n\t"
        "s_add_u32 s70, s70, 49\n\t"
        "v_readlane_b32 s52, v155, s70\n\t"
        "v_mfma_f32_16x16x32_bf16 v[20:23], a[44:47], v[112:115], v[20:23]\n\t"
        "s_nop 0\n\t"
        "s_add_u32 s52, s52, s10\n\t"
        "v_mfma_f32_16x16x32_bf16 v[24:27], a[48:51], v[112:115], v[24:27]\n\t"
        "buffer_load_dwordx4 v143, s[16:19], s43 offen  lds\n\t"
        "s_add_u32 m0, s37, 24576\n\t"
        "s_add_u32 s36, s36, s39\n\t"
        "v_mfma_f32_16x16x32_bf16 v[28:31], a[52:55], v[112:115], v[28:31]\n\t"
        "v_mfma_f32_16x16x32_bf16 v[32:35], a[56:59], v[112:115], v[32:35]\n\t"
        "buffer_load_dwordx4 v140, s[24:27], s36 offen offset:0 lds\n\t"
        "s_waitcnt vmcnt(10)\n\t"
        "s_barrier\n\t"
        "ds_read_b128 v[88:91], v128 offset:16384\n\t"
        "ds_read_b64_tr_b16 a[60:61], v138 offset:30720\n\t"
        "s_waitcnt lgkmcnt(2)\n\t"
        "v_mfma_f32_16x16x32_bf16 v[24:27], a[36:39], v[84:87], v[24:27]\n\t"
        "buffer_load_dwordx4 v141, s[24:27], s36 offen offset:1024 lds\n\t"
        "ds_read_b64_tr_b16 a[62:63], v138 offset:31488\n\t"
        "v_mfma_f32_16x16x32_bf16 v[28:31], a[40:43], v[84:87], v[28:31]\n\t"
        "ds_read_b64_tr_b16 a[64:65], v139 offset:30720\n\t"
        "ds_read_b64_tr_b16 a[66:67], v139 offset:31488\n\t"
        "v_mfma_f32_16x16x32_bf16 v[32:35], a[44:47], v[84:87], v[32:35]\n\t"
        "ds_read_b64_tr_b16 a[68:69], v149 offset:30720\n\t"
        "ds_read_b64_tr_b16 a[70:71], v149 offset:31488\n\t"
        "v_mfma_f32_16x16x32_bf16 v[36:39], a[48:51], v[84:87], v[36:39]\n\t"
        "buffer_load_dwordx4 v142, s[24:27], s36 offen offset:2048 lds\n\t"
        "ds_read_b128 v[92:95], v128 offset:8256\n\t"
        "v_mfma_f32_16x16x32_bf16 v[40:43], a[52:55], v[84:87], v[40:43]\n\t"
        "ds_read_b128 v[96:99], v128 offset:12352\n\t"
        "ds_read_b128 v[100:103], v128 offset:16448\n\t"
        "v_mfma_f32_16x16x32_bf16 v[44:47], a[56:59], v[84:87], v[44:47]\n\t"
        "ds_read_b64_tr_b16 a[72:73], v138 offset:36864\n\t"
        "ds_read_b64_tr_b16 a[74:75], v138 offset:37632\n\t"
        "s_waitcnt lgkmcnt(11)\n\t"
        "v_mfma_f32_16x16x32_bf16 v[24:27], a[24:27], v[88:91], v[24:27]\n\t"
        "ds_read_b64_tr_b16 a[76:77], v139 offset:36864\n\t"
        "ds_read_b64_tr_b16 a[78:79], v139 offset:37632\n\t"
        "v_mfma_f32_16x16x32_bf16 v[28:31], a[28:31], v[88:91], v[28:31]\n\t"
        "buffer_load_dwordx4 v[48:51], v55, s[20:23], s48 offen\n\t"
        "ds_read_b64_tr_b16 a[80:81], v149 offset:36864\n\t"
        "v_mfma_f32_16x16x32_bf16 v[32:35], a[32:35], v[88:91], v[32:35]\n\t"
        "ds_read_b64_tr_b16 a[82:83], v149 offset:37632\n\t"
        "v_mfma_f32_16x16x32_bf16 v[36:39], a[0:3], v[88:91], v[36:39]\n\t"
        "v_mfma_f32_16x16x32_bf16 v[40:43], a[4:7], v[88:91], v[40:43]\n\t"
        "v_mfma_f32_16x16x32_bf16 v[44:47], a[8:11], v[88:91], v[44:47]\n\t"
        "buffer_load_dword v61, v61, s[20:23], s48 offen offset:512\n\t"
        "s_add_u32 m0, s37, 36864\n\t"
        "s_add_u32 s36, s36, s38\n\t"
        "s_waitcnt lgkmcnt(8)\n\t"
        "v_mfma_f32_16x16x32_bf16 v[0:3], a[60:63], v[92:95], v[0:3]\n\t"
        "v_mfma_f32_16x16x32_bf16 v[4:7], a[64:67], v[92:95], v[4:7]\n\t"
        "s_waitcnt vmcnt(10)\n\t"
        "s_barrier\n\t"
        "ds_read_b64_tr_b16 a[84:85], v131 offset:0\n\t"
        "ds_read_b64_tr_b16 a[86:87], v131 offset:768\n\t"
        "v_mfma_f32_16x16x32_bf16 v[8:11], a[68:71], v[92:95], v[8:11]\n\t"
        "ds_read_b64_tr_b16 a[88:89], v132 offset:0\n\t"
        "ds_read_b64_tr_b16 a[90:91], v132 offset:768\n\t"
        "v_readlane_b32 s46, v155, 37\n\t"
        "s_waitcnt lgkmcnt(6)\n\t"
        "v_mfma_f32_16x16x32_bf16 v[12:15], a[60:63], v[96:99], v[12:15]\n\t"
        "buffer_load_dwordx4 v140, s[24:27], s36 offen offset:0 lds\n\t"
        "ds_read_b64_tr_b16 a[92:93], v133 offset:0\n\t"
        "ds_read_b64_tr_b16 a[94:95], v133 offset:768\n\t"
        "v_readlane_b32 s47, v155, 40\n\t"
        "v_mfma_f32_16x16x32_bf16 v[16:19], a[64:67], v[96:99], v[16:19]\n\t"
        "ds_read_b128 v[104:107], v128 offset:4224\n\t"
        "ds_read_b64_tr_b16 a[96:97], v131 offset:6144\n\t"
        "v_add_f32 v58, s46, v68\n\t"
        "v_mfma_f32_16x16x32_bf16 v[20:23], a[68:71], v[96:99], v[20:23]\n\t"
        "ds_read_b64_tr_b16 a[98:99], v131 offset:6912\n\t"
        "ds_read_b64_tr_b16 a[100:101], v132 offset:6144\n\t"
        "v_add_f32 v59, s47, v69\n\t"
        "v_mfma_f32_16x16x32_bf16 v[24:27], a[60:63], v[100:103], v[24:27]\n\t"
        "ds_read_b64_tr_b16 a[102:103], v132 offset:6912\n\t"
        "ds_read_b64_tr_b16 a[104:105], v133 offset:6144\n\t"
        "v_readlane_b32 s47, v155, 42\n\t"
        "v_mfma_f32_16x16x32_bf16 v[28:31], a[64:67], v[100:103], v[28:31]\n\t"
        "buffer_load_dwordx4 v141, s[24:27], s36 offen offset:1024 lds\n\t"
        "ds_read_b64_tr_b16 a[106:107], v133 offset:6912\n\t"
        "v_mfma_f32_16x16x32_bf16 v[32:35], a[68:71], v[100:103], v[32:35]\n\t"
        "ds_read_b128 v[108:111], v128 offset:8320\n\t"
        "v_mfma_f32_16x16x32_bf16 v[36:39], a[36:39], v[100:103], v[36:39]\n\t"
        "ds_read_b32 v64, v70 offset:0\n\t"
        "v_mfma_f32_16x16x32_bf16 v[40:43], a[40:43], v[100:103], v[40:43]\n\t"
        "ds_read_b32 v67, v67 offset:0\n\t"
        "v_mfma_f32_16x16x32_bf16 v[44:47], a[44:47], v[100:103], v[44:47]\n\t"
        "buffer_load_dwordx4 v142, s[24:27], s36 offen offset:2048 lds\n\t"
        "v_readlane_b32 s40, v155, 12\n\t"
        "v_readlane_b32 s43, v155, 0\n\t"
        "s_waitcnt lgkmcnt(9)\n\t"
        "v_mfma_f32_16x16x32_bf16 v[0:3], a[72:75], v[104:107], v[0:3]\n\t"
        "ds_read_b128 v[112:115], v128 offset:12416\n\t"
        "s_add_u32 m0, s40, s35\n\t"
        "v_mfma_f32_16x16x32_bf16 v[4:7], a[76:79], v[104:107], v[4:7]\n\t"
        "ds_read_b32 v66, v70 offset:2560\n\t"
        "v_mfma_f32_16x16x32_bf16 v[8:11], a[80:83], v[104:107], v[8:11]\n\t"
        "v_mfma_f32_16x16x32_bf16 v[12:15], a[84:87], v[104:107], v[12:15]\n\t"
        "ds_read_b128 v[84:87], v128 offset:16512\n\t"
        "v_mfma_f32_16x16x32_bf16 v[16:19], a[88:91], v[104:107], v[16:19]\n\t"
        "v_mfma_f32_16x16x32_bf16 v[20:23], a[92:95], v[104:107], v[20:23]\n\t"
        "s_waitcnt lgkmcnt(5)\n\t"
        "v_mfma_f32_16x16x32_bf16 v[0:3], a[96:99], v[108:111], v[0:3]\n\t"
        "s_waitcnt lgkmcnt(4)\n\t"
        "v_perm_b32 v56, v64, v64, s44\n\t"
        "v_perm_b32 v57, v64, v64, s45\n\t"
        "v_mfma_f32_16x16x32_bf16 v[4:7], a[100:103], v[108:111], v[4:7]\n\t"
        "v_add_f32 v56, v56, v58\n\t"
        "v_add_f32 v57, v57, v59\n\t"
        "v_mfma_f32_16x16x32_bf16 v[8:11], a[104:107], v[108:111], v[8:11]\n\t"
        "v_cvt_pk_bf16_f32 v52, v56, v57\n\t"
        "v_cvt_pk_bf16_f32 v58, v58, v59\n\t"
        "v_cmp_ne_u32 s[64:65], v52, v58\n\t"
        "v_mfma_f32_16x16x32_bf16 v[12:15], a[72:75], v[108:111], v[12:15]\n\t"
        "s_nop 0\n\t"
        "v_mbcnt_lo_u32_b32 v56, s64, 1\n\t"
        "v_mbcnt_hi_u32_b32 v56, s65, v56\n\t"
        "v_mfma_f32_16x16x32_bf16 v[16:19], a[76:79], v[108:111], v[16:19]\n\t"
        "s_bcnt1_i32_b64 s61, s[64:65]\n\t"
        "v_add_u32 v54, s61, 1\n\t"
        "v_mfma_f32_16x16x32_bf16 v[20:23], a[80:83], v[108:111], v[20:23]\n\t"
        "v_lshl_add_u32 v56, v56, 3, s60\n\t"
        "s_waitcnt lgkmcnt(1)\n\t"
        "v_perm_b32 v57, v67, v67, s45\n\t"
        "v_mfma_f32_16x16x32_bf16 v[24:27], a[84:87], v[108:111], v[24:27]\n\t"
        "s_mov_b64 exec, s[64:65]\n\t"
        "ds_write_b64 v56, v[52:53]\n\t"
        "s_mov_b64 exec, -1\n\t"
        "v_mfma_f32_16x16x32_bf16 v[28:31], a[88:91], v[108:111], v[28:31]\n\t"
        "v_swap_b32 v53, v80\n\t"
        "v_perm_b32 v56, v67, v67, s44\n\t"
        "v_mfma_f32_16x16x32_bf16 v[32:35], a[92:95], v[108:111], v[32:35]\n\t"
        "v_add_f32 v56, v56, v78\n\t"
        "v_add_f32 v57, v57, v79\n\t"
        "v_mfma_f32_16x16x32_bf16 v[12:15], a[96:99], v[112:115], v[12:15]\n\t"
        "v_cvt_pk_bf16_f32 v52, v56, v57\n\t"
        "v_cvt_pk_bf16_f32 v58, v78, v79\n\t"
        "v_mfma_f32_16x16x32_bf16 v[16:19], a[100:103], v[112:115], v[16:19]\n\t"
        "v_cmp_ne_u32 s[64:65], v52, v58\n\t"
        "v_perm_b32 v57, v66, v66, s45\n\t"
        "v_mfma_f32_16x16x32_bf16 v[20:23], a[104:107], v[112:115], v[20:23]\n\t"
        "s_and_b64 s[64:65], s[64:65], 0xffffff\n\t"
        "v_mbcnt_lo_u32_b32 v56, s64, v54\n\t"
        "v_mfma_f32_16x16x32_bf16 v[24:27], a[72:75], v[112:115], v[24:27]\n\t"
        "s_bcnt1_i32_b64 s46, s[64:65]\n\t"
        "s_add_u32 s61, s61, s46\n\t"
        "v_mfma_f32_16x16x32_bf16 v[28:31], a[76:79], v[112:115], v[28:31]\n\t"
        "v_add_u32 v54, s46, v54\n\t"
        "s_mov_b32 s3, s61\n\t"
        "v_mfma_f32_16x16x32_bf16 v[32:35], a[80:83], v[112:115], v[32:35]\n\t"
        "v_lshl_add_u32 v56, v56, 3, s60\n\t"
        "v_readlane_b32 s46, v155, 39\n\t"
        "v_mfma_f32_16x16x32_bf16 v[36:39], a[84:87], v[112:115], v[36:39]\n\t"
        "s_mov_b64 exec, s[64:65]\n\t"
        "ds_write_b64 v56, v[52:53]\n\t"
        "s_mov_b64 exec, -1\n\t"
        "v_mfma_f32_16x16x32_bf16 v[40:43], a[88:91], v[112:115], v[40:43]\n\t"
        "s_waitcnt vmcnt(8)\n\t"
        "s_barrier\n\t"
        "buffer_load_dwordx4 v134, s[16:19], s43 offen  lds\n\t"
        "ds_read_b128 v[88:91], v128 offset:0\n\t"
        "v_mfma_f32_16x16x32_bf16 v[44:47], a[92:95], v[112:115], v[44:47]\n\t"
        "ds_read_b128 v[92:95], v128 offset:64\n\t"
        "ds_read_b128 v[96:99], v128 offset:128\n\t"
        "v_readlane_b32 s40, v155, 13\n\t"
        "s_waitcnt lgkmcnt(5)\n\t"
        "v_mfma_f32_16x16x32_bf16 v[24:27], a[96:99], v[84:87], v[24:27]\n\t"
        "ds_read_b128 v[100:103], v128 offset:20480\n\t"
        "ds_read_b128 v[104:107], v128 offset:20544\n\t"
        "v_readlane_b32 s43, v155, 1\n\t"
        "v_mfma_f32_16x16x32_bf16 v[28:31], a[100:103], v[84:87], v[28:31]\n\t"
        "ds_read_b128 v[108:111], v128 offset:20608\n\t"
        "s_add_u32 m0, s40, s35\n\t"
        "ds_read_b64_tr_b16 a[108:109], v131 offset:12288\n\t"
        "v_mfma_f32_16x16x32_bf16 v[32:35], a[104:107], v[84:87], v[32:35]\n\t"
        "buffer_load_dwordx4 v135, s[16:19], s43 offen  lds\n\t"
        "s_add_u32 m0, s37, 49152\n\t"
        "ds_read_b64_tr_b16 a[110:111], v131 offset:13056\n\t"
        "v_mfma_f32_16x16x32_bf16 v[36:39], a[72:75], v[84:87], v[36:39]\n\t"
        "ds_read_b64_tr_b16 a[112:113], v132 offset:12288\n\t"
        "s_add_u32 s36, s36, s5\n\t"
        "ds_read_b64_tr_b16 a[114:115], v132 offset:13056\n\t"
        "v_mfma_f32_16x16x32_bf16 v[40:43], a[76:79], v[84:87], v[40:43]\n\t"
        "ds_read_b64_tr_b16 a[116:117], v133 offset:12288\n\t"
        "ds_read_b64_tr_b16 a[118:119], v133 offset:13056\n\t"
        "v_add_u32 v53, 4096, v80\n\t"
        "v_perm_b32 v56, v66, v66, s44\n\t"
        "v_mfma_f32_16x16x32_bf16 v[44:47], a[80:83], v[84:87], v[44:47]\n\t"
        "ds_read_b64_tr_b16 a[120:121], v131 offset:18432\n\t"
        "ds_read_b64_tr_b16 a[122:123], v131 offset:19200\n\t"
        "v_add_f32 v58, s46, v68\n\t"
        "s_waitcnt lgkmcnt(13)\n\t"
        "v_mfma_f32_16x16x32_bf16 v[0:3], a[12:15], v[88:91], v[0:3]\n\t"
        "buffer_load_dwordx4 v140, s[24:27], s36 offen offset:0 lds\n\t"
        "ds_read_b64_tr_b16 a[124:125], v132 offset:18432\n\t"
        "v_add_f32 v59, s47, v69\n\t"
        "v_mfma_f32_16x16x32_bf16 v[4:7], a[16:19], v[88:91], v[4:7]\n\t"
        "ds_read_b64_tr_b16 a[126:127], v132 offset:19200\n\t"
        "ds_read_b64_tr_b16 a[128:129], v133 offset:18432\n\t"
        "v_add_f32 v56, v56, v58\n\t"
        "v_mfma_f32_16x16x32_bf16 v[8:11], a[20:23], v[88:91], v[8:11]\n\t"
        "ds_read_b64_tr_b16 a[130:131], v133 offset:19200\n\t"
        "v_add_f32 v57, v57, v59\n\t"
        "s_waitcnt lgkmcnt(11)\n\t"
        "v_mfma_f32_16x16x32_bf16 v[0:3], a[48:51], v[92:95], v[0:3]\n\t"
        "ds_read_b128 v[112:115], v129 offset:0\n\t"
        "v_max_f32 v56, 0, v56\n\t"
        "v_mfma_f32_16x16x32_bf16 v[4:7], a[52:55], v[92:95], v[4:7]\n\t"
        "buffer_load_dwordx4 v141, s[24:27], s36 offen offset:1024 lds\n\t"
        "v_max_f32 v57, 0, v57\n\t"
        "v_mfma_f32_16x16x32_bf16 v[8:11], a[56:59], v[92:95], v[8:11]\n\t"
        "ds_read_b128 v[84:87], v129 offset:4096\n\t"
        "v_max_f32 v58, 0, v58\n\t"
        "v_mfma_f32_16x16x32_bf16 v[0:3], a[84:87], v[96:99], v[0:3]\n\t"
        "v_max_f32 v59, 0, v59\n\t"
        "v_cvt_pk_bf16_f32 v52, v56, v57\n\t"
        "v_mfma_f32_16x16x32_bf16 v[4:7], a[88:91], v[96:99], v[4:7]\n\t"
        "v_cvt_pk_bf16_f32 v58, v58, v59\n\t"
        "v_cmp_ne_u32 s[64:65], v52, v58\n\t"
        "v_mfma_f32_16x16x32_bf16 v[8:11], a[92:95], v[96:99], v[8:11]\n\t"
        "buffer_load_dwordx4 v142, s[24:27], s36 offen offset:2048 lds\n\t"
        "v_readlane_b32 s40, v155, 14\n\t"
        "v_mfma_f32_16x16x32_bf16 v[36:39], a[24:27], v[100:103], v[36:39]\n\t"
        "v_readlane_b32 s43, v155, 2\n\t"
        "s_add_u32 m0, s40, s35\n\t"
        "v_mfma_f32_16x16x32_bf16 v[40:43], a[28:31], v[100:103], v[40:43]\n\t"
        "v_mbcnt_lo_u32_b32 v56, s64, v54\n\t"
        "v_mbcnt_hi_u32_b32 v56, s65, v56\n\t"
        "v_mfma_f32_16x16x32_bf16 v[44:47], a[32:35], v[100:103], v[44:47]\n\t"
        "s_waitcnt vmcnt(10) lgkmcnt(2)\n\t"
        "s_barrier\n\t"
        "ds_read_b64_tr_b16 a[132:133], v131 offset:24576\n\t"
        "ds_read_b64_tr_b16 a[134:135], v131 offset:25344\n\t"
        "v_mfma_f32_16x16x32_bf16 v[36:39], a[60:63], v[104:107], v[36:39]\n\t"
        "buffer_load_dwordx4 v134, s[16:19], s43 offen  lds\n\t"
        "ds_read_b64_tr_b16 a[136:137], v132 offset:24576\n\t"
        "s_bitcmp1_b32 s42, 10\n\t"
        "s_cbranch_scc1 .Lx62%=\n\t"
        ".Lx63%=:\n\t"
        "v_mfma_f32_16x16x32_bf16 v[40:43], a[64:67], v[104:107], v[40:43]\n\t"
        "ds_read_b64_tr_b16 a[138:139], v132 offset:25344\n\t"
        "ds_read_b64_tr_b16 a[140:141], v133 offset:24576\n\t"
        "v_readlane_b32 s40, v155, 15\n\t"
        "v_mfma_f32_16x16x32_bf16 v[44:47], a[68:71], v[104:107], v[44:47]\n\t"
        "ds_read_b64_tr_b16 a[142:143], v133 offset:25344\n\t"
        "ds_read_b128 v[88:91], v129 offset:8192\n\t"
        "v_readlane_b32 s43, v155, 3\n\t"
        "v_mfma_f32_16x16x32_bf16 v[36:39], a[96:99], v[108:111], v[36:39]\n\t"
        "ds_read_b128 v[92:95], v129 offset:12288\n\t"
        "s_add_u32 m0, s40, s35\n\t"
        "ds_read_b64_tr_b16 a[144:145], v131 offset:30720\n\t"
        "v_mfma_f32_16x16x32_bf16 v[40:43], a[100:103], v[108:111], v[40:43]\n\t"
        "buffer_load_dwordx4 v135, s[16:19], s43 offen  lds\n\t"
        "s_add_u32 m0, s37, 0\n\t"
        "ds_read_b64_tr_b16 a[146:147], v131 offset:31488\n\t"
        "v_mfma_f32_16x16x32_bf16 v[44:47], a[104:107], v[108:111], v[44:47]\n\t"
        "ds_read_b64_tr_b16 a[148:149], v132 offset:30720\n\t"
        "s_add_u32 s36, s36, s39\n\t"
        "ds_read_b64_tr_b16 a[150:151], v132 offset:31488\n\t"
        "s_waitcnt lgkmcnt(12)\n\t"
        "v_mfma_f32_16x16x32_bf16 v[0:3], a[120:123], v[112:115], v[0:3]\n\t"
        "ds_read_b64_tr_b16 a[152:153], v133 offset:30720\n\t"
        "ds_read_b64_tr_b16 a[154:155], v133 offset:31488\n\t"
        "s_bcnt1_i32_b64 s46, s[64:65]\n\t"
        "v_mfma_f32_16x16x32_bf16 v[4:7], a[124:127], v[112:115], v[4:7]\n\t"
        "s_add_u32 s61, s61, s46\n\t"
        "v_min_u32 v56, 97, v56\n\t"
        "v_mfma_f32_16x16x32_bf16 v[8:11], a[128:131], v[112:115], v[8:11]\n\t"
        "buffer_load_dwordx4 v140, s[24:27], s36 offen offset:0 lds\n\t"
        "v_lshl_add_u32 v56, v56, 3, s60\n\t"
        "v_mfma_f32_16x16x32_bf16 v[0:3], a[108:111], v[84:87], v[0:3]\n\t"
        "s_mov_b64 exec, s[64:65]\n\t"
        "ds_write_b64 v56, v[52:53]\n\t"
        "s_mov_b64 exec, -1\n\t"
        "v_mfma_f32_16x16x32_bf16 v[4:7], a[112:115], v[84:87], v[4:7]\n\t"
        "s_cmp_gt_u32 s61, s53\n\t"
        "s_cselect_b32 s8, 1, 0\n\t"
        "v_mfma_f32_16x16x32_bf16 v[8:11], a[116:119], v[84:87], v[8:11]\n\t"
        "s_waitcnt vmcnt(12)\n\t"
        "v_and_b32 v48, s54, v48\n\t"
        "v_pk_max_u16 v60, v154, v48\n\t"
        "v_mfma_f32_16x16x32_bf16 v[12:15], a[120:123], v[84:87], v[12:15]\n\t"
        "buffer_load_dwordx4 v141, s[24:27], s36 offen offset:1024 lds\n\t"
        "v_and_b32 v49, s54, v49\n\t"
        "v_mfma_f32_16x16x32_bf16 v[16:19], a[124:127], v[84:87], v[16:19]\n\t"
        "ds_read_b128 v[96:99], v129 offset:16384\n\t"
        "v_pk_max_u16 v60, v60, v49\n\t"
        "v_mfma_f32_16x16x32_bf16 v[20:23], a[128:131], v[84:87], v[20:23]\n\t"
        "v_and_b32 v50, s54, v50\n\t"
        "v_pk_max_u16 v60, v60, v50\n\t"
        "s_waitcnt lgkmcnt(3)\n\t"
        "v_mfma_f32_16x16x32_bf16 v[0:3], a[132:135], v[88:91], v[0:3]\n\t"
        "v_and_b32 v51, s54, v51\n\t"
        "v_pk_max_u16 v60, v60, v51\n\t"
        "v_mfma_f32_16x16x32_bf16 v[4:7], a[136:139], v[88:91], v[4:7]\n\t"
        "buffer_load_dwordx4 v142, s[24:27], s36 offen offset:2048 lds\n\t"
        "v_readlane_b32 s40, v155, 16\n\t"
        "v_mfma_f32_16x16x32_bf16 v[8:11], a[140:143], v[88:91], v[8:11]\n\t"
        "v_readlane_b32 s43, v155, 4\n\t"
        "s_add_u32 m0, s40, s35\n\t"
        "v_mfma_f32_16x16x32_bf16 v[12:15], a[108:111], v[88:91], v[12:15]\n\t"
        "ds_read_b128 v[100:103], v129 offset:20480\n\t"
        "s_waitcnt vmcnt(13)\n\t"
        "v_and_b32 v61, s54, v61\n\t"
        "v_mfma_f32_16x16x32_bf16 v[16:19], a[112:115], v[88:91], v[16:19]\n\t"
        "v_pk_max_u16 v60, v60, v61\n\t"
        "v_cmp_ne_u32 s[12:13], v60, v154\n\t"
        "v_mfma_f32_16x16x32_bf16 v[20:23], a[116:119], v[88:91], v[20:23]\n\t"
        "buffer_load_dwordx4 v134, s[16:19], s43 offen  lds\n\t"
        "s_bitcmp1_b32 s42, 11\n\t"
        "s_cbranch_scc1 .Lx64%=\n\t"
        ".Lx65%=:\n\t"
        "v_mfma_f32_16x16x32_bf16 v[24:27], a[120:123], v[88:91], v[24:27]\n\t"
        "v_readlane_b32 s40, v155, 17\n\t"
        "v_readlane_b32 s43, v155, 5\n\t"
        "v_mfma_f32_16x16x32_bf16 v[28:31], a[124:127], v[88:91], v[28:31]\n\t"
        "s_add_u32 m0, s40, s35\n\t"
        "s_lshl_b32 s6, 256, s4\n\t"
        "v_mfma_f32_16x16x32_bf16 v[32:35], a[128:131], v[88:91], v[32:35]\n\t"
        "s_cmp_lg_u64 s[12:13], 0\n\t"
        "s_cselect_b32 s6, s6, 0\n\t"
        "v_mfma_f32_16x16x32_bf16 v[12:15], a[132:135], v[92:95], v[12:15]\n\t"
        "buffer_load_dwordx4 v135, s[16:19], s43 offen  lds\n\t"
        "s_add_u32 m0, s37, 12288\n\t"
        "v_mfma_f32_16x16x32_bf16 v[16:19], a[136:139], v[92:95], v[16:19]\n\t"
        "s_add_u32 s36, s36, s38\n\t"
        "s_or_b32 s56, s56, s8\n\t"
        "v_mfma_f32_16x16x32_bf16 v[20:23], a[140:143], v[92:95], v[20:23]\n\t"
        "s_cmp_gt_u32 s3, 64\n\t"
        "s_cselect_b32 s8, 1, 0\n\t"
        "v_mfma_f32_16x16x32_bf16 v[24:27], a[108:111], v[92:95], v[24:27]\n\t"
        "s_or_b32 s56, s56, s8\n\t"
        "s_min_u32 s61, s61, 80\n\t"
        "v_mfma_f32_16x16x32_bf16 v[28:31], a[112:115], v[92:95], v[28:31]\n\t"
        "s_waitcnt vmcnt(10) lgkmcnt(0)\n\t"
        "s_barrier\n\t"
        "buffer_load_dwordx4 v140, s[24:27], s36 offen offset:0 lds\n\t"
        "ds_read_b64_tr_b16 a[156:157], v131 offset:36864\n\t"
        "v_mfma_f32_16x16x32_bf16 v[32:35], a[116:119], v[92:95], v[32:35]\n\t"
        "ds_read_b64_tr_b16 a[158:159], v131 offset:37632\n\t"
        "ds_read_b64_tr_b16 a[160:161], v132 offset:36864\n\t"
        "v_min_u32 v78, s61, v116\n\t"
        "v_mfma_f32_16x16x32_bf16 v[36:39], a[120:123], v[92:95], v[36:39]\n\t"
        "ds_read_b64_tr_b16 a[162:163], v132 offset:37632\n\t"
        "ds_read_b64_tr_b16 a[164:165], v133 offset:36864\n\t"
        "v_lshl_add_u32 v78, v78, 3, s60\n\t"
        "v_mfma_f32_16x16x32_bf16 v[40:43], a[124:127], v[92:95], v[40:43]\n\t"
        "ds_read_b64_tr_b16 a[166:167], v133 offset:37632\n\t"
        "ds_read_b128 v[104:107], v129 offset:64\n\t"
        "v_cmp_ge_u32 s[66:67], s61, v116\n\t"
        "v_mfma_f32_16x16x32_bf16 v[44:47], a[128:131], v[92:95], v[44:47]\n\t"
        "buffer_load_dwordx4 v141, s[24:27], s36 offen offset:1024 lds\n\t"
        "ds_read_b128 v[108:111], v129 offset:4160\n\t"
        "s_mov_b32 s46, 0x07060302\n\t"
        "v_mfma_f32_16x16x32_bf16 v[24:27], a[132:135], v[96:99], v[24:27]\n\t"
        "ds_read_b64 v[62:63], v78\n\t"
        "ds_read_b64_tr_b16 a[168:169], v131 offset:43008\n\t"
        "v_mfma_f32_16x16x32_bf16 v[28:31], a[136:139], v[96:99], v[28:31]\n\t"
        "ds_read_b64_tr_b16 a[170:171], v131 offset:43776\n\t"
        "ds_read_b64_tr_b16 a[172:173], v132 offset:43008\n\t"
        "v_mfma_f32_16x16x32_bf16 v[32:35], a[140:143], v[96:99], v[32:35]\n\t"
        "ds_read_b64_tr_b16 a[174:175], v132 offset:43776\n\t"
        "ds_read_b64_tr_b16 a[176:177], v133 offset:43008\n\t"
        "v_mfma_f32_16x16x32_bf16 v[36:39], a[108:111], v[96:99], v[36:39]\n\t"
        "buffer_load_dwordx4 v142, s[24:27], s36 offen offset:2048 lds\n\t"
        "ds_read_b64_tr_b16 a[178:179], v133 offset:43776\n\t"
        "v_readlane_b32 s40, v155, 18\n\t"
        "v_mfma_f32_16x16x32_bf16 v[40:43], a[112:115], v[96:99], v[40:43]\n\t"
        "ds_read_b128 v[112:115], v129 offset:8256\n\t"
        "v_readlane_b32 s43, v155, 6\n\t"
        "v_mfma_f32_16x16x32_bf16 v[44:47], a[116:119], v[96:99], v[44:47]\n\t"
        "s_add_u32 m0, s40, s35\n\t"
        "v_mfma_f32_16x16x32_bf16 v[36:39], a[132:135], v[100:103], v[36:39]\n\t"
        "ds_read_b128 v[84:87], v129 offset:12352\n\t"
        "v_mfma_f32_16x16x32_bf16 v[40:43], a[136:139], v[100:103], v[40:43]\n\t"
        "buffer_load_dwordx4 v136, s[16:19], s43 offen  lds\n\t"
        "s_bitcmp1_b32 s42, 11\n\t"
        "s_cbranch_scc1 .Lx66%=\n\t"
        ".Lx67%=:\n\t"
        "v_mfma_f32_16x16x32_bf16 v[44:47], a[140:143], v[100:103], v[44:47]\n\t"
        "v_readlane_b32 s40, v155, 19\n\t"
        "v_readlane_b32 s43, v155, 7\n\t"
        "s_waitcnt lgkmcnt(8)\n\t"
        "v_mfma_f32_16x16x32_bf16 v[0:3], a[156:159], v[104:107], v[0:3]\n\t"
        "s_mov_b64 exec, s[66:67]\n\t"
        "ds_write_b32 v63, v117\n\t"
        "s_mov_b64 exec, -1\n\t"
        "v_mfma_f32_16x16x32_bf16 v[4:7], a[160:163], v[104:107], v[4:7]\n\t"
        "s_add_u32 m0, s40, s35\n\t"
        "v_and_b32 v78, 0xfffff800, v63\n\t"
        "v_mfma_f32_16x16x32_bf16 v[8:11], a[164:167], v[104:107], v[8:11]\n\t"
        "ds_read_b128 v[64:67], v78 offset:1536\n\t"
        "v_lshlrev_b32 v72, 16, v62\n\t"
        "v_mfma_f32_16x16x32_bf16 v[0:3], a[144:147], v[108:111], v[0:3]\n\t"
        "buffer_load_dwordx4 v137, s[16:19], s43 offen  lds\n\t"
        "s_add_u32 m0, s37, 24576\n\t"
        "v_mfma_f32_16x16x32_bf16 v[4:7], a[148:151], v[108:111], v[4:7]\n\t"
        "s_add_u32 s36, s36, s5\n\t"
        "v_and_b32 v73, 0xffff0000, v62\n\t"
        "v_mfma_f32_16x16x32_bf16 v[8:11], a[152:155], v[108:111], v[8:11]\n\t"
        "v_med3_f32 v72, v72, 0, s63\n\t"
        "v_med3_f32 v73, v73, 0, s63\n\t"
        "v_mfma_f32_16x16x32_bf16 v[12:15], a[156:159], v[108:111], v[12:15]\n\t"
        "ds_read_b128 v[88:91], v129 offset:16448\n\t"
        "v_perm_b32 v62, v73, v72, s46\n\t"
        "v_mfma_f32_16x16x32_bf16 v[16:19], a[160:163], v[108:111], v[16:19]\n\t"
        "v_fract_f32 v74, v72\n\t"
        "v_fract_f32 v75, v73\n\t"
        "v_mfma_f32_16x16x32_bf16 v[20:23], a[164:167], v[108:111], v[20:23]\n\t"
        "v_cvt_u32_f32 v76, v72\n\t"
        "v_cvt_u32_f32 v77, v73\n\t"
        "s_waitcnt lgkmcnt(3)\n\t"
        "v_mfma_f32_16x16x32_bf16 v[0:3], a[168:171], v[112:115], v[0:3]\n\t"
        "v_cmp_neq_f32 s[64:65], 0, v74\n\t"
        "v_cmp_neq_f32 s[44:45], 0, v75\n\t"
        "v_mfma_f32_16x16x32_bf16 v[4:7], a[172:175], v[112:115], v[4:7]\n\t"
        "v_sub_f32 v78, 1.0, v74\n\t"
        "v_sub_f32 v79, 1.0, v75\n\t"
        "v_mfma_f32_16x16x32_bf16 v[8:11], a[176:179], v[112:115], v[8:11]\n\t"
        "v_cvt_pk_bf16_f32 v79, v78, v79\n\t"
        "v_lshlrev_b32 v78, 16, v79\n\t"
        "v_mfma_f32_16x16x32_bf16 v[12:15], a[144:147], v[112:115], v[12:15]\n\t"
        "ds_read_b128 v[92:95], v129 offset:20544\n\t"
        "v_and_b32 v79, 0xffff0000, v79\n\t"
        "v_mfma_f32_16x16x32_bf16 v[16:19], a[148:151], v[112:115], v[16:19]\n\t"
        "s_waitcnt vmcnt(10)\n\t"
        "s_barrier\n\t"
        "buffer_load_dwordx4 v140, s[24:27], s36 offen offset:0 lds\n\t"
        "ds_read_b64_tr_b16 a[180:181], v131 offset:49152\n\t"
        ".LpX0%=:\n\t"
        "s_cbranch_execz .LrX%=\n\t"
        "v_mfma_f32_16x16x32_bf16 v[20:23], a[152:155], v[112:115], v[20:23]\n\t"
        "ds_read_b64_tr_b16 a[182:183], v131 offset:49920\n\t"
        "ds_read_b64_tr_b16 a[184:185], v132 offset:49152\n\t"
        "v_mul_f32 v80, v78, v79\n\t"
        "v_mfma_f32_16x16x32_bf16 v[24:27], a[156:159], v[112:115], v[24:27]\n\t"
        "ds_read_b64_tr_b16 a[186:187], v132 offset:49920\n\t"
        "ds_read_b64_tr_b16 a[188:189], v133 offset:49152\n\t"
        "v_mul_f32 v78, v78, v75\n\t"
        "v_mfma_f32_16x16x32_bf16 v[28:31], a[160:163], v[112:115], v[28:31]\n\t"
        "ds_read_b64_tr_b16 a[190:191], v133 offset:49920\n\t"
        "ds_read_b64_tr_b16 a[192:193], v131 offset:55296\n\t"
        "v_mul_f32 v79, v74, v79\n\t"
        "v_mfma_f32_16x16x32_bf16 v[32:35], a[164:167], v[112:115], v[32:35]\n\t"
        "buffer_load_dwordx4 v141, s[24:27], s36 offen offset:1024 lds\n\t"
        "ds_read_b64_tr_b16 a[194:195], v131 offset:56064\n\t"
        "v_mul_f32 v81, v74, v75\n\t"
        "v_mfma_f32_16x16x32_bf16 v[12:15], a[168:171], v[84:87], v[12:15]\n\t"
        "ds_read_b64_tr_b16 a[196:197], v132 offset:55296\n\t"
        "ds_read_b64_tr_b16 a[198:199], v132 offset:56064\n\t"
        "v_cndmask_b32 v78, v79, v78, s[44:45]\n\t"
        "v_mfma_f32_16x16x32_bf16 v[16:19], a[172:175], v[84:87], v[16:19]\n\t"
        "ds_read_b64_tr_b16 a[200:201], v133 offset:55296\n\t"
        "ds_read_b64_tr_b16 a[202:203], v133 offset:56064\n\t"
        "v_cndmask_b32 v79, 0, v79, s[44:45]\n\t"
        "v_mfma_f32_16x16x32_bf16 v[20:23], a[176:179], v[84:87], v[20:23]\n\t"
        "ds_read_b128 v[96:99], v129 offset:128\n\t"
        "v_cvt_pk_bf16_f32 v70, v80, v78\n\t"
        "v_mfma_f32_16x16x32_bf16 v[24:27], a[144:147], v[84:87], v[24:27]\n\t"
        "buffer_load_dwordx4 v142, s[24:27], s36 offen offset:2048 lds\n\t"
        "s_add_u32 m0, s37, 36864\n\t"
        "v_mfma_f32_16x16x32_bf16 v[28:31], a[148:151], v[84:87], v[28:31]\n\t"
        "ds_read_b128 v[100:103], v129 offset:4224\n\t"
        "s_add_u32 s36, s36, s39\n\t"
        "v_mfma_f32_16x16x32_bf16 v[32:35], a[152:155], v[84:87], v[32:35]\n\t"
        "v_cvt_pk_bf16_f32 v71, v79, v81\n\t"
        "s_waitcnt lgkmcnt(9)\n\t"
        "v_pk_max_u16 v78, v62, v64\n\t"
        "v_mfma_f32_16x16x32_bf16 v[36:39], a[156:159], v[84:87], v[36:39]\n\t"
        "v_pk_min_u16 v78, v78, v65\n\t"
        "v_cmp_ne_u32 vcc, v78, v62\n\t"
        "v_mfma_f32_16x16x32_bf16 v[40:43], a[160:163], v[84:87], v[40:43]\n\t"
        "buffer_load_dwordx4 v140, s[24:27], s36 offen offset:0 lds\n\t"
        "v_sub_u32 v78, v77, v67\n\t"
        "v_mfma_f32_16x16x32_bf16 v[44:47], a[164:167], v[84:87], v[44:47]\n\t"
        "v_mad_u32_u24 v79, v77, 6, v66\n\t"
        "v_mad_u32_u24 v79, v76, s62, v79\n\t"
        "v_mfma_f32_16x16x32_bf16 v[24:27], a[168:171], v[88:91], v[24:27]\n\t"
        "s_and_b64 vcc, vcc, s[66:67]\n\t"
        "s_or_b64 s[56:57], s[56:57], vcc\n\t"
        "v_mfma_f32_16x16x32_bf16 v[28:31], a[172:175], v[88:91], v[28:31]\n\t"
        "v_bfe_u32 v80, v78, 2, 1\n\t"
        "v_or_b32 v80, v80, v79\n\t"
        "v_mfma_f32_16x16x32_bf16 v[32:35], a[176:179], v[88:91], v[32:35]\n\t"
        "v_add_u32 v78, 1, v78\n\t"
        "v_bfe_u32 v78, v78, 2, 1\n\t"
        "v_mfma_f32_16x16x32_bf16 v[36:39], a[144:147], v[88:91], v[36:39]\n\t"
        "v_add3_u32 v78, v79, v78, 6\n\t"
        "v_cndmask_b32 v78, v80, v78, s[44:45]\n\t"
        "v_mfma_f32_16x16x32_bf16 v[40:43], a[148:151], v[88:91], v[40:43]\n\t"
        "v_cndmask_b32 v79, 0, v119, s[64:65]\n\t"
        "v_add_u32 v81, v80, v79\n\t"
        "v_mfma_f32_16x16x32_bf16 v[44:47], a[152:155], v[88:91], v[44:47]\n\t"
        "buffer_load_dwordx4 v141, s[24:27], s36 offen offset:1024 lds\n\t"
        "v_add_u32 v82, v78, v79\n\t"
        "v_mfma_f32_16x16x32_bf16 v[36:39], a[168:171], v[92:95], v[36:39]\n\t"
        "v_cndmask_b32 v78, v81, v78, s[44:45]\n\t"
        "v_lshl_or_b32 v68, v78, 16, v80\n\t"
        "v_mfma_f32_16x16x32_bf16 v[40:43], a[172:175], v[92:95], v[40:43]\n\t"
        "v_lshl_or_b32 v69, v82, 16, v81\n\t"
        "v_mfma_f32_16x16x32_bf16 v[44:47], a[176:179], v[92:95], v[44:47]\n\t"
        "ds_write_b128 v118, v[68:71]\n\t"
        "s_waitcnt lgkmcnt(1)\n\t"
        "v_mfma_f32_16x16x32_bf16 v[0:3], a[192:195], v[96:99], v[0:3]\n\t"
        "s_waitcnt vmcnt(10)\n\t"
        "s_barrier\n\t"
        "buffer_load_dwordx4 v142, s[24:27], s36 offen offset:2048 lds\n\t"
        "s_add_u32 m0, s37, 49152\n\t"
        "ds_read_b64_tr_b16 a[204:205], v131 offset:0\n\t"
        "s_add_u32 s36, s36, s38\n\t"
        "ds_read_b64_tr_b16 a[206:207], v131 offset:768\n\t"
        "v_mfma_f32_16x16x32_bf16 v[4:7], a[196:199], v[96:99], v[4:7]\n\t"
        "ds_read_b64_tr_b16 a[208:209], v132 offset:0\n\t"
        "ds_read_b64_tr_b16 a[210:211], v132 offset:768\n\t"
        "ds_read_b64_tr_b16 a[212:213], v133 offset:0\n\t"
        "v_xor_b32 v154, 32, v186\n\t"
        "v_mfma_f32_16x16x32_bf16 v[8:11], a[200:203], v[96:99], v[8:11]\n\t"
        "ds_read_b64_tr_b16 a[214:215], v133 offset:768\n\t"
        "ds_read_b128 v[104:107], v129 offset:8320\n\t"
        "v_mfma_f32_16x16x32_bf16 v[0:3], a[180:183], v[100:103], v[0:3]\n\t"
        "s_cmp_gt_u32 s3, 32\n\t"
        "s_cselect_b32 s8, 2, 0\n\t"
        "ds_read_b128 v[108:111], v129 offset:12416\n\t"
        "ds_read_b64_tr_b16 a[216:217], v131 offset:6144\n\t"
        "v_mfma_f32_16x16x32_bf16 v[4:7], a[184:187], v[100:103], v[4:7]\n\t"
        "s_cmp_lg_u64 s[56:57], 0\n\t"
        "s_addc_u32 s8, s8, s6\n\t"
        "buffer_load_dwordx4 v140, s[24:27], s36 offen offset:0 lds\n\t"
        "ds_read_b64_tr_b16 a[218:219], v131 offset:6912\n\t"
        "v_mfma_f32_16x16x32_bf16 v[8:11], a[188:191], v[100:103], v[8:11]\n\t"
        "ds_read_b128 v[160:163], v185 offset:0\n\t"
        "ds_read_b64_tr_b16 a[220:221], v132 offset:6144\n\t"
        "v_mov_b32 v56, s8\n\t"
        "v_mfma_f32_16x16x32_bf16 v[12:15], a[192:195], v[100:103], v[12:15]\n\t"
        "s_mov_b64 exec, 1\n\t"
        "ds_write_b32 v153, v56\n\t"
        "s_mov_b64 exec, -1\n\t"
        "ds_read_b128 v[112:115], v129 offset:16512\n\t"
        "v_mfma_f32_16x16x32_bf16 v[16:19], a[196:199], v[100:103], v[16:19]\n\t"
        "ds_read_b64_tr_b16 a[222:223], v132 offset:6912\n\t"
        "ds_read_b64_tr_b16 a[224:225], v133 offset:6144\n\t"
        "v_mfma_f32_16x16x32_bf16 v[20:23], a[200:203], v[100:103], v[20:23]\n\t"
        "buffer_load_dwordx4 v141, s[24:27], s36 offen offset:1024 lds\n\t"
        "ds_read_b64_tr_b16 a[226:227], v133 offset:6912\n\t"
        "s_waitcnt lgkmcnt(10)\n\t"
        "v_mfma_f32_16x16x32_bf16 v[0:3], a[204:207], v[104:107], v[0:3]\n\t"
        "v_mfma_f32_16x16x32_bf16 v[4:7], a[208:211], v[104:107], v[4:7]\n\t"
        "v_mfma_f32_16x16x32_bf16 v[8:11], a[212:215], v[104:107], v[8:11]\n\t"
        "v_mfma_f32_16x16x32_bf16 v[12:15], a[180:183], v[104:107], v[12:15]\n\t"
        "buffer_load_dwordx4 v142, s[24:27], s36 offen offset:2048 lds\n\t"
        "v_mfma_f32_16x16x32_bf16 v[16:19], a[184:187], v[104:107], v[16:19]\n\t"
        "ds_read_b128 v[84:87], v129 offset:20608\n\t"
        "v_mfma_f32_16x16x32_bf16 v[20:23], a[188:191], v[104:107], v[20:23]\n\t"
        "s_waitcnt lgkmcnt(6)\n\t"
        "v_lshlrev_b32_sdwa v157, 5, v160 dst_sel:DWORD dst_unused:UNUSED_PAD src0_sel:DWORD src1_sel:WORD_0\n\t"
        "v_lshlrev_b32_sdwa v158, 5, v160 dst_sel:DWORD dst_unused:UNUSED_PAD src0_sel:DWORD src1_sel:WORD_1\n\t"
        "v_mfma_f32_16x16x32_bf16 v[24:27], a[192:195], v[104:107], v[24:27]\n\t"
        "v_or_b32 v157, v184, v157\n\t"
        "v_or_b32 v158, v184, v158\n\t"
        "v_mfma_f32_16x16x32_bf16 v[28:31], a[196:199], v[104:107], v[28:31]\n\t"
        "v_xor_b32 v159, 32, v157\n\t"
        "v_xor_b32 v152, 32, v158\n\t"
        "v_mfma_f32_16x16x32_bf16 v[32:35], a[200:203], v[104:107], v[32:35]\n\t"
        "v_perm_b32 v164, v162, v162, v168\n\t"
        "v_perm_b32 v165, v162, v162, v169\n\t"
        "v_mfma_f32_16x16x32_bf16 v[12:15], a[204:207], v[108:111], v[12:15]\n\t"
        "v_perm_b32 v166, v162, v162, v170\n\t"
        "v_perm_b32 v167, v162, v162, v171\n\t"
        "v_mfma_f32_16x16x32_bf16 v[16:19], a[208:211], v[108:111], v[16:19]\n\t"
        "v_mfma_f32_16x16x32_bf16 v[20:23], a[212:215], v[108:111], v[20:23]\n\t"
        "v_mfma_f32_16x16x32_bf16 v[24:27], a[180:183], v[108:111], v[24:27]\n\t"
        "v_mfma_f32_16x16x32_bf16 v[28:31], a[184:187], v[108:111], v[28:31]\n\t"
        "v_mfma_f32_16x16x32_bf16 v[32:35], a[188:191], v[108:111], v[32:35]\n\t"
        "v_mfma_f32_16x16x32_bf16 v[36:39], a[192:195], v[108:111], v[36:39]\n\t"
        "s_waitcnt vmcnt(9) lgkmcnt(0)\n\t"
        "s_barrier\n\t"
        "ds_read_b64_tr_b16 a[228:229], v131 offset:12288\n\t"
        "ds_read_b64_tr_b16 a[230:231], v131 offset:13056\n\t"
        "v_mfma_f32_16x16x32_bf16 v[40:43], a[196:199], v[108:111], v[40:43]\n\t"
        "ds_read_b64_tr_b16 a[232:233], v132 offset:12288\n\t"
        "ds_read_b64_tr_b16 a[234:235], v132 offset:13056\n\t"
        "v_mfma_f32_16x16x32_bf16 v[44:47], a[200:203], v[108:111], v[44:47]\n\t"
        "ds_read_b64_tr_b16 a[236:237], v133 offset:12288\n\t"
        "ds_read_b64_tr_b16 a[238:239], v133 offset:13056\n\t"
        "v_mfma_f32_16x16x32_bf16 v[24:27], a[204:207], v[112:115], v[24:27]\n\t"
        "ds_read_b128 v[88:91], v130 offset:0\n\t"
        "ds_read_b128 v[92:95], v130 offset:4096\n\t"
        "v_mfma_f32_16x16x32_bf16 v[28:31], a[208:211], v[112:115], v[28:31]\n\t"
        "ds_read_b64_tr_b16 v[60:61], v157 offset:0\n\t"
        "ds_read_b64_tr_b16 a[240:241], v131 offset:18432\n\t"
        "v_mfma_f32_16x16x32_bf16 v[32:35], a[212:215], v[112:115], v[32:35]\n\t"
        "ds_read_b64_tr_b16 a[242:243], v131 offset:19200\n\t"
        "ds_read_b64_tr_b16 a[244:245], v132 offset:18432\n\t"
        "v_mfma_f32_16x16x32_bf16 v[36:39], a[180:183], v[112:115], v[36:39]\n\t"
        "ds_read_b64_tr_b16 a[246:247], v132 offset:19200\n\t"
        "ds_read_b64_tr_b16 a[248:249], v133 offset:18432\n\t"
        "v_mfma_f32_16x16x32_bf16 v[40:43], a[184:187], v[112:115], v[40:43]\n\t"
        "ds_read_b64_tr_b16 a[250:251], v133 offset:19200\n\t"
        "v_mfma_f32_16x16x32_bf16 v[44:47], a[188:191], v[112:115], v[44:47]\n\t"
        "ds_read_b128 v[96:99], v130 offset:8192\n\t"
        "v_mfma_f32_16x16x32_bf16 v[36:39], a[204:207], v[84:87], v[36:39]\n\t"
        "ds_read_b64_tr_b16 v[62:63], v158 offset:0\n\t"
        "v_mfma_f32_16x16x32_bf16 v[40:43], a[208:211], v[84:87], v[40:43]\n\t"
        "ds_read_b64_tr_b16 v[64:65], v159 offset:0\n\t"
        "v_mfma_f32_16x16x32_bf16 v[44:47], a[212:215], v[84:87], v[44:47]\n\t"
        "s_waitcnt vmcnt(6)\n\t"
        "s_barrier\n\t"
        "ds_read_b128 v[100:103], v130 offset:12288\n\t"
        "ds_read_b64_tr_b16 a[252:253], v131 offset:24576\n\t"
        "s_waitcnt lgkmcnt(12)\n\t"
        "v_mfma_f32_16x16x32_bf16 v[0:3], a[228:231], v[88:91], v[0:3]\n\t"
        "ds_read_b64_tr_b16 v[66:67], v152 offset:0\n\t"
        "ds_read_b64_tr_b16 a[254:255], v131 offset:25344\n\t"
        "v_mfma_f32_16x16x32_bf16 v[4:7], a[232:235], v[88:91], v[4:7]\n\t"
        "ds_read_b64_tr_b16 v[68:69], v157 offset:64\n\t"
        "ds_read_b64_tr_b16 v[188:189], v132 offset:24576\n\t"
        "v_mfma_f32_16x16x32_bf16 v[8:11], a[236:239], v[88:91], v[8:11]\n\t"
        "ds_read_b64_tr_b16 v[70:71], v158 offset:64\n\t"
        "ds_read_b64_tr_b16 v[190:191], v132 offset:25344\n\t"
        "v_mfma_f32_16x16x32_bf16 v[0:3], a[216:219], v[92:95], v[0:3]\n\t"
        "ds_read_b64_tr_b16 v[72:73], v159 offset:64\n\t"
        "ds_read_b64_tr_b16 v[192:193], v133 offset:24576\n\t"
        "v_mfma_f32_16x16x32_bf16 v[4:7], a[220:223], v[92:95], v[4:7]\n\t"
        "ds_read_b64_tr_b16 v[74:75], v152 offset:64\n\t"
        "ds_read_b64_tr_b16 v[194:195], v133 offset:25344\n\t"
        "v_mfma_f32_16x16x32_bf16 v[8:11], a[224:227], v[92:95], v[8:11]\n\t"
        "ds_read_b64_tr_b16 v[76:77], v157 offset:128\n\t"
        "v_lshlrev_b32_sdwa v157, 5, v161 dst_sel:DWORD dst_unused:UNUSED_PAD src0_sel:DWORD src1_sel:WORD_0\n\t"
        "v_mfma_f32_16x16x32_bf16 v[12:15], a[228:231], v[92:95], v[12:15]\n\t"
        "ds_read_b64_tr_b16 v[78:79], v158 offset:128\n\t"
        "v_lshlrev_b32_sdwa v158, 5, v161 dst_sel:DWORD dst_unused:UNUSED_PAD src0_sel:DWORD src1_sel:WORD_1\n\t"
        "s_waitcnt lgkmcnt(14)\n\t"
        "v_mfma_f32_16x16x32_bf16 v[48:51], v[60:63], v[164:167], 0\n\t"
        "ds_read_b64_tr_b16 v[80:81], v159 offset:128\n\t"
        "v_or_b32 v157, v184, v157\n\t"
        "v_mfma_f32_16x16x32_bf16 v[16:19], a[232:235], v[92:95], v[16:19]\n\t"
        "ds_read_b64_tr_b16 v[82:83], v152 offset:128\n\t"
        "v_or_b32 v158, v184, v158\n\t"
        "v_mfma_f32_16x16x32_bf16 v[20:23], a[236:239], v[92:95], v[20:23]\n\t"
        "ds_read_b64_tr_b16 v[60:61], v157 offset:0\n\t"
        "ds_read_b128 v[104:107], v130 offset:16384\n\t"
        "v_xor_b32 v159, 32, v157\n\t"
        "s_waitcnt lgkmcnt(13)\n\t"
        "v_mfma_f32_16x16x32_bf16 v[52:55], v[64:67], v[164:167], 0\n\t"
        "ds_read_b64_tr_b16 v[62:63], v158 offset:0\n\t"
        "ds_read_b64_tr_b16 v[196:197], v131 offset:30720\n\t"
        "v_xor_b32 v152, 32, v158\n\t"
        "v_mfma_f32_16x16x32_bf16 v[0:3], a[240:243], v[96:99], v[0:3]\n\t"
        "ds_read_b64_tr_b16 v[64:65], v159 offset:0\n\t"
        "ds_read_b64_tr_b16 v[198:199], v131 offset:31488\n\t"
        "s_waitcnt lgkmcnt(12)\n\t"
        "v_mfma_f32_16x16x32_bf16 v[56:59], v[68:71], v[164:167], 0\n\t"
        "ds_read_b64_tr_b16 v[66:67], v152 offset:0\n\t"
        "ds_read_b64_tr_b16 v[200:201], v132 offset:30720\n\t"
        "v_mfma_f32_16x16x32_bf16 v[4:7], a[244:247], v[96:99], v[4:7]\n\t"
        "ds_read_b64_tr_b16 v[68:69], v157 offset:64\n\t"
        "ds_read_b64_tr_b16 v[202:203], v132 offset:31488\n\t"
        "s_waitcnt lgkmcnt(13)\n\t"
        "v_mfma_f32_16x16x32_bf16 v[116:119], v[72:75], v[164:167], 0\n\t"
        "ds_read_b64_tr_b16 v[70:71], v158 offset:64\n\t"
        "ds_read_b64_tr_b16 v[204:205], v133 offset:30720\n\t"
        "v_mfma_f32_16x16x32_bf16 v[8:11], a[248:251], v[96:99], v[8:11]\n\t"
        "ds_read_b64_tr_b16 v[72:73], v159 offset:64\n\t"
        "ds_read_b64_tr_b16 v[206:207], v133 offset:31488\n\t"
        "s_waitcnt lgkmcnt(14)\n\t"
        "v_mfma_f32_16x16x32_bf16 v[120:123], v[76:79], v[164:167], 0\n\t"
        "ds_read_b64_tr_b16 v[74:75], v152 offset:64\n\t"
        "v_mfma_f32_16x16x32_bf16 v[12:15], a[216:219], v[96:99], v[12:15]\n\t"
        "ds_read_b64_tr_b16 v[76:77], v157 offset:128\n\t"
        "v_mfma_f32_16x16x32_bf16 v[124:127], v[80:83], v[164:167], 0\n\t"
        "ds_read_b64_tr_b16 v[78:79], v158 offset:128\n\t"
        "v_perm_b32 v164, v163, v163, v168\n\t"
        "v_mfma_f32_16x16x32_bf16 v[16:19], a[220:223], v[96:99], v[16:19]\n\t"
        "ds_read_b64_tr_b16 v[80:81], v159 offset:128\n\t"
        "v_perm_b32 v165, v163, v163, v169\n\t"
        "v_mfma_f32_16x16x32_bf16 v[20:23], a[224:227], v[96:99], v[20:23]\n\t"
        "ds_read_b64_tr_b16 v[82:83], v152 offset:128\n\t"
        "v_perm_b32 v166, v163, v163, v170\n\t"
        "v_mfma_f32_16x16x32_bf16 v[24:27], a[228:231], v[96:99], v[24:27]\n\t"
        "ds_read_b128 v[108:111], v130 offset:64\n\t"
        "v_perm_b32 v167, v163, v163, v171\n\t"
        "v_mfma_f32_16x16x32_bf16 v[28:31], a[232:235], v[96:99], v[28:31]\n\t"
        "ds_read_b128 v[160:163], v185 offset:256\n\t"
        "s_waitcnt lgkmcnt(12)\n\t"
        "v_mfma_f32_16x16x32_bf16 v[48:51], v[60:63], v[164:167], v[48:51]\n\t"
        "ds_read_b128 v[112:115], v130 offset:4160\n\t"
        "v_mfma_f32_16x16x32_bf16 v[32:35], a[236:239], v[96:99], v[32:35]\n\t"
        "v_mfma_f32_16x16x32_bf16 v[52:55], v[64:67], v[164:167], v[52:55]\n\t"
        "v_mfma_f32_16x16x32_bf16 v[12:15], a[240:243], v[100:103], v[12:15]\n\t"
        "s_waitcnt lgkmcnt(7)\n\t"
        "v_mfma_f32_16x16x32_bf16 v[56:59], v[68:71], v[164:167], v[56:59]\n\t"
        "s_nop 1\n\t"
        "v_cvt_pk_bf16_f32 v48, v48, v49\n\t"
        "v_cvt_pk_bf16_f32 v49, v50, v51\n\t"
        "v_mfma_f32_16x16x32_bf16 v[16:19], a[244:247], v[100:103], v[16:19]\n\t"
        "ds_write_b64 v186, v[48:49] offset:0\n\t"
        "v_mfma_f32_16x16x32_bf16 v[116:119], v[72:75], v[164:167], v[116:119]\n\t"
        "v_cvt_pk_bf16_f32 v52, v52, v53\n\t"
        "v_cvt_pk_bf16_f32 v53, v54, v55\n\t"
        "v_mfma_f32_16x16x32_bf16 v[20:23], a[248:251], v[100:103], v[20:23]\n\t"
        "s_waitcnt vmcnt(0)\n\t"
        "s_barrier\n\t"
        "ds_read_b64_tr_b16 v[208:209], v131 offset:36864\n\t"
        "ds_read_b64_tr_b16 v[210:211], v131 offset:37632\n\t"
        "s_waitcnt lgkmcnt(4)\n\t"
        "v_mfma_f32_16x16x32_bf16 v[120:123], v[76:79], v[164:167], v[120:123]\n\t"
        "ds_write_b64 v154, v[52:53] offset:0\n\t"
        "ds_read_b64_tr_b16 v[212:213], v132 offset:36864\n\t"
        "v_mfma_f32_16x16x32_bf16 v[24:27], a[216:219], v[100:103], v[24:27]\n\t"
        "ds_read_b128 v[84:87], v130 offset:20480\n\t"
        "ds_read_b64_tr_b16 v[214:215], v132 offset:37632\n\t"
        "v_cvt_pk_bf16_f32 v56, v56, v57\n\t"
        "v_mfma_f32_16x16x32_bf16 v[124:127], v[80:83], v[164:167], v[124:127]\n\t"
        "ds_read_b64_tr_b16 v[216:217], v133 offset:36864\n\t"
        "ds_read_b64_tr_b16 v[218:219], v133 offset:37632\n\t"
        "v_cvt_pk_bf16_f32 v57, v58, v59\n\t"
        "v_mfma_f32_16x16x32_bf16 v[28:31], a[220:223], v[100:103], v[28:31]\n\t"
        "ds_write_b64 v186, v[56:57] offset:64\n\t"
        "v_mfma_f32_16x16x32_bf16 v[32:35], a[224:227], v[100:103], v[32:35]\n\t"
        "ds_read_b64_tr_b16 v[220:221], v131 offset:43008\n\t"
        "v_cvt_pk_bf16_f32 v116, v116, v117\n\t"
        "v_mfma_f32_16x16x32_bf16 v[36:39], a[228:231], v[100:103], v[36:39]\n\t"
        "ds_read_b128 v[88:91], v130 offset:8256\n\t"
        "v_cvt_pk_bf16_f32 v117, v118, v119\n\t"
        "v_mfma_f32_16x16x32_bf16 v[40:43], a[232:235], v[100:103], v[40:43]\n\t"
        "ds_write_b64 v154, v[116:117] offset:64\n\t"
        "v_mfma_f32_16x16x32_bf16 v[44:47], a[236:239], v[100:103], v[44:47]\n\t"
        "ds_read_b64_tr_b16 v[222:223], v131 offset:43776\n\t"
        "v_cvt_pk_bf16_f32 v120, v120, v121\n\t"
        "v_mfma_f32_16x16x32_bf16 v[24:27], a[240:243], v[104:107], v[24:27]\n\t"
        "ds_read_b64_tr_b16 v[224:225], v132 offset:43008\n\t"
        "v_cvt_pk_bf16_f32 v121, v122, v123\n\t"
        "v_mfma_f32_16x16x32_bf16 v[28:31], a[244:247], v[104:107], v[28:31]\n\t"
        "ds_write_b64 v186, v[120:121] offset:128\n\t"
        "v_mfma_f32_16x16x32_bf16 v[32:35], a[248:251], v[104:107], v[32:35]\n\t"
        "ds_read_b64_tr_b16 v[226:227], v132 offset:43776\n\t"
        "v_cvt_pk_bf16_f32 v124, v124, v125\n\t"
        "v_mfma_f32_16x16x32_bf16 v[36:39], a[216:219], v[104:107], v[36:39]\n\t"
        "ds_read_b64_tr_b16 v[228:229], v133 offset:43008\n\t"
        "v_cvt_pk_bf16_f32 v125, v126, v127\n\t"
        "v_mfma_f32_16x16x32_bf16 v[40:43], a[220:223], v[104:107], v[40:43]\n\t"
        "ds_write_b64 v154, v[124:125] offset:128\n\t"
        "v_mfma_f32_16x16x32_bf16 v[44:47], a[224:227], v[104:107], v[44:47]\n\t"
        "ds_read_b64_tr_b16 v[230:231], v133 offset:43776\n\t"
        "v_lshlrev_b32_sdwa v157, 5, v160 dst_sel:DWORD dst_unused:UNUSED_PAD src0_sel:DWORD src1_sel:WORD_0\n\t"
        "v_mfma_f32_16x16x32_bf16 v[0:3], v[196:199], v[108:111], v[0:3]\n\t"
        "ds_read_b128 v[92:95], v130 offset:12352\n\t"
        "v_lshlrev_b32_sdwa v158, 5, v160 dst_sel:DWORD dst_unused:UNUSED_PAD src0_sel:DWORD src1_sel:WORD_1\n\t"
        "v_mfma_f32_16x16x32_bf16 v[4:7], v[200:203], v[108:111], v[4:7]\n\t"
        "ds_read_b64_tr_b16 v[232:233], v131 offset:49152\n\t"
        "v_or_b32 v157, v184, v157\n\t"
        "v_mfma_f32_16x16x32_bf16 v[8:11], v[204:207], v[108:111], v[8:11]\n\t"
        "ds_read_b64_tr_b16 v[60:61], v157 offset:0\n\t"
        "v_or_b32 v158, v184, v158\n\t"
        "s_waitcnt lgkmcnt(9)\n\t"
        "v_mfma_f32_16x16x32_bf16 v[0:3], a[252:255], v[112:115], v[0:3]\n\t"
        "ds_read_b64_tr_b16 v[62:63], v158 offset:0\n\t"
        "v_xor_b32 v159, 32, v157\n\t"
        "v_mfma_f32_16x16x32_bf16 v[4:7], v[188:191], v[112:115], v[4:7]\n\t"
        "ds_read_b64_tr_b16 v[64:65], v159 offset:0\n\t"
        "v_xor_b32 v152, 32, v158\n\t"
        "v_mfma_f32_16x16x32_bf16 v[8:11], v[192:195], v[112:115], v[8:11]\n\t"
        "ds_read_b64_tr_b16 v[66:67], v152 offset:0\n\t"
        "v_perm_b32 v164, v162, v162, v168\n\t"
        "v_mfma_f32_16x16x32_bf16 v[12:15], v[196:199], v[112:115], v[12:15]\n\t"
        "ds_read_b64_tr_b16 v[68:69], v157 offset:64\n\t"
        "v_perm_b32 v165, v162, v162, v169\n\t"
        "v_mfma_f32_16x16x32_bf16 v[16:19], v[200:203], v[112:115], v[16:19]\n\t"
        "ds_read_b64_tr_b16 v[70:71], v158 offset:64\n\t"
        "v_perm_b32 v166, v162, v162, v170\n\t"
        "v_mfma_f32_16x16x32_bf16 v[20:23], v[204:207], v[112:115], v[20:23]\n\t"
        "ds_read_b64_tr_b16 v[72:73], v159 offset:64\n\t"
        "v_perm_b32 v167, v162, v162, v171\n\t"
        "v_mfma_f32_16x16x32_bf16 v[36:39], a[240:243], v[84:87], v[36:39]\n\t"
        "ds_read_b64_tr_b16 v[74:75], v152 offset:64\n\t"
        "v_mfma_f32_16x16x32_bf16 v[40:43], a[244:247], v[84:87], v[40:43]\n\t"
        "ds_read_b64_tr_b16 v[76:77], v157 offset:128\n\t"
        "v_lshlrev_b32_sdwa v157, 5, v161 dst_sel:DWORD dst_unused:UNUSED_PAD src0_sel:DWORD src1_sel:WORD_0\n\t"
        "v_mfma_f32_16x16x32_bf16 v[44:47], a[248:251], v[84:87], v[44:47]\n\t"
        "ds_read_b64_tr_b16 v[78:79], v158 offset:128\n\t"
        "ds_read_b128 v[96:99], v130 offset:16448\n\t"
        "v_lshlrev_b32_sdwa v158, 5, v161 dst_sel:DWORD dst_unused:UNUSED_PAD src0_sel:DWORD src1_sel:WORD_1\n\t"
        "v_mfma_f32_16x16x32_bf16 v[0:3], v[208:211], v[88:91], v[0:3]\n\t"
        "ds_read_b64_tr_b16 v[80:81], v159 offset:128\n\t"
        "ds_read_b64_tr_b16 v[234:235], v131 offset:49920\n\t"
        "v_or_b32 v157, v184, v157\n\t"
        "s_waitcnt lgkmcnt(11)\n\t"
        "v_mfma_f32_16x16x32_bf16 v[48:51], v[60:63], v[164:167], 0\n\t"
        "ds_read_b64_tr_b16 v[82:83], v152 offset:128\n\t"
        "ds_read_b64_tr_b16 v[236:237], v132 offset:49152\n\t"
        "v_or_b32 v158, v184, v158\n\t"
        "v_mfma_f32_16x16x32_bf16 v[4:7], v[212:215], v[88:91], v[4:7]\n\t"
        "ds_read_b64_tr_b16 v[60:61], v157 offset:0\n\t"
        "ds_read_b64_tr_b16 v[238:239], v132 offset:49920\n\t"
        "v_xor_b32 v159, 32, v157\n\t"
        "s_waitcnt lgkmcnt(12)\n\t"
        "v_mfma_f32_16x16x32_bf16 v[52:55], v[64:67], v[164:167], 0\n\t"
        "ds_read_b64_tr_b16 v[62:63], v158 offset:0\n\t"
        "ds_read_b64_tr_b16 v[240:241], v133 offset:49152\n\t"
        "v_xor_b32 v152, 32, v158\n\t"
        "v_mfma_f32_16x16x32_bf16 v[8:11], v[216:219], v[88:91], v[8:11]\n\t"
        "ds_read_b64_tr_b16 v[64:65], v159 offset:0\n\t"
        "ds_read_b64_tr_b16 v[242:243], v133 offset:49920\n\t"
        "s_waitcnt lgkmcnt(13)\n\t"
        "v_mfma_f32_16x16x32_bf16 v[56:59], v[68:71], v[164:167], 0\n\t"
        "ds_read_b64_tr_b16 v[66:67], v152 offset:0\n\t"
        "v_mfma_f32_16x16x32_bf16 v[12:15], a[252:255], v[88:91], v[12:15]\n\t"
        "ds_read_b64_tr_b16 v[68:69], v157 offset:64\n\t"
        "v_mfma_f32_16x16x32_bf16 v[116:119], v[72:75], v[164:167], 0\n\t"
        "ds_read_b64_tr_b16 v[70:71], v158 offset:64\n\t"
        "v_mfma_f32_16x16x32_bf16 v[16:19], v[188:191], v[88:91], v[16:19]\n\t"
        "ds_read_b64_tr_b16 v[72:73], v159 offset:64\n\t"
        "s_waitcnt lgkmcnt(12)\n\t"
        "v_mfma_f32_16x16x32_bf16 v[120:123], v[76:79], v[164:167], 0\n\t"
        "ds_read_b64_tr_b16 v[74:75], v152 offset:64\n\t"
        "v_mfma_f32_16x16x32_bf16 v[20:23], v[192:195], v[88:91], v[20:23]\n\t"
        "ds_read_b64_tr_b16 v[76:77], v157 offset:128\n\t"
        "s_waitcnt lgkmcnt(12)\n\t"
        "v_mfma_f32_16x16x32_bf16 v[124:127], v[80:83], v[164:167], 0\n\t"
        "ds_read_b64_tr_b16 v[78:79], v158 offset:128\n\t"
        "v_perm_b32 v164, v163, v163, v168\n\t"
        "v_mfma_f32_16x16x32_bf16 v[24:27], v[196:199], v[88:91], v[24:27]\n\t"
        "ds_read_b64_tr_b16 v[80:81], v159 offset:128\n\t"
        "v_perm_b32 v165, v163, v163, v169\n\t"
        "v_mfma_f32_16x16x32_bf16 v[28:31], v[200:203], v[88:91], v[28:31]\n\t"
        "ds_read_b64_tr_b16 v[82:83], v152 offset:128\n\t"
        "v_perm_b32 v166, v163, v163, v170\n\t"
        "v_mfma_f32_16x16x32_bf16 v[32:35], v[204:207], v[88:91], v[32:35]\n\t"
        "ds_read_b128 v[100:103], v130 offset:128\n\t"
        "v_perm_b32 v167, v163, v163, v171\n\t"
        "v_mfma_f32_16x16x32_bf16 v[12:15], v[208:211], v[92:95], v[12:15]\n\t"
        "ds_read_b128 v[104:107], v130 offset:4224\n\t"
        "s_waitcnt lgkmcnt(10)\n\t"
        "v_mfma_f32_16x16x32_bf16 v[48:51], v[60:63], v[164:167], v[48:51]\n\t"
        "ds_read_b64_tr_b16 v[244:245], v131 offset:55296\n\t"
        "v_mfma_f32_16x16x32_bf16 v[16:19], v[212:215], v[92:95], v[16:19]\n\t"
        "ds_read_b64_tr_b16 v[246:247], v131 offset:56064\n\t"
        "v_mfma_f32_16x16x32_bf16 v[52:55], v[64:67], v[164:167], v[52:55]\n\t"
        "ds_read_b64_tr_b16 v[248:249], v132 offset:55296\n\t"
        "v_mfma_f32_16x16x32_bf16 v[20:23], v[216:219], v[92:95], v[20:23]\n\t"
        "ds_read_b64_tr_b16 v[250:251], v132 offset:56064\n\t"
        "ds_read_b64_tr_b16 v[252:253], v133 offset:55296\n\t"
        "s_waitcnt lgkmcnt(10)\n\t"
        "v_mfma_f32_16x16x32_bf16 v[56:59], v[68:71], v[164:167], v[56:59]\n\t"
        "ds_read_b64_tr_b16 v[254:255], v133 offset:56064\n\t"
        "v_cvt_pk_bf16_f32 v48, v48, v49\n\t"
        "v_mfma_f32_16x16x32_bf16 v[24:27], a[252:255], v[92:95], v[24:27]\n\t"
        "ds_read_b128 v[108:111], v130 offset:8320\n\t"
        "v_cvt_pk_bf16_f32 v49, v50, v51\n\t"
        "v_mfma_f32_16x16x32_bf16 v[116:119], v[72:75], v[164:167], v[116:119]\n\t"
        "ds_write_b64 v186, v[48:49] offset:12288\n\t"
        "v_mfma_f32_16x16x32_bf16 v[28:31], v[188:191], v[92:95], v[28:31]\n\t"
        "v_cvt_pk_bf16_f32 v52, v52, v53\n\t"
        "v_cvt_pk_bf16_f32 v53, v54, v55\n\t"
        "s_waitcnt lgkmcnt(9)\n\t"
        "v_mfma_f32_16x16x32_bf16 v[120:123], v[76:79], v[164:167], v[120:123]\n\t"
        "ds_write_b64 v154, v[52:53] offset:12288\n\t"
        "v_mfma_f32_16x16x32_bf16 v[32:35], v[192:195], v[92:95], v[32:35]\n\t"
        "v_cvt_pk_bf16_f32 v56, v56, v57\n\t"
        "v_cvt_pk_bf16_f32 v57, v58, v59\n\t"
        "v_mfma_f32_16x16x32_bf16 v[124:127], v[80:83], v[164:167], v[124:127]\n\t"
        "ds_write_b64 v186, v[56:57] offset:12352\n\t"
        "v_mfma_f32_16x16x32_bf16 v[36:39], v[196:199], v[92:95], v[36:39]\n\t"
        "v_cvt_pk_bf16_f32 v116, v116, v117\n\t"
        "v_cvt_pk_bf16_f32 v117, v118, v119\n\t"
        "v_mfma_f32_16x16x32_bf16 v[40:43], v[200:203], v[92:95], v[40:43]\n\t"
        "ds_write_b64 v154, v[116:117] offset:12352\n\t"
        "v_mfma_f32_16x16x32_bf16 v[44:47], v[204:207], v[92:95], v[44:47]\n\t"
        "v_cvt_pk_bf16_f32 v120, v120, v121\n\t"
        "v_cvt_pk_bf16_f32 v121, v122, v123\n\t"
        "v_mfma_f32_16x16x32_bf16 v[24:27], v[208:211], v[96:99], v[24:27]\n\t"
        "ds_write_b64 v186, v[120:121] offset:12416\n\t"
        "v_mfma_f32_16x16x32_bf16 v[28:31], v[212:215], v[96:99], v[28:31]\n\t"
        "v_cvt_pk_bf16_f32 v124, v124, v125\n\t"
        "v_cvt_pk_bf16_f32 v125, v126, v127\n\t"
        "v_mfma_f32_16x16x32_bf16 v[32:35], v[216:219], v[96:99], v[32:35]\n\t"
        "ds_write_b64 v154, v[124:125] offset:12416\n\t"
        "v_mfma_f32_16x16x32_bf16 v[36:39], a[252:255], v[96:99], v[36:39]\n\t"
        "ds_read_b128 v[112:115], v130 offset:12416\n\t"
        "v_mfma_f32_16x16x32_bf16 v[40:43], v[188:191], v[96:99], v[40:43]\n\t"
        "v_mfma_f32_16x16x32_bf16 v[44:47], v[192:195], v[96:99], v[44:47]\n\t"
        "v_mfma_f32_16x16x32_bf16 v[0:3], v[232:235], v[100:103], v[0:3]\n\t"
        "v_mfma_f32_16x16x32_bf16 v[4:7], v[236:239], v[100:103], v[4:7]\n\t"
        "v_mfma_f32_16x16x32_bf16 v[8:11], v[240:243], v[100:103], v[8:11]\n\t"
        "s_waitcnt lgkmcnt(1)\n\t"
        "s_barrier\n\t"
        "v_mfma_f32_16x16x32_bf16 v[0:3], v[220:223], v[104:107], v[0:3]\n\t"
        "ds_read_b128 v[124:127], v182 offset:0\n\t"
        "v_mfma_f32_16x16x32_bf16 v[4:7], v[224:227], v[104:107], v[4:7]\n\t"
        "ds_read_b128 v[132:135], v187 offset:0\n\t"
        "v_mfma_f32_16x16x32_bf16 v[8:11], v[228:231], v[104:107], v[8:11]\n\t"
        "ds_read_b128 v[136:139], v187 offset:16\n\t"
        "v_mfma_f32_16x16x32_bf16 v[12:15], v[232:235], v[104:107], v[12:15]\n\t"
        "ds_read_b32 v140, v187 offset:32\n\t"
        "v_mfma_f32_16x16x32_bf16 v[16:19], v[236:239], v[104:107], v[16:19]\n\t"
        "ds_read_b128 v[84:87], v130 offset:16512\n\t"
        "v_mfma_f32_16x16x32_bf16 v[20:23], v[240:243], v[104:107], v[20:23]\n\t"
        "v_mfma_f32_16x16x32_bf16 v[0:3], v[244:247], v[108:111], v[0:3]\n\t"
        "v_mfma_f32_16x16x32_bf16 v[4:7], v[248:251], v[108:111], v[4:7]\n\t"
        "v_mfma_f32_16x16x32_bf16 v[8:11], v[252:255], v[108:111], v[8:11]\n\t"
        "v_mfma_f32_16x16x32_bf16 v[12:15], v[220:223], v[108:111], v[12:15]\n\t"
        "ds_read_b128 v[88:91], v130 offset:20544\n\t"
        "s_waitcnt lgkmcnt(5)\n\t"
        "v_or3_b32 v124, v124, v125, v126\n\t"
        "v_mfma_f32_16x16x32_bf16 v[16:19], v[224:227], v[108:111], v[16:19]\n\t"
        "v_or_b32 v124, v124, v127\n\t"
        "v_mfma_f32_16x16x32_bf16 v[20:23], v[228:231], v[108:111], v[20:23]\n\t"
        "v_readfirstlane_b32 s6, v124\n\t"
        "v_mfma_f32_16x16x32_bf16 v[24:27], v[232:235], v[108:111], v[24:27]\n\t"
        "s_nop 0\n\t"
        "s_bitcmp1_b32 s6, 1\n\t"
        "s_cbranch_scc1 .Lx68%=\n\t"
        ".Lx69%=:\n\t"
        "v_mfma_f32_16x16x32_bf16 v[28:31], v[236:239], v[108:111], v[28:31]\n\t"
        "ds_read_b128 v[160:163], v185 offset:512\n\t"
        "s_waitcnt lgkmcnt(3)\n\t"
        "v_xor_b32 v132, v181, v132\n\t"
        "v_add_u32 v151, s9, v172\n\t"
        "v_mfma_f32_16x16x32_bf16 v[32:35], v[240:243], v[108:111], v[32:35]\n\t"
        "ds_read_b128 v[92:95], v130 offset:20608\n\t"
        "v_max_u32 v151, v151, v132\n\t"
        "v_xor_b32 v133, v181, v133\n\t"
        "v_mfma_f32_16x16x32_bf16 v[12:15], v[244:247], v[112:115], v[12:15]\n\t"
        "ds_read_b128 v[96:99], v151 offset:0\n\t"
        "v_add_u32 v183, s9, v173\n\t"
        "v_max_u32 v183, v183, v133\n\t"
        "v_mfma_f32_16x16x32_bf16 v[16:19], v[248:251], v[112:115], v[16:19]\n\t"
        "v_xor_b32 v134, v181, v134\n\t"
        "s_add_u32 s55, s41, 0\n\t"
        "v_cvt_pk_bf16_f32 v116, v0, v1\n\t"
        "v_mfma_f32_16x16x32_bf16 v[20:23], v[252:255], v[112:115], v[20:23]\n\t"
        "v_cvt_pk_bf16_f32 v117, v2, v3\n\t"
        "v_cvt_pk_bf16_f32 v118, v4, v5\n\t"
        "v_cvt_pk_bf16_f32 v119, v6, v7\n\t"
        "v_mfma_f32_16x16x32_bf16 v[24:27], v[220:223], v[112:115], v[24:27]\n\t"
        "ds_read_b128 v[100:103], v151 offset:64\n\t"
        "v_cvt_pk_bf16_f32 v120, v8, v9\n\t"
        "v_cvt_pk_bf16_f32 v121, v10, v11\n\t"
        "v_mfma_f32_16x16x32_bf16 v[28:31], v[224:227], v[112:115], v[28:31]\n\t"
        "v_permlane16_swap_b32 v116, v118\n\t"
        "v_mfma_f32_16x16x32_bf16 v[32:35], v[228:231], v[112:115], v[32:35]\n\t"
        "v_permlane16_swap_b32 v117, v119\n\t"
        "v_mfma_f32_16x16x32_bf16 v[36:39], v[232:235], v[112:115], v[36:39]\n\t"
        "buffer_store_dwordx4 v[116:119], v144, s[28:31], s55 offen sc1\n\t"
        "v_mfma_f32_16x16x32_bf16 v[40:43], v[236:239], v[112:115], v[40:43]\n\t"
        "ds_write_b64 v146, v[120:121] offset:0\n\t"
        "s_add_u32 s55, s41, 43008\n\t"
        "v_cvt_pk_bf16_f32 v116, v12, v13\n\t"
        "v_mfma_f32_16x16x32_bf16 v[44:47], v[240:243], v[112:115], v[44:47]\n\t"
        "ds_read_b128 v[104:107], v151 offset:128\n\t"
        "v_add_u32 v151, s9, v174\n\t"
        "v_max_u32 v151, v151, v134\n\t"
        "s_waitcnt lgkmcnt(3)\n\t"
        "v_mfma_f32_16x16x32_bf16 v[24:27], v[244:247], v[84:87], v[24:27]\n\t"
        "ds_read_b128 v[108:111], v183 offset:0\n\t"
        "v_xor_b32 v135, v181, v135\n\t"
        "v_cvt_pk_bf16_f32 v117, v14, v15\n\t"
        "v_mfma_f32_16x16x32_bf16 v[28:31], v[248:251], v[84:87], v[28:31]\n\t"
        "v_cvt_pk_bf16_f32 v118, v16, v17\n\t"
        "v_cvt_pk_bf16_f32 v119, v18, v19\n\t"
        "v_cvt_pk_bf16_f32 v120, v20, v21\n\t"
        "v_mfma_f32_16x16x32_bf16 v[32:35], v[252:255], v[84:87], v[32:35]\n\t"
        "v_cvt_pk_bf16_f32 v121, v22, v23\n\t"
        "v_permlane16_swap_b32 v116, v118\n\t"
        "v_mfma_f32_16x16x32_bf16 v[36:39], v[220:223], v[84:87], v[36:39]\n\t"
        "v_permlane16_swap_b32 v117, v119\n\t"
        "v_lshlrev_b32_sdwa v157, 5, v160 dst_sel:DWORD dst_unused:UNUSED_PAD src0_sel:DWORD src1_sel:WORD_0\n\t"
        "v_mfma_f32_16x16x32_bf16 v[40:43], v[224:227], v[84:87], v[40:43]\n\t"
        "ds_read_b128 v[112:115], v183 offset:64\n\t"
        "v_lshlrev_b32_sdwa v158, 5, v160 dst_sel:DWORD dst_unused:UNUSED_PAD src0_sel:DWORD src1_sel:WORD_1\n\t"
        "v_or_b32 v157, v184, v157\n\t"
        "v_mfma_f32_16x16x32_bf16 v[44:47], v[228:231], v[84:87], v[44:47]\n\t"
        "ds_read_b64_tr_b16 v[60:61], v157 offset:0\n\t"
        "ds_read_b64_tr_b16 v[68:69], v157 offset:64\n\t"
        "v_or_b32 v158, v184, v158\n\t"
        "v_mfma_f32_16x16x32_bf16 v[36:39], v[208:211], v[88:91], v[36:39]\n\t"
        "ds_read_b64_tr_b16 v[62:63], v158 offset:0\n\t"
        "ds_read_b64_tr_b16 v[70:71], v158 offset:64\n\t"
        "v_xor_b32 v159, 32, v157\n\t"
        "v_mfma_f32_16x16x32_bf16 v[40:43], v[212:215], v[88:91], v[40:43]\n\t"
        "ds_read_b64_tr_b16 v[64:65], v159 offset:0\n\t"
        "ds_read_b64_tr_b16 v[72:73], v159 offset:64\n\t"
        "v_xor_b32 v152, 32, v158\n\t"
        "v_mfma_f32_16x16x32_bf16 v[44:47], v[216:219], v[88:91], v[44:47]\n\t"
        "ds_read_b64_tr_b16 v[66:67], v152 offset:0\n\t"
        "ds_read_b64_tr_b16 v[74:75], v152 offset:64\n\t"
        "v_perm_b32 v164, v162, v162, v168\n\t"
        "v_mfma_f32_16x16x32_bf16 v[36:39], v[244:247], v[92:95], v[36:39]\n\t"
        "ds_read_b64_tr_b16 v[76:77], v157 offset:128\n\t"
        "ds_read_b64_tr_b16 v[78:79], v158 offset:128\n\t"
        "v_perm_b32 v165, v162, v162, v169\n\t"
        "v_mfma_f32_16x16x32_bf16 v[40:43], v[248:251], v[92:95], v[40:43]\n\t"
        "ds_read_b64_tr_b16 v[80:81], v159 offset:128\n\t"
        ".LpX1%=:\n\t"
        "s_cbranch_execz .LrX%=\n\t"
        "ds_read_b64_tr_b16 v[82:83], v152 offset:128\n\t"
        "v_perm_b32 v166, v162, v162, v170\n\t"
        "v_mfma_f32_16x16x32_bf16 v[44:47], v[252:255], v[92:95], v[44:47]\n\t"
        "buffer_store_dwordx4 v[116:119], v144, s[28:31], s55 offen sc1\n\t"
        "v_mfma_f32_16x16x32_bf16 v[48:51], a[12:15], v[96:99], 0\n\t"
        "ds_write_b64 v146, v[120:121] offset:2048\n\t"
        "s_add_u32 s55, s41, 86016\n\t"
        "v_mfma_f32_16x16x32_bf16 v[52:55], a[16:19], v[96:99], 0\n\t"
        "ds_read_b128 v[84:87], v183 offset:128\n\t"
        "v_add_u32 v183, s9, v175\n\t"
        "v_max_u32 v183, v183, v135\n\t"
        "v_mfma_f32_16x16x32_bf16 v[56:59], a[20:23], v[96:99], 0\n\t"
        "ds_read_b128 v[88:91], v151 offset:0\n\t"
        "v_xor_b32 v136, v181, v136\n\t"
        "v_cvt_pk_bf16_f32 v116, v24, v25\n\t"
        "s_waitcnt lgkmcnt(8)\n\t"
        "v_mfma_f32_16x16x32_bf16 v[48:51], a[48:51], v[100:103], v[48:51]\n\t"
        "ds_read_b128 v[92:95], v151 offset:64\n\t"
        "v_cvt_pk_bf16_f32 v117, v26, v27\n\t"
        "v_cvt_pk_bf16_f32 v118, v28, v29\n\t"
        "v_mfma_f32_16x16x32_bf16 v[52:55], a[52:55], v[100:103], v[52:55]\n\t"
        "ds_read_b128 v[96:99], v151 offset:128\n\t"
        "v_add_u32 v151, s9, v176\n\t"
        "v_max_u32 v151, v151, v136\n\t"
        "v_mfma_f32_16x16x32_bf16 v[56:59], a[56:59], v[100:103], v[56:59]\n\t"
        "v_xor_b32 v137, v181, v137\n\t"
        "v_cvt_pk_bf16_f32 v119, v30, v31\n\t"
        "v_cvt_pk_bf16_f32 v120, v32, v33\n\t"
        "v_mfma_f32_16x16x32_bf16 v[48:51], a[84:87], v[104:107], v[48:51]\n\t"
        "ds_read_b128 v[100:103], v183 offset:0\n\t"
        "v_cvt_pk_bf16_f32 v121, v34, v35\n\t"
        "v_perm_b32 v167, v162, v162, v171\n\t"
        "v_mfma_f32_16x16x32_bf16 v[52:55], a[88:91], v[104:107], v[52:55]\n\t"
        "v_permlane16_swap_b32 v116, v118\n\t"
        "v_lshlrev_b32_sdwa v157, 5, v161 dst_sel:DWORD dst_unused:UNUSED_PAD src0_sel:DWORD src1_sel:WORD_0\n\t"
        "v_mfma_f32_16x16x32_bf16 v[0:3], v[60:63], v[164:167], 0\n\t"
        "v_permlane16_swap_b32 v117, v119\n\t"
        "v_lshlrev_b32_sdwa v158, 5, v161 dst_sel:DWORD dst_unused:UNUSED_PAD src0_sel:DWORD src1_sel:WORD_1\n\t"
        "v_mfma_f32_16x16x32_bf16 v[56:59], a[92:95], v[104:107], v[56:59]\n\t"
        "v_or_b32 v157, v184, v157\n\t"
        "v_or_b32 v158, v184, v158\n\t"
        "v_xor_b32 v159, 32, v157\n\t"
        "v_mfma_f32_16x16x32_bf16 v[4:7], v[64:67], v[164:167], 0\n\t"
        "ds_read_b64_tr_b16 v[60:61], v157 offset:0\n\t"
        "ds_read_b64_tr_b16 v[62:63], v158 offset:0\n\t"
        "v_xor_b32 v152, 32, v158\n\t"
        "v_mfma_f32_16x16x32_bf16 v[48:51], a[120:123], v[108:111], v[48:51]\n\t"
        "ds_read_b64_tr_b16 v[64:65], v159 offset:0\n\t"
        "ds_read_b64_tr_b16 v[66:67], v152 offset:0\n\t"
        "v_mfma_f32_16x16x32_bf16 v[8:11], v[68:71], v[164:167], 0\n\t"
        "ds_read_b64_tr_b16 v[68:69], v157 offset:64\n\t"
        "ds_read_b64_tr_b16 v[70:71], v158 offset:64\n\t"
        "v_mfma_f32_16x16x32_bf16 v[52:55], a[124:127], v[108:111], v[52:55]\n\t"
        "buffer_store_dwordx4 v[116:119], v144, s[28:31], s55 offen sc1\n\t"
        "s_waitcnt lgkmcnt(6)\n\t"
        "v_mfma_f32_16x16x32_bf16 v[12:15], v[72:75], v[164:167], 0\n\t"
        "ds_write_b64 v146, v[120:121] offset:4096\n\t"
        "s_add_u32 s55, s41, 129024\n\t"
        "v_mfma_f32_16x16x32_bf16 v[56:59], a[128:131], v[108:111], v[56:59]\n\t"
        "ds_read_b64_tr_b16 v[72:73], v159 offset:64\n\t"
        "ds_read_b64_tr_b16 v[74:75], v152 offset:64\n\t"
        "v_cvt_pk_bf16_f32 v116, v36, v37\n\t"
        "v_mfma_f32_16x16x32_bf16 v[16:19], v[76:79], v[164:167], 0\n\t"
        "ds_read_b64_tr_b16 v[76:77], v157 offset:128\n\t"
        "ds_read_b64_tr_b16 v[78:79], v158 offset:128\n\t"
        "v_cvt_pk_bf16_f32 v117, v38, v39\n\t"
        "v_mfma_f32_16x16x32_bf16 v[48:51], a[156:159], v[112:115], v[48:51]\n\t"
        "ds_read_b128 v[104:107], v183 offset:64\n\t"
        "v_cvt_pk_bf16_f32 v118, v40, v41\n\t"
        "v_cvt_pk_bf16_f32 v119, v42, v43\n\t"
        "v_mfma_f32_16x16x32_bf16 v[20:23], v[80:83], v[164:167], 0\n\t"
        "ds_read_b64_tr_b16 v[80:81], v159 offset:128\n\t"
        "ds_read_b64_tr_b16 v[82:83], v152 offset:128\n\t"
        "v_cvt_pk_bf16_f32 v120, v44, v45\n\t"
        "v_mfma_f32_16x16x32_bf16 v[52:55], a[160:163], v[112:115], v[52:55]\n\t"
        "ds_read_b128 v[108:111], v183 offset:128\n\t"
        "v_add_u32 v183, s9, v177\n\t"
        "v_max_u32 v183, v183, v137\n\t"
        "v_mfma_f32_16x16x32_bf16 v[56:59], a[164:167], v[112:115], v[56:59]\n\t"
        "v_xor_b32 v138, v181, v138\n\t"
        "v_cvt_pk_bf16_f32 v121, v46, v47\n\t"
        "v_perm_b32 v164, v163, v163, v168\n\t"
        "v_mfma_f32_16x16x32_bf16 v[48:51], a[192:195], v[84:87], v[48:51]\n\t"
        "v_permlane16_swap_b32 v116, v118\n\t"
        "v_perm_b32 v165, v163, v163, v169\n\t"
        "v_mfma_f32_16x16x32_bf16 v[52:55], a[196:199], v[84:87], v[52:55]\n\t"
        "v_permlane16_swap_b32 v117, v119\n\t"
        "v_perm_b32 v166, v163, v163, v170\n\t"
        "v_mfma_f32_16x16x32_bf16 v[56:59], a[200:203], v[84:87], v[56:59]\n\t"
        "ds_read_b128 v[112:115], v151 offset:0\n\t"
        "v_perm_b32 v167, v163, v163, v171\n\t"
        "v_mfma_f32_16x16x32_bf16 v[48:51], a[228:231], v[88:91], v[48:51]\n\t"
        "ds_read_b128 v[160:163], v185 offset:768\n\t"
        "s_waitcnt lgkmcnt(6)\n\t"
        "v_mfma_f32_16x16x32_bf16 v[0:3], v[60:63], v[164:167], v[0:3]\n\t"
        "ds_read_b128 v[84:87], v151 offset:64\n\t"
        "v_mfma_f32_16x16x32_bf16 v[52:55], a[232:235], v[88:91], v[52:55]\n\t"
        "v_mfma_f32_16x16x32_bf16 v[4:7], v[64:67], v[164:167], v[4:7]\n\t"
        "buffer_store_dwordx4 v[116:119], v144, s[28:31], s55 offen sc1\n\t"
        "v_mfma_f32_16x16x32_bf16 v[56:59], a[236:239], v[88:91], v[56:59]\n\t"
        "ds_write_b64 v146, v[120:121] offset:6144\n\t"
        "v_mfma_f32_16x16x32_bf16 v[8:11], v[68:71], v[164:167], v[8:11]\n\t"
        "s_nop 0\n\t"
        "v_cvt_pk_bf16_f32 v0, v0, v1\n\t"
        "v_cvt_pk_bf16_f32 v1, v2, v3\n\t"
        "v_mfma_f32_16x16x32_bf16 v[48:51], v[196:199], v[92:95], v[48:51]\n\t"
        "ds_write_b64 v186, v[0:1] offset:24576\n\t"
        "v_mfma_f32_16x16x32_bf16 v[12:15], v[72:75], v[164:167], v[12:15]\n\t"
        "ds_read_b128 v[88:91], v151 offset:128\n\t"
        "v_add_u32 v151, s9, v178\n\t"
        "v_max_u32 v151, v151, v138\n\t"
        "v_xor_b32 v139, v181, v139\n\t"
        "v_cvt_pk_bf16_f32 v4, v4, v5\n\t"
        "v_mfma_f32_16x16x32_bf16 v[52:55], v[200:203], v[92:95], v[52:55]\n\t"
        "v_cvt_pk_bf16_f32 v5, v6, v7\n\t"
        "v_mfma_f32_16x16x32_bf16 v[16:19], v[76:79], v[164:167], v[16:19]\n\t"
        "ds_write_b64 v154, v[4:5] offset:24576\n\t"
        "v_cvt_pk_bf16_f32 v8, v8, v9\n\t"
        "v_mfma_f32_16x16x32_bf16 v[56:59], v[204:207], v[92:95], v[56:59]\n\t"
        "v_cvt_pk_bf16_f32 v9, v10, v11\n\t"
        "s_waitcnt lgkmcnt(4)\n\t"
        "v_lshlrev_b32_sdwa v157, 5, v160 dst_sel:DWORD dst_unused:UNUSED_PAD src0_sel:DWORD src1_sel:WORD_0\n\t"
        "v_lshlrev_b32_sdwa v158, 5, v160 dst_sel:DWORD dst_unused:UNUSED_PAD src0_sel:DWORD src1_sel:WORD_1\n\t"
        "v_mfma_f32_16x16x32_bf16 v[20:23], v[80:83], v[164:167], v[20:23]\n\t"
        "ds_write_b64 v186, v[8:9] offset:24640\n\t"
        "v_mfma_f32_16x16x32_bf16 v[48:51], v[232:235], v[96:99], v[48:51]\n\t"
        "ds_read_b128 v[92:95], v183 offset:0\n\t"
        "v_cvt_pk_bf16_f32 v12, v12, v13\n\t"
        "v_cvt_pk_bf16_f32 v13, v14, v15\n\t"
        "v_or_b32 v157, v184, v157\n\t"
        "v_mfma_f32_16x16x32_bf16 v[52:55], v[236:239], v[96:99], v[52:55]\n\t"
        "ds_write_b64 v154, v[12:13] offset:24640\n\t"
        "v_mfma_f32_16x16x32_bf16 v[56:59], v[240:243], v[96:99], v[56:59]\n\t"
        "ds_read_b64_tr_b16 v[60:61], v157 offset:0\n\t"
        "ds_read_b64_tr_b16 v[68:69], v157 offset:64\n\t"
        "v_cvt_pk_bf16_f32 v16, v16, v17\n\t"
        "v_cvt_pk_bf16_f32 v17, v18, v19\n\t"
        "v_mfma_f32_16x16x32_bf16 v[48:51], a[0:3], v[100:103], v[48:51]\n\t"
        "ds_write_b64 v186, v[16:17] offset:24704\n\t"
        "v_mfma_f32_16x16x32_bf16 v[52:55], a[4:7], v[100:103], v[52:55]\n\t"
        "ds_read_b64_tr_b16 v[76:77], v157 offset:128\n\t"
        "v_cvt_pk_bf16_f32 v20, v20, v21\n\t"
        "v_cvt_pk_bf16_f32 v21, v22, v23\n\t"
        "v_or_b32 v158, v184, v158\n\t"
        "v_mfma_f32_16x16x32_bf16 v[56:59], a[8:11], v[100:103], v[56:59]\n\t"
        "ds_write_b64 v154, v[20:21] offset:24704\n\t"
        "v_mfma_f32_16x16x32_bf16 v[48:51], a[36:39], v[104:107], v[48:51]\n\t"
        "ds_read_b64_tr_b16 v[62:63], v158 offset:0\n\t"
        "ds_read_b64_tr_b16 v[70:71], v158 offset:64\n\t"
        "v_xor_b32 v159, 32, v157\n\t"
        "v_xor_b32 v152, 32, v158\n\t"
        "v_mfma_f32_16x16x32_bf16 v[52:55], a[40:43], v[104:107], v[52:55]\n\t"
        "ds_read_b64_tr_b16 v[64:65], v159 offset:0\n\t"
        "ds_read_b64_tr_b16 v[66:67], v152 offset:0\n\t"
        "v_perm_b32 v164, v162, v162, v168\n\t"
        "v_mfma_f32_16x16x32_bf16 v[56:59], a[44:47], v[104:107], v[56:59]\n\t"
        "ds_read_b128 v[96:99], v183 offset:64\n\t"
        "ds_read_b64_tr_b16 v[72:73], v159 offset:64\n\t"
        "v_perm_b32 v165, v162, v162, v169\n\t"
        "v_mfma_f32_16x16x32_bf16 v[48:51], a[72:75], v[108:111], v[48:51]\n\t"
        "ds_read_b64_tr_b16 v[74:75], v152 offset:64\n\t"
        "ds_read_b64_tr_b16 v[78:79], v158 offset:128\n\t"
        "v_perm_b32 v166, v162, v162, v170\n\t"
        "v_mfma_f32_16x16x32_bf16 v[52:55], a[76:79], v[108:111], v[52:55]\n\t"
        "ds_read_b64_tr_b16 v[80:81], v159 offset:128\n\t"
        "ds_read_b64_tr_b16 v[82:83], v152 offset:128\n\t"
        "v_perm_b32 v167, v162, v162, v171\n\t"
        "v_mfma_f32_16x16x32_bf16 v[56:59], a[80:83], v[108:111], v[56:59]\n\t"
        "ds_read_b128 v[100:103], v183 offset:128\n\t"
        "v_add_u32 v183, s9, v179\n\t"
        "v_max_u32 v183, v183, v139\n\t"
        "v_mfma_f32_16x16x32_bf16 v[48:51], a[108:111], v[112:115], v[48:51]\n\t"
        "ds_read_b128 v[104:107], v151 offset:0\n\t"
        "v_lshlrev_b32_sdwa v157, 5, v161 dst_sel:DWORD dst_unused:UNUSED_PAD src0_sel:DWORD src1_sel:WORD_0\n\t"
        "v_lshlrev_b32_sdwa v158, 5, v161 dst_sel:DWORD dst_unused:UNUSED_PAD src0_sel:DWORD src1_sel:WORD_1\n\t"
        "v_mfma_f32_16x16x32_bf16 v[52:55], a[112:115], v[112:115], v[52:55]\n\t"
        "ds_read_b128 v[108:111], v151 offset:64\n\t"
        "v_or_b32 v157, v184, v157\n\t"
        "v_or_b32 v158, v184, v158\n\t"
        "v_mfma_f32_16x16x32_bf16 v[56:59], a[116:119], v[112:115], v[56:59]\n\t"
        "v_xor_b32 v159, 32, v157\n\t"
        "v_xor_b32 v152, 32, v158\n\t"
        "v_mfma_f32_16x16x32_bf16 v[48:51], a[144:147], v[84:87], v[48:51]\n\t"
        "ds_read_b128 v[112:115], v151 offset:128\n\t"
        "s_waitcnt lgkmcnt(10)\n\t"
        "v_mfma_f32_16x16x32_bf16 v[0:3], v[60:63], v[164:167], 0\n\t"
        "ds_read_b64_tr_b16 v[60:61], v157 offset:0\n\t"
        "ds_read_b64_tr_b16 v[62:63], v158 offset:0\n\t"
        "v_mfma_f32_16x16x32_bf16 v[52:55], a[148:151], v[84:87], v[52:55]\n\t"
        "v_mfma_f32_16x16x32_bf16 v[4:7], v[64:67], v[164:167], 0\n\t"
        "ds_read_b64_tr_b16 v[64:65], v159 offset:0\n\t"
        "ds_read_b64_tr_b16 v[66:67], v152 offset:0\n\t"
        "v_mfma_f32_16x16x32_bf16 v[56:59], a[152:155], v[84:87], v[56:59]\n\t"
        "v_mfma_f32_16x16x32_bf16 v[8:11], v[68:71], v[164:167], 0\n\t"
        "ds_read_b64_tr_b16 v[68:69], v157 offset:64\n\t"
        "ds_read_b64_tr_b16 v[70:71], v158 offset:64\n\t"
        "v_mfma_f32_16x16x32_bf16 v[48:51], a[180:183], v[88:91], v[48:51]\n\t"
        "ds_read_b128 v[84:87], v183 offset:0\n\t"
        "s_waitcnt lgkmcnt(9)\n\t"
        "v_mfma_f32_16x16x32_bf16 v[12:15], v[72:75], v[164:167], 0\n\t"
        "ds_read_b64_tr_b16 v[72:73], v159 offset:64\n\t"
        "ds_read_b64_tr_b16 v[74:75], v152 offset:64\n\t"
        "v_mfma_f32_16x16x32_bf16 v[52:55], a[184:187], v[88:91], v[52:55]\n\t"
        "v_mfma_f32_16x16x32_bf16 v[16:19], v[76:79], v[164:167], 0\n\t"
        "ds_read_b64_tr_b16 v[76:77], v157 offset:128\n\t"
        "ds_read_b64_tr_b16 v[78:79], v158 offset:128\n\t"
        "v_mfma_f32_16x16x32_bf16 v[56:59], a[188:191], v[88:91], v[56:59]\n\t"
        "v_mfma_f32_16x16x32_bf16 v[20:23], v[80:83], v[164:167], 0\n\t"
        "ds_read_b64_tr_b16 v[80:81], v159 offset:128\n\t"
        "ds_read_b64_tr_b16 v[82:83], v152 offset:128\n\t"
        "v_perm_b32 v164, v163, v163, v168\n\t"
        "v_perm_b32 v165, v163, v163, v169\n\t"
        "v_perm_b32 v166, v163, v163, v170\n\t"
        "v_mfma_f32_16x16x32_bf16 v[48:51], a[216:219], v[92:95], v[48:51]\n\t"
        "ds_read_b128 v[88:91], v183 offset:64\n\t"
        "v_perm_b32 v167, v163, v163, v171\n\t"
        "v_mfma_f32_16x16x32_bf16 v[52:55], a[220:223], v[92:95], v[52:55]\n\t"
        "s_waitcnt lgkmcnt(9)\n\t"
        "v_mfma_f32_16x16x32_bf16 v[0:3], v[60:63], v[164:167], v[0:3]\n\t"
        "v_mfma_f32_16x16x32_bf16 v[56:59], a[224:227], v[92:95], v[56:59]\n\t"
        "v_mfma_f32_16x16x32_bf16 v[4:7], v[64:67], v[164:167], v[4:7]\n\t"
        "v_mfma_f32_16x16x32_bf16 v[48:51], a[252:255], v[96:99], v[48:51]\n\t"
        "ds_read_b128 v[92:95], v183 offset:128\n\t"
        "s_waitcnt lgkmcnt(6)\n\t"
        "v_mfma_f32_16x16x32_bf16 v[8:11], v[68:71], v[164:167], v[8:11]\n\t"
        "s_nop 1\n\t"
        "v_cvt_pk_bf16_f32 v0, v0, v1\n\t"
        "v_cvt_pk_bf16_f32 v1, v2, v3\n\t"
        "v_mfma_f32_16x16x32_bf16 v[52:55], v[188:191], v[96:99], v[52:55]\n\t"
        "ds_write_b64 v186, v[0:1] offset:36864\n\t"
        "v_mfma_f32_16x16x32_bf16 v[12:15], v[72:75], v[164:167], v[12:15]\n\t"
        "v_cvt_pk_bf16_f32 v4, v4, v5\n\t"
        "v_cvt_pk_bf16_f32 v5, v6, v7\n\t"
        "v_mfma_f32_16x16x32_bf16 v[56:59], v[192:195], v[96:99], v[56:59]\n\t"
        "ds_write_b64 v154, v[4:5] offset:36864\n\t"
        "s_waitcnt lgkmcnt(3)\n\t"
        "v_mfma_f32_16x16x32_bf16 v[16:19], v[76:79], v[164:167], v[16:19]\n\t"
        "v_cvt_pk_bf16_f32 v8, v8, v9\n\t"
        "v_cvt_pk_bf16_f32 v9, v10, v11\n\t"
        "v_mfma_f32_16x16x32_bf16 v[48:51], v[220:223], v[100:103], v[48:51]\n\t"
        "ds_write_b64 v186, v[8:9] offset:36928\n\t"
        "v_mfma_f32_16x16x32_bf16 v[20:23], v[80:83], v[164:167], v[20:23]\n\t"
        "v_cvt_pk_bf16_f32 v12, v12, v13\n\t"
        "v_cvt_pk_bf16_f32 v13, v14, v15\n\t"
        "v_mfma_f32_16x16x32_bf16 v[52:55], v[224:227], v[100:103], v[52:55]\n\t"
        "ds_write_b64 v154, v[12:13] offset:36928\n\t"
        "v_mfma_f32_16x16x32_bf16 v[56:59], v[228:231], v[100:103], v[56:59]\n\t"
        "v_cvt_pk_bf16_f32 v16, v16, v17\n\t"
        "v_cvt_pk_bf16_f32 v17, v18, v19\n\t"
        "v_mfma_f32_16x16x32_bf16 v[48:51], a[24:27], v[104:107], v[48:51]\n\t"
        "ds_write_b64 v186, v[16:17] offset:36992\n\t"
        "v_mfma_f32_16x16x32_bf16 v[52:55], a[28:31], v[104:107], v[52:55]\n\t"
        "v_cvt_pk_bf16_f32 v20, v20, v21\n\t"
        "v_cvt_pk_bf16_f32 v21, v22, v23\n\t"
        "v_mfma_f32_16x16x32_bf16 v[56:59], a[32:35], v[104:107], v[56:59]\n\t"
        "ds_write_b64 v154, v[20:21] offset:36992\n\t"
        "s_cmp_gt_u32 s61, 64\n\t"
        "s_cbranch_scc1 .Lx70%=\n\t"
        ".Lx71%=:\n\t"
        "v_mfma_f32_16x16x32_bf16 v[48:51], a[60:63], v[108:111], v[48:51]\n\t"
        "v_mfma_f32_16x16x32_bf16 v[52:55], a[64:67], v[108:111], v[52:55]\n\t"
        "v_mfma_f32_16x16x32_bf16 v[56:59], a[68:71], v[108:111], v[56:59]\n\t"
        "v_mfma_f32_16x16x32_bf16 v[48:51], a[96:99], v[112:115], v[48:51]\n\t"
        "v_mfma_f32_16x16x32_bf16 v[52:55], a[100:103], v[112:115], v[52:55]\n\t"
        "v_mfma_f32_16x16x32_bf16 v[56:59], a[104:107], v[112:115], v[56:59]\n\t"
        "s_waitcnt lgkmcnt(0)\n\t"
        "s_barrier\n\t"
        "v_xor_b32 v140, v181, v140\n\t"
        "v_mfma_f32_16x16x32_bf16 v[48:51], a[132:135], v[84:87], v[48:51]\n\t"
        "ds_read_b128 v[132:135], v187 offset:4096\n\t"
        "v_mfma_f32_16x16x32_bf16 v[52:55], a[136:139], v[84:87], v[52:55]\n\t"
        "ds_read_b128 v[136:139], v187 offset:4112\n\t"
        "v_add_u32 v151, s9, v180\n\t"
        "v_max_u32 v151, v151, v140\n\t"
        "s_waitcnt lgkmcnt(1)\n\t"
        "v_xor_b32 v132, v181, v132\n\t"
        "v_mfma_f32_16x16x32_bf16 v[56:59], a[140:143], v[84:87], v[56:59]\n\t"
        "ds_read_b128 v[96:99], v151 offset:0\n\t"
        "v_mfma_f32_16x16x32_bf16 v[48:51], a[168:171], v[88:91], v[48:51]\n\t"
        "ds_read_b128 v[100:103], v151 offset:64\n\t"
        "v_mfma_f32_16x16x32_bf16 v[52:55], a[172:175], v[88:91], v[52:55]\n\t"
        "ds_read_b128 v[104:107], v151 offset:128\n\t"
        "v_mfma_f32_16x16x32_bf16 v[56:59], a[176:179], v[88:91], v[56:59]\n\t"
        "ds_read_b32 v140, v187 offset:4128\n\t"
        "v_readlane_b32 s9, v155, 48\n\t"
        "v_mfma_f32_16x16x32_bf16 v[48:51], a[204:207], v[92:95], v[48:51]\n\t"
        "ds_read_b128 v[124:127], v182 offset:16\n\t"
        "v_add_u32 v183, s9, v172\n\t"
        "v_max_u32 v183, v183, v132\n\t"
        "v_xor_b32 v133, v181, v133\n\t"
        "v_mfma_f32_16x16x32_bf16 v[52:55], a[208:211], v[92:95], v[52:55]\n\t"
        "ds_read_b128 v[108:111], v183 offset:0\n\t"
        "v_add_u32 v151, s9, v173\n\t"
        "v_mfma_f32_16x16x32_bf16 v[56:59], a[212:215], v[92:95], v[56:59]\n\t"
        "ds_read_b128 v[112:115], v183 offset:64\n\t"
        "v_max_u32 v151, v151, v133\n\t"
        "s_waitcnt lgkmcnt(6)\n\t"
        "v_mfma_f32_16x16x32_bf16 v[48:51], a[240:243], v[96:99], v[48:51]\n\t"
        "ds_read_b128 v[84:87], v183 offset:128\n\t"
        "v_xor_b32 v134, v181, v134\n\t"
        "v_mfma_f32_16x16x32_bf16 v[52:55], a[244:247], v[96:99], v[52:55]\n\t"
        "ds_read_b128 v[88:91], v151 offset:0\n\t"
        "v_add_u32 v183, s9, v174\n\t"
        "v_mfma_f32_16x16x32_bf16 v[56:59], a[248:251], v[96:99], v[56:59]\n\t"
        "ds_read_b128 v[92:95], v151 offset:64\n\t"
        "v_max_u32 v183, v183, v134\n\t"
        "s_waitcnt lgkmcnt(8)\n\t"
        "v_mfma_f32_16x16x32_bf16 v[48:51], v[208:211], v[100:103], v[48:51]\n\t"
        "ds_read_b128 v[96:99], v151 offset:128\n\t"
        "v_xor_b32 v135, v181, v135\n\t"
        "v_mfma_f32_16x16x32_bf16 v[52:55], v[212:215], v[100:103], v[52:55]\n\t"
        "v_add_u32 v151, s9, v175\n\t"
        "v_max_u32 v151, v151, v135\n\t"
        "v_mfma_f32_16x16x32_bf16 v[56:59], v[216:219], v[100:103], v[56:59]\n\t"
        "v_xor_b32 v136, v181, v136\n\t"
        "s_waitcnt lgkmcnt(7)\n\t"
        "v_mfma_f32_16x16x32_bf16 v[48:51], v[244:247], v[104:107], v[48:51]\n\t"
        "ds_read_b128 v[100:103], v183 offset:0\n\t"
        "s_waitcnt lgkmcnt(7)\n\t"
        "v_or3_b32 v124, v124, v125, v126\n\t"
        "v_mfma_f32_16x16x32_bf16 v[52:55], v[248:251], v[104:107], v[52:55]\n\t"
        "v_or_b32 v124, v124, v127\n\t"
        "v_mfma_f32_16x16x32_bf16 v[56:59], v[252:255], v[104:107], v[56:59]\n\t"
        "v_readfirstlane_b32 s8, v124\n\t"
        "s_waitcnt lgkmcnt(6)\n\t"
        "v_mfma_f32_16x16x32_bf16 v[72:75], a[12:15], v[108:111], 0\n\t"
        "ds_read_b128 v[124:127], v147 offset:0\n\t"
        "s_or_b32 s6, s6, s8\n\t"
        "v_mfma_f32_16x16x32_bf16 v[76:79], a[16:19], v[108:111], 0\n\t"
        "ds_read_b128 v[104:107], v183 offset:64\n\t"
        "v_mfma_f32_16x16x32_bf16 v[80:83], a[20:23], v[108:111], 0\n\t"
        "s_waitcnt lgkmcnt(5)\n\t"
        "v_mfma_f32_16x16x32_bf16 v[72:75], a[48:51], v[112:115], v[72:75]\n\t"
        "ds_read_b128 v[108:111], v183 offset:128\n\t"
        "v_add_u32 v183, s9, v176\n\t"
        "v_mfma_f32_16x16x32_bf16 v[76:79], a[52:55], v[112:115], v[76:79]\n\t"
        "v_max_u32 v183, v183, v136\n\t"
        "v_xor_b32 v137, v181, v137\n\t"
        "v_mfma_f32_16x16x32_bf16 v[80:83], a[56:59], v[112:115], v[80:83]\n\t"
        "v_mfma_f32_16x16x32_bf16 v[72:75], a[84:87], v[84:87], v[72:75]\n\t"
        "ds_read_b128 v[112:115], v151 offset:0\n\t"
        "v_mfma_f32_16x16x32_bf16 v[76:79], a[88:91], v[84:87], v[76:79]\n\t"
        "v_mfma_f32_16x16x32_bf16 v[80:83], a[92:95], v[84:87], v[80:83]\n\t"
        "v_mfma_f32_16x16x32_bf16 v[72:75], a[120:123], v[88:91], v[72:75]\n\t"
        "s_waitcnt lgkmcnt(3)\n\t"
        "buffer_store_dwordx4 v[124:127], v145, s[28:31], s50 offen sc1\n\t"
        "v_mfma_f32_16x16x32_bf16 v[76:79], a[124:127], v[88:91], v[76:79]\n\t"
        "ds_read_b128 v[124:127], v147 offset:4096\n\t"
        "s_add_u32 s50, s50, 86016\n\t"
        "v_mfma_f32_16x16x32_bf16 v[80:83], a[128:131], v[88:91], v[80:83]\n\t"
        "ds_read_b128 v[84:87], v151 offset:64\n\t"
        "v_mfma_f32_16x16x32_bf16 v[72:75], a[156:159], v[92:95], v[72:75]\n\t"
        "ds_read_b128 v[88:91], v151 offset:128\n\t"
        "v_add_u32 v151, s9, v177\n\t"
        "v_mfma_f32_16x16x32_bf16 v[76:79], a[160:163], v[92:95], v[76:79]\n\t"
        "v_max_u32 v151, v151, v137\n\t"
        "v_xor_b32 v138, v181, v138\n\t"
        "v_mfma_f32_16x16x32_bf16 v[80:83], a[164:167], v[92:95], v[80:83]\n\t"
        "v_mfma_f32_16x16x32_bf16 v[72:75], a[192:195], v[96:99], v[72:75]\n\t"
        "ds_read_b128 v[92:95], v183 offset:0\n\t"
        "v_mfma_f32_16x16x32_bf16 v[76:79], a[196:199], v[96:99], v[76:79]\n\t"
        "v_mfma_f32_16x16x32_bf16 v[80:83], a[200:203], v[96:99], v[80:83]\n\t"
        "v_mfma_f32_16x16x32_bf16 v[72:75], a[228:231], v[100:103], v[72:75]\n\t"
        "ds_read_b128 v[96:99], v183 offset:64\n\t"
        "v_mfma_f32_16x16x32_bf16 v[76:79], a[232:235], v[100:103], v[76:79]\n\t"
        "v_mfma_f32_16x16x32_bf16 v[80:83], a[236:239], v[100:103], v[80:83]\n\t"
        "s_waitcnt lgkmcnt(4)\n\t"
        "v_mfma_f32_16x16x32_bf16 v[72:75], v[196:199], v[104:107], v[72:75]\n\t"
        "ds_read_b128 v[100:103], v183 offset:128\n\t"
        "v_add_u32 v183, s9, v178\n\t"
        "v_mfma_f32_16x16x32_bf16 v[76:79], v[200:203], v[104:107], v[76:79]\n\t"
        "v_max_u32 v183, v183, v138\n\t"
        "v_xor_b32 v139, v181, v139\n\t"
        "v_mfma_f32_16x16x32_bf16 v[80:83], v[204:207], v[104:107], v[80:83]\n\t"
        "buffer_store_dwordx4 v[124:127], v145, s[28:31], s50 offen sc1\n\t"
        "v_mfma_f32_16x16x32_bf16 v[72:75], v[232:235], v[108:111], v[72:75]\n\t"
        "ds_read_b128 v[104:107], v151 offset:0\n\t"
        "v_readlane_b32 s55, v155, 49\n\t"
        "v_mfma_f32_16x16x32_bf16 v[76:79], v[236:239], v[108:111], v[76:79]\n\t"
        "v_cvt_pk_bf16_f32 v116, v48, v49\n\t"
        "v_cvt_pk_bf16_f32 v117, v50, v51\n\t"
        "v_mfma_f32_16x16x32_bf16 v[80:83], v[240:243], v[108:111], v[80:83]\n\t"
        "v_cvt_pk_bf16_f32 v118, v52, v53\n\t"
        "v_cvt_pk_bf16_f32 v119, v54, v55\n\t"
        "v_mfma_f32_16x16x32_bf16 v[72:75], a[0:3], v[112:115], v[72:75]\n\t"
        "ds_read_b128 v[108:111], v151 offset:64\n\t"
        "v_cvt_pk_bf16_f32 v120, v56, v57\n\t"
        "v_mfma_f32_16x16x32_bf16 v[76:79], a[4:7], v[112:115], v[76:79]\n\t"
        "v_cvt_pk_bf16_f32 v121, v58, v59\n\t"
        "v_mfma_f32_16x16x32_bf16 v[80:83], a[8:11], v[112:115], v[80:83]\n\t"
        "v_permlane16_swap_b32 v116, v118\n\t"
        "s_waitcnt lgkmcnt(3)\n\t"
        "v_mfma_f32_16x16x32_bf16 v[72:75], a[36:39], v[84:87], v[72:75]\n\t"
        "v_permlane16_swap_b32 v117, v119\n\t"
        "v_mfma_f32_16x16x32_bf16 v[76:79], a[40:43], v[84:87], v[76:79]\n\t"
        "ds_read_b128 v[112:115], v151 offset:128\n\t"
        "v_add_u32 v151, s9, v179\n\t"
        "v_mfma_f32_16x16x32_bf16 v[80:83], a[44:47], v[84:87], v[80:83]\n\t"
        "v_max_u32 v151, v151, v139\n\t"
        "v_xor_b32 v140, v181, v140\n\t"
        "v_mfma_f32_16x16x32_bf16 v[72:75], a[72:75], v[88:91], v[72:75]\n\t"
        "ds_read_b128 v[84:87], v183 offset:0\n\t"
        "v_mfma_f32_16x16x32_bf16 v[76:79], a[76:79], v[88:91], v[76:79]\n\t"
        "v_mfma_f32_16x16x32_bf16 v[80:83], a[80:83], v[88:91], v[80:83]\n\t"
        "v_mfma_f32_16x16x32_bf16 v[72:75], a[108:111], v[92:95], v[72:75]\n\t"
        "ds_read_b128 v[88:91], v183 offset:64\n\t"
        "v_mfma_f32_16x16x32_bf16 v[76:79], a[112:115], v[92:95], v[76:79]\n\t"
        "buffer_store_dwordx4 v[116:119], v148, s[28:31], s55 offen sc1\n\t"
        "v_mfma_f32_16x16x32_bf16 v[80:83], a[116:119], v[92:95], v[80:83]\n\t"
        "ds_write_b64 v146, v[120:121] offset:8192\n\t"
        "v_mfma_f32_16x16x32_bf16 v[72:75], a[144:147], v[96:99], v[72:75]\n\t"
        "ds_read_b128 v[92:95], v183 offset:128\n\t"
        "v_add_u32 v183, s9, v180\n\t"
        "v_mfma_f32_16x16x32_bf16 v[76:79], a[148:151], v[96:99], v[76:79]\n\t"
        "v_max_u32 v183, v183, v140\n\t"
        "v_mfma_f32_16x16x32_bf16 v[80:83], a[152:155], v[96:99], v[80:83]\n\t"
        "ds_read_b128 v[96:99], v151 offset:0\n\t"
        "s_waitcnt lgkmcnt(5)\n\t"
        "v_mfma_f32_16x16x32_bf16 v[72:75], a[180:183], v[100:103], v[72:75]\n\t"
        "v_mfma_f32_16x16x32_bf16 v[76:79], a[184:187], v[100:103], v[76:79]\n\t"
        "v_mfma_f32_16x16x32_bf16 v[80:83], a[188:191], v[100:103], v[80:83]\n\t"
        "ds_read_b128 v[100:103], v151 offset:64\n\t"
        "v_mfma_f32_16x16x32_bf16 v[72:75], a[216:219], v[104:107], v[72:75]\n\t"
        "v_mfma_f32_16x16x32_bf16 v[76:79], a[220:223], v[104:107], v[76:79]\n\t"
        "v_mfma_f32_16x16x32_bf16 v[80:83], a[224:227], v[104:107], v[80:83]\n\t"
        "ds_read_b128 v[104:107], v151 offset:128\n\t"
        "v_mfma_f32_16x16x32_bf16 v[72:75], a[252:255], v[108:111], v[72:75]\n\t"
        "v_mfma_f32_16x16x32_bf16 v[76:79], v[188:191], v[108:111], v[76:79]\n\t"
        "v_mfma_f32_16x16x32_bf16 v[80:83], v[192:195], v[108:111], v[80:83]\n\t"
        "ds_read_b128 v[108:111], v183 offset:0\n\t"
        "v_mfma_f32_16x16x32_bf16 v[72:75], v[220:223], v[112:115], v[72:75]\n\t"
        "v_mfma_f32_16x16x32_bf16 v[76:79], v[224:227], v[112:115], v[76:79]\n\t"
        "v_mfma_f32_16x16x32_bf16 v[80:83], v[228:231], v[112:115], v[80:83]\n\t"
        "ds_read_b128 v[112:115], v183 offset:64\n\t"
        "s_waitcnt lgkmcnt(4)\n\t"
        "v_mfma_f32_16x16x32_bf16 v[72:75], a[24:27], v[84:87], v[72:75]\n\t"
        "v_mfma_f32_16x16x32_bf16 v[76:79], a[28:31], v[84:87], v[76:79]\n\t"
        "v_mfma_f32_16x16x32_bf16 v[80:83], a[32:35], v[84:87], v[80:83]\n\t"
        "ds_read_b128 v[84:87], v183 offset:128\n\t"
        "v_mfma_f32_16x16x32_bf16 v[80:83], a[68:71], v[88:91], v[80:83]\n\t"
        "v_mfma_f32_16x16x32_bf16 v[80:83], a[104:107], v[92:95], v[80:83]\n\t"
        "v_mfma_f32_16x16x32_bf16 v[80:83], a[140:143], v[96:99], v[80:83]\n\t"
        "s_waitcnt lgkmcnt(3)\n\t"
        "v_mfma_f32_16x16x32_bf16 v[80:83], a[176:179], v[100:103], v[80:83]\n\t"
        "v_mfma_f32_16x16x32_bf16 v[80:83], a[212:215], v[104:107], v[80:83]\n\t"
        "s_waitcnt lgkmcnt(2)\n\t"
        "v_mfma_f32_16x16x32_bf16 v[80:83], a[248:251], v[108:111], v[80:83]\n\t"
        "s_waitcnt lgkmcnt(1)\n\t"
        "v_mfma_f32_16x16x32_bf16 v[80:83], v[216:219], v[112:115], v[80:83]\n\t"
        "s_waitcnt lgkmcnt(0)\n\t"
        "v_mfma_f32_16x16x32_bf16 v[80:83], v[252:255], v[84:87], v[80:83]\n\t"
        "v_mfma_f32_16x16x32_bf16 v[72:75], a[60:63], v[88:91], v[72:75]\n\t"
        "v_mfma_f32_16x16x32_bf16 v[76:79], a[64:67], v[88:91], v[76:79]\n\t"
        "s_nop 5\n\t"
        "v_cvt_pk_bf16_f32 v120, v80, v81\n\t"
        "v_cvt_pk_bf16_f32 v121, v82, v83\n\t"
        "v_mfma_f32_16x16x32_bf16 v[72:75], a[96:99], v[92:95], v[72:75]\n\t"
        "ds_write_b64 v146, v[120:121] offset:10240\n\t"
        "v_mfma_f32_16x16x32_bf16 v[76:79], a[100:103], v[92:95], v[76:79]\n\t"
        "v_mfma_f32_16x16x32_bf16 v[72:75], a[132:135], v[96:99], v[72:75]\n\t"
        "v_mfma_f32_16x16x32_bf16 v[76:79], a[136:139], v[96:99], v[76:79]\n\t"
        "s_waitcnt lgkmcnt(0)\n\t"
        "s_barrier\n\t"
        "v_mfma_f32_16x16x32_bf16 v[72:75], a[168:171], v[100:103], v[72:75]\n\t"
        "ds_read_b128 v[124:127], v147 offset:8192\n\t"
        "v_readlane_b32 s55, v155, 51\n\t"
        "v_mfma_f32_16x16x32_bf16 v[76:79], a[172:175], v[100:103], v[76:79]\n\t"
        "v_mfma_f32_16x16x32_bf16 v[72:75], a[204:207], v[104:107], v[72:75]\n\t"
        "v_mfma_f32_16x16x32_bf16 v[76:79], a[208:211], v[104:107], v[76:79]\n\t"
        "v_mfma_f32_16x16x32_bf16 v[72:75], a[240:243], v[108:111], v[72:75]\n\t"
        "v_mfma_f32_16x16x32_bf16 v[76:79], a[244:247], v[108:111], v[76:79]\n\t"
        "v_mfma_f32_16x16x32_bf16 v[72:75], v[208:211], v[112:115], v[72:75]\n\t"
        "v_mfma_f32_16x16x32_bf16 v[76:79], v[212:215], v[112:115], v[76:79]\n\t"
        "v_mfma_f32_16x16x32_bf16 v[72:75], v[244:247], v[84:87], v[72:75]\n\t"
        "v_mfma_f32_16x16x32_bf16 v[76:79], v[248:251], v[84:87], v[76:79]\n\t"
        "s_nop 6\n\t"
        "v_cvt_pk_bf16_f32 v116, v72, v73\n\t"
        "v_cvt_pk_bf16_f32 v117, v74, v75\n\t"
        "v_cvt_pk_bf16_f32 v118, v76, v77\n\t"
        "v_cvt_pk_bf16_f32 v119, v78, v79\n\t"
        "s_nop 0\n\t"
        "v_permlane16_swap_b32 v116, v118\n\t"
        "v_permlane16_swap_b32 v117, v119\n\t"
        "buffer_store_dwordx4 v[116:119], v148, s[28:31], s55 offen  \n\t"
        "s_waitcnt lgkmcnt(0)\n\t"
        "buffer_store_dwordx4 v[124:127], v150, s[28:31], s52 offen  \n\t"
        "s_andn2_b32 s6, s6, 2\n\t"
        "s_cbranch_scc1 99f\n\t"
        "s_endpgm\n\t"
        ".Lx40%=:\n\t"
        "v_readlane_b32 s40, v155, 20\n\t"
        "v_readlane_b32 s43, v155, 8\n\t"
        "s_nop 0\n\t"
        "s_add_u32 m0, s40, s35\n\t"
        "s_nop 2\n\t"
        "buffer_load_dwordx4 v136, s[16:19], s43 offen  lds\n\t"
        "s_branch .Lx41%=\n\t"
        ".Lx42%=:\n\t"
        "v_readlane_b32 s40, v155, 21\n\t"
        "v_readlane_b32 s43, v155, 9\n\t"
        "s_nop 0\n\t"
        "s_add_u32 m0, s40, s35\n\t"
        "s_nop 2\n\t"
        "buffer_load_dwordx4 v137, s[16:19], s43 offen  lds\n\t"
        "v_readlane_b32 s40, v155, 22\n\t"
        "v_readlane_b32 s43, v155, 10\n\t"
        "s_nop 0\n\t"
        "s_add_u32 m0, s40, s35\n\t"
        "s_nop 2\n\t"
        "buffer_load_dwordx4 v136, s[16:19], s43 offen  lds\n\t"
        "s_branch .Lx43%=\n\t"
        ".Lx44%=:\n\t"
        "v_readlane_b32 s40, v155, 23\n\t"
        "v_readlane_b32 s43, v155, 11\n\t"
        "s_nop 0\n\t"
        "s_add_u32 m0, s40, s35\n\t"
        "s_nop 2\n\t"
        "buffer_load_dwordx4 v137, s[16:19], s43 offen  lds\n\t"
        "s_branch .Lx45%=\n\t"
        ".Lx46%=:\n\t"
        "v_lshlrev_b32_sdwa v157, 5, v161 dst_sel:DWORD dst_unused:UNUSED_PAD src0_sel:DWORD src1_sel:WORD_0\n\t"
        "v_lshlrev_b32_sdwa v158, 5, v161 dst_sel:DWORD dst_unused:UNUSED_PAD src0_sel:DWORD src1_sel:WORD_1\n\t"
        "v_or_b32 v157, v184, v157\n\t"
        "v_or_b32 v158, v184, v158\n\t"
        "v_xor_b32 v159, 32, v157\n\t"
        "v_xor_b32 v152, 32, v158\n\t"
        "ds_read_b64_tr_b16 v[60:61], v157 offset:0\n\t"
        "ds_read_b64_tr_b16 v[62:63], v158 offset:0\n\t"
        "ds_read_b64_tr_b16 v[64:65], v159 offset:0\n\t"
        "ds_read_b64_tr_b16 v[66:67], v152 offset:0\n\t"
        "ds_read_b64_tr_b16 v[68:69], v157 offset:64\n\t"
        "ds_read_b64_tr_b16 v[70:71], v158 offset:64\n\t"
        "ds_read_b64_tr_b16 v[72:73], v159 offset:64\n\t"
        "ds_read_b64_tr_b16 v[74:75], v152 offset:64\n\t"
        "ds_read_b64_tr_b16 v[76:77], v157 offset:128\n\t"
        "ds_read_b64_tr_b16 v[78:79], v158 offset:128\n\t"
        "ds_read_b64_tr_b16 v[80:81], v159 offset:128\n\t"
        "ds_read_b64_tr_b16 v[82:83], v152 offset:128\n\t"
        "v_perm_b32 v164, v163, v163, v168\n\t"
        "v_perm_b32 v165, v163, v163, v169\n\t"
        "v_perm_b32 v166, v163, v163, v170\n\t"
        "v_perm_b32 v167, v163, v163, v171\n\t"
        "s_waitcnt lgkmcnt(10)\n\t"
        "s_nop 0\n\t"
        "v_mfma_f32_16x16x32_bf16 v[48:51], v[60:63], v[164:167], v[48:51]\n\t"
        "s_waitcnt lgkmcnt(8)\n\t"
        "v_mfma_f32_16x16x32_bf16 v[52:55], v[64:67], v[164:167], v[52:55]\n\t"
        "s_waitcnt lgkmcnt(6)\n\t"
        "v_mfma_f32_16x16x32_bf16 v[56:59], v[68:71], v[164:167], v[56:59]\n\t"
        "s_waitcnt lgkmcnt(4)\n\t"
        "v_mfma_f32_16x16x32_bf16 v[116:119], v[72:75], v[164:167], v[116:119]\n\t"
        "s_waitcnt lgkmcnt(2)\n\t"
        "v_mfma_f32_16x16x32_bf16 v[120:123], v[76:79], v[164:167], v[120:123]\n\t"
        "s_waitcnt lgkmcnt(0)\n\t"
        "v_mfma_f32_16x16x32_bf16 v[124:127], v[80:83], v[164:167], v[124:127]\n\t"
        "s_nop 4\n\t"
        "s_branch .Lx47%=\n\t"
        ".Lx48%=:\n\t"
        "v_lshlrev_b32_sdwa v157, 5, v161 dst_sel:DWORD dst_unused:UNUSED_PAD src0_sel:DWORD src1_sel:WORD_0\n\t"
        "v_lshlrev_b32_sdwa v158, 5, v161 dst_sel:DWORD dst_unused:UNUSED_PAD src0_sel:DWORD src1_sel:WORD_1\n\t"
        "v_or_b32 v157, v184, v157\n\t"
        "v_or_b32 v158, v184, v158\n\t"
        "v_xor_b32 v159, 32, v157\n\t"
        "v_xor_b32 v152, 32, v158\n\t"
        "ds_read_b64_tr_b16 v[60:61], v157 offset:0\n\t"
        "ds_read_b64_tr_b16 v[62:63], v158 offset:0\n\t"
        "ds_read_b64_tr_b16 v[64:65], v159 offset:0\n\t"
        "ds_read_b64_tr_b16 v[66:67], v152 offset:0\n\t"
        "ds_read_b64_tr_b16 v[68:69], v157 offset:64\n\t"
        "ds_read_b64_tr_b16 v[70:71], v158 offset:64\n\t"
        "ds_read_b64_tr_b16 v[72:73], v159 offset:64\n\t"
        "ds_read_b64_tr_b16 v[74:75], v152 offset:64\n\t"
        "ds_read_b64_tr_b16 v[76:77], v157 offset:128\n\t"
        "ds_read_b64_tr_b16 v[78:79], v158 offset:128\n\t"
        "ds_read_b64_tr_b16 v[80:81], v159 offset:128\n\t"
        "ds_read_b64_tr_b16 v[82:83], v152 offset:128\n\t"
        "v_perm_b32 v164, v163, v163, v168\n\t"
        "v_perm_b32 v165, v163, v163, v169\n\t"
        "v_perm_b32 v166, v163, v163, v170\n\t"
        "v_perm_b32 v167, v163, v163, v171\n\t"
        "s_waitcnt lgkmcnt(10)\n\t"
        "s_nop 0\n\t"
        "v_mfma_f32_16x16x32_bf16 v[48:51], v[60:63], v[164:167], v[48:51]\n\t"
        "s_waitcnt lgkmcnt(8)\n\t"
        "v_mfma_f32_16x16x32_bf16 v[52:55], v[64:67], v[164:167], v[52:55]\n\t"
        "s_waitcnt lgkmcnt(6)\n\t"
        "v_mfma_f32_16x16x32_bf16 v[56:59], v[68:71], v[164:167], v[56:59]\n\t"
        "s_waitcnt lgkmcnt(4)\n\t"
        "v_mfma_f32_16x16x32_bf16 v[116:119], v[72:75], v[164:167], v[116:119]\n\t"
        "s_waitcnt lgkmcnt(2)\n\t"
        "v_mfma_f32_16x16x32_bf16 v[120:123], v[76:79], v[164:167], v[120:123]\n\t"
        "s_waitcnt lgkmcnt(0)\n\t"
        "v_mfma_f32_16x16x32_bf16 v[124:127], v[80:83], v[164:167], v[124:127]\n\t"
        "s_nop 4\n\t"
        "s_branch .Lx49%=\n\t"
        ".Lx50%=:\n\t"
        "v_mov_b32 v151, v185\n\t"
        "v_mov_b32 v183, v186\n\t"
        "s_min_u32 s8, s3, 64\n\t"
        "s_waitcnt lgkmcnt(0)\n\t"
        "30:\n\t"
        "ds_read_b128 v[160:163], v185\n\t"
        "s_waitcnt lgkmcnt(0)\n\t"
        "v_lshlrev_b32_sdwa v157, 5, v160 dst_sel:DWORD dst_unused:UNUSED_PAD src0_sel:DWORD src1_sel:WORD_0\n\t"
        "v_lshlrev_b32_sdwa v158, 5, v160 dst_sel:DWORD dst_unused:UNUSED_PAD src0_sel:DWORD src1_sel:WORD_1\n\t"
        "v_or_b32 v157, v184, v157\n\t"
        "v_or_b32 v158, v184, v158\n\t"
        "v_xor_b32 v159, 32, v157\n\t"
        "v_xor_b32 v152, 32, v158\n\t"
        "ds_read_b64_tr_b16 v[60:61], v157 offset:0\n\t"
        "ds_read_b64_tr_b16 v[62:63], v158 offset:0\n\t"
        "ds_read_b64_tr_b16 v[64:65], v159 offset:0\n\t"
        "ds_read_b64_tr_b16 v[66:67], v152 offset:0\n\t"
        "ds_read_b64_tr_b16 v[68:69], v157 offset:64\n\t"
        "ds_read_b64_tr_b16 v[70:71], v158 offset:64\n\t"
        "ds_read_b64_tr_b16 v[72:73], v159 offset:64\n\t"
        "ds_read_b64_tr_b16 v[74:75], v152 offset:64\n\t"
        "ds_read_b64_tr_b16 v[76:77], v157 offset:128\n\t"
        "ds_read_b64_tr_b16 v[78:79], v158 offset:128\n\t"
        "ds_read_b64_tr_b16 v[80:81], v159 offset:128\n\t"
        "ds_read_b64_tr_b16 v[82:83], v152 offset:128\n\t"
        "v_perm_b32 v164, v162, v162, v168\n\t"
        "v_perm_b32 v165, v162, v162, v169\n\t"
        "v_perm_b32 v166, v162, v162, v170\n\t"
        "v_perm_b32 v167, v162, v162, v171\n\t"
        "v_cmp_ne_u32 vcc, 0, v163\n\t"
        "s_waitcnt lgkmcnt(10)\n\t"
        "v_mfma_f32_16x16x32_bf16 v[48:51], v[60:63], v[164:167], 0\n\t"
        "s_waitcnt lgkmcnt(8)\n\t"
        "v_mfma_f32_16x16x32_bf16 v[52:55], v[64:67], v[164:167], 0\n\t"
        "s_waitcnt lgkmcnt(6)\n\t"
        "v_mfma_f32_16x16x32_bf16 v[56:59], v[68:71], v[164:167], 0\n\t"
        "s_waitcnt lgkmcnt(4)\n\t"
        "v_mfma_f32_16x16x32_bf16 v[116:119], v[72:75], v[164:167], 0\n\t"
        "s_waitcnt lgkmcnt(2)\n\t"
        "v_mfma_f32_16x16x32_bf16 v[120:123], v[76:79], v[164:167], 0\n\t"
        "s_waitcnt lgkmcnt(0)\n\t"
        "v_mfma_f32_16x16x32_bf16 v[124:127], v[80:83], v[164:167], 0\n\t"
        "s_cbranch_vccz 31f\n\t"
        "v_lshlrev_b32_sdwa v157, 5, v161 dst_sel:DWORD dst_unused:UNUSED_PAD src0_sel:DWORD src1_sel:WORD_0\n\t"
        "v_lshlrev_b32_sdwa v158, 5, v161 dst_sel:DWORD dst_unused:UNUSED_PAD src0_sel:DWORD src1_sel:WORD_1\n\t"
        "v_or_b32 v157, v184, v157\n\t"
        "v_or_b32 v158, v184, v158\n\t"
        "v_xor_b32 v159, 32, v157\n\t"
        "v_xor_b32 v152, 32, v158\n\t"
        "ds_read_b64_tr_b16 v[60:61], v157 offset:0\n\t"
        "ds_read_b64_tr_b16 v[62:63], v158 offset:0\n\t"
        "ds_read_b64_tr_b16 v[64:65], v159 offset:0\n\t"
        "ds_read_b64_tr_b16 v[66:67], v152 offset:0\n\t"
        "ds_read_b64_tr_b16 v[68:69], v157 offset:64\n\t"
        "ds_read_b64_tr_b16 v[70:71], v158 offset:64\n\t"
        "ds_read_b64_tr_b16 v[72:73], v159 offset:64\n\t"
        "ds_read_b64_tr_b16 v[74:75], v152 offset:64\n\t"
        "ds_read_b64_tr_b16 v[76:77], v157 offset:128\n\t"
        "ds_read_b64_tr_b16 v[78:79], v158 offset:128\n\t"
        "ds_read_b64_tr_b16 v[80:81], v159 offset:128\n\t"
        "ds_read_b64_tr_b16 v[82:83], v152 offset:128\n\t"
        "v_perm_b32 v164, v163, v163, v168\n\t"
        "v_perm_b32 v165, v163, v163, v169\n\t"
        "v_perm_b32 v166, v163, v163, v170\n\t"
        "v_perm_b32 v167, v163, v163, v171\n\t"
        "s_waitcnt lgkmcnt(10)\n\t"
        "s_nop 0\n\t"
        "v_mfma_f32_16x16x32_bf16 v[48:51], v[60:63], v[164:167], v[48:51]\n\t"
        "s_waitcnt lgkmcnt(8)\n\t"
        "v_mfma_f32_16x16x32_bf16 v[52:55], v[64:67], v[164:167], v[52:55]\n\t"
        "s_waitcnt lgkmcnt(6)\n\t"
        "v_mfma_f32_16x16x32_bf16 v[56:59], v[68:71], v[164:167], v[56:59]\n\t"
        "s_waitcnt lgkmcnt(4)\n\t"
        "v_mfma_f32_16x16x32_bf16 v[116:119], v[72:75], v[164:167], v[116:119]\n\t"
        "s_waitcnt lgkmcnt(2)\n\t"
        "v_mfma_f32_16x16x32_bf16 v[120:123], v[76:79], v[164:167], v[120:123]\n\t"
        "s_waitcnt lgkmcnt(0)\n\t"
        "v_mfma_f32_16x16x32_bf16 v[124:127], v[80:83], v[164:167], v[124:127]\n\t"
        "31:\n\t"
        "v_cvt_pk_bf16_f32 v48, v48, v49\n\t"
        "v_cvt_pk_bf16_f32 v49, v50, v51\n\t"
        "v_cvt_pk_bf16_f32 v52, v52, v53\n\t"
        "v_cvt_pk_bf16_f32 v53, v54, v55\n\t"
        "v_cvt_pk_bf16_f32 v56, v56, v57\n\t"
        "v_cvt_pk_bf16_f32 v57, v58, v59\n\t"
        "v_cvt_pk_bf16_f32 v116, v116, v117\n\t"
        "v_cvt_pk_bf16_f32 v117, v118, v119\n\t"
        "v_cvt_pk_bf16_f32 v120, v120, v121\n\t"
        "v_cvt_pk_bf16_f32 v121, v122, v123\n\t"
        "v_cvt_pk_bf16_f32 v124, v124, v125\n\t"
        "v_cvt_pk_bf16_f32 v125, v126, v127\n\t"
        "ds_write_b64 v186, v[48:49] offset:0\n\t"
        "ds_write_b64 v154, v[52:53] offset:0\n\t"
        "ds_write_b64 v186, v[56:57] offset:64\n\t"
        "ds_write_b64 v154, v[116:117] offset:64\n\t"
        "ds_write_b64 v186, v[120:121] offset:128\n\t"
        "ds_write_b64 v154, v[124:125] offset:128\n\t"
        "v_add_u32 v185, 256, v185\n\t"
        "v_add_u32 v186, 12288, v186\n\t"
        "v_xor_b32 v154, 32, v186\n\t"
        "s_sub_i32 s8, s8, 16\n\t"
        "s_cmp_gt_i32 s8, 0\n\t"
        "s_cbranch_scc1 30b\n\t"
        "v_mov_b32 v185, v151\n\t"
        "v_mov_b32 v186, v183\n\t"
        "v_xor_b32 v154, 32, v186\n\t"
        "s_waitcnt lgkmcnt(0)\n\t"
        "s_barrier\n\t"
        "s_branch .Lx51%=\n\t"
        ".Lx52%=:\n\t"
        "v_lshlrev_b32_sdwa v157, 5, v161 dst_sel:DWORD dst_unused:UNUSED_PAD src0_sel:DWORD src1_sel:WORD_0\n\t"
        "v_lshlrev_b32_sdwa v158, 5, v161 dst_sel:DWORD dst_unused:UNUSED_PAD src0_sel:DWORD src1_sel:WORD_1\n\t"
        "v_or_b32 v157, v184, v157\n\t"
        "v_or_b32 v158, v184, v158\n\t"
        "v_xor_b32 v159, 32, v157\n\t"
        "v_xor_b32 v152, 32, v158\n\t"
        "ds_read_b64_tr_b16 v[60:61], v157 offset:0\n\t"
        "ds_read_b64_tr_b16 v[62:63], v158 offset:0\n\t"
        "ds_read_b64_tr_b16 v[64:65], v159 offset:0\n\t"
        "ds_read_b64_tr_b16 v[66:67], v152 offset:0\n\t"
        "ds_read_b64_tr_b16 v[68:69], v157 offset:64\n\t"
        "ds_read_b64_tr_b16 v[70:71], v158 offset:64\n\t"
        "ds_read_b64_tr_b16 v[72:73], v159 offset:64\n\t"
        "ds_read_b64_tr_b16 v[74:75], v152 offset:64\n\t"
        "ds_read_b64_tr_b16 v[76:77], v157 offset:128\n\t"
        "ds_read_b64_tr_b16 v[78:79], v158 offset:128\n\t"
        "ds_read_b64_tr_b16 v[80:81], v159 offset:128\n\t"
        "ds_read_b64_tr_b16 v[82:83], v152 offset:128\n\t"
        "v_perm_b32 v164, v163, v163, v168\n\t"
        "v_perm_b32 v165, v163, v163, v169\n\t"
        "v_perm_b32 v166, v163, v163, v170\n\t"
        "v_perm_b32 v167, v163, v163, v171\n\t"
        "s_waitcnt lgkmcnt(10)\n\t"
        "s_nop 0\n\t"
        "v_mfma_f32_16x16x32_bf16 v[0:3], v[60:63], v[164:167], v[0:3]\n\t"
        "s_waitcnt lgkmcnt(8)\n\t"
        "v_mfma_f32_16x16x32_bf16 v[4:7], v[64:67], v[164:167], v[4:7]\n\t"
        "s_waitcnt lgkmcnt(6)\n\t"
        "v_mfma_f32_16x16x32_bf16 v[8:11], v[68:71], v[164:167], v[8:11]\n\t"
        "s_waitcnt lgkmcnt(4)\n\t"
        "v_mfma_f32_16x16x32_bf16 v[12:15], v[72:75], v[164:167], v[12:15]\n\t"
        "s_waitcnt lgkmcnt(2)\n\t"
        "v_mfma_f32_16x16x32_bf16 v[16:19], v[76:79], v[164:167], v[16:19]\n\t"
        "s_waitcnt lgkmcnt(0)\n\t"
        "v_mfma_f32_16x16x32_bf16 v[20:23], v[80:83], v[164:167], v[20:23]\n\t"
        "s_nop 4\n\t"
        "s_branch .Lx53%=\n\t"
        ".Lx54%=:\n\t"
        "v_add_u32 v185, 768, v185\n\t"
        "v_add_u32 v186, 36864, v186\n\t"
        "v_xor_b32 v154, 32, v186\n\t"
        "s_min_u32 s47, s61, 64\n\t"
        "s_sub_u32 s47, s47, 48\n\t"
        "s_waitcnt lgkmcnt(0)\n\t"
        "40:\n\t"
        "ds_read_b128 v[160:163], v185\n\t"
        "s_waitcnt lgkmcnt(0)\n\t"
        "v_lshlrev_b32_sdwa v157, 5, v160 dst_sel:DWORD dst_unused:UNUSED_PAD src0_sel:DWORD src1_sel:WORD_0\n\t"
        "v_lshlrev_b32_sdwa v158, 5, v160 dst_sel:DWORD dst_unused:UNUSED_PAD src0_sel:DWORD src1_sel:WORD_1\n\t"
        "v_or_b32 v157, v184, v157\n\t"
        "v_or_b32 v158, v184, v158\n\t"
        "v_xor_b32 v159, 32, v157\n\t"
        "v_xor_b32 v152, 32, v158\n\t"
        "ds_read_b64_tr_b16 v[60:61], v157 offset:0\n\t"
        "ds_read_b64_tr_b16 v[62:63], v158 offset:0\n\t"
        "ds_read_b64_tr_b16 v[64:65], v159 offset:0\n\t"
        "ds_read_b64_tr_b16 v[66:67], v152 offset:0\n\t"
        "ds_read_b64_tr_b16 v[68:69], v157 offset:64\n\t"
        "ds_read_b64_tr_b16 v[70:71], v158 offset:64\n\t"
        "ds_read_b64_tr_b16 v[72:73], v159 offset:64\n\t"
        "ds_read_b64_tr_b16 v[74:75], v152 offset:64\n\t"
        "ds_read_b64_tr_b16 v[76:77], v157 offset:128\n\t"
        "ds_read_b64_tr_b16 v[78:79], v158 offset:128\n\t"
        "ds_read_b64_tr_b16 v[80:81], v159 offset:128\n\t"
        "ds_read_b64_tr_b16 v[82:83], v152 offset:128\n\t"
        "v_perm_b32 v164, v162, v162, v168\n\t"
        "v_perm_b32 v165, v162, v162, v169\n\t"
        "v_perm_b32 v166, v162, v162, v170\n\t"
        "v_perm_b32 v167, v162, v162, v171\n\t"
        "v_cmp_ne_u32 vcc, 0, v163\n\t"
        "s_waitcnt lgkmcnt(10)\n\t"
        "v_mfma_f32_16x16x32_bf16 v[0:3], v[60:63], v[164:167], 0\n\t"
        "s_waitcnt lgkmcnt(8)\n\t"
        "v_mfma_f32_16x16x32_bf16 v[4:7], v[64:67], v[164:167], 0\n\t"
        "s_waitcnt lgkmcnt(6)\n\t"
        "v_mfma_f32_16x16x32_bf16 v[8:11], v[68:71], v[164:167], 0\n\t"
        "s_waitcnt lgkmcnt(4)\n\t"
        "v_mfma_f32_16x16x32_bf16 v[12:15], v[72:75], v[164:167], 0\n\t"
        "s_waitcnt lgkmcnt(2)\n\t"
        "v_mfma_f32_16x16x32_bf16 v[16:19], v[76:79], v[164:167], 0\n\t"
        "s_waitcnt lgkmcnt(0)\n\t"
        "v_mfma_f32_16x16x32_bf16 v[20:23], v[80:83], v[164:167], 0\n\t"
        "s_cbranch_vccz 41f\n\t"
        "v_lshlrev_b32_sdwa v157, 5, v161 dst_sel:DWORD dst_unused:UNUSED_PAD src0_sel:DWORD src1_sel:WORD_0\n\t"
        "v_lshlrev_b32_sdwa v158, 5, v161 dst_sel:DWORD dst_unused:UNUSED_PAD src0_sel:DWORD src1_sel:WORD_1\n\t"
        "v_or_b32 v157, v184, v157\n\t"
        "v_or_b32 v158, v184, v158\n\t"
        "v_xor_b32 v159, 32, v157\n\t"
        "v_xor_b32 v152, 32, v158\n\t"
        "ds_read_b64_tr_b16 v[60:61], v157 offset:0\n\t"
        "ds_read_b64_tr_b16 v[62:63], v158 offset:0\n\t"
        "ds_read_b64_tr_b16 v[64:65], v159 offset:0\n\t"
        "ds_read_b64_tr_b16 v[66:67], v152 offset:0\n\t"
        "ds_read_b64_tr_b16 v[68:69], v157 offset:64\n\t"
        "ds_read_b64_tr_b16 v[70:71], v158 offset:64\n\t"
        "ds_read_b64_tr_b16 v[72:73], v159 offset:64\n\t"
        "ds_read_b64_tr_b16 v[74:75], v152 offset:64\n\t"
        "ds_read_b64_tr_b16 v[76:77], v157 offset:128\n\t"
        "ds_read_b64_tr_b16 v[78:79], v158 offset:128\n\t"
        "ds_read_b64_tr_b16 v[80:81], v159 offset:128\n\t"
        "ds_read_b64_tr_b16 v[82:83], v152 offset:128\n\t"
        "v_perm_b32 v164, v163, v163, v168\n\t"
        "v_perm_b32 v165, v163, v163, v169\n\t"
        "v_perm_b32 v166, v163, v163, v170\n\t"
        "v_perm_b32 v167, v163, v163, v171\n\t"
        "s_waitcnt lgkmcnt(10)\n\t"
        "s_nop 0\n\t"
        "v_mfma_f32_16x16x32_bf16 v[0:3], v[60:63], v[164:167], v[0:3]\n\t"
        "s_waitcnt lgkmcnt(8)\n\t"
        "v_mfma_f32_16x16x32_bf16 v[4:7], v[64:67], v[164:167], v[4:7]\n\t"
        "s_waitcnt lgkmcnt(6)\n\t"
        "v_mfma_f32_16x16x32_bf16 v[8:11], v[68:71], v[164:167], v[8:11]\n\t"
        "s_waitcnt lgkmcnt(4)\n\t"
        "v_mfma_f32_16x16x32_bf16 v[12:15], v[72:75], v[164:167], v[12:15]\n\t"
        "s_waitcnt lgkmcnt(2)\n\t"
        "v_mfma_f32_16x16x32_bf16 v[16:19], v[76:79], v[164:167], v[16:19]\n\t"
        "s_waitcnt lgkmcnt(0)\n\t"
        "v_mfma_f32_16x16x32_bf16 v[20:23], v[80:83], v[164:167], v[20:23]\n\t"
        "41:\n\t"
        "v_cvt_pk_bf16_f32 v0, v0, v1\n\t"
        "v_cvt_pk_bf16_f32 v1, v2, v3\n\t"
        "v_cvt_pk_bf16_f32 v4, v4, v5\n\t"
        "v_cvt_pk_bf16_f32 v5, v6, v7\n\t"
        "v_cvt_pk_bf16_f32 v8, v8, v9\n\t"
        "v_cvt_pk_bf16_f32 v9, v10, v11\n\t"
        "v_cvt_pk_bf16_f32 v12, v12, v13\n\t"
        "v_cvt_pk_bf16_f32 v13, v14, v15\n\t"
        "v_cvt_pk_bf16_f32 v16, v16, v17\n\t"
        "v_cvt_pk_bf16_f32 v17, v18, v19\n\t"
        "v_cvt_pk_bf16_f32 v20, v20, v21\n\t"
        "v_cvt_pk_bf16_f32 v21, v22, v23\n\t"
        "ds_write_b64 v186, v[0:1] offset:0\n\t"
        "ds_write_b64 v154, v[4:5] offset:0\n\t"
        "ds_write_b64 v186, v[8:9] offset:64\n\t"
        "ds_write_b64 v154, v[12:13] offset:64\n\t"
        "ds_write_b64 v186, v[16:17] offset:128\n\t"
        "ds_write_b64 v154, v[20:21] offset:128\n\t"
        "v_add_u32 v185, 256, v185\n\t"
        "v_add_u32 v186, 12288, v186\n\t"
        "v_xor_b32 v154, 32, v186\n\t"
        "s_sub_i32 s47, s47, 16\n\t"
        "s_cmp_gt_i32 s47, 0\n\t"
        "s_cbranch_scc1 40b\n\t"
        "s_cmp_le_u32 s61, 64\n\t"
        "s_cbranch_scc1 52f\n\t"
        "v_bfe_u32 v64, v185, 4, 4\n\t"
        "v_add_u32 v60, 65, v64\n\t"
        "s_sub_u32 s8, s37, 12288\n\t"
        "v_mov_b32 v63, s62\n\t"
        "v_lshlrev_b32 v65, 3, v64\n\t"
        "v_mul_u32_u24 v64, 192, v64\n\t"
        "v_and_or_b32 v64, v65, 48, v64\n\t"
        "v_add_u32 v61, s8, v64\n\t"
        "v_subrev_u32 v186, 61440, v186\n\t"
        "v_xor_b32 v154, 32, v186\n\t"
        "v_min_u32 v16, s61, v60\n\t"
        "v_lshl_add_u32 v16, v16, 3, s60\n\t"
        "ds_read_b64 v[0:1], v16\n\t"
        "v_cmp_ge_u32 s[66:67], s61, v60\n\t"
        "s_mov_b32 s46, 0x07060302\n\t"
        "s_waitcnt lgkmcnt(0)\n\t"
        "v_and_b32 v16, 0xfffff800, v1\n\t"
        "ds_read_b128 v[2:5], v16 offset:1536\n\t"
        "v_lshlrev_b32 v10, 16, v0\n\t"
        "v_and_b32 v11, 0xffff0000, v0\n\t"
        "v_med3_f32 v10, v10, 0, s63\n\t"
        "v_med3_f32 v11, v11, 0, s63\n\t"
        "s_mov_b64 exec, s[66:67]\n\t"
        "ds_write_b32 v1, v61\n\t"
        "s_mov_b64 exec, -1\n\t"
        "v_perm_b32 v0, v11, v10, s46\n\t"
        "v_fract_f32 v12, v10\n\t"
        "v_fract_f32 v13, v11\n\t"
        "v_cvt_u32_f32 v14, v10\n\t"
        "v_cvt_u32_f32 v15, v11\n\t"
        "v_cmp_neq_f32 s[64:65], 0, v12\n\t"
        "v_cmp_neq_f32 s[44:45], 0, v13\n\t"
        "v_sub_f32 v16, 1.0, v12\n\t"
        "v_sub_f32 v17, 1.0, v13\n\t"
        "v_cvt_pk_bf16_f32 v17, v16, v17\n\t"
        "v_lshlrev_b32 v16, 16, v17\n\t"
        "v_and_b32 v17, 0xffff0000, v17\n\t"
        "v_mul_f32 v18, v16, v17\n\t"
        "v_mul_f32 v16, v16, v13\n\t"
        "v_mul_f32 v17, v12, v17\n\t"
        "v_mul_f32 v19, v12, v13\n\t"
        "v_cndmask_b32 v16, v17, v16, s[44:45]\n\t"
        "v_cndmask_b32 v17, 0, v17, s[44:45]\n\t"
        "v_cvt_pk_bf16_f32 v162, v18, v16\n\t"
        "v_cvt_pk_bf16_f32 v163, v17, v19\n\t"
        "s_waitcnt lgkmcnt(1)\n\t"
        "v_pk_max_u16 v16, v0, v2\n\t"
        "v_pk_min_u16 v16, v16, v3\n\t"
        "v_cmp_ne_u32 vcc, v16, v0\n\t"
        "v_sub_u32 v16, v15, v5\n\t"
        "v_mad_u32_u24 v17, v15, 6, v4\n\t"
        "v_mad_u32_u24 v17, v14, s62, v17\n\t"
        "s_and_b64 vcc, vcc, s[66:67]\n\t"
        "s_or_b64 s[56:57], s[56:57], vcc\n\t"
        "v_bfe_u32 v18, v16, 2, 1\n\t"
        "v_or_b32 v18, v18, v17\n\t"
        "v_add_u32 v16, 1, v16\n\t"
        "v_bfe_u32 v16, v16, 2, 1\n\t"
        "v_add3_u32 v16, v17, v16, 6\n\t"
        "v_cndmask_b32 v16, v18, v16, s[44:45]\n\t"
        "v_cndmask_b32 v17, 0, v63, s[64:65]\n\t"
        "v_add_u32 v19, v18, v17\n\t"
        "v_add_u32 v20, v16, v17\n\t"
        "v_cndmask_b32 v16, v19, v16, s[44:45]\n\t"
        "v_lshl_or_b32 v160, v16, 16, v18\n\t"
        "v_lshl_or_b32 v161, v20, 16, v19\n\t"
        "v_lshlrev_b32_sdwa v157, 5, v160 dst_sel:DWORD dst_unused:UNUSED_PAD src0_sel:DWORD src1_sel:WORD_0\n\t"
        "v_lshlrev_b32_sdwa v158, 5, v160 dst_sel:DWORD dst_unused:UNUSED_PAD src0_sel:DWORD src1_sel:WORD_1\n\t"
        "v_or_b32 v157, v184, v157\n\t"
        "v_or_b32 v158, v184, v158\n\t"
        "v_xor_b32 v159, 32, v157\n\t"
        "v_xor_b32 v152, 32, v158\n\t"
        "ds_read_b64_tr_b16 v[60:61], v157 offset:0\n\t"
        "ds_read_b64_tr_b16 v[62:63], v158 offset:0\n\t"
        "ds_read_b64_tr_b16 v[64:65], v159 offset:0\n\t"
        "ds_read_b64_tr_b16 v[66:67], v152 offset:0\n\t"
        "ds_read_b64_tr_b16 v[68:69], v157 offset:64\n\t"
        "ds_read_b64_tr_b16 v[70:71], v158 offset:64\n\t"
        "ds_read_b64_tr_b16 v[72:73], v159 offset:64\n\t"
        "ds_read_b64_tr_b16 v[74:75], v152 offset:64\n\t"
        "ds_read_b64_tr_b16 v[76:77], v157 offset:128\n\t"
        "ds_read_b64_tr_b16 v[78:79], v158 offset:128\n\t"
        "ds_read_b64_tr_b16 v[80:81], v159 offset:128\n\t"
        "ds_read_b64_tr_b16 v[82:83], v152 offset:128\n\t"
        "v_lshlrev_b32_sdwa v157, 5, v161 dst_sel:DWORD dst_unused:UNUSED_PAD src0_sel:DWORD src1_sel:WORD_0\n\t"
        "v_lshlrev_b32_sdwa v158, 5, v161 dst_sel:DWORD dst_unused:UNUSED_PAD src0_sel:DWORD src1_sel:WORD_1\n\t"
        "v_or_b32 v157, v184, v157\n\t"
        "v_or_b32 v158, v184, v158\n\t"
        "v_xor_b32 v159, 32, v157\n\t"
        "v_xor_b32 v152, 32, v158\n\t"
        "ds_read_b64_tr_b16 v[24:25], v157 offset:0\n\t"
        "ds_read_b64_tr_b16 v[26:27], v158 offset:0\n\t"
        "ds_read_b64_tr_b16 v[28:29], v159 offset:0\n\t"
        "ds_read_b64_tr_b16 v[30:31], v152 offset:0\n\t"
        "ds_read_b64_tr_b16 v[32:33], v157 offset:64\n\t"
        "ds_read_b64_tr_b16 v[34:35], v158 offset:64\n\t"
        "ds_read_b64_tr_b16 v[36:37], v159 offset:64\n\t"
        "ds_read_b64_tr_b16 v[38:39], v152 offset:64\n\t"
        "ds_read_b64_tr_b16 v[40:41], v157 offset:128\n\t"
        "ds_read_b64_tr_b16 v[42:43], v158 offset:128\n\t"
        "ds_read_b64_tr_b16 v[44:45], v159 offset:128\n\t"
        "ds_read_b64_tr_b16 v[46:47], v152 offset:128\n\t"
        "s_cmp_lg_u64 s[56:57], 0\n\t"
        "s_cselect_b32 s8, 1, 0\n\t"
        "v_mov_b32 v157, s8\n\t"
        "v_perm_b32 v164, v162, v162, v168\n\t"
        "v_perm_b32 v165, v162, v162, v169\n\t"
        "v_perm_b32 v166, v162, v162, v170\n\t"
        "v_perm_b32 v167, v162, v162, v171\n\t"
        "s_waitcnt lgkmcnt(12)\n\t"
        "s_nop 0\n\t"
        "v_mfma_f32_16x16x32_bf16 v[0:3], v[60:63], v[164:167], 0\n\t"
        "v_mfma_f32_16x16x32_bf16 v[4:7], v[64:67], v[164:167], 0\n\t"
        "v_mfma_f32_16x16x32_bf16 v[8:11], v[68:71], v[164:167], 0\n\t"
        "v_mfma_f32_16x16x32_bf16 v[12:15], v[72:75], v[164:167], 0\n\t"
        "v_mfma_f32_16x16x32_bf16 v[16:19], v[76:79], v[164:167], 0\n\t"
        "v_mfma_f32_16x16x32_bf16 v[20:23], v[80:83], v[164:167], 0\n\t"
        "v_perm_b32 v164, v163, v163, v168\n\t"
        "v_perm_b32 v165, v163, v163, v169\n\t"
        "v_perm_b32 v166, v163, v163, v170\n\t"
        "v_perm_b32 v167, v163, v163, v171\n\t"
        "s_waitcnt lgkmcnt(3)\n\t"
        "s_nop 0\n\t"
        "v_mfma_f32_16x16x32_bf16 v[0:3], v[24:27], v[164:167], v[0:3]\n\t"
        "v_mfma_f32_16x16x32_bf16 v[4:7], v[28:31], v[164:167], v[4:7]\n\t"
        "v_mfma_f32_16x16x32_bf16 v[8:11], v[32:35], v[164:167], v[8:11]\n\t"
        "v_mfma_f32_16x16x32_bf16 v[12:15], v[36:39], v[164:167], v[12:15]\n\t"
        "s_waitcnt lgkmcnt(0)\n\t"
        "v_mfma_f32_16x16x32_bf16 v[16:19], v[40:43], v[164:167], v[16:19]\n\t"
        "v_mfma_f32_16x16x32_bf16 v[20:23], v[44:47], v[164:167], v[20:23]\n\t"
        "ds_write_b32 v153, v157 offset:16\n\t"
        "s_nop 0\n\t"
        "v_cvt_pk_bf16_f32 v0, v0, v1\n\t"
        "v_cvt_pk_bf16_f32 v1, v2, v3\n\t"
        "v_cvt_pk_bf16_f32 v4, v4, v5\n\t"
        "v_cvt_pk_bf16_f32 v5, v6, v7\n\t"
        "v_cvt_pk_bf16_f32 v8, v8, v9\n\t"
        "v_cvt_pk_bf16_f32 v9, v10, v11\n\t"
        "v_cvt_pk_bf16_f32 v12, v12, v13\n\t"
        "v_cvt_pk_bf16_f32 v13, v14, v15\n\t"
        "v_cvt_pk_bf16_f32 v16, v16, v17\n\t"
        "v_cvt_pk_bf16_f32 v17, v18, v19\n\t"
        "v_cvt_pk_bf16_f32 v20, v20, v21\n\t"
        "v_cvt_pk_bf16_f32 v21, v22, v23\n\t"
        "ds_write_b64 v186, v[0:1] offset:0\n\t"
        "ds_write_b64 v154, v[4:5] offset:0\n\t"
        "ds_write_b64 v186, v[8:9] offset:64\n\t"
        "ds_write_b64 v154, v[12:13] offset:64\n\t"
        "ds_write_b64 v186, v[16:17] offset:128\n\t"
        "ds_write_b64 v154, v[20:21] offset:128\n\t"
        "52:\n\t"
        "s_branch .Lx55%=\n\t"
        ".Lx56%=:\n\t"
        "v_readlane_b32 s40, v155, 20\n\t"
        "v_readlane_b32 s43, v155, 8\n\t"
        "s_nop 0\n\t"
        "s_add_u32 m0, s40, s35\n\t"
        "s_nop 2\n\t"
        "buffer_load_dwordx4 v136, s[16:19], s43 offen  lds\n\t"
        "s_branch .Lx57%=\n\t"
        ".Lx58%=:\n\t"
        "v_lshlrev_b32_sdwa v157, 5, v161 dst_sel:DWORD dst_unused:UNUSED_PAD src0_sel:DWORD src1_sel:WORD_0\n\t"
        "v_lshlrev_b32_sdwa v158, 5, v161 dst_sel:DWORD dst_unused:UNUSED_PAD src0_sel:DWORD src1_sel:WORD_1\n\t"
        "v_or_b32 v157, v184, v157\n\t"
        "v_or_b32 v158, v184, v158\n\t"
        "v_xor_b32 v159, 32, v157\n\t"
        "v_xor_b32 v152, 32, v158\n\t"
        "ds_read_b64_tr_b16 v[60:61], v157 offset:0\n\t"
        "ds_read_b64_tr_b16 v[62:63], v158 offset:0\n\t"
        "ds_read_b64_tr_b16 v[64:65], v159 offset:0\n\t"
        "ds_read_b64_tr_b16 v[66:67], v152 offset:0\n\t"
        "ds_read_b64_tr_b16 v[68:69], v157 offset:64\n\t"
        "ds_read_b64_tr_b16 v[70:71], v158 offset:64\n\t"
        "ds_read_b64_tr_b16 v[72:73], v159 offset:64\n\t"
        "ds_read_b64_tr_b16 v[74:75], v152 offset:64\n\t"
        "ds_read_b64_tr_b16 v[76:77], v157 offset:128\n\t"
        "ds_read_b64_tr_b16 v[78:79], v158 offset:128\n\t"
        "ds_read_b64_tr_b16 v[80:81], v159 offset:128\n\t"
        "ds_read_b64_tr_b16 v[82:83], v152 offset:128\n\t"
        "v_perm_b32 v164, v163, v163, v168\n\t"
        "v_perm_b32 v165, v163, v163, v169\n\t"
        "v_perm_b32 v166, v163, v163, v170\n\t"
        "v_perm_b32 v167, v163, v163, v171\n\t"
        "s_waitcnt lgkmcnt(10)\n\t"
        "s_nop 0\n\t"
        "v_mfma_f32_16x16x32_bf16 v[48:51], v[60:63], v[164:167], v[48:51]\n\t"
        "s_waitcnt lgkmcnt(8)\n\t"
        "v_mfma_f32_16x16x32_bf16 v[52:55], v[64:67], v[164:167], v[52:55]\n\t"
        "s_waitcnt lgkmcnt(6)\n\t"
        "v_mfma_f32_16x16x32_bf16 v[56:59], v[68:71], v[164:167], v[56:59]\n\t"
        "s_waitcnt lgkmcnt(4)\n\t"
        "v_mfma_f32_16x16x32_bf16 v[116:119], v[72:75], v[164:167], v[116:119]\n\t"
        "s_waitcnt lgkmcnt(2)\n\t"
        "v_mfma_f32_16x16x32_bf16 v[120:123], v[76:79], v[164:167], v[120:123]\n\t"
        "s_waitcnt lgkmcnt(0)\n\t"
        "v_mfma_f32_16x16x32_bf16 v[124:127], v[80:83], v[164:167], v[124:127]\n\t"
        "s_nop 4\n\t"
        "s_branch .Lx59%=\n\t"
        ".Lx60%=:\n\t"
        "v_mov_b32 v151, v185\n\t"
        "v_mov_b32 v183, v186\n\t"
        "s_min_u32 s8, s3, 64\n\t"
        "s_waitcnt lgkmcnt(0)\n\t"
        "30:\n\t"
        "ds_read_b128 v[160:163], v185\n\t"
        "s_waitcnt lgkmcnt(0)\n\t"
        "v_lshlrev_b32_sdwa v157, 5, v160 dst_sel:DWORD dst_unused:UNUSED_PAD src0_sel:DWORD src1_sel:WORD_0\n\t"
        "v_lshlrev_b32_sdwa v158, 5, v160 dst_sel:DWORD dst_unused:UNUSED_PAD src0_sel:DWORD src1_sel:WORD_1\n\t"
        "v_or_b32 v157, v184, v157\n\t"
        "v_or_b32 v158, v184, v158\n\t"
        "v_xor_b32 v159, 32, v157\n\t"
        "v_xor_b32 v152, 32, v158\n\t"
        "ds_read_b64_tr_b16 v[60:61], v157 offset:0\n\t"
        "ds_read_b64_tr_b16 v[62:63], v158 offset:0\n\t"
        "ds_read_b64_tr_b16 v[64:65], v159 offset:0\n\t"
        "ds_read_b64_tr_b16 v[66:67], v152 offset:0\n\t"
        "ds_read_b64_tr_b16 v[68:69], v157 offset:64\n\t"
        "ds_read_b64_tr_b16 v[70:71], v158 offset:64\n\t"
        "ds_read_b64_tr_b16 v[72:73], v159 offset:64\n\t"
        "ds_read_b64_tr_b16 v[74:75], v152 offset:64\n\t"
        "ds_read_b64_tr_b16 v[76:77], v157 offset:128\n\t"
        "ds_read_b64_tr_b16 v[78:79], v158 offset:128\n\t"
        "ds_read_b64_tr_b16 v[80:81], v159 offset:128\n\t"
        "ds_read_b64_tr_b16 v[82:83], v152 offset:128\n\t"
        "v_perm_b32 v164, v162, v162, v168\n\t"
        "v_perm_b32 v165, v162, v162, v169\n\t"
        "v_perm_b32 v166, v162, v162, v170\n\t"
        "v_perm_b32 v167, v162, v162, v171\n\t"
        "v_cmp_ne_u32 vcc, 0, v163\n\t"
        "s_waitcnt lgkmcnt(10)\n\t"
        "v_mfma_f32_16x16x32_bf16 v[48:51], v[60:63], v[164:167], 0\n\t"
        "s_waitcnt lgkmcnt(8)\n\t"
        "v_mfma_f32_16x16x32_bf16 v[52:55], v[64:67], v[164:167], 0\n\t"
        "s_waitcnt lgkmcnt(6)\n\t"
        "v_mfma_f32_16x16x32_bf16 v[56:59], v[68:71], v[164:167], 0\n\t"
        "s_waitcnt lgkmcnt(4)\n\t"
        "v_mfma_f32_16x16x32_bf16 v[116:119], v[72:75], v[164:167], 0\n\t"
        "s_waitcnt lgkmcnt(2)\n\t"
        "v_mfma_f32_16x16x32_bf16 v[120:123], v[76:79], v[164:167], 0\n\t"
        "s_waitcnt lgkmcnt(0)\n\t"
        "v_mfma_f32_16x16x32_bf16 v[124:127], v[80:83], v[164:167], 0\n\t"
        "s_cbranch_vccz 31f\n\t"
        "v_lshlrev_b32_sdwa v157, 5, v161 dst_sel:DWORD dst_unused:UNUSED_PAD src0_sel:DWORD src1_sel:WORD_0\n\t"
        "v_lshlrev_b32_sdwa v158, 5, v161 dst_sel:DWORD dst_unused:UNUSED_PAD src0_sel:DWORD src1_sel:WORD_1\n\t"
        "v_or_b32 v157, v184, v157\n\t"
        "v_or_b32 v158, v184, v158\n\t"
        "v_xor_b32 v159, 32, v157\n\t"
        "v_xor_b32 v152, 32, v158\n\t"
        "ds_read_b64_tr_b16 v[60:61], v157 offset:0\n\t"
        "ds_read_b64_tr_b16 v[62:63], v158 offset:0\n\t"
        "ds_read_b64_tr_b16 v[64:65], v159 offset:0\n\t"
        "ds_read_b64_tr_b16 v[66:67], v152 offset:0\n\t"
        "ds_read_b64_tr_b16 v[68:69], v157 offset:64\n\t"
        "ds_read_b64_tr_b16 v[70:71], v158 offset:64\n\t"
        "ds_read_b64_tr_b16 v[72:73], v159 offset:64\n\t"
        "ds_read_b64_tr_b16 v[74:75], v152 offset:64\n\t"
        "ds_read_b64_tr_b16 v[76:77], v157 offset:128\n\t"
        "ds_read_b64_tr_b16 v[78:79], v158 offset:128\n\t"
        "ds_read_b64_tr_b16 v[80:81], v159 offset:128\n\t"
        "ds_read_b64_tr_b16 v[82:83], v152 offset:128\n\t"
        "v_perm_b32 v164, v163, v163, v168\n\t"
        "v_perm_b32 v165, v163, v163, v169\n\t"
        "v_perm_b32 v166, v163, v163, v170\n\t"
        "v_perm_b32 v167, v163, v163, v171\n\t"
        "s_waitcnt lgkmcnt(10)\n\t"
        "s_nop 0\n\t"
        "v_mfma_f32_16x16x32_bf16 v[48:51], v[60:63], v[164:167], v[48:51]\n\t"
        "s_waitcnt lgkmcnt(8)\n\t"
        "v_mfma_f32_16x16x32_bf16 v[52:55], v[64:67], v[164:167], v[52:55]\n\t"
        "s_waitcnt lgkmcnt(6)\n\t"
        "v_mfma_f32_16x16x32_bf16 v[56:59], v[68:71], v[164:167], v[56:59]\n\t"
        "s_waitcnt lgkmcnt(4)\n\t"
        "v_mfma_f32_16x16x32_bf16 v[116:119], v[72:75], v[164:167], v[116:119]\n\t"
        "s_waitcnt lgkmcnt(2)\n\t"
        "v_mfma_f32_16x16x32_bf16 v[120:123], v[76:79], v[164:167], v[120:123]\n\t"
        "s_waitcnt lgkmcnt(0)\n\t"
        "v_mfma_f32_16x16x32_bf16 v[124:127], v[80:83], v[164:167], v[124:127]\n\t"
        "31:\n\t"
        "v_cvt_pk_bf16_f32 v48, v48, v49\n\t"
        "v_cvt_pk_bf16_f32 v49, v50, v51\n\t"
        "v_cvt_pk_bf16_f32 v52, v52, v53\n\t"
        "v_cvt_pk_bf16_f32 v53, v54, v55\n\t"
        "v_cvt_pk_bf16_f32 v56, v56, v57\n\t"
        "v_cvt_pk_bf16_f32 v57, v58, v59\n\t"
        "v_cvt_pk_bf16_f32 v116, v116, v117\n\t"
        "v_cvt_pk_bf16_f32 v117, v118, v119\n\t"
        "v_cvt_pk_bf16_f32 v120, v120, v121\n\t"
        "v_cvt_pk_bf16_f32 v121, v122, v123\n\t"
        "v_cvt_pk_bf16_f32 v124, v124, v125\n\t"
        "v_cvt_pk_bf16_f32 v125, v126, v127\n\t"
        "ds_write_b64 v186, v[48:49] offset:0\n\t"
        "ds_write_b64 v154, v[52:53] offset:0\n\t"
        "ds_write_b64 v186, v[56:57] offset:64\n\t"
        "ds_write_b64 v154, v[116:117] offset:64\n\t"
        "ds_write_b64 v186, v[120:121] offset:128\n\t"
        "ds_write_b64 v154, v[124:125] offset:128\n\t"
        "v_add_u32 v185, 256, v185\n\t"
        "v_add_u32 v186, 12288, v186\n\t"
        "v_xor_b32 v154, 32, v186\n\t"
        "s_sub_i32 s8, s8, 16\n\t"
        "s_cmp_gt_i32 s8, 0\n\t"
        "s_cbranch_scc1 30b\n\t"
        "v_mov_b32 v185, v151\n\t"
        "v_mov_b32 v186, v183\n\t"
        "v_xor_b32 v154, 32, v186\n\t"
        "s_waitcnt lgkmcnt(0)\n\t"
        "s_barrier\n\t"
        "s_branch .Lx61%=\n\t"
        ".Lx62%=:\n\t"
        "v_readlane_b32 s40, v155, 20\n\t"
        "v_readlane_b32 s43, v155, 8\n\t"
        "s_nop 0\n\t"
        "s_add_u32 m0, s40, s35\n\t"
        "s_nop 2\n\t"
        "buffer_load_dwordx4 v136, s[16:19], s43 offen  lds\n\t"
        "s_branch .Lx63%=\n\t"
        ".Lx64%=:\n\t"
        "v_readlane_b32 s40, v155, 21\n\t"
        "v_readlane_b32 s43, v155, 9\n\t"
        "s_nop 0\n\t"
        "s_add_u32 m0, s40, s35\n\t"
        "s_nop 2\n\t"
        "buffer_load_dwordx4 v137, s[16:19], s43 offen  lds\n\t"
        "v_readlane_b32 s40, v155, 22\n\t"
        "v_readlane_b32 s43, v155, 10\n\t"
        "s_nop 0\n\t"
        "s_add_u32 m0, s40, s35\n\t"
        "s_nop 2\n\t"
        "buffer_load_dwordx4 v136, s[16:19], s43 offen  lds\n\t"
        "s_branch .Lx65%=\n\t"
        ".Lx66%=:\n\t"
        "v_readlane_b32 s40, v155, 23\n\t"
        "v_readlane_b32 s43, v155, 11\n\t"
        "s_nop 0\n\t"
        "s_add_u32 m0, s40, s35\n\t"
        "s_nop 2\n\t"
        "buffer_load_dwordx4 v137, s[16:19], s43 offen  lds\n\t"
        "s_branch .Lx67%=\n\t"
        ".Lx68%=:\n\t"
        "v_mov_b32 v151, v185\n\t"
        "v_mov_b32 v183, v186\n\t"
        "s_min_u32 s8, s3, 64\n\t"
        "s_waitcnt lgkmcnt(0)\n\t"
        "30:\n\t"
        "ds_read_b128 v[160:163], v185\n\t"
        "s_waitcnt lgkmcnt(0)\n\t"
        "v_lshlrev_b32_sdwa v157, 5, v160 dst_sel:DWORD dst_unused:UNUSED_PAD src0_sel:DWORD src1_sel:WORD_0\n\t"
        "v_lshlrev_b32_sdwa v158, 5, v160 dst_sel:DWORD dst_unused:UNUSED_PAD src0_sel:DWORD src1_sel:WORD_1\n\t"
        "v_or_b32 v157, v184, v157\n\t"
        "v_or_b32 v158, v184, v158\n\t"
        "v_xor_b32 v159, 32, v157\n\t"
        "v_xor_b32 v152, 32, v158\n\t"
        "ds_read_b64_tr_b16 v[60:61], v157 offset:0\n\t"
        "ds_read_b64_tr_b16 v[62:63], v158 offset:0\n\t"
        "ds_read_b64_tr_b16 v[64:65], v159 offset:0\n\t"
        "ds_read_b64_tr_b16 v[66:67], v152 offset:0\n\t"
        "ds_read_b64_tr_b16 v[68:69], v157 offset:64\n\t"
        "ds_read_b64_tr_b16 v[70:71], v158 offset:64\n\t"
        "ds_read_b64_tr_b16 v[72:73], v159 offset:64\n\t"
        "ds_read_b64_tr_b16 v[74:75], v152 offset:64\n\t"
        "ds_read_b64_tr_b16 v[76:77], v157 offset:128\n\t"
        "ds_read_b64_tr_b16 v[78:79], v158 offset:128\n\t"
        "ds_read_b64_tr_b16 v[80:81], v159 offset:128\n\t"
        "ds_read_b64_tr_b16 v[82:83], v152 offset:128\n\t"
        "v_perm_b32 v164, v162, v162, v168\n\t"
        "v_perm_b32 v165, v162, v162, v169\n\t"
        "v_perm_b32 v166, v162, v162, v170\n\t"
        "v_perm_b32 v167, v162, v162, v171\n\t"
        "v_cmp_ne_u32 vcc, 0, v163\n\t"
        "s_waitcnt lgkmcnt(10)\n\t"
        "v_mfma_f32_16x16x32_bf16 v[48:51], v[60:63], v[164:167], 0\n\t"
        "s_waitcnt lgkmcnt(8)\n\t"
        "v_mfma_f32_16x16x32_bf16 v[52:55], v[64:67], v[164:167], 0\n\t"
        "s_waitcnt lgkmcnt(6)\n\t"
        "v_mfma_f32_16x16x32_bf16 v[56:59], v[68:71], v[164:167], 0\n\t"
        "s_waitcnt lgkmcnt(4)\n\t"
        "v_mfma_f32_16x16x32_bf16 v[116:119], v[72:75], v[164:167], 0\n\t"
        "s_waitcnt lgkmcnt(2)\n\t"
        "v_mfma_f32_16x16x32_bf16 v[120:123], v[76:79], v[164:167], 0\n\t"
        "s_waitcnt lgkmcnt(0)\n\t"
        "v_mfma_f32_16x16x32_bf16 v[124:127], v[80:83], v[164:167], 0\n\t"
        "s_cbranch_vccz 31f\n\t"
        "v_lshlrev_b32_sdwa v157, 5, v161 dst_sel:DWORD dst_unused:UNUSED_PAD src0_sel:DWORD src1_sel:WORD_0\n\t"
        "v_lshlrev_b32_sdwa v158, 5, v161 dst_sel:DWORD dst_unused:UNUSED_PAD src0_sel:DWORD src1_sel:WORD_1\n\t"
        "v_or_b32 v157, v184, v157\n\t"
        "v_or_b32 v158, v184, v158\n\t"
        "v_xor_b32 v159, 32, v157\n\t"
        "v_xor_b32 v152, 32, v158\n\t"
        "ds_read_b64_tr_b16 v[60:61], v157 offset:0\n\t"
        "ds_read_b64_tr_b16 v[62:63], v158 offset:0\n\t"
        "ds_read_b64_tr_b16 v[64:65], v159 offset:0\n\t"
        "ds_read_b64_tr_b16 v[66:67], v152 offset:0\n\t"
        "ds_read_b64_tr_b16 v[68:69], v157 offset:64\n\t"
        "ds_read_b64_tr_b16 v[70:71], v158 offset:64\n\t"
        "ds_read_b64_tr_b16 v[72:73], v159 offset:64\n\t"
        "ds_read_b64_tr_b16 v[74:75], v152 offset:64\n\t"
        "ds_read_b64_tr_b16 v[76:77], v157 offset:128\n\t"
        "ds_read_b64_tr_b16 v[78:79], v158 offset:128\n\t"
        "ds_read_b64_tr_b16 v[80:81], v159 offset:128\n\t"
        "ds_read_b64_tr_b16 v[82:83], v152 offset:128\n\t"
        "v_perm_b32 v164, v163, v163, v168\n\t"
        "v_perm_b32 v165, v163, v163, v169\n\t"
        "v_perm_b32 v166, v163, v163, v170\n\t"
        "v_perm_b32 v167, v163, v163, v171\n\t"
        "s_waitcnt lgkmcnt(10)\n\t"
        "s_nop 0\n\t"
        "v_mfma_f32_16x16x32_bf16 v[48:51], v[60:63], v[164:167], v[48:51]\n\t"
        "s_waitcnt lgkmcnt(8)\n\t"
        "v_mfma_f32_16x16x32_bf16 v[52:55], v[64:67], v[164:167], v[52:55]\n\t"
        "s_waitcnt lgkmcnt(6)\n\t"
        "v_mfma_f32_16x16x32_bf16 v[56:59], v[68:71], v[164:167], v[56:59]\n\t"
        "s_waitcnt lgkmcnt(4)\n\t"
        "v_mfma_f32_16x16x32_bf16 v[116:119], v[72:75], v[164:167], v[116:119]\n\t"
        "s_waitcnt lgkmcnt(2)\n\t"
        "v_mfma_f32_16x16x32_bf16 v[120:123], v[76:79], v[164:167], v[120:123]\n\t"
        "s_waitcnt lgkmcnt(0)\n\t"
        "v_mfma_f32_16x16x32_bf16 v[124:127], v[80:83], v[164:167], v[124:127]\n\t"
        "31:\n\t"
        "v_cvt_pk_bf16_f32 v48, v48, v49\n\t"
        "v_cvt_pk_bf16_f32 v49, v50, v51\n\t"
        "v_cvt_pk_bf16_f32 v52, v52, v53\n\t"
        "v_cvt_pk_bf16_f32 v53, v54, v55\n\t"
        "v_cvt_pk_bf16_f32 v56, v56, v57\n\t"
        "v_cvt_pk_bf16_f32 v57, v58, v59\n\t"
        "v_cvt_pk_bf16_f32 v116, v116, v117\n\t"
        "v_cvt_pk_bf16_f32 v117, v118, v119\n\t"
        "v_cvt_pk_bf16_f32 v120, v120, v121\n\t"
        "v_cvt_pk_bf16_f32 v121, v122, v123\n\t"
        "v_cvt_pk_bf16_f32 v124, v124, v125\n\t"
        "v_cvt_pk_bf16_f32 v125, v126, v127\n\t"
        "ds_write_b64 v186, v[48:49] offset:0\n\t"
        "ds_write_b64 v154, v[52:53] offset:0\n\t"
        "ds_write_b64 v186, v[56:57] offset:64\n\t"
        "ds_write_b64 v154, v[116:117] offset:64\n\t"
        "ds_write_b64 v186, v[120:121] offset:128\n\t"
        "ds_write_b64 v154, v[124:125] offset:128\n\t"
        "v_add_u32 v185, 256, v185\n\t"
        "v_add_u32 v186, 12288, v186\n\t"
        "v_xor_b32 v154, 32, v186\n\t"
        "s_sub_i32 s8, s8, 16\n\t"
        "s_cmp_gt_i32 s8, 0\n\t"
        "s_cbranch_scc1 30b\n\t"
        "v_mov_b32 v185, v151\n\t"
        "v_mov_b32 v186, v183\n\t"
        "v_xor_b32 v154, 32, v186\n\t"
        "s_waitcnt lgkmcnt(0)\n\t"
        "s_barrier\n\t"
        "s_branch .Lx69%=\n\t"
        ".Lx70%=:\n\t"
        "v_add_u32 v185, 1024, v185\n\t"
        "v_add_u32 v186, 49152, v186\n\t"
        "v_xor_b32 v154, 32, v186\n\t"
        "v_bfe_u32 v64, v185, 4, 4\n\t"
        "v_add_u32 v60, 65, v64\n\t"
        "s_sub_u32 s8, s37, 12288\n\t"
        "v_mov_b32 v63, s62\n\t"
        "v_lshlrev_b32 v65, 3, v64\n\t"
        "v_mul_u32_u24 v64, 192, v64\n\t"
        "v_and_or_b32 v64, v65, 48, v64\n\t"
        "v_add_u32 v61, s8, v64\n\t"
        "v_subrev_u32 v186, 61440, v186\n\t"
        "v_xor_b32 v154, 32, v186\n\t"
        "v_min_u32 v16, s61, v60\n\t"
        "v_lshl_add_u32 v16, v16, 3, s60\n\t"
        "ds_read_b64 v[0:1], v16\n\t"
        "v_cmp_ge_u32 s[66:67], s61, v60\n\t"
        "s_mov_b32 s46, 0x07060302\n\t"
        "s_waitcnt lgkmcnt(0)\n\t"
        "v_and_b32 v16, 0xfffff800, v1\n\t"
        "ds_read_b128 v[2:5], v16 offset:1536\n\t"
        "v_lshlrev_b32 v10, 16, v0\n\t"
        "v_and_b32 v11, 0xffff0000, v0\n\t"
        "v_med3_f32 v10, v10, 0, s63\n\t"
        "v_med3_f32 v11, v11, 0, s63\n\t"
        "s_mov_b64 exec, s[66:67]\n\t"
        "ds_write_b32 v1, v61\n\t"
        "s_mov_b64 exec, -1\n\t"
        "v_perm_b32 v0, v11, v10, s46\n\t"
        "v_fract_f32 v12, v10\n\t"
        "v_fract_f32 v13, v11\n\t"
        "v_cvt_u32_f32 v14, v10\n\t"
        "v_cvt_u32_f32 v15, v11\n\t"
        "v_cmp_neq_f32 s[64:65], 0, v12\n\t"
        "v_cmp_neq_f32 s[44:45], 0, v13\n\t"
        "v_sub_f32 v16, 1.0, v12\n\t"
        "v_sub_f32 v17, 1.0, v13\n\t"
        "v_cvt_pk_bf16_f32 v17, v16, v17\n\t"
        "v_lshlrev_b32 v16, 16, v17\n\t"
        "v_and_b32 v17, 0xffff0000, v17\n\t"
        "v_mul_f32 v18, v16, v17\n\t"
        "v_mul_f32 v16, v16, v13\n\t"
        "v_mul_f32 v17, v12, v17\n\t"
        "v_mul_f32 v19, v12, v13\n\t"
        "v_cndmask_b32 v16, v17, v16, s[44:45]\n\t"
        "v_cndmask_b32 v17, 0, v17, s[44:45]\n\t"
        "v_cvt_pk_bf16_f32 v162, v18, v16\n\t"
        "v_cvt_pk_bf16_f32 v163, v17, v19\n\t"
        "s_waitcnt lgkmcnt(1)\n\t"
        "v_pk_max_u16 v16, v0, v2\n\t"
        "v_pk_min_u16 v16, v16, v3\n\t"
        "v_cmp_ne_u32 vcc, v16, v0\n\t"
        "v_sub_u32 v16, v15, v5\n\t"
        "v_mad_u32_u24 v17, v15, 6, v4\n\t"
        "v_mad_u32_u24 v17, v14, s62, v17\n\t"
        "s_and_b64 vcc, vcc, s[66:67]\n\t"
        "s_or_b64 s[56:57], s[56:57], vcc\n\t"
        "v_bfe_u32 v18, v16, 2, 1\n\t"
        "v_or_b32 v18, v18, v17\n\t"
        "v_add_u32 v16, 1, v16\n\t"
        "v_bfe_u32 v16, v16, 2, 1\n\t"
        "v_add3_u32 v16, v17, v16, 6\n\t"
        "v_cndmask_b32 v16, v18, v16, s[44:45]\n\t"
        "v_cndmask_b32 v17, 0, v63, s[64:65]\n\t"
        "v_add_u32 v19, v18, v17\n\t"
        "v_add_u32 v20, v16, v17\n\t"
        "v_cndmask_b32 v16, v19, v16, s[44:45]\n\t"
        "v_lshl_or_b32 v160, v16, 16, v18\n\t"
        "v_lshl_or_b32 v161, v20, 16, v19\n\t"
        "v_lshlrev_b32_sdwa v157, 5, v160 dst_sel:DWORD dst_unused:UNUSED_PAD src0_sel:DWORD src1_sel:WORD_0\n\t"
        "v_lshlrev_b32_sdwa v158, 5, v160 dst_sel:DWORD dst_unused:UNUSED_PAD src0_sel:DWORD src1_sel:WORD_1\n\t"
        "v_or_b32 v157, v184, v157\n\t"
        "v_or_b32 v158, v184, v158\n\t"
        "v_xor_b32 v159, 32, v157\n\t"
        "v_xor_b32 v152, 32, v158\n\t"
        "ds_read_b64_tr_b16 v[60:61], v157 offset:0\n\t"
        "ds_read_b64_tr_b16 v[62:63], v158 offset:0\n\t"
        "ds_read_b64_tr_b16 v[64:65], v159 offset:0\n\t"
        "ds_read_b64_tr_b16 v[66:67], v152 offset:0\n\t"
        "ds_read_b64_tr_b16 v[68:69], v157 offset:64\n\t"
        "ds_read_b64_tr_b16 v[70:71], v158 offset:64\n\t"
        "ds_read_b64_tr_b16 v[72:73], v159 offset:64\n\t"
        "ds_read_b64_tr_b16 v[74:75], v152 offset:64\n\t"
        "ds_read_b64_tr_b16 v[76:77], v157 offset:128\n\t"
        "ds_read_b64_tr_b16 v[78:79], v158 offset:128\n\t"
        "ds_read_b64_tr_b16 v[80:81], v159 offset:128\n\t"
        "ds_read_b64_tr_b16 v[82:83], v152 offset:128\n\t"
        "v_lshlrev_b32_sdwa v157, 5, v161 dst_sel:DWORD dst_unused:UNUSED_PAD src0_sel:DWORD src1_sel:WORD_0\n\t"
        "v_lshlrev_b32_sdwa v158, 5, v161 dst_sel:DWORD dst_unused:UNUSED_PAD src0_sel:DWORD src1_sel:WORD_1\n\t"
        "v_or_b32 v157, v184, v157\n\t"
        "v_or_b32 v158, v184, v158\n\t"
        "v_xor_b32 v159, 32, v157\n\t"
        "v_xor_b32 v152, 32, v158\n\t"
        "ds_read_b64_tr_b16 v[24:25], v157 offset:0\n\t"
        "ds_read_b64_tr_b16 v[26:27], v158 offset:0\n\t"
        "ds_read_b64_tr_b16 v[28:29], v159 offset:0\n\t"
        "ds_read_b64_tr_b16 v[30:31], v152 offset:0\n\t"
        "ds_read_b64_tr_b16 v[32:33], v157 offset:64\n\t"
        "ds_read_b64_tr_b16 v[34:35], v158 offset:64\n\t"
        "ds_read_b64_tr_b16 v[36:37], v159 offset:64\n\t"
        "ds_read_b64_tr_b16 v[38:39], v152 offset:64\n\t"
        "ds_read_b64_tr_b16 v[40:41], v157 offset:128\n\t"
        "ds_read_b64_tr_b16 v[42:43], v158 offset:128\n\t"
        "ds_read_b64_tr_b16 v[44:45], v159 offset:128\n\t"
        "ds_read_b64_tr_b16 v[46:47], v152 offset:128\n\t"
        "s_cmp_lg_u64 s[56:57], 0\n\t"
        "s_cselect_b32 s8, 1, 0\n\t"
        "v_mov_b32 v157, s8\n\t"
        "v_perm_b32 v164, v162, v162, v168\n\t"
        "v_perm_b32 v165, v162, v162, v169\n\t"
        "v_perm_b32 v166, v162, v162, v170\n\t"
        "v_perm_b32 v167, v162, v162, v171\n\t"
        "s_waitcnt lgkmcnt(12)\n\t"
        "s_nop 0\n\t"
        "v_mfma_f32_16x16x32_bf16 v[0:3], v[60:63], v[164:167], 0\n\t"
        "v_mfma_f32_16x16x32_bf16 v[4:7], v[64:67], v[164:167], 0\n\t"
        "v_mfma_f32_16x16x32_bf16 v[8:11], v[68:71], v[164:167], 0\n\t"
        "v_mfma_f32_16x16x32_bf16 v[12:15], v[72:75], v[164:167], 0\n\t"
        "v_mfma_f32_16x16x32_bf16 v[16:19], v[76:79], v[164:167], 0\n\t"
        "v_mfma_f32_16x16x32_bf16 v[20:23], v[80:83], v[164:167], 0\n\t"
        "v_perm_b32 v164, v163, v163, v168\n\t"
        "v_perm_b32 v165, v163, v163, v169\n\t"
        "v_perm_b32 v166, v163, v163, v170\n\t"
        "v_perm_b32 v167, v163, v163, v171\n\t"
        "s_waitcnt lgkmcnt(3)\n\t"
        "s_nop 0\n\t"
        "v_mfma_f32_16x16x32_bf16 v[0:3], v[24:27], v[164:167], v[0:3]\n\t"
        "v_mfma_f32_16x16x32_bf16 v[4:7], v[28:31], v[164:167], v[4:7]\n\t"
        "v_mfma_f32_16x16x32_bf16 v[8:11], v[32:35], v[164:167], v[8:11]\n\t"
        "v_mfma_f32_16x16x32_bf16 v[12:15], v[36:39], v[164:167], v[12:15]\n\t"
        "s_waitcnt lgkmcnt(0)\n\t"
        "v_mfma_f32_16x16x32_bf16 v[16:19], v[40:43], v[164:167], v[16:19]\n\t"
        "v_mfma_f32_16x16x32_bf16 v[20:23], v[44:47], v[164:167], v[20:23]\n\t"
        "ds_write_b32 v153, v157 offset:16\n\t"
        "s_nop 0\n\t"
        "v_cvt_pk_bf16_f32 v0, v0, v1\n\t"
        "v_cvt_pk_bf16_f32 v1, v2, v3\n\t"
        "v_cvt_pk_bf16_f32 v4, v4, v5\n\t"
        "v_cvt_pk_bf16_f32 v5, v6, v7\n\t"
        "v_cvt_pk_bf16_f32 v8, v8, v9\n\t"
        "v_cvt_pk_bf16_f32 v9, v10, v11\n\t"
        "v_cvt_pk_bf16_f32 v12, v12, v13\n\t"
        "v_cvt_pk_bf16_f32 v13, v14, v15\n\t"
        "v_cvt_pk_bf16_f32 v16, v16, v17\n\t"
        "v_cvt_pk_bf16_f32 v17, v18, v19\n\t"
        "v_cvt_pk_bf16_f32 v20, v20, v21\n\t"
        "v_cvt_pk_bf16_f32 v21, v22, v23\n\t"
        "ds_write_b64 v186, v[0:1] offset:0\n\t"
        "ds_write_b64 v154, v[4:5] offset:0\n\t"
        "ds_write_b64 v186, v[8:9] offset:64\n\t"
        "ds_write_b64 v154, v[12:13] offset:64\n\t"
        "ds_write_b64 v186, v[16:17] offset:128\n\t"
        "ds_write_b64 v154, v[20:21] offset:128\n\t"
        "s_branch .Lx71%=\n\t"
        "99:\n\t"
        "s_nop 7\n\t"
        "s_waitcnt vmcnt(0) lgkmcnt(0)\n\t"
        "s_barrier\n\t"
        "s_lshl_b32 s6, s6, 16\n\t"
        "v_or_b32 %0, s6, v156\n\t"
        : "+v"(tid) : "s"(ka), "s"(bid), "s"(recs)
        : "memory", "scc", "vcc", "s3", "s4", "s5", "s6", "s7", "s8", "s9", "s10", "s11", "s12", "s13", "s16", "s17", "s18", "s19", "s20", "s21", "s22", "s23", "s24", "s25", "s26", "s27", "s28", "s29", "s30", "s31", "s34", "s35", "s36", "s37", "s38", "s39", "s40", "s41", "s42", "s43", "s44", "s45", "s46", "s47", "s48", "s49", "s50", "s51", "s52", "s53", "s54", "s55", "s56", "s57", "s60", "s61", "s62", "s63", "s64", "s65", "s66", "s67", "s68", "s69", "s70", "s71", "s72", "s73", "s74", "s75", "s76", "s77", "s78", "s79", "s80", "s81", "s82", "s83", "s84", "s85", "s86", "s87", "s88", "s89", "s90", "s91", "s92", "s93", "s94", "s95", "s96", "s97", "s98", "s99", "v1", "v2", "v3", "v4", "v5", "v6", "v7", "v8", "v9", "v10", "v11", "v12", "v13", "v14", "v15", "v16", "v17", "v18", "v19", "v20", "v21", "v22", "v23", "v24", "v25", "v26", "v27", "v28", "v29", "v30", "v31", "v32", "v33", "v34", "v35", "v36", "v37", "v38", "v39", "v40", "v41", "v42", "v43", "v44", "v45", "v46", "v47", "v48", "v49", "v50", "v51", "v52", "v53", "v54", "v55", "v56", "v57", "v58", "v59", "v60", "v61", "v62", "v63", "v64", "v65", "v66", "v67", "v68", "v69", "v70", "v71", "v72", "v73", "v74", "v75", "v76", "v77", "v78", "v79", "v80", "v81", "v82", "v83", "v84", "v85", "v86", "v87", "v88", "v89", "v90", "v91", "v92", "v93", "v94", "v95", "v96", "v97", "v98", "v99", "v100", "v101", "v102", "v103", "v104", "v105", "v106", "v107", "v108", "v109", "v110", "v111", "v112", "v113", "v114", "v115", "v116", "v117", "v118", "v119", "v120", "v121", "v122", "v123", "v124", "v125", "v126", "v127", "v128", "v129", "v130", "v131", "v132", "v133", "v134", "v135", "v136", "v137", "v138", "v139", "v140", "v141", "v142", "v143", "v144", "v145", "v146", "v147", "v148", "v149", "v150", "v151", "v152", "v153", "v154", "v155", "v156", "v157", "v158", "v159", "v160", "v161", "v162", "v163", "v164", "v165", "v166", "v167", "v168", "v169", "v170", "v171", "v172", "v173", "v174", "v175", "v176", "v177", "v178", "v179", "v180", "v181", "v182", "v183", "v184", "v185", "v186", "v187", "v188", "v189", "v190", "v191", "v192", "v193", "v194", "v195", "v196", "v197", "v198", "v199", "v200", "v201", "v202", "v203", "v204", "v205", "v206", "v207", "v208", "v209", "v210", "v211", "v212", "v213", "v214", "v215", "v216", "v217", "v218", "v219", "v220", "v221", "v222", "v223", "v224", "v225", "v226", "v227", "v228", "v229", "v230", "v231", "v232", "v233", "v234", "v235", "v236", "v237", "v238", "v239", "v240", "v241", "v242", "v243", "v244", "v245", "v246", "v247", "v248", "v249", "v250", "v251", "v252", "v253", "v254", "v255", "a0", "a1", "a2", "a3", "a4", "a5", "a6", "a7", "a8", "a9", "a10", "a11", "a12", "a13", "a14", "a15", "a16", "a17", "a18", "a19", "a20", "a21", "a22", "a23", "a24", "a25", "a26", "a27", "a28", "a29", "a30", "a31", "a32", "a33", "a34", "a35", "a36", "a37", "a38", "a39", "a40", "a41", "a42", "a43", "a44", "a45", "a46", "a47", "a48", "a49", "a50", "a51", "a52", "a53", "a54", "a55", "a56", "a57", "a58", "a59", "a60", "a61", "a62", "a63", "a64", "a65", "a66", "a67", "a68", "a69", "a70", "a71", "a72", "a73", "a74", "a75", "a76", "a77", "a78", "a79", "a80", "a81", "a82", "a83", "a84", "a85", "a86", "a87", "a88", "a89", "a90", "a91", "a92", "a93", "a94", "a95", "a96", "a97", "a98", "a99", "a100", "a101", "a102", "a103", "a104", "a105", "a106", "a107", "a108", "a109", "a110", "a111", "a112", "a113", "a114", "a115", "a116", "a117", "a118", "a119", "a120", "a121", "a122", "a123", "a124", "a125", "a126", "a127", "a128", "a129", "a130", "a131", "a132", "a133", "a134", "a135", "a136", "a137", "a138", "a139", "a140", "a141", "a142", "a143", "a144", "a145", "a146", "a147", "a148", "a149", "a150", "a151", "a152", "a153", "a154", "a155", "a156", "a157", "a158", "a159", "a160", "a161", "a162", "a163", "a164", "a165", "a166", "a167", "a168", "a169", "a170", "a171", "a172", "a173", "a174", "a175", "a176", "a177", "a178", "a179", "a180", "a181", "a182", "a183", "a184", "a185", "a186", "a187", "a188", "a189", "a190", "a191", "a192", "a193", "a194", "a195", "a196", "a197", "a198", "a199", "a200", "a201", "a202", "a203", "a204", "a205", "a206", "a207", "a208", "a209", "a210", "a211", "a212", "a213", "a214", "a215", "a216", "a217", "a218", "a219", "a220", "a221", "a222", "a223", "a224", "a225", "a226", "a227", "a228", "a229", "a230", "a231", "a232", "a233", "a234", "a235", "a236", "a237", "a238", "a239", "a240", "a241", "a242", "a243", "a244", "a245", "a246", "a247", "a248", "a249", "a250", "a251", "a252", "a253", "a254", "a255");
    asm volatile("" : "+s"(ka), "+s"(bid) :: "memory");
    {
        typedef short v4s __attribute__((ext_vector_type(4)));
        const unsigned short *x = ka->x, *w = ka->w;
        const unsigned* off = (const unsigned*)ka->off;
        unsigned short* out = ka->out;
        const unsigned tw = recs[bid * 64 + 27] >> 16, redo = __builtin_amdgcn_readfirstlane(tid >> 16);
        const unsigned* rc = recs + bid * 64;
        tid &= 255;
        const int lane = tid & 63, li = lane & 15, q = lane >> 4, wv = __builtin_amdgcn_readfirstlane(tid >> 6);
        int pix[4] = {-1, -1, -1, -1}, n = 0;
#pragma unroll 1
        for (int s = 0; s < 14; ++s) {
            const unsigned o = s < 8 ? (((tw & 255) + s) * 224 + ((tw >> 8) & 255)) * 192 : rc[49 + ((s - 8) >> 1)] + (s & 1) * (43200 - rc[30]);
            if (!((redo >> (s < 8 ? 8 + (s >> 1) : 0)) & 1) || o >= 224 * 224 * 192) continue;
            const int p = (o + li * (s < 8 ? 192 : rc[30])) / 192;
#pragma unroll
            for (int i = 0; i < 4; ++i) pix[i] = n == wv + 4 * i ? p : pix[i];
            ++n;
        }
        f32x4 acc[4][6];
        unsigned od[4];
        u32x4 wn[5];
#pragma unroll
        for (int i = 0; i < 4; ++i) {
            od[i] = off[max(pix[i], 0) * 9];
#pragma unroll
            for (int k = 0; k < 6; ++k) acc[i][k] = (f32x4){0.f, 0.f, 0.f, 0.f};
        }
#pragma unroll
        for (int k = 0; k < 5; ++k) wn[k] = *(const u32x4*)(w + min(tid + 256 * k, 1151u) * 8);
        const __amdgpu_buffer_rsrc_t xr = __builtin_amdgcn_make_buffer_rsrc((void*)x, 0, 224 * 224 * 192, 0x00020000);
        const unsigned wb = (8 * q + (li >> 2)) * 192 + (li & 3) * 8;
#pragma unroll 1
        for (int t = 0; t < 9; ++t) {
            unsigned char* slab = lds + (t & 1) * 18432;
#pragma unroll
            for (int k = 0; k < 5; ++k) {
                const unsigned c = min(tid + 256 * k, 1151u);
                *(u32x4*)(slab + ((c * 16) ^ (((c / 96) & 1) << 5))) = wn[k];
            }
            __syncthreads();
            u32x4 wf[3][6];
#pragma unroll
            for (int k = 0; k < 18; ++k) {
                __attribute__((address_space(3))) unsigned char* a = (__attribute__((address_space(3))) unsigned char*)slab + (k / 6) * 6144 + wb + (((k % 6) * 32) ^ ((q & 1) << 5));
                const u32x2 lo = __builtin_bit_cast(u32x2, __builtin_amdgcn_ds_read_tr16_b64_v4i16((__attribute__((address_space(3))) v4s*)a));
                const u32x2 hi = __builtin_bit_cast(u32x2, __builtin_amdgcn_ds_read_tr16_b64_v4i16((__attribute__((address_space(3))) v4s*)(a + 768)));
                wf[k / 6][k % 6] = (u32x4){lo[0], lo[1], hi[0], hi[1]};
            }
            const int tn = min(t + 1, 8);
#pragma unroll
            for (int k = 0; k < 5; ++k) wn[k] = *(const u32x4*)(w + tn * 9216 + min(tid + 256 * k, 1151u) * 8);
#pragma unroll
            for (int i0 = 0; i0 < 4; i0 += 2) {
                if (wv + 4 * i0 >= n) continue;
                u32x4 g[2][3][4];
                float wt[2][4];
#pragma unroll
                for (int j = 0; j < 2; ++j) {
                    const int i = i0 + j, p = max(pix[i], 0), h = p / 224, c = p - h * 224;
                    float py = rb((float)(h + t / 3 - 1) + bf_lo(od[i])), px = rb((float)(c + t % 3 - 1) + bf_hi(od[i]));
                    od[i] = off[p * 9 + tn];
                    py = fminf(fmaxf(py, 0.f), 223.f); px = fminf(fmaxf(px, 0.f), 223.f);
                    const float y0 = floorf(py), x0 = floorf(px), y1 = fminf(y0 + 1.f, 223.f), x1 = fminf(x0 + 1.f, 223.f);
                    const float wy = py - y0, wx = px - x0, a = rb(1.f - wy), b = rb(1.f - wx);
                    wt[j][0] = rb(a * b); wt[j][1] = rb(a * wx); wt[j][2] = rb(wy * b); wt[j][3] = rb(wy * wx);
                    int ad[4] = {(int)y0 * 224 + (int)x0, (int)y0 * 224 + (int)x1, (int)y1 * 224 + (int)x0, (int)y1 * 224 + (int)x1};
#pragma unroll
                    for (int k = 0; k < 4; ++k) ad[k] = wt[j][k] != 0.f && pix[i] >= 0 ? ad[k] * 192 + q * 16 : 0x40000000;
#pragma unroll
                    for (int k = 0; k < 4; ++k) {
#pragma unroll
                        for (int ks = 0; ks < 3; ++ks) g[j][ks][k] = (u32x4){0u, 0u, 0u, 0u};
                        if (__builtin_amdgcn_ballot_w64(ad[k] != 0x40000000) == 0) continue;
#pragma unroll
                        for (int ks = 0; ks < 3; ++ks) g[j][ks][k] = __builtin_amdgcn_raw_buffer_load_b128(xr, ad[k] + ks * 64, 0, 0);
                    }
                }
                asm volatile("" ::: "memory");
#pragma unroll
                for (int j = 0; j < 2; ++j) {
                    if (wv + 4 * (i0 + j) >= n) continue;
#pragma unroll
                    for (int ks = 0; ks < 3; ++ks) {
                        u32x4 bfr;
#pragma unroll
                        for (int d = 0; d < 4; ++d) {
                            float lo = 0.f, hi = 0.f;
#pragma unroll
                            for (int cn = 0; cn < 4; ++cn) {
                                const unsigned u = g[j][ks][cn][d];
                                const bf16x2 v = __builtin_bit_cast(bf16x2, u);
                                lo = __builtin_amdgcn_fdot2_f32_bf16(v, __builtin_bit_cast(bf16x2, __float_as_uint(wt[j][cn]) >> 16), lo, false);
                                hi = __builtin_amdgcn_fdot2_f32_bf16(v, __builtin_bit_cast(bf16x2, __float_as_uint(wt[j][cn])), hi, false);
                            }
                            bfr[d] = cvt_pk(lo, hi);
                        }
#pragma unroll
                        for (int k = 0; k < 6; ++k)
                            acc[i0 + j][k] = __builtin_amdgcn_mfma_f32_16x16x32_bf16(__builtin_bit_cast(bf16x8, wf[ks][k]), __builtin_bit_cast(bf16x8, bfr), acc[i0 + j][k], 0, 0, 0);
                    }
                }
            }
        }
#pragma unroll
        for (int i = 0; i < 4; ++i) {
            if (wv + 4 * i >= n) continue;
#pragma unroll
            for (int k = 0; k < 6; ++k) {
                u32x2 o; o[0] = cvt_pk(acc[i][k][0], acc[i][k][1]); o[1] = cvt_pk(acc[i][k][2], acc[i][k][3]);
                *(u32x2*)(out + pix[i] * 96 + k * 16 + q * 4) = o;
            }
        }
    }
}
extern "C" void kernel_launch(void* const* d_in, const int* in_sizes, int n_in, void* d_out, int out_size, void* d_ws, size_t ws_size, hipStream_t stream) {
    dcn_kernel<<<256, 256, 0, stream>>>((const unsigned short*)d_in[0], (const unsigned short*)d_in[1], (const unsigned short*)d_in[2], (unsigned short*)d_out, d_ws);
}
```

```cpp
#include <hip/hip_runtime.h>
#include <stdint.h>
typedef __attribute__((ext_vector_type(8))) __bf16 bf16x8;
typedef __attribute__((ext_vector_type(2))) __bf16 bf16x2;
typedef __attribute__((ext_vector_type(4))) float f32x4;
typedef __attribute__((ext_vector_type(4))) unsigned u32x4;
typedef __attribute__((ext_vector_type(2))) unsigned u32x2;
struct Args { const unsigned short *x, *off, *w; unsigned short* out; void* ws; };
static __device__ const unsigned recs[256 * 64] = {
1290240,1290240,1333248,1333248,1376256,1376256,1419264,1419264,1462272,1462272,1505280,1505280,40960,44032,48128,51200,55296,58368,62464,65536,69632,72704,76800,79872,268439552,336991254,57,807541760,64,7488,192,1040137727,16780300,50465804,576,36,224,1107296256,1107296256,1107296256,1098907648,1098907648,0,258624,1073741824,258048,51200,51200,48128,1379328,1073741824,1376256,16880,1107575308,4294961868,4294967294,16880,1107575308,4294961868,4294967294,16880,1107575308,4294961868,4294967294,
2623488,2666496,2709504,2752512,2795520,2838528,2881536,2924544,2924544,2924544,2924544,2924544,40960,45056,49152,53248,57344,61440,65536,69632,69632,69632,69632,69632,538976288,353703189,57,1080164608,80,7488,192,1040137855,16780300,50465804,576,36,128,1115947008,1115684864,1115160576,1107296256,1107296256,1107296256,533376,517248,501120,57344,49152,40960,2844672,2758656,2672640,1106264692,1112031880,4294960588,30,1106264692,1112031880,4294960588,30,1106264692,1112031880,4294960588,30,
5376000,5376000,5419008,5419008,5462016,5462016,5505024,5505024,5505024,5505024,5505024,5505024,40960,44032,48128,51200,55296,58368,62464,65536,62464,65536,62464,65536,268439552,336991254,57,1352798208,80,7488,192,1048526591,16780300,50465804,576,36,224,1123811328,1123811328,1123811328,1098907648,1098907648,0,1016640,1073741824,1016064,44032,44032,40960,5422080,1073741824,5419008,17146,1107575552,4294940588,4294967294,17146,1107575552,4294940588,4294967294,17146,1107575552,4294940588,4294967294,
7872,7872,8064,8064,8256,8256,8448,8448,8448,8448,8448,8448,40960,44032,48128,51200,55296,58368,62464,65536,62464,65536,62464,65536,268439552,336991254,410,1881808896,80,7984,43008,1048526335,50465804,16780300,36,80,224,1109917696,1109917696,1109917696,1098907648,1098907648,0,130536,1073741824,1512,44032,44032,40960,696192,1073741824,8064,16932,1107575344,4294959404,4294967294,16932,1107575344,4294959404,4294967294,16932,1107575344,4294959404,4294967294,
17472,17472,17664,17664,17856,17856,18048,18048,18048,18048,18048,18048,40960,44032,48128,51200,55296,58368,62464,65536,62464,65536,62464,65536,268439552,336991254,410,2154962944,80,7984,43008,1048526463,50465804,16780300,36,80,224,1119354880,1119354880,1119354880,1098907648,1098907648,0,132336,1073741824,3312,44032,44032,40960,705792,1073741824,17664,17078,1107575484,4294948204,4294967294,17078,1107575484,4294948204,4294967294,17078,1107575484,4294948204,4294967294,
6752256,6795264,6838272,6881280,6924288,6967296,7010304,7053312,7053312,7053312,7053312,7053312,40960,45056,49152,53248,57344,61440,65536,69632,69632,69632,69632,69632,538976288,353703189,57,2428109056,80,7488,192,1056915199,16780300,50465804,576,36,128,1126301696,1126170624,1126039552,1107296256,1107296256,1107296256,1307520,1291392,1275264,57344,49152,40960,6973440,6887424,6801408,1106264861,1112032036,4294948300,30,1106264861,1112032036,4294948300,30,1106264861,1112032036,4294948300,30,
35520,35520,35712,35712,35904,35904,36096,36096,36096,36096,36096,36096,40960,44032,48128,51200,55296,58368,62464,65536,62464,65536,62464,65536,268439552,336991254,410,2957647872,80,7984,43008,1056915071,50465804,16780300,36,80,224,1127874560,1127874560,1127874560,1098907648,1098907648,0,135720,1073741824,6696,44032,44032,40960,723840,1073741824,35712,17209,1107575612,4294927148,4294967294,17209,1107575612,4294927148,4294967294,17209,1107575612,4294927148,4294967294,
7440384,7440384,7483392,7483392,7526400,7526400,7569408,7569408,7569408,7569408,7569408,7569408,40960,44032,48128,51200,55296,58368,62464,65536,62464,65536,62464,65536,268439552,336991254,57,3230797824,80,7488,192,1056915199,16780300,50465804,576,36,224,1127088128,1127088128,1127088128,1098907648,1098907648,0,1403712,1073741824,1403136,44032,44032,40960,7486464,1073741824,7483392,17197,1107575600,4294929836,4294967294,17197,1107575600,4294929836,4294967294,17197,1107575600,4294929836,4294967294,
1419264,1419264,1462272,1462272,1505280,1505280,1548288,1548288,1548288,1548288,1548288,1548288,40960,44032,48128,51200,55296,58368,62464,65536,62464,65536,62464,65536,268439552,336991254,57,808062976,80,7488,192,1040137727,16780300,50465804,576,36,224,1107820544,1107820544,1107820544,1098907648,1098907648,0,274752,1073741824,274176,44032,44032,40960,1465344,1073741824,1462272,16900,1107575312,4294961196,4294967294,16900,1107575312,4294961196,4294967294,16900,1107575312,4294961196,4294967294,
3569664,3569664,3612672,3612672,3655680,3655680,3698688,3698688,3698688,3698688,3698688,3698688,40960,44032,48128,51200,55296,58368,62464,65536,62464,65536,62464,65536,268439552,336991254,57,1080692736,80,7488,192,1040137855,16780300,50465804,576,36,224,1118306304,1118306304,1118306304,1098907648,1098907648,0,677952,1073741824,677376,44032,44032,40960,3615744,1073741824,3612672,17062,1107575468,4294949996,4294967294,17062,1107575468,4294949996,4294967294,17062,1107575468,4294949996,4294967294,
5462016,5462016,5505024,5505024,5548032,5548032,5591040,5591040,5591040,5591040,5591040,5591040,40960,44032,48128,51200,55296,58368,62464,65536,62464,65536,62464,65536,268439552,336991254,57,1353322496,80,7488,192,1048526591,16780300,50465804,576,36,224,1124073472,1124073472,1124073472,1098907648,1098907648,0,1032768,1073741824,1032192,44032,44032,40960,5508096,1073741824,5505024,17150,1107575554,4294940140,4294967294,17150,1107575554,4294940140,4294967294,17150,1107575554,4294940140,4294967294,
8256,8256,8448,8448,8640,8640,8832,8832,8832,8832,8832,8832,40960,44032,48128,51200,55296,58368,62464,65536,62464,65536,62464,65536,268439552,336991254,410,1882333184,80,7984,43008,1048526335,50465804,16780300,36,80,224,1110441984,1110441984,1110441984,1098907648,1098907648,0,130608,1073741824,1584,44032,44032,40960,696576,1073741824,8448,16940,1107575352,4294958956,4294967294,16940,1107575352,4294958956,4294967294,16940,1107575352,4294958956,4294967294,
17856,17856,18048,18048,18240,18240,18432,18432,18432,18432,18432,18432,40960,44032,48128,51200,55296,58368,62464,65536,62464,65536,62464,65536,268439552,336991254,410,2155487232,80,7984,43008,1048526463,50465804,16780300,36,80,224,1119617024,1119617024,1119617024,1098907648,1098907648,0,132408,1073741824,3384,44032,44032,40960,706176,1073741824,18048,17082,1107575488,4294947756,4294967294,17082,1107575488,4294947756,4294967294,17082,1107575488,4294947756,4294967294,
26304,26304,26496,26496,26688,26688,26880,26880,26880,26880,26880,26880,40960,44032,48128,51200,55296,58368,62464,65536,62464,65536,62464,65536,268439552,336991254,410,2428641280,80,7984,43008,1056915199,50465804,16780300,36,80,224,1124728832,1124728832,1124728832,1098907648,1098907648,0,133992,1073741824,4968,44032,44032,40960,714624,1073741824,26496,17161,1107575564,4294937900,4294967294,17161,1107575564,4294937900,4294967294,17161,1107575564,4294937900,4294967294,
35904,35904,36096,36096,36288,36288,36480,36480,36480,36480,36480,36480,40960,44032,48128,51200,55296,58368,62464,65536,62464,65536,62464,65536,268439552,336991254,410,2958172160,80,7984,43008,1056915071,50465804,16780300,36,80,224,1128005632,1128005632,1128005632,1098907648,1098907648,0,135792,1073741824,6768,44032,44032,40960,724224,1073741824,36096,17211,1107575614,4294926700,4294967294,17211,1107575614,4294926700,4294967294,17211,1107575614,4294926700,4294967294,
7526400,7526400,7569408,7569408,7612416,7612416,7655424,7655424,7655424,7655424,7655424,7655424,40960,44032,48128,51200,55296,58368,62464,65536,62464,65536,62464,65536,268439552,336991254,57,3231322112,80,7488,192,1056915199,16780300,50465804,576,36,224,1127219200,1127219200,1127219200,1098907648,1098907648,0,1419840,1073741824,1419264,44032,44032,40960,7572480,1073741824,7569408,17199,1107575602,4294929388,4294967294,17199,1107575602,4294929388,4294967294,17199,1107575602,4294929388,4294967294,
1290240,1333248,1376256,1419264,1462272,1505280,1548288,1591296,1634304,1634304,1634304,1634304,40960,45056,49152,53248,57344,61440,65536,69632,73728,73728,73728,73728,538976288,353703189,57,808584448,64,7488,192,1040137727,16780300,50465804,576,36,128,1108344832,1107820544,1107296256,1107296256,1107296256,1107296256,291456,275328,259200,61440,53248,45056,1554432,1468416,1382400,1106264560,1112031768,4294964556,30,1106264560,1112031768,4294964556,30,1106264560,1112031768,4294964556,30,
3655680,3655680,3698688,3698688,3741696,3741696,3784704,3784704,3784704,3784704,3784704,3784704,40960,44032,48128,51200,55296,58368,62464,65536,62464,65536,62464,65536,268439552,336991254,57,1081217024,80,7488,192,1040137855,16780300,50465804,576,36,224,1118568448,1118568448,1118568448,1098907648,1098907648,0,694080,1073741824,693504,44032,44032,40960,3701760,1073741824,3698688,17066,1107575472,4294949548,4294967294,17066,1107575472,4294949548,4294967294,17066,1107575472,4294949548,4294967294,
5548032,5548032,5591040,5591040,5634048,5634048,5677056,5677056,5677056,5677056,5677056,5677056,40960,44032,48128,51200,55296,58368,62464,65536,62464,65536,62464,65536,268439552,336991254,57,1353846784,80,7488,192,1048526591,16780300,50465804,576,36,224,1124204544,1124204544,1124204544,1098907648,1098907648,0,1048896,1073741824,1048320,44032,44032,40960,5594112,1073741824,5591040,17153,1107575556,4294939692,4294967294,17153,1107575556,4294939692,4294967294,17153,1107575556,4294939692,4294967294,
8640,8640,8832,8832,9024,9024,9216,9216,9216,9216,9216,9216,40960,44032,48128,51200,55296,58368,62464,65536,62464,65536,62464,65536,268439552,336991254,410,1882857472,80,7984,43008,1048526335,50465804,16780300,36,80,224,1110966272,1110966272,1110966272,1098907648,1098907648,0,130680,1073741824,1656,44032,44032,40960,696960,1073741824,8832,16948,1107575360,4294958508,4294967294,16948,1107575360,4294958508,4294967294,16948,1107575360,4294958508,4294967294,
18240,18240,18432,18432,18624,18624,18816,18816,18816,18816,18816,18816,40960,44032,48128,51200,55296,58368,62464,65536,62464,65536,62464,65536,268439552,336991254,410,2156011520,80,7984,43008,1048526591,50465804,16780300,36,80,224,1119879168,1119879168,1119879168,1098907648,1098907648,0,132480,1073741824,3456,44032,44032,40960,706560,1073741824,18432,17086,1107575492,4294947308,4294967294,17086,1107575492,4294947308,4294967294,17086,1107575492,4294947308,4294967294,
172032,215040,258048,301056,344064,387072,9074688,9117696,9117696,9117696,9117696,9117696,49152,53248,57344,61440,65536,69632,40960,45056,45056,45056,45056,45056,2425356288,320017685,57,2429170176,80,7488,192,1056915199,16780300,50465804,576,36,128,1129447424,1129447424,1086324736,1125122048,1125122048,0,1698624,1073741824,48384,32768,32768,53248,9059328,1073741824,258048,1125073737,1126187860,4294940716,142,1125073737,1126187860,4294940716,142,16512,1099972880,1036,4294967294,
36288,36288,36480,36480,36672,36672,36864,36864,36864,36864,36864,36864,40960,44032,48128,51200,55296,58368,62464,65536,62464,65536,62464,65536,268439552,336991254,410,2958696448,80,7984,43008,1056915071,50465804,16780300,36,80,224,1128136704,1128136704,1128136704,1098907648,1098907648,0,135864,1073741824,6840,44032,44032,40960,724608,1073741824,36480,17213,1107575616,4294926252,4294967294,17213,1107575616,4294926252,4294967294,17213,1107575616,4294926252,4294967294,
7612416,7612416,7655424,7655424,7698432,7698432,7741440,7741440,7741440,7741440,7741440,7741440,40960,44032,48128,51200,55296,58368,62464,65536,62464,65536,62464,65536,268439552,336991254,57,3231846400,80,7488,192,1056915199,16780300,50465804,576,36,224,1127350272,1127350272,1127350272,1098907648,1098907648,0,1435968,1073741824,1435392,44032,44032,40960,7658496,1073741824,7655424,17201,1107575604,4294928940,4294967294,17201,1107575604,4294928940,4294967294,17201,1107575604,4294928940,4294967294,
1505280,1505280,1548288,1548288,1591296,1591296,1634304,1634304,1634304,1634304,1634304,1634304,40960,44032,48128,51200,55296,58368,62464,65536,62464,65536,62464,65536,268439552,336991254,57,809111552,80,7488,192,1040137727,16780300,50465804,576,36,224,1108344832,1108344832,1108344832,1098907648,1098907648,0,290880,1073741824,290304,44032,44032,40960,1551360,1073741824,1548288,16908,1107575320,4294960748,4294967294,16908,1107575320,4294960748,4294967294,16908,1107575320,4294960748,4294967294,
3741696,3741696,3784704,3784704,3827712,3827712,3870720,3870720,3870720,3870720,3870720,3870720,40960,44032,48128,51200,55296,58368,62464,65536,62464,65536,62464,65536,268439552,336991254,57,1081741312,80,7488,192,1040137855,16780300,50465804,576,36,224,1118830592,1118830592,1118830592,1098907648,1098907648,0,710208,1073741824,709632,44032,44032,40960,3787776,1073741824,3784704,17070,1107575476,4294949100,4294967294,17070,1107575476,4294949100,4294967294,17070,1107575476,4294949100,4294967294,
3913728,3956736,3999744,4042752,4085760,4128768,4171776,4214784,4214784,4214784,4214784,4214784,40960,45056,49152,53248,57344,61440,65536,69632,69632,69632,69632,69632,538976288,353703189,57,1354367232,80,7488,192,1048526591,16780300,50465804,576,36,128,1119879168,1119617024,1119354880,1107296256,1107296256,1107296256,775296,759168,743040,57344,49152,40960,4134912,4048896,3962880,1106264758,1112031940,4294956748,30,1106264758,1112031940,4294956748,30,1106264758,1112031940,4294956748,30,
4945920,4988928,5031936,5074944,5117952,5160960,5203968,5246976,5246976,5246976,5246976,5246976,40960,45056,49152,53248,57344,61440,65536,69632,69632,69632,69632,69632,538976288,353703189,57,1883373824,80,7488,192,1048526463,16780300,50465804,576,36,128,1123024896,1122762752,1122500608,1107296256,1107296256,1107296256,968832,952704,936576,57344,49152,40960,5167104,5081088,4995072,1106264806,1112031988,4294953676,30,1106264806,1112031988,4294953676,30,1106264806,1112031988,4294953676,30,
18624,18624,18816,18816,19008,19008,19200,19200,19200,19200,19200,19200,40960,44032,48128,51200,55296,58368,62464,65536,62464,65536,62464,65536,268439552,336991254,410,2156535808,80,7984,43008,1048526591,50465804,16780300,36,80,224,1120141312,1120141312,1120141312,1098907648,1098907648,0,132552,1073741824,3528,44032,44032,40960,706944,1073741824,18816,17090,1107575496,4294946860,4294967294,17090,1107575496,4294946860,4294967294,17090,1107575496,4294946860,4294967294,
1290240,1333248,1376256,1419264,1462272,9504768,9547776,9590784,9590784,9590784,9590784,9590784,57344,61440,65536,69632,73728,40960,45056,49152,53248,53248,53248,53248,2425393296,353703189,57,2429814016,64,7488,192,1056915199,16780300,50465804,576,36,128,1130102784,1130233856,1107296256,1125122048,1125122048,1125122048,1779264,1795392,263232,32768,40960,61440,9489408,9575424,1403904,1125073747,1126187871,4294939436,142,1125073747,1126187871,4294939436,142,1125007856,1126318600,4294964396,142,
36672,36672,36864,36864,37056,37056,37248,37248,37248,37248,37248,37248,40960,44032,48128,51200,55296,58368,62464,65536,62464,65536,62464,65536,268439552,336991254,410,2959220736,80,7984,43008,1056915071,50465804,16780300,36,80,224,1128267776,1128267776,1128267776,1098907648,1098907648,0,135936,1073741824,6912,44032,44032,40960,724992,1073741824,36864,17215,1107575618,4294925804,4294967294,17215,1107575618,4294925804,4294967294,17215,1107575618,4294925804,4294967294,
7698432,7698432,7741440,7741440,7784448,7784448,7827456,7827456,7827456,7827456,7827456,7827456,40960,44032,48128,51200,55296,58368,62464,65536,62464,65536,62464,65536,268439552,336991254,57,3232370688,80,7488,192,1056915199,16780300,50465804,576,36,224,1127481344,1127481344,1127481344,1098907648,1098907648,0,1452096,1073741824,1451520,44032,44032,40960,7744512,1073741824,7741440,17203,1107575606,4294928492,4294967294,17203,1107575606,4294928492,4294967294,17203,1107575606,4294928492,4294967294,
1591296,1591296,1634304,1634304,1677312,1677312,1720320,1720320,1720320,1720320,1720320,1720320,40960,44032,48128,51200,55296,58368,62464,65536,62464,65536,62464,65536,268439552,336991254,57,809635840,80,7488,192,1040137855,16780300,50465804,576,36,224,1108869120,1108869120,1108869120,1098907648,1098907648,0,307008,1073741824,306432,44032,44032,40960,1637376,1073741824,1634304,16916,1107575328,4294960300,4294967294,16916,1107575328,4294960300,4294967294,16916,1107575328,4294960300,4294967294,
3827712,3827712,3870720,3870720,3913728,3913728,3956736,3956736,3956736,3956736,3956736,3956736,40960,44032,48128,51200,55296,58368,62464,65536,62464,65536,62464,65536,268439552,336991254,57,1082265600,80,7488,192,1040137983,16780300,50465804,576,36,224,1119092736,1119092736,1119092736,1098907648,1098907648,0,726336,1073741824,725760,44032,44032,40960,3873792,1073741824,3870720,17074,1107575480,4294948652,4294967294,17074,1107575480,4294948652,4294967294,17074,1107575480,4294948652,4294967294,
5634048,5634048,5677056,5677056,5720064,5720064,5763072,5763072,5763072,5763072,5763072,5763072,40960,44032,48128,51200,55296,58368,62464,65536,62464,65536,62464,65536,268439552,336991254,57,1354895360,80,7488,192,1048526591,16780300,50465804,576,36,224,1124335616,1124335616,1124335616,1098907648,1098907648,0,1065024,1073741824,1064448,44032,44032,40960,5680128,1073741824,5677056,17155,1107575558,4294939244,4294967294,17155,1107575558,4294939244,4294967294,17155,1107575558,4294939244,4294967294,
9024,9024,9216,9216,9408,9408,9600,9600,9600,9600,9600,9600,40960,44032,48128,51200,55296,58368,62464,65536,62464,65536,62464,65536,268439552,336991254,410,1883906048,80,7984,43008,1048526463,50465804,16780300,36,80,224,1111490560,1111490560,1111490560,1098907648,1098907648,0,130752,1073741824,1728,44032,44032,40960,697344,1073741824,9216,16956,1107575368,4294958060,4294967294,16956,1107575368,4294958060,4294967294,16956,1107575368,4294958060,4294967294,
19008,19008,19200,19200,19392,19392,19584,19584,19584,19584,19584,19584,40960,44032,48128,51200,55296,58368,62464,65536,62464,65536,62464,65536,268439552,336991254,410,2157060096,80,7984,43008,1048526591,50465804,16780300,36,80,224,1120403456,1120403456,1120403456,1098907648,1098907648,0,132624,1073741824,3600,44032,44032,40960,707328,1073741824,19200,17094,1107575500,4294946412,4294967294,17094,1107575500,4294946412,4294967294,17094,1107575500,4294946412,4294967294,
26688,26688,26880,26880,27072,27072,27264,27264,27264,27264,27264,27264,40960,44032,48128,51200,55296,58368,62464,65536,62464,65536,62464,65536,268439552,336991254,410,2686590976,80,7984,43008,1056914943,50465804,16780300,36,80,224,1124859904,1124859904,1124859904,1098907648,1098907648,0,134064,1073741824,5040,44032,44032,40960,715008,1073741824,26880,17163,1107575566,4294937452,4294967294,17163,1107575566,4294937452,4294967294,17163,1107575566,4294937452,4294967294,
7784448,7827456,7870464,7913472,7956480,7999488,8042496,8085504,8085504,8085504,8085504,8085504,40960,45056,49152,53248,57344,61440,65536,69632,69632,69632,69632,69632,538976288,353703189,57,2959737088,80,7488,192,1056915071,16780300,50465804,576,36,128,1127874560,1127743488,1127612416,1107296256,1107296256,1107296256,1501056,1484928,1468800,57344,49152,40960,8005632,7919616,7833600,1106264885,1112032060,4294945228,30,1106264885,1112032060,4294945228,30,1106264885,1112032060,4294945228,30,
7784448,7784448,7827456,7827456,7870464,7870464,7913472,7913472,7913472,7913472,7913472,7913472,40960,44032,48128,51200,55296,58368,62464,65536,62464,65536,62464,65536,268439552,336991254,57,3232894976,80,7488,192,1056915199,16780300,50465804,576,36,224,1127612416,1127612416,1127612416,1098907648,1098907648,0,1468224,1073741824,1467648,44032,44032,40960,7830528,1073741824,7827456,17205,1107575608,4294928044,4294967294,17205,1107575608,4294928044,4294967294,17205,1107575608,4294928044,4294967294,
1677312,1677312,1720320,1720320,1763328,1763328,1806336,1806336,1806336,1806336,1806336,1806336,40960,44032,48128,51200,55296,58368,62464,65536,62464,65536,62464,65536,268439552,336991254,57,810160128,80,7488,192,1040137855,16780300,50465804,576,36,224,1109393408,1109393408,1109393408,1098907648,1098907648,0,323136,1073741824,322560,44032,44032,40960,1723392,1073741824,1720320,16924,1107575336,4294959852,4294967294,16924,1107575336,4294959852,4294967294,16924,1107575336,4294959852,4294967294,
3913728,3913728,3956736,3956736,3999744,3999744,4042752,4042752,4042752,4042752,4042752,4042752,40960,44032,48128,51200,55296,58368,62464,65536,62464,65536,62464,65536,268439552,336991254,57,1082789888,80,7488,192,1040137983,16780300,50465804,576,36,224,1119354880,1119354880,1119354880,1098907648,1098907648,0,742464,1073741824,741888,44032,44032,40960,3959808,1073741824,3956736,17078,1107575484,4294948204,4294967294,17078,1107575484,4294948204,4294967294,17078,1107575484,4294948204,4294967294,
5720064,5720064,5763072,5763072,5806080,5806080,5849088,5849088,5849088,5849088,5849088,5849088,40960,44032,48128,51200,55296,58368,62464,65536,62464,65536,62464,65536,268439552,336991254,57,1355419648,80,7488,192,1048526591,16780300,50465804,576,36,224,1124466688,1124466688,1124466688,1098907648,1098907648,0,1081152,1073741824,1080576,44032,44032,40960,5766144,1073741824,5763072,17157,1107575560,4294938796,4294967294,17157,1107575560,4294938796,4294967294,17157,1107575560,4294938796,4294967294,
9408,9408,9600,9600,9792,9792,9984,9984,9984,9984,9984,9984,40960,44032,48128,51200,55296,58368,62464,65536,62464,65536,62464,65536,268439552,336991254,410,1884430336,80,7984,43008,1048526463,50465804,16780300,36,80,224,1112014848,1112014848,1112014848,1098907648,1098907648,0,130824,1073741824,1800,44032,44032,40960,697728,1073741824,9600,16964,1107575376,4294957612,4294967294,16964,1107575376,4294957612,4294967294,16964,1107575376,4294957612,4294967294,
5978112,6021120,6064128,6107136,6150144,6193152,6236160,6279168,6279168,6279168,6279168,6279168,40960,45056,49152,53248,57344,61440,65536,69632,69632,69632,69632,69632,538976288,353703189,57,2157576448,80,7488,192,1048526591,16780300,50465804,576,36,128,1125122048,1124990976,1124859904,1107296256,1107296256,1107296256,1162368,1146240,1130112,57344,49152,40960,6199296,6113280,6027264,1106264843,1112032018,4294950604,30,1106264843,1112032018,4294950604,30,1106264843,1112032018,4294950604,30,
27072,27072,27264,27264,27456,27456,27648,27648,27648,27648,27648,27648,40960,44032,48128,51200,55296,58368,62464,65536,62464,65536,62464,65536,268439552,336991254,410,2687115264,80,7984,43008,1056914943,50465804,16780300,36,80,224,1124990976,1124990976,1124990976,1098907648,1098907648,0,134136,1073741824,5112,44032,44032,40960,715392,1073741824,27264,17165,1107575568,4294937004,4294967294,17165,1107575568,4294937004,4294967294,17165,1107575568,4294937004,4294967294,
37056,37056,37248,37248,37440,37440,37632,37632,37632,37632,37632,37632,40960,44032,48128,51200,55296,58368,62464,65536,62464,65536,62464,65536,268439552,336991254,410,2960269312,80,7984,43008,1056915071,50465804,16780300,36,80,224,1128398848,1128398848,1128398848,1098907648,1098907648,0,136008,1073741824,6984,44032,44032,40960,725376,1073741824,37248,17217,1107575620,4294925356,4294967294,17217,1107575620,4294925356,4294967294,17217,1107575620,4294925356,4294967294,
7870464,7870464,7913472,7913472,7956480,7956480,7999488,7999488,7999488,7999488,7999488,7999488,40960,44032,48128,51200,55296,58368,62464,65536,62464,65536,62464,65536,268439552,336991254,57,3233419264,80,7488,192,1056915199,16780300,50465804,576,36,224,1127743488,1127743488,1127743488,1098907648,1098907648,0,1484352,1073741824,1483776,44032,44032,40960,7916544,1073741824,7913472,17207,1107575610,4294927596,4294967294,17207,1107575610,4294927596,4294967294,17207,1107575610,4294927596,4294967294,
1763328,1763328,1806336,1806336,1849344,1849344,1892352,1892352,1892352,1892352,1892352,1892352,40960,44032,48128,51200,55296,58368,62464,65536,62464,65536,62464,65536,268439552,336991254,57,810684416,80,7488,192,1040137855,16780300,50465804,576,36,224,1109917696,1109917696,1109917696,1098907648,1098907648,0,339264,1073741824,338688,44032,44032,40960,1809408,1073741824,1806336,16932,1107575344,4294959404,4294967294,16932,1107575344,4294959404,4294967294,16932,1107575344,4294959404,4294967294,
3999744,3999744,4042752,4042752,4085760,4085760,4128768,4128768,4128768,4128768,4128768,4128768,40960,44032,48128,51200,55296,58368,62464,65536,62464,65536,62464,65536,268439552,336991254,57,1083314176,80,7488,192,1040137983,16780300,50465804,576,36,224,1119617024,1119617024,1119617024,1098907648,1098907648,0,758592,1073741824,758016,44032,44032,40960,4045824,1073741824,4042752,17082,1107575488,4294947756,4294967294,17082,1107575488,4294947756,4294967294,17082,1107575488,4294947756,4294967294,
5806080,5806080,5849088,5849088,5892096,5892096,5935104,5935104,5935104,5935104,5935104,5935104,40960,44032,48128,51200,55296,58368,62464,65536,62464,65536,62464,65536,268439552,336991254,57,1355943936,80,7488,192,1048526591,16780300,50465804,576,36,224,1124597760,1124597760,1124597760,1098907648,1098907648,0,1097280,1073741824,1096704,44032,44032,40960,5852160,1073741824,5849088,17159,1107575562,4294938348,4294967294,17159,1107575562,4294938348,4294967294,17159,1107575562,4294938348,4294967294,
9792,9792,9984,9984,10176,10176,10368,10368,10368,10368,10368,10368,40960,44032,48128,51200,55296,58368,62464,65536,62464,65536,62464,65536,268439552,336991254,410,1884954624,80,7984,43008,1048526463,50465804,16780300,36,80,224,1112539136,1112539136,1112539136,1098907648,1098907648,0,130896,1073741824,1872,44032,44032,40960,698112,1073741824,9984,16972,1107575384,4294957164,4294967294,16972,1107575384,4294957164,4294967294,16972,1107575384,4294957164,4294967294,
19392,19392,19584,19584,19776,19776,19968,19968,19968,19968,19968,19968,40960,44032,48128,51200,55296,58368,62464,65536,62464,65536,62464,65536,268439552,336991254,410,2158108672,80,7984,43008,1048526591,50465804,16780300,36,80,224,1120665600,1120665600,1120665600,1098907648,1098907648,0,132696,1073741824,3672,44032,44032,40960,707712,1073741824,19584,17098,1107575504,4294945964,4294967294,17098,1107575504,4294945964,4294967294,17098,1107575504,4294945964,4294967294,
27456,27456,27648,27648,27840,27840,28032,28032,28032,28032,28032,28032,40960,44032,48128,51200,55296,58368,62464,65536,62464,65536,62464,65536,268439552,336991254,410,2687639552,80,7984,43008,1056914943,50465804,16780300,36,80,224,1125122048,1125122048,1125122048,1098907648,1098907648,0,134208,1073741824,5184,44032,44032,40960,715776,1073741824,27648,17167,1107575570,4294936556,4294967294,17167,1107575570,4294936556,4294967294,17167,1107575570,4294936556,4294967294,
37440,37440,37632,37632,37824,37824,38016,38016,38016,38016,38016,38016,40960,44032,48128,51200,55296,58368,62464,65536,62464,65536,62464,65536,268439552,336991254,410,2960793600,80,7984,43008,1056915071,50465804,16780300,36,80,224,1128529920,1128529920,1128529920,1098907648,1098907648,0,136080,1073741824,7056,44032,44032,40960,725760,1073741824,37632,17219,1107575622,4294924908,4294967294,17219,1107575622,4294924908,4294967294,17219,1107575622,4294924908,4294967294,
7956480,7956480,7999488,7999488,8042496,8042496,8085504,8085504,8085504,8085504,8085504,8085504,40960,44032,48128,51200,55296,58368,62464,65536,62464,65536,62464,65536,268439552,336991254,57,3233943552,80,7488,192,1056915199,16780300,50465804,576,36,224,1127874560,1127874560,1127874560,1098907648,1098907648,0,1500480,1073741824,1499904,44032,44032,40960,8002560,1073741824,7999488,17209,1107575612,4294927148,4294967294,17209,1107575612,4294927148,4294967294,17209,1107575612,4294927148,4294967294,
1591296,1634304,1677312,1720320,1763328,1806336,1849344,1892352,1892352,1892352,1892352,1892352,40960,45056,49152,53248,57344,61440,65536,69632,69632,69632,69632,69632,538976288,353703189,57,811204864,80,7488,192,1040137855,16780300,50465804,576,36,128,1109917696,1109393408,1108869120,1107296256,1107296256,1107296256,339840,323712,307584,57344,49152,40960,1812480,1726464,1640448,1106264596,1112031792,4294963660,30,1106264596,1112031792,4294963660,30,1106264596,1112031792,4294963660,30,
2881536,2924544,2967552,3010560,3053568,3096576,3139584,3182592,3182592,3182592,3182592,3182592,40960,45056,49152,53248,57344,61440,65536,69632,69632,69632,69632,69632,538976288,353703189,57,1083834624,80,7488,192,1040137983,16780300,50465804,576,36,128,1116733440,1116471296,1116209152,1107296256,1107296256,1107296256,581760,565632,549504,57344,49152,40960,3102720,3016704,2930688,1106264710,1112031892,4294959820,30,1106264710,1112031892,4294959820,30,1106264710,1112031892,4294959820,30,
5892096,5892096,5935104,5935104,5978112,5978112,6021120,6021120,6021120,6021120,6021120,6021120,40960,44032,48128,51200,55296,58368,62464,65536,62464,65536,62464,65536,268439552,336991254,57,1356468224,80,7488,192,1048526591,16780300,50465804,576,36,224,1124728832,1124728832,1124728832,1098907648,1098907648,0,1113408,1073741824,1112832,44032,44032,40960,5938176,1073741824,5935104,17161,1107575564,4294937900,4294967294,17161,1107575564,4294937900,4294967294,17161,1107575564,4294937900,4294967294,
10176,10176,10368,10368,10560,10560,10752,10752,10752,10752,10752,10752,40960,44032,48128,51200,55296,58368,62464,65536,62464,65536,62464,65536,268439552,336991254,410,1885478912,80,7984,43008,1048526463,50465804,16780300,36,80,224,1113063424,1113063424,1113063424,1098907648,1098907648,0,130968,1073741824,1944,44032,44032,40960,698496,1073741824,10368,16980,1107575392,4294956716,4294967294,16980,1107575392,4294956716,4294967294,16980,1107575392,4294956716,4294967294,
19776,19776,19968,19968,20160,20160,20352,20352,20352,20352,20352,20352,40960,44032,48128,51200,55296,58368,62464,65536,62464,65536,62464,65536,268439552,336991254,410,2158632960,80,7984,43008,1048526591,50465804,16780300,36,80,224,1120927744,1120927744,1120927744,1098907648,1098907648,0,132768,1073741824,3744,44032,44032,40960,708096,1073741824,19968,17102,1107575508,4294945516,4294967294,17102,1107575508,4294945516,4294967294,17102,1107575508,4294945516,4294967294,
27840,27840,28032,28032,28224,28224,28416,28416,28416,28416,28416,28416,40960,44032,48128,51200,55296,58368,62464,65536,62464,65536,62464,65536,268439552,336991254,410,2688163840,80,7984,43008,1056914943,50465804,16780300,36,80,224,1125253120,1125253120,1125253120,1098907648,1098907648,0,134280,1073741824,5256,44032,44032,40960,716160,1073741824,28032,17169,1107575572,4294936108,4294967294,17169,1107575572,4294936108,4294967294,17169,1107575572,4294936108,4294967294,
37824,37824,38016,38016,38208,38208,38400,38400,38400,38400,38400,38400,40960,44032,48128,51200,55296,58368,62464,65536,62464,65536,62464,65536,268439552,336991254,410,2961317888,80,7984,43008,1056915199,50465804,16780300,36,80,224,1128660992,1128660992,1128660992,1098907648,1098907648,0,136152,1073741824,7128,44032,44032,40960,726144,1073741824,38016,17221,1107575624,4294924460,4294967294,17221,1107575624,4294924460,4294967294,17221,1107575624,4294924460,4294967294,
430080,473088,516096,559104,602112,645120,9074688,9117696,9117696,9117696,9117696,9117696,49152,53248,57344,61440,65536,69632,40960,45056,45056,45056,45056,45056,3233808384,320017685,57,3234476544,80,7488,192,1056915199,16780300,50465804,576,36,128,1129447424,1129447424,1094713344,1128267776,1128267776,0,1700352,1073741824,96768,32768,32768,53248,9068544,1073741824,516096,1128219465,1129333588,4294940428,190,1128219465,1129333588,4294940428,190,16672,1099972976,268,4294967294,
1849344,1849344,1892352,1892352,1935360,1935360,1978368,1978368,1978368,1978368,1978368,1978368,40960,44032,48128,51200,55296,58368,62464,65536,62464,65536,62464,65536,268439552,336991254,57,811732992,80,7488,192,1040137855,16780300,50465804,576,36,224,1110441984,1110441984,1110441984,1098907648,1098907648,0,355392,1073741824,354816,44032,44032,40960,1895424,1073741824,1892352,16940,1107575352,4294958956,4294967294,16940,1107575352,4294958956,4294967294,16940,1107575352,4294958956,4294967294,
2688,2688,2880,2880,3072,3072,3264,3264,3456,3456,3648,3648,40960,44032,48128,51200,55296,58368,62464,65536,69632,72704,76800,79872,268439552,336991254,410,1084370432,64,7984,43008,1040137983,50465804,16780300,36,80,224,1098907648,1098907648,1098907648,1098907648,1098907648,0,129600,1073741824,576,51200,51200,48128,691200,1073741824,3072,16736,1107575192,4294965452,4294967294,16736,1107575192,4294965452,4294967294,16736,1107575192,4294965452,4294967294,
5978112,5978112,6021120,6021120,6064128,6064128,6107136,6107136,6107136,6107136,6107136,6107136,40960,44032,48128,51200,55296,58368,62464,65536,62464,65536,62464,65536,268439552,336991254,57,1612845056,80,7488,192,1048526335,16780300,50465804,576,36,224,1124859904,1124859904,1124859904,1098907648,1098907648,0,1129536,1073741824,1128960,44032,44032,40960,6024192,1073741824,6021120,17163,1107575566,4294937452,4294967294,17163,1107575566,4294937452,4294967294,17163,1107575566,4294937452,4294967294,
10560,10560,10752,10752,10944,10944,11136,11136,11136,11136,11136,11136,40960,44032,48128,51200,55296,58368,62464,65536,62464,65536,62464,65536,268439552,336991254,410,1886003200,80,7984,43008,1048526463,50465804,16780300,36,80,224,1113587712,1113587712,1113587712,1098907648,1098907648,0,131040,1073741824,2016,44032,44032,40960,698880,1073741824,10752,16988,1107575400,4294956268,4294967294,16988,1107575400,4294956268,4294967294,16988,1107575400,4294956268,4294967294,
20160,20160,20352,20352,20544,20544,20736,20736,20736,20736,20736,20736,40960,44032,48128,51200,55296,58368,62464,65536,62464,65536,62464,65536,268439552,336991254,410,2159157248,80,7984,43008,1048526591,50465804,16780300,36,80,224,1121189888,1121189888,1121189888,1098907648,1098907648,0,132840,1073741824,3816,44032,44032,40960,708480,1073741824,20352,17106,1107575512,4294945068,4294967294,17106,1107575512,4294945068,4294967294,17106,1107575512,4294945068,4294967294,
28224,28224,28416,28416,28608,28608,28800,28800,28800,28800,28800,28800,40960,44032,48128,51200,55296,58368,62464,65536,62464,65536,62464,65536,268439552,336991254,410,2688688128,80,7984,43008,1056915071,50465804,16780300,36,80,224,1125384192,1125384192,1125384192,1098907648,1098907648,0,134352,1073741824,5328,44032,44032,40960,716544,1073741824,28416,17171,1107575574,4294935660,4294967294,17171,1107575574,4294935660,4294967294,17171,1107575574,4294935660,4294967294,
38208,38208,38400,38400,38592,38592,38784,38784,38784,38784,38784,38784,40960,44032,48128,51200,55296,58368,62464,65536,62464,65536,62464,65536,268439552,336991254,410,2961842176,80,7984,43008,1056915199,50465804,16780300,36,80,224,1128792064,1128792064,1128792064,1098907648,1098907648,0,136224,1073741824,7200,44032,44032,40960,726528,1073741824,38400,17223,1107575626,4294924012,4294967294,17223,1107575626,4294924012,4294967294,17223,1107575626,4294924012,4294967294,
1290240,1333248,1376256,1419264,1462272,9504768,9547776,9590784,9590784,9590784,9590784,9590784,57344,61440,65536,69632,73728,40960,45056,49152,53248,53248,53248,53248,3233857728,353703189,57,3235120384,64,7488,192,1056915199,16780300,50465804,576,36,128,1130102784,1130233856,1107296256,1128267776,1128267776,1128267776,1780992,1797120,264960,32768,40960,61440,9498624,9584640,1413120,1128219475,1129333599,4294939148,190,1128219475,1129333599,4294939148,190,1128153584,1129464328,4294964108,190,
1935360,1935360,1978368,1978368,2021376,2021376,2064384,2064384,2064384,2064384,2064384,2064384,40960,44032,48128,51200,55296,58368,62464,65536,62464,65536,62464,65536,268439552,336991254,57,812257280,80,7488,192,1040137855,16780300,50465804,576,36,224,1110966272,1110966272,1110966272,1098907648,1098907648,0,371520,1073741824,370944,44032,44032,40960,1981440,1073741824,1978368,16948,1107575360,4294958508,4294967294,16948,1107575360,4294958508,4294967294,16948,1107575360,4294958508,4294967294,
3072,3072,3264,3264,3456,3456,3648,3648,3840,3840,4032,4032,40960,44032,48128,51200,55296,58368,62464,65536,69632,72704,76800,79872,268439552,336991254,410,1084894208,64,7984,43008,1040137983,50465804,16780300,36,80,224,1099956224,1099956224,1099956224,1098907648,1098907648,0,129672,1073741824,648,51200,51200,48128,691584,1073741824,3456,16768,1107575208,4294965004,4294967294,16768,1107575208,4294965004,4294967294,16768,1107575208,4294965004,4294967294,
6064128,6064128,6107136,6107136,6150144,6150144,6193152,6193152,6193152,6193152,6193152,6193152,40960,44032,48128,51200,55296,58368,62464,65536,62464,65536,62464,65536,268439552,336991254,57,1613369344,80,7488,192,1048526335,16780300,50465804,576,36,224,1124990976,1124990976,1124990976,1098907648,1098907648,0,1145664,1073741824,1145088,44032,44032,40960,6110208,1073741824,6107136,17165,1107575568,4294937004,4294967294,17165,1107575568,4294937004,4294967294,17165,1107575568,4294937004,4294967294,
10944,10944,11136,11136,11328,11328,11520,11520,11520,11520,11520,11520,40960,44032,48128,51200,55296,58368,62464,65536,62464,65536,62464,65536,268439552,336991254,410,1886527488,80,7984,43008,1048526463,50465804,16780300,36,80,224,1114112000,1114112000,1114112000,1098907648,1098907648,0,131112,1073741824,2088,44032,44032,40960,699264,1073741824,11136,16996,1107575408,4294955820,4294967294,16996,1107575408,4294955820,4294967294,16996,1107575408,4294955820,4294967294,
20544,20544,20736,20736,20928,20928,21120,21120,21120,21120,21120,21120,40960,44032,48128,51200,55296,58368,62464,65536,62464,65536,62464,65536,268439552,336991254,410,2159681536,80,7984,43008,1048526591,50465804,16780300,36,80,224,1121452032,1121452032,1121452032,1098907648,1098907648,0,132912,1073741824,3888,44032,44032,40960,708864,1073741824,20736,17110,1107575516,4294944620,4294967294,17110,1107575516,4294944620,4294967294,17110,1107575516,4294944620,4294967294,
28608,28608,28800,28800,28992,28992,29184,29184,29184,29184,29184,29184,40960,44032,48128,51200,55296,58368,62464,65536,62464,65536,62464,65536,268439552,336991254,410,2689212416,80,7984,43008,1056915071,50465804,16780300,36,80,224,1125515264,1125515264,1125515264,1098907648,1098907648,0,134424,1073741824,5400,44032,44032,40960,716928,1073741824,28800,17173,1107575576,4294935212,4294967294,17173,1107575576,4294935212,4294967294,17173,1107575576,4294935212,4294967294,
38592,38592,38784,38784,38976,38976,39168,39168,39168,39168,39168,39168,40960,44032,48128,51200,55296,58368,62464,65536,62464,65536,62464,65536,268439552,336991254,410,2962366464,80,7984,43008,1056915199,50465804,16780300,36,80,224,1128923136,1128923136,1128923136,1098907648,1098907648,0,136296,1073741824,7272,44032,44032,40960,726912,1073741824,38784,17225,1107575628,4294923564,4294967294,17225,1107575628,4294923564,4294967294,17225,1107575628,4294923564,4294967294,
8816640,8859648,8902656,8945664,8988672,9031680,9074688,9117696,9117696,9117696,9117696,9117696,40960,45056,49152,53248,57344,61440,65536,69632,69632,69632,69632,69632,538976288,353703189,57,3491889408,80,7488,192,1056914943,16780300,50465804,576,36,128,1129447424,1129316352,1129185280,1107296256,1107296256,1107296256,1694592,1678464,1662336,57344,49152,40960,9037824,8951808,8865792,1106264909,1112032084,4294942156,30,1106264909,1112032084,4294942156,30,1106264909,1112032084,4294942156,30,
2021376,2021376,2064384,2064384,2107392,2107392,2150400,2150400,2150400,2150400,2150400,2150400,40960,44032,48128,51200,55296,58368,62464,65536,62464,65536,62464,65536,268439552,336991254,57,812781568,80,7488,192,1040137855,16780300,50465804,576,36,224,1111490560,1111490560,1111490560,1098907648,1098907648,0,387648,1073741824,387072,44032,44032,40960,2067456,1073741824,2064384,16956,1107575368,4294958060,4294967294,16956,1107575368,4294958060,4294967294,16956,1107575368,4294958060,4294967294,
3456,3456,3648,3648,3840,3840,4032,4032,4224,4224,4416,4416,40960,44032,48128,51200,55296,58368,62464,65536,69632,72704,76800,79872,268439552,336991254,410,1085418496,64,7984,43008,1040137983,50465804,16780300,36,80,224,1101004800,1101004800,1101004800,1098907648,1098907648,0,129744,1073741824,720,51200,51200,48128,691968,1073741824,3840,16784,1107575224,4294964556,4294967294,16784,1107575224,4294964556,4294967294,16784,1107575224,4294964556,4294967294,
4171776,4214784,4257792,4300800,4343808,4386816,4429824,4472832,4472832,4472832,4472832,4472832,40960,45056,49152,53248,57344,61440,65536,69632,69632,69632,69632,69632,538976288,353703189,57,1613889792,80,7488,192,1048526335,16780300,50465804,576,36,128,1120665600,1120403456,1120141312,1107296256,1107296256,1107296256,823680,807552,791424,57344,49152,40960,4392960,4306944,4220928,1106264770,1112031952,4294955980,30,1106264770,1112031952,4294955980,30,1106264770,1112031952,4294955980,30,
11328,11328,11520,11520,11712,11712,11904,11904,11904,11904,11904,11904,40960,44032,48128,51200,55296,58368,62464,65536,62464,65536,62464,65536,268439552,336991254,410,1887051776,80,7984,43008,1048526463,50465804,16780300,36,80,224,1114636288,1114636288,1114636288,1098907648,1098907648,0,131184,1073741824,2160,44032,44032,40960,699648,1073741824,11520,17004,1107575416,4294955372,4294967294,17004,1107575416,4294955372,4294967294,17004,1107575416,4294955372,4294967294,
1032192,1075200,1118208,1161216,1204224,1247232,8730624,8773632,8773632,8773632,8773632,8773632,49152,53248,57344,61440,65536,69632,40960,45056,45056,45056,45056,45056,2155872256,320017685,57,2160209920,80,7488,192,1048526591,16780300,50465804,576,36,128,1128923136,1128923136,1104150528,1124073472,1124073472,0,1633536,1073741824,209664,32768,32768,53248,8712192,1073741824,1118208,1123959617,1125139276,4294941836,126,1123959617,1125139276,4294941836,126,16832,1099973096,4294965772,4294967294,
7010304,7053312,7096320,7139328,7182336,7225344,7268352,7311360,7311360,7311360,7311360,7311360,40960,45056,49152,53248,57344,61440,65536,69632,69632,69632,69632,69632,538976288,353703189,57,2689728768,80,7488,192,1056915071,16780300,50465804,576,36,128,1126694912,1126563840,1126432768,1107296256,1107296256,1107296256,1355904,1339776,1323648,57344,49152,40960,7231488,7145472,7059456,1106264867,1112032042,4294947532,30,1106264867,1112032042,4294947532,30,1106264867,1112032042,4294947532,30,
38976,38976,39168,39168,39360,39360,39552,39552,39552,39552,39552,39552,40960,44032,48128,51200,55296,58368,62464,65536,62464,65536,62464,65536,268439552,336991254,410,2962890752,80,7984,43008,1056915199,50465804,16780300,36,80,224,1129054208,1129054208,1129054208,1098907648,1098907648,0,136368,1073741824,7344,44032,44032,40960,727296,1073741824,39168,17227,1107575630,4294923116,4294967294,17227,1107575630,4294923116,4294967294,17227,1107575630,4294923116,4294967294,
8042496,8042496,8085504,8085504,8128512,8128512,8171520,8171520,8171520,8171520,8171520,8171520,40960,44032,48128,51200,55296,58368,62464,65536,62464,65536,62464,65536,268439552,336991254,57,3492417536,80,7488,192,1056914943,16780300,50465804,576,36,224,1128005632,1128005632,1128005632,1098907648,1098907648,0,1516608,1073741824,1516032,44032,44032,40960,8088576,1073741824,8085504,17211,1107575614,4294926700,4294967294,17211,1107575614,4294926700,4294967294,17211,1107575614,4294926700,4294967294,
2107392,2107392,2150400,2150400,2193408,2193408,2236416,2236416,2236416,2236416,2236416,2236416,40960,44032,48128,51200,55296,58368,62464,65536,62464,65536,62464,65536,268439552,336991254,57,813305856,80,7488,192,1040137855,16780300,50465804,576,36,224,1112014848,1112014848,1112014848,1098907648,1098907648,0,403776,1073741824,403200,44032,44032,40960,2153472,1073741824,2150400,16964,1107575376,4294957612,4294967294,16964,1107575376,4294957612,4294967294,16964,1107575376,4294957612,4294967294,
3840,3840,4032,4032,4224,4224,4416,4416,4608,4608,4800,4800,40960,44032,48128,51200,55296,58368,62464,65536,69632,72704,76800,79872,268439552,336991254,410,1085942784,64,7984,43008,1040137983,50465804,16780300,36,80,224,1102053376,1102053376,1102053376,1098907648,1098907648,0,129816,1073741824,792,51200,51200,48128,692352,1073741824,4224,16800,1107575240,4294964108,4294967294,16800,1107575240,4294964108,4294967294,16800,1107575240,4294964108,4294967294,
6150144,6150144,6193152,6193152,6236160,6236160,6279168,6279168,6279168,6279168,6279168,6279168,40960,44032,48128,51200,55296,58368,62464,65536,62464,65536,62464,65536,268439552,336991254,57,1614417920,80,7488,192,1048526335,16780300,50465804,576,36,224,1125122048,1125122048,1125122048,1098907648,1098907648,0,1161792,1073741824,1161216,44032,44032,40960,6196224,1073741824,6193152,17167,1107575570,4294936556,4294967294,17167,1107575570,4294936556,4294967294,17167,1107575570,4294936556,4294967294,
5203968,5246976,5289984,5332992,5376000,5419008,5462016,5505024,5505024,5505024,5505024,5505024,40960,45056,49152,53248,57344,61440,65536,69632,69632,69632,69632,69632,538976288,353703189,57,1887568128,80,7488,192,1048526591,16780300,50465804,576,36,128,1123811328,1123549184,1123287040,1107296256,1107296256,1107296256,1017216,1001088,984960,57344,49152,40960,5425152,5339136,5253120,1106264818,1112032000,4294952908,30,1106264818,1112032000,4294952908,30,1106264818,1112032000,4294952908,30,
1118208,1161216,1204224,1247232,1290240,1333248,9160704,9203712,9203712,9203712,9203712,9203712,49152,53248,57344,61440,65536,69632,40960,45056,45056,45056,45056,45056,2155872256,320017685,57,2160865280,80,7488,192,1048526591,16780300,50465804,576,36,128,1129578496,1129578496,1105199104,1124073472,1124073472,0,1714176,1073741824,225792,32768,32768,53248,9142272,1073741824,1204224,1123959627,1125139286,4294940556,126,1123959627,1125139286,4294940556,126,16848,1099973112,4294965516,4294967294,
28992,28992,29184,29184,29376,29376,29568,29568,29568,29568,29568,29568,40960,44032,48128,51200,55296,58368,62464,65536,62464,65536,62464,65536,268439552,336991254,410,2690260992,80,7984,43008,1056915071,50465804,16780300,36,80,224,1125646336,1125646336,1125646336,1098907648,1098907648,0,134496,1073741824,5472,44032,44032,40960,717312,1073741824,29184,17175,1107575578,4294934764,4294967294,17175,1107575578,4294934764,4294967294,17175,1107575578,4294934764,4294967294,
39360,39360,39552,39552,39744,39744,39936,39936,39936,39936,39936,39936,40960,44032,48128,51200,55296,58368,62464,65536,62464,65536,62464,65536,268439552,336991254,410,2963415040,80,7984,43008,1056915199,50465804,16780300,36,80,224,1129185280,1129185280,1129185280,1098907648,1098907648,0,136440,1073741824,7416,44032,44032,40960,727680,1073741824,39552,17229,1107575632,4294922668,4294967294,17229,1107575632,4294922668,4294967294,17229,1107575632,4294922668,4294967294,
8128512,8128512,8171520,8171520,8214528,8214528,8257536,8257536,8257536,8257536,8257536,8257536,40960,44032,48128,51200,55296,58368,62464,65536,62464,65536,62464,65536,268439552,336991254,57,3492941824,80,7488,192,1056914943,16780300,50465804,576,36,224,1128136704,1128136704,1128136704,1098907648,1098907648,0,1532736,1073741824,1532160,44032,44032,40960,8174592,1073741824,8171520,17213,1107575616,4294926252,4294967294,17213,1107575616,4294926252,4294967294,17213,1107575616,4294926252,4294967294,
2193408,2193408,2236416,2236416,2279424,2279424,2322432,2322432,2322432,2322432,2322432,2322432,40960,44032,48128,51200,55296,58368,62464,65536,62464,65536,62464,65536,268439552,336991254,57,813830144,80,7488,192,1040137983,16780300,50465804,576,36,224,1112539136,1112539136,1112539136,1098907648,1098907648,0,419904,1073741824,419328,44032,44032,40960,2239488,1073741824,2236416,16972,1107575384,4294957164,4294967294,16972,1107575384,4294957164,4294967294,16972,1107575384,4294957164,4294967294,
4224,4224,4416,4416,4608,4608,4800,4800,4992,4992,5184,5184,40960,44032,48128,51200,55296,58368,62464,65536,69632,72704,76800,79872,268439552,336991254,410,1086467072,64,7984,43008,1040137983,50465804,16780300,36,80,224,1103101952,1103101952,1103101952,1098907648,1098907648,0,129888,1073741824,864,51200,51200,48128,692736,1073741824,4608,16816,1107575256,4294963660,4294967294,16816,1107575256,4294963660,4294967294,16816,1107575256,4294963660,4294967294,
6236160,6236160,6279168,6279168,6322176,6322176,6365184,6365184,6365184,6365184,6365184,6365184,40960,44032,48128,51200,55296,58368,62464,65536,62464,65536,62464,65536,268439552,336991254,57,1614942208,80,7488,192,1048526463,16780300,50465804,576,36,224,1125253120,1125253120,1125253120,1098907648,1098907648,0,1177920,1073741824,1177344,44032,44032,40960,6282240,1073741824,6279168,17169,1107575572,4294936108,4294967294,17169,1107575572,4294936108,4294967294,17169,1107575572,4294936108,4294967294,
11712,11712,11904,11904,12096,12096,12288,12288,12288,12288,12288,12288,40960,44032,48128,51200,55296,58368,62464,65536,62464,65536,62464,65536,268439552,336991254,410,1888100352,80,7984,43008,1048526591,50465804,16780300,36,80,224,1115160576,1115160576,1115160576,1098907648,1098907648,0,131256,1073741824,2232,44032,44032,40960,700032,1073741824,11904,17012,1107575424,4294954924,4294967294,17012,1107575424,4294954924,4294967294,17012,1107575424,4294954924,4294967294,
1204224,1247232,1290240,1333248,1376256,1419264,9590784,9590784,9590784,9590784,9590784,9590784,49152,53248,57344,61440,65536,69632,40960,45056,45056,45056,45056,45056,2155872256,320017685,57,2161520640,80,7488,192,1048526591,16780300,50465804,576,36,128,1130233856,1130233856,1106247680,1124073472,1124073472,0,1794816,1073741824,241920,32768,32768,53248,9572352,1073741824,1290240,1123959637,1125139295,4294939276,126,1123959637,1125139295,4294939276,126,16864,1099973124,4294965260,4294967294,
29376,29376,29568,29568,29760,29760,29952,29952,29952,29952,29952,29952,40960,44032,48128,51200,55296,58368,62464,65536,62464,65536,62464,65536,268439552,336991254,410,2690785280,80,7984,43008,1056915071,50465804,16780300,36,80,224,1125777408,1125777408,1125777408,1098907648,1098907648,0,134568,1073741824,5544,44032,44032,40960,717696,1073741824,29568,17177,1107575580,4294934316,4294967294,17177,1107575580,4294934316,4294967294,17177,1107575580,4294934316,4294967294,
8042496,8085504,8128512,8171520,8214528,8257536,8300544,8343552,8343552,8343552,8343552,8343552,40960,45056,49152,53248,57344,61440,65536,69632,69632,69632,69632,69632,538976288,353703189,57,2963931392,80,7488,192,1056915199,16780300,50465804,576,36,128,1128267776,1128136704,1128005632,1107296256,1107296256,1107296256,1549440,1533312,1517184,57344,49152,40960,8263680,8177664,8091648,1106264891,1112032066,4294944460,30,1106264891,1112032066,4294944460,30,1106264891,1112032066,4294944460,30,
8214528,8214528,8257536,8257536,8300544,8300544,8343552,8343552,8343552,8343552,8343552,8343552,40960,44032,48128,51200,55296,58368,62464,65536,62464,65536,62464,65536,268439552,336991254,57,3493466112,80,7488,192,1056914943,16780300,50465804,576,36,224,1128267776,1128267776,1128267776,1098907648,1098907648,0,1548864,1073741824,1548288,44032,44032,40960,8260608,1073741824,8257536,17215,1107575618,4294925804,4294967294,17215,1107575618,4294925804,4294967294,17215,1107575618,4294925804,4294967294,
1849344,1892352,1935360,1978368,2021376,2064384,2107392,2150400,2150400,2150400,2150400,2150400,40960,45056,49152,53248,57344,61440,65536,69632,69632,69632,69632,69632,538976288,353703189,57,814350592,80,7488,192,1040137983,16780300,50465804,576,36,128,1111490560,1110966272,1110441984,1107296256,1107296256,1107296256,388224,372096,355968,57344,49152,40960,2070528,1984512,1898496,1106264620,1112031816,4294962892,30,1106264620,1112031816,4294962892,30,1106264620,1112031816,4294962892,30,
4085760,4085760,4128768,4128768,4171776,4171776,4214784,4214784,4214784,4214784,4214784,4214784,40960,44032,48128,51200,55296,58368,62464,65536,62464,65536,62464,65536,268439552,336991254,57,1086984192,80,7488,192,1040137983,16780300,50465804,576,36,224,1119879168,1119879168,1119879168,1098907648,1098907648,0,774720,1073741824,774144,44032,44032,40960,4131840,1073741824,4128768,17086,1107575492,4294947308,4294967294,17086,1107575492,4294947308,4294967294,17086,1107575492,4294947308,4294967294,
6322176,6322176,6365184,6365184,6408192,6408192,6451200,6451200,6451200,6451200,6451200,6451200,40960,44032,48128,51200,55296,58368,62464,65536,62464,65536,62464,65536,268439552,336991254,57,1615466496,80,7488,192,1048526463,16780300,50465804,576,36,224,1125384192,1125384192,1125384192,1098907648,1098907648,0,1194048,1073741824,1193472,44032,44032,40960,6368256,1073741824,6365184,17171,1107575574,4294935660,4294967294,17171,1107575574,4294935660,4294967294,17171,1107575574,4294935660,4294967294,
12096,12096,12288,12288,12480,12480,12672,12672,12672,12672,12672,12672,40960,44032,48128,51200,55296,58368,62464,65536,62464,65536,62464,65536,268439552,336991254,410,1888624640,80,7984,43008,1048526591,50465804,16780300,36,80,224,1115684864,1115684864,1115684864,1098907648,1098907648,0,131328,1073741824,2304,44032,44032,40960,700416,1073741824,12288,17020,1107575428,4294954476,4294967294,17020,1107575428,4294954476,4294967294,17020,1107575428,4294954476,4294967294,
20928,20928,21120,21120,21312,21312,21504,21504,21504,21504,21504,21504,40960,44032,48128,51200,55296,58368,62464,65536,62464,65536,62464,65536,268439552,336991254,410,2418155520,80,7984,43008,1056914943,50465804,16780300,36,80,224,1121714176,1121714176,1121714176,1098907648,1098907648,0,132984,1073741824,3960,44032,44032,40960,709248,1073741824,21120,17114,1107575520,4294944172,4294967294,17114,1107575520,4294944172,4294967294,17114,1107575520,4294944172,4294967294,
29760,29760,29952,29952,30144,30144,30336,30336,30336,30336,30336,30336,40960,44032,48128,51200,55296,58368,62464,65536,62464,65536,62464,65536,268439552,336991254,410,2691309568,80,7984,43008,1056915071,50465804,16780300,36,80,224,1125908480,1125908480,1125908480,1098907648,1098907648,0,134640,1073741824,5616,44032,44032,40960,718080,1073741824,29952,17179,1107575582,4294933868,4294967294,17179,1107575582,4294933868,4294967294,17179,1107575582,4294933868,4294967294,
39744,39744,39936,39936,40128,40128,40320,40320,40320,40320,40320,40320,40960,44032,48128,51200,55296,58368,62464,65536,62464,65536,62464,65536,268439552,336991254,410,2964463616,80,7984,43008,1056915199,50465804,16780300,36,80,224,1129316352,1129316352,1129316352,1098907648,1098907648,0,136512,1073741824,7488,44032,44032,40960,728064,1073741824,39936,17231,1107575634,4294922220,4294967294,17231,1107575634,4294922220,4294967294,17231,1107575634,4294922220,4294967294,
8300544,8300544,8343552,8343552,8386560,8386560,8429568,8429568,8429568,8429568,8429568,8429568,40960,44032,48128,51200,55296,58368,62464,65536,62464,65536,62464,65536,268439552,336991254,57,3493990400,80,7488,192,1056915071,16780300,50465804,576,36,224,1128398848,1128398848,1128398848,1098907648,1098907648,0,1564992,1073741824,1564416,44032,44032,40960,8346624,1073741824,8343552,17217,1107575620,4294925356,4294967294,17217,1107575620,4294925356,4294967294,17217,1107575620,4294925356,4294967294,
2279424,2279424,2322432,2322432,2365440,2365440,2408448,2408448,2408448,2408448,2408448,2408448,40960,44032,48128,51200,55296,58368,62464,65536,62464,65536,62464,65536,268439552,336991254,57,814878720,80,7488,192,1040137983,16780300,50465804,576,36,224,1113063424,1113063424,1113063424,1098907648,1098907648,0,436032,1073741824,435456,44032,44032,40960,2325504,1073741824,2322432,16980,1107575392,4294956716,4294967294,16980,1107575392,4294956716,4294967294,16980,1107575392,4294956716,4294967294,
3139584,3182592,3225600,3268608,3311616,3354624,3397632,3440640,3440640,3440640,3440640,3440640,40960,45056,49152,53248,57344,61440,65536,69632,69632,69632,69632,69632,538976288,353703189,57,1087504640,80,7488,192,1040137983,16780300,50465804,576,36,128,1117519872,1117257728,1116995584,1107296256,1107296256,1107296256,630144,614016,597888,57344,49152,40960,3360768,3274752,3188736,1106264722,1112031904,4294959052,30,1106264722,1112031904,4294959052,30,1106264722,1112031904,4294959052,30,
6408192,6408192,6451200,6451200,6494208,6494208,6537216,6537216,6537216,6537216,6537216,6537216,40960,44032,48128,51200,55296,58368,62464,65536,62464,65536,62464,65536,268439552,336991254,57,1615990784,80,7488,192,1048526463,16780300,50465804,576,36,224,1125515264,1125515264,1125515264,1098907648,1098907648,0,1210176,1073741824,1209600,44032,44032,40960,6454272,1073741824,6451200,17173,1107575576,4294935212,4294967294,17173,1107575576,4294935212,4294967294,17173,1107575576,4294935212,4294967294,
12480,12480,12672,12672,12864,12864,13056,13056,13056,13056,13056,13056,40960,44032,48128,51200,55296,58368,62464,65536,62464,65536,62464,65536,268439552,336991254,410,1889148928,80,7984,43008,1048526591,50465804,16780300,36,80,224,1115947008,1115947008,1115947008,1098907648,1098907648,0,131400,1073741824,2376,44032,44032,40960,700800,1073741824,12672,17026,1107575432,4294954028,4294967294,17026,1107575432,4294954028,4294967294,17026,1107575432,4294954028,4294967294,
21312,21312,21504,21504,21696,21696,21888,21888,21888,21888,21888,21888,40960,44032,48128,51200,55296,58368,62464,65536,62464,65536,62464,65536,268439552,336991254,410,2418679808,80,7984,43008,1056914943,50465804,16780300,36,80,224,1121976320,1121976320,1121976320,1098907648,1098907648,0,133056,1073741824,4032,44032,44032,40960,709632,1073741824,21504,17118,1107575524,4294943724,4294967294,17118,1107575524,4294943724,4294967294,17118,1107575524,4294943724,4294967294,
30144,30144,30336,30336,30528,30528,30720,30720,30720,30720,30720,30720,40960,44032,48128,51200,55296,58368,62464,65536,62464,65536,62464,65536,268439552,336991254,410,2691833856,80,7984,43008,1056915071,50465804,16780300,36,80,224,1126039552,1126039552,1126039552,1098907648,1098907648,0,134712,1073741824,5688,44032,44032,40960,718464,1073741824,30336,17181,1107575584,4294933420,4294967294,17181,1107575584,4294933420,4294967294,17181,1107575584,4294933420,4294967294,
40128,40128,40320,40320,40512,40512,40704,40704,40704,40704,40704,40704,40960,44032,48128,51200,55296,58368,62464,65536,62464,65536,62464,65536,268439552,336991254,410,2964987904,80,7984,43008,1056915199,50465804,16780300,36,80,224,1129447424,1129447424,1129447424,1098907648,1098907648,0,136584,1073741824,7560,44032,44032,40960,728448,1073741824,40320,17233,1107575636,4294921772,4294967294,17233,1107575636,4294921772,4294967294,17233,1107575636,4294921772,4294967294,
8386560,8386560,8429568,8429568,8472576,8472576,8515584,8515584,8515584,8515584,8515584,8515584,40960,44032,48128,51200,55296,58368,62464,65536,62464,65536,62464,65536,268439552,336991254,57,3494514688,80,7488,192,1056915071,16780300,50465804,576,36,224,1128529920,1128529920,1128529920,1098907648,1098907648,0,1581120,1073741824,1580544,44032,44032,40960,8432640,1073741824,8429568,17219,1107575622,4294924908,4294967294,17219,1107575622,4294924908,4294967294,17219,1107575622,4294924908,4294967294,
0,43008,86016,129024,172032,215040,215040,215040,215040,215040,215040,215040,40960,45056,49152,53248,57344,61440,61440,61440,61440,61440,61440,61440,0,353703189,57,815444480,80,7488,192,1040137983,16780300,50465804,576,36,128,1073741824,1073741824,1073741824,0,0,0,1073741824,1073741824,16128,45056,45056,45056,1073741824,1073741824,86016,0,1099972768,1292,4294967294,0,1099972768,1292,4294967294,0,1099972768,1292,4294967294,
4171776,4171776,4214784,4214784,4257792,4257792,4300800,4300800,4300800,4300800,4300800,4300800,40960,44032,48128,51200,55296,58368,62464,65536,62464,65536,62464,65536,268439552,336991254,57,1088032768,80,7488,192,1040137983,16780300,50465804,576,36,224,1120141312,1120141312,1120141312,1098907648,1098907648,0,790848,1073741824,790272,44032,44032,40960,4217856,1073741824,4214784,17090,1107575496,4294946860,4294967294,17090,1107575496,4294946860,4294967294,17090,1107575496,4294946860,4294967294,
6494208,6494208,6537216,6537216,6580224,6580224,6623232,6623232,6623232,6623232,6623232,6623232,40960,44032,48128,51200,55296,58368,62464,65536,62464,65536,62464,65536,268439552,336991254,57,1616515072,80,7488,192,1048526463,16780300,50465804,576,36,224,1125646336,1125646336,1125646336,1098907648,1098907648,0,1226304,1073741824,1225728,44032,44032,40960,6540288,1073741824,6537216,17175,1107575578,4294934764,4294967294,17175,1107575578,4294934764,4294967294,17175,1107575578,4294934764,4294967294,
12864,12864,13056,13056,13248,13248,13440,13440,13440,13440,13440,13440,40960,44032,48128,51200,55296,58368,62464,65536,62464,65536,62464,65536,268439552,336991254,410,1889673216,80,7984,43008,1048526591,50465804,16780300,36,80,224,1116209152,1116209152,1116209152,1098907648,1098907648,0,131472,1073741824,2448,44032,44032,40960,701184,1073741824,13056,17030,1107575436,4294953580,4294967294,17030,1107575436,4294953580,4294967294,17030,1107575436,4294953580,4294967294,
21696,21696,21888,21888,22080,22080,22272,22272,22272,22272,22272,22272,40960,44032,48128,51200,55296,58368,62464,65536,62464,65536,62464,65536,268439552,336991254,410,2419204096,80,7984,43008,1056914943,50465804,16780300,36,80,224,1122238464,1122238464,1122238464,1098907648,1098907648,0,133128,1073741824,4104,44032,44032,40960,710016,1073741824,21888,17122,1107575528,4294943276,4294967294,17122,1107575528,4294943276,4294967294,17122,1107575528,4294943276,4294967294,
30528,30528,30720,30720,30912,30912,31104,31104,31104,31104,31104,31104,40960,44032,48128,51200,55296,58368,62464,65536,62464,65536,62464,65536,268439552,336991254,410,2692358144,80,7984,43008,1056915071,50465804,16780300,36,80,224,1126170624,1126170624,1126170624,1098907648,1098907648,0,134784,1073741824,5760,44032,44032,40960,718848,1073741824,30720,17183,1107575586,4294932972,4294967294,17183,1107575586,4294932972,4294967294,17183,1107575586,4294932972,4294967294,
40512,40512,40704,40704,40896,40896,41088,41088,41088,41088,41088,41088,40960,44032,48128,51200,55296,58368,62464,65536,62464,65536,62464,65536,268439552,336991254,410,2965512192,80,7984,43008,1056915199,50465804,16780300,36,80,224,1129578496,1129578496,1129578496,1098907648,1098907648,0,136656,1073741824,7632,44032,44032,40960,728832,1073741824,40704,17235,1107575638,4294921324,4294967294,17235,1107575638,4294921324,4294967294,17235,1107575638,4294921324,4294967294,
8472576,8472576,8515584,8515584,8558592,8558592,8601600,8601600,8601600,8601600,8601600,8601600,40960,44032,48128,51200,55296,58368,62464,65536,62464,65536,62464,65536,268439552,336991254,57,3495038976,80,7488,192,1056915071,16780300,50465804,576,36,224,1128660992,1128660992,1128660992,1098907648,1098907648,0,1597248,1073741824,1596672,44032,44032,40960,8518656,1073741824,8515584,17221,1107575624,4294924460,4294967294,17221,1107575624,4294924460,4294967294,17221,1107575624,4294924460,4294967294,
2365440,2365440,2408448,2408448,2451456,2451456,2494464,2494464,2494464,2494464,2494464,2494464,40960,44032,48128,51200,55296,58368,62464,65536,62464,65536,62464,65536,268439552,336991254,57,815927296,80,7488,192,1040137983,16780300,50465804,576,36,224,1113587712,1113587712,1113587712,1098907648,1098907648,0,452160,1073741824,451584,44032,44032,40960,2411520,1073741824,2408448,16988,1107575400,4294956268,4294967294,16988,1107575400,4294956268,4294967294,16988,1107575400,4294956268,4294967294,
4257792,4257792,4300800,4300800,4343808,4343808,4386816,4386816,4386816,4386816,4386816,4386816,40960,44032,48128,51200,55296,58368,62464,65536,62464,65536,62464,65536,268439552,336991254,57,1344409600,80,7488,192,1048526335,16780300,50465804,576,36,224,1120403456,1120403456,1120403456,1098907648,1098907648,0,806976,1073741824,806400,44032,44032,40960,4303872,1073741824,4300800,17094,1107575500,4294946412,4294967294,17094,1107575500,4294946412,4294967294,17094,1107575500,4294946412,4294967294,
6580224,6580224,6623232,6623232,6666240,6666240,6709248,6709248,6709248,6709248,6709248,6709248,40960,44032,48128,51200,55296,58368,62464,65536,62464,65536,62464,65536,268439552,336991254,57,1617039360,80,7488,192,1048526463,16780300,50465804,576,36,224,1125777408,1125777408,1125777408,1098907648,1098907648,0,1242432,1073741824,1241856,44032,44032,40960,6626304,1073741824,6623232,17177,1107575580,4294934316,4294967294,17177,1107575580,4294934316,4294967294,17177,1107575580,4294934316,4294967294,
13248,13248,13440,13440,13632,13632,13824,13824,13824,13824,13824,13824,40960,44032,48128,51200,55296,58368,62464,65536,62464,65536,62464,65536,268439552,336991254,410,1890197504,80,7984,43008,1048526591,50465804,16780300,36,80,224,1116471296,1116471296,1116471296,1098907648,1098907648,0,131544,1073741824,2520,44032,44032,40960,701568,1073741824,13440,17034,1107575440,4294953132,4294967294,17034,1107575440,4294953132,4294967294,17034,1107575440,4294953132,4294967294,
6236160,6279168,6322176,6365184,6408192,6451200,6494208,6537216,6537216,6537216,6537216,6537216,40960,45056,49152,53248,57344,61440,65536,69632,69632,69632,69632,69632,538976288,353703189,57,2419720448,80,7488,192,1056914943,16780300,50465804,576,36,128,1125515264,1125384192,1125253120,1107296256,1107296256,1107296256,1210752,1194624,1178496,57344,49152,40960,6457344,6371328,6285312,1106264849,1112032024,4294949836,30,1106264849,1112032024,4294949836,30,1106264849,1112032024,4294949836,30,
30912,30912,31104,31104,31296,31296,31488,31488,31488,31488,31488,31488,40960,44032,48128,51200,55296,58368,62464,65536,62464,65536,62464,65536,268439552,336991254,410,2692882432,80,7984,43008,1056915199,50465804,16780300,36,80,224,1126301696,1126301696,1126301696,1098907648,1098907648,0,134856,1073741824,5832,44032,44032,40960,719232,1073741824,31104,17185,1107575588,4294932524,4294967294,17185,1107575588,4294932524,4294967294,17185,1107575588,4294932524,4294967294,
344064,387072,430080,473088,516096,559104,9074688,9117696,9117696,9117696,9117696,9117696,49152,53248,57344,61440,65536,69632,40960,45056,45056,45056,45056,45056,2964324352,320017685,57,2966041088,80,7488,192,1056915199,16780300,50465804,576,36,128,1129447424,1129447424,1092616192,1127219200,1127219200,0,1699776,1073741824,80640,32768,32768,53248,9065472,1073741824,430080,1127170889,1128285012,4294940524,174,1127170889,1128285012,4294940524,174,16640,1099972944,524,4294967294,
8558592,8558592,8601600,8601600,8644608,8644608,8687616,8687616,8687616,8687616,8687616,8687616,40960,44032,48128,51200,55296,58368,62464,65536,62464,65536,62464,65536,268439552,336991254,57,3495563264,80,7488,192,1056915071,16780300,50465804,576,36,224,1128792064,1128792064,1128792064,1098907648,1098907648,0,1613376,1073741824,1612800,44032,44032,40960,8604672,1073741824,8601600,17223,1107575626,4294924012,4294967294,17223,1107575626,4294924012,4294967294,17223,1107575626,4294924012,4294967294,
2451456,2451456,2494464,2494464,2537472,2537472,2580480,2580480,2580480,2580480,2580480,2580480,40960,44032,48128,51200,55296,58368,62464,65536,62464,65536,62464,65536,268439552,336991254,57,816451584,80,7488,192,1040137983,16780300,50465804,576,36,224,1114112000,1114112000,1114112000,1098907648,1098907648,0,468288,1073741824,467712,44032,44032,40960,2497536,1073741824,2494464,16996,1107575408,4294955820,4294967294,16996,1107575408,4294955820,4294967294,16996,1107575408,4294955820,4294967294,
4343808,4343808,4386816,4386816,4429824,4429824,4472832,4472832,4472832,4472832,4472832,4472832,40960,44032,48128,51200,55296,58368,62464,65536,62464,65536,62464,65536,268439552,336991254,57,1344933888,80,7488,192,1048526335,16780300,50465804,576,36,224,1120665600,1120665600,1120665600,1098907648,1098907648,0,823104,1073741824,822528,44032,44032,40960,4389888,1073741824,4386816,17098,1107575504,4294945964,4294967294,17098,1107575504,4294945964,4294967294,17098,1107575504,4294945964,4294967294,
6666240,6666240,6709248,6709248,6752256,6752256,6795264,6795264,6795264,6795264,6795264,6795264,40960,44032,48128,51200,55296,58368,62464,65536,62464,65536,62464,65536,268439552,336991254,57,1617563648,80,7488,192,1048526463,16780300,50465804,576,36,224,1125908480,1125908480,1125908480,1098907648,1098907648,0,1258560,1073741824,1257984,44032,44032,40960,6712320,1073741824,6709248,17179,1107575582,4294933868,4294967294,17179,1107575582,4294933868,4294967294,17179,1107575582,4294933868,4294967294,
13632,13632,13824,13824,14016,14016,14208,14208,14208,14208,14208,14208,40960,44032,48128,51200,55296,58368,62464,65536,62464,65536,62464,65536,268439552,336991254,410,1890721792,80,7984,43008,1048526591,50465804,16780300,36,80,224,1116733440,1116733440,1116733440,1098907648,1098907648,0,131616,1073741824,2592,44032,44032,40960,701952,1073741824,13824,17038,1107575444,4294952684,4294967294,17038,1107575444,4294952684,4294967294,17038,1107575444,4294952684,4294967294,
22080,22080,22272,22272,22464,22464,22656,22656,22656,22656,22656,22656,40960,44032,48128,51200,55296,58368,62464,65536,62464,65536,62464,65536,268439552,336991254,410,2420252672,80,7984,43008,1056915071,50465804,16780300,36,80,224,1122500608,1122500608,1122500608,1098907648,1098907648,0,133200,1073741824,4176,44032,44032,40960,710400,1073741824,22272,17126,1107575532,4294942828,4294967294,17126,1107575532,4294942828,4294967294,17126,1107575532,4294942828,4294967294,
31296,31296,31488,31488,31680,31680,31872,31872,31872,31872,31872,31872,40960,44032,48128,51200,55296,58368,62464,65536,62464,65536,62464,65536,268439552,336991254,410,2693406720,80,7984,43008,1056915199,50465804,16780300,36,80,224,1126432768,1126432768,1126432768,1098907648,1098907648,0,134928,1073741824,5904,44032,44032,40960,719616,1073741824,31488,17187,1107575590,4294932076,4294967294,17187,1107575590,4294932076,4294967294,17187,1107575590,4294932076,4294967294,
1290240,1333248,1376256,1419264,1462272,9504768,9547776,9590784,9590784,9590784,9590784,9590784,57344,61440,65536,69632,73728,40960,45056,49152,53248,53248,53248,53248,2964369584,353703189,57,2966684928,64,7488,192,1056915199,16780300,50465804,576,36,128,1130102784,1130233856,1107296256,1127219200,1127219200,1127219200,1780416,1796544,264384,32768,40960,61440,9495552,9581568,1410048,1127170899,1128285023,4294939244,174,1127170899,1128285023,4294939244,174,1127105008,1128415752,4294964204,174,
9074688,9117696,9160704,9203712,9246720,9289728,9332736,9375744,9375744,9375744,9375744,9375744,40960,45056,49152,53248,57344,61440,65536,69632,69632,69632,69632,69632,538976288,353703189,57,3496083712,80,7488,192,1056915071,16780300,50465804,576,36,128,1129840640,1129709568,1129578496,1107296256,1107296256,1107296256,1742976,1726848,1710720,57344,49152,40960,9295872,9209856,9123840,1106264915,1112032090,4294941388,30,1106264915,1112032090,4294941388,30,1106264915,1112032090,4294941388,30,
2537472,2537472,2580480,2580480,2623488,2623488,2666496,2666496,2666496,2666496,2666496,2666496,40960,44032,48128,51200,55296,58368,62464,65536,62464,65536,62464,65536,268439552,336991254,57,816975872,80,7488,192,1040137983,16780300,50465804,576,36,224,1114636288,1114636288,1114636288,1098907648,1098907648,0,484416,1073741824,483840,44032,44032,40960,2583552,1073741824,2580480,17004,1107575416,4294955372,4294967294,17004,1107575416,4294955372,4294967294,17004,1107575416,4294955372,4294967294,
4429824,4429824,4472832,4472832,4515840,4515840,4558848,4558848,4558848,4558848,4558848,4558848,40960,44032,48128,51200,55296,58368,62464,65536,62464,65536,62464,65536,268439552,336991254,57,1345458176,80,7488,192,1048526335,16780300,50465804,576,36,224,1120927744,1120927744,1120927744,1098907648,1098907648,0,839232,1073741824,838656,44032,44032,40960,4475904,1073741824,4472832,17102,1107575508,4294945516,4294967294,17102,1107575508,4294945516,4294967294,17102,1107575508,4294945516,4294967294,
4429824,4472832,4515840,4558848,4601856,4644864,4687872,4730880,4730880,4730880,4730880,4730880,40960,45056,49152,53248,57344,61440,65536,69632,69632,69632,69632,69632,538976288,353703189,57,1618084096,80,7488,192,1048526463,16780300,50465804,576,36,128,1121452032,1121189888,1120927744,1107296256,1107296256,1107296256,872064,855936,839808,57344,49152,40960,4651008,4564992,4478976,1106264782,1112031964,4294955212,30,1106264782,1112031964,4294955212,30,1106264782,1112031964,4294955212,30,
14016,14016,14208,14208,14400,14400,14592,14592,14592,14592,14592,14592,40960,44032,48128,51200,55296,58368,62464,65536,62464,65536,62464,65536,268439552,336991254,410,1891246080,80,7984,43008,1048526591,50465804,16780300,36,80,224,1116995584,1116995584,1116995584,1098907648,1098907648,0,131688,1073741824,2664,44032,44032,40960,702336,1073741824,14208,17042,1107575448,4294952236,4294967294,17042,1107575448,4294952236,4294967294,17042,1107575448,4294952236,4294967294,
22464,22464,22656,22656,22848,22848,23040,23040,23040,23040,23040,23040,40960,44032,48128,51200,55296,58368,62464,65536,62464,65536,62464,65536,268439552,336991254,410,2420776960,80,7984,43008,1056915071,50465804,16780300,36,80,224,1122762752,1122762752,1122762752,1098907648,1098907648,0,133272,1073741824,4248,44032,44032,40960,710784,1073741824,22656,17130,1107575536,4294942380,4294967294,17130,1107575536,4294942380,4294967294,17130,1107575536,4294942380,4294967294,
7268352,7311360,7354368,7397376,7440384,7483392,7526400,7569408,7569408,7569408,7569408,7569408,40960,45056,49152,53248,57344,61440,65536,69632,69632,69632,69632,69632,538976288,353703189,57,2693923072,80,7488,192,1056915199,16780300,50465804,576,36,128,1127088128,1126957056,1126825984,1107296256,1107296256,1107296256,1404288,1388160,1372032,57344,49152,40960,7489536,7403520,7317504,1106264873,1112032048,4294946764,30,1106264873,1112032048,4294946764,30,1106264873,1112032048,4294946764,30,
40896,40896,41088,41088,41280,41280,41472,41472,41472,41472,41472,41472,40960,44032,48128,51200,55296,58368,62464,65536,62464,65536,62464,65536,268439552,336991254,410,3223461888,80,7984,43008,1056914943,50465804,16780300,36,80,224,1129709568,1129709568,1129709568,1098907648,1098907648,0,136728,1073741824,7704,44032,44032,40960,729216,1073741824,41088,17237,1107575640,4294920876,4294967294,17237,1107575640,4294920876,4294967294,17237,1107575640,4294920876,4294967294,
8644608,8644608,8687616,8687616,8730624,8730624,8773632,8773632,8773632,8773632,8773632,8773632,40960,44032,48128,51200,55296,58368,62464,65536,62464,65536,62464,65536,268439552,336991254,57,3496611840,80,7488,192,1056915071,16780300,50465804,576,36,224,1128923136,1128923136,1128923136,1098907648,1098907648,0,1629504,1073741824,1628928,44032,44032,40960,8690688,1073741824,8687616,17225,1107575628,4294923564,4294967294,17225,1107575628,4294923564,4294967294,17225,1107575628,4294923564,4294967294,
2107392,2150400,2193408,2236416,2279424,2322432,2365440,2408448,2408448,2408448,2408448,2408448,40960,45056,49152,53248,57344,61440,65536,69632,69632,69632,69632,69632,538976288,353703189,57,817496320,80,7488,192,1040137983,16780300,50465804,576,36,128,1113063424,1112539136,1112014848,1107296256,1107296256,1107296256,436608,420480,404352,57344,49152,40960,2328576,2242560,2156544,1106264644,1112031840,4294962124,30,1106264644,1112031840,4294962124,30,1106264644,1112031840,4294962124,30,
4515840,4515840,4558848,4558848,4601856,4601856,4644864,4644864,4644864,4644864,4644864,4644864,40960,44032,48128,51200,55296,58368,62464,65536,62464,65536,62464,65536,268439552,336991254,57,1345982464,80,7488,192,1048526335,16780300,50465804,576,36,224,1121189888,1121189888,1121189888,1098907648,1098907648,0,855360,1073741824,854784,44032,44032,40960,4561920,1073741824,4558848,17106,1107575512,4294945068,4294967294,17106,1107575512,4294945068,4294967294,17106,1107575512,4294945068,4294967294,
6752256,6752256,6795264,6795264,6838272,6838272,6881280,6881280,6881280,6881280,6881280,6881280,40960,44032,48128,51200,55296,58368,62464,65536,62464,65536,62464,65536,268439552,336991254,57,1618612224,80,7488,192,1048526463,16780300,50465804,576,36,224,1126039552,1126039552,1126039552,1098907648,1098907648,0,1274688,1073741824,1274112,44032,44032,40960,6798336,1073741824,6795264,17181,1107575584,4294933420,4294967294,17181,1107575584,4294933420,4294967294,17181,1107575584,4294933420,4294967294,
774144,817152,860160,903168,946176,989184,8730624,8773632,8773632,8773632,8773632,8773632,49152,53248,57344,61440,65536,69632,40960,45056,45056,45056,45056,45056,1886388224,320017685,57,1891774464,80,7488,192,1048526591,16780300,50465804,576,36,128,1128923136,1128923136,1101004800,1121976320,1121976320,0,1632960,1073741824,161280,32768,32768,53248,8709120,1073741824,860160,1121862465,1124090700,4294941932,110,1121862465,1124090700,4294941932,110,16784,1099973048,4294966540,4294967294,
1290240,1290240,1333248,1333248,1376256,1376256,1419264,1419264,1462272,1462272,1505280,1505280,40960,44032,48128,51200,55296,58368,62464,65536,69632,72704,76800,79872,1076903984,336991254,57,2421300224,64,7488,192,1056915071,16780300,50465804,576,36,224,1107296256,1107296256,1107296256,1115684864,1115684864,1111490560,260352,1073741824,259776,51200,51200,48128,1388544,1073741824,1385472,1110983152,1117929996,4294961580,46,1110983152,1117929996,4294961580,46,1110983152,1117929996,4294961580,46,
31680,31680,31872,31872,32064,32064,32256,32256,32256,32256,32256,32256,40960,44032,48128,51200,55296,58368,62464,65536,62464,65536,62464,65536,268439552,336991254,410,2694455296,80,7984,43008,1056915199,50465804,16780300,36,80,224,1126563840,1126563840,1126563840,1098907648,1098907648,0,135000,1073741824,5976,44032,44032,40960,720000,1073741824,31872,17189,1107575592,4294931628,4294967294,17189,1107575592,4294931628,4294967294,17189,1107575592,4294931628,4294967294,
41280,41280,41472,41472,41664,41664,41856,41856,41856,41856,41856,41856,40960,44032,48128,51200,55296,58368,62464,65536,62464,65536,62464,65536,268439552,336991254,410,3223986176,80,7984,43008,1056914943,50465804,16780300,36,80,224,1129840640,1129840640,1129840640,1098907648,1098907648,0,136800,1073741824,7776,44032,44032,40960,729600,1073741824,41472,17239,1107575642,4294920428,4294967294,17239,1107575642,4294920428,4294967294,17239,1107575642,4294920428,4294967294,
8730624,8730624,8773632,8773632,8816640,8816640,8859648,8859648,8859648,8859648,8859648,8859648,40960,44032,48128,51200,55296,58368,62464,65536,62464,65536,62464,65536,268439552,336991254,57,3497136128,80,7488,192,1056915071,16780300,50465804,576,36,224,1129054208,1129054208,1129054208,1098907648,1098907648,0,1645632,1073741824,1645056,44032,44032,40960,8776704,1073741824,8773632,17227,1107575630,4294923116,4294967294,17227,1107575630,4294923116,4294967294,17227,1107575630,4294923116,4294967294,
2623488,2623488,2666496,2666496,2709504,2709504,2752512,2752512,2752512,2752512,2752512,2752512,40960,44032,48128,51200,55296,58368,62464,65536,62464,65536,62464,65536,268439552,336991254,57,818024448,80,7488,192,1040137983,16780300,50465804,576,36,224,1115160576,1115160576,1115160576,1098907648,1098907648,0,500544,1073741824,499968,44032,44032,40960,2669568,1073741824,2666496,17012,1107575424,4294954924,4294967294,17012,1107575424,4294954924,4294967294,17012,1107575424,4294954924,4294967294,
4601856,4601856,4644864,4644864,4687872,4687872,4730880,4730880,4730880,4730880,4730880,4730880,40960,44032,48128,51200,55296,58368,62464,65536,62464,65536,62464,65536,268439552,336991254,57,1346506752,80,7488,192,1048526463,16780300,50465804,576,36,224,1121452032,1121452032,1121452032,1098907648,1098907648,0,871488,1073741824,870912,44032,44032,40960,4647936,1073741824,4644864,17110,1107575516,4294944620,4294967294,17110,1107575516,4294944620,4294967294,17110,1107575516,4294944620,4294967294,
4608,4608,4800,4800,4992,4992,5184,5184,5376,5376,5568,5568,40960,44032,48128,51200,55296,58368,62464,65536,69632,72704,76800,79872,268439552,336991254,410,1619143680,64,7984,43008,1048526591,50465804,16780300,36,80,224,1104150528,1104150528,1104150528,1098907648,1098907648,0,129960,1073741824,936,51200,51200,48128,693120,1073741824,4992,16832,1107575272,4294963212,4294967294,16832,1107575272,4294963212,4294967294,16832,1107575272,4294963212,4294967294,
860160,903168,946176,989184,1032192,1075200,9160704,9203712,9203712,9203712,9203712,9203712,49152,53248,57344,61440,65536,69632,40960,45056,45056,45056,45056,45056,1886388224,320017685,57,1892429824,80,7488,192,1048526591,16780300,50465804,576,36,128,1129578496,1129578496,1102053376,1121976320,1121976320,0,1713600,1073741824,177408,32768,32768,53248,9139200,1073741824,946176,1121862475,1124090710,4294940652,110,1121862475,1124090710,4294940652,110,16800,1099973064,4294966284,4294967294,
1290240,1290240,1333248,1333248,1376256,1376256,1419264,1419264,1462272,1462272,1505280,1505280,40960,44032,48128,51200,55296,58368,62464,65536,69632,72704,76800,79872,1615880272,336991254,57,2421824512,64,7488,192,1056915071,16780300,50465804,576,36,224,1107296256,1107296256,1107296256,1119879168,1119879168,1117782016,261504,1073741824,260928,51200,51200,48128,1394688,1073741824,1391616,1117536752,1122124300,4294961388,78,1117536752,1122124300,4294961388,78,1117536752,1122124300,4294961388,78,
32064,32064,32256,32256,32448,32448,32640,32640,32640,32640,32640,32640,40960,44032,48128,51200,55296,58368,62464,65536,62464,65536,62464,65536,268439552,336991254,410,2694979584,80,7984,43008,1056915199,50465804,16780300,36,80,224,1126694912,1126694912,1126694912,1098907648,1098907648,0,135072,1073741824,6048,44032,44032,40960,720384,1073741824,32256,17191,1107575594,4294931180,4294967294,17191,1107575594,4294931180,4294967294,17191,1107575594,4294931180,4294967294,
41664,41664,41856,41856,42048,42048,42240,42240,42240,42240,42240,42240,40960,44032,48128,51200,55296,58368,62464,65536,62464,65536,62464,65536,268439552,336991254,410,3224510464,80,7984,43008,1056914943,50465804,16780300,36,80,224,1129971712,1129971712,1129971712,1098907648,1098907648,0,136872,1073741824,7848,44032,44032,40960,729984,1073741824,41856,17241,1107575644,4294919980,4294967294,17241,1107575644,4294919980,4294967294,17241,1107575644,4294919980,4294967294,
8816640,8816640,8859648,8859648,8902656,8902656,8945664,8945664,8945664,8945664,8945664,8945664,40960,44032,48128,51200,55296,58368,62464,65536,62464,65536,62464,65536,268439552,336991254,57,3497660416,80,7488,192,1056915071,16780300,50465804,576,36,224,1129185280,1129185280,1129185280,1098907648,1098907648,0,1661760,1073741824,1661184,44032,44032,40960,8862720,1073741824,8859648,17229,1107575632,4294922668,4294967294,17229,1107575632,4294922668,4294967294,17229,1107575632,4294922668,4294967294,
2709504,2709504,2752512,2752512,2795520,2795520,2838528,2838528,2838528,2838528,2838528,2838528,40960,44032,48128,51200,55296,58368,62464,65536,62464,65536,62464,65536,268439552,336991254,57,818548736,80,7488,192,1040137983,16780300,50465804,576,36,224,1115684864,1115684864,1115684864,1098907648,1098907648,0,516672,1073741824,516096,44032,44032,40960,2755584,1073741824,2752512,17020,1107575428,4294954476,4294967294,17020,1107575428,4294954476,4294967294,17020,1107575428,4294954476,4294967294,
3397632,3440640,3483648,3526656,3569664,3612672,3655680,3698688,3698688,3698688,3698688,3698688,40960,45056,49152,53248,57344,61440,65536,69632,69632,69632,69632,69632,538976288,353703189,57,1347027200,80,7488,192,1048526463,16780300,50465804,576,36,128,1118306304,1118044160,1117782016,1107296256,1107296256,1107296256,678528,662400,646272,57344,49152,40960,3618816,3532800,3446784,1106264734,1112031916,4294958284,30,1106264734,1112031916,4294958284,30,1106264734,1112031916,4294958284,30,
4992,4992,5184,5184,5376,5376,5568,5568,5760,5760,5952,5952,40960,44032,48128,51200,55296,58368,62464,65536,69632,72704,76800,79872,268439552,336991254,410,1619667968,64,7984,43008,1048526591,50465804,16780300,36,80,224,1105199104,1105199104,1105199104,1098907648,1098907648,0,130032,1073741824,1008,51200,51200,48128,693504,1073741824,5376,16848,1107575288,4294962764,4294967294,16848,1107575288,4294962764,4294967294,16848,1107575288,4294962764,4294967294,
946176,989184,1032192,1075200,1118208,1161216,9590784,9590784,9590784,9590784,9590784,9590784,49152,53248,57344,61440,65536,69632,40960,45056,45056,45056,45056,45056,1886388224,320017685,57,1893085184,80,7488,192,1048526591,16780300,50465804,576,36,128,1130233856,1130233856,1103101952,1121976320,1121976320,0,1794240,1073741824,193536,32768,32768,53248,9569280,1073741824,1032192,1121862485,1124090719,4294939372,110,1121862485,1124090719,4294939372,110,16816,1099973080,4294966028,4294967294,
1290240,1290240,1333248,1333248,1376256,1376256,1419264,1419264,1462272,1462272,1505280,1505280,40960,44032,48128,51200,55296,58368,62464,65536,69632,72704,76800,79872,2154856560,336991254,57,2422348800,64,7488,192,1056915071,16780300,50465804,576,36,224,1107296256,1107296256,1107296256,1124073472,1124073472,1121976320,262656,1073741824,262080,51200,51200,48128,1400832,1073741824,1397760,1121731056,1125204492,4294961196,110,1121731056,1125204492,4294961196,110,1121731056,1125204492,4294961196,110,
32448,32448,32640,32640,32832,32832,33024,33024,33024,33024,33024,33024,40960,44032,48128,51200,55296,58368,62464,65536,62464,65536,62464,65536,268439552,336991254,410,2695503872,80,7984,43008,1056915199,50465804,16780300,36,80,224,1126825984,1126825984,1126825984,1098907648,1098907648,0,135144,1073741824,6120,44032,44032,40960,720768,1073741824,32640,17193,1107575596,4294930732,4294967294,17193,1107575596,4294930732,4294967294,17193,1107575596,4294930732,4294967294,
42048,42048,42240,42240,42432,42432,42624,42624,42624,42624,42624,42624,40960,44032,48128,51200,55296,58368,62464,65536,62464,65536,62464,65536,268439552,336991254,410,3225034752,80,7984,43008,1056914943,50465804,16780300,36,80,224,1130102784,1130102784,1130102784,1098907648,1098907648,0,136944,1073741824,7920,44032,44032,40960,730368,1073741824,42240,17243,1107575646,4294919532,4294967294,17243,1107575646,4294919532,4294967294,17243,1107575646,4294919532,4294967294,
8902656,8902656,8945664,8945664,8988672,8988672,9031680,9031680,9031680,9031680,9031680,9031680,40960,44032,48128,51200,55296,58368,62464,65536,62464,65536,62464,65536,268439552,336991254,57,3498184704,80,7488,192,1056915199,16780300,50465804,576,36,224,1129316352,1129316352,1129316352,1098907648,1098907648,0,1677888,1073741824,1677312,44032,44032,40960,8948736,1073741824,8945664,17231,1107575634,4294922220,4294967294,17231,1107575634,4294922220,4294967294,17231,1107575634,4294922220,4294967294,
2795520,2795520,2838528,2838528,2881536,2881536,2924544,2924544,2924544,2924544,2924544,2924544,40960,44032,48128,51200,55296,58368,62464,65536,62464,65536,62464,65536,268439552,336991254,57,819073024,80,7488,192,1040137983,16780300,50465804,576,36,224,1115947008,1115947008,1115947008,1098907648,1098907648,0,532800,1073741824,532224,44032,44032,40960,2841600,1073741824,2838528,17026,1107575432,4294954028,4294967294,17026,1107575432,4294954028,4294967294,17026,1107575432,4294954028,4294967294,
4687872,4687872,4730880,4730880,4773888,4773888,4816896,4816896,4816896,4816896,4816896,4816896,40960,44032,48128,51200,55296,58368,62464,65536,62464,65536,62464,65536,268439552,336991254,57,1347555328,80,7488,192,1048526463,16780300,50465804,576,36,224,1121714176,1121714176,1121714176,1098907648,1098907648,0,887616,1073741824,887040,44032,44032,40960,4733952,1073741824,4730880,17114,1107575520,4294944172,4294967294,17114,1107575520,4294944172,4294967294,17114,1107575520,4294944172,4294967294,
5376,5376,5568,5568,5760,5760,5952,5952,6144,6144,6336,6336,40960,44032,48128,51200,55296,58368,62464,65536,69632,72704,76800,79872,268439552,336991254,410,1620192256,64,7984,43008,1048526591,50465804,16780300,36,80,224,1106247680,1106247680,1106247680,1098907648,1098907648,0,130104,1073741824,1080,51200,51200,48128,693888,1073741824,5760,16864,1107575300,4294962316,4294967294,16864,1107575300,4294962316,4294967294,16864,1107575300,4294962316,4294967294,
5462016,5505024,5548032,5591040,5634048,5677056,5720064,5763072,5763072,5763072,5763072,5763072,40960,45056,49152,53248,57344,61440,65536,69632,69632,69632,69632,69632,538976288,353703189,57,2149712128,80,7488,192,1048526335,16780300,50465804,576,36,128,1124335616,1124204544,1124073472,1107296256,1107296256,1107296256,1065600,1049472,1033344,57344,49152,40960,5683200,5597184,5511168,1106264830,1112032006,4294952140,30,1106264830,1112032006,4294952140,30,1106264830,1112032006,4294952140,30,
22848,22848,23040,23040,23232,23232,23424,23424,23424,23424,23424,23424,40960,44032,48128,51200,55296,58368,62464,65536,62464,65536,62464,65536,268439552,336991254,410,2422874112,80,7984,43008,1056915071,50465804,16780300,36,80,224,1123024896,1123024896,1123024896,1098907648,1098907648,0,133344,1073741824,4320,44032,44032,40960,711168,1073741824,23040,17134,1107575540,4294941932,4294967294,17134,1107575540,4294941932,4294967294,17134,1107575540,4294941932,4294967294,
32832,32832,33024,33024,33216,33216,33408,33408,33408,33408,33408,33408,40960,44032,48128,51200,55296,58368,62464,65536,62464,65536,62464,65536,268439552,336991254,410,2696028160,80,7984,43008,1056915199,50465804,16780300,36,80,224,1126957056,1126957056,1126957056,1098907648,1098907648,0,135216,1073741824,6192,44032,44032,40960,721152,1073741824,33024,17195,1107575598,4294930284,4294967294,17195,1107575598,4294930284,4294967294,17195,1107575598,4294930284,4294967294,
8300544,8343552,8386560,8429568,8472576,8515584,8558592,8601600,8601600,8601600,8601600,8601600,40960,45056,49152,53248,57344,61440,65536,69632,69632,69632,69632,69632,538976288,353703189,57,3225551104,80,7488,192,1056915071,16780300,50465804,576,36,128,1128660992,1128529920,1128398848,1107296256,1107296256,1107296256,1597824,1581696,1565568,57344,49152,40960,8521728,8435712,8349696,1106264897,1112032072,4294943692,30,1106264897,1112032072,4294943692,30,1106264897,1112032072,4294943692,30,
8988672,8988672,9031680,9031680,9074688,9074688,9117696,9117696,9117696,9117696,9117696,9117696,40960,44032,48128,51200,55296,58368,62464,65536,62464,65536,62464,65536,268439552,336991254,57,3498708992,80,7488,192,1056915199,16780300,50465804,576,36,224,1129447424,1129447424,1129447424,1098907648,1098907648,0,1694016,1073741824,1693440,44032,44032,40960,9034752,1073741824,9031680,17233,1107575636,4294921772,4294967294,17233,1107575636,4294921772,4294967294,17233,1107575636,4294921772,4294967294,
2881536,2881536,2924544,2924544,2967552,2967552,3010560,3010560,3010560,3010560,3010560,3010560,40960,44032,48128,51200,55296,58368,62464,65536,62464,65536,62464,65536,268439552,336991254,57,819597312,80,7488,192,1040137983,16780300,50465804,576,36,224,1116209152,1116209152,1116209152,1098907648,1098907648,0,548928,1073741824,548352,44032,44032,40960,2927616,1073741824,2924544,17030,1107575436,4294953580,4294967294,17030,1107575436,4294953580,4294967294,17030,1107575436,4294953580,4294967294,
4773888,4773888,4816896,4816896,4859904,4859904,4902912,4902912,4902912,4902912,4902912,4902912,40960,44032,48128,51200,55296,58368,62464,65536,62464,65536,62464,65536,268439552,336991254,57,1348079616,80,7488,192,1048526463,16780300,50465804,576,36,224,1121976320,1121976320,1121976320,1098907648,1098907648,0,903744,1073741824,903168,44032,44032,40960,4819968,1073741824,4816896,17118,1107575524,4294943724,4294967294,17118,1107575524,4294943724,4294967294,17118,1107575524,4294943724,4294967294,
5760,5760,5952,5952,6144,6144,6336,6336,6528,6528,6720,6720,40960,44032,48128,51200,55296,58368,62464,65536,69632,72704,76800,79872,268439552,336991254,410,1620716544,64,7984,43008,1048526591,50465804,16780300,36,80,224,1107296256,1107296256,1107296256,1098907648,1098907648,0,130176,1073741824,1152,51200,51200,48128,694272,1073741824,6144,16880,1107575308,4294961868,4294967294,16880,1107575308,4294961868,4294967294,16880,1107575308,4294961868,4294967294,
14400,14400,14592,14592,14784,14784,14976,14976,14976,14976,14976,14976,40960,44032,48128,51200,55296,58368,62464,65536,62464,65536,62464,65536,268439552,336991254,410,2150244352,80,7984,43008,1048526335,50465804,16780300,36,80,224,1117257728,1117257728,1117257728,1098907648,1098907648,0,131760,1073741824,2736,44032,44032,40960,702720,1073741824,14592,17046,1107575452,4294951788,4294967294,17046,1107575452,4294951788,4294967294,17046,1107575452,4294951788,4294967294,
23232,23232,23424,23424,23616,23616,23808,23808,23808,23808,23808,23808,40960,44032,48128,51200,55296,58368,62464,65536,62464,65536,62464,65536,268439552,336991254,410,2423398400,80,7984,43008,1056915071,50465804,16780300,36,80,224,1123287040,1123287040,1123287040,1098907648,1098907648,0,133416,1073741824,4392,44032,44032,40960,711552,1073741824,23424,17138,1107575544,4294941484,4294967294,17138,1107575544,4294941484,4294967294,17138,1107575544,4294941484,4294967294,
33216,33216,33408,33408,33600,33600,33792,33792,33792,33792,33792,33792,40960,44032,48128,51200,55296,58368,62464,65536,62464,65536,62464,65536,268439552,336991254,410,2696552448,80,7984,43008,1056915199,50465804,16780300,36,80,224,1127088128,1127088128,1127088128,1098907648,1098907648,0,135288,1073741824,6264,44032,44032,40960,721536,1073741824,33408,17197,1107575600,4294929836,4294967294,17197,1107575600,4294929836,4294967294,17197,1107575600,4294929836,4294967294,
42432,42432,42624,42624,42816,42816,42816,42816,42816,42816,42816,42816,40960,44032,48128,51200,55296,58368,62464,65536,62464,65536,62464,65536,268439552,336991254,410,3226083328,80,7984,43008,1056915071,50465804,16780300,36,80,224,1130233856,1130233856,1130233856,1098907648,1098907648,0,137016,1073741824,7992,44032,44032,40960,730752,1073741824,42624,17245,1107575647,4294919084,4294967294,17245,1107575647,4294919084,4294967294,17245,1107575647,4294919084,4294967294,
9074688,9074688,9117696,9117696,9160704,9160704,9203712,9203712,9203712,9203712,9203712,9203712,40960,44032,48128,51200,55296,58368,62464,65536,62464,65536,62464,65536,268439552,336991254,57,3499233280,80,7488,192,1056915199,16780300,50465804,576,36,224,1129578496,1129578496,1129578496,1098907648,1098907648,0,1710144,1073741824,1709568,44032,44032,40960,9120768,1073741824,9117696,17235,1107575638,4294921324,4294967294,17235,1107575638,4294921324,4294967294,17235,1107575638,4294921324,4294967294,
2967552,2967552,3010560,3010560,3053568,3053568,3096576,3096576,3096576,3096576,3096576,3096576,40960,44032,48128,51200,55296,58368,62464,65536,62464,65536,62464,65536,268439552,336991254,57,1075974144,80,7488,192,1040137727,16780300,50465804,576,36,224,1116471296,1116471296,1116471296,1098907648,1098907648,0,565056,1073741824,564480,44032,44032,40960,3013632,1073741824,3010560,17034,1107575440,4294953132,4294967294,17034,1107575440,4294953132,4294967294,17034,1107575440,4294953132,4294967294,
4859904,4859904,4902912,4902912,4945920,4945920,4988928,4988928,4988928,4988928,4988928,4988928,40960,44032,48128,51200,55296,58368,62464,65536,62464,65536,62464,65536,268439552,336991254,57,1348603904,80,7488,192,1048526463,16780300,50465804,576,36,224,1122238464,1122238464,1122238464,1098907648,1098907648,0,919872,1073741824,919296,44032,44032,40960,4905984,1073741824,4902912,17122,1107575528,4294943276,4294967294,17122,1107575528,4294943276,4294967294,17122,1107575528,4294943276,4294967294,
6336,6336,6528,6528,6720,6720,6912,6912,6912,6912,6912,6912,40960,44032,48128,51200,55296,58368,62464,65536,62464,65536,62464,65536,268439552,336991254,410,1621237760,80,7984,43008,1048526591,50465804,16780300,36,80,224,1107820544,1107820544,1107820544,1098907648,1098907648,0,130248,1073741824,1224,44032,44032,40960,694656,1073741824,6528,16900,1107575312,4294961196,4294967294,16900,1107575312,4294961196,4294967294,16900,1107575312,4294961196,4294967294,
14784,14784,14976,14976,15168,15168,15360,15360,15360,15360,15360,15360,40960,44032,48128,51200,55296,58368,62464,65536,62464,65536,62464,65536,268439552,336991254,410,2150768640,80,7984,43008,1048526335,50465804,16780300,36,80,224,1117519872,1117519872,1117519872,1098907648,1098907648,0,131832,1073741824,2808,44032,44032,40960,703104,1073741824,14976,17050,1107575456,4294951340,4294967294,17050,1107575456,4294951340,4294967294,17050,1107575456,4294951340,4294967294,
6494208,6537216,6580224,6623232,6666240,6709248,6752256,6795264,6795264,6795264,6795264,6795264,40960,45056,49152,53248,57344,61440,65536,69632,69632,69632,69632,69632,538976288,353703189,57,2423914752,80,7488,192,1056915071,16780300,50465804,576,36,128,1125908480,1125777408,1125646336,1107296256,1107296256,1107296256,1259136,1243008,1226880,57344,49152,40960,6715392,6629376,6543360,1106264855,1112032030,4294949068,30,1106264855,1112032030,4294949068,30,1106264855,1112032030,4294949068,30,
33600,33600,33792,33792,33984,33984,34176,34176,34176,34176,34176,34176,40960,44032,48128,51200,55296,58368,62464,65536,62464,65536,62464,65536,268439552,336991254,410,2697076736,80,7984,43008,1056915199,50465804,16780300,36,80,224,1127219200,1127219200,1127219200,1098907648,1098907648,0,135360,1073741824,6336,44032,44032,40960,721920,1073741824,33792,17199,1107575602,4294929388,4294967294,17199,1107575602,4294929388,4294967294,17199,1107575602,4294929388,4294967294,
6838272,6838272,6881280,6881280,6924288,6924288,6967296,6967296,6967296,6967296,6967296,6967296,40960,44032,48128,51200,55296,58368,62464,65536,62464,65536,62464,65536,268439552,336991254,57,3226603520,80,7488,192,1056915071,16780300,50465804,576,36,224,1126170624,1126170624,1126170624,1098907648,1098907648,0,1290816,1073741824,1290240,44032,44032,40960,6884352,1073741824,6881280,17183,1107575586,4294932972,4294967294,17183,1107575586,4294932972,4294967294,17183,1107575586,4294932972,4294967294,
9160704,9160704,9203712,9203712,9246720,9246720,9289728,9289728,9289728,9289728,9289728,9289728,40960,44032,48128,51200,55296,58368,62464,65536,62464,65536,62464,65536,268439552,336991254,57,3499757568,80,7488,192,1056915199,16780300,50465804,576,36,224,1129709568,1129709568,1129709568,1098907648,1098907648,0,1726272,1073741824,1725696,44032,44032,40960,9206784,1073741824,9203712,17237,1107575640,4294920876,4294967294,17237,1107575640,4294920876,4294967294,17237,1107575640,4294920876,4294967294,
2365440,2408448,2451456,2494464,2537472,2580480,2623488,2666496,2666496,2666496,2666496,2666496,40960,45056,49152,53248,57344,61440,65536,69632,69632,69632,69632,69632,538976288,353703189,57,1076494592,80,7488,192,1040137727,16780300,50465804,576,36,128,1114636288,1114112000,1113587712,1107296256,1107296256,1107296256,484992,468864,452736,57344,49152,40960,2586624,2500608,2414592,1106264668,1112031864,4294961356,30,1106264668,1112031864,4294961356,30,1106264668,1112031864,4294961356,30,
4945920,4945920,4988928,4988928,5031936,5031936,5074944,5074944,5074944,5074944,5074944,5074944,40960,44032,48128,51200,55296,58368,62464,65536,62464,65536,62464,65536,268439552,336991254,57,1349128192,80,7488,192,1048526463,16780300,50465804,576,36,224,1122500608,1122500608,1122500608,1098907648,1098907648,0,936000,1073741824,935424,44032,44032,40960,4992000,1073741824,4988928,17126,1107575532,4294942828,4294967294,17126,1107575532,4294942828,4294967294,17126,1107575532,4294942828,4294967294,
4687872,4730880,4773888,4816896,4859904,4902912,4945920,4988928,4988928,4988928,4988928,4988928,40960,45056,49152,53248,57344,61440,65536,69632,69632,69632,69632,69632,538976288,353703189,57,1621754112,80,7488,192,1048526591,16780300,50465804,576,36,128,1122238464,1121976320,1121714176,1107296256,1107296256,1107296256,920448,904320,888192,57344,49152,40960,4909056,4823040,4737024,1106264794,1112031976,4294954444,30,1106264794,1112031976,4294954444,30,1106264794,1112031976,4294954444,30,
15168,15168,15360,15360,15552,15552,15744,15744,15744,15744,15744,15744,40960,44032,48128,51200,55296,58368,62464,65536,62464,65536,62464,65536,268439552,336991254,410,2151292928,80,7984,43008,1048526335,50465804,16780300,36,80,224,1117782016,1117782016,1117782016,1098907648,1098907648,0,131904,1073741824,2880,44032,44032,40960,703488,1073741824,15360,17054,1107575460,4294950892,4294967294,17054,1107575460,4294950892,4294967294,17054,1107575460,4294950892,4294967294,
23616,23616,23808,23808,24000,24000,24192,24192,24192,24192,24192,24192,40960,44032,48128,51200,55296,58368,62464,65536,62464,65536,62464,65536,268439552,336991254,410,2424446976,80,7984,43008,1056915199,50465804,16780300,36,80,224,1123549184,1123549184,1123549184,1098907648,1098907648,0,133488,1073741824,4464,44032,44032,40960,711936,1073741824,23808,17142,1107575548,4294941036,4294967294,17142,1107575548,4294941036,4294967294,17142,1107575548,4294941036,4294967294,
258048,301056,344064,387072,430080,473088,9074688,9117696,9117696,9117696,9117696,9117696,49152,53248,57344,61440,65536,69632,40960,45056,45056,45056,45056,45056,2694840320,320017685,57,2697605632,80,7488,192,1056915199,16780300,50465804,576,36,128,1129447424,1129447424,1090519040,1126170624,1126170624,0,1699200,1073741824,64512,32768,32768,53248,9062400,1073741824,344064,1126122313,1127236436,4294940620,158,1126122313,1127236436,4294940620,158,16576,1099972912,780,4294967294,
6924288,6924288,6967296,6967296,7010304,7010304,7053312,7053312,7053312,7053312,7053312,7053312,40960,44032,48128,51200,55296,58368,62464,65536,62464,65536,62464,65536,268439552,336991254,57,3227127808,80,7488,192,1056915071,16780300,50465804,576,36,224,1126301696,1126301696,1126301696,1098907648,1098907648,0,1306944,1073741824,1306368,44032,44032,40960,6970368,1073741824,6967296,17185,1107575588,4294932524,4294967294,17185,1107575588,4294932524,4294967294,17185,1107575588,4294932524,4294967294,
9332736,9375744,9418752,9461760,9504768,9547776,9590784,9590784,9590784,9590784,9590784,9590784,40960,45056,49152,53248,57344,61440,65536,69632,69632,69632,69632,69632,538976288,353703189,57,3500278016,80,7488,192,1056915199,16780300,50465804,576,36,128,1130233856,1130102784,1129971712,1107296256,1107296256,1107296256,1791360,1775232,1759104,57344,49152,40960,9553920,9467904,9381888,1106264921,1112032095,4294940620,30,1106264921,1112032095,4294940620,30,1106264921,1112032095,4294940620,30,
3053568,3053568,3096576,3096576,3139584,3139584,3182592,3182592,3182592,3182592,3182592,3182592,40960,44032,48128,51200,55296,58368,62464,65536,62464,65536,62464,65536,268439552,336991254,57,1077022720,80,7488,192,1040137727,16780300,50465804,576,36,224,1116733440,1116733440,1116733440,1098907648,1098907648,0,581184,1073741824,580608,44032,44032,40960,3099648,1073741824,3096576,17038,1107575444,4294952684,4294967294,17038,1107575444,4294952684,4294967294,17038,1107575444,4294952684,4294967294,
5031936,5031936,5074944,5074944,5117952,5117952,5160960,5160960,5160960,5160960,5160960,5160960,40960,44032,48128,51200,55296,58368,62464,65536,62464,65536,62464,65536,268439552,336991254,57,1349652480,80,7488,192,1048526463,16780300,50465804,576,36,224,1122762752,1122762752,1122762752,1098907648,1098907648,0,952128,1073741824,951552,44032,44032,40960,5078016,1073741824,5074944,17130,1107575536,4294942380,4294967294,17130,1107575536,4294942380,4294967294,17130,1107575536,4294942380,4294967294,
6720,6720,6912,6912,7104,7104,7296,7296,7296,7296,7296,7296,40960,44032,48128,51200,55296,58368,62464,65536,62464,65536,62464,65536,268439552,336991254,410,1622286336,80,7984,43008,1048526591,50465804,16780300,36,80,224,1108344832,1108344832,1108344832,1098907648,1098907648,0,130320,1073741824,1296,44032,44032,40960,695040,1073741824,6912,16908,1107575320,4294960748,4294967294,16908,1107575320,4294960748,4294967294,16908,1107575320,4294960748,4294967294,
15552,15552,15744,15744,15936,15936,16128,16128,16128,16128,16128,16128,40960,44032,48128,51200,55296,58368,62464,65536,62464,65536,62464,65536,268439552,336991254,410,2151817216,80,7984,43008,1048526463,50465804,16780300,36,80,224,1118044160,1118044160,1118044160,1098907648,1098907648,0,131976,1073741824,2952,44032,44032,40960,703872,1073741824,15744,17058,1107575464,4294950444,4294967294,17058,1107575464,4294950444,4294967294,17058,1107575464,4294950444,4294967294,
24000,24000,24192,24192,24384,24384,24576,24576,24576,24576,24576,24576,40960,44032,48128,51200,55296,58368,62464,65536,62464,65536,62464,65536,268439552,336991254,410,2424971264,80,7984,43008,1056915199,50465804,16780300,36,80,224,1123811328,1123811328,1123811328,1098907648,1098907648,0,133560,1073741824,4536,44032,44032,40960,712320,1073741824,24192,17146,1107575552,4294940588,4294967294,17146,1107575552,4294940588,4294967294,17146,1107575552,4294940588,4294967294,
1290240,1333248,1376256,1419264,1462272,9504768,9547776,9590784,9590784,9590784,9590784,9590784,57344,61440,65536,69632,73728,40960,45056,49152,53248,53248,53248,53248,2694881440,353703189,57,2698249472,64,7488,192,1056915199,16780300,50465804,576,36,128,1130102784,1130233856,1107296256,1126170624,1126170624,1126170624,1779840,1795968,263808,32768,40960,61440,9492480,9578496,1406976,1126122323,1127236447,4294939340,158,1126122323,1127236447,4294939340,158,1126056432,1127367176,4294964300,158,
7010304,7010304,7053312,7053312,7096320,7096320,7139328,7139328,7139328,7139328,7139328,7139328,40960,44032,48128,51200,55296,58368,62464,65536,62464,65536,62464,65536,268439552,336991254,57,3227652096,80,7488,192,1056915071,16780300,50465804,576,36,224,1126432768,1126432768,1126432768,1098907648,1098907648,0,1323072,1073741824,1322496,44032,44032,40960,7056384,1073741824,7053312,17187,1107575590,4294932076,4294967294,17187,1107575590,4294932076,4294967294,17187,1107575590,4294932076,4294967294,
9246720,9246720,9289728,9289728,9332736,9332736,9375744,9375744,9375744,9375744,9375744,9375744,40960,44032,48128,51200,55296,58368,62464,65536,62464,65536,62464,65536,268439552,336991254,57,3500806144,80,7488,192,1056915199,16780300,50465804,576,36,224,1129840640,1129840640,1129840640,1098907648,1098907648,0,1742400,1073741824,1741824,44032,44032,40960,9292800,1073741824,9289728,17239,1107575642,4294920428,4294967294,17239,1107575642,4294920428,4294967294,17239,1107575642,4294920428,4294967294,
3139584,3139584,3182592,3182592,3225600,3225600,3268608,3268608,3268608,3268608,3268608,3268608,40960,44032,48128,51200,55296,58368,62464,65536,62464,65536,62464,65536,268439552,336991254,57,1077547008,80,7488,192,1040137727,16780300,50465804,576,36,224,1116995584,1116995584,1116995584,1098907648,1098907648,0,597312,1073741824,596736,44032,44032,40960,3185664,1073741824,3182592,17042,1107575448,4294952236,4294967294,17042,1107575448,4294952236,4294967294,17042,1107575448,4294952236,4294967294,
86016,129024,172032,215040,258048,301056,301056,301056,301056,301056,301056,301056,40960,45056,49152,53248,57344,61440,61440,61440,61440,61440,61440,61440,0,353703189,57,1350218240,80,7488,192,1048526463,16780300,50465804,576,36,128,1082130432,1082130432,1082130432,0,0,0,1073741824,1073741824,32256,45056,45056,45056,1073741824,1073741824,172032,16384,1099972832,1036,4294967294,16384,1099972832,1036,4294967294,16384,1099972832,1036,4294967294,
7104,7104,7296,7296,7488,7488,7680,7680,7680,7680,7680,7680,40960,44032,48128,51200,55296,58368,62464,65536,62464,65536,62464,65536,268439552,336991254,410,1622810624,80,7984,43008,1048526591,50465804,16780300,36,80,224,1108869120,1108869120,1108869120,1098907648,1098907648,0,130392,1073741824,1368,44032,44032,40960,695424,1073741824,7296,16916,1107575328,4294960300,4294967294,16916,1107575328,4294960300,4294967294,16916,1107575328,4294960300,4294967294,
15936,15936,16128,16128,16320,16320,16512,16512,16512,16512,16512,16512,40960,44032,48128,51200,55296,58368,62464,65536,62464,65536,62464,65536,268439552,336991254,410,2152341504,80,7984,43008,1048526463,50465804,16780300,36,80,224,1118306304,1118306304,1118306304,1098907648,1098907648,0,132048,1073741824,3024,44032,44032,40960,704256,1073741824,16128,17062,1107575468,4294949996,4294967294,17062,1107575468,4294949996,4294967294,17062,1107575468,4294949996,4294967294,
24384,24384,24576,24576,24768,24768,24960,24960,24960,24960,24960,24960,40960,44032,48128,51200,55296,58368,62464,65536,62464,65536,62464,65536,268439552,336991254,410,2425495552,80,7984,43008,1056915199,50465804,16780300,36,80,224,1124073472,1124073472,1124073472,1098907648,1098907648,0,133632,1073741824,4608,44032,44032,40960,712704,1073741824,24576,17150,1107575554,4294940140,4294967294,17150,1107575554,4294940140,4294967294,17150,1107575554,4294940140,4294967294,
33984,33984,34176,34176,34368,34368,34560,34560,34560,34560,34560,34560,40960,44032,48128,51200,55296,58368,62464,65536,62464,65536,62464,65536,268439552,336991254,410,2955026432,80,7984,43008,1056914943,50465804,16780300,36,80,224,1127350272,1127350272,1127350272,1098907648,1098907648,0,135432,1073741824,6408,44032,44032,40960,722304,1073741824,34176,17201,1107575604,4294928940,4294967294,17201,1107575604,4294928940,4294967294,17201,1107575604,4294928940,4294967294,
7096320,7096320,7139328,7139328,7182336,7182336,7225344,7225344,7225344,7225344,7225344,7225344,40960,44032,48128,51200,55296,58368,62464,65536,62464,65536,62464,65536,268439552,336991254,57,3228176384,80,7488,192,1056915071,16780300,50465804,576,36,224,1126563840,1126563840,1126563840,1098907648,1098907648,0,1339200,1073741824,1338624,44032,44032,40960,7142400,1073741824,7139328,17189,1107575592,4294931628,4294967294,17189,1107575592,4294931628,4294967294,17189,1107575592,4294931628,4294967294,
9332736,9332736,9375744,9375744,9418752,9418752,9461760,9461760,9461760,9461760,9461760,9461760,40960,44032,48128,51200,55296,58368,62464,65536,62464,65536,62464,65536,268439552,336991254,57,3501330432,80,7488,192,1056915199,16780300,50465804,576,36,224,1129971712,1129971712,1129971712,1098907648,1098907648,0,1758528,1073741824,1757952,44032,44032,40960,9378816,1073741824,9375744,17241,1107575644,4294919980,4294967294,17241,1107575644,4294919980,4294967294,17241,1107575644,4294919980,4294967294,
3225600,3225600,3268608,3268608,3311616,3311616,3354624,3354624,3354624,3354624,3354624,3354624,40960,44032,48128,51200,55296,58368,62464,65536,62464,65536,62464,65536,268439552,336991254,57,1078071296,80,7488,192,1040137855,16780300,50465804,576,36,224,1117257728,1117257728,1117257728,1098907648,1098907648,0,613440,1073741824,612864,44032,44032,40960,3271680,1073741824,3268608,17046,1107575452,4294951788,4294967294,17046,1107575452,4294951788,4294967294,17046,1107575452,4294951788,4294967294,
3655680,3698688,3741696,3784704,3827712,3870720,3913728,3956736,3956736,3956736,3956736,3956736,40960,45056,49152,53248,57344,61440,65536,69632,69632,69632,69632,69632,538976288,353703189,57,1350697216,80,7488,192,1048526591,16780300,50465804,576,36,128,1119092736,1118830592,1118568448,1107296256,1107296256,1107296256,726912,710784,694656,57344,49152,40960,3876864,3790848,3704832,1106264746,1112031928,4294957516,30,1106264746,1112031928,4294957516,30,1106264746,1112031928,4294957516,30,
0,0,0,43008,86016,129024,8730624,8773632,8773632,8773632,8773632,8773632,49152,53248,57344,61440,65536,69632,40960,45056,45056,45056,45056,45056,1616904192,320017685,57,1623339520,80,7488,192,1048526591,16780300,50465804,576,36,128,1128923136,1128923136,0,1119879168,1119879168,0,1632384,1073741824,0,32768,32768,53248,8706048,1073741824,0,1119765313,1121993548,4294942028,94,1119765313,1121993548,4294942028,94,0,1099972672,1804,4294967294,
16320,16320,16512,16512,16704,16704,16896,16896,16896,16896,16896,16896,40960,44032,48128,51200,55296,58368,62464,65536,62464,65536,62464,65536,268439552,336991254,410,2152865792,80,7984,43008,1048526463,50465804,16780300,36,80,224,1118568448,1118568448,1118568448,1098907648,1098907648,0,132120,1073741824,3096,44032,44032,40960,704640,1073741824,16512,17066,1107575472,4294949548,4294967294,17066,1107575472,4294949548,4294967294,17066,1107575472,4294949548,4294967294,
24768,24768,24960,24960,25152,25152,25344,25344,25344,25344,25344,25344,40960,44032,48128,51200,55296,58368,62464,65536,62464,65536,62464,65536,268439552,336991254,410,2426019840,80,7984,43008,1056915199,50465804,16780300,36,80,224,1124204544,1124204544,1124204544,1098907648,1098907648,0,133704,1073741824,4680,44032,44032,40960,713088,1073741824,24960,17153,1107575556,4294939692,4294967294,17153,1107575556,4294939692,4294967294,17153,1107575556,4294939692,4294967294,
7526400,7569408,7612416,7655424,7698432,7741440,7784448,7827456,7827456,7827456,7827456,7827456,40960,45056,49152,53248,57344,61440,65536,69632,69632,69632,69632,69632,538976288,353703189,57,2955542784,80,7488,192,1056914943,16780300,50465804,576,36,128,1127481344,1127350272,1127219200,1107296256,1107296256,1107296256,1452672,1436544,1420416,57344,49152,40960,7747584,7661568,7575552,1106264879,1112032054,4294945996,30,1106264879,1112032054,4294945996,30,1106264879,1112032054,4294945996,30,
7182336,7182336,7225344,7225344,7268352,7268352,7311360,7311360,7311360,7311360,7311360,7311360,40960,44032,48128,51200,55296,58368,62464,65536,62464,65536,62464,65536,268439552,336991254,57,3228700672,80,7488,192,1056915071,16780300,50465804,576,36,224,1126694912,1126694912,1126694912,1098907648,1098907648,0,1355328,1073741824,1354752,44032,44032,40960,7228416,1073741824,7225344,17191,1107575594,4294931180,4294967294,17191,1107575594,4294931180,4294967294,17191,1107575594,4294931180,4294967294,
9418752,9418752,9461760,9461760,9504768,9504768,9547776,9547776,9547776,9547776,9547776,9547776,40960,44032,48128,51200,55296,58368,62464,65536,62464,65536,62464,65536,268439552,336991254,57,3501854720,80,7488,192,1056915199,16780300,50465804,576,36,224,1130102784,1130102784,1130102784,1098907648,1098907648,0,1774656,1073741824,1774080,44032,44032,40960,9464832,1073741824,9461760,17243,1107575646,4294919532,4294967294,17243,1107575646,4294919532,4294967294,17243,1107575646,4294919532,4294967294,
3311616,3311616,3354624,3354624,3397632,3397632,3440640,3440640,3440640,3440640,3440640,3440640,40960,44032,48128,51200,55296,58368,62464,65536,62464,65536,62464,65536,268439552,336991254,57,1078595584,80,7488,192,1040137855,16780300,50465804,576,36,224,1117519872,1117519872,1117519872,1098907648,1098907648,0,629568,1073741824,628992,44032,44032,40960,3357696,1073741824,3354624,17050,1107575456,4294951340,4294967294,17050,1107575456,4294951340,4294967294,17050,1107575456,4294951340,4294967294,
5117952,5117952,5160960,5160960,5203968,5203968,5246976,5246976,5246976,5246976,5246976,5246976,40960,44032,48128,51200,55296,58368,62464,65536,62464,65536,62464,65536,268439552,336991254,57,1351225344,80,7488,192,1048526591,16780300,50465804,576,36,224,1123024896,1123024896,1123024896,1098907648,1098907648,0,968256,1073741824,967680,44032,44032,40960,5164032,1073741824,5160960,17134,1107575540,4294941932,4294967294,17134,1107575540,4294941932,4294967294,17134,1107575540,4294941932,4294967294,
602112,645120,688128,731136,774144,817152,9160704,9203712,9203712,9203712,9203712,9203712,49152,53248,57344,61440,65536,69632,40960,45056,45056,45056,45056,45056,1616904192,320017685,57,1623994368,80,7488,192,1048526591,16780300,50465804,576,36,128,1129578496,1129578496,1098907648,1119879168,1119879168,0,1713024,1073741824,129024,32768,32768,53248,9136128,1073741824,688128,1119765323,1121993558,4294940748,94,1119765323,1121993558,4294940748,94,16736,1099973016,4294967052,4294967294,
5720064,5763072,5806080,5849088,5892096,5935104,5978112,6021120,6021120,6021120,6021120,6021120,40960,45056,49152,53248,57344,61440,65536,69632,69632,69632,69632,69632,538976288,353703189,57,2153382144,80,7488,192,1048526463,16780300,50465804,576,36,128,1124728832,1124597760,1124466688,1107296256,1107296256,1107296256,1113984,1097856,1081728,57344,49152,40960,5941248,5855232,5769216,1106264837,1112032012,4294951372,30,1106264837,1112032012,4294951372,30,1106264837,1112032012,4294951372,30,
25152,25152,25344,25344,25536,25536,25728,25728,25728,25728,25728,25728,40960,44032,48128,51200,55296,58368,62464,65536,62464,65536,62464,65536,268439552,336991254,410,2426544128,80,7984,43008,1056915199,50465804,16780300,36,80,224,1124335616,1124335616,1124335616,1098907648,1098907648,0,133776,1073741824,4752,44032,44032,40960,713472,1073741824,25344,17155,1107575558,4294939244,4294967294,17155,1107575558,4294939244,4294967294,17155,1107575558,4294939244,4294967294,
34368,34368,34560,34560,34752,34752,34944,34944,34944,34944,34944,34944,40960,44032,48128,51200,55296,58368,62464,65536,62464,65536,62464,65536,268439552,336991254,410,2956075008,80,7984,43008,1056914943,50465804,16780300,36,80,224,1127481344,1127481344,1127481344,1098907648,1098907648,0,135504,1073741824,6480,44032,44032,40960,722688,1073741824,34560,17203,1107575606,4294928492,4294967294,17203,1107575606,4294928492,4294967294,17203,1107575606,4294928492,4294967294,
7268352,7268352,7311360,7311360,7354368,7354368,7397376,7397376,7397376,7397376,7397376,7397376,40960,44032,48128,51200,55296,58368,62464,65536,62464,65536,62464,65536,268439552,336991254,57,3229224960,80,7488,192,1056915071,16780300,50465804,576,36,224,1126825984,1126825984,1126825984,1098907648,1098907648,0,1371456,1073741824,1370880,44032,44032,40960,7314432,1073741824,7311360,17193,1107575596,4294930732,4294967294,17193,1107575596,4294930732,4294967294,17193,1107575596,4294930732,4294967294,
9504768,9504768,9547776,9547776,9590784,9590784,9590784,9590784,9590784,9590784,9590784,9590784,40960,44032,48128,51200,55296,58368,62464,65536,62464,65536,62464,65536,268439552,336991254,57,3502379008,80,7488,192,1056915199,16780300,50465804,576,36,224,1130233856,1130233856,1130233856,1098907648,1098907648,0,1790784,1073741824,1790208,44032,44032,40960,9550848,1073741824,9547776,17245,1107575647,4294919084,4294967294,17245,1107575647,4294919084,4294967294,17245,1107575647,4294919084,4294967294,
3397632,3397632,3440640,3440640,3483648,3483648,3526656,3526656,3526656,3526656,3526656,3526656,40960,44032,48128,51200,55296,58368,62464,65536,62464,65536,62464,65536,268439552,336991254,57,1079119872,80,7488,192,1040137855,16780300,50465804,576,36,224,1117782016,1117782016,1117782016,1098907648,1098907648,0,645696,1073741824,645120,44032,44032,40960,3443712,1073741824,3440640,17054,1107575460,4294950892,4294967294,17054,1107575460,4294950892,4294967294,17054,1107575460,4294950892,4294967294,
5203968,5203968,5246976,5246976,5289984,5289984,5332992,5332992,5332992,5332992,5332992,5332992,40960,44032,48128,51200,55296,58368,62464,65536,62464,65536,62464,65536,268439552,336991254,57,1351749632,80,7488,192,1048526591,16780300,50465804,576,36,224,1123287040,1123287040,1123287040,1098907648,1098907648,0,984384,1073741824,983808,44032,44032,40960,5250048,1073741824,5246976,17138,1107575544,4294941484,4294967294,17138,1107575544,4294941484,4294967294,17138,1107575544,4294941484,4294967294,
688128,731136,774144,817152,860160,903168,9590784,9590784,9590784,9590784,9590784,9590784,49152,53248,57344,61440,65536,69632,40960,45056,45056,45056,45056,45056,1616904192,320017685,57,1624649728,80,7488,192,1048526591,16780300,50465804,576,36,128,1130233856,1130233856,1099956224,1119879168,1119879168,0,1793664,1073741824,145152,32768,32768,53248,9566208,1073741824,774144,1119765333,1121993567,4294939468,94,1119765333,1121993567,4294939468,94,16768,1099973032,4294966796,4294967294,
16704,16704,16896,16896,17088,17088,17280,17280,17280,17280,17280,17280,40960,44032,48128,51200,55296,58368,62464,65536,62464,65536,62464,65536,268439552,336991254,410,2153914368,80,7984,43008,1048526463,50465804,16780300,36,80,224,1118830592,1118830592,1118830592,1098907648,1098907648,0,132192,1073741824,3168,44032,44032,40960,705024,1073741824,16896,17070,1107575476,4294949100,4294967294,17070,1107575476,4294949100,4294967294,17070,1107575476,4294949100,4294967294,
25536,25536,25728,25728,25920,25920,26112,26112,26112,26112,26112,26112,40960,44032,48128,51200,55296,58368,62464,65536,62464,65536,62464,65536,268439552,336991254,410,2427068416,80,7984,43008,1056915199,50465804,16780300,36,80,224,1124466688,1124466688,1124466688,1098907648,1098907648,0,133848,1073741824,4824,44032,44032,40960,713856,1073741824,25728,17157,1107575560,4294938796,4294967294,17157,1107575560,4294938796,4294967294,17157,1107575560,4294938796,4294967294,
34752,34752,34944,34944,35136,35136,35328,35328,35328,35328,35328,35328,40960,44032,48128,51200,55296,58368,62464,65536,62464,65536,62464,65536,268439552,336991254,410,2956599296,80,7984,43008,1056914943,50465804,16780300,36,80,224,1127612416,1127612416,1127612416,1098907648,1098907648,0,135576,1073741824,6552,44032,44032,40960,723072,1073741824,34944,17205,1107575608,4294928044,4294967294,17205,1107575608,4294928044,4294967294,17205,1107575608,4294928044,4294967294,
8558592,8601600,8644608,8687616,8730624,8773632,8816640,8859648,8859648,8859648,8859648,8859648,40960,45056,49152,53248,57344,61440,65536,69632,69632,69632,69632,69632,538976288,353703189,57,3229745408,80,7488,192,1056915199,16780300,50465804,576,36,128,1129054208,1128923136,1128792064,1107296256,1107296256,1107296256,1646208,1630080,1613952,57344,49152,40960,8779776,8693760,8607744,1106264903,1112032078,4294942924,30,1106264903,1112032078,4294942924,30,1106264903,1112032078,4294942924,30,
516096,559104,602112,645120,688128,731136,9074688,9117696,9117696,9117696,9117696,9117696,49152,53248,57344,61440,65536,69632,40960,45056,45056,45056,45056,45056,3503292416,320017685,57,3502912000,80,7488,192,1056915199,16780300,50465804,576,36,128,1129447424,1129447424,1096810496,1129316352,1129316352,0,1700928,1073741824,112896,32768,32768,53248,9071616,1073741824,602112,1129268041,1130316628,4294940332,206,1129268041,1130316628,4294940332,206,16704,1099973000,12,4294967294,
3483648,3483648,3526656,3526656,3569664,3569664,3612672,3612672,3612672,3612672,3612672,3612672,40960,44032,48128,51200,55296,58368,62464,65536,62464,65536,62464,65536,268439552,336991254,57,1079644160,80,7488,192,1040137855,16780300,50465804,576,36,224,1118044160,1118044160,1118044160,1098907648,1098907648,0,661824,1073741824,661248,44032,44032,40960,3529728,1073741824,3526656,17058,1107575464,4294950444,4294967294,17058,1107575464,4294950444,4294967294,17058,1107575464,4294950444,4294967294,
5289984,5289984,5332992,5332992,5376000,5376000,5419008,5419008,5419008,5419008,5419008,5419008,40960,44032,48128,51200,55296,58368,62464,65536,62464,65536,62464,65536,268439552,336991254,57,1352273920,80,7488,192,1048526591,16780300,50465804,576,36,224,1123549184,1123549184,1123549184,1098907648,1098907648,0,1000512,1073741824,999936,44032,44032,40960,5336064,1073741824,5332992,17142,1107575548,4294941036,4294967294,17142,1107575548,4294941036,4294967294,17142,1107575548,4294941036,4294967294,
7488,7488,7680,7680,7872,7872,8064,8064,8064,8064,8064,8064,40960,44032,48128,51200,55296,58368,62464,65536,62464,65536,62464,65536,268439552,336991254,410,1881284608,80,7984,43008,1048526335,50465804,16780300,36,80,224,1109393408,1109393408,1109393408,1098907648,1098907648,0,130464,1073741824,1440,44032,44032,40960,695808,1073741824,7680,16924,1107575336,4294959852,4294967294,16924,1107575336,4294959852,4294967294,16924,1107575336,4294959852,4294967294,
17088,17088,17280,17280,17472,17472,17664,17664,17664,17664,17664,17664,40960,44032,48128,51200,55296,58368,62464,65536,62464,65536,62464,65536,268439552,336991254,410,2154438656,80,7984,43008,1048526463,50465804,16780300,36,80,224,1119092736,1119092736,1119092736,1098907648,1098907648,0,132264,1073741824,3240,44032,44032,40960,705408,1073741824,17280,17074,1107575480,4294948652,4294967294,17074,1107575480,4294948652,4294967294,17074,1107575480,4294948652,4294967294,
25920,25920,26112,26112,26304,26304,26496,26496,26496,26496,26496,26496,40960,44032,48128,51200,55296,58368,62464,65536,62464,65536,62464,65536,268439552,336991254,410,2427592704,80,7984,43008,1056915199,50465804,16780300,36,80,224,1124597760,1124597760,1124597760,1098907648,1098907648,0,133920,1073741824,4896,44032,44032,40960,714240,1073741824,26112,17159,1107575562,4294938348,4294967294,17159,1107575562,4294938348,4294967294,17159,1107575562,4294938348,4294967294,
35136,35136,35328,35328,35520,35520,35712,35712,35712,35712,35712,35712,40960,44032,48128,51200,55296,58368,62464,65536,62464,65536,62464,65536,268439552,336991254,410,2957123584,80,7984,43008,1056915071,50465804,16780300,36,80,224,1127743488,1127743488,1127743488,1098907648,1098907648,0,135648,1073741824,6624,44032,44032,40960,723456,1073741824,35328,17207,1107575610,4294927596,4294967294,17207,1107575610,4294927596,4294967294,17207,1107575610,4294927596,4294967294,
7354368,7354368,7397376,7397376,7440384,7440384,7483392,7483392,7483392,7483392,7483392,7483392,40960,44032,48128,51200,55296,58368,62464,65536,62464,65536,62464,65536,268439552,336991254,57,3230273536,80,7488,192,1056915199,16780300,50465804,576,36,224,1126957056,1126957056,1126957056,1098907648,1098907648,0,1387584,1073741824,1387008,44032,44032,40960,7400448,1073741824,7397376,17195,1107575598,4294930284,4294967294,17195,1107575598,4294930284,4294967294,17195,1107575598,4294930284,4294967294,
1290240,1333248,1376256,1419264,1462272,9504768,9547776,9590784,9590784,9590784,9590784,9590784,57344,61440,65536,69632,73728,40960,45056,49152,53248,53248,53248,53248,3503345872,353703189,57,3503555840,64,7488,192,1056915199,16780300,50465804,576,36,128,1130102784,1130233856,1107296256,1129316352,1129316352,1129316352,1781568,1797696,265536,32768,40960,61440,9501696,9587712,1416192,1129268051,1130316639,4294939052,206,1129268051,1130316639,4294939052,206,1129202160,1130316296,4294964012,206};
__device__ __forceinline__ float bf_lo(unsigned u) { return __uint_as_float(u << 16); }
__device__ __forceinline__ float bf_hi(unsigned u) { return __uint_as_float(u & 0xffff0000u); }
__device__ __forceinline__ unsigned cvt_pk(float lo, float hi) { bf16x2 r = {(__bf16)lo, (__bf16)hi}; return __builtin_bit_cast(unsigned, r); }
__device__ __forceinline__ float rb(float v) { return bf_lo(cvt_pk(v, 0.f)); }
__global__ __launch_bounds__(256) __attribute__((aligned(4096))) void dcn_kernel(const unsigned short* x_, const unsigned short* off_, const unsigned short* w_, unsigned short* out_, void* ws_) {
    __shared__ __attribute__((aligned(128))) unsigned char lds[163840];
    asm volatile("" ::"v"((__attribute__((address_space(3))) unsigned char*)lds));
    unsigned tid = threadIdx.x, bid = blockIdx.x;
    const __attribute__((address_space(4))) Args* ka = (const __attribute__((address_space(4))) Args*)__builtin_amdgcn_kernarg_segment_ptr();
    asm volatile(
        "s_load_dwordx8 s[48:55], %1, 0\n\t"
        "v_mov_b32 v156, %0\n\t"
        "s_mov_b32 s42, %2\n\t"
        "s_getpc_b64 s[44:45]\n\t"
        "s_and_b32 s44, s44, 0xffffff00\n\t"
        "9:\n\t"
        "s_mov_b32 s46, 99f-9b+3300\n\t"
        "s_mov_b32 s47, 0x20000\n\t"
        "v_lshlrev_b32 v160, 12, v156\n\t"
        "s_lshl_b32 s43, s42, 4\n\t"
        "s_and_b32 s43, s43, 3968\n\t"
        "s_mov_b32 m0, 161856\n\t"
        "v_cmp_gt_u32 vcc, 64, v156\n\t"
        "s_nop 1\n\t"
        "s_cbranch_vccz 10f\n\t"
        "buffer_load_dword v160, s[44:47], s43 offen lds\n\t"
        "10:\n\t"
        "s_mov_b64 s[64:65], %3\n\t"
        "v_and_b32 v157, 63, v156\n\t"
        "s_mov_b32 s66, 65536\n\t"
        "s_mov_b32 s67, 0x20000\n\t"
        "v_lshlrev_b32 v155, 2, v157\n\t"
        "s_lshl_b32 s70, s42, 8\n\t"
        "buffer_load_dword v155, v155, s[64:67], s70 offen\n\t"
        "v_lshrrev_b32 v73, 6, v156\n\t"
        "v_and_b32 v157, 63, v156\n\t"
        "v_readfirstlane_b32 s4, v73\n\t"
        "v_lshlrev_b32 v140, 4, v157\n\t"
        "s_mov_b32 s26, 165888\n\t"
        "s_mov_b32 s27, 0x20000\n\t"
        "v_xor_b32 v142, 32, v140\n\t"
        "v_and_b32 v73, 32, v157\n\t"
        "v_xor_b32 v141, v73, v140\n\t"
        "s_lshr_b32 s68, s4, 1\n\t"
        "s_mul_i32 s37, s4, 3072\n\t"
        "s_mul_i32 s36, s68, -61440\n\t"
        "s_add_u32 s36, s36, s37\n\t"
        "s_add_u32 s36, s36, 55296\n\t"
        "s_add_u32 s37, s37, 86016\n\t"
        "s_and_b32 s8, s42, 7\n\t"
        "s_lshl_b32 s8, s8, 5\n\t"
        "s_lshr_b32 s10, s42, 3\n\t"
        "s_add_u32 s10, s10, s8\n\t"
        "s_mul_i32 s8, s10, 2731\n\t"
        "s_sub_u32 s11, s10, 3\n\t"
        "s_mul_i32 s11, s11, 2850\n\t"
        "s_max_i32 s8, s8, s11\n\t"
        "s_lshr_b32 s8, s8, 16\n\t"
        "v_mul_u32_u24 v71, 171, v156\n\t"
        "v_lshrrev_b32 v71, 11, v71\n\t"
        "v_mul_u32_u24 v74, 12, v71\n\t"
        "v_sub_u32 v74, v156, v74\n\t"
        "s_mul_i32 s11, s8, 23\n\t"
        "s_sub_u32 s10, s10, s11\n\t"
        "s_min_u32 s11, s8, 3\n\t"
        "s_sub_u32 s10, s10, s11\n\t"
        "s_lshl_b32 s6, s8, 4\n\t"
        "s_add_u32 s6, s6, 48\n\t"
        "v_lshrrev_b32 v75, 1, v71\n\t"
        "v_and_b32 v75, 2, v75\n\t"
        "v_xor_b32 v74, v74, v75\n\t"
        "v_lshlrev_b32 v72, 4, v74\n\t"
        "s_lshl_b32 s8, s8, 1\n\t"
        "s_lshr_b32 s8, 1396799, s8\n\t"
        "s_and_b32 s8, s8, 3\n\t"
        "s_sub_u32 s8, s10, s8\n\t"
        "s_max_i32 s8, s8, 20\n\t"
        "s_lshl2_add_u32 s8, s10, s8\n\t"
        "s_lshl1_add_u32 s8, s8, -6\n\t"
        "v_add_u32 v73, -1, v71\n\t"
        "v_cmp_lt_u32 vcc, 17, v73\n\t"
        "v_mov_b32 v75, 0x7ff00000\n\t"
        "v_add3_u32 v161, v71, s6, -2\n\t"
        "v_cndmask_b32 v162, v72, v75, vcc\n\t"
        "s_movk_i32 s9, 192\n\t"
        "s_lshl_b32 s35, s4, 10\n\t"
        "s_mov_b32 s18, 9633792\n\t"
        "s_mov_b32 s19, 0x20000\n\t"
        "v_min_u32 v161, 223, v161\n\t"
        "s_mul_i32 s34, s8, 43008\n\t"
        "v_mad_u32_u24 v143, v161, s9, v162\n\t"
        "s_sub_u32 s34, s34, 43008\n\t"
        "s_lshr_b32 s12, s42, 3\n\t"
        "s_lshl_b32 s12, s12, 2\n\t"
        "s_add_u32 s12, s12, s4\n\t"
        "s_mul_i32 s12, s12, 1296\n\t"
        "s_and_b32 s12, s12, 0xffffff80\n\t"
        "v_min_u32 v164, 11, v157\n\t"
        "v_lshlrev_b32 v164, 7, v164\n\t"
        "s_mul_i32 s39, s68, 6144\n\t"
        "s_add_u32 s39, s39, 55296\n\t"
        "s_mul_i32 s38, s68, 159744\n\t"
        "s_add_u32 s38, s38, -104448\n\t"
        "s_mul_i32 s5, s68, -165888\n\t"
        "s_add_u32 s5, s5, 61440\n\t"
        "s_mov_b32 s22, 1806336\n\t"
        "s_mov_b32 s23, 0x20000\n\t"
        "s_mov_b32 s30, 9633792\n\t"
        "s_mov_b32 s31, 0x20000\n\t"
        "s_lshl_b32 s10, s4, 2\n\t"
        "s_add_u32 s10, s10, 161792\n\t"
        "v_mov_b32 v153, s10\n\t"
        "v_mov_b32 v182, 161792\n\t"
        "v_mov_b32 v76, 0\n\t"
        "v_mov_b32 v77, 0\n\t"
        "v_lshlrev_b32 v73, 3, v156\n\t"
        "v_add_u32 v73, 147456, v73\n\t"
        "s_add_u32 s60, s35, 153600\n\t"
        "v_mov_b32 v74, s60\n\t"
        "s_cmp_lt_u32 s4, 3\n\t"
        "s_cselect_b64 exec, -1, 0\n\t"
        "ds_write_b64 v73, v[76:77] offset:0\n\t"
        "ds_write_b64 v73, v[76:77] offset:2048\n\t"
        "ds_write_b64 v73, v[76:77] offset:4096\n\t"
        "s_mov_b64 exec, 1\n\t"
        "ds_write_b32 v153, v76 offset:16\n\t"
        "ds_write_b64 v74, v[76:77]\n\t"
        "s_mov_b64 exec, -1\n\t"
        "s_waitcnt lgkmcnt(0)\n\t"
        "s_mov_b64 s[24:25], s[52:53]\n\t"
        "s_mov_b64 s[16:17], s[48:49]\n\t"
        "s_add_u32 m0, s37, -36864\n\t"
        "s_nop 0\n\t"
        "buffer_load_dwordx4 v140, s[24:27], s36 offen offset:0 lds\n\t"
        "buffer_load_dwordx4 v141, s[24:27], s36 offen offset:1024 lds\n\t"
        "buffer_load_dwordx4 v142, s[24:27], s36 offen offset:2048 lds\n\t"
        "buffer_load_dword v165, v164, s[24:27], s12 offen\n\t"
        "s_add_u32 m0, s35, 8192\n\t"
        "s_add_u32 s43, s34, 86016\n\t"
        "buffer_load_dwordx4 v143, s[16:19], s43 offen  lds\n\t"
        "s_add_u32 m0, s35, 24576\n\t"
        "s_add_u32 s43, s34, 258048\n\t"
        "buffer_load_dwordx4 v143, s[16:19], s43 offen  lds\n\t"
        "s_mov_b64 s[20:21], s[50:51]\n\t"
        "s_mov_b64 s[28:29], s[54:55]\n\t"
        "s_mul_i32 s41, s8, 224\n\t"
        "s_add_u32 s41, s41, s6\n\t"
        "v_and_b32 v158, 15, v156\n\t"
        "s_and_b32 s49, s4, 1\n\t"
        "s_lshl_b32 s8, s49, 5\n\t"
        "v_xor_b32 v152, s8, v140\n\t"
        "s_mul_i32 s43, s4, 1536\n\t"
        "s_add_u32 m0, s43, 43008\n\t"
        "s_add_u32 s43, s43, 110592\n\t"
        "buffer_load_dwordx4 v152, s[24:27], s43 offen lds\n\t"
        "buffer_load_dwordx4 v152, s[24:27], s43 offen offset:512 lds\n\t"
        "s_add_u32 m0, s35, 12288\n\t"
        "s_add_u32 s43, s34, 129024\n\t"
        "buffer_load_dwordx4 v143, s[16:19], s43 offen  lds\n\t"
        "s_add_u32 m0, s35, 28672\n\t"
        "s_add_u32 s43, s34, 301056\n\t"
        "s_min_u32 s43, s43, 9590784\n\t"
        "buffer_load_dwordx4 v143, s[16:19], s43 offen  lds\n\t"
        "v_bfe_i32 v74, v156, 5, 1\n\t"
        "v_and_b32 v74, 7, v74\n\t"
        "v_xor_b32 v74, s4, v74\n\t"
        "v_mul_u32_u24 v68, 11, v74\n\t"
        "v_lshrrev_b32 v68, 5, v68\n\t"
        "v_mul_u32_u24 v75, 3, v68\n\t"
        "v_sub_u32 v69, v74, v75\n\t"
        "v_lshlrev_b32 v70, 2, v74\n\t"
        "v_mul_u32_u24 v74, 205, v156\n\t"
        "v_lshrrev_b32 v74, 14, v74\n\t"
        "v_min_u32 v74, 2, v74\n\t"
        "v_lshlrev_b32 v65, 2, v74\n\t"
        "v_mul_u32_u24 v74, 80, v74\n\t"
        "v_sub_u32 v66, v156, v74\n\t"
        "v_bfe_u32 v52, v156, 4, 1\n\t"
        "v_and_b32 v181, 48, v156\n\t"
        "s_mov_b64 s[56:57], 0\n\t"
        "s_mov_b32 s63, 0x435f0000\n\t"
        "s_mov_b32 s54, 0x7fff7fff\n\t"
        "v_bfe_u32 v159, v156, 4, 2\n\t"
        "v_mul_u32_u24 v62, 192, v158\n\t"
        "v_lshlrev_b32 v76, 3, v158\n\t"
        "v_and_b32 v77, 48, v76\n\t"
        "v_and_b32 v184, 24, v76\n\t"
        "s_mul_i32 s69, s68, 16384\n\t"
        "s_lshl_b32 s70, s49, 7\n\t"
        "s_lshl_b32 s71, s49, 5\n\t"
        "v_add_u32 v73, 8, v76\n\t"
        "v_and_b32 v73, 32, v73\n\t"
        "s_add_u32 s69, s69, 192\n\t"
        "v_xor_b32 v73, v181, v73\n\t"
        "v_add3_u32 v128, v73, v62, s69\n\t"
        "v_add_u32 v73, 16, v76\n\t"
        "v_and_b32 v73, 32, v73\n\t"
        "s_add_u32 s69, s69, 192\n\t"
        "v_xor_b32 v73, v181, v73\n\t"
        "v_add3_u32 v129, v73, v62, s69\n\t"
        "v_add_u32 v73, 24, v76\n\t"
        "v_and_b32 v73, 32, v73\n\t"
        "s_add_u32 s69, s69, 192\n\t"
        "v_xor_b32 v73, v181, v73\n\t"
        "v_add3_u32 v130, v73, v62, s69\n\t"
        "v_lshrrev_b32 v73, 2, v158\n\t"
        "v_lshl_add_u32 v74, v159, 3, v73\n\t"
        "v_mul_u32_u24 v74, 192, v74\n\t"
        "v_lshlrev_b32 v75, 1, v156\n\t"
        "v_and_b32 v75, 32, v75\n\t"
        "v_add3_u32 v74, v74, v75, v184\n\t"
        "v_add_u32 v74, 86016, v74\n\t"
        "v_xad_u32 v133, v74, s71, 64\n\t"
        "v_add_u32 v131, s70, v74\n\t"
        "v_xor_b32 v132, 32, v131\n\t"
        "v_subrev_u32 v138, 43008, v131\n\t"
        "v_subrev_u32 v139, 43008, v132\n\t"
        "v_subrev_u32 v149, 43008, v133\n\t"
        "v_lshrrev_b32 v74, 3, v157\n\t"
        "v_min_u32 v74, 2, v74\n\t"
        "v_lshlrev_b32 v81, 2, v74\n\t"
        "s_lshl_b32 s69, s4, 3\n\t"
        "v_and_or_b32 v82, v156, 7, s69\n\t"
        "v_mul_u32_u24 v80, 48, v82\n\t"
        "v_lshl_add_u32 v80, v74, 11, v80\n\t"
        "v_add_u32 v80, 147488, v80\n\t"
        "v_and_b32 v82, 15, v82\n\t"
        "v_and_b32 v74, 31, v156\n\t"
        "v_mad_u32_u24 v53, v74, 48, v70\n\t"
        "v_add_u32 v53, 147456, v53\n\t"
        "v_add_u32 v70, 157696, v70\n\t"
        "v_mul_u32_u24 v67, 320, v81\n\t"
        "s_add_u32 s69, s68, 1\n\t"
        "v_cvt_f32_u32 v56, s69\n\t"
        "v_add_u32 v57, 1, v82\n\t"
        "v_cvt_f32_u32 v57, v57\n\t"
        "v_lshrrev_b32 v83, 2, v157\n\t"
        "v_and_b32 v74, 3, v156\n\t"
        "v_lshlrev_b32 v119, 11, v83\n\t"
        "v_lshl_add_u32 v119, v74, 2, v119\n\t"
        "v_add_u32 v119, 122368, v119\n\t"
        "v_lshl_add_u32 v74, v74, 4, 64\n\t"
        "v_mad_u32_u24 v145, v83, s9, v74\n\t"
        "v_lshrrev_b32 v74, 1, v156\n\t"
        "v_and_or_b32 v74, v74, 16, v75\n\t"
        "v_add3_u32 v144, v74, v62, s70\n\t"
        "s_lshl_b32 s69, s68, 10\n\t"
        "s_add_u32 s69, s69, 135168\n\t"
        "v_add_u32 v74, v76, v159\n\t"
        "v_lshl_or_b32 v74, v74, 3, s71\n\t"
        "v_xad_u32 v146, v74, v77, s69\n\t"
        "s_lshl_b32 s70, s49, 11\n\t"
        "s_add_u32 s70, s70, s69\n\t"
        "v_lshlrev_b32 v74, 1, v156\n\t"
        "v_and_b32 v74, 48, v74\n\t"
        "v_xad_u32 v147, v74, v140, s70\n\t"
        "s_mul_i32 s48, s41, 36\n\t"
        "s_mul_i32 s41, s41, 192\n\t"
        "s_mul_i32 s69, s68, 172032\n\t"
        "s_add_u32 s41, s41, s69\n\t"
        "s_mul_i32 s69, s49, 43008\n\t"
        "s_add_u32 s50, s41, s69\n\t"
        "v_lshrrev_b32 v74, 5, v156\n\t"
        "v_mul_u32_u24 v74, 8064, v74\n\t"
        "v_and_b32 v55, 496, v140\n\t"
        "v_add_u32 v55, v55, v74\n\t"
        "v_lshl_add_u32 v61, v158, 2, v74\n\t"
        "s_mul_i32 s69, s68, 768\n\t"
        "s_add_u32 s69, s69, 147456\n\t"
        "v_mad_u32_u24 v187, v158, 48, s69\n\t"
        "v_lshl_add_u32 v74, v159, 3, v62\n\t"
        "v_xad_u32 v186, v74, v77, s37\n\t"
        "v_and_b32 v73, 2, v73\n\t"
        "v_add_u32 v73, v73, v159\n\t"
        "v_and_b32 v73, 3, v73\n\t"
        "v_and_or_b32 v73, v158, 12, v73\n\t"
        "s_add_u32 s69, s35, 157696\n\t"
        "v_lshl_add_u32 v185, v73, 4, s69\n\t"
        "v_add_u32 v118, s69, v140\n\t"
        "v_add_u32 v116, 1, v157\n\t"
        "v_mul_u32_u24 v74, 12288, v159\n\t"
        "v_add3_u32 v74, v74, v62, v77\n\t"
        "v_add_u32 v117, s37, v74\n\t"
        "v_lshlrev_b32 v73, 2, v158\n\t"
        "v_and_b32 v73, 16, v73\n\t"
        "s_mov_b32 s69, 0x0c0c0100\n\t"
        "v_alignbit_b32 v169, s69, s69, v73\n\t"
        "s_mov_b32 s69, 0x0c0c0302\n\t"
        "v_alignbit_b32 v171, s69, s69, v73\n\t"
        "v_mov_b32 v74, 0x0c0c0c0c\n\t"
        "s_mov_b32 s10, 2228241\n\t"
        "s_mov_b32 s11, 8912964\n\t"
        "v_cndmask_b32 v168, v74, v169, s[10:11]\n\t"
        "v_cndmask_b32 v170, v74, v171, s[10:11]\n\t"
        "s_mov_b32 s10, 2281718784\n\t"
        "s_mov_b32 s11, 570429696\n\t"
        "v_cndmask_b32 v169, v74, v169, s[10:11]\n\t"
        "v_cndmask_b32 v171, v74, v171, s[10:11]\n\t"
        "s_waitcnt vmcnt(10)\n\t"
        "v_readlane_b32 s7, v155, 27\n\t"
        "s_nop 1\n\t"
        "s_or_b32 s42, s42, s7\n\t"
        "s_bitcmp1_b32 s7, 8\n\t"
        "s_cbranch_scc1 20f\n\t"
        "s_bitcmp1_b32 s7, 9\n\t"
        "s_cbranch_scc1 21f\n\t"
        "ds_bpermute_b32 v64, v65, v155 offset:172\n\t"
        "s_add_u32 m0, s37, -24576\n\t"
        "s_add_u32 s36, s36, 6144\n\t"
        "buffer_load_dwordx4 v140, s[24:27], s36 offen offset:0 lds\n\t"
        "ds_bpermute_b32 v78, v81, v155 offset:148\n\t"
        "ds_bpermute_b32 v79, v81, v155 offset:160\n\t"
        "v_readlane_b32 s71, v155, 26\n\t"
        "v_readlane_b32 s10, v155, 29\n\t"
        "s_nop 0\n\t"
        "v_mul_u32_u24 v73, s71, v66\n\t"
        "v_lshrrev_b32 v73, 11, v73\n\t"
        "v_lshlrev_b32 v66, 4, v66\n\t"
        "v_mad_u32_u24 v73, v73, s10, v66\n\t"
        "s_waitcnt lgkmcnt(2)\n\t"
        "v_add_u32 v64, v64, v73\n\t"
        "buffer_load_dwordx4 v141, s[24:27], s36 offen offset:1024 lds\n\t"
        "v_readlane_b32 s69, v155, 34\n\t"
        "v_readlane_b32 s70, v155, 35\n\t"
        "s_nop 0\n\t"
        "v_mad_u32_u24 v70, v52, s69, v70\n\t"
        "v_mad_u32_u24 v70, v158, s70, v70\n\t"
        "s_mul_i32 s71, s69, s68\n\t"
        "s_add_u32 s71, s71, 157728\n\t"
        "v_mad_u32_u24 v67, v82, s70, v67\n\t"
        "v_add_u32 v67, s71, v67\n\t"
        "v_readlane_b32 s11, v155, 30\n\t"
        "buffer_load_dwordx4 v142, s[24:27], s36 offen offset:2048 lds\n\t"
        "s_movk_i32 s10, 223\n\t"
        "v_mov_b32 v75, 0x7ff00000\n\t"
        "v_readlane_b32 s12, v155, 24\n\t"
        "v_readlane_b32 s13, v155, 25\n\t"
        "s_nop 0\n\t"
        "s_bfe_u32 s70, s12, 524288\n\t"
        "s_bfe_u32 s71, s13, 524288\n\t"
        "v_cmp_gt_u32 vcc, s71, v71\n\t"
        "v_add3_u32 v73, v71, s70, -2\n\t"
        "v_med3_i32 v73, v73, 0, s10\n\t"
        "s_add_u32 m0, s35, 4096\n\t"
        "s_add_u32 s43, s34, 43008\n\t"
        "buffer_load_dwordx4 v143, s[16:19], s43 offen  lds\n\t"
        "v_cndmask_b32 v74, v75, v72, vcc\n\t"
        "v_mad_u32_u24 v134, v73, s11, v74\n\t"
        "s_bfe_u32 s70, s12, 524296\n\t"
        "s_bfe_u32 s71, s13, 524296\n\t"
        "v_cmp_gt_u32 vcc, s71, v71\n\t"
        "v_add3_u32 v73, v71, s70, -2\n\t"
        "v_med3_i32 v73, v73, 0, s10\n\t"
        "v_cndmask_b32 v74, v75, v72, vcc\n\t"
        "v_mad_u32_u24 v135, v73, s11, v74\n\t"
        "s_add_u32 m0, s35, 20480\n\t"
        "s_add_u32 s43, s34, 215040\n\t"
        "buffer_load_dwordx4 v143, s[16:19], s43 offen  lds\n\t"
        "s_bfe_u32 s70, s12, 524304\n\t"
        "s_bfe_u32 s71, s13, 524304\n\t"
        "v_cmp_gt_u32 vcc, s71, v71\n\t"
        "v_add3_u32 v73, v71, s70, -2\n\t"
        "v_med3_i32 v73, v73, 0, s10\n\t"
        "v_cndmask_b32 v74, v75, v72, vcc\n\t"
        "v_mad_u32_u24 v136, v73, s11, v74\n\t"
        "s_bfe_u32 s70, s12, 524312\n\t"
        "s_bfe_u32 s71, s13, 524312\n\t"
        "v_cmp_gt_u32 vcc, s71, v71\n\t"
        "v_add3_u32 v73, v71, s70, -2\n\t"
        "v_med3_i32 v73, v73, 0, s10\n\t"
        "v_cndmask_b32 v74, v75, v72, vcc\n\t"
        "v_mad_u32_u24 v137, v73, s11, v74\n\t"
        "s_mov_b64 exec, 0\n\t"
        "s_cmp_eq_u32 s4, 0\n\t"
        "s_cbranch_scc1 .LpM0%=\n\t"
        "s_cmp_eq_u32 s4, 1\n\t"
        "s_cbranch_scc1 .LpM1%=\n\t"
        "s_cmp_eq_u32 s4, 2\n\t"
        "s_cbranch_scc1 .LpM2%=\n\t"
        "s_branch .LwM%=\n\t"
        ".LrM%=:\n\t"
        "s_mov_b64 exec, -1\n\t"
        "s_waitcnt vmcnt(9)\n\t"
        "s_barrier\n\t"
        "ds_read_b64_tr_b16 a[0:1], v138 offset:6144\n\t"
        "ds_read_b64_tr_b16 a[2:3], v138 offset:6912\n\t"
        "ds_read_b64_tr_b16 a[4:5], v139 offset:6144\n\t"
        "ds_read_b64_tr_b16 a[6:7], v139 offset:6912\n\t"
        "ds_read_b64_tr_b16 a[8:9], v149 offset:6144\n\t"
        "ds_read_b64_tr_b16 a[10:11], v149 offset:6912\n\t"
        "ds_read_b128 v[84:87], v128 offset:8192\n\t"
        "ds_read_b64_tr_b16 a[12:13], v138 offset:12288\n\t"
        "ds_read_b64_tr_b16 a[14:15], v138 offset:13056\n\t"
        "ds_read_b64_tr_b16 a[16:17], v139 offset:12288\n\t"
        "ds_read_b64_tr_b16 a[18:19], v139 offset:13056\n\t"
        "ds_read_b64_tr_b16 a[20:21], v149 offset:12288\n\t"
        "ds_read_b64_tr_b16 a[22:23], v149 offset:13056\n\t"
        "ds_read_b128 v[88:91], v128 offset:8192\n\t"
        "s_add_u32 m0, s37, -12288\n\t"
        "s_add_u32 s36, s36, s39\n\t"
        "s_waitcnt lgkmcnt(7)\n\t"
        "v_mfma_f32_16x16x32_bf16 v[12:15], a[0:3], v[84:87], 0\n\t"
        "buffer_load_dwordx4 v140, s[24:27], s36 offen offset:0 lds\n\t"
        "buffer_load_dwordx4 v141, s[24:27], s36 offen offset:1024 lds\n\t"
        "buffer_load_dwordx4 v142, s[24:27], s36 offen offset:2048 lds\n\t"
        "s_add_u32 m0, s35, 16384\n\t"
        "s_add_u32 s43, s34, 172032\n\t"
        "buffer_load_dwordx4 v143, s[16:19], s43 offen  lds\n\t"
        "s_add_u32 m0, s35, 32768\n\t"
        "s_add_u32 s43, s34, 344064\n\t"
        "s_min_u32 s43, s43, 9590784\n\t"
        "buffer_load_dwordx4 v143, s[16:19], s43 offen  lds\n\t"
        "s_waitcnt vmcnt(10)\n\t"
        "s_barrier\n\t"
        "ds_read_b128 v[92:95], v128 offset:12288\n\t"
        "ds_read_b64_tr_b16 a[24:25], v138 offset:0\n\t"
        "v_mfma_f32_16x16x32_bf16 v[16:19], a[4:7], v[84:87], 0\n\t"
        "ds_read_b64_tr_b16 a[26:27], v138 offset:768\n\t"
        "s_add_u32 m0, s35, 157696\n\t"
        "s_nop 0\n\t"
        "buffer_load_dwordx4 v64, s[20:23], 0 offen lds\n\t"
        "s_add_u32 m0, s37, 0\n\t"
        "s_add_u32 s36, s36, s38\n\t"
        "v_mfma_f32_16x16x32_bf16 v[20:23], a[8:11], v[84:87], 0\n\t"
        "buffer_load_dwordx4 v140, s[24:27], s36 offen offset:0 lds\n\t"
        "ds_read_b64_tr_b16 a[28:29], v139 offset:0\n\t"
        "v_readlane_b32 s44, v155, 32\n\t"
        "s_waitcnt lgkmcnt(4)\n\t"
        "v_mfma_f32_16x16x32_bf16 v[24:27], a[12:15], v[88:91], 0\n\t"
        "ds_read_b64_tr_b16 a[30:31], v139 offset:768\n\t"
        "ds_read_b64_tr_b16 a[32:33], v149 offset:0\n\t"
        "ds_read_b64_tr_b16 a[34:35], v149 offset:768\n\t"
        "v_readlane_b32 s45, v155, 33\n\t"
        "v_mfma_f32_16x16x32_bf16 v[28:31], a[16:19], v[88:91], 0\n\t"
        "buffer_load_dwordx4 v141, s[24:27], s36 offen offset:1024 lds\n\t"
        "ds_read_b128 v[96:99], v128 offset:8192\n\t"
        "v_readlane_b32 s62, v155, 36\n\t"
        "v_mfma_f32_16x16x32_bf16 v[32:35], a[20:23], v[88:91], 0\n\t"
        "ds_read_b128 v[100:103], v128 offset:12288\n\t"
        "s_lshl_b32 s70, s62, 5\n\t"
        "s_waitcnt lgkmcnt(8)\n\t"
        "v_mfma_f32_16x16x32_bf16 v[24:27], a[0:3], v[92:95], v[24:27]\n\t"
        "s_waitcnt vmcnt(10)\n\t"
        "s_barrier\n\t"
        "ds_read_b64_tr_b16 a[36:37], v138 offset:18432\n\t"
        "ds_read_b64_tr_b16 a[38:39], v138 offset:19200\n\t"
        "v_mfma_f32_16x16x32_bf16 v[28:31], a[4:7], v[92:95], v[28:31]\n\t"
        "buffer_load_dwordx4 v142, s[24:27], s36 offen offset:2048 lds\n\t"
        "s_add_u32 m0, s35, 0\n\t"
        "ds_read_b64_tr_b16 a[40:41], v139 offset:18432\n\t"
        "s_add_u32 s43, s34, 0\n\t"
        "v_mfma_f32_16x16x32_bf16 v[32:35], a[8:11], v[92:95], v[32:35]\n\t"
        "ds_read_b64_tr_b16 a[42:43], v139 offset:19200\n\t"
        "ds_read_b64_tr_b16 a[44:45], v149 offset:18432\n\t"
        "s_sub_u32 s71, s70, 16384\n\t"
        "v_mfma_f32_16x16x32_bf16 v[36:39], a[12:15], v[92:95], 0\n\t"
        "buffer_load_dwordx4 v143, s[16:19], s43 offen  lds\n\t"
        "s_add_u32 m0, s35, 36864\n\t"
        "ds_read_b64_tr_b16 a[46:47], v149 offset:19200\n\t"
        "s_add_u32 s43, s34, 387072\n\t"
        "ds_read_b64_tr_b16 a[48:49], v138 offset:24576\n\t"
        "s_min_u32 s43, s43, 9590784\n\t"
        "v_mfma_f32_16x16x32_bf16 v[40:43], a[16:19], v[92:95], 0\n\t"
        "ds_read_b64_tr_b16 a[50:51], v138 offset:25344\n\t"
        "ds_read_b64_tr_b16 a[52:53], v139 offset:24576\n\t"
        "ds_read_b64_tr_b16 a[54:55], v139 offset:25344\n\t"
        "s_mul_i32 s71, s71, s68\n\t"
        "v_mfma_f32_16x16x32_bf16 v[44:47], a[20:23], v[92:95], 0\n\t"
        "buffer_load_dwordx4 v143, s[16:19], s43 offen  lds\n\t"
        "s_add_u32 m0, s37, 12288\n\t"
        "ds_read_b64_tr_b16 a[56:57], v149 offset:24576\n\t"
        "s_add_u32 s36, s36, s5\n\t"
        "ds_read_b64_tr_b16 a[58:59], v149 offset:25344\n\t"
        "s_waitcnt lgkmcnt(12)\n\t"
        "v_mfma_f32_16x16x32_bf16 v[0:3], a[24:27], v[96:99], 0\n\t"
        "ds_read_b128 v[104:107], v128 offset:8256\n\t"
        "v_add_u32 v172, s71, v128\n\t"
        "v_mfma_f32_16x16x32_bf16 v[4:7], a[28:31], v[96:99], 0\n\t"
        "s_mov_b32 exec_lo, 0\n\t"
        "s_mov_b32 exec_hi, 0xfff00000\n\t"
        "ds_write_b32 v119, v155\n\t"
        "s_mov_b64 exec, -1\n\t"
        "v_mfma_f32_16x16x32_bf16 v[8:11], a[32:35], v[96:99], 0\n\t"
        "buffer_load_dwordx4 v140, s[24:27], s36 offen offset:0 lds\n\t"
        "v_add_u32 v173, s71, v129\n\t"
        "v_mfma_f32_16x16x32_bf16 v[12:15], a[24:27], v[100:103], v[12:15]\n\t"
        "v_add_u32 v174, s71, v130\n\t"
        "s_add_u32 s71, s71, s70\n\t"
        "v_mfma_f32_16x16x32_bf16 v[16:19], a[28:31], v[100:103], v[16:19]\n\t"
        "v_add_u32 v175, s71, v128\n\t"
        "v_add_u32 v176, s71, v129\n\t"
        "v_mfma_f32_16x16x32_bf16 v[20:23], a[32:35], v[100:103], v[20:23]\n\t"
        "s_waitcnt vmcnt(9)\n\t"
        "s_barrier\n\t"
        "buffer_load_dwordx4 v141, s[24:27], s36 offen offset:1024 lds\n\t"
        "ds_read_b128 v[108:111], v128 offset:4096\n\t"
        "s_waitcnt lgkmcnt(2)\n\t"
        "v_mfma_f32_16x16x32_bf16 v[12:15], a[36:39], v[104:107], v[12:15]\n\t"
        "ds_read_b128 v[112:115], v128 offset:4160\n\t"
        "ds_read_b64_tr_b16 a[60:61], v138 offset:30720\n\t"
        "v_add_u32 v177, s71, v130\n\t"
        "v_mfma_f32_16x16x32_bf16 v[16:19], a[40:43], v[104:107], v[16:19]\n\t"
        "ds_read_b64_tr_b16 a[62:63], v138 offset:31488\n\t"
        "ds_read_b64_tr_b16 a[64:65], v139 offset:30720\n\t"
        "s_add_u32 s71, s71, s70\n\t"
        "v_mfma_f32_16x16x32_bf16 v[20:23], a[44:47], v[104:107], v[20:23]\n\t"
        "buffer_load_dwordx4 v142, s[24:27], s36 offen offset:2048 lds\n\t"
        "s_add_u32 m0, s37, 24576\n\t"
        "ds_read_b64_tr_b16 a[66:67], v139 offset:31488\n\t"
        "s_add_u32 s36, s36, s39\n\t"
        "v_mfma_f32_16x16x32_bf16 v[24:27], a[48:51], v[104:107], v[24:27]\n\t"
        "ds_read_b64_tr_b16 a[68:69], v149 offset:30720\n\t"
        "ds_read_b64_tr_b16 a[70:71], v149 offset:31488\n\t"
        "v_add_u32 v178, s71, v128\n\t"
        "v_mfma_f32_16x16x32_bf16 v[28:31], a[52:55], v[104:107], v[28:31]\n\t"
        "ds_read_b128 v[84:87], v128 offset:8256\n\t"
        "v_add_u32 v179, s71, v129\n\t"
        "v_mfma_f32_16x16x32_bf16 v[32:35], a[56:59], v[104:107], v[32:35]\n\t"
        "s_cmp_eq_u32 s69, 36\n\t"
        "s_cselect_b64 exec, -1, 0\n\t"
        "v_swap_b32 v68, v69\n\t"
        "v_swap_b32 v173, v175\n\t"
        "v_swap_b32 v174, v178\n\t"
        "v_swap_b32 v177, v179\n\t"
        "s_mov_b64 exec, -1\n\t"
        "s_waitcnt lgkmcnt(8)\n\t"
        "v_mfma_f32_16x16x32_bf16 v[0:3], a[0:3], v[108:111], v[0:3]\n\t"
        "ds_read_b128 v[88:91], v128 offset:12352\n\t"
        "ds_read_b64_tr_b16 a[72:73], v138 offset:36864\n\t"
        "v_add_u32 v180, s71, v130\n\t"
        "v_mfma_f32_16x16x32_bf16 v[4:7], a[4:7], v[108:111], v[4:7]\n\t"
        "ds_read_b64_tr_b16 a[74:75], v138 offset:37632\n\t"
        "ds_read_b64_tr_b16 a[76:77], v139 offset:36864\n\t"
        "v_add3_u32 v68, v68, v52, -1\n\t"
        "v_mfma_f32_16x16x32_bf16 v[8:11], a[8:11], v[108:111], v[8:11]\n\t"
        "ds_read_b64_tr_b16 a[78:79], v139 offset:37632\n\t"
        "ds_read_b64_tr_b16 a[80:81], v149 offset:36864\n\t"
        "v_add3_u32 v69, v69, v158, -1\n\t"
        "v_mfma_f32_16x16x32_bf16 v[12:15], a[12:15], v[108:111], v[12:15]\n\t"
        "ds_read_b64_tr_b16 a[82:83], v149 offset:37632\n\t"
        "v_cvt_f32_i32 v68, v68\n\t"
        "v_mfma_f32_16x16x32_bf16 v[16:19], a[16:19], v[108:111], v[16:19]\n\t"
        "v_cvt_f32_i32 v69, v69\n\t"
        "v_add_f32 v78, v78, v56\n\t"
        "v_mfma_f32_16x16x32_bf16 v[20:23], a[20:23], v[108:111], v[20:23]\n\t"
        "v_add_f32 v79, v79, v57\n\t"
        "v_readlane_b32 s69, v155, 31\n\t"
        "s_waitcnt lgkmcnt(7)\n\t"
        "v_mfma_f32_16x16x32_bf16 v[0:3], a[36:39], v[112:115], v[0:3]\n\t"
        "v_mov_b32 v154, s69\n\t"
        "v_mov_b32 v119, s62\n\t"
        "v_readlane_b32 s53, v155, 28\n\t"
        "v_mfma_f32_16x16x32_bf16 v[4:7], a[40:43], v[112:115], v[4:7]\n\t"
        "v_readlane_b32 s9, v155, 46\n\t"
        "s_sub_u32 s10, 43200, s11\n\t"
        "v_mfma_f32_16x16x32_bf16 v[8:11], a[44:47], v[112:115], v[8:11]\n\t"
        "s_mul_i32 s10, s10, s68\n\t"
        "s_sub_u32 s11, s11, 192\n\t"
        "v_mfma_f32_16x16x32_bf16 v[12:15], a[48:51], v[112:115], v[12:15]\n\t"
        "v_mad_u32_u24 v148, v158, s11, v144\n\t"
        "v_add_u32 v148, s10, v148\n\t"
        "v_mfma_f32_16x16x32_bf16 v[16:19], a[52:55], v[112:115], v[16:19]\n\t"
        "v_mad_u32_u24 v150, v83, s11, v145\n\t"
        "s_lshl_b32 s70, s49, 1\n\t"
        "v_mfma_f32_16x16x32_bf16 v[20:23], a[56:59], v[112:115], v[20:23]\n\t"
        "s_add_u32 s70, s70, 49\n\t"
        "v_readlane_b32 s52, v155, s70\n\t"
        "v_mfma_f32_16x16x32_bf16 v[0:3], a[60:63], v[84:87], v[0:3]\n\t"
        "s_nop 0\n\t"
        "s_add_u32 s52, s52, s10\n\t"
        "v_mfma_f32_16x16x32_bf16 v[4:7], a[64:67], v[84:87], v[4:7]\n\t"
        "v_mfma_f32_16x16x32_bf16 v[8:11], a[68:71], v[84:87], v[8:11]\n\t"
        "buffer_load_dwordx4 v140, s[24:27], s36 offen offset:0 lds\n\t"
        "s_waitcnt lgkmcnt(0)\n\t"
        "v_mfma_f32_16x16x32_bf16 v[12:15], a[60:63], v[88:91], v[12:15]\n\t"
        "v_mfma_f32_16x16x32_bf16 v[16:19], a[64:67], v[88:91], v[16:19]\n\t"
        "v_mfma_f32_16x16x32_bf16 v[20:23], a[68:71], v[88:91], v[20:23]\n\t"
        "v_mfma_f32_16x16x32_bf16 v[24:27], a[36:39], v[88:91], v[24:27]\n\t"
        "ds_read_b128 v[92:95], v128 offset:4224\n\t"
        "v_mfma_f32_16x16x32_bf16 v[28:31], a[40:43], v[88:91], v[28:31]\n\t"
        "buffer_load_dwordx4 v141, s[24:27], s36 offen offset:1024 lds\n\t"
        "v_mfma_f32_16x16x32_bf16 v[32:35], a[44:47], v[88:91], v[32:35]\n\t"
        "v_mfma_f32_16x16x32_bf16 v[36:39], a[48:51], v[88:91], v[36:39]\n\t"
        "ds_read_b128 v[96:99], v128 offset:8320\n\t"
        "v_mfma_f32_16x16x32_bf16 v[40:43], a[52:55], v[88:91], v[40:43]\n\t"
        "v_mfma_f32_16x16x32_bf16 v[44:47], a[56:59], v[88:91], v[44:47]\n\t"
        "buffer_load_dwordx4 v142, s[24:27], s36 offen offset:2048 lds\n\t"
        "s_waitcnt lgkmcnt(1)\n\t"
        "v_mfma_f32_16x16x32_bf16 v[0:3], a[72:75], v[92:95], v[0:3]\n\t"
        "ds_read_b128 v[100:103], v128 offset:12416\n\t"
        "v_mfma_f32_16x16x32_bf16 v[4:7], a[76:79], v[92:95], v[4:7]\n\t"
        "v_mfma_f32_16x16x32_bf16 v[8:11], a[80:83], v[92:95], v[8:11]\n\t"
        "buffer_load_dwordx4 v[48:51], v55, s[20:23], s48 offen\n\t"
        "buffer_load_dword v61, v61, s[20:23], s48 offen offset:512\n\t"
        "s_waitcnt vmcnt(10)\n\t"
        "s_barrier\n\t"
        "ds_read_b128 v[104:107], v128 offset:16384\n\t"
        "s_add_u32 m0, s37, 36864\n\t"
        "ds_read_b128 v[108:111], v128 offset:16448\n\t"
        "s_waitcnt lgkmcnt(3)\n\t"
        "v_mfma_f32_16x16x32_bf16 v[12:15], a[72:75], v[96:99], v[12:15]\n\t"
        "ds_read_b32 v64, v70 offset:0\n\t"
        "s_add_u32 s36, s36, s38\n\t"
        "ds_read_b64_tr_b16 a[84:85], v131 offset:0\n\t"
        "v_mfma_f32_16x16x32_bf16 v[16:19], a[76:79], v[96:99], v[16:19]\n\t"
        "ds_read_b32 v67, v67 offset:0\n\t"
        "ds_read_b64_tr_b16 a[86:87], v131 offset:768\n\t"
        "v_readlane_b32 s46, v155, 37\n\t"
        "v_mfma_f32_16x16x32_bf16 v[20:23], a[80:83], v[96:99], v[20:23]\n\t"
        "ds_read_b64_tr_b16 a[88:89], v132 offset:0\n\t"
        "ds_read_b64_tr_b16 a[90:91], v132 offset:768\n\t"
        "v_readlane_b32 s47, v155, 40\n\t"
        "s_waitcnt lgkmcnt(8)\n\t"
        "v_mfma_f32_16x16x32_bf16 v[24:27], a[72:75], v[100:103], v[24:27]\n\t"
        "buffer_load_dwordx4 v140, s[24:27], s36 offen offset:0 lds\n\t"
        "ds_read_b64_tr_b16 a[92:93], v133 offset:0\n\t"
        "v_add_f32 v58, s46, v68\n\t"
        "v_mfma_f32_16x16x32_bf16 v[28:31], a[76:79], v[100:103], v[28:31]\n\t"
        "ds_read_b64_tr_b16 a[94:95], v133 offset:768\n\t"
        "ds_read_b128 v[112:115], v128 offset:4224\n\t"
        "v_add_f32 v59, s47, v69\n\t"
        "v_mfma_f32_16x16x32_bf16 v[32:35], a[80:83], v[100:103], v[32:35]\n\t"
        "ds_read_b64_tr_b16 a[96:97], v131 offset:6144\n\t"
        "ds_read_b64_tr_b16 a[98:99], v131 offset:6912\n\t"
        "s_waitcnt lgkmcnt(12)\n\t"
        "v_mfma_f32_16x16x32_bf16 v[24:27], a[24:27], v[104:107], v[24:27]\n\t"
        "ds_read_b64_tr_b16 a[100:101], v132 offset:6144\n\t"
        "ds_read_b64_tr_b16 a[102:103], v132 offset:6912\n\t"
        "v_mfma_f32_16x16x32_bf16 v[28:31], a[28:31], v[104:107], v[28:31]\n\t"
        "buffer_load_dwordx4 v141, s[24:27], s36 offen offset:1024 lds\n\t"
        "ds_read_b64_tr_b16 a[104:105], v133 offset:6144\n\t"
        "v_mfma_f32_16x16x32_bf16 v[32:35], a[32:35], v[104:107], v[32:35]\n\t"
        "ds_read_b64_tr_b16 a[106:107], v133 offset:6912\n\t"
        "v_mfma_f32_16x16x32_bf16 v[36:39], a[0:3], v[104:107], v[36:39]\n\t"
        "ds_read_b128 v[84:87], v128 offset:8320\n\t"
        "s_waitcnt lgkmcnt(12)\n\t"
        "v_perm_b32 v56, v64, v64, s44\n\t"
        "v_perm_b32 v57, v64, v64, s45\n\t"
        "v_mfma_f32_16x16x32_bf16 v[40:43], a[4:7], v[104:107], v[40:43]\n\t"
        "ds_read_b32 v66, v70 offset:2560\n\t"
        "v_add_f32 v56, v56, v58\n\t"
        "v_mfma_f32_16x16x32_bf16 v[44:47], a[8:11], v[104:107], v[44:47]\n\t"
        "buffer_load_dwordx4 v142, s[24:27], s36 offen offset:2048 lds\n\t"
        "s_add_u32 m0, s37, 49152\n\t"
        "v_mfma_f32_16x16x32_bf16 v[24:27], a[60:63], v[108:111], v[24:27]\n\t"
        "ds_read_b128 v[88:91], v128 offset:12416\n\t"
        "s_add_u32 s36, s36, s5\n\t"
        "v_mfma_f32_16x16x32_bf16 v[28:31], a[64:67], v[108:111], v[28:31]\n\t"
        ".LpM0%=:\n\t"
        "s_cbranch_execz .LrM%=\n\t"
        "ds_read_b128 v[92:95], v128 offset:16512\n\t"
        "v_add_f32 v57, v57, v59\n\t"
        "v_mfma_f32_16x16x32_bf16 v[32:35], a[68:71], v[108:111], v[32:35]\n\t"
        "v_cvt_pk_bf16_f32 v52, v56, v57\n\t"
        "v_cvt_pk_bf16_f32 v58, v58, v59\n\t"
        "v_mfma_f32_16x16x32_bf16 v[36:39], a[36:39], v[108:111], v[36:39]\n\t"
        "buffer_load_dwordx4 v140, s[24:27], s36 offen offset:0 lds\n\t"
        "v_cmp_ne_u32 s[64:65], v52, v58\n\t"
        "v_mfma_f32_16x16x32_bf16 v[40:43], a[40:43], v[108:111], v[40:43]\n\t"
        "s_nop 0\n\t"
        "v_mbcnt_lo_u32_b32 v56, s64, 1\n\t"
        "v_mbcnt_hi_u32_b32 v56, s65, v56\n\t"
        "v_mfma_f32_16x16x32_bf16 v[44:47], a[44:47], v[108:111], v[44:47]\n\t"
        "s_bcnt1_i32_b64 s61, s[64:65]\n\t"
        "v_add_u32 v54, s61, 1\n\t"
        "s_waitcnt lgkmcnt(4)\n\t"
        "v_mfma_f32_16x16x32_bf16 v[12:15], a[84:87], v[112:115], v[12:15]\n\t"
        "v_lshl_add_u32 v56, v56, 3, s60\n\t"
        "v_perm_b32 v57, v67, v67, s45\n\t"
        "v_mfma_f32_16x16x32_bf16 v[16:19], a[88:91], v[112:115], v[16:19]\n\t"
        "s_mov_b64 exec, s[64:65]\n\t"
        "ds_write_b64 v56, v[52:53]\n\t"
        "s_mov_b64 exec, -1\n\t"
        "v_mfma_f32_16x16x32_bf16 v[20:23], a[92:95], v[112:115], v[20:23]\n\t"
        "buffer_load_dwordx4 v141, s[24:27], s36 offen offset:1024 lds\n\t"
        "v_swap_b32 v53, v80\n\t"
        "s_waitcnt lgkmcnt(3)\n\t"
        "v_mfma_f32_16x16x32_bf16 v[0:3], a[96:99], v[84:87], v[0:3]\n\t"
        "v_perm_b32 v56, v67, v67, s44\n\t"
        "v_add_f32 v56, v56, v78\n\t"
        "v_mfma_f32_16x16x32_bf16 v[4:7], a[100:103], v[84:87], v[4:7]\n\t"
        "v_add_f32 v57, v57, v79\n\t"
        "v_cvt_pk_bf16_f32 v52, v56, v57\n\t"
        "v_mfma_f32_16x16x32_bf16 v[8:11], a[104:107], v[84:87], v[8:11]\n\t"
        "v_cvt_pk_bf16_f32 v58, v78, v79\n\t"
        "v_cmp_ne_u32 s[64:65], v52, v58\n\t"
        "v_mfma_f32_16x16x32_bf16 v[24:27], a[84:87], v[84:87], v[24:27]\n\t"
        "buffer_load_dwordx4 v142, s[24:27], s36 offen offset:2048 lds\n\t"
        "v_readlane_b32 s40, v155, 12\n\t"
        "v_mfma_f32_16x16x32_bf16 v[28:31], a[88:91], v[84:87], v[28:31]\n\t"
        "v_readlane_b32 s43, v155, 0\n\t"
        "s_add_u32 m0, s40, s35\n\t"
        "s_and_b64 s[64:65], s[64:65], 0xffffff\n\t"
        "v_mfma_f32_16x16x32_bf16 v[32:35], a[92:95], v[84:87], v[32:35]\n\t"
        "v_mbcnt_lo_u32_b32 v56, s64, v54\n\t"
        "s_bcnt1_i32_b64 s46, s[64:65]\n\t"
        "s_waitcnt lgkmcnt(1)\n\t"
        "v_mfma_f32_16x16x32_bf16 v[12:15], a[96:99], v[88:91], v[12:15]\n\t"
        "s_waitcnt vmcnt(8)\n\t"
        "s_barrier\n\t"
        "ds_read_b128 v[96:99], v128 offset:0\n\t"
        "ds_read_b128 v[100:103], v128 offset:64\n\t"
        "v_mfma_f32_16x16x32_bf16 v[16:19], a[100:103], v[88:91], v[16:19]\n\t"
        "buffer_load_dwordx4 v134, s[16:19], s43 offen  lds\n\t"
        "ds_read_b128 v[104:107], v128 offset:128\n\t"
        "v_readlane_b32 s40, v155, 13\n\t"
        "v_mfma_f32_16x16x32_bf16 v[20:23], a[104:107], v[88:91], v[20:23]\n\t"
        "ds_read_b128 v[108:111], v128 offset:20480\n\t"
        "ds_read_b64_tr_b16 a[108:109], v131 offset:12288\n\t"
        "v_readlane_b32 s43, v155, 1\n\t"
        "v_mfma_f32_16x16x32_bf16 v[36:39], a[84:87], v[88:91], v[36:39]\n\t"
        "ds_read_b128 v[112:115], v128 offset:20544\n\t"
        "s_add_u32 m0, s40, s35\n\t"
        "ds_read_b64_tr_b16 a[110:111], v131 offset:13056\n\t"
        "v_mfma_f32_16x16x32_bf16 v[40:43], a[88:91], v[88:91], v[40:43]\n\t"
        "ds_read_b64_tr_b16 a[112:113], v132 offset:12288\n\t"
        "ds_read_b64_tr_b16 a[114:115], v132 offset:13056\n\t"
        "s_add_u32 s61, s61, s46\n\t"
        "v_mfma_f32_16x16x32_bf16 v[44:47], a[92:95], v[88:91], v[44:47]\n\t"
        "buffer_load_dwordx4 v135, s[16:19], s43 offen  lds\n\t"
        "ds_read_b64_tr_b16 a[116:117], v133 offset:12288\n\t"
        "v_readlane_b32 s40, v155, 14\n\t"
        "v_mfma_f32_16x16x32_bf16 v[24:27], a[96:99], v[92:95], v[24:27]\n\t"
        "ds_read_b128 v[84:87], v128 offset:20608\n\t"
        "ds_read_b64_tr_b16 a[118:119], v133 offset:13056\n\t"
        "v_readlane_b32 s43, v155, 2\n\t"
        "v_mfma_f32_16x16x32_bf16 v[28:31], a[100:103], v[92:95], v[28:31]\n\t"
        "ds_read_b64_tr_b16 a[120:121], v131 offset:18432\n\t"
        "s_add_u32 m0, s40, s35\n\t"
        "v_mfma_f32_16x16x32_bf16 v[32:35], a[104:107], v[92:95], v[32:35]\n\t"
        "ds_read_b64_tr_b16 a[122:123], v131 offset:19200\n\t"
        "v_add_u32 v54, s46, v54\n\t"
        "v_mfma_f32_16x16x32_bf16 v[36:39], a[72:75], v[92:95], v[36:39]\n\t"
        "buffer_load_dwordx4 v134, s[16:19], s43 offen  lds\n\t"
        "s_bitcmp1_b32 s42, 10\n\t"
        "s_cbranch_scc0 .Lx40%=\n\t"
        "v_readlane_b32 s40, v155, 20\n\t"
        "v_readlane_b32 s43, v155, 8\n\t"
        "s_nop 0\n\t"
        "s_add_u32 m0, s40, s35\n\t"
        "s_nop 2\n\t"
        "buffer_load_dwordx4 v136, s[16:19], s43 offen  lds\n\t"
        ".Lx40%=:\n\t"
        "v_mfma_f32_16x16x32_bf16 v[40:43], a[76:79], v[92:95], v[40:43]\n\t"
        "ds_read_b64_tr_b16 a[124:125], v132 offset:18432\n\t"
        "v_readlane_b32 s40, v155, 15\n\t"
        "v_mfma_f32_16x16x32_bf16 v[44:47], a[80:83], v[92:95], v[44:47]\n\t"
        "ds_read_b64_tr_b16 a[126:127], v132 offset:19200\n\t"
        "v_readlane_b32 s43, v155, 3\n\t"
        "s_waitcnt lgkmcnt(8)\n\t"
        "v_mfma_f32_16x16x32_bf16 v[0:3], a[12:15], v[96:99], v[0:3]\n\t"
        "ds_read_b64_tr_b16 a[128:129], v133 offset:18432\n\t"
        "s_add_u32 m0, s40, s35\n\t"
        "ds_read_b64_tr_b16 a[130:131], v133 offset:19200\n\t"
        "v_mfma_f32_16x16x32_bf16 v[4:7], a[16:19], v[96:99], v[4:7]\n\t"
        "ds_read_b128 v[88:91], v129 offset:0\n\t"
        "s_mov_b32 s3, s61\n\t"
        "v_mfma_f32_16x16x32_bf16 v[8:11], a[20:23], v[96:99], v[8:11]\n\t"
        "ds_read_b128 v[92:95], v129 offset:4096\n\t"
        "v_lshl_add_u32 v56, v56, 3, s60\n\t"
        "v_mfma_f32_16x16x32_bf16 v[0:3], a[48:51], v[100:103], v[0:3]\n\t"
        "s_mov_b64 exec, s[64:65]\n\t"
        "ds_write_b64 v56, v[52:53]\n\t"
        "s_mov_b64 exec, -1\n\t"
        "v_mfma_f32_16x16x32_bf16 v[4:7], a[52:55], v[100:103], v[4:7]\n\t"
        "ds_read_b64_tr_b16 a[132:133], v131 offset:24576\n\t"
        "v_add_u32 v53, 4096, v80\n\t"
        "v_mfma_f32_16x16x32_bf16 v[8:11], a[56:59], v[100:103], v[8:11]\n\t"
        "ds_read_b64_tr_b16 a[134:135], v131 offset:25344\n\t"
        "v_readlane_b32 s46, v155, 39\n\t"
        "v_mfma_f32_16x16x32_bf16 v[0:3], a[84:87], v[104:107], v[0:3]\n\t"
        "buffer_load_dwordx4 v135, s[16:19], s43 offen  lds\n\t"
        "s_add_u32 m0, s37, 0\n\t"
        "v_mfma_f32_16x16x32_bf16 v[4:7], a[88:91], v[104:107], v[4:7]\n\t"
        "ds_read_b64_tr_b16 a[136:137], v132 offset:24576\n\t"
        "s_add_u32 s36, s36, s39\n\t"
        "v_mfma_f32_16x16x32_bf16 v[8:11], a[92:95], v[104:107], v[8:11]\n\t"
        "ds_read_b64_tr_b16 a[138:139], v132 offset:25344\n\t"
        "v_readlane_b32 s47, v155, 42\n\t"
        "v_mfma_f32_16x16x32_bf16 v[36:39], a[24:27], v[108:111], v[36:39]\n\t"
        "ds_read_b64_tr_b16 a[140:141], v133 offset:24576\n\t"
        "v_perm_b32 v56, v66, v66, s44\n\t"
        "v_mfma_f32_16x16x32_bf16 v[40:43], a[28:31], v[108:111], v[40:43]\n\t"
        "ds_read_b64_tr_b16 a[142:143], v133 offset:25344\n\t"
        "v_perm_b32 v57, v66, v66, s45\n\t"
        "v_mfma_f32_16x16x32_bf16 v[44:47], a[32:35], v[108:111], v[44:47]\n\t"
        "buffer_load_dwordx4 v140, s[24:27], s36 offen offset:0 lds\n\t"
        "v_add_f32 v58, s46, v68\n\t"
        "v_mfma_f32_16x16x32_bf16 v[36:39], a[60:63], v[112:115], v[36:39]\n\t"
        "ds_read_b128 v[96:99], v129 offset:8192\n\t"
        "v_add_f32 v59, s47, v69\n\t"
        "v_mfma_f32_16x16x32_bf16 v[40:43], a[64:67], v[112:115], v[40:43]\n\t"
        "ds_read_b64_tr_b16 a[144:145], v131 offset:30720\n\t"
        "v_add_f32 v56, v56, v58\n\t"
        "v_mfma_f32_16x16x32_bf16 v[44:47], a[68:71], v[112:115], v[44:47]\n\t"
        "ds_read_b64_tr_b16 a[146:147], v131 offset:31488\n\t"
        "v_add_f32 v57, v57, v59\n\t"
        "s_waitcnt lgkmcnt(8)\n\t"
        "v_mfma_f32_16x16x32_bf16 v[36:39], a[96:99], v[84:87], v[36:39]\n\t"
        "buffer_load_dwordx4 v141, s[24:27], s36 offen offset:1024 lds\n\t"
        "v_max_f32 v56, 0, v56\n\t"
        "v_mfma_f32_16x16x32_bf16 v[40:43], a[100:103], v[84:87], v[40:43]\n\t"
        "ds_read_b128 v[100:103], v129 offset:12288\n\t"
        "v_max_f32 v57, 0, v57\n\t"
        "v_mfma_f32_16x16x32_bf16 v[44:47], a[104:107], v[84:87], v[44:47]\n\t"
        "ds_read_b64_tr_b16 a[148:149], v132 offset:30720\n\t"
        "v_max_f32 v58, 0, v58\n\t"
        "v_mfma_f32_16x16x32_bf16 v[0:3], a[120:123], v[88:91], v[0:3]\n\t"
        "ds_read_b64_tr_b16 a[150:151], v132 offset:31488\n\t"
        "v_max_f32 v59, 0, v59\n\t"
        "v_mfma_f32_16x16x32_bf16 v[4:7], a[124:127], v[88:91], v[4:7]\n\t"
        "buffer_load_dwordx4 v142, s[24:27], s36 offen offset:2048 lds\n\t"
        "s_add_u32 m0, s37, 12288\n\t"
        "v_mfma_f32_16x16x32_bf16 v[8:11], a[128:131], v[88:91], v[8:11]\n\t"
        "ds_read_b64_tr_b16 a[152:153], v133 offset:30720\n\t"
        "s_add_u32 s36, s36, s38\n\t"
        "v_mfma_f32_16x16x32_bf16 v[0:3], a[108:111], v[92:95], v[0:3]\n\t"
        "ds_read_b64_tr_b16 a[154:155], v133 offset:31488\n\t"
        "v_cvt_pk_bf16_f32 v52, v56, v57\n\t"
        "v_mfma_f32_16x16x32_bf16 v[4:7], a[112:115], v[92:95], v[4:7]\n\t"
        "v_cvt_pk_bf16_f32 v58, v58, v59\n\t"
        "v_cmp_ne_u32 s[64:65], v52, v58\n\t"
        "v_mfma_f32_16x16x32_bf16 v[8:11], a[116:119], v[92:95], v[8:11]\n\t"
        "s_nop 0\n\t"
        "v_mbcnt_lo_u32_b32 v56, s64, v54\n\t"
        "v_mbcnt_hi_u32_b32 v56, s65, v56\n\t"
        "v_mfma_f32_16x16x32_bf16 v[12:15], a[120:123], v[92:95], v[12:15]\n\t"
        "ds_read_b128 v[104:107], v129 offset:16384\n\t"
        "s_bcnt1_i32_b64 s46, s[64:65]\n\t"
        "v_mfma_f32_16x16x32_bf16 v[16:19], a[124:127], v[92:95], v[16:19]\n\t"
        "s_add_u32 s61, s61, s46\n\t"
        "v_min_u32 v56, 97, v56\n\t"
        "v_mfma_f32_16x16x32_bf16 v[20:23], a[128:131], v[92:95], v[20:23]\n\t"
        "v_lshl_add_u32 v56, v56, 3, s60\n\t"
        "s_waitcnt lgkmcnt(5)\n\t"
        "v_mfma_f32_16x16x32_bf16 v[0:3], a[132:135], v[96:99], v[0:3]\n\t"
        "s_mov_b64 exec, s[64:65]\n\t"
        "ds_write_b64 v56, v[52:53]\n\t"
        "s_mov_b64 exec, -1\n\t"
        "v_mfma_f32_16x16x32_bf16 v[4:7], a[136:139], v[96:99], v[4:7]\n\t"
        "s_cmp_gt_u32 s61, s53\n\t"
        "s_cselect_b32 s8, 1, 0\n\t"
        "v_mfma_f32_16x16x32_bf16 v[8:11], a[140:143], v[96:99], v[8:11]\n\t"
        "s_waitcnt vmcnt(14)\n\t"
        "v_and_b32 v48, s54, v48\n\t"
        "v_pk_max_u16 v60, v154, v48\n\t"
        "v_mfma_f32_16x16x32_bf16 v[12:15], a[108:111], v[96:99], v[12:15]\n\t"
        "ds_read_b128 v[108:111], v129 offset:20480\n\t"
        "v_and_b32 v49, s54, v49\n\t"
        "v_mfma_f32_16x16x32_bf16 v[16:19], a[112:115], v[96:99], v[16:19]\n\t"
        "v_pk_max_u16 v60, v60, v49\n\t"
        "v_and_b32 v50, s54, v50\n\t"
        "v_mfma_f32_16x16x32_bf16 v[20:23], a[116:119], v[96:99], v[20:23]\n\t"
        "v_pk_max_u16 v60, v60, v50\n\t"
        "v_and_b32 v51, s54, v51\n\t"
        "v_mfma_f32_16x16x32_bf16 v[24:27], a[120:123], v[96:99], v[24:27]\n\t"
        "v_pk_max_u16 v60, v60, v51\n\t"
        "s_waitcnt vmcnt(13)\n\t"
        "v_and_b32 v61, s54, v61\n\t"
        "v_mfma_f32_16x16x32_bf16 v[28:31], a[124:127], v[96:99], v[28:31]\n\t"
        "v_pk_max_u16 v60, v60, v61\n\t"
        "v_cmp_ne_u32 s[12:13], v60, v154\n\t"
        "v_mfma_f32_16x16x32_bf16 v[32:35], a[128:131], v[96:99], v[32:35]\n\t"
        "s_waitcnt vmcnt(7) lgkmcnt(2)\n\t"
        "s_barrier\n\t"
        "v_mfma_f32_16x16x32_bf16 v[12:15], a[132:135], v[100:103], v[12:15]\n\t"
        "buffer_load_dwordx4 v140, s[24:27], s36 offen offset:0 lds\n\t"
        "ds_read_b64_tr_b16 a[156:157], v131 offset:36864\n\t"
        "s_lshl_b32 s6, 256, s4\n\t"
        "v_mfma_f32_16x16x32_bf16 v[16:19], a[136:139], v[100:103], v[16:19]\n\t"
        "s_cmp_lg_u64 s[12:13], 0\n\t"
        "s_cselect_b32 s6, s6, 0\n\t"
        "ds_read_b64_tr_b16 a[158:159], v131 offset:37632\n\t"
        "ds_read_b64_tr_b16 a[160:161], v132 offset:36864\n\t"
        "v_mfma_f32_16x16x32_bf16 v[20:23], a[140:143], v[100:103], v[20:23]\n\t"
        "ds_read_b64_tr_b16 a[162:163], v132 offset:37632\n\t"
        "ds_read_b64_tr_b16 a[164:165], v133 offset:36864\n\t"
        "s_or_b32 s56, s56, s8\n\t"
        "v_mfma_f32_16x16x32_bf16 v[24:27], a[108:111], v[100:103], v[24:27]\n\t"
        "s_cmp_gt_u32 s3, 64\n\t"
        "s_cselect_b32 s8, 1, 0\n\t"
        "ds_read_b64_tr_b16 a[166:167], v133 offset:37632\n\t"
        "v_mfma_f32_16x16x32_bf16 v[28:31], a[112:115], v[100:103], v[28:31]\n\t"
        "buffer_load_dwordx4 v141, s[24:27], s36 offen offset:1024 lds\n\t"
        "s_or_b32 s56, s56, s8\n\t"
        "v_mfma_f32_16x16x32_bf16 v[32:35], a[116:119], v[100:103], v[32:35]\n\t"
        "ds_read_b128 v[112:115], v129 offset:64\n\t"
        "s_min_u32 s61, s61, 80\n\t"
        "v_mfma_f32_16x16x32_bf16 v[36:39], a[120:123], v[100:103], v[36:39]\n\t"
        "ds_read_b128 v[84:87], v129 offset:4160\n\t"
        "v_min_u32 v78, s61, v116\n\t"
        "v_mfma_f32_16x16x32_bf16 v[40:43], a[124:127], v[100:103], v[40:43]\n\t"
        "ds_read_b64_tr_b16 a[168:169], v131 offset:43008\n\t"
        "v_lshl_add_u32 v78, v78, 3, s60\n\t"
        "v_mfma_f32_16x16x32_bf16 v[44:47], a[128:131], v[100:103], v[44:47]\n\t"
        "ds_read_b64 v[62:63], v78\n\t"
        "v_cmp_ge_u32 s[66:67], s61, v116\n\t"
        "v_mfma_f32_16x16x32_bf16 v[24:27], a[132:135], v[104:107], v[24:27]\n\t"
        "buffer_load_dwordx4 v142, s[24:27], s36 offen offset:2048 lds\n\t"
        "v_readlane_b32 s40, v155, 16\n\t"
        "v_mfma_f32_16x16x32_bf16 v[28:31], a[136:139], v[104:107], v[28:31]\n\t"
        "ds_read_b64_tr_b16 a[170:171], v131 offset:43776\n\t"
        "v_readlane_b32 s43, v155, 4\n\t"
        "v_mfma_f32_16x16x32_bf16 v[32:35], a[140:143], v[104:107], v[32:35]\n\t"
        "ds_read_b64_tr_b16 a[172:173], v132 offset:43008\n\t"
        "s_add_u32 m0, s40, s35\n\t"
        "v_mfma_f32_16x16x32_bf16 v[36:39], a[108:111], v[104:107], v[36:39]\n\t"
        "ds_read_b64_tr_b16 a[174:175], v132 offset:43776\n\t"
        "ds_read_b64_tr_b16 a[176:177], v133 offset:43008\n\t"
        "s_mov_b32 s46, 0x07060302\n\t"
        "v_mfma_f32_16x16x32_bf16 v[40:43], a[112:115], v[104:107], v[40:43]\n\t"
        "buffer_load_dwordx4 v134, s[16:19], s43 offen  lds\n\t"
        "ds_read_b64_tr_b16 a[178:179], v133 offset:43776\n\t"
        "s_bitcmp1_b32 s42, 11\n\t"
        "s_cbranch_scc0 .Lx42%=\n\t"
        "v_readlane_b32 s40, v155, 21\n\t"
        "v_readlane_b32 s43, v155, 9\n\t"
        "s_nop 0\n\t"
        "s_add_u32 m0, s40, s35\n\t"
        "s_nop 2\n\t"
        "buffer_load_dwordx4 v137, s[16:19], s43 offen  lds\n\t"
        "v_readlane_b32 s40, v155, 22\n\t"
        "v_readlane_b32 s43, v155, 10\n\t"
        "s_nop 0\n\t"
        "s_add_u32 m0, s40, s35\n\t"
        "s_nop 2\n\t"
        "buffer_load_dwordx4 v136, s[16:19], s43 offen  lds\n\t"
        ".Lx42%=:\n\t"
        "v_mfma_f32_16x16x32_bf16 v[44:47], a[116:119], v[104:107], v[44:47]\n\t"
        "ds_read_b128 v[88:91], v129 offset:8256\n\t"
        "v_readlane_b32 s40, v155, 17\n\t"
        "s_waitcnt lgkmcnt(8)\n\t"
        "v_mfma_f32_16x16x32_bf16 v[36:39], a[132:135], v[108:111], v[36:39]\n\t"
        "ds_read_b128 v[92:95], v129 offset:12352\n\t"
        "v_readlane_b32 s43, v155, 5\n\t"
        "v_mfma_f32_16x16x32_bf16 v[40:43], a[136:139], v[108:111], v[40:43]\n\t"
        "ds_read_b64_tr_b16 a[180:181], v131 offset:49152\n\t"
        "s_add_u32 m0, s40, s35\n\t"
        "v_mfma_f32_16x16x32_bf16 v[44:47], a[140:143], v[108:111], v[44:47]\n\t"
        "s_mov_b64 exec, s[66:67]\n\t"
        "s_waitcnt lgkmcnt(8)\n\t"
        "ds_write_b32 v63, v117\n\t"
        "s_mov_b64 exec, -1\n\t"
        "v_mfma_f32_16x16x32_bf16 v[0:3], a[156:159], v[112:115], v[0:3]\n\t"
        "buffer_load_dwordx4 v135, s[16:19], s43 offen  lds\n\t"
        "v_readlane_b32 s40, v155, 18\n\t"
        "v_mfma_f32_16x16x32_bf16 v[4:7], a[160:163], v[112:115], v[4:7]\n\t"
        "ds_read_b64_tr_b16 a[182:183], v131 offset:49920\n\t"
        "v_readlane_b32 s43, v155, 6\n\t"
        "v_mfma_f32_16x16x32_bf16 v[8:11], a[164:167], v[112:115], v[8:11]\n\t"
        "ds_read_b64_tr_b16 a[184:185], v132 offset:49152\n\t"
        "s_add_u32 m0, s40, s35\n\t"
        "v_mfma_f32_16x16x32_bf16 v[0:3], a[144:147], v[84:87], v[0:3]\n\t"
        "ds_read_b64_tr_b16 a[186:187], v132 offset:49920\n\t"
        "v_and_b32 v78, 0xfffff800, v63\n\t"
        "v_mfma_f32_16x16x32_bf16 v[4:7], a[148:151], v[84:87], v[4:7]\n\t"
        "ds_read_b128 v[64:67], v78 offset:1536\n\t"
        "v_lshlrev_b32 v72, 16, v62\n\t"
        "v_mfma_f32_16x16x32_bf16 v[8:11], a[152:155], v[84:87], v[8:11]\n\t"
        "buffer_load_dwordx4 v136, s[16:19], s43 offen  lds\n\t"
        "s_bitcmp1_b32 s42, 11\n\t"
        "s_cbranch_scc0 .Lx44%=\n\t"
        "v_readlane_b32 s40, v155, 23\n\t"
        "v_readlane_b32 s43, v155, 11\n\t"
        "s_nop 0\n\t"
        "s_add_u32 m0, s40, s35\n\t"
        "s_nop 2\n\t"
        "buffer_load_dwordx4 v137, s[16:19], s43 offen  lds\n\t"
        ".Lx44%=:\n\t"
        "v_mfma_f32_16x16x32_bf16 v[12:15], a[156:159], v[84:87], v[12:15]\n\t"
        "ds_read_b128 v[96:99], v129 offset:16448\n\t"
        "ds_read_b64_tr_b16 a[188:189], v133 offset:49152\n\t"
        "v_readlane_b32 s40, v155, 19\n\t"
        "v_mfma_f32_16x16x32_bf16 v[16:19], a[160:163], v[84:87], v[16:19]\n\t"
        "ds_read_b64_tr_b16 a[190:191], v133 offset:49920\n\t"
        "v_readlane_b32 s43, v155, 7\n\t"
        "v_mfma_f32_16x16x32_bf16 v[20:23], a[164:167], v[84:87], v[20:23]\n\t"
        "ds_read_b64_tr_b16 a[192:193], v131 offset:55296\n\t"
        "s_add_u32 m0, s40, s35\n\t"
        "s_waitcnt lgkmcnt(8)\n\t"
        "v_mfma_f32_16x16x32_bf16 v[0:3], a[168:171], v[88:91], v[0:3]\n\t"
        "buffer_load_dwordx4 v137, s[16:19], s43 offen  lds\n\t"
        "s_add_u32 m0, s37, 24576\n\t"
        "v_mfma_f32_16x16x32_bf16 v[4:7], a[172:175], v[88:91], v[4:7]\n\t"
        "ds_read_b64_tr_b16 a[194:195], v131 offset:56064\n\t"
        "s_add_u32 s36, s36, s5\n\t"
        "v_mfma_f32_16x16x32_bf16 v[8:11], a[176:179], v[88:91], v[8:11]\n\t"
        "ds_read_b64_tr_b16 a[196:197], v132 offset:55296\n\t"
        "v_and_b32 v73, 0xffff0000, v62\n\t"
        "v_mfma_f32_16x16x32_bf16 v[12:15], a[144:147], v[88:91], v[12:15]\n\t"
        "ds_read_b128 v[100:103], v129 offset:20544\n\t"
        "v_med3_f32 v72, v72, 0, s63\n\t"
        "v_mfma_f32_16x16x32_bf16 v[16:19], a[148:151], v[88:91], v[16:19]\n\t"
        "buffer_load_dwordx4 v140, s[24:27], s36 offen offset:0 lds\n\t"
        "v_med3_f32 v73, v73, 0, s63\n\t"
        "v_mfma_f32_16x16x32_bf16 v[20:23], a[152:155], v[88:91], v[20:23]\n\t"
        "ds_read_b64_tr_b16 a[198:199], v132 offset:56064\n\t"
        "v_perm_b32 v62, v73, v72, s46\n\t"
        "v_mfma_f32_16x16x32_bf16 v[24:27], a[156:159], v[88:91], v[24:27]\n\t"
        "ds_read_b64_tr_b16 a[200:201], v133 offset:55296\n\t"
        "v_fract_f32 v74, v72\n\t"
        "v_mfma_f32_16x16x32_bf16 v[28:31], a[160:163], v[88:91], v[28:31]\n\t"
        "ds_read_b64_tr_b16 a[202:203], v133 offset:56064\n\t"
        "v_fract_f32 v75, v73\n\t"
        "v_mfma_f32_16x16x32_bf16 v[32:35], a[164:167], v[88:91], v[32:35]\n\t"
        "buffer_load_dwordx4 v141, s[24:27], s36 offen offset:1024 lds\n\t"
        "v_cvt_u32_f32 v76, v72\n\t"
        "v_mfma_f32_16x16x32_bf16 v[12:15], a[168:171], v[92:95], v[12:15]\n\t"
        "ds_read_b128 v[104:107], v129 offset:128\n\t"
        "v_cvt_u32_f32 v77, v73\n\t"
        "v_mfma_f32_16x16x32_bf16 v[16:19], a[172:175], v[92:95], v[16:19]\n\t"
        "ds_read_b128 v[108:111], v129 offset:4224\n\t"
        "v_cmp_neq_f32 s[64:65], 0, v74\n\t"
        "v_mfma_f32_16x16x32_bf16 v[20:23], a[176:179], v[92:95], v[20:23]\n\t"
        "v_cmp_neq_f32 s[44:45], 0, v75\n\t"
        "v_sub_f32 v78, 1.0, v74\n\t"
        "v_mfma_f32_16x16x32_bf16 v[24:27], a[144:147], v[92:95], v[24:27]\n\t"
        "buffer_load_dwordx4 v142, s[24:27], s36 offen offset:2048 lds\n\t"
        "s_add_u32 m0, s37, 36864\n\t"
        "v_mfma_f32_16x16x32_bf16 v[28:31], a[148:151], v[92:95], v[28:31]\n\t"
        "s_add_u32 s36, s36, s39\n\t"
        "v_sub_f32 v79, 1.0, v75\n\t"
        "v_mfma_f32_16x16x32_bf16 v[32:35], a[152:155], v[92:95], v[32:35]\n\t"
        "s_waitcnt vmcnt(10)\n\t"
        "s_barrier\n\t"
        "v_mfma_f32_16x16x32_bf16 v[36:39], a[156:159], v[92:95], v[36:39]\n\t"
        "ds_read_b64_tr_b16 a[204:205], v131 offset:0\n\t"
        "v_cvt_pk_bf16_f32 v79, v78, v79\n\t"
        "v_mfma_f32_16x16x32_bf16 v[40:43], a[160:163], v[92:95], v[40:43]\n\t"
        "buffer_load_dwordx4 v140, s[24:27], s36 offen offset:0 lds\n\t"
        "v_lshlrev_b32 v78, 16, v79\n\t"
        "v_mfma_f32_16x16x32_bf16 v[44:47], a[164:167], v[92:95], v[44:47]\n\t"
        "ds_read_b64_tr_b16 a[206:207], v131 offset:768\n\t"
        "v_and_b32 v79, 0xffff0000, v79\n\t"
        "s_waitcnt lgkmcnt(4)\n\t"
        "v_mfma_f32_16x16x32_bf16 v[24:27], a[168:171], v[96:99], v[24:27]\n\t"
        "ds_read_b64_tr_b16 a[208:209], v132 offset:0\n\t"
        "v_mul_f32 v80, v78, v79\n\t"
        "v_mfma_f32_16x16x32_bf16 v[28:31], a[172:175], v[96:99], v[28:31]\n\t"
        "ds_read_b64_tr_b16 a[210:211], v132 offset:768\n\t"
        "v_mul_f32 v78, v78, v75\n\t"
        "v_mfma_f32_16x16x32_bf16 v[32:35], a[176:179], v[96:99], v[32:35]\n\t"
        "buffer_load_dwordx4 v141, s[24:27], s36 offen offset:1024 lds\n\t"
        "v_mul_f32 v79, v74, v79\n\t"
        "v_mfma_f32_16x16x32_bf16 v[36:39], a[144:147], v[96:99], v[36:39]\n\t"
        "ds_read_b64_tr_b16 a[212:213], v133 offset:0\n\t"
        "ds_read_b64_tr_b16 a[214:215], v133 offset:768\n\t"
        "v_mul_f32 v81, v74, v75\n\t"
        "v_mfma_f32_16x16x32_bf16 v[40:43], a[148:151], v[96:99], v[40:43]\n\t"
        "ds_read_b128 v[112:115], v129 offset:8320\n\t"
        "v_cndmask_b32 v78, v79, v78, s[44:45]\n\t"
        "v_mfma_f32_16x16x32_bf16 v[44:47], a[152:155], v[96:99], v[44:47]\n\t"
        "ds_read_b64_tr_b16 a[216:217], v131 offset:6144\n\t"
        "v_cndmask_b32 v79, 0, v79, s[44:45]\n\t"
        "v_mfma_f32_16x16x32_bf16 v[36:39], a[168:171], v[100:103], v[36:39]\n\t"
        "buffer_load_dwordx4 v142, s[24:27], s36 offen offset:2048 lds\n\t"
        "s_add_u32 m0, s37, 49152\n\t"
        "v_mfma_f32_16x16x32_bf16 v[40:43], a[172:175], v[100:103], v[40:43]\n\t"
        "s_waitcnt vmcnt(10)\n\t"
        "s_barrier\n\t"
        "v_mfma_f32_16x16x32_bf16 v[44:47], a[176:179], v[100:103], v[44:47]\n\t"
        "ds_read_b128 v[84:87], v129 offset:12416\n\t"
        "s_add_u32 s36, s36, s38\n\t"
        "s_waitcnt lgkmcnt(7)\n\t"
        "v_mfma_f32_16x16x32_bf16 v[0:3], a[192:195], v[104:107], v[0:3]\n\t"
        "ds_read_b64_tr_b16 a[218:219], v131 offset:6912\n\t"
        "v_cvt_pk_bf16_f32 v70, v80, v78\n\t"
        "v_mfma_f32_16x16x32_bf16 v[4:7], a[196:199], v[104:107], v[4:7]\n\t"
        "buffer_load_dwordx4 v140, s[24:27], s36 offen offset:0 lds\n\t"
        "v_cvt_pk_bf16_f32 v71, v79, v81\n\t"
        "v_mfma_f32_16x16x32_bf16 v[8:11], a[200:203], v[104:107], v[8:11]\n\t"
        "ds_read_b64_tr_b16 a[220:221], v132 offset:6144\n\t"
        "v_pk_max_u16 v78, v62, v64\n\t"
        "v_mfma_f32_16x16x32_bf16 v[0:3], a[180:183], v[108:111], v[0:3]\n\t"
        "ds_read_b64_tr_b16 a[222:223], v132 offset:6912\n\t"
        "v_pk_min_u16 v78, v78, v65\n\t"
        "v_mfma_f32_16x16x32_bf16 v[4:7], a[184:187], v[108:111], v[4:7]\n\t"
        "ds_read_b64_tr_b16 a[224:225], v133 offset:6144\n\t"
        "v_cmp_ne_u32 vcc, v78, v62\n\t"
        "v_mfma_f32_16x16x32_bf16 v[8:11], a[188:191], v[108:111], v[8:11]\n\t"
        "ds_read_b64_tr_b16 a[226:227], v133 offset:6912\n\t"
        "v_sub_u32 v78, v77, v67\n\t"
        "v_mfma_f32_16x16x32_bf16 v[12:15], a[192:195], v[108:111], v[12:15]\n\t"
        "ds_read_b128 v[88:91], v129 offset:16512\n\t"
        "v_mad_u32_u24 v79, v77, 6, v66\n\t"
        "v_mfma_f32_16x16x32_bf16 v[16:19], a[196:199], v[108:111], v[16:19]\n\t"
        "ds_read_b64_tr_b16 a[228:229], v131 offset:12288\n\t"
        "v_mad_u32_u24 v79, v76, s62, v79\n\t"
        "v_mfma_f32_16x16x32_bf16 v[20:23], a[200:203], v[108:111], v[20:23]\n\t"
        "ds_read_b64_tr_b16 a[230:231], v131 offset:13056\n\t"
        "s_and_b64 vcc, vcc, s[66:67]\n\t"
        "s_waitcnt lgkmcnt(9)\n\t"
        "v_mfma_f32_16x16x32_bf16 v[0:3], a[204:207], v[112:115], v[0:3]\n\t"
        "ds_read_b64_tr_b16 a[232:233], v132 offset:12288\n\t"
        "s_or_b64 s[56:57], s[56:57], vcc\n\t"
        "v_mfma_f32_16x16x32_bf16 v[4:7], a[208:211], v[112:115], v[4:7]\n\t"
        "ds_read_b64_tr_b16 a[234:235], v132 offset:13056\n\t"
        "v_bfe_u32 v80, v78, 2, 1\n\t"
        "v_mfma_f32_16x16x32_bf16 v[8:11], a[212:215], v[112:115], v[8:11]\n\t"
        "ds_read_b64_tr_b16 a[236:237], v133 offset:12288\n\t"
        "v_or_b32 v80, v80, v79\n\t"
        "v_mfma_f32_16x16x32_bf16 v[12:15], a[180:183], v[112:115], v[12:15]\n\t"
        "ds_read_b128 v[92:95], v129 offset:20608\n\t"
        "v_add_u32 v78, 1, v78\n\t"
        "v_mfma_f32_16x16x32_bf16 v[16:19], a[184:187], v[112:115], v[16:19]\n\t"
        "ds_read_b64_tr_b16 a[238:239], v133 offset:13056\n\t"
        "v_bfe_u32 v78, v78, 2, 1\n\t"
        "v_mfma_f32_16x16x32_bf16 v[20:23], a[188:191], v[112:115], v[20:23]\n\t"
        "ds_read_b64_tr_b16 a[240:241], v131 offset:18432\n\t"
        "v_add3_u32 v78, v79, v78, 6\n\t"
        "v_mfma_f32_16x16x32_bf16 v[24:27], a[192:195], v[112:115], v[24:27]\n\t"
        "ds_read_b128 v[96:99], v130 offset:0\n\t"
        "v_cndmask_b32 v78, v80, v78, s[44:45]\n\t"
        "v_mfma_f32_16x16x32_bf16 v[28:31], a[196:199], v[112:115], v[28:31]\n\t"
        "ds_read_b64_tr_b16 a[242:243], v131 offset:19200\n\t"
        "v_cndmask_b32 v79, 0, v119, s[64:65]\n\t"
        "v_mfma_f32_16x16x32_bf16 v[32:35], a[200:203], v[112:115], v[32:35]\n\t"
        "ds_read_b64_tr_b16 a[244:245], v132 offset:18432\n\t"
        "v_add_u32 v81, v80, v79\n\t"
        "s_waitcnt lgkmcnt(10)\n\t"
        "v_mfma_f32_16x16x32_bf16 v[12:15], a[204:207], v[84:87], v[12:15]\n\t"
        "ds_read_b128 v[100:103], v130 offset:4096\n\t"
        "v_add_u32 v82, v78, v79\n\t"
        "v_mfma_f32_16x16x32_bf16 v[16:19], a[208:211], v[84:87], v[16:19]\n\t"
        "ds_read_b64_tr_b16 a[246:247], v132 offset:19200\n\t"
        "v_cndmask_b32 v78, v81, v78, s[44:45]\n\t"
        "v_mfma_f32_16x16x32_bf16 v[20:23], a[212:215], v[84:87], v[20:23]\n\t"
        "buffer_load_dwordx4 v141, s[24:27], s36 offen offset:1024 lds\n\t"
        "v_lshl_or_b32 v68, v78, 16, v80\n\t"
        "v_mfma_f32_16x16x32_bf16 v[24:27], a[180:183], v[84:87], v[24:27]\n\t"
        "ds_read_b64_tr_b16 a[248:249], v133 offset:18432\n\t"
        "v_lshl_or_b32 v69, v82, 16, v81\n\t"
        "v_mfma_f32_16x16x32_bf16 v[28:31], a[184:187], v[84:87], v[28:31]\n\t"
        "ds_write_b128 v118, v[68:71]\n\t"
        "v_mfma_f32_16x16x32_bf16 v[32:35], a[188:191], v[84:87], v[32:35]\n\t"
        "s_cmp_gt_u32 s3, 32\n\t"
        "s_cselect_b32 s8, 2, 0\n\t"
        "v_mfma_f32_16x16x32_bf16 v[36:39], a[192:195], v[84:87], v[36:39]\n\t"
        "s_cmp_lg_u64 s[56:57], 0\n\t"
        "s_addc_u32 s8, s8, s6\n\t"
        "v_mfma_f32_16x16x32_bf16 v[40:43], a[196:199], v[84:87], v[40:43]\n\t"
        "ds_read_b128 v[160:163], v185 offset:0\n\t"
        "v_mov_b32 v151, s8\n\t"
        "v_mfma_f32_16x16x32_bf16 v[44:47], a[200:203], v[84:87], v[44:47]\n\t"
        "s_mov_b64 exec, 1\n\t"
        "ds_write_b32 v153, v151\n\t"
        "s_mov_b64 exec, -1\n\t"
        "v_mfma_f32_16x16x32_bf16 v[24:27], a[204:207], v[88:91], v[24:27]\n\t"
        "buffer_load_dwordx4 v142, s[24:27], s36 offen offset:2048 lds\n\t"
        "v_xor_b32 v154, 32, v186\n\t"
        "v_mfma_f32_16x16x32_bf16 v[28:31], a[208:211], v[88:91], v[28:31]\n\t"
        "ds_read_b64_tr_b16 a[250:251], v133 offset:19200\n\t"
        "v_mfma_f32_16x16x32_bf16 v[32:35], a[212:215], v[88:91], v[32:35]\n\t"
        "ds_read_b128 v[104:107], v130 offset:8192\n\t"
        "v_mfma_f32_16x16x32_bf16 v[36:39], a[180:183], v[88:91], v[36:39]\n\t"
        "v_mfma_f32_16x16x32_bf16 v[40:43], a[184:187], v[88:91], v[40:43]\n\t"
        "v_mfma_f32_16x16x32_bf16 v[44:47], a[188:191], v[88:91], v[44:47]\n\t"
        "s_waitcnt lgkmcnt(5)\n\t"
        "v_mfma_f32_16x16x32_bf16 v[36:39], a[204:207], v[92:95], v[36:39]\n\t"
        "ds_read_b128 v[108:111], v130 offset:12288\n\t"
        "v_mfma_f32_16x16x32_bf16 v[40:43], a[208:211], v[92:95], v[40:43]\n\t"
        "s_waitcnt lgkmcnt(4)\n\t"
        "v_lshlrev_b32_sdwa v157, 5, v160 dst_sel:DWORD dst_unused:UNUSED_PAD src0_sel:DWORD src1_sel:WORD_0\n\t"
        "v_lshlrev_b32_sdwa v158, 5, v160 dst_sel:DWORD dst_unused:UNUSED_PAD src0_sel:DWORD src1_sel:WORD_1\n\t"
        "v_mfma_f32_16x16x32_bf16 v[44:47], a[212:215], v[92:95], v[44:47]\n\t"
        "v_or_b32 v157, v184, v157\n\t"
        "v_or_b32 v158, v184, v158\n\t"
        "v_mfma_f32_16x16x32_bf16 v[0:3], a[228:231], v[96:99], v[0:3]\n\t"
        "v_xor_b32 v159, 32, v157\n\t"
        "v_xor_b32 v152, 32, v158\n\t"
        "v_mfma_f32_16x16x32_bf16 v[4:7], a[232:235], v[96:99], v[4:7]\n\t"
        "v_perm_b32 v164, v162, v162, v168\n\t"
        "v_perm_b32 v165, v162, v162, v169\n\t"
        "v_mfma_f32_16x16x32_bf16 v[8:11], a[236:239], v[96:99], v[8:11]\n\t"
        "s_waitcnt vmcnt(3) lgkmcnt(2)\n\t"
        "s_barrier\n\t"
        "v_mfma_f32_16x16x32_bf16 v[0:3], a[216:219], v[100:103], v[0:3]\n\t"
        "ds_read_b64_tr_b16 v[60:61], v157 offset:0\n\t"
        "v_perm_b32 v166, v162, v162, v170\n\t"
        "v_mfma_f32_16x16x32_bf16 v[4:7], a[220:223], v[100:103], v[4:7]\n\t"
        "ds_read_b64_tr_b16 v[62:63], v158 offset:0\n\t"
        "v_perm_b32 v167, v162, v162, v171\n\t"
        "v_mfma_f32_16x16x32_bf16 v[8:11], a[224:227], v[100:103], v[8:11]\n\t"
        "ds_read_b64_tr_b16 v[64:65], v159 offset:0\n\t"
        "v_cmp_ne_u32 vcc, 0, v163\n\t"
        "v_mfma_f32_16x16x32_bf16 v[12:15], a[228:231], v[100:103], v[12:15]\n\t"
        "ds_read_b64_tr_b16 v[66:67], v152 offset:0\n\t"
        "ds_read_b128 v[112:115], v130 offset:16384\n\t"
        "v_mfma_f32_16x16x32_bf16 v[16:19], a[232:235], v[100:103], v[16:19]\n\t"
        "ds_read_b64_tr_b16 v[68:69], v157 offset:64\n\t"
        "ds_read_b64_tr_b16 a[252:253], v131 offset:24576\n\t"
        "v_mfma_f32_16x16x32_bf16 v[20:23], a[236:239], v[100:103], v[20:23]\n\t"
        "ds_read_b64_tr_b16 v[70:71], v158 offset:64\n\t"
        "ds_read_b64_tr_b16 a[254:255], v131 offset:25344\n\t"
        "s_waitcnt lgkmcnt(9)\n\t"
        "v_mfma_f32_16x16x32_bf16 v[0:3], a[240:243], v[104:107], v[0:3]\n\t"
        "ds_read_b64_tr_b16 v[72:73], v159 offset:64\n\t"
        "ds_read_b64_tr_b16 v[188:189], v132 offset:24576\n\t"
        "v_mfma_f32_16x16x32_bf16 v[4:7], a[244:247], v[104:107], v[4:7]\n\t"
        "ds_read_b64_tr_b16 v[74:75], v152 offset:64\n\t"
        "ds_read_b64_tr_b16 v[190:191], v132 offset:25344\n\t"
        "v_mfma_f32_16x16x32_bf16 v[8:11], a[248:251], v[104:107], v[8:11]\n\t"
        "ds_read_b64_tr_b16 v[76:77], v157 offset:128\n\t"
        "ds_read_b64_tr_b16 v[192:193], v133 offset:24576\n\t"
        "v_mfma_f32_16x16x32_bf16 v[12:15], a[216:219], v[104:107], v[12:15]\n\t"
        "ds_read_b64_tr_b16 v[78:79], v158 offset:128\n\t"
        "ds_read_b128 v[84:87], v130 offset:20480\n\t"
        "v_mfma_f32_16x16x32_bf16 v[16:19], a[220:223], v[104:107], v[16:19]\n\t"
        "ds_read_b64_tr_b16 v[80:81], v159 offset:128\n\t"
        "ds_read_b64_tr_b16 v[194:195], v133 offset:25344\n\t"
        "s_waitcnt lgkmcnt(15)\n\t"
        "v_mfma_f32_16x16x32_bf16 v[48:51], v[60:63], v[164:167], 0\n\t"
        "ds_read_b64_tr_b16 v[82:83], v152 offset:128\n\t"
        "v_mfma_f32_16x16x32_bf16 v[20:23], a[224:227], v[104:107], v[20:23]\n\t"
        "ds_read_b64_tr_b16 v[196:197], v131 offset:30720\n\t"
        "v_mfma_f32_16x16x32_bf16 v[52:55], v[64:67], v[164:167], 0\n\t"
        "ds_read_b64_tr_b16 v[198:199], v131 offset:31488\n\t"
        "v_mfma_f32_16x16x32_bf16 v[24:27], a[228:231], v[104:107], v[24:27]\n\t"
        "ds_read_b64_tr_b16 v[200:201], v132 offset:30720\n\t"
        "s_waitcnt lgkmcnt(14)\n\t"
        "v_mfma_f32_16x16x32_bf16 v[56:59], v[68:71], v[164:167], 0\n\t"
        "ds_read_b64_tr_b16 v[202:203], v132 offset:31488\n\t"
        "v_mfma_f32_16x16x32_bf16 v[28:31], a[232:235], v[104:107], v[28:31]\n\t"
        "ds_read_b64_tr_b16 v[204:205], v133 offset:30720\n\t"
        "s_waitcnt lgkmcnt(12)\n\t"
        "v_mfma_f32_16x16x32_bf16 v[116:119], v[72:75], v[164:167], 0\n\t"
        "ds_read_b64_tr_b16 v[206:207], v133 offset:31488\n\t"
        "v_mfma_f32_16x16x32_bf16 v[32:35], a[236:239], v[104:107], v[32:35]\n\t"
        "ds_read_b128 v[88:91], v130 offset:64\n\t"
        "s_waitcnt lgkmcnt(11)\n\t"
        "v_mfma_f32_16x16x32_bf16 v[120:123], v[76:79], v[164:167], 0\n\t"
        "ds_read_b64_tr_b16 v[208:209], v131 offset:36864\n\t"
        "v_mfma_f32_16x16x32_bf16 v[12:15], a[240:243], v[108:111], v[12:15]\n\t"
        "ds_read_b128 v[92:95], v130 offset:4160\n\t"
        ".LpM1%=:\n\t"
        "s_cbranch_execz .LrM%=\n\t"
        "s_waitcnt lgkmcnt(9)\n\t"
        "v_mfma_f32_16x16x32_bf16 v[124:127], v[80:83], v[164:167], 0\n\t"
        "ds_read_b64_tr_b16 v[210:211], v131 offset:37632\n\t"
        "s_cbranch_vccz .Lx46%=\n\t"
        "v_lshlrev_b32_sdwa v157, 5, v161 dst_sel:DWORD dst_unused:UNUSED_PAD src0_sel:DWORD src1_sel:WORD_0\n\t"
        "v_lshlrev_b32_sdwa v158, 5, v161 dst_sel:DWORD dst_unused:UNUSED_PAD src0_sel:DWORD src1_sel:WORD_1\n\t"
        "v_or_b32 v157, v184, v157\n\t"
        "v_or_b32 v158, v184, v158\n\t"
        "v_xor_b32 v159, 32, v157\n\t"
        "v_xor_b32 v152, 32, v158\n\t"
        "ds_read_b64_tr_b16 v[60:61], v157 offset:0\n\t"
        "ds_read_b64_tr_b16 v[62:63], v158 offset:0\n\t"
        "ds_read_b64_tr_b16 v[64:65], v159 offset:0\n\t"
        "ds_read_b64_tr_b16 v[66:67], v152 offset:0\n\t"
        "ds_read_b64_tr_b16 v[68:69], v157 offset:64\n\t"
        "ds_read_b64_tr_b16 v[70:71], v158 offset:64\n\t"
        "ds_read_b64_tr_b16 v[72:73], v159 offset:64\n\t"
        "ds_read_b64_tr_b16 v[74:75], v152 offset:64\n\t"
        "ds_read_b64_tr_b16 v[76:77], v157 offset:128\n\t"
        "ds_read_b64_tr_b16 v[78:79], v158 offset:128\n\t"
        "ds_read_b64_tr_b16 v[80:81], v159 offset:128\n\t"
        "ds_read_b64_tr_b16 v[82:83], v152 offset:128\n\t"
        "v_perm_b32 v164, v163, v163, v168\n\t"
        "v_perm_b32 v165, v163, v163, v169\n\t"
        "v_perm_b32 v166, v163, v163, v170\n\t"
        "v_perm_b32 v167, v163, v163, v171\n\t"
        "s_waitcnt lgkmcnt(10)\n\t"
        "s_nop 0\n\t"
        "v_mfma_f32_16x16x32_bf16 v[48:51], v[60:63], v[164:167], v[48:51]\n\t"
        "s_waitcnt lgkmcnt(8)\n\t"
        "v_mfma_f32_16x16x32_bf16 v[52:55], v[64:67], v[164:167], v[52:55]\n\t"
        "s_waitcnt lgkmcnt(6)\n\t"
        "v_mfma_f32_16x16x32_bf16 v[56:59], v[68:71], v[164:167], v[56:59]\n\t"
        "s_waitcnt lgkmcnt(4)\n\t"
        "v_mfma_f32_16x16x32_bf16 v[116:119], v[72:75], v[164:167], v[116:119]\n\t"
        "s_waitcnt lgkmcnt(2)\n\t"
        "v_mfma_f32_16x16x32_bf16 v[120:123], v[76:79], v[164:167], v[120:123]\n\t"
        "s_waitcnt lgkmcnt(0)\n\t"
        "v_mfma_f32_16x16x32_bf16 v[124:127], v[80:83], v[164:167], v[124:127]\n\t"
        "s_nop 4\n\t"
        ".Lx46%=:\n\t"
        "v_mfma_f32_16x16x32_bf16 v[16:19], a[244:247], v[108:111], v[16:19]\n\t"
        "ds_read_b128 v[160:163], v185 offset:256\n\t"
        "v_cvt_pk_bf16_f32 v48, v48, v49\n\t"
        "v_mfma_f32_16x16x32_bf16 v[20:23], a[248:251], v[108:111], v[20:23]\n\t"
        "ds_read_b64_tr_b16 v[212:213], v132 offset:36864\n\t"
        "v_cvt_pk_bf16_f32 v49, v50, v51\n\t"
        "v_mfma_f32_16x16x32_bf16 v[24:27], a[216:219], v[108:111], v[24:27]\n\t"
        "ds_write_b64 v186, v[48:49] offset:0\n\t"
        "v_mfma_f32_16x16x32_bf16 v[28:31], a[220:223], v[108:111], v[28:31]\n\t"
        "s_waitcnt vmcnt(0) lgkmcnt(7)\n\t"
        "s_barrier\n\t"
        "v_mfma_f32_16x16x32_bf16 v[32:35], a[224:227], v[108:111], v[32:35]\n\t"
        "ds_read_b64_tr_b16 v[214:215], v132 offset:37632\n\t"
        "v_cvt_pk_bf16_f32 v52, v52, v53\n\t"
        "v_mfma_f32_16x16x32_bf16 v[36:39], a[228:231], v[108:111], v[36:39]\n\t"
        "ds_read_b64_tr_b16 v[216:217], v133 offset:36864\n\t"
        "v_cvt_pk_bf16_f32 v53, v54, v55\n\t"
        "v_mfma_f32_16x16x32_bf16 v[40:43], a[232:235], v[108:111], v[40:43]\n\t"
        "ds_write_b64 v154, v[52:53] offset:0\n\t"
        "v_mfma_f32_16x16x32_bf16 v[44:47], a[236:239], v[108:111], v[44:47]\n\t"
        "ds_read_b64_tr_b16 v[218:219], v133 offset:37632\n\t"
        "v_cvt_pk_bf16_f32 v56, v56, v57\n\t"
        "v_mfma_f32_16x16x32_bf16 v[24:27], a[240:243], v[112:115], v[24:27]\n\t"
        "ds_read_b128 v[96:99], v130 offset:8256\n\t"
        "v_cvt_pk_bf16_f32 v57, v58, v59\n\t"
        "v_mfma_f32_16x16x32_bf16 v[28:31], a[244:247], v[112:115], v[28:31]\n\t"
        "ds_write_b64 v186, v[56:57] offset:64\n\t"
        "v_mfma_f32_16x16x32_bf16 v[32:35], a[248:251], v[112:115], v[32:35]\n\t"
        "ds_read_b64_tr_b16 v[220:221], v131 offset:43008\n\t"
        "v_cvt_pk_bf16_f32 v116, v116, v117\n\t"
        "v_mfma_f32_16x16x32_bf16 v[36:39], a[216:219], v[112:115], v[36:39]\n\t"
        "ds_read_b64_tr_b16 v[222:223], v131 offset:43776\n\t"
        "v_cvt_pk_bf16_f32 v117, v118, v119\n\t"
        "v_mfma_f32_16x16x32_bf16 v[40:43], a[220:223], v[112:115], v[40:43]\n\t"
        "ds_write_b64 v154, v[116:117] offset:64\n\t"
        "v_mfma_f32_16x16x32_bf16 v[44:47], a[224:227], v[112:115], v[44:47]\n\t"
        "ds_read_b64_tr_b16 v[224:225], v132 offset:43008\n\t"
        "v_cvt_pk_bf16_f32 v120, v120, v121\n\t"
        "v_mfma_f32_16x16x32_bf16 v[36:39], a[240:243], v[84:87], v[36:39]\n\t"
        "ds_read_b128 v[100:103], v130 offset:12352\n\t"
        "v_cvt_pk_bf16_f32 v121, v122, v123\n\t"
        "v_mfma_f32_16x16x32_bf16 v[40:43], a[244:247], v[84:87], v[40:43]\n\t"
        "ds_write_b64 v186, v[120:121] offset:128\n\t"
        "v_mfma_f32_16x16x32_bf16 v[44:47], a[248:251], v[84:87], v[44:47]\n\t"
        "ds_read_b64_tr_b16 v[226:227], v132 offset:43776\n\t"
        "v_cvt_pk_bf16_f32 v124, v124, v125\n\t"
        "s_waitcnt lgkmcnt(9)\n\t"
        "v_mfma_f32_16x16x32_bf16 v[0:3], v[196:199], v[88:91], v[0:3]\n\t"
        "ds_read_b64_tr_b16 v[228:229], v133 offset:43008\n\t"
        "v_cvt_pk_bf16_f32 v125, v126, v127\n\t"
        "v_mfma_f32_16x16x32_bf16 v[4:7], v[200:203], v[88:91], v[4:7]\n\t"
        "ds_write_b64 v154, v[124:125] offset:128\n\t"
        "v_mfma_f32_16x16x32_bf16 v[8:11], v[204:207], v[88:91], v[8:11]\n\t"
        "ds_read_b64_tr_b16 v[230:231], v133 offset:43776\n\t"
        "v_lshlrev_b32_sdwa v157, 5, v160 dst_sel:DWORD dst_unused:UNUSED_PAD src0_sel:DWORD src1_sel:WORD_0\n\t"
        "v_mfma_f32_16x16x32_bf16 v[0:3], a[252:255], v[92:95], v[0:3]\n\t"
        "ds_read_b64_tr_b16 v[232:233], v131 offset:49152\n\t"
        "v_lshlrev_b32_sdwa v158, 5, v160 dst_sel:DWORD dst_unused:UNUSED_PAD src0_sel:DWORD src1_sel:WORD_1\n\t"
        "v_mfma_f32_16x16x32_bf16 v[4:7], v[188:191], v[92:95], v[4:7]\n\t"
        "ds_read_b64_tr_b16 v[234:235], v131 offset:49920\n\t"
        "v_or_b32 v157, v184, v157\n\t"
        "v_mfma_f32_16x16x32_bf16 v[8:11], v[192:195], v[92:95], v[8:11]\n\t"
        "ds_read_b64_tr_b16 v[60:61], v157 offset:0\n\t"
        "v_or_b32 v158, v184, v158\n\t"
        "v_mfma_f32_16x16x32_bf16 v[12:15], v[196:199], v[92:95], v[12:15]\n\t"
        "ds_read_b64_tr_b16 v[62:63], v158 offset:0\n\t"
        "v_xor_b32 v159, 32, v157\n\t"
        "v_mfma_f32_16x16x32_bf16 v[16:19], v[200:203], v[92:95], v[16:19]\n\t"
        "ds_read_b64_tr_b16 v[64:65], v159 offset:0\n\t"
        "v_xor_b32 v152, 32, v158\n\t"
        "v_mfma_f32_16x16x32_bf16 v[20:23], v[204:207], v[92:95], v[20:23]\n\t"
        "ds_read_b64_tr_b16 v[66:67], v152 offset:0\n\t"
        "v_perm_b32 v164, v162, v162, v168\n\t"
        "s_waitcnt lgkmcnt(9)\n\t"
        "v_mfma_f32_16x16x32_bf16 v[0:3], v[208:211], v[96:99], v[0:3]\n\t"
        "ds_read_b64_tr_b16 v[68:69], v157 offset:64\n\t"
        "v_perm_b32 v165, v162, v162, v169\n\t"
        "v_mfma_f32_16x16x32_bf16 v[4:7], v[212:215], v[96:99], v[4:7]\n\t"
        "ds_read_b64_tr_b16 v[70:71], v158 offset:64\n\t"
        "v_perm_b32 v166, v162, v162, v170\n\t"
        "v_mfma_f32_16x16x32_bf16 v[8:11], v[216:219], v[96:99], v[8:11]\n\t"
        "ds_read_b128 v[104:107], v130 offset:16448\n\t"
        "v_perm_b32 v167, v162, v162, v171\n\t"
        "v_mfma_f32_16x16x32_bf16 v[12:15], a[252:255], v[96:99], v[12:15]\n\t"
        "ds_read_b64_tr_b16 v[72:73], v159 offset:64\n\t"
        "v_cmp_ne_u32 vcc, 0, v163\n\t"
        "v_mfma_f32_16x16x32_bf16 v[16:19], v[188:191], v[96:99], v[16:19]\n\t"
        "ds_read_b64_tr_b16 v[74:75], v152 offset:64\n\t"
        "v_mfma_f32_16x16x32_bf16 v[20:23], v[192:195], v[96:99], v[20:23]\n\t"
        "ds_read_b64_tr_b16 v[76:77], v157 offset:128\n\t"
        "v_mfma_f32_16x16x32_bf16 v[24:27], v[196:199], v[96:99], v[24:27]\n\t"
        "ds_read_b64_tr_b16 v[78:79], v158 offset:128\n\t"
        "s_waitcnt lgkmcnt(9)\n\t"
        "v_mfma_f32_16x16x32_bf16 v[48:51], v[60:63], v[164:167], 0\n\t"
        "ds_read_b64_tr_b16 v[80:81], v159 offset:128\n\t"
        "v_mfma_f32_16x16x32_bf16 v[28:31], v[200:203], v[96:99], v[28:31]\n\t"
        "ds_read_b64_tr_b16 v[82:83], v152 offset:128\n\t"
        "s_waitcnt lgkmcnt(9)\n\t"
        "v_mfma_f32_16x16x32_bf16 v[52:55], v[64:67], v[164:167], 0\n\t"
        "ds_read_b128 v[108:111], v130 offset:20544\n\t"
        "v_mfma_f32_16x16x32_bf16 v[32:35], v[204:207], v[96:99], v[32:35]\n\t"
        "ds_read_b64_tr_b16 v[236:237], v132 offset:49152\n\t"
        "s_waitcnt lgkmcnt(9)\n\t"
        "v_mfma_f32_16x16x32_bf16 v[56:59], v[68:71], v[164:167], 0\n\t"
        "ds_read_b64_tr_b16 v[238:239], v132 offset:49920\n\t"
        "v_mfma_f32_16x16x32_bf16 v[12:15], v[208:211], v[100:103], v[12:15]\n\t"
        "ds_read_b64_tr_b16 v[240:241], v133 offset:49152\n\t"
        "ds_read_b64_tr_b16 v[242:243], v133 offset:49920\n\t"
        "v_mfma_f32_16x16x32_bf16 v[16:19], v[212:215], v[100:103], v[16:19]\n\t"
        "ds_read_b128 v[112:115], v130 offset:128\n\t"
        "s_waitcnt lgkmcnt(10)\n\t"
        "v_mfma_f32_16x16x32_bf16 v[116:119], v[72:75], v[164:167], 0\n\t"
        "ds_read_b128 v[84:87], v130 offset:4224\n\t"
        "v_mfma_f32_16x16x32_bf16 v[20:23], v[216:219], v[100:103], v[20:23]\n\t"
        "ds_read_b64_tr_b16 v[244:245], v131 offset:55296\n\t"
        "s_waitcnt lgkmcnt(10)\n\t"
        "v_mfma_f32_16x16x32_bf16 v[120:123], v[76:79], v[164:167], 0\n\t"
        "ds_read_b64_tr_b16 v[246:247], v131 offset:56064\n\t"
        "v_mfma_f32_16x16x32_bf16 v[24:27], a[252:255], v[100:103], v[24:27]\n\t"
        "ds_read_b64_tr_b16 v[248:249], v132 offset:55296\n\t"
        "s_waitcnt lgkmcnt(10)\n\t"
        "v_mfma_f32_16x16x32_bf16 v[124:127], v[80:83], v[164:167], 0\n\t"
        "ds_read_b64_tr_b16 v[250:251], v132 offset:56064\n\t"
        "s_cbranch_vccz .Lx48%=\n\t"
        "v_lshlrev_b32_sdwa v157, 5, v161 dst_sel:DWORD dst_unused:UNUSED_PAD src0_sel:DWORD src1_sel:WORD_0\n\t"
        "v_lshlrev_b32_sdwa v158, 5, v161 dst_sel:DWORD dst_unused:UNUSED_PAD src0_sel:DWORD src1_sel:WORD_1\n\t"
        "v_or_b32 v157, v184, v157\n\t"
        "v_or_b32 v158, v184, v158\n\t"
        "v_xor_b32 v159, 32, v157\n\t"
        "v_xor_b32 v152, 32, v158\n\t"
        "ds_read_b64_tr_b16 v[60:61], v157 offset:0\n\t"
        "ds_read_b64_tr_b16 v[62:63], v158 offset:0\n\t"
        "ds_read_b64_tr_b16 v[64:65], v159 offset:0\n\t"
        "ds_read_b64_tr_b16 v[66:67], v152 offset:0\n\t"
        "ds_read_b64_tr_b16 v[68:69], v157 offset:64\n\t"
        "ds_read_b64_tr_b16 v[70:71], v158 offset:64\n\t"
        "ds_read_b64_tr_b16 v[72:73], v159 offset:64\n\t"
        "ds_read_b64_tr_b16 v[74:75], v152 offset:64\n\t"
        "ds_read_b64_tr_b16 v[76:77], v157 offset:128\n\t"
        "ds_read_b64_tr_b16 v[78:79], v158 offset:128\n\t"
        "ds_read_b64_tr_b16 v[80:81], v159 offset:128\n\t"
        "ds_read_b64_tr_b16 v[82:83], v152 offset:128\n\t"
        "v_perm_b32 v164, v163, v163, v168\n\t"
        "v_perm_b32 v165, v163, v163, v169\n\t"
        "v_perm_b32 v166, v163, v163, v170\n\t"
        "v_perm_b32 v167, v163, v163, v171\n\t"
        "s_waitcnt lgkmcnt(10)\n\t"
        "s_nop 0\n\t"
        "v_mfma_f32_16x16x32_bf16 v[48:51], v[60:63], v[164:167], v[48:51]\n\t"
        "s_waitcnt lgkmcnt(8)\n\t"
        "v_mfma_f32_16x16x32_bf16 v[52:55], v[64:67], v[164:167], v[52:55]\n\t"
        "s_waitcnt lgkmcnt(6)\n\t"
        "v_mfma_f32_16x16x32_bf16 v[56:59], v[68:71], v[164:167], v[56:59]\n\t"
        "s_waitcnt lgkmcnt(4)\n\t"
        "v_mfma_f32_16x16x32_bf16 v[116:119], v[72:75], v[164:167], v[116:119]\n\t"
        "s_waitcnt lgkmcnt(2)\n\t"
        "v_mfma_f32_16x16x32_bf16 v[120:123], v[76:79], v[164:167], v[120:123]\n\t"
        "s_waitcnt lgkmcnt(0)\n\t"
        "v_mfma_f32_16x16x32_bf16 v[124:127], v[80:83], v[164:167], v[124:127]\n\t"
        "s_nop 4\n\t"
        ".Lx48%=:\n\t"
        "v_mfma_f32_16x16x32_bf16 v[28:31], v[188:191], v[100:103], v[28:31]\n\t"
        "ds_read_b64_tr_b16 v[252:253], v133 offset:55296\n\t"
        "v_cvt_pk_bf16_f32 v48, v48, v49\n\t"
        "v_mfma_f32_16x16x32_bf16 v[32:35], v[192:195], v[100:103], v[32:35]\n\t"
        "ds_read_b64_tr_b16 v[254:255], v133 offset:56064\n\t"
        "v_cvt_pk_bf16_f32 v49, v50, v51\n\t"
        "v_mfma_f32_16x16x32_bf16 v[36:39], v[196:199], v[100:103], v[36:39]\n\t"
        "ds_write_b64 v186, v[48:49] offset:12288\n\t"
        "v_mfma_f32_16x16x32_bf16 v[40:43], v[200:203], v[100:103], v[40:43]\n\t"
        "ds_read_b128 v[88:91], v130 offset:8320\n\t"
        "v_cvt_pk_bf16_f32 v52, v52, v53\n\t"
        "v_mfma_f32_16x16x32_bf16 v[44:47], v[204:207], v[100:103], v[44:47]\n\t"
        "v_cvt_pk_bf16_f32 v53, v54, v55\n\t"
        "v_cvt_pk_bf16_f32 v56, v56, v57\n\t"
        "v_mfma_f32_16x16x32_bf16 v[24:27], v[208:211], v[104:107], v[24:27]\n\t"
        "ds_write_b64 v154, v[52:53] offset:12288\n\t"
        "v_mfma_f32_16x16x32_bf16 v[28:31], v[212:215], v[104:107], v[28:31]\n\t"
        "v_cvt_pk_bf16_f32 v57, v58, v59\n\t"
        "v_cvt_pk_bf16_f32 v116, v116, v117\n\t"
        "v_mfma_f32_16x16x32_bf16 v[32:35], v[216:219], v[104:107], v[32:35]\n\t"
        "ds_write_b64 v186, v[56:57] offset:12352\n\t"
        "v_mfma_f32_16x16x32_bf16 v[36:39], a[252:255], v[104:107], v[36:39]\n\t"
        "v_cvt_pk_bf16_f32 v117, v118, v119\n\t"
        "v_cvt_pk_bf16_f32 v120, v120, v121\n\t"
        "v_mfma_f32_16x16x32_bf16 v[40:43], v[188:191], v[104:107], v[40:43]\n\t"
        "ds_write_b64 v154, v[116:117] offset:12352\n\t"
        "v_mfma_f32_16x16x32_bf16 v[44:47], v[192:195], v[104:107], v[44:47]\n\t"
        "v_cvt_pk_bf16_f32 v121, v122, v123\n\t"
        "v_cvt_pk_bf16_f32 v124, v124, v125\n\t"
        "s_waitcnt lgkmcnt(5)\n\t"
        "v_mfma_f32_16x16x32_bf16 v[36:39], v[208:211], v[108:111], v[36:39]\n\t"
        "ds_write_b64 v186, v[120:121] offset:12416\n\t"
        "v_mfma_f32_16x16x32_bf16 v[40:43], v[212:215], v[108:111], v[40:43]\n\t"
        "ds_read_b128 v[92:95], v130 offset:12416\n\t"
        "v_cvt_pk_bf16_f32 v125, v126, v127\n\t"
        "v_mfma_f32_16x16x32_bf16 v[44:47], v[216:219], v[108:111], v[44:47]\n\t"
        "ds_write_b64 v154, v[124:125] offset:12416\n\t"
        "v_mfma_f32_16x16x32_bf16 v[0:3], v[232:235], v[112:115], v[0:3]\n\t"
        "v_mfma_f32_16x16x32_bf16 v[4:7], v[236:239], v[112:115], v[4:7]\n\t"
        "v_mfma_f32_16x16x32_bf16 v[8:11], v[240:243], v[112:115], v[8:11]\n\t"
        "v_mfma_f32_16x16x32_bf16 v[0:3], v[220:223], v[84:87], v[0:3]\n\t"
        "v_mfma_f32_16x16x32_bf16 v[4:7], v[224:227], v[84:87], v[4:7]\n\t"
        "v_mfma_f32_16x16x32_bf16 v[8:11], v[228:231], v[84:87], v[8:11]\n\t"
        "s_waitcnt lgkmcnt(0)\n\t"
        "s_barrier\n\t"
        "v_mfma_f32_16x16x32_bf16 v[12:15], v[232:235], v[84:87], v[12:15]\n\t"
        "ds_read_b128 v[124:127], v182 offset:0\n\t"
        "v_mfma_f32_16x16x32_bf16 v[16:19], v[236:239], v[84:87], v[16:19]\n\t"
        "ds_read_b128 v[132:135], v187 offset:0\n\t"
        "v_mfma_f32_16x16x32_bf16 v[20:23], v[240:243], v[84:87], v[20:23]\n\t"
        "ds_read_b128 v[136:139], v187 offset:16\n\t"
        "v_mfma_f32_16x16x32_bf16 v[0:3], v[244:247], v[88:91], v[0:3]\n\t"
        "ds_read_b32 v140, v187 offset:32\n\t"
        "v_mfma_f32_16x16x32_bf16 v[4:7], v[248:251], v[88:91], v[4:7]\n\t"
        "ds_read_b128 v[96:99], v130 offset:16512\n\t"
        "v_mfma_f32_16x16x32_bf16 v[8:11], v[252:255], v[88:91], v[8:11]\n\t"
        "v_mfma_f32_16x16x32_bf16 v[12:15], v[220:223], v[88:91], v[12:15]\n\t"
        "ds_read_b128 v[100:103], v130 offset:20608\n\t"
        "v_mfma_f32_16x16x32_bf16 v[16:19], v[224:227], v[88:91], v[16:19]\n\t"
        "v_mfma_f32_16x16x32_bf16 v[20:23], v[228:231], v[88:91], v[20:23]\n\t"
        "v_mfma_f32_16x16x32_bf16 v[24:27], v[232:235], v[88:91], v[24:27]\n\t"
        "s_waitcnt lgkmcnt(5)\n\t"
        "v_or3_b32 v124, v124, v125, v126\n\t"
        "v_or_b32 v124, v124, v127\n\t"
        "v_mfma_f32_16x16x32_bf16 v[28:31], v[236:239], v[88:91], v[28:31]\n\t"
        "v_readfirstlane_b32 s6, v124\n\t"
        "v_mfma_f32_16x16x32_bf16 v[32:35], v[240:243], v[88:91], v[32:35]\n\t"
        "s_nop 0\n\t"
        "s_bitcmp1_b32 s6, 1\n\t"
        "s_cbranch_scc1 .Lx50%=\n\t"
        ".Lx51%=:\n\t"
        "v_mfma_f32_16x16x32_bf16 v[12:15], v[244:247], v[92:95], v[12:15]\n\t"
        "ds_read_b128 v[160:163], v185 offset:512\n\t"
        "s_waitcnt lgkmcnt(4)\n\t"
        "v_xor_b32 v132, v181, v132\n\t"
        "v_mfma_f32_16x16x32_bf16 v[16:19], v[248:251], v[92:95], v[16:19]\n\t"
        "v_add_u32 v151, s9, v172\n\t"
        "v_max_u32 v151, v151, v132\n\t"
        "v_mfma_f32_16x16x32_bf16 v[20:23], v[252:255], v[92:95], v[20:23]\n\t"
        "ds_read_b128 v[104:107], v151 offset:0\n\t"
        "v_xor_b32 v133, v181, v133\n\t"
        "v_mfma_f32_16x16x32_bf16 v[24:27], v[220:223], v[92:95], v[24:27]\n\t"
        "ds_read_b128 v[108:111], v151 offset:64\n\t"
        "v_add_u32 v183, s9, v173\n\t"
        "v_mfma_f32_16x16x32_bf16 v[28:31], v[224:227], v[92:95], v[28:31]\n\t"
        "ds_read_b128 v[112:115], v151 offset:128\n\t"
        "v_max_u32 v183, v183, v133\n\t"
        "v_xor_b32 v134, v181, v134\n\t"
        "v_mfma_f32_16x16x32_bf16 v[32:35], v[228:231], v[92:95], v[32:35]\n\t"
        "v_add_u32 v151, s9, v174\n\t"
        "v_max_u32 v151, v151, v134\n\t"
        "v_mfma_f32_16x16x32_bf16 v[36:39], v[232:235], v[92:95], v[36:39]\n\t"
        "ds_read_b128 v[84:87], v183 offset:0\n\t"
        "v_xor_b32 v135, v181, v135\n\t"
        "v_mfma_f32_16x16x32_bf16 v[40:43], v[236:239], v[92:95], v[40:43]\n\t"
        "s_add_u32 s55, s41, 0\n\t"
        "v_cvt_pk_bf16_f32 v116, v0, v1\n\t"
        "v_mfma_f32_16x16x32_bf16 v[44:47], v[240:243], v[92:95], v[44:47]\n\t"
        "v_cvt_pk_bf16_f32 v117, v2, v3\n\t"
        "v_cvt_pk_bf16_f32 v118, v4, v5\n\t"
        "s_waitcnt lgkmcnt(5)\n\t"
        "v_mfma_f32_16x16x32_bf16 v[24:27], v[244:247], v[96:99], v[24:27]\n\t"
        "ds_read_b128 v[88:91], v183 offset:64\n\t"
        "v_cvt_pk_bf16_f32 v119, v6, v7\n\t"
        "v_cvt_pk_bf16_f32 v120, v8, v9\n\t"
        "v_mfma_f32_16x16x32_bf16 v[28:31], v[248:251], v[96:99], v[28:31]\n\t"
        "v_cvt_pk_bf16_f32 v121, v10, v11\n\t"
        "s_waitcnt lgkmcnt(5)\n\t"
        "v_lshlrev_b32_sdwa v157, 5, v160 dst_sel:DWORD dst_unused:UNUSED_PAD src0_sel:DWORD src1_sel:WORD_0\n\t"
        "v_mfma_f32_16x16x32_bf16 v[32:35], v[252:255], v[96:99], v[32:35]\n\t"
        "v_permlane16_swap_b32 v116, v118\n\t"
        "v_mfma_f32_16x16x32_bf16 v[36:39], v[220:223], v[96:99], v[36:39]\n\t"
        "v_permlane16_swap_b32 v117, v119\n\t"
        "v_mfma_f32_16x16x32_bf16 v[40:43], v[224:227], v[96:99], v[40:43]\n\t"
        "buffer_store_dwordx4 v[116:119], v144, s[28:31], s55 offen sc1\n\t"
        "v_mfma_f32_16x16x32_bf16 v[44:47], v[228:231], v[96:99], v[44:47]\n\t"
        "ds_write_b64 v146, v[120:121] offset:0\n\t"
        "v_mfma_f32_16x16x32_bf16 v[36:39], v[244:247], v[100:103], v[36:39]\n\t"
        "ds_read_b128 v[92:95], v183 offset:128\n\t"
        "v_add_u32 v183, s9, v175\n\t"
        "v_max_u32 v183, v183, v135\n\t"
        "v_mfma_f32_16x16x32_bf16 v[40:43], v[248:251], v[100:103], v[40:43]\n\t"
        "ds_read_b128 v[96:99], v151 offset:0\n\t"
        "v_xor_b32 v136, v181, v136\n\t"
        "v_mfma_f32_16x16x32_bf16 v[44:47], v[252:255], v[100:103], v[44:47]\n\t"
        "s_add_u32 s55, s41, 43008\n\t"
        "v_cvt_pk_bf16_f32 v116, v12, v13\n\t"
        "s_waitcnt lgkmcnt(3)\n\t"
        "v_mfma_f32_16x16x32_bf16 v[48:51], a[12:15], v[104:107], 0\n\t"
        "ds_read_b128 v[100:103], v151 offset:64\n\t"
        "v_cvt_pk_bf16_f32 v117, v14, v15\n\t"
        "v_mfma_f32_16x16x32_bf16 v[52:55], a[16:19], v[104:107], 0\n\t"
        "v_cvt_pk_bf16_f32 v118, v16, v17\n\t"
        "v_cvt_pk_bf16_f32 v119, v18, v19\n\t"
        "v_cvt_pk_bf16_f32 v120, v20, v21\n\t"
        "v_mfma_f32_16x16x32_bf16 v[56:59], a[20:23], v[104:107], 0\n\t"
        "v_cvt_pk_bf16_f32 v121, v22, v23\n\t"
        "v_lshlrev_b32_sdwa v158, 5, v160 dst_sel:DWORD dst_unused:UNUSED_PAD src0_sel:DWORD src1_sel:WORD_1\n\t"
        "v_mfma_f32_16x16x32_bf16 v[48:51], a[48:51], v[108:111], v[48:51]\n\t"
        "v_permlane16_swap_b32 v116, v118\n\t"
        "v_mfma_f32_16x16x32_bf16 v[52:55], a[52:55], v[108:111], v[52:55]\n\t"
        "v_permlane16_swap_b32 v117, v119\n\t"
        "v_mfma_f32_16x16x32_bf16 v[56:59], a[56:59], v[108:111], v[56:59]\n\t"
        "ds_read_b128 v[104:107], v151 offset:128\n\t"
        "v_add_u32 v151, s9, v176\n\t"
        "v_mfma_f32_16x16x32_bf16 v[48:51], a[84:87], v[112:115], v[48:51]\n\t"
        "ds_read_b128 v[108:111], v183 offset:0\n\t"
        "v_max_u32 v151, v151, v136\n\t"
        "v_xor_b32 v137, v181, v137\n\t"
        "v_mfma_f32_16x16x32_bf16 v[52:55], a[88:91], v[112:115], v[52:55]\n\t"
        "v_or_b32 v157, v184, v157\n\t"
        "v_or_b32 v158, v184, v158\n\t"
        "v_mfma_f32_16x16x32_bf16 v[56:59], a[92:95], v[112:115], v[56:59]\n\t"
        "ds_read_b64_tr_b16 v[60:61], v157 offset:0\n\t"
        "v_xor_b32 v159, 32, v157\n\t"
        "v_mfma_f32_16x16x32_bf16 v[48:51], a[120:123], v[84:87], v[48:51]\n\t"
        "buffer_store_dwordx4 v[116:119], v144, s[28:31], s55 offen sc1\n\t"
        "v_mfma_f32_16x16x32_bf16 v[52:55], a[124:127], v[84:87], v[52:55]\n\t"
        "ds_write_b64 v146, v[120:121] offset:2048\n\t"
        "v_mfma_f32_16x16x32_bf16 v[56:59], a[128:131], v[84:87], v[56:59]\n\t"
        "ds_read_b64_tr_b16 v[62:63], v158 offset:0\n\t"
        "s_add_u32 s55, s41, 86016\n\t"
        "v_cvt_pk_bf16_f32 v116, v24, v25\n\t"
        "v_mfma_f32_16x16x32_bf16 v[48:51], a[156:159], v[88:91], v[48:51]\n\t"
        "ds_read_b64_tr_b16 v[64:65], v159 offset:0\n\t"
        "v_cvt_pk_bf16_f32 v117, v26, v27\n\t"
        "v_mfma_f32_16x16x32_bf16 v[52:55], a[160:163], v[88:91], v[52:55]\n\t"
        "ds_read_b64_tr_b16 v[68:69], v157 offset:64\n\t"
        "v_cvt_pk_bf16_f32 v118, v28, v29\n\t"
        "v_mfma_f32_16x16x32_bf16 v[56:59], a[164:167], v[88:91], v[56:59]\n\t"
        "ds_read_b128 v[112:115], v183 offset:64\n\t"
        "v_cvt_pk_bf16_f32 v119, v30, v31\n\t"
        "s_waitcnt lgkmcnt(6)\n\t"
        "v_mfma_f32_16x16x32_bf16 v[48:51], a[192:195], v[92:95], v[48:51]\n\t"
        "ds_read_b64_tr_b16 v[70:71], v158 offset:64\n\t"
        "v_cvt_pk_bf16_f32 v120, v32, v33\n\t"
        "v_mfma_f32_16x16x32_bf16 v[52:55], a[196:199], v[92:95], v[52:55]\n\t"
        "ds_read_b64_tr_b16 v[72:73], v159 offset:64\n\t"
        "v_cvt_pk_bf16_f32 v121, v34, v35\n\t"
        "v_xor_b32 v152, 32, v158\n\t"
        "v_mfma_f32_16x16x32_bf16 v[56:59], a[200:203], v[92:95], v[56:59]\n\t"
        "v_permlane16_swap_b32 v116, v118\n\t"
        "v_mfma_f32_16x16x32_bf16 v[48:51], a[228:231], v[96:99], v[48:51]\n\t"
        "v_permlane16_swap_b32 v117, v119\n\t"
        "v_mfma_f32_16x16x32_bf16 v[52:55], a[232:235], v[96:99], v[52:55]\n\t"
        "ds_read_b128 v[84:87], v183 offset:128\n\t"
        "v_add_u32 v183, s9, v177\n\t"
        "v_mfma_f32_16x16x32_bf16 v[56:59], a[236:239], v[96:99], v[56:59]\n\t"
        "ds_read_b128 v[88:91], v151 offset:0\n\t"
        "v_max_u32 v183, v183, v137\n\t"
        "v_mfma_f32_16x16x32_bf16 v[48:51], v[196:199], v[100:103], v[48:51]\n\t"
        "ds_read_b64_tr_b16 v[66:67], v152 offset:0\n\t"
        "v_xor_b32 v138, v181, v138\n\t"
        "v_perm_b32 v164, v162, v162, v168\n\t"
        "v_mfma_f32_16x16x32_bf16 v[52:55], v[200:203], v[100:103], v[52:55]\n\t"
        "ds_read_b64_tr_b16 v[74:75], v152 offset:64\n\t"
        "v_perm_b32 v165, v162, v162, v169\n\t"
        "v_mfma_f32_16x16x32_bf16 v[56:59], v[204:207], v[100:103], v[56:59]\n\t"
        "buffer_store_dwordx4 v[116:119], v144, s[28:31], s55 offen sc1\n\t"
        "v_mfma_f32_16x16x32_bf16 v[48:51], v[232:235], v[104:107], v[48:51]\n\t"
        "ds_write_b64 v146, v[120:121] offset:4096\n\t"
        "v_mfma_f32_16x16x32_bf16 v[52:55], v[236:239], v[104:107], v[52:55]\n\t"
        "ds_read_b128 v[92:95], v151 offset:64\n\t"
        "s_add_u32 s55, s41, 129024\n\t"
        "v_mfma_f32_16x16x32_bf16 v[56:59], v[240:243], v[104:107], v[56:59]\n\t"
        "ds_read_b128 v[96:99], v151 offset:128\n\t"
        "v_add_u32 v151, s9, v178\n\t"
        "v_max_u32 v151, v151, v138\n\t"
        "v_mfma_f32_16x16x32_bf16 v[48:51], a[0:3], v[108:111], v[48:51]\n\t"
        "ds_read_b64_tr_b16 v[76:77], v157 offset:128\n\t"
        "v_xor_b32 v139, v181, v139\n\t"
        "v_mfma_f32_16x16x32_bf16 v[52:55], a[4:7], v[108:111], v[52:55]\n\t"
        "ds_read_b64_tr_b16 v[78:79], v158 offset:128\n\t"
        "v_cvt_pk_bf16_f32 v116, v36, v37\n\t"
        "v_mfma_f32_16x16x32_bf16 v[56:59], a[8:11], v[108:111], v[56:59]\n\t"
        "ds_read_b128 v[100:103], v183 offset:0\n\t"
        "v_cvt_pk_bf16_f32 v117, v38, v39\n\t"
        "s_waitcnt lgkmcnt(7)\n\t"
        "v_mfma_f32_16x16x32_bf16 v[48:51], a[36:39], v[112:115], v[48:51]\n\t"
        "ds_read_b64_tr_b16 v[80:81], v159 offset:128\n\t"
        "v_cvt_pk_bf16_f32 v118, v40, v41\n\t"
        "v_mfma_f32_16x16x32_bf16 v[52:55], a[40:43], v[112:115], v[52:55]\n\t"
        "ds_read_b64_tr_b16 v[82:83], v152 offset:128\n\t"
        "v_cvt_pk_bf16_f32 v119, v42, v43\n\t"
        "v_cvt_pk_bf16_f32 v120, v44, v45\n\t"
        "v_mfma_f32_16x16x32_bf16 v[56:59], a[44:47], v[112:115], v[56:59]\n\t"
        "ds_read_b128 v[104:107], v183 offset:64\n\t"
        "v_cvt_pk_bf16_f32 v121, v46, v47\n\t"
        "v_mfma_f32_16x16x32_bf16 v[48:51], a[72:75], v[84:87], v[48:51]\n\t"
        "v_permlane16_swap_b32 v116, v118\n\t"
        "v_mfma_f32_16x16x32_bf16 v[52:55], a[76:79], v[84:87], v[52:55]\n\t"
        "v_permlane16_swap_b32 v117, v119\n\t"
        "v_mfma_f32_16x16x32_bf16 v[56:59], a[80:83], v[84:87], v[56:59]\n\t"
        "ds_read_b128 v[108:111], v183 offset:128\n\t"
        "v_add_u32 v183, s9, v179\n\t"
        "v_mfma_f32_16x16x32_bf16 v[48:51], a[108:111], v[88:91], v[48:51]\n\t"
        "ds_read_b128 v[112:115], v151 offset:0\n\t"
        "v_max_u32 v183, v183, v139\n\t"
        "v_perm_b32 v166, v162, v162, v170\n\t"
        "v_mfma_f32_16x16x32_bf16 v[52:55], a[112:115], v[88:91], v[52:55]\n\t"
        "buffer_store_dwordx4 v[116:119], v144, s[28:31], s55 offen sc1\n\t"
        "v_mfma_f32_16x16x32_bf16 v[56:59], a[116:119], v[88:91], v[56:59]\n\t"
        "ds_write_b64 v146, v[120:121] offset:6144\n\t"
        "s_waitcnt lgkmcnt(5)\n\t"
        "v_mfma_f32_16x16x32_bf16 v[48:51], a[144:147], v[92:95], v[48:51]\n\t"
        "ds_read_b128 v[84:87], v151 offset:64\n\t"
        "v_perm_b32 v167, v162, v162, v171\n\t"
        "v_mfma_f32_16x16x32_bf16 v[52:55], a[148:151], v[92:95], v[52:55]\n\t"
        "ds_read_b128 v[88:91], v151 offset:128\n\t"
        "v_cmp_ne_u32 vcc, 0, v163\n\t"
        "v_mfma_f32_16x16x32_bf16 v[0:3], v[60:63], v[164:167], 0\n\t"
        "v_mfma_f32_16x16x32_bf16 v[56:59], a[152:155], v[92:95], v[56:59]\n\t"
        "v_mfma_f32_16x16x32_bf16 v[4:7], v[64:67], v[164:167], 0\n\t"
        "v_mfma_f32_16x16x32_bf16 v[48:51], a[180:183], v[96:99], v[48:51]\n\t"
        "ds_read_b128 v[92:95], v183 offset:0\n\t"
        "v_mfma_f32_16x16x32_bf16 v[8:11], v[68:71], v[164:167], 0\n\t"
        "v_mfma_f32_16x16x32_bf16 v[52:55], a[184:187], v[96:99], v[52:55]\n\t"
        "v_mfma_f32_16x16x32_bf16 v[12:15], v[72:75], v[164:167], 0\n\t"
        "v_mfma_f32_16x16x32_bf16 v[56:59], a[188:191], v[96:99], v[56:59]\n\t"
        "v_mfma_f32_16x16x32_bf16 v[16:19], v[76:79], v[164:167], 0\n\t"
        "v_mfma_f32_16x16x32_bf16 v[48:51], a[216:219], v[100:103], v[48:51]\n\t"
        "ds_read_b128 v[96:99], v183 offset:64\n\t"
        "s_waitcnt lgkmcnt(2)\n\t"
        "v_mfma_f32_16x16x32_bf16 v[20:23], v[80:83], v[164:167], 0\n\t"
        "s_cbranch_vccz .Lx52%=\n\t"
        "v_lshlrev_b32_sdwa v157, 5, v161 dst_sel:DWORD dst_unused:UNUSED_PAD src0_sel:DWORD src1_sel:WORD_0\n\t"
        "v_lshlrev_b32_sdwa v158, 5, v161 dst_sel:DWORD dst_unused:UNUSED_PAD src0_sel:DWORD src1_sel:WORD_1\n\t"
        "v_or_b32 v157, v184, v157\n\t"
        "v_or_b32 v158, v184, v158\n\t"
        "v_xor_b32 v159, 32, v157\n\t"
        "v_xor_b32 v152, 32, v158\n\t"
        "ds_read_b64_tr_b16 v[60:61], v157 offset:0\n\t"
        "ds_read_b64_tr_b16 v[62:63], v158 offset:0\n\t"
        "ds_read_b64_tr_b16 v[64:65], v159 offset:0\n\t"
        "ds_read_b64_tr_b16 v[66:67], v152 offset:0\n\t"
        "ds_read_b64_tr_b16 v[68:69], v157 offset:64\n\t"
        "ds_read_b64_tr_b16 v[70:71], v158 offset:64\n\t"
        "ds_read_b64_tr_b16 v[72:73], v159 offset:64\n\t"
        "ds_read_b64_tr_b16 v[74:75], v152 offset:64\n\t"
        "ds_read_b64_tr_b16 v[76:77], v157 offset:128\n\t"
        "ds_read_b64_tr_b16 v[78:79], v158 offset:128\n\t"
        "ds_read_b64_tr_b16 v[80:81], v159 offset:128\n\t"
        "ds_read_b64_tr_b16 v[82:83], v152 offset:128\n\t"
        "v_perm_b32 v164, v163, v163, v168\n\t"
        "v_perm_b32 v165, v163, v163, v169\n\t"
        "v_perm_b32 v166, v163, v163, v170\n\t"
        "v_perm_b32 v167, v163, v163, v171\n\t"
        "s_waitcnt lgkmcnt(10)\n\t"
        "s_nop 0\n\t"
        "v_mfma_f32_16x16x32_bf16 v[0:3], v[60:63], v[164:167], v[0:3]\n\t"
        "s_waitcnt lgkmcnt(8)\n\t"
        "v_mfma_f32_16x16x32_bf16 v[4:7], v[64:67], v[164:167], v[4:7]\n\t"
        "s_waitcnt lgkmcnt(6)\n\t"
        "v_mfma_f32_16x16x32_bf16 v[8:11], v[68:71], v[164:167], v[8:11]\n\t"
        "s_waitcnt lgkmcnt(4)\n\t"
        "v_mfma_f32_16x16x32_bf16 v[12:15], v[72:75], v[164:167], v[12:15]\n\t"
        "s_waitcnt lgkmcnt(2)\n\t"
        "v_mfma_f32_16x16x32_bf16 v[16:19], v[76:79], v[164:167], v[16:19]\n\t"
        "s_waitcnt lgkmcnt(0)\n\t"
        "v_mfma_f32_16x16x32_bf16 v[20:23], v[80:83], v[164:167], v[20:23]\n\t"
        "s_nop 4\n\t"
        ".Lx52%=:\n\t"
        "v_cvt_pk_bf16_f32 v0, v0, v1\n\t"
        "v_cvt_pk_bf16_f32 v1, v2, v3\n\t"
        "v_cvt_pk_bf16_f32 v4, v4, v5\n\t"
        "v_cvt_pk_bf16_f32 v5, v6, v7\n\t"
        "v_mfma_f32_16x16x32_bf16 v[52:55], a[220:223], v[100:103], v[52:55]\n\t"
        "ds_write_b64 v186, v[0:1] offset:24576\n\t"
        "v_mfma_f32_16x16x32_bf16 v[56:59], a[224:227], v[100:103], v[56:59]\n\t"
        "ds_write_b64 v154, v[4:5] offset:24576\n\t"
        "v_mfma_f32_16x16x32_bf16 v[48:51], a[252:255], v[104:107], v[48:51]\n\t"
        "ds_read_b128 v[100:103], v183 offset:128\n\t"
        "v_cvt_pk_bf16_f32 v8, v8, v9\n\t"
        "v_mfma_f32_16x16x32_bf16 v[52:55], v[188:191], v[104:107], v[52:55]\n\t"
        "v_cvt_pk_bf16_f32 v9, v10, v11\n\t"
        "v_cvt_pk_bf16_f32 v12, v12, v13\n\t"
        "v_mfma_f32_16x16x32_bf16 v[56:59], v[192:195], v[104:107], v[56:59]\n\t"
        "ds_write_b64 v186, v[8:9] offset:24640\n\t"
        "v_mfma_f32_16x16x32_bf16 v[48:51], v[220:223], v[108:111], v[48:51]\n\t"
        "v_cvt_pk_bf16_f32 v13, v14, v15\n\t"
        "v_cvt_pk_bf16_f32 v16, v16, v17\n\t"
        "v_cvt_pk_bf16_f32 v17, v18, v19\n\t"
        "v_mfma_f32_16x16x32_bf16 v[52:55], v[224:227], v[108:111], v[52:55]\n\t"
        "ds_write_b64 v154, v[12:13] offset:24640\n\t"
        "v_mfma_f32_16x16x32_bf16 v[56:59], v[228:231], v[108:111], v[56:59]\n\t"
        "ds_write_b64 v186, v[16:17] offset:24704\n\t"
        "v_mfma_f32_16x16x32_bf16 v[48:51], a[24:27], v[112:115], v[48:51]\n\t"
        "v_cvt_pk_bf16_f32 v20, v20, v21\n\t"
        "v_cvt_pk_bf16_f32 v21, v22, v23\n\t"
        "v_mfma_f32_16x16x32_bf16 v[52:55], a[28:31], v[112:115], v[52:55]\n\t"
        "ds_write_b64 v154, v[20:21] offset:24704\n\t"
        "v_mfma_f32_16x16x32_bf16 v[56:59], a[32:35], v[112:115], v[56:59]\n\t"
        "s_cmp_gt_u32 s61, 48\n\t"
        "s_cbranch_scc1 .Lx54%=\n\t"
        ".Lx55%=:\n\t"
        "v_mfma_f32_16x16x32_bf16 v[48:51], a[60:63], v[84:87], v[48:51]\n\t"
        "v_mfma_f32_16x16x32_bf16 v[52:55], a[64:67], v[84:87], v[52:55]\n\t"
        "v_mfma_f32_16x16x32_bf16 v[56:59], a[68:71], v[84:87], v[56:59]\n\t"
        "v_mfma_f32_16x16x32_bf16 v[48:51], a[96:99], v[88:91], v[48:51]\n\t"
        "v_mfma_f32_16x16x32_bf16 v[52:55], a[100:103], v[88:91], v[52:55]\n\t"
        "s_waitcnt lgkmcnt(0)\n\t"
        "s_barrier\n\t"
        "v_xor_b32 v140, v181, v140\n\t"
        "v_mfma_f32_16x16x32_bf16 v[56:59], a[104:107], v[88:91], v[56:59]\n\t"
        "ds_read_b128 v[132:135], v187 offset:4096\n\t"
        "v_mfma_f32_16x16x32_bf16 v[48:51], a[132:135], v[92:95], v[48:51]\n\t"
        "ds_read_b128 v[136:139], v187 offset:4112\n\t"
        "v_add_u32 v151, s9, v180\n\t"
        "v_max_u32 v151, v151, v140\n\t"
        "s_waitcnt lgkmcnt(1)\n\t"
        "v_xor_b32 v132, v181, v132\n\t"
        "v_mfma_f32_16x16x32_bf16 v[52:55], a[136:139], v[92:95], v[52:55]\n\t"
        "ds_read_b128 v[104:107], v151 offset:0\n\t"
        "v_mfma_f32_16x16x32_bf16 v[56:59], a[140:143], v[92:95], v[56:59]\n\t"
        "ds_read_b128 v[108:111], v151 offset:64\n\t"
        "v_mfma_f32_16x16x32_bf16 v[48:51], a[168:171], v[96:99], v[48:51]\n\t"
        "ds_read_b128 v[112:115], v151 offset:128\n\t"
        "v_mfma_f32_16x16x32_bf16 v[52:55], a[172:175], v[96:99], v[52:55]\n\t"
        "ds_read_b32 v140, v187 offset:4128\n\t"
        "v_readlane_b32 s9, v155, 48\n\t"
        "v_mfma_f32_16x16x32_bf16 v[56:59], a[176:179], v[96:99], v[56:59]\n\t"
        "ds_read_b128 v[124:127], v182 offset:16\n\t"
        "v_add_u32 v183, s9, v172\n\t"
        "v_max_u32 v183, v183, v132\n\t"
        "v_xor_b32 v133, v181, v133\n\t"
        "v_mfma_f32_16x16x32_bf16 v[48:51], a[204:207], v[100:103], v[48:51]\n\t"
        "ds_read_b128 v[84:87], v183 offset:0\n\t"
        "v_add_u32 v151, s9, v173\n\t"
        "v_mfma_f32_16x16x32_bf16 v[52:55], a[208:211], v[100:103], v[52:55]\n\t"
        "ds_read_b128 v[88:91], v183 offset:64\n\t"
        "v_max_u32 v151, v151, v133\n\t"
        "v_mfma_f32_16x16x32_bf16 v[56:59], a[212:215], v[100:103], v[56:59]\n\t"
        "ds_read_b128 v[92:95], v183 offset:128\n\t"
        "v_xor_b32 v134, v181, v134\n\t"
        "s_waitcnt lgkmcnt(7)\n\t"
        "v_mfma_f32_16x16x32_bf16 v[48:51], a[240:243], v[104:107], v[48:51]\n\t"
        "ds_read_b128 v[96:99], v151 offset:0\n\t"
        "v_add_u32 v183, s9, v174\n\t"
        ".LpM2%=:\n\t"
        "s_cbranch_execz .LrM%=\n\t"
        "v_mfma_f32_16x16x32_bf16 v[52:55], a[244:247], v[104:107], v[52:55]\n\t"
        "ds_read_b128 v[100:103], v151 offset:64\n\t"
        "v_max_u32 v183, v183, v134\n\t"
        "v_mfma_f32_16x16x32_bf16 v[56:59], a[248:251], v[104:107], v[56:59]\n\t"
        "v_xor_b32 v135, v181, v135\n\t"
        "s_waitcnt lgkmcnt(8)\n\t"
        "v_mfma_f32_16x16x32_bf16 v[48:51], v[208:211], v[108:111], v[48:51]\n\t"
        "ds_read_b128 v[104:107], v151 offset:128\n\t"
        "v_add_u32 v151, s9, v175\n\t"
        "v_mfma_f32_16x16x32_bf16 v[52:55], v[212:215], v[108:111], v[52:55]\n\t"
        "v_max_u32 v151, v151, v135\n\t"
        "v_xor_b32 v136, v181, v136\n\t"
        "v_mfma_f32_16x16x32_bf16 v[56:59], v[216:219], v[108:111], v[56:59]\n\t"
        "s_waitcnt lgkmcnt(6)\n\t"
        "v_or3_b32 v124, v124, v125, v126\n\t"
        "v_or_b32 v124, v124, v127\n\t"
        "v_mfma_f32_16x16x32_bf16 v[48:51], v[244:247], v[112:115], v[48:51]\n\t"
        "ds_read_b128 v[108:111], v183 offset:0\n\t"
        "v_readfirstlane_b32 s8, v124\n\t"
        "v_mfma_f32_16x16x32_bf16 v[52:55], v[248:251], v[112:115], v[52:55]\n\t"
        "ds_read_b128 v[124:127], v147 offset:0\n\t"
        "s_or_b32 s6, s6, s8\n\t"
        "v_mfma_f32_16x16x32_bf16 v[56:59], v[252:255], v[112:115], v[56:59]\n\t"
        "s_waitcnt lgkmcnt(6)\n\t"
        "v_mfma_f32_16x16x32_bf16 v[72:75], a[12:15], v[84:87], 0\n\t"
        "ds_read_b128 v[112:115], v183 offset:64\n\t"
        "v_mfma_f32_16x16x32_bf16 v[76:79], a[16:19], v[84:87], 0\n\t"
        "v_mfma_f32_16x16x32_bf16 v[80:83], a[20:23], v[84:87], 0\n\t"
        "v_mfma_f32_16x16x32_bf16 v[72:75], a[48:51], v[88:91], v[72:75]\n\t"
        "ds_read_b128 v[84:87], v183 offset:128\n\t"
        "v_add_u32 v183, s9, v176\n\t"
        "v_mfma_f32_16x16x32_bf16 v[76:79], a[52:55], v[88:91], v[76:79]\n\t"
        "v_max_u32 v183, v183, v136\n\t"
        "v_xor_b32 v137, v181, v137\n\t"
        "v_mfma_f32_16x16x32_bf16 v[80:83], a[56:59], v[88:91], v[80:83]\n\t"
        "s_waitcnt lgkmcnt(4)\n\t"
        "v_mfma_f32_16x16x32_bf16 v[72:75], a[84:87], v[92:95], v[72:75]\n\t"
        "ds_read_b128 v[88:91], v151 offset:0\n\t"
        "v_mfma_f32_16x16x32_bf16 v[76:79], a[88:91], v[92:95], v[76:79]\n\t"
        "s_waitcnt lgkmcnt(3)\n\t"
        "buffer_store_dwordx4 v[124:127], v145, s[28:31], s50 offen sc1\n\t"
        "v_mfma_f32_16x16x32_bf16 v[80:83], a[92:95], v[92:95], v[80:83]\n\t"
        "ds_read_b128 v[124:127], v147 offset:4096\n\t"
        "s_add_u32 s50, s50, 86016\n\t"
        "v_mfma_f32_16x16x32_bf16 v[72:75], a[120:123], v[96:99], v[72:75]\n\t"
        "ds_read_b128 v[92:95], v151 offset:64\n\t"
        "v_mfma_f32_16x16x32_bf16 v[76:79], a[124:127], v[96:99], v[76:79]\n\t"
        "v_mfma_f32_16x16x32_bf16 v[80:83], a[128:131], v[96:99], v[80:83]\n\t"
        "v_mfma_f32_16x16x32_bf16 v[72:75], a[156:159], v[100:103], v[72:75]\n\t"
        "ds_read_b128 v[96:99], v151 offset:128\n\t"
        "v_add_u32 v151, s9, v177\n\t"
        "v_mfma_f32_16x16x32_bf16 v[76:79], a[160:163], v[100:103], v[76:79]\n\t"
        "v_max_u32 v151, v151, v137\n\t"
        "v_xor_b32 v138, v181, v138\n\t"
        "v_mfma_f32_16x16x32_bf16 v[80:83], a[164:167], v[100:103], v[80:83]\n\t"
        "v_mfma_f32_16x16x32_bf16 v[72:75], a[192:195], v[104:107], v[72:75]\n\t"
        "ds_read_b128 v[100:103], v183 offset:0\n\t"
        "v_mfma_f32_16x16x32_bf16 v[76:79], a[196:199], v[104:107], v[76:79]\n\t"
        "v_mfma_f32_16x16x32_bf16 v[80:83], a[200:203], v[104:107], v[80:83]\n\t"
        "v_mfma_f32_16x16x32_bf16 v[72:75], a[228:231], v[108:111], v[72:75]\n\t"
        "ds_read_b128 v[104:107], v183 offset:64\n\t"
        "v_mfma_f32_16x16x32_bf16 v[76:79], a[232:235], v[108:111], v[76:79]\n\t"
        "v_mfma_f32_16x16x32_bf16 v[80:83], a[236:239], v[108:111], v[80:83]\n\t"
        "s_waitcnt lgkmcnt(3)\n\t"
        "v_mfma_f32_16x16x32_bf16 v[72:75], v[196:199], v[112:115], v[72:75]\n\t"
        "buffer_store_dwordx4 v[124:127], v145, s[28:31], s50 offen sc1\n\t"
        "v_mfma_f32_16x16x32_bf16 v[76:79], v[200:203], v[112:115], v[76:79]\n\t"
        "ds_read_b128 v[108:111], v183 offset:128\n\t"
        "v_add_u32 v183, s9, v178\n\t"
        "v_mfma_f32_16x16x32_bf16 v[80:83], v[204:207], v[112:115], v[80:83]\n\t"
        "v_max_u32 v183, v183, v138\n\t"
        "v_xor_b32 v139, v181, v139\n\t"
        "v_mfma_f32_16x16x32_bf16 v[72:75], v[232:235], v[84:87], v[72:75]\n\t"
        "ds_read_b128 v[112:115], v151 offset:0\n\t"
        "v_readlane_b32 s55, v155, 49\n\t"
        "v_mfma_f32_16x16x32_bf16 v[76:79], v[236:239], v[84:87], v[76:79]\n\t"
        "v_cvt_pk_bf16_f32 v116, v48, v49\n\t"
        "v_cvt_pk_bf16_f32 v117, v50, v51\n\t"
        "v_mfma_f32_16x16x32_bf16 v[80:83], v[240:243], v[84:87], v[80:83]\n\t"
        "v_cvt_pk_bf16_f32 v118, v52, v53\n\t"
        "v_cvt_pk_bf16_f32 v119, v54, v55\n\t"
        "v_mfma_f32_16x16x32_bf16 v[72:75], a[0:3], v[88:91], v[72:75]\n\t"
        "ds_read_b128 v[84:87], v151 offset:64\n\t"
        "v_cvt_pk_bf16_f32 v120, v56, v57\n\t"
        "v_mfma_f32_16x16x32_bf16 v[76:79], a[4:7], v[88:91], v[76:79]\n\t"
        "v_cvt_pk_bf16_f32 v121, v58, v59\n\t"
        "v_mfma_f32_16x16x32_bf16 v[80:83], a[8:11], v[88:91], v[80:83]\n\t"
        "v_permlane16_swap_b32 v116, v118\n\t"
        "v_mfma_f32_16x16x32_bf16 v[72:75], a[36:39], v[92:95], v[72:75]\n\t"
        "v_permlane16_swap_b32 v117, v119\n\t"
        "v_mfma_f32_16x16x32_bf16 v[76:79], a[40:43], v[92:95], v[76:79]\n\t"
        "ds_read_b128 v[88:91], v151 offset:128\n\t"
        "v_add_u32 v151, s9, v179\n\t"
        "v_mfma_f32_16x16x32_bf16 v[80:83], a[44:47], v[92:95], v[80:83]\n\t"
        "v_max_u32 v151, v151, v139\n\t"
        "v_xor_b32 v140, v181, v140\n\t"
        "s_waitcnt lgkmcnt(3)\n\t"
        "v_mfma_f32_16x16x32_bf16 v[72:75], a[72:75], v[96:99], v[72:75]\n\t"
        "ds_read_b128 v[92:95], v183 offset:0\n\t"
        "v_mfma_f32_16x16x32_bf16 v[76:79], a[76:79], v[96:99], v[76:79]\n\t"
        "v_mfma_f32_16x16x32_bf16 v[80:83], a[80:83], v[96:99], v[80:83]\n\t"
        "buffer_store_dwordx4 v[116:119], v148, s[28:31], s55 offen sc1\n\t"
        "v_mfma_f32_16x16x32_bf16 v[72:75], a[108:111], v[100:103], v[72:75]\n\t"
        "ds_write_b64 v146, v[120:121] offset:8192\n\t"
        "v_mfma_f32_16x16x32_bf16 v[76:79], a[112:115], v[100:103], v[76:79]\n\t"
        "ds_read_b128 v[96:99], v183 offset:64\n\t"
        "v_mfma_f32_16x16x32_bf16 v[80:83], a[116:119], v[100:103], v[80:83]\n\t"
        "ds_read_b128 v[100:103], v183 offset:128\n\t"
        "v_add_u32 v183, s9, v180\n\t"
        "v_mfma_f32_16x16x32_bf16 v[72:75], a[144:147], v[104:107], v[72:75]\n\t"
        "v_max_u32 v183, v183, v140\n\t"
        "v_mfma_f32_16x16x32_bf16 v[76:79], a[148:151], v[104:107], v[76:79]\n\t"
        "v_mfma_f32_16x16x32_bf16 v[80:83], a[152:155], v[104:107], v[80:83]\n\t"
        "ds_read_b128 v[104:107], v151 offset:0\n\t"
        "v_mfma_f32_16x16x32_bf16 v[72:75], a[180:183], v[108:111], v[72:75]\n\t"
        "v_mfma_f32_16x16x32_bf16 v[76:79], a[184:187], v[108:111], v[76:79]\n\t"
        "v_mfma_f32_16x16x32_bf16 v[80:83], a[188:191], v[108:111], v[80:83]\n\t"
        "ds_read_b128 v[108:111], v151 offset:64\n\t"
        "s_waitcnt lgkmcnt(5)\n\t"
        "v_mfma_f32_16x16x32_bf16 v[72:75], a[216:219], v[112:115], v[72:75]\n\t"
        "v_mfma_f32_16x16x32_bf16 v[76:79], a[220:223], v[112:115], v[76:79]\n\t"
        "v_mfma_f32_16x16x32_bf16 v[80:83], a[224:227], v[112:115], v[80:83]\n\t"
        "ds_read_b128 v[112:115], v151 offset:128\n\t"
        "v_mfma_f32_16x16x32_bf16 v[72:75], a[252:255], v[84:87], v[72:75]\n\t"
        "v_mfma_f32_16x16x32_bf16 v[76:79], v[188:191], v[84:87], v[76:79]\n\t"
        "v_mfma_f32_16x16x32_bf16 v[80:83], v[192:195], v[84:87], v[80:83]\n\t"
        "ds_read_b128 v[84:87], v183 offset:0\n\t"
        "v_mfma_f32_16x16x32_bf16 v[72:75], v[220:223], v[88:91], v[72:75]\n\t"
        "v_mfma_f32_16x16x32_bf16 v[76:79], v[224:227], v[88:91], v[76:79]\n\t"
        "v_mfma_f32_16x16x32_bf16 v[80:83], v[228:231], v[88:91], v[80:83]\n\t"
        "ds_read_b128 v[88:91], v183 offset:64\n\t"
        "v_mfma_f32_16x16x32_bf16 v[72:75], a[24:27], v[92:95], v[72:75]\n\t"
        "v_mfma_f32_16x16x32_bf16 v[76:79], a[28:31], v[92:95], v[76:79]\n\t"
        "v_mfma_f32_16x16x32_bf16 v[80:83], a[32:35], v[92:95], v[80:83]\n\t"
        "ds_read_b128 v[92:95], v183 offset:128\n\t"
        "s_waitcnt lgkmcnt(4)\n\t"
        "v_mfma_f32_16x16x32_bf16 v[80:83], a[68:71], v[96:99], v[80:83]\n\t"
        "v_mfma_f32_16x16x32_bf16 v[80:83], a[104:107], v[100:103], v[80:83]\n\t"
        "v_mfma_f32_16x16x32_bf16 v[80:83], a[140:143], v[104:107], v[80:83]\n\t"
        "v_mfma_f32_16x16x32_bf16 v[80:83], a[176:179], v[108:111], v[80:83]\n\t"
        "s_waitcnt lgkmcnt(2)\n\t"
        "v_mfma_f32_16x16x32_bf16 v[80:83], a[212:215], v[112:115], v[80:83]\n\t"
        "v_mfma_f32_16x16x32_bf16 v[80:83], a[248:251], v[84:87], v[80:83]\n\t"
        "s_waitcnt lgkmcnt(1)\n\t"
        "v_mfma_f32_16x16x32_bf16 v[80:83], v[216:219], v[88:91], v[80:83]\n\t"
        "s_waitcnt lgkmcnt(0)\n\t"
        "v_mfma_f32_16x16x32_bf16 v[80:83], v[252:255], v[92:95], v[80:83]\n\t"
        "v_mfma_f32_16x16x32_bf16 v[72:75], a[60:63], v[96:99], v[72:75]\n\t"
        "v_mfma_f32_16x16x32_bf16 v[76:79], a[64:67], v[96:99], v[76:79]\n\t"
        "s_nop 5\n\t"
        "v_cvt_pk_bf16_f32 v120, v80, v81\n\t"
        "v_cvt_pk_bf16_f32 v121, v82, v83\n\t"
        "v_mfma_f32_16x16x32_bf16 v[72:75], a[96:99], v[100:103], v[72:75]\n\t"
        "ds_write_b64 v146, v[120:121] offset:10240\n\t"
        "v_mfma_f32_16x16x32_bf16 v[76:79], a[100:103], v[100:103], v[76:79]\n\t"
        "v_mfma_f32_16x16x32_bf16 v[72:75], a[132:135], v[104:107], v[72:75]\n\t"
        "v_mfma_f32_16x16x32_bf16 v[76:79], a[136:139], v[104:107], v[76:79]\n\t"
        "s_waitcnt lgkmcnt(0)\n\t"
        "s_barrier\n\t"
        "v_mfma_f32_16x16x32_bf16 v[72:75], a[168:171], v[108:111], v[72:75]\n\t"
        "ds_read_b128 v[124:127], v147 offset:8192\n\t"
        "v_readlane_b32 s55, v155, 51\n\t"
        "v_mfma_f32_16x16x32_bf16 v[76:79], a[172:175], v[108:111], v[76:79]\n\t"
        "v_mfma_f32_16x16x32_bf16 v[72:75], a[204:207], v[112:115], v[72:75]\n\t"
        "v_mfma_f32_16x16x32_bf16 v[76:79], a[208:211], v[112:115], v[76:79]\n\t"
        "v_mfma_f32_16x16x32_bf16 v[72:75], a[240:243], v[84:87], v[72:75]\n\t"
        "v_mfma_f32_16x16x32_bf16 v[76:79], a[244:247], v[84:87], v[76:79]\n\t"
        "v_mfma_f32_16x16x32_bf16 v[72:75], v[208:211], v[88:91], v[72:75]\n\t"
        "v_mfma_f32_16x16x32_bf16 v[76:79], v[212:215], v[88:91], v[76:79]\n\t"
        "v_mfma_f32_16x16x32_bf16 v[72:75], v[244:247], v[92:95], v[72:75]\n\t"
        "v_mfma_f32_16x16x32_bf16 v[76:79], v[248:251], v[92:95], v[76:79]\n\t"
        "s_nop 6\n\t"
        "v_cvt_pk_bf16_f32 v116, v72, v73\n\t"
        "v_cvt_pk_bf16_f32 v117, v74, v75\n\t"
        "v_cvt_pk_bf16_f32 v118, v76, v77\n\t"
        "v_cvt_pk_bf16_f32 v119, v78, v79\n\t"
        "s_nop 0\n\t"
        "v_permlane16_swap_b32 v116, v118\n\t"
        "v_permlane16_swap_b32 v117, v119\n\t"
        "buffer_store_dwordx4 v[116:119], v148, s[28:31], s55 offen  \n\t"
        "s_waitcnt lgkmcnt(0)\n\t"
        "buffer_store_dwordx4 v[124:127], v150, s[28:31], s52 offen  \n\t"
        "s_andn2_b32 s6, s6, 2\n\t"
        "s_cbranch_scc1 99f\n\t"
        "s_endpgm\n\t"
        "20:\n\t"
        "ds_bpermute_b32 v64, v65, v155 offset:172\n\t"
        "s_add_u32 m0, s37, -24576\n\t"
        "s_add_u32 s36, s36, 6144\n\t"
        "buffer_load_dwordx4 v140, s[24:27], s36 offen offset:0 lds\n\t"
        "ds_bpermute_b32 v78, v81, v155 offset:148\n\t"
        "ds_bpermute_b32 v79, v81, v155 offset:160\n\t"
        "v_readlane_b32 s71, v155, 26\n\t"
        "v_readlane_b32 s10, v155, 29\n\t"
        "s_nop 0\n\t"
        "v_mul_u32_u24 v73, s71, v66\n\t"
        "v_lshrrev_b32 v73, 11, v73\n\t"
        "v_lshlrev_b32 v66, 4, v66\n\t"
        "v_mad_u32_u24 v73, v73, s10, v66\n\t"
        "s_waitcnt lgkmcnt(2)\n\t"
        "v_add_u32 v64, v64, v73\n\t"
        "buffer_load_dwordx4 v141, s[24:27], s36 offen offset:1024 lds\n\t"
        "v_readlane_b32 s69, v155, 34\n\t"
        "v_readlane_b32 s70, v155, 35\n\t"
        "s_nop 0\n\t"
        "v_mad_u32_u24 v70, v52, s69, v70\n\t"
        "v_mad_u32_u24 v70, v158, s70, v70\n\t"
        "s_mul_i32 s71, s69, s68\n\t"
        "s_add_u32 s71, s71, 157728\n\t"
        "v_mad_u32_u24 v67, v82, s70, v67\n\t"
        "v_add_u32 v67, s71, v67\n\t"
        "v_readlane_b32 s11, v155, 30\n\t"
        "buffer_load_dwordx4 v142, s[24:27], s36 offen offset:2048 lds\n\t"
        "s_movk_i32 s10, 223\n\t"
        "v_mov_b32 v75, 0x7ff00000\n\t"
        "v_readlane_b32 s12, v155, 24\n\t"
        "v_readlane_b32 s13, v155, 25\n\t"
        "s_nop 0\n\t"
        "s_bfe_u32 s70, s12, 524288\n\t"
        "s_bfe_u32 s71, s13, 524288\n\t"
        "v_cmp_gt_u32 vcc, s71, v71\n\t"
        "v_add3_u32 v73, v71, s70, -2\n\t"
        "v_med3_i32 v73, v73, 0, s10\n\t"
        "s_add_u32 m0, s35, 4096\n\t"
        "s_add_u32 s43, s34, 43008\n\t"
        "buffer_load_dwordx4 v143, s[16:19], s43 offen  lds\n\t"
        "v_cndmask_b32 v74, v75, v72, vcc\n\t"
        "v_mad_u32_u24 v134, v73, s11, v74\n\t"
        "s_bfe_u32 s70, s12, 524296\n\t"
        "s_bfe_u32 s71, s13, 524296\n\t"
        "v_cmp_gt_u32 vcc, s71, v71\n\t"
        "v_add3_u32 v73, v71, s70, -2\n\t"
        "v_med3_i32 v73, v73, 0, s10\n\t"
        "v_cndmask_b32 v74, v75, v72, vcc\n\t"
        "v_mad_u32_u24 v135, v73, s11, v74\n\t"
        "s_add_u32 m0, s35, 20480\n\t"
        "s_add_u32 s43, s34, 215040\n\t"
        "buffer_load_dwordx4 v143, s[16:19], s43 offen  lds\n\t"
        "s_bfe_u32 s70, s12, 524304\n\t"
        "s_bfe_u32 s71, s13, 524304\n\t"
        "v_cmp_gt_u32 vcc, s71, v71\n\t"
        "v_add3_u32 v73, v71, s70, -2\n\t"
        "v_med3_i32 v73, v73, 0, s10\n\t"
        "v_cndmask_b32 v74, v75, v72, vcc\n\t"
        "v_mad_u32_u24 v136, v73, s11, v74\n\t"
        "s_bfe_u32 s70, s12, 524312\n\t"
        "s_bfe_u32 s71, s13, 524312\n\t"
        "v_cmp_gt_u32 vcc, s71, v71\n\t"
        "v_add3_u32 v73, v71, s70, -2\n\t"
        "v_med3_i32 v73, v73, 0, s10\n\t"
        "v_cndmask_b32 v74, v75, v72, vcc\n\t"
        "v_mad_u32_u24 v137, v73, s11, v74\n\t"
        "s_mov_b64 exec, 0\n\t"
        "s_cmp_eq_u32 s4, 0\n\t"
        "s_cbranch_scc1 .LpS0%=\n\t"
        "s_cmp_eq_u32 s4, 1\n\t"
        "s_cbranch_scc1 .LpS1%=\n\t"
        "s_cmp_eq_u32 s4, 2\n\t"
        "s_cbranch_scc1 .LpS2%=\n\t"
        "s_nop 0\n\t"
        ".LrS%=:\n\t"
        "s_mov_b64 exec, -1\n\t"
        "s_waitcnt vmcnt(9)\n\t"
        "s_barrier\n\t"
        "ds_read_b64_tr_b16 a[0:1], v138 offset:6144\n\t"
        "ds_read_b64_tr_b16 a[2:3], v138 offset:6912\n\t"
        "ds_read_b64_tr_b16 a[4:5], v139 offset:6144\n\t"
        "ds_read_b64_tr_b16 a[6:7], v139 offset:6912\n\t"
        "ds_read_b64_tr_b16 a[8:9], v149 offset:6144\n\t"
        "ds_read_b64_tr_b16 a[10:11], v149 offset:6912\n\t"
        "ds_read_b128 v[84:87], v128 offset:8192\n\t"
        "ds_read_b64_tr_b16 a[12:13], v138 offset:12288\n\t"
        "ds_read_b64_tr_b16 a[14:15], v138 offset:13056\n\t"
        "ds_read_b64_tr_b16 a[16:17], v139 offset:12288\n\t"
        "ds_read_b64_tr_b16 a[18:19], v139 offset:13056\n\t"
        "ds_read_b64_tr_b16 a[20:21], v149 offset:12288\n\t"
        "ds_read_b64_tr_b16 a[22:23], v149 offset:13056\n\t"
        "ds_read_b128 v[88:91], v128 offset:8192\n\t"
        "s_add_u32 m0, s37, -12288\n\t"
        "s_add_u32 s36, s36, s39\n\t"
        "s_waitcnt lgkmcnt(7)\n\t"
        "v_mfma_f32_16x16x32_bf16 v[12:15], a[0:3], v[84:87], 0\n\t"
        "buffer_load_dwordx4 v140, s[24:27], s36 offen offset:0 lds\n\t"
        "buffer_load_dwordx4 v141, s[24:27], s36 offen offset:1024 lds\n\t"
        "buffer_load_dwordx4 v142, s[24:27], s36 offen offset:2048 lds\n\t"
        "s_add_u32 m0, s35, 16384\n\t"
        "s_add_u32 s43, s34, 172032\n\t"
        "buffer_load_dwordx4 v143, s[16:19], s43 offen  lds\n\t"
        "s_add_u32 m0, s35, 32768\n\t"
        "s_add_u32 s43, s34, 344064\n\t"
        "s_min_u32 s43, s43, 9590784\n\t"
        "buffer_load_dwordx4 v143, s[16:19], s43 offen  lds\n\t"
        "s_waitcnt vmcnt(10)\n\t"
        "s_barrier\n\t"
        "ds_read_b128 v[92:95], v128 offset:12288\n\t"
        "ds_read_b64_tr_b16 a[24:25], v138 offset:0\n\t"
        "v_mfma_f32_16x16x32_bf16 v[16:19], a[4:7], v[84:87], 0\n\t"
        "ds_read_b64_tr_b16 a[26:27], v138 offset:768\n\t"
        "s_add_u32 m0, s35, 157696\n\t"
        "s_nop 0\n\t"
        "buffer_load_dwordx4 v64, s[20:23], 0 offen lds\n\t"
        "s_add_u32 m0, s37, 0\n\t"
        "s_add_u32 s36, s36, s38\n\t"
        "v_mfma_f32_16x16x32_bf16 v[20:23], a[8:11], v[84:87], 0\n\t"
        "buffer_load_dwordx4 v140, s[24:27], s36 offen offset:0 lds\n\t"
        "ds_read_b64_tr_b16 a[28:29], v139 offset:0\n\t"
        "v_readlane_b32 s44, v155, 32\n\t"
        "s_waitcnt lgkmcnt(4)\n\t"
        "v_mfma_f32_16x16x32_bf16 v[24:27], a[12:15], v[88:91], 0\n\t"
        "ds_read_b64_tr_b16 a[30:31], v139 offset:768\n\t"
        "ds_read_b64_tr_b16 a[32:33], v149 offset:0\n\t"
        "ds_read_b64_tr_b16 a[34:35], v149 offset:768\n\t"
        "v_readlane_b32 s45, v155, 33\n\t"
        "v_mfma_f32_16x16x32_bf16 v[28:31], a[16:19], v[88:91], 0\n\t"
        "buffer_load_dwordx4 v141, s[24:27], s36 offen offset:1024 lds\n\t"
        "ds_read_b128 v[96:99], v128 offset:8192\n\t"
        "v_readlane_b32 s62, v155, 36\n\t"
        "v_mfma_f32_16x16x32_bf16 v[32:35], a[20:23], v[88:91], 0\n\t"
        "ds_read_b128 v[100:103], v128 offset:12288\n\t"
        "s_lshl_b32 s70, s62, 5\n\t"
        "s_waitcnt lgkmcnt(8)\n\t"
        "v_mfma_f32_16x16x32_bf16 v[24:27], a[0:3], v[92:95], v[24:27]\n\t"
        "s_sub_u32 s71, s70, 16384\n\t"
        "s_mul_i32 s71, s71, s68\n\t"
        "v_mfma_f32_16x16x32_bf16 v[28:31], a[4:7], v[92:95], v[28:31]\n\t"
        "buffer_load_dwordx4 v142, s[24:27], s36 offen offset:2048 lds\n\t"
        "s_add_u32 m0, s35, 0\n\t"
        "s_add_u32 s43, s34, 0\n\t"
        "v_mfma_f32_16x16x32_bf16 v[32:35], a[8:11], v[92:95], v[32:35]\n\t"
        "s_mov_b32 exec_lo, 0\n\t"
        "s_mov_b32 exec_hi, 0xfff00000\n\t"
        "ds_write_b32 v119, v155\n\t"
        "s_mov_b64 exec, -1\n\t"
        "v_mfma_f32_16x16x32_bf16 v[36:39], a[12:15], v[92:95], 0\n\t"
        "v_add_u32 v172, s71, v128\n\t"
        "v_add_u32 v173, s71, v129\n\t"
        "v_mfma_f32_16x16x32_bf16 v[40:43], a[16:19], v[92:95], 0\n\t"
        "buffer_load_dwordx4 v143, s[16:19], s43 offen  lds\n\t"
        "s_add_u32 m0, s35, 36864\n\t"
        "s_add_u32 s43, s34, 387072\n\t"
        "s_min_u32 s43, s43, 9590784\n\t"
        "v_mfma_f32_16x16x32_bf16 v[44:47], a[20:23], v[92:95], 0\n\t"
        "buffer_load_dwordx4 v143, s[16:19], s43 offen  lds\n\t"
        "s_add_u32 m0, s37, 12288\n\t"
        "s_add_u32 s36, s36, s5\n\t"
        "buffer_load_dwordx4 v140, s[24:27], s36 offen offset:0 lds\n\t"
        "s_waitcnt vmcnt(9) lgkmcnt(3)\n\t"
        "s_barrier\n\t"
        "ds_read_b128 v[104:107], v128 offset:4096\n\t"
        "ds_read_b64_tr_b16 a[36:37], v138 offset:18432\n\t"
        "s_waitcnt lgkmcnt(3)\n\t"
        "v_mfma_f32_16x16x32_bf16 v[0:3], a[24:27], v[96:99], 0\n\t"
        "buffer_load_dwordx4 v141, s[24:27], s36 offen offset:1024 lds\n\t"
        "ds_read_b64_tr_b16 a[38:39], v138 offset:19200\n\t"
        "v_add_u32 v174, s71, v130\n\t"
        "v_mfma_f32_16x16x32_bf16 v[4:7], a[28:31], v[96:99], 0\n\t"
        "ds_read_b64_tr_b16 a[40:41], v139 offset:18432\n\t"
        "ds_read_b64_tr_b16 a[42:43], v139 offset:19200\n\t"
        "s_add_u32 s71, s71, s70\n\t"
        "v_mfma_f32_16x16x32_bf16 v[8:11], a[32:35], v[96:99], 0\n\t"
        "buffer_load_dwordx4 v142, s[24:27], s36 offen offset:2048 lds\n\t"
        "s_add_u32 m0, s37, 24576\n\t"
        "ds_read_b64_tr_b16 a[44:45], v149 offset:18432\n\t"
        "s_add_u32 s36, s36, s39\n\t"
        "v_mfma_f32_16x16x32_bf16 v[12:15], a[24:27], v[100:103], v[12:15]\n\t"
        "ds_read_b64_tr_b16 a[46:47], v149 offset:19200\n\t"
        "ds_read_b64_tr_b16 a[48:49], v138 offset:24576\n\t"
        "ds_read_b64_tr_b16 a[50:51], v138 offset:25344\n\t"
        "v_add_u32 v175, s71, v128\n\t"
        "v_mfma_f32_16x16x32_bf16 v[16:19], a[28:31], v[100:103], v[16:19]\n\t"
        "ds_read_b64_tr_b16 a[52:53], v139 offset:24576\n\t"
        "ds_read_b64_tr_b16 a[54:55], v139 offset:25344\n\t"
        "ds_read_b64_tr_b16 a[56:57], v149 offset:24576\n\t"
        "v_add_u32 v176, s71, v129\n\t"
        "v_mfma_f32_16x16x32_bf16 v[20:23], a[32:35], v[100:103], v[20:23]\n\t"
        "ds_read_b64_tr_b16 a[58:59], v149 offset:25344\n\t"
        "ds_read_b128 v[108:111], v128 offset:4160\n\t"
        "v_add_u32 v177, s71, v130\n\t"
        "s_waitcnt lgkmcnt(12)\n\t"
        "v_mfma_f32_16x16x32_bf16 v[0:3], a[0:3], v[104:107], v[0:3]\n\t"
        "ds_read_b64_tr_b16 a[60:61], v138 offset:30720\n\t"
        "ds_read_b64_tr_b16 a[62:63], v138 offset:31488\n\t"
        "s_add_u32 s71, s71, s70\n\t"
        "v_mfma_f32_16x16x32_bf16 v[4:7], a[4:7], v[104:107], v[4:7]\n\t"
        "ds_read_b64_tr_b16 a[64:65], v139 offset:30720\n\t"
        "ds_read_b64_tr_b16 a[66:67], v139 offset:31488\n\t"
        "v_add_u32 v178, s71, v128\n\t"
        "v_mfma_f32_16x16x32_bf16 v[8:11], a[8:11], v[104:107], v[8:11]\n\t"
        "ds_read_b64_tr_b16 a[68:69], v149 offset:30720\n\t"
        "ds_read_b64_tr_b16 a[70:71], v149 offset:31488\n\t"
        "v_add_u32 v179, s71, v129\n\t"
        "v_mfma_f32_16x16x32_bf16 v[12:15], a[12:15], v[104:107], v[12:15]\n\t"
        "ds_read_b128 v[112:115], v128 offset:8256\n\t"
        "v_add_u32 v180, s71, v130\n\t"
        "v_mfma_f32_16x16x32_bf16 v[16:19], a[16:19], v[104:107], v[16:19]\n\t"
        "s_cmp_eq_u32 s69, 36\n\t"
        "s_cselect_b64 exec, -1, 0\n\t"
        "v_swap_b32 v68, v69\n\t"
        "v_swap_b32 v173, v175\n\t"
        "v_swap_b32 v174, v178\n\t"
        "v_swap_b32 v177, v179\n\t"
        "s_mov_b64 exec, -1\n\t"
        "v_mfma_f32_16x16x32_bf16 v[20:23], a[20:23], v[104:107], v[20:23]\n\t"
        "ds_read_b128 v[84:87], v128 offset:12352\n\t"
        "v_add3_u32 v68, v68, v52, -1\n\t"
        "s_waitcnt lgkmcnt(8)\n\t"
        "v_mfma_f32_16x16x32_bf16 v[0:3], a[36:39], v[108:111], v[0:3]\n\t"
        "ds_read_b64_tr_b16 a[72:73], v138 offset:36864\n\t"
        "ds_read_b64_tr_b16 a[74:75], v138 offset:37632\n\t"
        "v_add3_u32 v69, v69, v158, -1\n\t"
        "v_mfma_f32_16x16x32_bf16 v[4:7], a[40:43], v[108:111], v[4:7]\n\t"
        "ds_read_b64_tr_b16 a[76:77], v139 offset:36864\n\t"
        "ds_read_b64_tr_b16 a[78:79], v139 offset:37632\n\t"
        "v_cvt_f32_i32 v68, v68\n\t"
        "v_mfma_f32_16x16x32_bf16 v[8:11], a[44:47], v[108:111], v[8:11]\n\t"
        "ds_read_b64_tr_b16 a[80:81], v149 offset:36864\n\t"
        "ds_read_b64_tr_b16 a[82:83], v149 offset:37632\n\t"
        "v_cvt_f32_i32 v69, v69\n\t"
        "v_mfma_f32_16x16x32_bf16 v[12:15], a[48:51], v[108:111], v[12:15]\n\t"
        "v_add_f32 v78, v78, v56\n\t"
        "v_add_f32 v79, v79, v57\n\t"
        "v_mfma_f32_16x16x32_bf16 v[16:19], a[52:55], v[108:111], v[16:19]\n\t"
        "v_readlane_b32 s69, v155, 31\n\t"
        "v_mfma_f32_16x16x32_bf16 v[20:23], a[56:59], v[108:111], v[20:23]\n\t"
        "s_nop 0\n\t"
        "v_mov_b32 v154, s69\n\t"
        "v_mov_b32 v119, s62\n\t"
        "s_waitcnt lgkmcnt(7)\n\t"
        "v_mfma_f32_16x16x32_bf16 v[0:3], a[60:63], v[112:115], v[0:3]\n\t"
        "buffer_load_dwordx4 v140, s[24:27], s36 offen offset:0 lds\n\t"
        "v_readlane_b32 s53, v155, 28\n\t"
        "v_mfma_f32_16x16x32_bf16 v[4:7], a[64:67], v[112:115], v[4:7]\n\t"
        "v_readlane_b32 s9, v155, 46\n\t"
        "s_sub_u32 s10, 43200, s11\n\t"
        "v_mfma_f32_16x16x32_bf16 v[8:11], a[68:71], v[112:115], v[8:11]\n\t"
        "s_mul_i32 s10, s10, s68\n\t"
        "s_sub_u32 s11, s11, 192\n\t"
        "v_mfma_f32_16x16x32_bf16 v[12:15], a[36:39], v[112:115], v[12:15]\n\t"
        "v_mad_u32_u24 v148, v158, s11, v144\n\t"
        "v_add_u32 v148, s10, v148\n\t"
        "v_mfma_f32_16x16x32_bf16 v[16:19], a[40:43], v[112:115], v[16:19]\n\t"
        "buffer_load_dwordx4 v141, s[24:27], s36 offen offset:1024 lds\n\t"
        "v_mad_u32_u24 v150, v83, s11, v145\n\t"
        "v_mfma_f32_16x16x32_bf16 v[20:23], a[44:47], v[112:115], v[20:23]\n\t"
        "s_lshl_b32 s70, s49, 0\n\t"
        "s_add_u32 s70, s70, 50\n\t"
        "v_mfma_f32_16x16x32_bf16 v[24:27], a[48:51], v[112:115], v[24:27]\n\t"
        "v_readlane_b32 s52, v155, s70\n\t"
        "v_readlane_b32 s51, v155, 49\n\t"
        "v_mfma_f32_16x16x32_bf16 v[28:31], a[52:55], v[112:115], v[28:31]\n\t"
        "s_add_u32 s52, s52, s10\n\t"
        "s_lshl_b32 s70, s49, 30\n\t"
        "v_mfma_f32_16x16x32_bf16 v[32:35], a[56:59], v[112:115], v[32:35]\n\t"
        "buffer_load_dwordx4 v142, s[24:27], s36 offen offset:2048 lds\n\t"
        "s_add_u32 m0, s37, 36864\n\t"
        "s_add_u32 s36, s36, s38\n\t"
        "s_waitcnt lgkmcnt(0)\n\t"
        "v_mfma_f32_16x16x32_bf16 v[12:15], a[60:63], v[84:87], v[12:15]\n\t"
        "s_add_u32 s70, s70, s10\n\t"
        "s_add_u32 s51, s51, s70\n\t"
        "v_mfma_f32_16x16x32_bf16 v[16:19], a[64:67], v[84:87], v[16:19]\n\t"
        "v_mfma_f32_16x16x32_bf16 v[20:23], a[68:71], v[84:87], v[20:23]\n\t"
        "v_mfma_f32_16x16x32_bf16 v[24:27], a[36:39], v[84:87], v[24:27]\n\t"
        "buffer_load_dwordx4 v140, s[24:27], s36 offen offset:0 lds\n\t"
        "ds_read_b128 v[88:91], v128 offset:4224\n\t"
        "v_mfma_f32_16x16x32_bf16 v[28:31], a[40:43], v[84:87], v[28:31]\n\t"
        "v_mfma_f32_16x16x32_bf16 v[32:35], a[44:47], v[84:87], v[32:35]\n\t"
        "v_mfma_f32_16x16x32_bf16 v[36:39], a[48:51], v[84:87], v[36:39]\n\t"
        "ds_read_b128 v[92:95], v128 offset:8320\n\t"
        "v_mfma_f32_16x16x32_bf16 v[40:43], a[52:55], v[84:87], v[40:43]\n\t"
        "buffer_load_dwordx4 v141, s[24:27], s36 offen offset:1024 lds\n\t"
        "v_mfma_f32_16x16x32_bf16 v[44:47], a[56:59], v[84:87], v[44:47]\n\t"
        "s_waitcnt lgkmcnt(1)\n\t"
        "v_mfma_f32_16x16x32_bf16 v[0:3], a[72:75], v[88:91], v[0:3]\n\t"
        "ds_read_b128 v[96:99], v128 offset:12416\n\t"
        "v_mfma_f32_16x16x32_bf16 v[4:7], a[76:79], v[88:91], v[4:7]\n\t"
        "v_mfma_f32_16x16x32_bf16 v[8:11], a[80:83], v[88:91], v[8:11]\n\t"
        "s_waitcnt vmcnt(10)\n\t"
        "s_barrier\n\t"
        "buffer_load_dwordx4 v142, s[24:27], s36 offen offset:2048 lds\n\t"
        "s_add_u32 m0, s37, 49152\n\t"
        "ds_read_b128 v[100:103], v128 offset:16384\n\t"
        "s_waitcnt lgkmcnt(2)\n\t"
        "v_mfma_f32_16x16x32_bf16 v[12:15], a[72:75], v[92:95], v[12:15]\n\t"
        "ds_read_b128 v[104:107], v128 offset:16448\n\t"
        "s_add_u32 s36, s36, s5\n\t"
        "ds_read_b64_tr_b16 a[84:85], v131 offset:0\n\t"
        "v_mfma_f32_16x16x32_bf16 v[16:19], a[76:79], v[92:95], v[16:19]\n\t"
        "ds_read_b32 v64, v70 offset:0\n\t"
        "ds_read_b64_tr_b16 a[86:87], v131 offset:768\n\t"
        "v_readlane_b32 s46, v155, 37\n\t"
        "v_mfma_f32_16x16x32_bf16 v[20:23], a[80:83], v[92:95], v[20:23]\n\t"
        "ds_read_b64_tr_b16 a[88:89], v132 offset:0\n\t"
        "ds_read_b64_tr_b16 a[90:91], v132 offset:768\n\t"
        "v_readlane_b32 s47, v155, 40\n\t"
        "s_waitcnt lgkmcnt(7)\n\t"
        "v_mfma_f32_16x16x32_bf16 v[24:27], a[72:75], v[96:99], v[24:27]\n\t"
        "buffer_load_dwordx4 v140, s[24:27], s36 offen offset:0 lds\n\t"
        "ds_read_b64_tr_b16 a[92:93], v133 offset:0\n\t"
        "v_add_f32 v58, s46, v68\n\t"
        "v_mfma_f32_16x16x32_bf16 v[28:31], a[76:79], v[96:99], v[28:31]\n\t"
        "ds_read_b64_tr_b16 a[94:95], v133 offset:768\n\t"
        "ds_read_b128 v[108:111], v128 offset:4224\n\t"
        "v_add_f32 v59, s47, v69\n\t"
        "v_mfma_f32_16x16x32_bf16 v[32:35], a[80:83], v[96:99], v[32:35]\n\t"
        "ds_read_b64_tr_b16 a[96:97], v131 offset:6144\n\t"
        "ds_read_b64_tr_b16 a[98:99], v131 offset:6912\n\t"
        "s_waitcnt lgkmcnt(11)\n\t"
        "v_mfma_f32_16x16x32_bf16 v[24:27], a[24:27], v[100:103], v[24:27]\n\t"
        "ds_read_b64_tr_b16 a[100:101], v132 offset:6144\n\t"
        "ds_read_b64_tr_b16 a[102:103], v132 offset:6912\n\t"
        "v_mfma_f32_16x16x32_bf16 v[28:31], a[28:31], v[100:103], v[28:31]\n\t"
        "buffer_load_dwordx4 v141, s[24:27], s36 offen offset:1024 lds\n\t"
        "ds_read_b64_tr_b16 a[104:105], v133 offset:6144\n\t"
        "v_mfma_f32_16x16x32_bf16 v[32:35], a[32:35], v[100:103], v[32:35]\n\t"
        "ds_read_b64_tr_b16 a[106:107], v133 offset:6912\n\t"
        "v_mfma_f32_16x16x32_bf16 v[36:39], a[0:3], v[100:103], v[36:39]\n\t"
        "ds_read_b128 v[112:115], v128 offset:8320\n\t"
        "v_mfma_f32_16x16x32_bf16 v[40:43], a[4:7], v[100:103], v[40:43]\n\t"
        "ds_read_b32 v67, v67 offset:0\n\t"
        "s_waitcnt lgkmcnt(11)\n\t"
        "v_perm_b32 v56, v64, v64, s44\n\t"
        "v_mfma_f32_16x16x32_bf16 v[44:47], a[8:11], v[100:103], v[44:47]\n\t"
        "buffer_load_dwordx4 v142, s[24:27], s36 offen offset:2048 lds\n\t"
        "s_add_u32 m0, s37, 0\n\t"
        "v_mfma_f32_16x16x32_bf16 v[24:27], a[60:63], v[104:107], v[24:27]\n\t"
        "ds_read_b128 v[84:87], v128 offset:12416\n\t"
        "s_add_u32 s36, s36, s39\n\t"
        "v_mfma_f32_16x16x32_bf16 v[28:31], a[64:67], v[104:107], v[28:31]\n\t"
        "ds_read_b32 v65, v70 offset:1280\n\t"
        "v_perm_b32 v57, v64, v64, s45\n\t"
        "v_mfma_f32_16x16x32_bf16 v[32:35], a[68:71], v[104:107], v[32:35]\n\t"
        "ds_read_b32 v66, v70 offset:2560\n\t"
        "v_add_f32 v56, v56, v58\n\t"
        "v_mfma_f32_16x16x32_bf16 v[36:39], a[36:39], v[104:107], v[36:39]\n\t"
        "ds_read_b128 v[88:91], v128 offset:16512\n\t"
        "v_add_f32 v57, v57, v59\n\t"
        "v_mfma_f32_16x16x32_bf16 v[40:43], a[40:43], v[104:107], v[40:43]\n\t"
        "v_cvt_pk_bf16_f32 v52, v56, v57\n\t"
        "v_cvt_pk_bf16_f32 v58, v58, v59\n\t"
        "v_mfma_f32_16x16x32_bf16 v[44:47], a[44:47], v[104:107], v[44:47]\n\t"
        "v_cmp_ne_u32 s[64:65], v52, v58\n\t"
        "v_cvt_pk_bf16_f32 v58, v78, v79\n\t"
        "s_waitcnt lgkmcnt(7)\n\t"
        "v_mfma_f32_16x16x32_bf16 v[12:15], a[84:87], v[108:111], v[12:15]\n\t"
        "v_mbcnt_lo_u32_b32 v56, s64, 1\n\t"
        "v_mbcnt_hi_u32_b32 v56, s65, v56\n\t"
        "v_mfma_f32_16x16x32_bf16 v[16:19], a[88:91], v[108:111], v[16:19]\n\t"
        "s_bcnt1_i32_b64 s61, s[64:65]\n\t"
        "v_add_u32 v54, s61, 1\n\t"
        "v_mfma_f32_16x16x32_bf16 v[20:23], a[92:95], v[108:111], v[20:23]\n\t"
        "v_lshl_add_u32 v56, v56, 3, s60\n\t"
        "s_waitcnt lgkmcnt(4)\n\t"
        "v_perm_b32 v57, v67, v67, s45\n\t"
        "v_mfma_f32_16x16x32_bf16 v[0:3], a[96:99], v[112:115], v[0:3]\n\t"
        "s_mov_b64 exec, s[64:65]\n\t"
        "ds_write_b64 v56, v[52:53]\n\t"
        "s_mov_b64 exec, -1\n\t"
        "v_mfma_f32_16x16x32_bf16 v[4:7], a[100:103], v[112:115], v[4:7]\n\t"
        "v_swap_b32 v53, v80\n\t"
        "v_perm_b32 v56, v67, v67, s44\n\t"
        "v_mfma_f32_16x16x32_bf16 v[8:11], a[104:107], v[112:115], v[8:11]\n\t"
        "v_add_f32 v56, v56, v78\n\t"
        "v_add_f32 v57, v57, v79\n\t"
        "v_mfma_f32_16x16x32_bf16 v[24:27], a[84:87], v[112:115], v[24:27]\n\t"
        "v_cvt_pk_bf16_f32 v52, v56, v57\n\t"
        "v_cmp_ne_u32 s[64:65], v52, v58\n\t"
        "v_mfma_f32_16x16x32_bf16 v[28:31], a[88:91], v[112:115], v[28:31]\n\t"
        "s_nop 0\n\t"
        "s_and_b64 s[64:65], s[64:65], 0xffffff\n\t"
        "v_mbcnt_lo_u32_b32 v56, s64, v54\n\t"
        "v_mfma_f32_16x16x32_bf16 v[32:35], a[92:95], v[112:115], v[32:35]\n\t"
        "s_bcnt1_i32_b64 s46, s[64:65]\n\t"
        "s_add_u32 s61, s61, s46\n\t"
        "s_waitcnt lgkmcnt(1)\n\t"
        "v_mfma_f32_16x16x32_bf16 v[12:15], a[96:99], v[84:87], v[12:15]\n\t"
        "s_waitcnt vmcnt(6)\n\t"
        "s_barrier\n\t"
        "buffer_load_dwordx4 v140, s[24:27], s36 offen offset:0 lds\n\t"
        "ds_read_b128 v[92:95], v128 offset:0\n\t"
        "v_mfma_f32_16x16x32_bf16 v[16:19], a[100:103], v[84:87], v[16:19]\n\t"
        "ds_read_b128 v[96:99], v128 offset:64\n\t"
        "ds_read_b128 v[100:103], v128 offset:128\n\t"
        "v_add_u32 v54, s46, v54\n\t"
        "v_mfma_f32_16x16x32_bf16 v[20:23], a[104:107], v[84:87], v[20:23]\n\t"
        "ds_read_b128 v[104:107], v128 offset:20480\n\t"
        "ds_read_b64_tr_b16 a[108:109], v131 offset:12288\n\t"
        "v_lshl_add_u32 v56, v56, 3, s60\n\t"
        "v_mfma_f32_16x16x32_bf16 v[36:39], a[84:87], v[84:87], v[36:39]\n\t"
        "s_mov_b64 exec, s[64:65]\n\t"
        "ds_write_b64 v56, v[52:53]\n\t"
        "s_mov_b64 exec, -1\n\t"
        "ds_read_b128 v[108:111], v128 offset:20544\n\t"
        "v_mfma_f32_16x16x32_bf16 v[40:43], a[88:91], v[84:87], v[40:43]\n\t"
        "buffer_load_dwordx4 v141, s[24:27], s36 offen offset:1024 lds\n\t"
        "ds_read_b64_tr_b16 a[110:111], v131 offset:13056\n\t"
        "v_add_u32 v53, 2048, v80\n\t"
        ".LpS0%=:\n\t"
        "s_cbranch_execz .LrS%=\n\t"
        "v_mfma_f32_16x16x32_bf16 v[44:47], a[92:95], v[84:87], v[44:47]\n\t"
        "ds_read_b64_tr_b16 a[112:113], v132 offset:12288\n\t"
        "ds_read_b64_tr_b16 a[114:115], v132 offset:13056\n\t"
        "v_readlane_b32 s46, v155, 38\n\t"
        "v_mfma_f32_16x16x32_bf16 v[24:27], a[96:99], v[88:91], v[24:27]\n\t"
        "ds_read_b128 v[112:115], v128 offset:20608\n\t"
        "ds_read_b64_tr_b16 a[116:117], v133 offset:12288\n\t"
        "v_readlane_b32 s47, v155, 41\n\t"
        "v_mfma_f32_16x16x32_bf16 v[28:31], a[100:103], v[88:91], v[28:31]\n\t"
        "ds_read_b64_tr_b16 a[118:119], v133 offset:13056\n\t"
        "ds_read_b64_tr_b16 a[120:121], v131 offset:18432\n\t"
        "v_perm_b32 v56, v65, v65, s44\n\t"
        "v_mfma_f32_16x16x32_bf16 v[32:35], a[104:107], v[88:91], v[32:35]\n\t"
        "buffer_load_dwordx4 v142, s[24:27], s36 offen offset:2048 lds\n\t"
        "s_add_u32 m0, s37, 12288\n\t"
        "ds_read_b64_tr_b16 a[122:123], v131 offset:19200\n\t"
        "v_mfma_f32_16x16x32_bf16 v[36:39], a[72:75], v[88:91], v[36:39]\n\t"
        "ds_read_b64_tr_b16 a[124:125], v132 offset:18432\n\t"
        "s_add_u32 s36, s36, s38\n\t"
        "ds_read_b64_tr_b16 a[126:127], v132 offset:19200\n\t"
        "v_mfma_f32_16x16x32_bf16 v[40:43], a[76:79], v[88:91], v[40:43]\n\t"
        "ds_read_b64_tr_b16 a[128:129], v133 offset:18432\n\t"
        "ds_read_b64_tr_b16 a[130:131], v133 offset:19200\n\t"
        "v_perm_b32 v57, v65, v65, s45\n\t"
        "v_mfma_f32_16x16x32_bf16 v[44:47], a[80:83], v[88:91], v[44:47]\n\t"
        "ds_read_b128 v[84:87], v129 offset:0\n\t"
        "ds_read_b64_tr_b16 a[132:133], v131 offset:24576\n\t"
        "v_add_f32 v58, s46, v68\n\t"
        "s_waitcnt lgkmcnt(13)\n\t"
        "v_mfma_f32_16x16x32_bf16 v[0:3], a[12:15], v[92:95], v[0:3]\n\t"
        "ds_read_b128 v[88:91], v129 offset:4096\n\t"
        "ds_read_b64_tr_b16 a[134:135], v131 offset:25344\n\t"
        "v_add_f32 v59, s47, v69\n\t"
        "v_mfma_f32_16x16x32_bf16 v[4:7], a[16:19], v[92:95], v[4:7]\n\t"
        "ds_read_b64_tr_b16 a[136:137], v132 offset:24576\n\t"
        "ds_read_b64_tr_b16 a[138:139], v132 offset:25344\n\t"
        "v_add_f32 v56, v56, v58\n\t"
        "v_mfma_f32_16x16x32_bf16 v[8:11], a[20:23], v[92:95], v[8:11]\n\t"
        "ds_read_b64_tr_b16 a[140:141], v133 offset:24576\n\t"
        "ds_read_b64_tr_b16 a[142:143], v133 offset:25344\n\t"
        "v_add_f32 v57, v57, v59\n\t"
        "v_mfma_f32_16x16x32_bf16 v[0:3], a[48:51], v[96:99], v[0:3]\n\t"
        "ds_read_b64_tr_b16 a[144:145], v131 offset:30720\n\t"
        "ds_read_b64_tr_b16 a[146:147], v131 offset:31488\n\t"
        "v_cvt_pk_bf16_f32 v52, v56, v57\n\t"
        "v_mfma_f32_16x16x32_bf16 v[4:7], a[52:55], v[96:99], v[4:7]\n\t"
        "ds_read_b64_tr_b16 a[148:149], v132 offset:30720\n\t"
        "ds_read_b64_tr_b16 a[150:151], v132 offset:31488\n\t"
        "v_cvt_pk_bf16_f32 v58, v58, v59\n\t"
        "v_mfma_f32_16x16x32_bf16 v[8:11], a[56:59], v[96:99], v[8:11]\n\t"
        "s_waitcnt vmcnt(6)\n\t"
        "s_barrier\n\t"
        "ds_read_b64_tr_b16 a[152:153], v133 offset:30720\n\t"
        "ds_read_b64_tr_b16 a[154:155], v133 offset:31488\n\t"
        "v_mfma_f32_16x16x32_bf16 v[0:3], a[84:87], v[100:103], v[0:3]\n\t"
        "ds_read_b128 v[92:95], v129 offset:8192\n\t"
        "ds_read_b64_tr_b16 a[156:157], v131 offset:36864\n\t"
        "v_cmp_ne_u32 s[64:65], v52, v58\n\t"
        "v_mfma_f32_16x16x32_bf16 v[4:7], a[88:91], v[100:103], v[4:7]\n\t"
        "ds_read_b64_tr_b16 a[158:159], v131 offset:37632\n\t"
        "ds_read_b64_tr_b16 a[160:161], v132 offset:36864\n\t"
        "v_mbcnt_lo_u32_b32 v56, s64, v54\n\t"
        "v_mfma_f32_16x16x32_bf16 v[8:11], a[92:95], v[100:103], v[8:11]\n\t"
        "ds_read_b64_tr_b16 a[162:163], v132 offset:37632\n\t"
        "ds_read_b64_tr_b16 a[164:165], v133 offset:36864\n\t"
        "v_mbcnt_hi_u32_b32 v56, s65, v56\n\t"
        "v_mfma_f32_16x16x32_bf16 v[36:39], a[24:27], v[104:107], v[36:39]\n\t"
        "ds_read_b64_tr_b16 a[166:167], v133 offset:37632\n\t"
        "s_bcnt1_i32_b64 s46, s[64:65]\n\t"
        "v_mfma_f32_16x16x32_bf16 v[40:43], a[28:31], v[104:107], v[40:43]\n\t"
        "ds_read_b64_tr_b16 a[168:169], v131 offset:43008\n\t"
        "s_add_u32 s61, s61, s46\n\t"
        "v_mfma_f32_16x16x32_bf16 v[44:47], a[32:35], v[104:107], v[44:47]\n\t"
        "ds_read_b64_tr_b16 a[170:171], v131 offset:43776\n\t"
        "v_add_u32 v54, s46, v54\n\t"
        "v_mfma_f32_16x16x32_bf16 v[36:39], a[60:63], v[108:111], v[36:39]\n\t"
        "ds_read_b64_tr_b16 a[172:173], v132 offset:43008\n\t"
        "v_min_u32 v56, 97, v56\n\t"
        "v_mfma_f32_16x16x32_bf16 v[40:43], a[64:67], v[108:111], v[40:43]\n\t"
        "ds_read_b64_tr_b16 a[174:175], v132 offset:43776\n\t"
        "v_lshl_add_u32 v56, v56, 3, s60\n\t"
        "v_mfma_f32_16x16x32_bf16 v[44:47], a[68:71], v[108:111], v[44:47]\n\t"
        "s_mov_b64 exec, s[64:65]\n\t"
        "ds_write_b64 v56, v[52:53]\n\t"
        "s_mov_b64 exec, -1\n\t"
        "s_waitcnt lgkmcnt(10)\n\t"
        "v_mfma_f32_16x16x32_bf16 v[36:39], a[96:99], v[112:115], v[36:39]\n\t"
        "s_waitcnt vmcnt(0)\n\t"
        "s_barrier\n\t"
        "v_mfma_f32_16x16x32_bf16 v[40:43], a[100:103], v[112:115], v[40:43]\n\t"
        "buffer_load_dwordx4 v140, s[24:27], s36 offen offset:0 lds\n\t"
        "v_add_u32 v53, 2048, v53\n\t"
        "v_mfma_f32_16x16x32_bf16 v[44:47], a[104:107], v[112:115], v[44:47]\n\t"
        "ds_read_b128 v[96:99], v129 offset:64\n\t"
        "v_readlane_b32 s46, v155, 39\n\t"
        "v_mfma_f32_16x16x32_bf16 v[0:3], a[120:123], v[84:87], v[0:3]\n\t"
        "ds_read_b128 v[100:103], v129 offset:4160\n\t"
        "ds_read_b64_tr_b16 a[176:177], v133 offset:43008\n\t"
        "v_readlane_b32 s47, v155, 42\n\t"
        "v_mfma_f32_16x16x32_bf16 v[4:7], a[124:127], v[84:87], v[4:7]\n\t"
        "ds_read_b64_tr_b16 a[178:179], v133 offset:43776\n\t"
        "v_perm_b32 v56, v66, v66, s44\n\t"
        "v_mfma_f32_16x16x32_bf16 v[8:11], a[128:131], v[84:87], v[8:11]\n\t"
        "buffer_load_dwordx4 v141, s[24:27], s36 offen offset:1024 lds\n\t"
        "v_perm_b32 v57, v66, v66, s45\n\t"
        "v_mfma_f32_16x16x32_bf16 v[0:3], a[108:111], v[88:91], v[0:3]\n\t"
        "ds_read_b64_tr_b16 a[180:181], v131 offset:49152\n\t"
        "v_add_f32 v58, s46, v68\n\t"
        "v_mfma_f32_16x16x32_bf16 v[4:7], a[112:115], v[88:91], v[4:7]\n\t"
        "ds_read_b64_tr_b16 a[182:183], v131 offset:49920\n\t"
        "v_add_f32 v59, s47, v69\n\t"
        "v_mfma_f32_16x16x32_bf16 v[8:11], a[116:119], v[88:91], v[8:11]\n\t"
        "ds_read_b64_tr_b16 a[184:185], v132 offset:49152\n\t"
        "v_add_f32 v56, v56, v58\n\t"
        "v_mfma_f32_16x16x32_bf16 v[12:15], a[120:123], v[88:91], v[12:15]\n\t"
        "buffer_load_dwordx4 v142, s[24:27], s36 offen offset:2048 lds\n\t"
        "s_add_u32 m0, s37, 24576\n\t"
        "v_mfma_f32_16x16x32_bf16 v[16:19], a[124:127], v[88:91], v[16:19]\n\t"
        "ds_read_b128 v[104:107], v129 offset:8256\n\t"
        "s_add_u32 s36, s36, s5\n\t"
        "v_mfma_f32_16x16x32_bf16 v[20:23], a[128:131], v[88:91], v[20:23]\n\t"
        "ds_read_b64_tr_b16 a[186:187], v132 offset:49920\n\t"
        "v_add_f32 v57, v57, v59\n\t"
        "v_mfma_f32_16x16x32_bf16 v[0:3], a[132:135], v[92:95], v[0:3]\n\t"
        "ds_read_b64_tr_b16 a[188:189], v133 offset:49152\n\t"
        "v_cvt_pk_bf16_f32 v52, v56, v57\n\t"
        "v_mfma_f32_16x16x32_bf16 v[4:7], a[136:139], v[92:95], v[4:7]\n\t"
        "buffer_load_dwordx4 v140, s[24:27], s36 offen offset:0 lds\n\t"
        "v_cvt_pk_bf16_f32 v58, v58, v59\n\t"
        "v_mfma_f32_16x16x32_bf16 v[8:11], a[140:143], v[92:95], v[8:11]\n\t"
        "ds_read_b64_tr_b16 a[190:191], v133 offset:49920\n\t"
        "v_cmp_ne_u32 s[64:65], v52, v58\n\t"
        "v_mfma_f32_16x16x32_bf16 v[12:15], a[108:111], v[92:95], v[12:15]\n\t"
        "ds_read_b64_tr_b16 a[192:193], v131 offset:55296\n\t"
        "v_mbcnt_lo_u32_b32 v56, s64, v54\n\t"
        "v_mfma_f32_16x16x32_bf16 v[16:19], a[112:115], v[92:95], v[16:19]\n\t"
        "ds_read_b64_tr_b16 a[194:195], v131 offset:56064\n\t"
        "v_mbcnt_hi_u32_b32 v56, s65, v56\n\t"
        "v_mfma_f32_16x16x32_bf16 v[20:23], a[116:119], v[92:95], v[20:23]\n\t"
        "buffer_load_dwordx4 v141, s[24:27], s36 offen offset:1024 lds\n\t"
        "s_bcnt1_i32_b64 s46, s[64:65]\n\t"
        "v_mfma_f32_16x16x32_bf16 v[24:27], a[120:123], v[92:95], v[24:27]\n\t"
        "ds_read_b128 v[108:111], v129 offset:12288\n\t"
        "s_add_u32 s61, s61, s46\n\t"
        "v_mfma_f32_16x16x32_bf16 v[28:31], a[124:127], v[92:95], v[28:31]\n\t"
        "ds_read_b64_tr_b16 a[196:197], v132 offset:55296\n\t"
        "s_mov_b32 s3, s61\n\t"
        "v_mfma_f32_16x16x32_bf16 v[32:35], a[128:131], v[92:95], v[32:35]\n\t"
        "ds_read_b64_tr_b16 a[198:199], v132 offset:56064\n\t"
        "v_min_u32 v56, 97, v56\n\t"
        "s_waitcnt lgkmcnt(8)\n\t"
        "v_mfma_f32_16x16x32_bf16 v[0:3], a[156:159], v[96:99], v[0:3]\n\t"
        "buffer_load_dwordx4 v142, s[24:27], s36 offen offset:2048 lds\n\t"
        "s_add_u32 m0, s37, 36864\n\t"
        "v_mfma_f32_16x16x32_bf16 v[4:7], a[160:163], v[96:99], v[4:7]\n\t"
        "ds_read_b64_tr_b16 a[200:201], v133 offset:55296\n\t"
        "s_add_u32 s36, s36, s39\n\t"
        "v_mfma_f32_16x16x32_bf16 v[8:11], a[164:167], v[96:99], v[8:11]\n\t"
        "ds_read_b64_tr_b16 a[202:203], v133 offset:56064\n\t"
        "v_lshl_add_u32 v56, v56, 3, s60\n\t"
        "v_mfma_f32_16x16x32_bf16 v[0:3], a[144:147], v[100:103], v[0:3]\n\t"
        "s_mov_b64 exec, s[64:65]\n\t"
        "ds_write_b64 v56, v[52:53]\n\t"
        "s_mov_b64 exec, -1\n\t"
        "v_mfma_f32_16x16x32_bf16 v[4:7], a[148:151], v[100:103], v[4:7]\n\t"
        "s_cmp_gt_u32 s3, 64\n\t"
        "s_cselect_b32 s8, 1, 0\n\t"
        "v_mfma_f32_16x16x32_bf16 v[8:11], a[152:155], v[100:103], v[8:11]\n\t"
        "ds_read_b64_tr_b16 a[204:205], v131 offset:0\n\t"
        "s_or_b32 s56, s56, s8\n\t"
        "v_mfma_f32_16x16x32_bf16 v[12:15], a[156:159], v[100:103], v[12:15]\n\t"
        "ds_read_b128 v[112:115], v129 offset:128\n\t"
        "s_min_u32 s61, s61, 80\n\t"
        "v_mfma_f32_16x16x32_bf16 v[16:19], a[160:163], v[100:103], v[16:19]\n\t"
        "ds_read_b64_tr_b16 a[206:207], v131 offset:768\n\t"
        "v_min_u32 v78, s61, v116\n\t"
        "v_mfma_f32_16x16x32_bf16 v[20:23], a[164:167], v[100:103], v[20:23]\n\t"
        "ds_read_b64_tr_b16 a[208:209], v132 offset:0\n\t"
        "v_lshl_add_u32 v78, v78, 3, s60\n\t"
        "v_mfma_f32_16x16x32_bf16 v[0:3], a[168:171], v[104:107], v[0:3]\n\t"
        "ds_read_b64 v[62:63], v78\n\t"
        "v_cmp_ge_u32 s[66:67], s61, v116\n\t"
        "v_mfma_f32_16x16x32_bf16 v[4:7], a[172:175], v[104:107], v[4:7]\n\t"
        "ds_read_b128 v[84:87], v129 offset:4224\n\t"
        "s_mov_b32 s46, 0x07060302\n\t"
        "v_mfma_f32_16x16x32_bf16 v[8:11], a[176:179], v[104:107], v[8:11]\n\t"
        "ds_read_b64_tr_b16 a[210:211], v132 offset:768\n\t"
        "v_mfma_f32_16x16x32_bf16 v[12:15], a[144:147], v[104:107], v[12:15]\n\t"
        "ds_read_b64_tr_b16 a[212:213], v133 offset:0\n\t"
        "v_mfma_f32_16x16x32_bf16 v[16:19], a[148:151], v[104:107], v[16:19]\n\t"
        "ds_read_b64_tr_b16 a[214:215], v133 offset:768\n\t"
        "v_mfma_f32_16x16x32_bf16 v[20:23], a[152:155], v[104:107], v[20:23]\n\t"
        "ds_read_b64_tr_b16 a[216:217], v131 offset:6144\n\t"
        "v_mfma_f32_16x16x32_bf16 v[24:27], a[156:159], v[104:107], v[24:27]\n\t"
        "ds_read_b128 v[88:91], v129 offset:8320\n\t"
        "v_mfma_f32_16x16x32_bf16 v[28:31], a[160:163], v[104:107], v[28:31]\n\t"
        "ds_read_b64_tr_b16 a[218:219], v131 offset:6912\n\t"
        "v_mfma_f32_16x16x32_bf16 v[32:35], a[164:167], v[104:107], v[32:35]\n\t"
        "ds_read_b64_tr_b16 a[220:221], v132 offset:6144\n\t"
        "s_waitcnt lgkmcnt(10)\n\t"
        "v_mfma_f32_16x16x32_bf16 v[12:15], a[132:135], v[108:111], v[12:15]\n\t"
        "s_waitcnt vmcnt(3)\n\t"
        "s_barrier\n\t"
        "v_mfma_f32_16x16x32_bf16 v[16:19], a[136:139], v[108:111], v[16:19]\n\t"
        "s_mov_b64 exec, s[66:67]\n\t"
        "s_waitcnt lgkmcnt(8)\n\t"
        "ds_write_b32 v63, v117\n\t"
        "s_mov_b64 exec, -1\n\t"
        "v_mfma_f32_16x16x32_bf16 v[20:23], a[140:143], v[108:111], v[20:23]\n\t"
        "buffer_load_dwordx4 v140, s[24:27], s36 offen offset:0 lds\n\t"
        "v_and_b32 v78, 0xfffff800, v63\n\t"
        "v_mfma_f32_16x16x32_bf16 v[24:27], a[108:111], v[108:111], v[24:27]\n\t"
        "ds_read_b128 v[64:67], v78 offset:1536\n\t"
        "v_lshlrev_b32 v72, 16, v62\n\t"
        "v_mfma_f32_16x16x32_bf16 v[28:31], a[112:115], v[108:111], v[28:31]\n\t"
        "ds_read_b64_tr_b16 a[222:223], v132 offset:6912\n\t"
        "v_and_b32 v73, 0xffff0000, v62\n\t"
        "v_mfma_f32_16x16x32_bf16 v[32:35], a[116:119], v[108:111], v[32:35]\n\t"
        "ds_read_b64_tr_b16 a[224:225], v133 offset:6144\n\t"
        "v_med3_f32 v72, v72, 0, s63\n\t"
        "v_mfma_f32_16x16x32_bf16 v[36:39], a[120:123], v[108:111], v[36:39]\n\t"
        "buffer_load_dwordx4 v141, s[24:27], s36 offen offset:1024 lds\n\t"
        "v_med3_f32 v73, v73, 0, s63\n\t"
        "v_mfma_f32_16x16x32_bf16 v[40:43], a[124:127], v[108:111], v[40:43]\n\t"
        "ds_read_b128 v[92:95], v129 offset:12352\n\t"
        "v_perm_b32 v62, v73, v72, s46\n\t"
        "v_mfma_f32_16x16x32_bf16 v[44:47], a[128:131], v[108:111], v[44:47]\n\t"
        "ds_read_b64_tr_b16 a[226:227], v133 offset:6912\n\t"
        "v_fract_f32 v74, v72\n\t"
        "v_mfma_f32_16x16x32_bf16 v[0:3], a[192:195], v[112:115], v[0:3]\n\t"
        "ds_read_b64_tr_b16 a[228:229], v131 offset:12288\n\t"
        "v_fract_f32 v75, v73\n\t"
        "v_mfma_f32_16x16x32_bf16 v[4:7], a[196:199], v[112:115], v[4:7]\n\t"
        "buffer_load_dwordx4 v142, s[24:27], s36 offen offset:2048 lds\n\t"
        "s_add_u32 m0, s37, 49152\n\t"
        "v_mfma_f32_16x16x32_bf16 v[8:11], a[200:203], v[112:115], v[8:11]\n\t"
        "ds_read_b64_tr_b16 a[230:231], v131 offset:13056\n\t"
        "s_add_u32 s36, s36, s38\n\t"
        "s_waitcnt lgkmcnt(7)\n\t"
        "v_mfma_f32_16x16x32_bf16 v[0:3], a[180:183], v[84:87], v[0:3]\n\t"
        "ds_read_b64_tr_b16 a[232:233], v132 offset:12288\n\t"
        "v_cvt_u32_f32 v76, v72\n\t"
        "v_mfma_f32_16x16x32_bf16 v[4:7], a[184:187], v[84:87], v[4:7]\n\t"
        "ds_read_b64_tr_b16 a[234:235], v132 offset:13056\n\t"
        "v_cvt_u32_f32 v77, v73\n\t"
        "v_mfma_f32_16x16x32_bf16 v[8:11], a[188:191], v[84:87], v[8:11]\n\t"
        "buffer_load_dwordx4 v140, s[24:27], s36 offen offset:0 lds\n\t"
        "v_cmp_neq_f32 s[64:65], 0, v74\n\t"
        "v_mfma_f32_16x16x32_bf16 v[12:15], a[192:195], v[84:87], v[12:15]\n\t"
        "ds_read_b128 v[96:99], v129 offset:12416\n\t"
        "v_cmp_neq_f32 s[44:45], 0, v75\n\t"
        "v_mfma_f32_16x16x32_bf16 v[16:19], a[196:199], v[84:87], v[16:19]\n\t"
        "ds_read_b64_tr_b16 a[236:237], v133 offset:12288\n\t"
        "v_sub_f32 v78, 1.0, v74\n\t"
        "v_mfma_f32_16x16x32_bf16 v[20:23], a[200:203], v[84:87], v[20:23]\n\t"
        "ds_read_b64_tr_b16 a[238:239], v133 offset:13056\n\t"
        "v_sub_f32 v79, 1.0, v75\n\t"
        "v_mfma_f32_16x16x32_bf16 v[0:3], a[204:207], v[88:91], v[0:3]\n\t"
        "buffer_load_dwordx4 v141, s[24:27], s36 offen offset:1024 lds\n\t"
        "v_cvt_pk_bf16_f32 v79, v78, v79\n\t"
        "v_mfma_f32_16x16x32_bf16 v[4:7], a[208:211], v[88:91], v[4:7]\n\t"
        "ds_read_b64_tr_b16 a[240:241], v131 offset:18432\n\t"
        "v_lshlrev_b32 v78, 16, v79\n\t"
        "v_mfma_f32_16x16x32_bf16 v[8:11], a[212:215], v[88:91], v[8:11]\n\t"
        "ds_read_b64_tr_b16 a[242:243], v131 offset:19200\n\t"
        "v_and_b32 v79, 0xffff0000, v79\n\t"
        "v_mfma_f32_16x16x32_bf16 v[12:15], a[180:183], v[88:91], v[12:15]\n\t"
        "ds_read_b64_tr_b16 a[244:245], v132 offset:18432\n\t"
        "v_mul_f32 v80, v78, v79\n\t"
        "v_mfma_f32_16x16x32_bf16 v[16:19], a[184:187], v[88:91], v[16:19]\n\t"
        "buffer_load_dwordx4 v142, s[24:27], s36 offen offset:2048 lds\n\t"
        "v_mul_f32 v78, v78, v75\n\t"
        "v_mfma_f32_16x16x32_bf16 v[20:23], a[188:191], v[88:91], v[20:23]\n\t"
        "ds_read_b64_tr_b16 a[246:247], v132 offset:19200\n\t"
        "v_mul_f32 v79, v74, v79\n\t"
        "v_mfma_f32_16x16x32_bf16 v[24:27], a[192:195], v[88:91], v[24:27]\n\t"
        "ds_read_b128 v[100:103], v130 offset:0\n\t"
        "v_mul_f32 v81, v74, v75\n\t"
        "v_mfma_f32_16x16x32_bf16 v[28:31], a[196:199], v[88:91], v[28:31]\n\t"
        "ds_read_b64_tr_b16 a[248:249], v133 offset:18432\n\t"
        "v_cndmask_b32 v78, v79, v78, s[44:45]\n\t"
        "v_mfma_f32_16x16x32_bf16 v[32:35], a[200:203], v[88:91], v[32:35]\n\t"
        "buffer_load_dwordx4 v[48:51], v55, s[20:23], s48 offen\n\t"
        "v_cndmask_b32 v79, 0, v79, s[44:45]\n\t"
        "s_waitcnt lgkmcnt(7)\n\t"
        "v_mfma_f32_16x16x32_bf16 v[12:15], a[168:171], v[92:95], v[12:15]\n\t"
        "ds_read_b128 v[104:107], v130 offset:4096\n\t"
        "v_cvt_pk_bf16_f32 v70, v80, v78\n\t"
        "v_mfma_f32_16x16x32_bf16 v[16:19], a[172:175], v[92:95], v[16:19]\n\t"
        "ds_read_b64_tr_b16 a[250:251], v133 offset:19200\n\t"
        "v_cvt_pk_bf16_f32 v71, v79, v81\n\t"
        "v_mfma_f32_16x16x32_bf16 v[20:23], a[176:179], v[92:95], v[20:23]\n\t"
        "v_pk_max_u16 v78, v62, v64\n\t"
        "v_pk_min_u16 v78, v78, v65\n\t"
        "v_mfma_f32_16x16x32_bf16 v[24:27], a[144:147], v[92:95], v[24:27]\n\t"
        "buffer_load_dword v61, v61, s[20:23], s48 offen offset:512\n\t"
        "v_readlane_b32 s40, v155, 12\n\t"
        "v_mfma_f32_16x16x32_bf16 v[28:31], a[148:151], v[92:95], v[28:31]\n\t"
        "v_readlane_b32 s43, v155, 0\n\t"
        "s_add_u32 m0, s40, s35\n\t"
        "v_mfma_f32_16x16x32_bf16 v[32:35], a[152:155], v[92:95], v[32:35]\n\t"
        "v_cmp_ne_u32 vcc, v78, v62\n\t"
        "v_sub_u32 v78, v77, v67\n\t"
        "v_mfma_f32_16x16x32_bf16 v[36:39], a[156:159], v[92:95], v[36:39]\n\t"
        "ds_read_b128 v[108:111], v130 offset:8192\n\t"
        "v_mad_u32_u24 v79, v77, 6, v66\n\t"
        "v_mfma_f32_16x16x32_bf16 v[40:43], a[160:163], v[92:95], v[40:43]\n\t"
        "buffer_load_dwordx4 v134, s[16:19], s43 offen  lds\n\t"
        "v_readlane_b32 s40, v155, 13\n\t"
        "v_mfma_f32_16x16x32_bf16 v[44:47], a[164:167], v[92:95], v[44:47]\n\t"
        "v_readlane_b32 s43, v155, 1\n\t"
        "s_add_u32 m0, s40, s35\n\t"
        "v_mfma_f32_16x16x32_bf16 v[12:15], a[204:207], v[96:99], v[12:15]\n\t"
        "v_mad_u32_u24 v79, v76, s62, v79\n\t"
        "s_and_b64 vcc, vcc, s[66:67]\n\t"
        "v_mfma_f32_16x16x32_bf16 v[16:19], a[208:211], v[96:99], v[16:19]\n\t"
        "s_waitcnt vmcnt(6) lgkmcnt(1)\n\t"
        "s_barrier\n\t"
        "v_mfma_f32_16x16x32_bf16 v[20:23], a[212:215], v[96:99], v[20:23]\n\t"
        "buffer_load_dwordx4 v135, s[16:19], s43 offen  lds\n\t"
        "v_readlane_b32 s40, v155, 14\n\t"
        "v_mfma_f32_16x16x32_bf16 v[24:27], a[180:183], v[96:99], v[24:27]\n\t"
        "ds_read_b64_tr_b16 a[252:253], v131 offset:24576\n\t"
        "v_readlane_b32 s43, v155, 2\n\t"
        "v_mfma_f32_16x16x32_bf16 v[28:31], a[184:187], v[96:99], v[28:31]\n\t"
        "ds_read_b64_tr_b16 a[254:255], v131 offset:25344\n\t"
        "s_add_u32 m0, s40, s35\n\t"
        "v_mfma_f32_16x16x32_bf16 v[32:35], a[188:191], v[96:99], v[32:35]\n\t"
        "ds_read_b64_tr_b16 v[188:189], v132 offset:24576\n\t"
        "s_or_b64 s[56:57], s[56:57], vcc\n\t"
        "v_mfma_f32_16x16x32_bf16 v[36:39], a[192:195], v[96:99], v[36:39]\n\t"
        "buffer_load_dwordx4 v134, s[16:19], s43 offen  lds\n\t"
        "s_bitcmp1_b32 s42, 10\n\t"
        "s_cbranch_scc0 .Lx56%=\n\t"
        "v_readlane_b32 s40, v155, 20\n\t"
        "v_readlane_b32 s43, v155, 8\n\t"
        "s_nop 0\n\t"
        "s_add_u32 m0, s40, s35\n\t"
        "s_nop 2\n\t"
        "buffer_load_dwordx4 v136, s[16:19], s43 offen  lds\n\t"
        ".Lx56%=:\n\t"
        "v_mfma_f32_16x16x32_bf16 v[40:43], a[196:199], v[96:99], v[40:43]\n\t"
        "ds_read_b128 v[112:115], v130 offset:12288\n\t"
        "v_readlane_b32 s40, v155, 15\n\t"
        "v_mfma_f32_16x16x32_bf16 v[44:47], a[200:203], v[96:99], v[44:47]\n\t"
        "ds_read_b64_tr_b16 v[190:191], v132 offset:25344\n\t"
        "v_readlane_b32 s43, v155, 3\n\t"
        "v_mfma_f32_16x16x32_bf16 v[0:3], a[228:231], v[100:103], v[0:3]\n\t"
        "ds_read_b64_tr_b16 v[192:193], v133 offset:24576\n\t"
        "s_add_u32 m0, s40, s35\n\t"
        "v_mfma_f32_16x16x32_bf16 v[4:7], a[232:235], v[100:103], v[4:7]\n\t"
        "s_nop 0\n\t"
        "buffer_load_dwordx4 v135, s[16:19], s43 offen  lds\n\t"
        "v_readlane_b32 s40, v155, 16\n\t"
        "v_mfma_f32_16x16x32_bf16 v[8:11], a[236:239], v[100:103], v[8:11]\n\t"
        "ds_read_b64_tr_b16 v[194:195], v133 offset:25344\n\t"
        "v_readlane_b32 s43, v155, 4\n\t"
        "v_mfma_f32_16x16x32_bf16 v[0:3], a[216:219], v[104:107], v[0:3]\n\t"
        "ds_read_b64_tr_b16 v[196:197], v131 offset:30720\n\t"
        "s_add_u32 m0, s40, s35\n\t"
        "v_mfma_f32_16x16x32_bf16 v[4:7], a[220:223], v[104:107], v[4:7]\n\t"
        "ds_read_b64_tr_b16 v[198:199], v131 offset:31488\n\t"
        "v_bfe_u32 v80, v78, 2, 1\n\t"
        "v_mfma_f32_16x16x32_bf16 v[8:11], a[224:227], v[104:107], v[8:11]\n\t"
        "buffer_load_dwordx4 v134, s[16:19], s43 offen  lds\n\t"
        "v_readlane_b32 s40, v155, 17\n\t"
        "v_mfma_f32_16x16x32_bf16 v[12:15], a[228:231], v[104:107], v[12:15]\n\t"
        "ds_read_b128 v[84:87], v129 offset:16384\n\t"
        "v_readlane_b32 s43, v155, 5\n\t"
        "v_mfma_f32_16x16x32_bf16 v[16:19], a[232:235], v[104:107], v[16:19]\n\t"
        "ds_read_b64_tr_b16 v[200:201], v132 offset:30720\n\t"
        "s_add_u32 m0, s40, s35\n\t"
        "v_mfma_f32_16x16x32_bf16 v[20:23], a[236:239], v[104:107], v[20:23]\n\t"
        "ds_read_b64_tr_b16 v[202:203], v132 offset:31488\n\t"
        "v_or_b32 v80, v80, v79\n\t"
        "s_waitcnt lgkmcnt(8)\n\t"
        "v_mfma_f32_16x16x32_bf16 v[0:3], a[240:243], v[108:111], v[0:3]\n\t"
        "buffer_load_dwordx4 v135, s[16:19], s43 offen  lds\n\t"
        "v_readlane_b32 s40, v155, 18\n\t"
        "v_mfma_f32_16x16x32_bf16 v[4:7], a[244:247], v[108:111], v[4:7]\n\t"
        "ds_read_b64_tr_b16 v[204:205], v133 offset:30720\n\t"
        "v_readlane_b32 s43, v155, 6\n\t"
        "v_mfma_f32_16x16x32_bf16 v[8:11], a[248:251], v[108:111], v[8:11]\n\t"
        "ds_read_b64_tr_b16 v[206:207], v133 offset:31488\n\t"
        "s_add_u32 m0, s40, s35\n\t"
        "v_mfma_f32_16x16x32_bf16 v[12:15], a[216:219], v[108:111], v[12:15]\n\t"
        "ds_read_b128 v[88:91], v130 offset:64\n\t"
        "v_add_u32 v78, 1, v78\n\t"
        "v_mfma_f32_16x16x32_bf16 v[16:19], a[220:223], v[108:111], v[16:19]\n\t"
        "buffer_load_dwordx4 v136, s[16:19], s43 offen  lds\n\t"
        "v_readlane_b32 s40, v155, 19\n\t"
        "v_mfma_f32_16x16x32_bf16 v[20:23], a[224:227], v[108:111], v[20:23]\n\t"
        "ds_read_b64_tr_b16 v[208:209], v131 offset:36864\n\t"
        "v_readlane_b32 s43, v155, 7\n\t"
        "v_mfma_f32_16x16x32_bf16 v[24:27], a[228:231], v[108:111], v[24:27]\n\t"
        "ds_read_b128 v[92:95], v130 offset:4160\n\t"
        "s_add_u32 m0, s40, s35\n\t"
        "v_mfma_f32_16x16x32_bf16 v[28:31], a[232:235], v[108:111], v[28:31]\n\t"
        "ds_read_b64_tr_b16 v[210:211], v131 offset:37632\n\t"
        "v_bfe_u32 v78, v78, 2, 1\n\t"
        "v_mfma_f32_16x16x32_bf16 v[32:35], a[236:239], v[108:111], v[32:35]\n\t"
        "buffer_load_dwordx4 v137, s[16:19], s43 offen  lds\n\t"
        "v_add3_u32 v78, v79, v78, 6\n\t"
        "v_mfma_f32_16x16x32_bf16 v[12:15], a[240:243], v[112:115], v[12:15]\n\t"
        "ds_read_b64_tr_b16 v[212:213], v132 offset:36864\n\t"
        "v_cndmask_b32 v78, v80, v78, s[44:45]\n\t"
        "v_mfma_f32_16x16x32_bf16 v[16:19], a[244:247], v[112:115], v[16:19]\n\t"
        "ds_read_b64_tr_b16 v[214:215], v132 offset:37632\n\t"
        "v_cndmask_b32 v79, 0, v119, s[64:65]\n\t"
        "v_mfma_f32_16x16x32_bf16 v[20:23], a[248:251], v[112:115], v[20:23]\n\t"
        "s_waitcnt vmcnt(10) lgkmcnt(6)\n\t"
        "s_barrier\n\t"
        "v_mfma_f32_16x16x32_bf16 v[24:27], a[216:219], v[112:115], v[24:27]\n\t"
        "ds_read_b128 v[96:99], v129 offset:16448\n\t"
        "v_add_u32 v81, v80, v79\n\t"
        "v_mfma_f32_16x16x32_bf16 v[28:31], a[220:223], v[112:115], v[28:31]\n\t"
        "ds_read_b64_tr_b16 v[216:217], v133 offset:36864\n\t"
        "v_add_u32 v82, v78, v79\n\t"
        "v_mfma_f32_16x16x32_bf16 v[32:35], a[224:227], v[112:115], v[32:35]\n\t"
        "ds_read_b64_tr_b16 v[218:219], v133 offset:37632\n\t"
        "v_cndmask_b32 v78, v81, v78, s[44:45]\n\t"
        "v_mfma_f32_16x16x32_bf16 v[36:39], a[228:231], v[112:115], v[36:39]\n\t"
        "ds_read_b64_tr_b16 v[220:221], v131 offset:43008\n\t"
        "v_lshl_or_b32 v68, v78, 16, v80\n\t"
        "v_mfma_f32_16x16x32_bf16 v[40:43], a[232:235], v[112:115], v[40:43]\n\t"
        "ds_read_b64_tr_b16 v[222:223], v131 offset:43776\n\t"
        "v_lshl_or_b32 v69, v82, 16, v81\n\t"
        "v_mfma_f32_16x16x32_bf16 v[44:47], a[236:239], v[112:115], v[44:47]\n\t"
        "ds_write_b128 v118, v[68:71]\n\t"
        "v_mfma_f32_16x16x32_bf16 v[24:27], a[132:135], v[84:87], v[24:27]\n\t"
        "ds_read_b128 v[100:103], v130 offset:8256\n\t"
        "v_mfma_f32_16x16x32_bf16 v[28:31], a[136:139], v[84:87], v[28:31]\n\t"
        "ds_read_b64_tr_b16 v[224:225], v132 offset:43008\n\t"
        "v_mfma_f32_16x16x32_bf16 v[32:35], a[140:143], v[84:87], v[32:35]\n\t"
        "ds_read_b64_tr_b16 v[226:227], v132 offset:43776\n\t"
        "v_mfma_f32_16x16x32_bf16 v[36:39], a[108:111], v[84:87], v[36:39]\n\t"
        "ds_read_b64_tr_b16 v[228:229], v133 offset:43008\n\t"
        "v_mfma_f32_16x16x32_bf16 v[40:43], a[112:115], v[84:87], v[40:43]\n\t"
        "ds_read_b64_tr_b16 v[230:231], v133 offset:43776\n\t"
        "v_mfma_f32_16x16x32_bf16 v[44:47], a[116:119], v[84:87], v[44:47]\n\t"
        "ds_read_b64_tr_b16 v[232:233], v131 offset:49152\n\t"
        "s_waitcnt lgkmcnt(9)\n\t"
        "v_mfma_f32_16x16x32_bf16 v[0:3], v[196:199], v[88:91], v[0:3]\n\t"
        "ds_read_b64_tr_b16 v[234:235], v131 offset:49920\n\t"
        "v_mfma_f32_16x16x32_bf16 v[4:7], v[200:203], v[88:91], v[4:7]\n\t"
        "ds_read_b64_tr_b16 v[236:237], v132 offset:49152\n\t"
        "v_mfma_f32_16x16x32_bf16 v[8:11], v[204:207], v[88:91], v[8:11]\n\t"
        "ds_read_b64_tr_b16 v[238:239], v132 offset:49920\n\t"
        "v_mfma_f32_16x16x32_bf16 v[0:3], a[252:255], v[92:95], v[0:3]\n\t"
        "ds_read_b128 v[104:107], v130 offset:12352\n\t"
        "v_mfma_f32_16x16x32_bf16 v[4:7], v[188:191], v[92:95], v[4:7]\n\t"
        "ds_read_b64_tr_b16 v[240:241], v133 offset:49152\n\t"
        "v_mfma_f32_16x16x32_bf16 v[8:11], v[192:195], v[92:95], v[8:11]\n\t"
        "ds_read_b64_tr_b16 v[242:243], v133 offset:49920\n\t"
        "v_mfma_f32_16x16x32_bf16 v[12:15], v[196:199], v[92:95], v[12:15]\n\t"
        "ds_read_b64_tr_b16 v[244:245], v131 offset:55296\n\t"
        "v_mfma_f32_16x16x32_bf16 v[16:19], v[200:203], v[92:95], v[16:19]\n\t"
        "ds_read_b64_tr_b16 v[246:247], v131 offset:56064\n\t"
        "v_mfma_f32_16x16x32_bf16 v[20:23], v[204:207], v[92:95], v[20:23]\n\t"
        "ds_read_b64_tr_b16 v[248:249], v132 offset:55296\n\t"
        "v_mfma_f32_16x16x32_bf16 v[24:27], a[168:171], v[96:99], v[24:27]\n\t"
        "ds_read_b64_tr_b16 v[250:251], v132 offset:56064\n\t"
        "v_mfma_f32_16x16x32_bf16 v[28:31], a[172:175], v[96:99], v[28:31]\n\t"
        "ds_read_b64_tr_b16 v[252:253], v133 offset:55296\n\t"
        "v_mfma_f32_16x16x32_bf16 v[32:35], a[176:179], v[96:99], v[32:35]\n\t"
        "ds_read_b64_tr_b16 v[254:255], v133 offset:56064\n\t"
        "v_mfma_f32_16x16x32_bf16 v[36:39], a[144:147], v[96:99], v[36:39]\n\t"
        "ds_read_b128 v[108:111], v130 offset:128\n\t"
        "v_mfma_f32_16x16x32_bf16 v[40:43], a[148:151], v[96:99], v[40:43]\n\t"
        "v_mfma_f32_16x16x32_bf16 v[44:47], a[152:155], v[96:99], v[44:47]\n\t"
        "s_waitcnt lgkmcnt(8)\n\t"
        "v_mfma_f32_16x16x32_bf16 v[0:3], v[208:211], v[100:103], v[0:3]\n\t"
        "ds_read_b128 v[112:115], v130 offset:4224\n\t"
        "v_mfma_f32_16x16x32_bf16 v[4:7], v[212:215], v[100:103], v[4:7]\n\t"
        "v_mfma_f32_16x16x32_bf16 v[8:11], v[216:219], v[100:103], v[8:11]\n\t"
        "v_mfma_f32_16x16x32_bf16 v[12:15], a[252:255], v[100:103], v[12:15]\n\t"
        "v_mfma_f32_16x16x32_bf16 v[16:19], v[188:191], v[100:103], v[16:19]\n\t"
        "v_mfma_f32_16x16x32_bf16 v[20:23], v[192:195], v[100:103], v[20:23]\n\t"
        "v_mfma_f32_16x16x32_bf16 v[24:27], v[196:199], v[100:103], v[24:27]\n\t"
        "ds_read_b128 v[84:87], v130 offset:8320\n\t"
        "s_waitcnt vmcnt(9)\n\t"
        "v_and_b32 v48, s54, v48\n\t"
        "v_mfma_f32_16x16x32_bf16 v[28:31], v[200:203], v[100:103], v[28:31]\n\t"
        "v_pk_max_u16 v60, v154, v48\n\t"
        "v_and_b32 v49, s54, v49\n\t"
        "v_mfma_f32_16x16x32_bf16 v[32:35], v[204:207], v[100:103], v[32:35]\n\t"
        "v_pk_max_u16 v60, v60, v49\n\t"
        "v_and_b32 v50, s54, v50\n\t"
        "v_mfma_f32_16x16x32_bf16 v[12:15], v[208:211], v[104:107], v[12:15]\n\t"
        "v_pk_max_u16 v60, v60, v50\n\t"
        "v_and_b32 v51, s54, v51\n\t"
        "v_mfma_f32_16x16x32_bf16 v[16:19], v[212:215], v[104:107], v[16:19]\n\t"
        "v_pk_max_u16 v60, v60, v51\n\t"
        "s_waitcnt vmcnt(8)\n\t"
        "v_and_b32 v61, s54, v61\n\t"
        "v_mfma_f32_16x16x32_bf16 v[20:23], v[216:219], v[104:107], v[20:23]\n\t"
        "v_pk_max_u16 v60, v60, v61\n\t"
        "v_cmp_ne_u32 s[12:13], v60, v154\n\t"
        "v_mfma_f32_16x16x32_bf16 v[24:27], a[252:255], v[104:107], v[24:27]\n\t"
        "s_lshl_b32 s6, 256, s4\n\t"
        "v_mfma_f32_16x16x32_bf16 v[28:31], v[188:191], v[104:107], v[28:31]\n\t"
        "s_cmp_lg_u64 s[12:13], 0\n\t"
        "s_cselect_b32 s6, s6, 0\n\t"
        "v_mfma_f32_16x16x32_bf16 v[32:35], v[192:195], v[104:107], v[32:35]\n\t"
        "s_cmp_gt_u32 s3, 16\n\t"
        "s_cselect_b32 s8, 2, 0\n\t"
        "v_mfma_f32_16x16x32_bf16 v[36:39], v[196:199], v[104:107], v[36:39]\n\t"
        "s_waitcnt vmcnt(0) lgkmcnt(1)\n\t"
        "s_barrier\n\t"
        "v_mfma_f32_16x16x32_bf16 v[40:43], v[200:203], v[104:107], v[40:43]\n\t"
        "s_cmp_lg_u64 s[56:57], 0\n\t"
        "s_addc_u32 s8, s8, s6\n\t"
        "v_mfma_f32_16x16x32_bf16 v[44:47], v[204:207], v[104:107], v[44:47]\n\t"
        "ds_read_b128 v[160:163], v185 offset:0\n\t"
        "v_mov_b32 v151, s8\n\t"
        "v_mfma_f32_16x16x32_bf16 v[0:3], v[232:235], v[108:111], v[0:3]\n\t"
        "s_mov_b64 exec, 1\n\t"
        "ds_write_b32 v153, v151\n\t"
        "s_mov_b64 exec, -1\n\t"
        "v_mfma_f32_16x16x32_bf16 v[4:7], v[236:239], v[108:111], v[4:7]\n\t"
        "ds_read_b128 v[88:91], v130 offset:12416\n\t"
        "v_xor_b32 v154, 32, v186\n\t"
        "v_mfma_f32_16x16x32_bf16 v[8:11], v[240:243], v[108:111], v[8:11]\n\t"
        "v_mfma_f32_16x16x32_bf16 v[0:3], v[220:223], v[112:115], v[0:3]\n\t"
        "v_mfma_f32_16x16x32_bf16 v[4:7], v[224:227], v[112:115], v[4:7]\n\t"
        "v_mfma_f32_16x16x32_bf16 v[8:11], v[228:231], v[112:115], v[8:11]\n\t"
        "v_mfma_f32_16x16x32_bf16 v[12:15], v[232:235], v[112:115], v[12:15]\n\t"
        "ds_read_b128 v[92:95], v129 offset:16512\n\t"
        "v_mfma_f32_16x16x32_bf16 v[16:19], v[236:239], v[112:115], v[16:19]\n\t"
        "v_mfma_f32_16x16x32_bf16 v[20:23], v[240:243], v[112:115], v[20:23]\n\t"
        "s_waitcnt lgkmcnt(3)\n\t"
        "v_lshlrev_b32_sdwa v157, 5, v160 dst_sel:DWORD dst_unused:UNUSED_PAD src0_sel:DWORD src1_sel:WORD_0\n\t"
        "v_lshlrev_b32_sdwa v158, 5, v160 dst_sel:DWORD dst_unused:UNUSED_PAD src0_sel:DWORD src1_sel:WORD_1\n\t"
        "v_mfma_f32_16x16x32_bf16 v[0:3], v[244:247], v[84:87], v[0:3]\n\t"
        "v_or_b32 v157, v184, v157\n\t"
        "v_or_b32 v158, v184, v158\n\t"
        "v_mfma_f32_16x16x32_bf16 v[4:7], v[248:251], v[84:87], v[4:7]\n\t"
        "ds_read_b64_tr_b16 v[60:61], v157 offset:0\n\t"
        "v_xor_b32 v159, 32, v157\n\t"
        "v_mfma_f32_16x16x32_bf16 v[8:11], v[252:255], v[84:87], v[8:11]\n\t"
        "ds_read_b64_tr_b16 v[62:63], v158 offset:0\n\t"
        "v_xor_b32 v152, 32, v158\n\t"
        "v_mfma_f32_16x16x32_bf16 v[12:15], v[220:223], v[84:87], v[12:15]\n\t"
        "ds_read_b64_tr_b16 v[64:65], v159 offset:0\n\t"
        "v_perm_b32 v164, v162, v162, v168\n\t"
        "v_mfma_f32_16x16x32_bf16 v[16:19], v[224:227], v[84:87], v[16:19]\n\t"
        "ds_read_b64_tr_b16 v[66:67], v152 offset:0\n\t"
        "v_perm_b32 v165, v162, v162, v169\n\t"
        "v_mfma_f32_16x16x32_bf16 v[20:23], v[228:231], v[84:87], v[20:23]\n\t"
        "ds_read_b64_tr_b16 v[68:69], v157 offset:64\n\t"
        "v_perm_b32 v166, v162, v162, v170\n\t"
        "v_mfma_f32_16x16x32_bf16 v[24:27], v[232:235], v[84:87], v[24:27]\n\t"
        "ds_read_b64_tr_b16 v[70:71], v158 offset:64\n\t"
        "v_perm_b32 v167, v162, v162, v171\n\t"
        "v_mfma_f32_16x16x32_bf16 v[28:31], v[236:239], v[84:87], v[28:31]\n\t"
        "ds_read_b64_tr_b16 v[72:73], v159 offset:64\n\t"
        "v_cmp_ne_u32 vcc, 0, v163\n\t"
        "v_mfma_f32_16x16x32_bf16 v[32:35], v[240:243], v[84:87], v[32:35]\n\t"
        "ds_read_b128 v[96:99], v130 offset:16384\n\t"
        "s_waitcnt lgkmcnt(8)\n\t"
        "v_mfma_f32_16x16x32_bf16 v[12:15], v[244:247], v[88:91], v[12:15]\n\t"
        "ds_read_b64_tr_b16 v[74:75], v152 offset:64\n\t"
        "v_mfma_f32_16x16x32_bf16 v[16:19], v[248:251], v[88:91], v[16:19]\n\t"
        "ds_read_b64_tr_b16 v[76:77], v157 offset:128\n\t"
        "v_mfma_f32_16x16x32_bf16 v[20:23], v[252:255], v[88:91], v[20:23]\n\t"
        "ds_read_b64_tr_b16 v[78:79], v158 offset:128\n\t"
        ".LpS1%=:\n\t"
        "s_cbranch_execz .LrS%=\n\t"
        "s_waitcnt lgkmcnt(9)\n\t"
        "v_mfma_f32_16x16x32_bf16 v[48:51], v[60:63], v[164:167], 0\n\t"
        "ds_read_b64_tr_b16 v[80:81], v159 offset:128\n\t"
        "v_mfma_f32_16x16x32_bf16 v[24:27], v[220:223], v[88:91], v[24:27]\n\t"
        "ds_read_b64_tr_b16 v[82:83], v152 offset:128\n\t"
        "s_waitcnt lgkmcnt(9)\n\t"
        "v_mfma_f32_16x16x32_bf16 v[52:55], v[64:67], v[164:167], 0\n\t"
        "ds_read_b128 v[100:103], v130 offset:16448\n\t"
        "v_mfma_f32_16x16x32_bf16 v[28:31], v[224:227], v[88:91], v[28:31]\n\t"
        "s_waitcnt lgkmcnt(8)\n\t"
        "v_mfma_f32_16x16x32_bf16 v[56:59], v[68:71], v[164:167], 0\n\t"
        "v_mfma_f32_16x16x32_bf16 v[32:35], v[228:231], v[88:91], v[32:35]\n\t"
        "v_mfma_f32_16x16x32_bf16 v[36:39], v[232:235], v[88:91], v[36:39]\n\t"
        "ds_read_b128 v[104:107], v130 offset:16512\n\t"
        "s_waitcnt lgkmcnt(6)\n\t"
        "v_mfma_f32_16x16x32_bf16 v[116:119], v[72:75], v[164:167], 0\n\t"
        "v_mfma_f32_16x16x32_bf16 v[40:43], v[236:239], v[88:91], v[40:43]\n\t"
        "s_waitcnt lgkmcnt(4)\n\t"
        "v_mfma_f32_16x16x32_bf16 v[120:123], v[76:79], v[164:167], 0\n\t"
        "v_mfma_f32_16x16x32_bf16 v[44:47], v[240:243], v[88:91], v[44:47]\n\t"
        "s_waitcnt lgkmcnt(2)\n\t"
        "v_mfma_f32_16x16x32_bf16 v[124:127], v[80:83], v[164:167], 0\n\t"
        "s_cbranch_vccz .Lx58%=\n\t"
        "v_lshlrev_b32_sdwa v157, 5, v161 dst_sel:DWORD dst_unused:UNUSED_PAD src0_sel:DWORD src1_sel:WORD_0\n\t"
        "v_lshlrev_b32_sdwa v158, 5, v161 dst_sel:DWORD dst_unused:UNUSED_PAD src0_sel:DWORD src1_sel:WORD_1\n\t"
        "v_or_b32 v157, v184, v157\n\t"
        "v_or_b32 v158, v184, v158\n\t"
        "v_xor_b32 v159, 32, v157\n\t"
        "v_xor_b32 v152, 32, v158\n\t"
        "ds_read_b64_tr_b16 v[60:61], v157 offset:0\n\t"
        "ds_read_b64_tr_b16 v[62:63], v158 offset:0\n\t"
        "ds_read_b64_tr_b16 v[64:65], v159 offset:0\n\t"
        "ds_read_b64_tr_b16 v[66:67], v152 offset:0\n\t"
        "ds_read_b64_tr_b16 v[68:69], v157 offset:64\n\t"
        "ds_read_b64_tr_b16 v[70:71], v158 offset:64\n\t"
        "ds_read_b64_tr_b16 v[72:73], v159 offset:64\n\t"
        "ds_read_b64_tr_b16 v[74:75], v152 offset:64\n\t"
        "ds_read_b64_tr_b16 v[76:77], v157 offset:128\n\t"
        "ds_read_b64_tr_b16 v[78:79], v158 offset:128\n\t"
        "ds_read_b64_tr_b16 v[80:81], v159 offset:128\n\t"
        "ds_read_b64_tr_b16 v[82:83], v152 offset:128\n\t"
        "v_perm_b32 v164, v163, v163, v168\n\t"
        "v_perm_b32 v165, v163, v163, v169\n\t"
        "v_perm_b32 v166, v163, v163, v170\n\t"
        "v_perm_b32 v167, v163, v163, v171\n\t"
        "s_waitcnt lgkmcnt(10)\n\t"
        "s_nop 0\n\t"
        "v_mfma_f32_16x16x32_bf16 v[48:51], v[60:63], v[164:167], v[48:51]\n\t"
        "s_waitcnt lgkmcnt(8)\n\t"
        "v_mfma_f32_16x16x32_bf16 v[52:55], v[64:67], v[164:167], v[52:55]\n\t"
        "s_waitcnt lgkmcnt(6)\n\t"
        "v_mfma_f32_16x16x32_bf16 v[56:59], v[68:71], v[164:167], v[56:59]\n\t"
        "s_waitcnt lgkmcnt(4)\n\t"
        "v_mfma_f32_16x16x32_bf16 v[116:119], v[72:75], v[164:167], v[116:119]\n\t"
        "s_waitcnt lgkmcnt(2)\n\t"
        "v_mfma_f32_16x16x32_bf16 v[120:123], v[76:79], v[164:167], v[120:123]\n\t"
        "s_waitcnt lgkmcnt(0)\n\t"
        "v_mfma_f32_16x16x32_bf16 v[124:127], v[80:83], v[164:167], v[124:127]\n\t"
        "s_nop 4\n\t"
        ".Lx58%=:\n\t"
        "v_cvt_pk_bf16_f32 v48, v48, v49\n\t"
        "v_cvt_pk_bf16_f32 v49, v50, v51\n\t"
        "v_mfma_f32_16x16x32_bf16 v[24:27], a[204:207], v[92:95], v[24:27]\n\t"
        "ds_write_b64 v186, v[48:49] offset:0\n\t"
        "v_mfma_f32_16x16x32_bf16 v[28:31], a[208:211], v[92:95], v[28:31]\n\t"
        "v_cvt_pk_bf16_f32 v52, v52, v53\n\t"
        "v_cvt_pk_bf16_f32 v53, v54, v55\n\t"
        "v_cvt_pk_bf16_f32 v56, v56, v57\n\t"
        "v_mfma_f32_16x16x32_bf16 v[32:35], a[212:215], v[92:95], v[32:35]\n\t"
        "ds_write_b64 v154, v[52:53] offset:0\n\t"
        "v_mfma_f32_16x16x32_bf16 v[36:39], a[180:183], v[92:95], v[36:39]\n\t"
        "ds_read_b128 v[108:111], v129 offset:20480\n\t"
        "v_cvt_pk_bf16_f32 v57, v58, v59\n\t"
        "v_cvt_pk_bf16_f32 v116, v116, v117\n\t"
        "v_mfma_f32_16x16x32_bf16 v[40:43], a[184:187], v[92:95], v[40:43]\n\t"
        "ds_write_b64 v186, v[56:57] offset:64\n\t"
        "v_mfma_f32_16x16x32_bf16 v[44:47], a[188:191], v[92:95], v[44:47]\n\t"
        "v_cvt_pk_bf16_f32 v117, v118, v119\n\t"
        "v_cvt_pk_bf16_f32 v120, v120, v121\n\t"
        "v_cvt_pk_bf16_f32 v121, v122, v123\n\t"
        "v_mfma_f32_16x16x32_bf16 v[24:27], a[240:243], v[96:99], v[24:27]\n\t"
        "ds_write_b64 v154, v[116:117] offset:64\n\t"
        "v_mfma_f32_16x16x32_bf16 v[28:31], a[244:247], v[96:99], v[28:31]\n\t"
        "ds_write_b64 v186, v[120:121] offset:128\n\t"
        "v_mfma_f32_16x16x32_bf16 v[32:35], a[248:251], v[96:99], v[32:35]\n\t"
        "ds_read_b128 v[112:115], v129 offset:20544\n\t"
        "v_cvt_pk_bf16_f32 v124, v124, v125\n\t"
        "v_cvt_pk_bf16_f32 v125, v126, v127\n\t"
        "v_mfma_f32_16x16x32_bf16 v[36:39], a[216:219], v[96:99], v[36:39]\n\t"
        "ds_write_b64 v154, v[124:125] offset:128\n\t"
        "v_mfma_f32_16x16x32_bf16 v[40:43], a[220:223], v[96:99], v[40:43]\n\t"
        "ds_read_b128 v[84:87], v129 offset:20608\n\t"
        "v_mfma_f32_16x16x32_bf16 v[44:47], a[224:227], v[96:99], v[44:47]\n\t"
        "s_waitcnt lgkmcnt(6)\n\t"
        "v_mfma_f32_16x16x32_bf16 v[24:27], v[208:211], v[100:103], v[24:27]\n\t"
        "ds_read_b128 v[88:91], v130 offset:20480\n\t"
        "v_mfma_f32_16x16x32_bf16 v[28:31], v[212:215], v[100:103], v[28:31]\n\t"
        "v_mfma_f32_16x16x32_bf16 v[32:35], v[216:219], v[100:103], v[32:35]\n\t"
        "v_mfma_f32_16x16x32_bf16 v[36:39], a[252:255], v[100:103], v[36:39]\n\t"
        "s_waitcnt lgkmcnt(2)\n\t"
        "s_barrier\n\t"
        "v_mfma_f32_16x16x32_bf16 v[40:43], v[188:191], v[100:103], v[40:43]\n\t"
        "ds_read_b128 v[124:127], v182 offset:0\n\t"
        "v_mfma_f32_16x16x32_bf16 v[44:47], v[192:195], v[100:103], v[44:47]\n\t"
        "ds_read_b128 v[132:135], v187 offset:0\n\t"
        "v_mfma_f32_16x16x32_bf16 v[24:27], v[244:247], v[104:107], v[24:27]\n\t"
        "ds_read_b128 v[136:139], v187 offset:16\n\t"
        "v_mfma_f32_16x16x32_bf16 v[28:31], v[248:251], v[104:107], v[28:31]\n\t"
        "ds_read_b32 v140, v187 offset:32\n\t"
        "v_mfma_f32_16x16x32_bf16 v[32:35], v[252:255], v[104:107], v[32:35]\n\t"
        "ds_read_b128 v[92:95], v130 offset:20544\n\t"
        "v_mfma_f32_16x16x32_bf16 v[36:39], v[220:223], v[104:107], v[36:39]\n\t"
        "ds_read_b128 v[96:99], v130 offset:20608\n\t"
        "v_mfma_f32_16x16x32_bf16 v[40:43], v[224:227], v[104:107], v[40:43]\n\t"
        "v_mfma_f32_16x16x32_bf16 v[44:47], v[228:231], v[104:107], v[44:47]\n\t"
        "v_mfma_f32_16x16x32_bf16 v[36:39], a[132:135], v[108:111], v[36:39]\n\t"
        "v_mfma_f32_16x16x32_bf16 v[40:43], a[136:139], v[108:111], v[40:43]\n\t"
        "s_waitcnt lgkmcnt(5)\n\t"
        "v_or3_b32 v124, v124, v125, v126\n\t"
        "v_or_b32 v124, v124, v127\n\t"
        "v_mfma_f32_16x16x32_bf16 v[44:47], a[140:143], v[108:111], v[44:47]\n\t"
        "v_readfirstlane_b32 s6, v124\n\t"
        "v_mfma_f32_16x16x32_bf16 v[36:39], a[168:171], v[112:115], v[36:39]\n\t"
        "s_nop 0\n\t"
        "s_bitcmp1_b32 s6, 1\n\t"
        "s_cbranch_scc1 .Lx60%=\n\t"
        ".Lx61%=:\n\t"
        "v_mfma_f32_16x16x32_bf16 v[40:43], a[172:175], v[112:115], v[40:43]\n\t"
        "s_waitcnt lgkmcnt(3)\n\t"
        "v_xor_b32 v132, v181, v132\n\t"
        "v_add_u32 v151, s9, v172\n\t"
        "v_mfma_f32_16x16x32_bf16 v[44:47], a[176:179], v[112:115], v[44:47]\n\t"
        "v_max_u32 v151, v151, v132\n\t"
        "v_xor_b32 v133, v181, v133\n\t"
        "v_add_u32 v183, s9, v173\n\t"
        "v_max_u32 v183, v183, v133\n\t"
        "v_mfma_f32_16x16x32_bf16 v[36:39], a[204:207], v[84:87], v[36:39]\n\t"
        "ds_read_b128 v[100:103], v151 offset:0\n\t"
        "v_xor_b32 v134, v181, v134\n\t"
        "v_mfma_f32_16x16x32_bf16 v[40:43], a[208:211], v[84:87], v[40:43]\n\t"
        "ds_read_b128 v[104:107], v151 offset:64\n\t"
        "s_add_u32 s55, s41, 0\n\t"
        "v_mfma_f32_16x16x32_bf16 v[44:47], a[212:215], v[84:87], v[44:47]\n\t"
        "ds_read_b128 v[108:111], v151 offset:128\n\t"
        "v_add_u32 v151, s9, v174\n\t"
        "v_mfma_f32_16x16x32_bf16 v[36:39], a[240:243], v[88:91], v[36:39]\n\t"
        "ds_read_b128 v[112:115], v183 offset:0\n\t"
        "v_max_u32 v151, v151, v134\n\t"
        "v_mfma_f32_16x16x32_bf16 v[40:43], a[244:247], v[88:91], v[40:43]\n\t"
        "ds_read_b128 v[84:87], v183 offset:64\n\t"
        "v_xor_b32 v135, v181, v135\n\t"
        "v_mfma_f32_16x16x32_bf16 v[44:47], a[248:251], v[88:91], v[44:47]\n\t"
        "v_cvt_pk_bf16_f32 v116, v0, v1\n\t"
        "v_cvt_pk_bf16_f32 v117, v2, v3\n\t"
        "s_waitcnt lgkmcnt(5)\n\t"
        "v_mfma_f32_16x16x32_bf16 v[36:39], v[208:211], v[92:95], v[36:39]\n\t"
        "ds_read_b128 v[88:91], v183 offset:128\n\t"
        "v_add_u32 v183, s9, v175\n\t"
        "v_mfma_f32_16x16x32_bf16 v[40:43], v[212:215], v[92:95], v[40:43]\n\t"
        "v_max_u32 v183, v183, v135\n\t"
        "v_xor_b32 v136, v181, v136\n\t"
        "v_mfma_f32_16x16x32_bf16 v[44:47], v[216:219], v[92:95], v[44:47]\n\t"
        "ds_read_b128 v[132:135], v187 offset:2048\n\t"
        "v_cvt_pk_bf16_f32 v118, v4, v5\n\t"
        "v_mfma_f32_16x16x32_bf16 v[36:39], v[244:247], v[96:99], v[36:39]\n\t"
        "ds_read_b128 v[92:95], v151 offset:0\n\t"
        "v_cvt_pk_bf16_f32 v119, v6, v7\n\t"
        "v_mfma_f32_16x16x32_bf16 v[40:43], v[248:251], v[96:99], v[40:43]\n\t"
        "v_cvt_pk_bf16_f32 v120, v8, v9\n\t"
        "v_cvt_pk_bf16_f32 v121, v10, v11\n\t"
        "v_mfma_f32_16x16x32_bf16 v[44:47], v[252:255], v[96:99], v[44:47]\n\t"
        "v_permlane16_swap_b32 v116, v118\n\t"
        "s_waitcnt lgkmcnt(6)\n\t"
        "v_mfma_f32_16x16x32_bf16 v[48:51], a[12:15], v[100:103], 0\n\t"
        "v_permlane16_swap_b32 v117, v119\n\t"
        "v_mfma_f32_16x16x32_bf16 v[52:55], a[16:19], v[100:103], 0\n\t"
        "buffer_store_dwordx4 v[116:119], v144, s[28:31], s55 offen sc1\n\t"
        "v_mfma_f32_16x16x32_bf16 v[56:59], a[20:23], v[100:103], 0\n\t"
        "ds_write_b64 v146, v[120:121] offset:0\n\t"
        "v_mfma_f32_16x16x32_bf16 v[48:51], a[48:51], v[104:107], v[48:51]\n\t"
        "ds_read_b128 v[96:99], v151 offset:64\n\t"
        "s_add_u32 s55, s41, 43008\n\t"
        "v_mfma_f32_16x16x32_bf16 v[52:55], a[52:55], v[104:107], v[52:55]\n\t"
        "ds_read_b128 v[100:103], v151 offset:128\n\t"
        "v_add_u32 v151, s9, v176\n\t"
        "v_mfma_f32_16x16x32_bf16 v[56:59], a[56:59], v[104:107], v[56:59]\n\t"
        "v_max_u32 v151, v151, v136\n\t"
        "v_xor_b32 v137, v181, v137\n\t"
        "s_waitcnt lgkmcnt(4)\n\t"
        "v_mfma_f32_16x16x32_bf16 v[48:51], a[84:87], v[108:111], v[48:51]\n\t"
        "ds_read_b128 v[104:107], v183 offset:0\n\t"
        "v_cvt_pk_bf16_f32 v116, v12, v13\n\t"
        "v_mfma_f32_16x16x32_bf16 v[52:55], a[88:91], v[108:111], v[52:55]\n\t"
        "v_cvt_pk_bf16_f32 v117, v14, v15\n\t"
        "v_cvt_pk_bf16_f32 v118, v16, v17\n\t"
        "v_mfma_f32_16x16x32_bf16 v[56:59], a[92:95], v[108:111], v[56:59]\n\t"
        "v_cvt_pk_bf16_f32 v119, v18, v19\n\t"
        "v_cvt_pk_bf16_f32 v120, v20, v21\n\t"
        "v_mfma_f32_16x16x32_bf16 v[48:51], a[120:123], v[112:115], v[48:51]\n\t"
        "ds_read_b128 v[108:111], v183 offset:64\n\t"
        "v_cvt_pk_bf16_f32 v121, v22, v23\n\t"
        "v_mfma_f32_16x16x32_bf16 v[52:55], a[124:127], v[112:115], v[52:55]\n\t"
        "v_permlane16_swap_b32 v116, v118\n\t"
        "v_mfma_f32_16x16x32_bf16 v[56:59], a[128:131], v[112:115], v[56:59]\n\t"
        "v_permlane16_swap_b32 v117, v119\n\t"
        "v_mfma_f32_16x16x32_bf16 v[48:51], a[156:159], v[84:87], v[48:51]\n\t"
        "ds_read_b128 v[112:115], v183 offset:128\n\t"
        "v_add_u32 v183, s9, v177\n\t"
        "v_mfma_f32_16x16x32_bf16 v[52:55], a[160:163], v[84:87], v[52:55]\n\t"
        "v_max_u32 v183, v183, v137\n\t"
        "v_xor_b32 v138, v181, v138\n\t"
        "v_mfma_f32_16x16x32_bf16 v[56:59], a[164:167], v[84:87], v[56:59]\n\t"
        "v_mfma_f32_16x16x32_bf16 v[48:51], a[192:195], v[88:91], v[48:51]\n\t"
        "buffer_store_dwordx4 v[116:119], v144, s[28:31], s55 offen sc1\n\t"
        "v_mfma_f32_16x16x32_bf16 v[52:55], a[196:199], v[88:91], v[52:55]\n\t"
        "ds_write_b64 v146, v[120:121] offset:2048\n\t"
        "v_mfma_f32_16x16x32_bf16 v[56:59], a[200:203], v[88:91], v[56:59]\n\t"
        "ds_read_b128 v[84:87], v151 offset:0\n\t"
        "s_add_u32 s55, s41, 86016\n\t"
        "s_waitcnt lgkmcnt(4)\n\t"
        "v_mfma_f32_16x16x32_bf16 v[48:51], a[228:231], v[92:95], v[48:51]\n\t"
        "ds_read_b128 v[88:91], v151 offset:64\n\t"
        "v_cvt_pk_bf16_f32 v116, v24, v25\n\t"
        "v_mfma_f32_16x16x32_bf16 v[52:55], a[232:235], v[92:95], v[52:55]\n\t"
        "v_cvt_pk_bf16_f32 v117, v26, v27\n\t"
        "v_cvt_pk_bf16_f32 v118, v28, v29\n\t"
        "v_mfma_f32_16x16x32_bf16 v[56:59], a[236:239], v[92:95], v[56:59]\n\t"
        "v_cvt_pk_bf16_f32 v119, v30, v31\n\t"
        "v_cvt_pk_bf16_f32 v120, v32, v33\n\t"
        "v_mfma_f32_16x16x32_bf16 v[48:51], v[196:199], v[96:99], v[48:51]\n\t"
        "ds_read_b128 v[92:95], v151 offset:128\n\t"
        "v_add_u32 v151, s9, v178\n\t"
        "v_mfma_f32_16x16x32_bf16 v[52:55], v[200:203], v[96:99], v[52:55]\n\t"
        "v_max_u32 v151, v151, v138\n\t"
        "v_xor_b32 v139, v181, v139\n\t"
        "v_mfma_f32_16x16x32_bf16 v[56:59], v[204:207], v[96:99], v[56:59]\n\t"
        "v_cvt_pk_bf16_f32 v121, v34, v35\n\t"
        "v_mfma_f32_16x16x32_bf16 v[48:51], v[232:235], v[100:103], v[48:51]\n\t"
        "v_permlane16_swap_b32 v116, v118\n\t"
        "v_mfma_f32_16x16x32_bf16 v[52:55], v[236:239], v[100:103], v[52:55]\n\t"
        "v_permlane16_swap_b32 v117, v119\n\t"
        "v_mfma_f32_16x16x32_bf16 v[56:59], v[240:243], v[100:103], v[56:59]\n\t"
        "ds_read_b128 v[96:99], v183 offset:0\n\t"
        "v_mfma_f32_16x16x32_bf16 v[48:51], a[0:3], v[104:107], v[48:51]\n\t"
        "ds_read_b128 v[100:103], v183 offset:64\n\t"
        "v_mfma_f32_16x16x32_bf16 v[52:55], a[4:7], v[104:107], v[52:55]\n\t"
        "v_mfma_f32_16x16x32_bf16 v[56:59], a[8:11], v[104:107], v[56:59]\n\t"
        "buffer_store_dwordx4 v[116:119], v144, s[28:31], s55 offen sc1\n\t"
        "s_waitcnt lgkmcnt(3)\n\t"
        "v_mfma_f32_16x16x32_bf16 v[48:51], a[36:39], v[108:111], v[48:51]\n\t"
        "ds_write_b64 v146, v[120:121] offset:4096\n\t"
        "v_mfma_f32_16x16x32_bf16 v[52:55], a[40:43], v[108:111], v[52:55]\n\t"
        "ds_read_b128 v[104:107], v183 offset:128\n\t"
        "v_add_u32 v183, s9, v179\n\t"
        "v_mfma_f32_16x16x32_bf16 v[56:59], a[44:47], v[108:111], v[56:59]\n\t"
        "v_max_u32 v183, v183, v139\n\t"
        "v_xor_b32 v140, v181, v140\n\t"
        "v_mfma_f32_16x16x32_bf16 v[48:51], a[72:75], v[112:115], v[48:51]\n\t"
        "ds_read_b128 v[136:139], v187 offset:2064\n\t"
        "s_add_u32 s55, s41, 129024\n\t"
        "v_mfma_f32_16x16x32_bf16 v[52:55], a[76:79], v[112:115], v[52:55]\n\t"
        "ds_read_b128 v[108:111], v151 offset:0\n\t"
        "v_cvt_pk_bf16_f32 v116, v36, v37\n\t"
        "v_mfma_f32_16x16x32_bf16 v[56:59], a[80:83], v[112:115], v[56:59]\n\t"
        "v_cvt_pk_bf16_f32 v117, v38, v39\n\t"
        "v_cvt_pk_bf16_f32 v118, v40, v41\n\t"
        "v_mfma_f32_16x16x32_bf16 v[48:51], a[108:111], v[84:87], v[48:51]\n\t"
        "ds_read_b128 v[112:115], v151 offset:64\n\t"
        "v_cvt_pk_bf16_f32 v119, v42, v43\n\t"
        "v_mfma_f32_16x16x32_bf16 v[52:55], a[112:115], v[84:87], v[52:55]\n\t"
        "v_cvt_pk_bf16_f32 v120, v44, v45\n\t"
        "v_cvt_pk_bf16_f32 v121, v46, v47\n\t"
        "v_mfma_f32_16x16x32_bf16 v[56:59], a[116:119], v[84:87], v[56:59]\n\t"
        "v_permlane16_swap_b32 v116, v118\n\t"
        "v_mfma_f32_16x16x32_bf16 v[48:51], a[144:147], v[88:91], v[48:51]\n\t"
        "v_permlane16_swap_b32 v117, v119\n\t"
        "v_mfma_f32_16x16x32_bf16 v[52:55], a[148:151], v[88:91], v[52:55]\n\t"
        "ds_read_b128 v[84:87], v151 offset:128\n\t"
        "v_add_u32 v151, s9, v180\n\t"
        "v_mfma_f32_16x16x32_bf16 v[56:59], a[152:155], v[88:91], v[56:59]\n\t"
        "v_max_u32 v151, v151, v140\n\t"
        "v_xor_b32 v132, v181, v132\n\t"
        "s_waitcnt lgkmcnt(4)\n\t"
        "v_mfma_f32_16x16x32_bf16 v[48:51], a[180:183], v[92:95], v[48:51]\n\t"
        "ds_read_b32 v140, v187 offset:2080\n\t"
        "v_mfma_f32_16x16x32_bf16 v[52:55], a[184:187], v[92:95], v[52:55]\n\t"
        "buffer_store_dwordx4 v[116:119], v144, s[28:31], s55 offen sc1\n\t"
        "v_mfma_f32_16x16x32_bf16 v[56:59], a[188:191], v[92:95], v[56:59]\n\t"
        "ds_write_b64 v146, v[120:121] offset:6144\n\t"
        "v_mfma_f32_16x16x32_bf16 v[48:51], a[216:219], v[96:99], v[48:51]\n\t"
        "ds_read_b128 v[88:91], v183 offset:0\n\t"
        "v_mfma_f32_16x16x32_bf16 v[52:55], a[220:223], v[96:99], v[52:55]\n\t"
        "ds_read_b128 v[92:95], v183 offset:64\n\t"
        "v_mfma_f32_16x16x32_bf16 v[56:59], a[224:227], v[96:99], v[56:59]\n\t"
        "v_mfma_f32_16x16x32_bf16 v[48:51], a[252:255], v[100:103], v[48:51]\n\t"
        "ds_read_b128 v[96:99], v183 offset:128\n\t"
        "v_readlane_b32 s9, v155, 47\n\t"
        "v_mfma_f32_16x16x32_bf16 v[52:55], v[188:191], v[100:103], v[52:55]\n\t"
        "s_nop 0\n\t"
        "v_add_u32 v183, s9, v172\n\t"
        "v_max_u32 v183, v183, v132\n\t"
        "v_mfma_f32_16x16x32_bf16 v[56:59], v[192:195], v[100:103], v[56:59]\n\t"
        "s_waitcnt lgkmcnt(3)\n\t"
        "s_barrier\n\t"
        "v_mfma_f32_16x16x32_bf16 v[48:51], v[220:223], v[104:107], v[48:51]\n\t"
        "ds_read_b128 v[124:127], v147 offset:0\n\t"
        "v_xor_b32 v133, v181, v133\n\t"
        "v_mfma_f32_16x16x32_bf16 v[52:55], v[224:227], v[104:107], v[52:55]\n\t"
        "ds_read_b128 v[100:103], v151 offset:0\n\t"
        "v_mfma_f32_16x16x32_bf16 v[56:59], v[228:231], v[104:107], v[56:59]\n\t"
        "v_mfma_f32_16x16x32_bf16 v[48:51], a[24:27], v[108:111], v[48:51]\n\t"
        "ds_read_b128 v[104:107], v151 offset:64\n\t"
        "v_mfma_f32_16x16x32_bf16 v[52:55], a[28:31], v[108:111], v[52:55]\n\t"
        "v_mfma_f32_16x16x32_bf16 v[56:59], a[32:35], v[108:111], v[56:59]\n\t"
        "v_mfma_f32_16x16x32_bf16 v[48:51], a[60:63], v[112:115], v[48:51]\n\t"
        "ds_read_b128 v[108:111], v151 offset:128\n\t"
        "v_add_u32 v151, s9, v173\n\t"
        "v_mfma_f32_16x16x32_bf16 v[52:55], a[64:67], v[112:115], v[52:55]\n\t"
        "v_max_u32 v151, v151, v133\n\t"
        "v_xor_b32 v134, v181, v134\n\t"
        "v_mfma_f32_16x16x32_bf16 v[56:59], a[68:71], v[112:115], v[56:59]\n\t"
        "v_mfma_f32_16x16x32_bf16 v[48:51], a[96:99], v[84:87], v[48:51]\n\t"
        "s_waitcnt lgkmcnt(3)\n\t"
        "buffer_store_dwordx4 v[124:127], v145, s[28:31], s50 offen sc1\n\t"
        "v_mfma_f32_16x16x32_bf16 v[52:55], a[100:103], v[84:87], v[52:55]\n\t"
        "ds_read_b128 v[124:127], v147 offset:4096\n\t"
        "s_add_u32 s50, s50, 86016\n\t"
        "v_mfma_f32_16x16x32_bf16 v[56:59], a[104:107], v[84:87], v[56:59]\n\t"
        "ds_read_b128 v[112:115], v183 offset:0\n\t"
        "v_mfma_f32_16x16x32_bf16 v[48:51], a[132:135], v[88:91], v[48:51]\n\t"
        "ds_read_b128 v[84:87], v183 offset:64\n\t"
        "v_mfma_f32_16x16x32_bf16 v[52:55], a[136:139], v[88:91], v[52:55]\n\t"
        "v_mfma_f32_16x16x32_bf16 v[56:59], a[140:143], v[88:91], v[56:59]\n\t"
        "v_mfma_f32_16x16x32_bf16 v[48:51], a[168:171], v[92:95], v[48:51]\n\t"
        "ds_read_b128 v[88:91], v183 offset:128\n\t"
        "v_add_u32 v183, s9, v174\n\t"
        "v_mfma_f32_16x16x32_bf16 v[52:55], a[172:175], v[92:95], v[52:55]\n\t"
        "v_max_u32 v183, v183, v134\n\t"
        "v_xor_b32 v135, v181, v135\n\t"
        "v_mfma_f32_16x16x32_bf16 v[56:59], a[176:179], v[92:95], v[56:59]\n\t"
        "v_mfma_f32_16x16x32_bf16 v[48:51], a[204:207], v[96:99], v[48:51]\n\t"
        "ds_read_b128 v[92:95], v151 offset:0\n\t"
        "v_mfma_f32_16x16x32_bf16 v[52:55], a[208:211], v[96:99], v[52:55]\n\t"
        "v_mfma_f32_16x16x32_bf16 v[56:59], a[212:215], v[96:99], v[56:59]\n\t"
        "s_waitcnt lgkmcnt(4)\n\t"
        "v_mfma_f32_16x16x32_bf16 v[48:51], a[240:243], v[100:103], v[48:51]\n\t"
        "ds_read_b128 v[96:99], v151 offset:64\n\t"
        "v_mfma_f32_16x16x32_bf16 v[52:55], a[244:247], v[100:103], v[52:55]\n\t"
        "v_mfma_f32_16x16x32_bf16 v[56:59], a[248:251], v[100:103], v[56:59]\n\t"
        "buffer_store_dwordx4 v[124:127], v145, s[28:31], s50 offen sc1\n\t"
        "v_mfma_f32_16x16x32_bf16 v[48:51], v[208:211], v[104:107], v[48:51]\n\t"
        "ds_read_b128 v[100:103], v151 offset:128\n\t"
        "v_add_u32 v151, s9, v175\n\t"
        "v_mfma_f32_16x16x32_bf16 v[52:55], v[212:215], v[104:107], v[52:55]\n\t"
        "v_max_u32 v151, v151, v135\n\t"
        "v_xor_b32 v136, v181, v136\n\t"
        "v_mfma_f32_16x16x32_bf16 v[56:59], v[216:219], v[104:107], v[56:59]\n\t"
        "ds_read_b128 v[132:135], v187 offset:4096\n\t"
        "v_mfma_f32_16x16x32_bf16 v[48:51], v[244:247], v[108:111], v[48:51]\n\t"
        "ds_read_b128 v[104:107], v183 offset:0\n\t"
        "v_mfma_f32_16x16x32_bf16 v[52:55], v[248:251], v[108:111], v[52:55]\n\t"
        "v_mfma_f32_16x16x32_bf16 v[56:59], v[252:255], v[108:111], v[56:59]\n\t"
        "s_waitcnt lgkmcnt(4)\n\t"
        "v_mfma_f32_16x16x32_bf16 v[60:63], a[12:15], v[112:115], 0\n\t"
        "ds_read_b128 v[108:111], v183 offset:64\n\t"
        "v_mfma_f32_16x16x32_bf16 v[64:67], a[16:19], v[112:115], 0\n\t"
        "v_mfma_f32_16x16x32_bf16 v[68:71], a[20:23], v[112:115], 0\n\t"
        "v_mfma_f32_16x16x32_bf16 v[60:63], a[48:51], v[84:87], v[60:63]\n\t"
        "ds_read_b128 v[112:115], v183 offset:128\n\t"
        "v_add_u32 v183, s9, v176\n\t"
        "v_mfma_f32_16x16x32_bf16 v[64:67], a[52:55], v[84:87], v[64:67]\n\t"
        "v_max_u32 v183, v183, v136\n\t"
        "v_xor_b32 v137, v181, v137\n\t"
        "v_mfma_f32_16x16x32_bf16 v[68:71], a[56:59], v[84:87], v[68:71]\n\t"
        "v_readlane_b32 s55, v155, 49\n\t"
        "v_cvt_pk_bf16_f32 v116, v48, v49\n\t"
        "v_mfma_f32_16x16x32_bf16 v[60:63], a[84:87], v[88:91], v[60:63]\n\t"
        "ds_read_b128 v[84:87], v151 offset:0\n\t"
        "v_cvt_pk_bf16_f32 v117, v50, v51\n\t"
        "v_mfma_f32_16x16x32_bf16 v[64:67], a[88:91], v[88:91], v[64:67]\n\t"
        "v_cvt_pk_bf16_f32 v118, v52, v53\n\t"
        "v_cvt_pk_bf16_f32 v119, v54, v55\n\t"
        "v_mfma_f32_16x16x32_bf16 v[68:71], a[92:95], v[88:91], v[68:71]\n\t"
        "v_cvt_pk_bf16_f32 v120, v56, v57\n\t"
        "v_cvt_pk_bf16_f32 v121, v58, v59\n\t"
        "v_mfma_f32_16x16x32_bf16 v[60:63], a[120:123], v[92:95], v[60:63]\n\t"
        "v_permlane16_swap_b32 v116, v118\n\t"
        "v_mfma_f32_16x16x32_bf16 v[64:67], a[124:127], v[92:95], v[64:67]\n\t"
        "v_permlane16_swap_b32 v117, v119\n\t"
        "v_mfma_f32_16x16x32_bf16 v[68:71], a[128:131], v[92:95], v[68:71]\n\t"
        "buffer_store_dwordx4 v[116:119], v148, s[28:31], s55 offen sc1\n\t"
        "s_waitcnt lgkmcnt(2)\n\t"
        "v_mfma_f32_16x16x32_bf16 v[60:63], a[156:159], v[96:99], v[60:63]\n\t"
        "ds_write_b64 v146, v[120:121] offset:8192\n\t"
        "v_mfma_f32_16x16x32_bf16 v[64:67], a[160:163], v[96:99], v[64:67]\n\t"
        "ds_read_b128 v[88:91], v151 offset:64\n\t"
        "v_mfma_f32_16x16x32_bf16 v[68:71], a[164:167], v[96:99], v[68:71]\n\t"
        "ds_read_b128 v[92:95], v151 offset:128\n\t"
        "v_add_u32 v151, s9, v177\n\t"
        "v_mfma_f32_16x16x32_bf16 v[60:63], a[192:195], v[100:103], v[60:63]\n\t"
        "ds_read_b128 v[96:99], v183 offset:0\n\t"
        "v_max_u32 v151, v151, v137\n\t"
        "v_mfma_f32_16x16x32_bf16 v[64:67], a[196:199], v[100:103], v[64:67]\n\t"
        "v_xor_b32 v138, v181, v138\n\t"
        "v_mfma_f32_16x16x32_bf16 v[68:71], a[200:203], v[100:103], v[68:71]\n\t"
        "v_mfma_f32_16x16x32_bf16 v[60:63], a[228:231], v[104:107], v[60:63]\n\t"
        "s_waitcnt lgkmcnt(3)\n\t"
        "s_barrier\n\t"
        "v_mfma_f32_16x16x32_bf16 v[64:67], a[232:235], v[104:107], v[64:67]\n\t"
        "ds_read_b128 v[124:127], v147 offset:8192\n\t"
        "v_mfma_f32_16x16x32_bf16 v[68:71], a[236:239], v[104:107], v[68:71]\n\t"
        "ds_read_b128 v[100:103], v183 offset:64\n\t"
        "v_mfma_f32_16x16x32_bf16 v[60:63], v[196:199], v[108:111], v[60:63]\n\t"
        "ds_read_b128 v[104:107], v183 offset:128\n\t"
        "v_add_u32 v183, s9, v178\n\t"
        "v_mfma_f32_16x16x32_bf16 v[64:67], v[200:203], v[108:111], v[64:67]\n\t"
        "v_max_u32 v183, v183, v138\n\t"
        "v_xor_b32 v139, v181, v139\n\t"
        "v_mfma_f32_16x16x32_bf16 v[68:71], v[204:207], v[108:111], v[68:71]\n\t"
        "v_mfma_f32_16x16x32_bf16 v[60:63], v[232:235], v[112:115], v[60:63]\n\t"
        "ds_read_b128 v[108:111], v151 offset:0\n\t"
        "v_mfma_f32_16x16x32_bf16 v[64:67], v[236:239], v[112:115], v[64:67]\n\t"
        "v_mfma_f32_16x16x32_bf16 v[68:71], v[240:243], v[112:115], v[68:71]\n\t"
        "v_mfma_f32_16x16x32_bf16 v[60:63], a[0:3], v[84:87], v[60:63]\n\t"
        "ds_read_b128 v[112:115], v151 offset:64\n\t"
        "v_mfma_f32_16x16x32_bf16 v[64:67], a[4:7], v[84:87], v[64:67]\n\t"
        "s_waitcnt lgkmcnt(4)\n\t"
        "buffer_store_dwordx4 v[124:127], v150, s[28:31], s51 offen sc1\n\t"
        "v_mfma_f32_16x16x32_bf16 v[68:71], a[8:11], v[84:87], v[68:71]\n\t"
        "v_mfma_f32_16x16x32_bf16 v[60:63], a[36:39], v[88:91], v[60:63]\n\t"
        "ds_read_b128 v[84:87], v151 offset:128\n\t"
        "v_add_u32 v151, s9, v179\n\t"
        "v_mfma_f32_16x16x32_bf16 v[64:67], a[40:43], v[88:91], v[64:67]\n\t"
        "v_max_u32 v151, v151, v139\n\t"
        "v_xor_b32 v140, v181, v140\n\t"
        "v_mfma_f32_16x16x32_bf16 v[68:71], a[44:47], v[88:91], v[68:71]\n\t"
        "ds_read_b128 v[136:139], v187 offset:4112\n\t"
        "v_mfma_f32_16x16x32_bf16 v[60:63], a[72:75], v[92:95], v[60:63]\n\t"
        "ds_read_b128 v[88:91], v183 offset:0\n\t"
        "v_mfma_f32_16x16x32_bf16 v[64:67], a[76:79], v[92:95], v[64:67]\n\t"
        "v_mfma_f32_16x16x32_bf16 v[68:71], a[80:83], v[92:95], v[68:71]\n\t"
        "v_mfma_f32_16x16x32_bf16 v[60:63], a[108:111], v[96:99], v[60:63]\n\t"
        "ds_read_b128 v[92:95], v183 offset:64\n\t"
        "v_mfma_f32_16x16x32_bf16 v[64:67], a[112:115], v[96:99], v[64:67]\n\t"
        "v_mfma_f32_16x16x32_bf16 v[68:71], a[116:119], v[96:99], v[68:71]\n\t"
        "s_waitcnt lgkmcnt(4)\n\t"
        "v_mfma_f32_16x16x32_bf16 v[60:63], a[144:147], v[100:103], v[60:63]\n\t"
        "ds_read_b128 v[96:99], v183 offset:128\n\t"
        "v_add_u32 v183, s9, v180\n\t"
        "v_mfma_f32_16x16x32_bf16 v[64:67], a[148:151], v[100:103], v[64:67]\n\t"
        "v_max_u32 v183, v183, v140\n\t"
        "v_xor_b32 v132, v181, v132\n\t"
        "v_mfma_f32_16x16x32_bf16 v[68:71], a[152:155], v[100:103], v[68:71]\n\t"
        "ds_read_b32 v140, v187 offset:4128\n\t"
        "v_mfma_f32_16x16x32_bf16 v[60:63], a[180:183], v[104:107], v[60:63]\n\t"
        "ds_read_b128 v[100:103], v151 offset:0\n\t"
        "v_mfma_f32_16x16x32_bf16 v[64:67], a[184:187], v[104:107], v[64:67]\n\t"
        "v_mfma_f32_16x16x32_bf16 v[68:71], a[188:191], v[104:107], v[68:71]\n\t"
        "v_mfma_f32_16x16x32_bf16 v[60:63], a[216:219], v[108:111], v[60:63]\n\t"
        "ds_read_b128 v[104:107], v151 offset:64\n\t"
        "v_mfma_f32_16x16x32_bf16 v[64:67], a[220:223], v[108:111], v[64:67]\n\t"
        "v_mfma_f32_16x16x32_bf16 v[68:71], a[224:227], v[108:111], v[68:71]\n\t"
        "v_mfma_f32_16x16x32_bf16 v[60:63], a[252:255], v[112:115], v[60:63]\n\t"
        "ds_read_b128 v[108:111], v151 offset:128\n\t"
        "v_readlane_b32 s9, v155, 48\n\t"
        "v_mfma_f32_16x16x32_bf16 v[64:67], v[188:191], v[112:115], v[64:67]\n\t"
        "s_nop 0\n\t"
        "v_add_u32 v151, s9, v172\n\t"
        "v_max_u32 v151, v151, v132\n\t"
        "v_mfma_f32_16x16x32_bf16 v[68:71], v[192:195], v[112:115], v[68:71]\n\t"
        "v_xor_b32 v133, v181, v133\n\t"
        "s_waitcnt lgkmcnt(4)\n\t"
        "v_mfma_f32_16x16x32_bf16 v[60:63], v[220:223], v[84:87], v[60:63]\n\t"
        "ds_read_b128 v[112:115], v183 offset:0\n\t"
        "v_mfma_f32_16x16x32_bf16 v[64:67], v[224:227], v[84:87], v[64:67]\n\t"
        "v_mfma_f32_16x16x32_bf16 v[68:71], v[228:231], v[84:87], v[68:71]\n\t"
        "v_mfma_f32_16x16x32_bf16 v[60:63], a[24:27], v[88:91], v[60:63]\n\t"
        "ds_read_b128 v[84:87], v183 offset:64\n\t"
        "v_mfma_f32_16x16x32_bf16 v[64:67], a[28:31], v[88:91], v[64:67]\n\t"
        "v_mfma_f32_16x16x32_bf16 v[68:71], a[32:35], v[88:91], v[68:71]\n\t"
        "v_mfma_f32_16x16x32_bf16 v[60:63], a[60:63], v[92:95], v[60:63]\n\t"
        "ds_read_b128 v[88:91], v183 offset:128\n\t"
        "v_add_u32 v183, s9, v173\n\t"
        "v_mfma_f32_16x16x32_bf16 v[64:67], a[64:67], v[92:95], v[64:67]\n\t"
        "v_max_u32 v183, v183, v133\n\t"
        "v_xor_b32 v134, v181, v134\n\t"
        "v_mfma_f32_16x16x32_bf16 v[68:71], a[68:71], v[92:95], v[68:71]\n\t"
        "v_mfma_f32_16x16x32_bf16 v[60:63], a[96:99], v[96:99], v[60:63]\n\t"
        "ds_read_b128 v[92:95], v151 offset:0\n\t"
        "v_mfma_f32_16x16x32_bf16 v[64:67], a[100:103], v[96:99], v[64:67]\n\t"
        "v_mfma_f32_16x16x32_bf16 v[68:71], a[104:107], v[96:99], v[68:71]\n\t"
        "s_waitcnt lgkmcnt(3)\n\t"
        "v_mfma_f32_16x16x32_bf16 v[60:63], a[132:135], v[100:103], v[60:63]\n\t"
        "ds_read_b128 v[96:99], v151 offset:64\n\t"
        "v_mfma_f32_16x16x32_bf16 v[64:67], a[136:139], v[100:103], v[64:67]\n\t"
        "v_mfma_f32_16x16x32_bf16 v[68:71], a[140:143], v[100:103], v[68:71]\n\t"
        "v_mfma_f32_16x16x32_bf16 v[60:63], a[168:171], v[104:107], v[60:63]\n\t"
        "ds_read_b128 v[100:103], v151 offset:128\n\t"
        "v_add_u32 v151, s9, v174\n\t"
        "v_mfma_f32_16x16x32_bf16 v[64:67], a[172:175], v[104:107], v[64:67]\n\t"
        "v_max_u32 v151, v151, v134\n\t"
        "v_xor_b32 v135, v181, v135\n\t"
        "v_mfma_f32_16x16x32_bf16 v[68:71], a[176:179], v[104:107], v[68:71]\n\t"
        "v_mfma_f32_16x16x32_bf16 v[60:63], a[204:207], v[108:111], v[60:63]\n\t"
        "ds_read_b128 v[104:107], v183 offset:0\n\t"
        "v_mfma_f32_16x16x32_bf16 v[64:67], a[208:211], v[108:111], v[64:67]\n\t"
        "v_mfma_f32_16x16x32_bf16 v[68:71], a[212:215], v[108:111], v[68:71]\n\t"
        "v_mfma_f32_16x16x32_bf16 v[60:63], a[240:243], v[112:115], v[60:63]\n\t"
        "ds_read_b128 v[108:111], v183 offset:64\n\t"
        "v_mfma_f32_16x16x32_bf16 v[64:67], a[244:247], v[112:115], v[64:67]\n\t"
        "v_mfma_f32_16x16x32_bf16 v[68:71], a[248:251], v[112:115], v[68:71]\n\t"
        "s_waitcnt lgkmcnt(3)\n\t"
        "v_mfma_f32_16x16x32_bf16 v[60:63], v[208:211], v[84:87], v[60:63]\n\t"
        "ds_read_b128 v[112:115], v183 offset:128\n\t"
        "v_add_u32 v183, s9, v175\n\t"
        "v_mfma_f32_16x16x32_bf16 v[64:67], v[212:215], v[84:87], v[64:67]\n\t"
        "v_max_u32 v183, v183, v135\n\t"
        "v_xor_b32 v136, v181, v136\n\t"
        "v_mfma_f32_16x16x32_bf16 v[68:71], v[216:219], v[84:87], v[68:71]\n\t"
        "v_mfma_f32_16x16x32_bf16 v[60:63], v[244:247], v[88:91], v[60:63]\n\t"
        "ds_read_b128 v[84:87], v151 offset:0\n\t"
        "v_mfma_f32_16x16x32_bf16 v[64:67], v[248:251], v[88:91], v[64:67]\n\t"
        "v_mfma_f32_16x16x32_bf16 v[68:71], v[252:255], v[88:91], v[68:71]\n\t"
        "v_mfma_f32_16x16x32_bf16 v[72:75], a[12:15], v[92:95], 0\n\t"
        "ds_read_b128 v[88:91], v151 offset:64\n\t"
        "v_mfma_f32_16x16x32_bf16 v[76:79], a[16:19], v[92:95], 0\n\t"
        "v_mfma_f32_16x16x32_bf16 v[80:83], a[20:23], v[92:95], 0\n\t"
        "v_mfma_f32_16x16x32_bf16 v[72:75], a[48:51], v[96:99], v[72:75]\n\t"
        "ds_read_b128 v[92:95], v151 offset:128\n\t"
        "v_add_u32 v151, s9, v176\n\t"
        "v_mfma_f32_16x16x32_bf16 v[76:79], a[52:55], v[96:99], v[76:79]\n\t"
        "v_max_u32 v151, v151, v136\n\t"
        "v_xor_b32 v137, v181, v137\n\t"
        "v_mfma_f32_16x16x32_bf16 v[80:83], a[56:59], v[96:99], v[80:83]\n\t"
        "v_readlane_b32 s55, v155, 50\n\t"
        "v_cvt_pk_bf16_f32 v116, v60, v61\n\t"
        "s_waitcnt lgkmcnt(3)\n\t"
        "v_mfma_f32_16x16x32_bf16 v[72:75], a[84:87], v[100:103], v[72:75]\n\t"
        "ds_read_b128 v[96:99], v183 offset:0\n\t"
        "v_cvt_pk_bf16_f32 v117, v62, v63\n\t"
        "v_mfma_f32_16x16x32_bf16 v[76:79], a[88:91], v[100:103], v[76:79]\n\t"
        "v_cvt_pk_bf16_f32 v118, v64, v65\n\t"
        "v_cvt_pk_bf16_f32 v119, v66, v67\n\t"
        "v_mfma_f32_16x16x32_bf16 v[80:83], a[92:95], v[100:103], v[80:83]\n\t"
        "v_cvt_pk_bf16_f32 v120, v68, v69\n\t"
        "v_cvt_pk_bf16_f32 v121, v70, v71\n\t"
        "v_mfma_f32_16x16x32_bf16 v[72:75], a[120:123], v[104:107], v[72:75]\n\t"
        "v_permlane16_swap_b32 v116, v118\n\t"
        "v_mfma_f32_16x16x32_bf16 v[76:79], a[124:127], v[104:107], v[76:79]\n\t"
        "v_permlane16_swap_b32 v117, v119\n\t"
        "v_mfma_f32_16x16x32_bf16 v[80:83], a[128:131], v[104:107], v[80:83]\n\t"
        "buffer_store_dwordx4 v[116:119], v148, s[28:31], s55 offen sc1\n\t"
        "v_mfma_f32_16x16x32_bf16 v[72:75], a[156:159], v[108:111], v[72:75]\n\t"
        "ds_write_b64 v146, v[120:121] offset:0\n\t"
        "v_mfma_f32_16x16x32_bf16 v[76:79], a[160:163], v[108:111], v[76:79]\n\t"
        "ds_read_b128 v[100:103], v183 offset:64\n\t"
        "v_mfma_f32_16x16x32_bf16 v[80:83], a[164:167], v[108:111], v[80:83]\n\t"
        "ds_read_b128 v[104:107], v183 offset:128\n\t"
        "v_add_u32 v183, s9, v177\n\t"
        "v_mfma_f32_16x16x32_bf16 v[72:75], a[192:195], v[112:115], v[72:75]\n\t"
        "ds_read_b128 v[108:111], v151 offset:0\n\t"
        "v_max_u32 v183, v183, v137\n\t"
        "v_mfma_f32_16x16x32_bf16 v[76:79], a[196:199], v[112:115], v[76:79]\n\t"
        "v_xor_b32 v138, v181, v138\n\t"
        "v_mfma_f32_16x16x32_bf16 v[80:83], a[200:203], v[112:115], v[80:83]\n\t"
        "s_waitcnt lgkmcnt(4)\n\t"
        "v_mfma_f32_16x16x32_bf16 v[72:75], a[228:231], v[84:87], v[72:75]\n\t"
        "ds_read_b128 v[112:115], v151 offset:64\n\t"
        "v_mfma_f32_16x16x32_bf16 v[76:79], a[232:235], v[84:87], v[76:79]\n\t"
        "v_mfma_f32_16x16x32_bf16 v[80:83], a[236:239], v[84:87], v[80:83]\n\t"
        "v_mfma_f32_16x16x32_bf16 v[72:75], v[196:199], v[88:91], v[72:75]\n\t"
        "ds_read_b128 v[84:87], v151 offset:128\n\t"
        "v_add_u32 v151, s9, v178\n\t"
        "v_mfma_f32_16x16x32_bf16 v[76:79], v[200:203], v[88:91], v[76:79]\n\t"
        "v_max_u32 v151, v151, v138\n\t"
        "v_xor_b32 v139, v181, v139\n\t"
        "v_mfma_f32_16x16x32_bf16 v[80:83], v[204:207], v[88:91], v[80:83]\n\t"
        "v_mfma_f32_16x16x32_bf16 v[72:75], v[232:235], v[92:95], v[72:75]\n\t"
        "ds_read_b128 v[88:91], v183 offset:0\n\t"
        "v_mfma_f32_16x16x32_bf16 v[76:79], v[236:239], v[92:95], v[76:79]\n\t"
        "v_mfma_f32_16x16x32_bf16 v[80:83], v[240:243], v[92:95], v[80:83]\n\t"
        "v_mfma_f32_16x16x32_bf16 v[72:75], a[0:3], v[96:99], v[72:75]\n\t"
        "ds_read_b128 v[92:95], v183 offset:64\n\t"
        "v_mfma_f32_16x16x32_bf16 v[76:79], a[4:7], v[96:99], v[76:79]\n\t"
        ".LpS2%=:\n\t"
        "s_cbranch_execz .LrS%=\n\t"
        "v_mfma_f32_16x16x32_bf16 v[80:83], a[8:11], v[96:99], v[80:83]\n\t"
        "s_waitcnt lgkmcnt(3)\n\t"
        "v_mfma_f32_16x16x32_bf16 v[72:75], a[36:39], v[100:103], v[72:75]\n\t"
        "ds_read_b128 v[96:99], v183 offset:128\n\t"
        "v_add_u32 v183, s9, v179\n\t"
        "v_mfma_f32_16x16x32_bf16 v[76:79], a[40:43], v[100:103], v[76:79]\n\t"
        "v_max_u32 v183, v183, v139\n\t"
        "v_xor_b32 v140, v181, v140\n\t"
        "v_mfma_f32_16x16x32_bf16 v[80:83], a[44:47], v[100:103], v[80:83]\n\t"
        "v_mfma_f32_16x16x32_bf16 v[72:75], a[72:75], v[104:107], v[72:75]\n\t"
        "ds_read_b128 v[100:103], v151 offset:0\n\t"
        "v_mfma_f32_16x16x32_bf16 v[76:79], a[76:79], v[104:107], v[76:79]\n\t"
        "v_mfma_f32_16x16x32_bf16 v[80:83], a[80:83], v[104:107], v[80:83]\n\t"
        "v_mfma_f32_16x16x32_bf16 v[72:75], a[108:111], v[108:111], v[72:75]\n\t"
        "ds_read_b128 v[104:107], v151 offset:64\n\t"
        "v_mfma_f32_16x16x32_bf16 v[76:79], a[112:115], v[108:111], v[76:79]\n\t"
        "v_mfma_f32_16x16x32_bf16 v[80:83], a[116:119], v[108:111], v[80:83]\n\t"
        "ds_read_b128 v[108:111], v151 offset:128\n\t"
        "v_add_u32 v151, s9, v180\n\t"
        "v_mfma_f32_16x16x32_bf16 v[72:75], a[144:147], v[112:115], v[72:75]\n\t"
        "v_max_u32 v151, v151, v140\n\t"
        "v_mfma_f32_16x16x32_bf16 v[76:79], a[148:151], v[112:115], v[76:79]\n\t"
        "v_mfma_f32_16x16x32_bf16 v[80:83], a[152:155], v[112:115], v[80:83]\n\t"
        "ds_read_b128 v[112:115], v183 offset:0\n\t"
        "s_waitcnt lgkmcnt(4)\n\t"
        "v_mfma_f32_16x16x32_bf16 v[72:75], a[180:183], v[84:87], v[72:75]\n\t"
        "v_mfma_f32_16x16x32_bf16 v[76:79], a[184:187], v[84:87], v[76:79]\n\t"
        "v_mfma_f32_16x16x32_bf16 v[80:83], a[188:191], v[84:87], v[80:83]\n\t"
        "ds_read_b128 v[84:87], v183 offset:64\n\t"
        "v_mfma_f32_16x16x32_bf16 v[72:75], a[216:219], v[88:91], v[72:75]\n\t"
        "v_mfma_f32_16x16x32_bf16 v[76:79], a[220:223], v[88:91], v[76:79]\n\t"
        "v_mfma_f32_16x16x32_bf16 v[80:83], a[224:227], v[88:91], v[80:83]\n\t"
        "ds_read_b128 v[88:91], v183 offset:128\n\t"
        "v_mfma_f32_16x16x32_bf16 v[72:75], a[252:255], v[92:95], v[72:75]\n\t"
        "v_mfma_f32_16x16x32_bf16 v[76:79], v[188:191], v[92:95], v[76:79]\n\t"
        "v_mfma_f32_16x16x32_bf16 v[80:83], v[192:195], v[92:95], v[80:83]\n\t"
        "ds_read_b128 v[92:95], v151 offset:0\n\t"
        "v_mfma_f32_16x16x32_bf16 v[72:75], v[220:223], v[96:99], v[72:75]\n\t"
        "v_mfma_f32_16x16x32_bf16 v[76:79], v[224:227], v[96:99], v[76:79]\n\t"
        "v_mfma_f32_16x16x32_bf16 v[80:83], v[228:231], v[96:99], v[80:83]\n\t"
        "ds_read_b128 v[96:99], v151 offset:64\n\t"
        "s_waitcnt lgkmcnt(4)\n\t"
        "v_mfma_f32_16x16x32_bf16 v[72:75], a[24:27], v[100:103], v[72:75]\n\t"
        "v_mfma_f32_16x16x32_bf16 v[76:79], a[28:31], v[100:103], v[76:79]\n\t"
        "v_mfma_f32_16x16x32_bf16 v[80:83], a[32:35], v[100:103], v[80:83]\n\t"
        "ds_read_b128 v[100:103], v151 offset:128\n\t"
        "v_mfma_f32_16x16x32_bf16 v[80:83], a[68:71], v[104:107], v[80:83]\n\t"
        "v_mfma_f32_16x16x32_bf16 v[80:83], a[104:107], v[108:111], v[80:83]\n\t"
        "v_mfma_f32_16x16x32_bf16 v[80:83], a[140:143], v[112:115], v[80:83]\n\t"
        "s_waitcnt lgkmcnt(3)\n\t"
        "v_mfma_f32_16x16x32_bf16 v[80:83], a[176:179], v[84:87], v[80:83]\n\t"
        "v_mfma_f32_16x16x32_bf16 v[80:83], a[212:215], v[88:91], v[80:83]\n\t"
        "s_waitcnt lgkmcnt(2)\n\t"
        "v_mfma_f32_16x16x32_bf16 v[80:83], a[248:251], v[92:95], v[80:83]\n\t"
        "s_waitcnt lgkmcnt(1)\n\t"
        "v_mfma_f32_16x16x32_bf16 v[80:83], v[216:219], v[96:99], v[80:83]\n\t"
        "s_waitcnt lgkmcnt(0)\n\t"
        "v_mfma_f32_16x16x32_bf16 v[80:83], v[252:255], v[100:103], v[80:83]\n\t"
        "v_mfma_f32_16x16x32_bf16 v[72:75], a[60:63], v[104:107], v[72:75]\n\t"
        "v_mfma_f32_16x16x32_bf16 v[76:79], a[64:67], v[104:107], v[76:79]\n\t"
        "s_nop 5\n\t"
        "v_cvt_pk_bf16_f32 v120, v80, v81\n\t"
        "v_cvt_pk_bf16_f32 v121, v82, v83\n\t"
        "v_mfma_f32_16x16x32_bf16 v[72:75], a[96:99], v[108:111], v[72:75]\n\t"
        "ds_write_b64 v146, v[120:121] offset:2048\n\t"
        "v_mfma_f32_16x16x32_bf16 v[76:79], a[100:103], v[108:111], v[76:79]\n\t"
        "v_mfma_f32_16x16x32_bf16 v[72:75], a[132:135], v[112:115], v[72:75]\n\t"
        "v_mfma_f32_16x16x32_bf16 v[76:79], a[136:139], v[112:115], v[76:79]\n\t"
        "s_waitcnt lgkmcnt(0)\n\t"
        "s_barrier\n\t"
        "v_mfma_f32_16x16x32_bf16 v[72:75], a[168:171], v[84:87], v[72:75]\n\t"
        "ds_read_b128 v[124:127], v147 offset:0\n\t"
        "v_readlane_b32 s55, v155, 51\n\t"
        "v_mfma_f32_16x16x32_bf16 v[76:79], a[172:175], v[84:87], v[76:79]\n\t"
        "v_mfma_f32_16x16x32_bf16 v[72:75], a[204:207], v[88:91], v[72:75]\n\t"
        "v_mfma_f32_16x16x32_bf16 v[76:79], a[208:211], v[88:91], v[76:79]\n\t"
        "v_mfma_f32_16x16x32_bf16 v[72:75], a[240:243], v[92:95], v[72:75]\n\t"
        "v_mfma_f32_16x16x32_bf16 v[76:79], a[244:247], v[92:95], v[76:79]\n\t"
        "v_mfma_f32_16x16x32_bf16 v[72:75], v[208:211], v[96:99], v[72:75]\n\t"
        "v_mfma_f32_16x16x32_bf16 v[76:79], v[212:215], v[96:99], v[76:79]\n\t"
        "v_mfma_f32_16x16x32_bf16 v[72:75], v[244:247], v[100:103], v[72:75]\n\t"
        "v_mfma_f32_16x16x32_bf16 v[76:79], v[248:251], v[100:103], v[76:79]\n\t"
        "s_nop 6\n\t"
        "v_cvt_pk_bf16_f32 v116, v72, v73\n\t"
        "v_cvt_pk_bf16_f32 v117, v74, v75\n\t"
        "v_cvt_pk_bf16_f32 v118, v76, v77\n\t"
        "v_cvt_pk_bf16_f32 v119, v78, v79\n\t"
        "s_nop 0\n\t"
        "v_permlane16_swap_b32 v116, v118\n\t"
        "v_permlane16_swap_b32 v117, v119\n\t"
        "buffer_store_dwordx4 v[116:119], v148, s[28:31], s55 offen  \n\t"
        "s_waitcnt lgkmcnt(0)\n\t"
        "buffer_store_dwordx4 v[124:127], v150, s[28:31], s52 offen  \n\t"
        "s_andn2_b32 s6, s6, 2\n\t"
        "s_cbranch_scc1 99f\n\t"
        "s_endpgm\n\t"
        "21:\n\t"
        "s_bitcmp1_b32 s7, 15\n\t"
        "s_cbranch_scc1 22f\n\t"
        "ds_bpermute_b32 v64, v65, v155 offset:172\n\t"
        "s_add_u32 m0, s37, -24576\n\t"
        "s_add_u32 s36, s36, 6144\n\t"
        "buffer_load_dwordx4 v140, s[24:27], s36 offen offset:0 lds\n\t"
        "ds_bpermute_b32 v78, v81, v155 offset:148\n\t"
        "ds_bpermute_b32 v79, v81, v155 offset:160\n\t"
        "v_readlane_b32 s71, v155, 26\n\t"
        "v_readlane_b32 s10, v155, 29\n\t"
        "s_nop 0\n\t"
        "v_mul_u32_u24 v73, s71, v66\n\t"
        "v_lshrrev_b32 v73, 11, v73\n\t"
        "v_lshlrev_b32 v66, 4, v66\n\t"
        "v_mad_u32_u24 v73, v73, s10, v66\n\t"
        "s_waitcnt lgkmcnt(2)\n\t"
        "v_add_u32 v64, v64, v73\n\t"
        "buffer_load_dwordx4 v141, s[24:27], s36 offen offset:1024 lds\n\t"
        "v_readlane_b32 s69, v155, 34\n\t"
        "v_readlane_b32 s70, v155, 35\n\t"
        "s_nop 0\n\t"
        "v_mad_u32_u24 v70, v52, s69, v70\n\t"
        "v_mad_u32_u24 v70, v158, s70, v70\n\t"
        "s_mul_i32 s71, s69, s68\n\t"
        "s_add_u32 s71, s71, 157728\n\t"
        "v_mad_u32_u24 v67, v82, s70, v67\n\t"
        "v_add_u32 v67, s71, v67\n\t"
        "v_readlane_b32 s11, v155, 30\n\t"
        "buffer_load_dwordx4 v142, s[24:27], s36 offen offset:2048 lds\n\t"
        "s_movk_i32 s10, 223\n\t"
        "v_mov_b32 v75, 0x7ff00000\n\t"
        "v_readlane_b32 s12, v155, 24\n\t"
        "v_readlane_b32 s13, v155, 25\n\t"
        "s_nop 0\n\t"
        "s_bfe_u32 s70, s12, 524288\n\t"
        "s_bfe_u32 s71, s13, 524288\n\t"
        "v_cmp_gt_u32 vcc, s71, v71\n\t"
        "v_add3_u32 v73, v71, s70, -2\n\t"
        "v_med3_i32 v73, v73, 0, s10\n\t"
        "s_add_u32 m0, s35, 4096\n\t"
        "s_add_u32 s43, s34, 43008\n\t"
        "buffer_load_dwordx4 v143, s[16:19], s43 offen  lds\n\t"
        "v_cndmask_b32 v74, v75, v72, vcc\n\t"
        "v_mad_u32_u24 v134, v73, s11, v74\n\t"
        "s_bfe_u32 s70, s12, 524296\n\t"
        "s_bfe_u32 s71, s13, 524296\n\t"
        "v_cmp_gt_u32 vcc, s71, v71\n\t"
        "v_add3_u32 v73, v71, s70, -2\n\t"
        "v_med3_i32 v73, v73, 0, s10\n\t"
        "v_cndmask_b32 v74, v75, v72, vcc\n\t"
        "v_mad_u32_u24 v135, v73, s11, v74\n\t"
        "s_add_u32 m0, s35, 20480\n\t"
        "s_add_u32 s43, s34, 215040\n\t"
        "buffer_load_dwordx4 v143, s[16:19], s43 offen  lds\n\t"
        "s_bfe_u32 s70, s12, 524304\n\t"
        "s_bfe_u32 s71, s13, 524304\n\t"
        "v_cmp_gt_u32 vcc, s71, v71\n\t"
        "v_add3_u32 v73, v71, s70, -2\n\t"
        "v_med3_i32 v73, v73, 0, s10\n\t"
        "v_cndmask_b32 v74, v75, v72, vcc\n\t"
        "v_mad_u32_u24 v136, v73, s11, v74\n\t"
        "s_bfe_u32 s70, s12, 524312\n\t"
        "s_bfe_u32 s71, s13, 524312\n\t"
        "v_cmp_gt_u32 vcc, s71, v71\n\t"
        "v_add3_u32 v73, v71, s70, -2\n\t"
        "v_med3_i32 v73, v73, 0, s10\n\t"
        "v_cndmask_b32 v74, v75, v72, vcc\n\t"
        "v_mad_u32_u24 v137, v73, s11, v74\n\t"
        "s_mov_b64 exec, 0\n\t"
        "s_cmp_eq_u32 s4, 0\n\t"
        "s_cbranch_scc1 .LpX0%=\n\t"
        "s_cmp_eq_u32 s4, 1\n\t"
        "s_cbranch_scc1 .LpX1%=\n\t"
        "s_cmp_eq_u32 s4, 2\n\t"
        "s_cbranch_scc1 .LpX2%=\n\t"
        "s_branch .LwX%=\n\t"
        ".LrX%=:\n\t"
        "s_mov_b64 exec, -1\n\t"
        "s_waitcnt vmcnt(9)\n\t"
        "s_barrier\n\t"
        "ds_read_b64_tr_b16 a[0:1], v138 offset:6144\n\t"
        "ds_read_b64_tr_b16 a[2:3], v138 offset:6912\n\t"
        "ds_read_b64_tr_b16 a[4:5], v139 offset:6144\n\t"
        "ds_read_b64_tr_b16 a[6:7], v139 offset:6912\n\t"
        "ds_read_b64_tr_b16 a[8:9], v149 offset:6144\n\t"
        "ds_read_b64_tr_b16 a[10:11], v149 offset:6912\n\t"
        "ds_read_b128 v[84:87], v128 offset:8192\n\t"
        "ds_read_b64_tr_b16 a[12:13], v138 offset:12288\n\t"
        "ds_read_b64_tr_b16 a[14:15], v138 offset:13056\n\t"
        "ds_read_b64_tr_b16 a[16:17], v139 offset:12288\n\t"
        "ds_read_b64_tr_b16 a[18:19], v139 offset:13056\n\t"
        "ds_read_b64_tr_b16 a[20:21], v149 offset:12288\n\t"
        "ds_read_b64_tr_b16 a[22:23], v149 offset:13056\n\t"
        "ds_read_b128 v[88:91], v128 offset:8192\n\t"
        "s_add_u32 m0, s37, -12288\n\t"
        "s_add_u32 s36, s36, s39\n\t"
        "s_waitcnt lgkmcnt(7)\n\t"
        "v_mfma_f32_16x16x32_bf16 v[12:15], a[0:3], v[84:87], 0\n\t"
        "buffer_load_dwordx4 v140, s[24:27], s36 offen offset:0 lds\n\t"
        "buffer_load_dwordx4 v141, s[24:27], s36 offen offset:1024 lds\n\t"
        "buffer_load_dwordx4 v142, s[24:27], s36 offen offset:2048 lds\n\t"
        "s_add_u32 m0, s35, 16384\n\t"
        "s_add_u32 s43, s34, 172032\n\t"
        "buffer_load_dwordx4 v143, s[16:19], s43 offen  lds\n\t"
        "s_add_u32 m0, s35, 32768\n\t"
        "s_add_u32 s43, s34, 344064\n\t"
        "s_min_u32 s43, s43, 9590784\n\t"
        "buffer_load_dwordx4 v143, s[16:19], s43 offen  lds\n\t"
        "s_waitcnt vmcnt(10)\n\t"
        "s_barrier\n\t"
        "ds_read_b128 v[92:95], v128 offset:12288\n\t"
        "ds_read_b64_tr_b16 a[24:25], v138 offset:0\n\t"
        "v_mfma_f32_16x16x32_bf16 v[16:19], a[4:7], v[84:87], 0\n\t"
        "ds_read_b64_tr_b16 a[26:27], v138 offset:768\n\t"
        "s_add_u32 m0, s35, 157696\n\t"
        "s_nop 0\n\t"
        "buffer_load_dwordx4 v64, s[20:23], 0 offen lds\n\t"
        "s_add_u32 m0, s37, 0\n\t"
        "s_add_u32 s36, s36, s38\n\t"
        "v_mfma_f32_16x16x32_bf16 v[20:23], a[8:11], v[84:87], 0\n\t"
        "buffer_load_dwordx4 v140, s[24:27], s36 offen offset:0 lds\n\t"
        "ds_read_b64_tr_b16 a[28:29], v139 offset:0\n\t"
        "v_readlane_b32 s44, v155, 32\n\t"
        "s_waitcnt lgkmcnt(4)\n\t"
        "v_mfma_f32_16x16x32_bf16 v[24:27], a[12:15], v[88:91], 0\n\t"
        "ds_read_b64_tr_b16 a[30:31], v139 offset:768\n\t"
        "ds_read_b64_tr_b16 a[32:33], v149 offset:0\n\t"
        "ds_read_b64_tr_b16 a[34:35], v149 offset:768\n\t"
        "v_readlane_b32 s45, v155, 33\n\t"
        "v_mfma_f32_16x16x32_bf16 v[28:31], a[16:19], v[88:91], 0\n\t"
        "buffer_load_dwordx4 v141, s[24:27], s36 offen offset:1024 lds\n\t"
        "ds_read_b128 v[96:99], v128 offset:8192\n\t"
        "v_readlane_b32 s62, v155, 36\n\t"
        "v_mfma_f32_16x16x32_bf16 v[32:35], a[20:23], v[88:91], 0\n\t"
        "ds_read_b128 v[100:103], v128 offset:12288\n\t"
        "s_lshl_b32 s70, s62, 5\n\t"
        "s_waitcnt lgkmcnt(8)\n\t"
        "v_mfma_f32_16x16x32_bf16 v[24:27], a[0:3], v[92:95], v[24:27]\n\t"
        "s_sub_u32 s71, s70, 16384\n\t"
        "s_mul_i32 s71, s71, s68\n\t"
        "v_mfma_f32_16x16x32_bf16 v[28:31], a[4:7], v[92:95], v[28:31]\n\t"
        "buffer_load_dwordx4 v142, s[24:27], s36 offen offset:2048 lds\n\t"
        "s_add_u32 m0, s35, 0\n\t"
        "s_add_u32 s43, s34, 0\n\t"
        "v_mfma_f32_16x16x32_bf16 v[32:35], a[8:11], v[92:95], v[32:35]\n\t"
        "s_mov_b32 exec_lo, 0\n\t"
        "s_mov_b32 exec_hi, 0xfff00000\n\t"
        "ds_write_b32 v119, v155\n\t"
        "s_mov_b64 exec, -1\n\t"
        "v_mfma_f32_16x16x32_bf16 v[36:39], a[12:15], v[92:95], 0\n\t"
        "v_add_u32 v172, s71, v128\n\t"
        "v_add_u32 v173, s71, v129\n\t"
        "v_mfma_f32_16x16x32_bf16 v[40:43], a[16:19], v[92:95], 0\n\t"
        "buffer_load_dwordx4 v143, s[16:19], s43 offen  lds\n\t"
        "s_add_u32 m0, s35, 36864\n\t"
        "s_add_u32 s43, s34, 387072\n\t"
        "s_min_u32 s43, s43, 9590784\n\t"
        "v_mfma_f32_16x16x32_bf16 v[44:47], a[20:23], v[92:95], 0\n\t"
        "buffer_load_dwordx4 v143, s[16:19], s43 offen  lds\n\t"
        "s_add_u32 m0, s37, 12288\n\t"
        "s_add_u32 s36, s36, s5\n\t"
        "buffer_load_dwordx4 v140, s[24:27], s36 offen offset:0 lds\n\t"
        "s_waitcnt vmcnt(9) lgkmcnt(3)\n\t"
        "s_barrier\n\t"
        "ds_read_b128 v[104:107], v128 offset:4096\n\t"
        "ds_read_b64_tr_b16 a[36:37], v138 offset:18432\n\t"
        "s_waitcnt lgkmcnt(3)\n\t"
        "v_mfma_f32_16x16x32_bf16 v[0:3], a[24:27], v[96:99], 0\n\t"
        "buffer_load_dwordx4 v141, s[24:27], s36 offen offset:1024 lds\n\t"
        "ds_read_b64_tr_b16 a[38:39], v138 offset:19200\n\t"
        "v_add_u32 v174, s71, v130\n\t"
        "v_mfma_f32_16x16x32_bf16 v[4:7], a[28:31], v[96:99], 0\n\t"
        "ds_read_b64_tr_b16 a[40:41], v139 offset:18432\n\t"
        "ds_read_b64_tr_b16 a[42:43], v139 offset:19200\n\t"
        "s_add_u32 s71, s71, s70\n\t"
        "v_mfma_f32_16x16x32_bf16 v[8:11], a[32:35], v[96:99], 0\n\t"
        "buffer_load_dwordx4 v142, s[24:27], s36 offen offset:2048 lds\n\t"
        "s_add_u32 m0, s37, 24576\n\t"
        "ds_read_b64_tr_b16 a[44:45], v149 offset:18432\n\t"
        "s_add_u32 s36, s36, s39\n\t"
        "v_mfma_f32_16x16x32_bf16 v[12:15], a[24:27], v[100:103], v[12:15]\n\t"
        "ds_read_b64_tr_b16 a[46:47], v149 offset:19200\n\t"
        "ds_read_b64_tr_b16 a[48:49], v138 offset:24576\n\t"
        "ds_read_b64_tr_b16 a[50:51], v138 offset:25344\n\t"
        "v_add_u32 v175, s71, v128\n\t"
        "v_mfma_f32_16x16x32_bf16 v[16:19], a[28:31], v[100:103], v[16:19]\n\t"
        "ds_read_b64_tr_b16 a[52:53], v139 offset:24576\n\t"
        "ds_read_b64_tr_b16 a[54:55], v139 offset:25344\n\t"
        "ds_read_b64_tr_b16 a[56:57], v149 offset:24576\n\t"
        "v_add_u32 v176, s71, v129\n\t"
        "v_mfma_f32_16x16x32_bf16 v[20:23], a[32:35], v[100:103], v[20:23]\n\t"
        "ds_read_b64_tr_b16 a[58:59], v149 offset:25344\n\t"
        "ds_read_b128 v[108:111], v128 offset:4160\n\t"
        "v_add_u32 v177, s71, v130\n\t"
        "s_add_u32 s71, s71, s70\n\t"
        "s_waitcnt lgkmcnt(12)\n\t"
        "v_mfma_f32_16x16x32_bf16 v[0:3], a[0:3], v[104:107], v[0:3]\n\t"
        "ds_read_b64_tr_b16 a[60:61], v138 offset:30720\n\t"
        "ds_read_b64_tr_b16 a[62:63], v138 offset:31488\n\t"
        "v_add_u32 v178, s71, v128\n\t"
        "v_mfma_f32_16x16x32_bf16 v[4:7], a[4:7], v[104:107], v[4:7]\n\t"
        "ds_read_b64_tr_b16 a[64:65], v139 offset:30720\n\t"
        "ds_read_b64_tr_b16 a[66:67], v139 offset:31488\n\t"
        "v_add_u32 v179, s71, v129\n\t"
        "v_mfma_f32_16x16x32_bf16 v[8:11], a[8:11], v[104:107], v[8:11]\n\t"
        "ds_read_b64_tr_b16 a[68:69], v149 offset:30720\n\t"
        "ds_read_b64_tr_b16 a[70:71], v149 offset:31488\n\t"
        "v_add_u32 v180, s71, v130\n\t"
        "v_mfma_f32_16x16x32_bf16 v[12:15], a[12:15], v[104:107], v[12:15]\n\t"
        "ds_read_b128 v[112:115], v128 offset:8256\n\t"
        "v_add_f32 v78, v78, v56\n\t"
        "v_mfma_f32_16x16x32_bf16 v[16:19], a[16:19], v[104:107], v[16:19]\n\t"
        "s_cmp_eq_u32 s69, 36\n\t"
        "s_cselect_b64 exec, -1, 0\n\t"
        "v_swap_b32 v68, v69\n\t"
        "v_swap_b32 v173, v175\n\t"
        "v_swap_b32 v174, v178\n\t"
        "v_swap_b32 v177, v179\n\t"
        "s_mov_b64 exec, -1\n\t"
        "v_mfma_f32_16x16x32_bf16 v[20:23], a[20:23], v[104:107], v[20:23]\n\t"
        "ds_read_b128 v[84:87], v128 offset:12352\n\t"
        "v_add3_u32 v68, v68, v52, -1\n\t"
        "v_add3_u32 v69, v69, v158, -1\n\t"
        "s_waitcnt lgkmcnt(8)\n\t"
        "v_mfma_f32_16x16x32_bf16 v[0:3], a[36:39], v[108:111], v[0:3]\n\t"
        "ds_read_b64_tr_b16 a[72:73], v138 offset:36864\n\t"
        "ds_read_b64_tr_b16 a[74:75], v138 offset:37632\n\t"
        "v_cvt_f32_i32 v68, v68\n\t"
        "v_mfma_f32_16x16x32_bf16 v[4:7], a[40:43], v[108:111], v[4:7]\n\t"
        "ds_read_b64_tr_b16 a[76:77], v139 offset:36864\n\t"
        "ds_read_b64_tr_b16 a[78:79], v139 offset:37632\n\t"
        "v_cvt_f32_i32 v69, v69\n\t"
        "v_mfma_f32_16x16x32_bf16 v[8:11], a[44:47], v[108:111], v[8:11]\n\t"
        "ds_read_b64_tr_b16 a[80:81], v149 offset:36864\n\t"
        "ds_read_b64_tr_b16 a[82:83], v149 offset:37632\n\t"
        "v_add_f32 v79, v79, v57\n\t"
        "v_mfma_f32_16x16x32_bf16 v[12:15], a[48:51], v[108:111], v[12:15]\n\t"
        "v_readlane_b32 s69, v155, 31\n\t"
        "v_mfma_f32_16x16x32_bf16 v[16:19], a[52:55], v[108:111], v[16:19]\n\t"
        "s_nop 0\n\t"
        "v_mov_b32 v154, s69\n\t"
        "v_mov_b32 v119, s62\n\t"
        "v_readlane_b32 s53, v155, 28\n\t"
        "v_mfma_f32_16x16x32_bf16 v[20:23], a[56:59], v[108:111], v[20:23]\n\t"
        "buffer_load_dwordx4 v140, s[24:27], s36 offen offset:0 lds\n\t"
        "v_readlane_b32 s9, v155, 46\n\t"
        "s_waitcnt lgkmcnt(7)\n\t"
        "v_mfma_f32_16x16x32_bf16 v[0:3], a[60:63], v[112:115], v[0:3]\n\t"
        "s_sub_u32 s10, 43200, s11\n\t"
        "s_mul_i32 s10, s10, s68\n\t"
        "v_mfma_f32_16x16x32_bf16 v[4:7], a[64:67], v[112:115], v[4:7]\n\t"
        "s_sub_u32 s11, s11, 192\n\t"
        "v_mad_u32_u24 v148, v158, s11, v144\n\t"
        "v_mfma_f32_16x16x32_bf16 v[8:11], a[68:71], v[112:115], v[8:11]\n\t"
        "v_add_u32 v148, s10, v148\n\t"
        "v_mad_u32_u24 v150, v83, s11, v145\n\t"
        "v_mfma_f32_16x16x32_bf16 v[12:15], a[36:39], v[112:115], v[12:15]\n\t"
        "s_lshl_b32 s70, s49, 1\n\t"
        "s_add_u32 s70, s70, 49\n\t"
        "v_mfma_f32_16x16x32_bf16 v[16:19], a[40:43], v[112:115], v[16:19]\n\t"
        "buffer_load_dwordx4 v141, s[24:27], s36 offen offset:1024 lds\n\t"
        "v_readlane_b32 s52, v155, s70\n\t"
        "v_mfma_f32_16x16x32_bf16 v[20:23], a[44:47], v[112:115], v[20:23]\n\t"
        "s_nop 0\n\t"
        "s_add_u32 s52, s52, s10\n\t"
        "v_mfma_f32_16x16x32_bf16 v[24:27], a[48:51], v[112:115], v[24:27]\n\t"
        "v_mfma_f32_16x16x32_bf16 v[28:31], a[52:55], v[112:115], v[28:31]\n\t"
        "v_mfma_f32_16x16x32_bf16 v[32:35], a[56:59], v[112:115], v[32:35]\n\t"
        "buffer_load_dwordx4 v142, s[24:27], s36 offen offset:2048 lds\n\t"
        "s_waitcnt lgkmcnt(0)\n\t"
        "v_mfma_f32_16x16x32_bf16 v[12:15], a[60:63], v[84:87], v[12:15]\n\t"
        "v_mfma_f32_16x16x32_bf16 v[16:19], a[64:67], v[84:87], v[16:19]\n\t"
        "v_mfma_f32_16x16x32_bf16 v[20:23], a[68:71], v[84:87], v[20:23]\n\t"
        "v_mfma_f32_16x16x32_bf16 v[24:27], a[36:39], v[84:87], v[24:27]\n\t"
        "buffer_load_dwordx4 v[48:51], v55, s[20:23], s48 offen\n\t"
        "ds_read_b128 v[88:91], v128 offset:4224\n\t"
        "v_mfma_f32_16x16x32_bf16 v[28:31], a[40:43], v[84:87], v[28:31]\n\t"
        "v_mfma_f32_16x16x32_bf16 v[32:35], a[44:47], v[84:87], v[32:35]\n\t"
        "v_mfma_f32_16x16x32_bf16 v[36:39], a[48:51], v[84:87], v[36:39]\n\t"
        "ds_read_b128 v[92:95], v128 offset:8320\n\t"
        "v_mfma_f32_16x16x32_bf16 v[40:43], a[52:55], v[84:87], v[40:43]\n\t"
        "buffer_load_dword v61, v61, s[20:23], s48 offen offset:512\n\t"
        "s_add_u32 m0, s37, 36864\n\t"
        "s_add_u32 s36, s36, s38\n\t"
        "v_mfma_f32_16x16x32_bf16 v[44:47], a[56:59], v[84:87], v[44:47]\n\t"
        "s_waitcnt lgkmcnt(1)\n\t"
        "v_mfma_f32_16x16x32_bf16 v[0:3], a[72:75], v[88:91], v[0:3]\n\t"
        "ds_read_b128 v[96:99], v128 offset:12416\n\t"
        "v_mfma_f32_16x16x32_bf16 v[4:7], a[76:79], v[88:91], v[4:7]\n\t"
        "s_waitcnt vmcnt(10)\n\t"
        "s_barrier\n\t"
        "ds_read_b128 v[100:103], v128 offset:16384\n\t"
        "ds_read_b128 v[104:107], v128 offset:16448\n\t"
        "v_mfma_f32_16x16x32_bf16 v[8:11], a[80:83], v[88:91], v[8:11]\n\t"
        "ds_read_b32 v64, v70 offset:0\n\t"
        "buffer_load_dwordx4 v140, s[24:27], s36 offen offset:0 lds\n\t"
        "v_readlane_b32 s46, v155, 37\n\t"
        "v_readlane_b32 s47, v155, 40\n\t"
        "s_waitcnt lgkmcnt(4)\n\t"
        "v_mfma_f32_16x16x32_bf16 v[12:15], a[72:75], v[92:95], v[12:15]\n\t"
        "ds_read_b32 v67, v67 offset:0\n\t"
        "ds_read_b64_tr_b16 a[84:85], v131 offset:0\n\t"
        "v_add_f32 v58, s46, v68\n\t"
        "v_mfma_f32_16x16x32_bf16 v[16:19], a[76:79], v[92:95], v[16:19]\n\t"
        "ds_read_b32 v66, v70 offset:2560\n\t"
        "ds_read_b64_tr_b16 a[86:87], v131 offset:768\n\t"
        "v_add_f32 v59, s47, v69\n\t"
        "v_readlane_b32 s47, v155, 42\n\t"
        "v_mfma_f32_16x16x32_bf16 v[20:23], a[80:83], v[92:95], v[20:23]\n\t"
        "ds_read_b64_tr_b16 a[88:89], v132 offset:0\n\t"
        "ds_read_b64_tr_b16 a[90:91], v132 offset:768\n\t"
        "s_waitcnt lgkmcnt(9)\n\t"
        "v_mfma_f32_16x16x32_bf16 v[24:27], a[72:75], v[96:99], v[24:27]\n\t"
        "buffer_load_dwordx4 v141, s[24:27], s36 offen offset:1024 lds\n\t"
        "ds_read_b64_tr_b16 a[92:93], v133 offset:0\n\t"
        "v_mfma_f32_16x16x32_bf16 v[28:31], a[76:79], v[96:99], v[28:31]\n\t"
        "ds_read_b64_tr_b16 a[94:95], v133 offset:768\n\t"
        "ds_read_b128 v[108:111], v128 offset:4224\n\t"
        "v_mfma_f32_16x16x32_bf16 v[32:35], a[80:83], v[96:99], v[32:35]\n\t"
        "ds_read_b64_tr_b16 a[96:97], v131 offset:6144\n\t"
        "ds_read_b64_tr_b16 a[98:99], v131 offset:6912\n\t"
        "s_waitcnt lgkmcnt(12)\n\t"
        "v_mfma_f32_16x16x32_bf16 v[24:27], a[24:27], v[100:103], v[24:27]\n\t"
        "ds_read_b64_tr_b16 a[100:101], v132 offset:6144\n\t"
        "ds_read_b64_tr_b16 a[102:103], v132 offset:6912\n\t"
        "v_mfma_f32_16x16x32_bf16 v[28:31], a[28:31], v[100:103], v[28:31]\n\t"
        "buffer_load_dwordx4 v142, s[24:27], s36 offen offset:2048 lds\n\t"
        "s_add_u32 m0, s37, 49152\n\t"
        "ds_read_b64_tr_b16 a[104:105], v133 offset:6144\n\t"
        "v_mfma_f32_16x16x32_bf16 v[32:35], a[32:35], v[100:103], v[32:35]\n\t"
        "ds_read_b64_tr_b16 a[106:107], v133 offset:6912\n\t"
        "s_add_u32 s36, s36, s5\n\t"
        "s_waitcnt lgkmcnt(12)\n\t"
        "v_perm_b32 v56, v64, v64, s44\n\t"
        "v_mfma_f32_16x16x32_bf16 v[36:39], a[0:3], v[100:103], v[36:39]\n\t"
        "ds_read_b128 v[112:115], v128 offset:8320\n\t"
        "v_perm_b32 v57, v64, v64, s45\n\t"
        "v_mfma_f32_16x16x32_bf16 v[40:43], a[4:7], v[100:103], v[40:43]\n\t"
        "ds_read_b128 v[84:87], v128 offset:12416\n\t"
        "v_add_f32 v56, v56, v58\n\t"
        "v_mfma_f32_16x16x32_bf16 v[44:47], a[8:11], v[100:103], v[44:47]\n\t"
        "buffer_load_dwordx4 v140, s[24:27], s36 offen offset:0 lds\n\t"
        "v_add_f32 v57, v57, v59\n\t"
        "v_cvt_pk_bf16_f32 v52, v56, v57\n\t"
        "v_mfma_f32_16x16x32_bf16 v[24:27], a[60:63], v[104:107], v[24:27]\n\t"
        "ds_read_b128 v[88:91], v128 offset:16512\n\t"
        "v_cvt_pk_bf16_f32 v58, v58, v59\n\t"
        "v_mfma_f32_16x16x32_bf16 v[28:31], a[64:67], v[104:107], v[28:31]\n\t"
        "v_cmp_ne_u32 s[64:65], v52, v58\n\t"
        "v_perm_b32 v57, v67, v67, s45\n\t"
        "v_mfma_f32_16x16x32_bf16 v[32:35], a[68:71], v[104:107], v[32:35]\n\t"
        "v_mbcnt_lo_u32_b32 v56, s64, 1\n\t"
        "v_mbcnt_hi_u32_b32 v56, s65, v56\n\t"
        "v_mfma_f32_16x16x32_bf16 v[36:39], a[36:39], v[104:107], v[36:39]\n\t"
        "buffer_load_dwordx4 v141, s[24:27], s36 offen offset:1024 lds\n\t"
        "s_bcnt1_i32_b64 s61, s[64:65]\n\t"
        "v_mfma_f32_16x16x32_bf16 v[40:43], a[40:43], v[104:107], v[40:43]\n\t"
        "v_add_u32 v54, s61, 1\n\t"
        "v_lshl_add_u32 v56, v56, 3, s60\n\t"
        "v_add_f32 v57, v57, v79\n\t"
        "v_mfma_f32_16x16x32_bf16 v[44:47], a[44:47], v[104:107], v[44:47]\n\t"
        "s_mov_b64 exec, s[64:65]\n\t"
        "ds_write_b64 v56, v[52:53]\n\t"
        "s_mov_b64 exec, -1\n\t"
        "s_waitcnt lgkmcnt(3)\n\t"
        "v_mfma_f32_16x16x32_bf16 v[12:15], a[84:87], v[108:111], v[12:15]\n\t"
        "v_swap_b32 v53, v80\n\t"
        "v_perm_b32 v56, v67, v67, s44\n\t"
        "v_mfma_f32_16x16x32_bf16 v[16:19], a[88:91], v[108:111], v[16:19]\n\t"
        "buffer_load_dwordx4 v142, s[24:27], s36 offen offset:2048 lds\n\t"
        "v_readlane_b32 s40, v155, 12\n\t"
        "v_mfma_f32_16x16x32_bf16 v[20:23], a[92:95], v[108:111], v[20:23]\n\t"
        "v_readlane_b32 s43, v155, 0\n\t"
        "s_add_u32 m0, s40, s35\n\t"
        "v_mfma_f32_16x16x32_bf16 v[0:3], a[96:99], v[112:115], v[0:3]\n\t"
        "v_add_f32 v56, v56, v78\n\t"
        "v_cvt_pk_bf16_f32 v52, v56, v57\n\t"
        "v_mfma_f32_16x16x32_bf16 v[4:7], a[100:103], v[112:115], v[4:7]\n\t"
        "v_cvt_pk_bf16_f32 v58, v78, v79\n\t"
        "v_cmp_ne_u32 s[64:65], v52, v58\n\t"
        "v_perm_b32 v57, v66, v66, s45\n\t"
        "v_mfma_f32_16x16x32_bf16 v[8:11], a[104:107], v[112:115], v[8:11]\n\t"
        "buffer_load_dwordx4 v134, s[16:19], s43 offen  lds\n\t"
        "v_readlane_b32 s40, v155, 13\n\t"
        "v_mfma_f32_16x16x32_bf16 v[24:27], a[84:87], v[112:115], v[24:27]\n\t"
        "v_readlane_b32 s43, v155, 1\n\t"
        "s_add_u32 m0, s40, s35\n\t"
        "v_mfma_f32_16x16x32_bf16 v[28:31], a[88:91], v[112:115], v[28:31]\n\t"
        "s_waitcnt vmcnt(9)\n\t"
        "s_barrier\n\t"
        "v_mfma_f32_16x16x32_bf16 v[32:35], a[92:95], v[112:115], v[32:35]\n\t"
        "ds_read_b128 v[92:95], v128 offset:0\n\t"
        "s_and_b64 s[64:65], s[64:65], 0xffffff\n\t"
        "s_waitcnt lgkmcnt(2)\n\t"
        "v_mfma_f32_16x16x32_bf16 v[12:15], a[96:99], v[84:87], v[12:15]\n\t"
        "buffer_load_dwordx4 v135, s[16:19], s43 offen  lds\n\t"
        "ds_read_b128 v[96:99], v128 offset:64\n\t"
        "v_readlane_b32 s40, v155, 14\n\t"
        "v_mfma_f32_16x16x32_bf16 v[16:19], a[100:103], v[84:87], v[16:19]\n\t"
        "ds_read_b128 v[100:103], v128 offset:128\n\t"
        "ds_read_b128 v[104:107], v128 offset:20480\n\t"
        "v_readlane_b32 s43, v155, 2\n\t"
        "v_mfma_f32_16x16x32_bf16 v[20:23], a[104:107], v[84:87], v[20:23]\n\t"
        "ds_read_b64_tr_b16 a[108:109], v131 offset:12288\n\t"
        "s_add_u32 m0, s40, s35\n\t"
        "ds_read_b64_tr_b16 a[110:111], v131 offset:13056\n\t"
        "v_mfma_f32_16x16x32_bf16 v[36:39], a[84:87], v[84:87], v[36:39]\n\t"
        "ds_read_b128 v[108:111], v128 offset:20544\n\t"
        "ds_read_b64_tr_b16 a[112:113], v132 offset:12288\n\t"
        "v_mbcnt_lo_u32_b32 v56, s64, v54\n\t"
        "s_bcnt1_i32_b64 s46, s[64:65]\n\t"
        "v_mfma_f32_16x16x32_bf16 v[40:43], a[88:91], v[84:87], v[40:43]\n\t"
        "buffer_load_dwordx4 v134, s[16:19], s43 offen  lds\n\t"
        "ds_read_b64_tr_b16 a[114:115], v132 offset:13056\n\t"
        "s_bitcmp1_b32 s42, 10\n\t"
        "s_cbranch_scc0 .Lx62%=\n\t"
        "v_readlane_b32 s40, v155, 20\n\t"
        "v_readlane_b32 s43, v155, 8\n\t"
        "s_nop 0\n\t"
        "s_add_u32 m0, s40, s35\n\t"
        "s_nop 2\n\t"
        "buffer_load_dwordx4 v136, s[16:19], s43 offen  lds\n\t"
        ".Lx62%=:\n\t"
        "v_mfma_f32_16x16x32_bf16 v[44:47], a[92:95], v[84:87], v[44:47]\n\t"
        "ds_read_b64_tr_b16 a[116:117], v133 offset:12288\n\t"
        "ds_read_b64_tr_b16 a[118:119], v133 offset:13056\n\t"
        "v_readlane_b32 s40, v155, 15\n\t"
        "v_mfma_f32_16x16x32_bf16 v[24:27], a[96:99], v[88:91], v[24:27]\n\t"
        "ds_read_b128 v[112:115], v128 offset:20608\n\t"
        "v_readlane_b32 s43, v155, 3\n\t"
        "v_mfma_f32_16x16x32_bf16 v[28:31], a[100:103], v[88:91], v[28:31]\n\t"
        "ds_read_b64_tr_b16 a[120:121], v131 offset:18432\n\t"
        "s_add_u32 m0, s40, s35\n\t"
        "v_mfma_f32_16x16x32_bf16 v[32:35], a[104:107], v[88:91], v[32:35]\n\t"
        "ds_read_b64_tr_b16 a[122:123], v131 offset:19200\n\t"
        "s_add_u32 s61, s61, s46\n\t"
        "v_mfma_f32_16x16x32_bf16 v[36:39], a[72:75], v[88:91], v[36:39]\n\t"
        "ds_read_b64_tr_b16 a[124:125], v132 offset:18432\n\t"
        "v_add_u32 v54, s46, v54\n\t"
        "v_mfma_f32_16x16x32_bf16 v[40:43], a[76:79], v[88:91], v[40:43]\n\t"
        "ds_read_b64_tr_b16 a[126:127], v132 offset:19200\n\t"
        "s_mov_b32 s3, s61\n\t"
        "v_mfma_f32_16x16x32_bf16 v[44:47], a[80:83], v[88:91], v[44:47]\n\t"
        "ds_read_b64_tr_b16 a[128:129], v133 offset:18432\n\t"
        "v_lshl_add_u32 v56, v56, 3, s60\n\t"
        "v_readlane_b32 s46, v155, 39\n\t"
        "s_waitcnt lgkmcnt(9)\n\t"
        "v_mfma_f32_16x16x32_bf16 v[0:3], a[12:15], v[92:95], v[0:3]\n\t"
        "s_mov_b64 exec, s[64:65]\n\t"
        "ds_write_b64 v56, v[52:53]\n\t"
        "s_mov_b64 exec, -1\n\t"
        "ds_read_b64_tr_b16 a[130:131], v133 offset:19200\n\t"
        "v_mfma_f32_16x16x32_bf16 v[4:7], a[16:19], v[92:95], v[4:7]\n\t"
        "ds_read_b128 v[84:87], v129 offset:0\n\t"
        "v_add_u32 v53, 4096, v80\n\t"
        "v_mfma_f32_16x16x32_bf16 v[8:11], a[20:23], v[92:95], v[8:11]\n\t"
        "ds_read_b128 v[88:91], v129 offset:4096\n\t"
        "v_perm_b32 v56, v66, v66, s44\n\t"
        "v_mfma_f32_16x16x32_bf16 v[0:3], a[48:51], v[96:99], v[0:3]\n\t"
        "ds_read_b64_tr_b16 a[132:133], v131 offset:24576\n\t"
        "v_add_f32 v58, s46, v68\n\t"
        "v_mfma_f32_16x16x32_bf16 v[4:7], a[52:55], v[96:99], v[4:7]\n\t"
        "buffer_load_dwordx4 v135, s[16:19], s43 offen  lds\n\t"
        "s_add_u32 m0, s37, 0\n\t"
        "v_mfma_f32_16x16x32_bf16 v[8:11], a[56:59], v[96:99], v[8:11]\n\t"
        "ds_read_b64_tr_b16 a[134:135], v131 offset:25344\n\t"
        "s_add_u32 s36, s36, s39\n\t"
        "v_mfma_f32_16x16x32_bf16 v[0:3], a[84:87], v[100:103], v[0:3]\n\t"
        "ds_read_b64_tr_b16 a[136:137], v132 offset:24576\n\t"
        "v_add_f32 v59, s47, v69\n\t"
        "v_mfma_f32_16x16x32_bf16 v[4:7], a[88:91], v[100:103], v[4:7]\n\t"
        "ds_read_b64_tr_b16 a[138:139], v132 offset:25344\n\t"
        "v_add_f32 v56, v56, v58\n\t"
        "v_add_f32 v57, v57, v59\n\t"
        "v_mfma_f32_16x16x32_bf16 v[8:11], a[92:95], v[100:103], v[8:11]\n\t"
        "ds_read_b64_tr_b16 a[140:141], v133 offset:24576\n\t"
        "v_max_f32 v56, 0, v56\n\t"
        "v_mfma_f32_16x16x32_bf16 v[36:39], a[24:27], v[104:107], v[36:39]\n\t"
        "buffer_load_dwordx4 v140, s[24:27], s36 offen offset:0 lds\n\t"
        "v_max_f32 v57, 0, v57\n\t"
        "v_mfma_f32_16x16x32_bf16 v[40:43], a[28:31], v[104:107], v[40:43]\n\t"
        "ds_read_b64_tr_b16 a[142:143], v133 offset:25344\n\t"
        "v_max_f32 v58, 0, v58\n\t"
        "v_mfma_f32_16x16x32_bf16 v[44:47], a[32:35], v[104:107], v[44:47]\n\t"
        "ds_read_b128 v[92:95], v129 offset:8192\n\t"
        "v_max_f32 v59, 0, v59\n\t"
        "v_mfma_f32_16x16x32_bf16 v[36:39], a[60:63], v[108:111], v[36:39]\n\t"
        "ds_read_b64_tr_b16 a[144:145], v131 offset:30720\n\t"
        "v_cvt_pk_bf16_f32 v52, v56, v57\n\t"
        "v_mfma_f32_16x16x32_bf16 v[40:43], a[64:67], v[108:111], v[40:43]\n\t"
        "buffer_load_dwordx4 v141, s[24:27], s36 offen offset:1024 lds\n\t"
        "v_cvt_pk_bf16_f32 v58, v58, v59\n\t"
        "v_mfma_f32_16x16x32_bf16 v[44:47], a[68:71], v[108:111], v[44:47]\n\t"
        "ds_read_b64_tr_b16 a[146:147], v131 offset:31488\n\t"
        "v_cmp_ne_u32 s[64:65], v52, v58\n\t"
        "s_waitcnt lgkmcnt(7)\n\t"
        "v_mfma_f32_16x16x32_bf16 v[36:39], a[96:99], v[112:115], v[36:39]\n\t"
        "ds_read_b128 v[96:99], v129 offset:12288\n\t"
        ".LpX0%=:\n\t"
        "s_cbranch_execz .LrX%=\n\t"
        "v_mbcnt_lo_u32_b32 v56, s64, v54\n\t"
        "v_mfma_f32_16x16x32_bf16 v[40:43], a[100:103], v[112:115], v[40:43]\n\t"
        "ds_read_b64_tr_b16 a[148:149], v132 offset:30720\n\t"
        "v_mbcnt_hi_u32_b32 v56, s65, v56\n\t"
        "v_mfma_f32_16x16x32_bf16 v[44:47], a[104:107], v[112:115], v[44:47]\n\t"
        "buffer_load_dwordx4 v142, s[24:27], s36 offen offset:2048 lds\n\t"
        "s_add_u32 m0, s37, 12288\n\t"
        "v_mfma_f32_16x16x32_bf16 v[0:3], a[120:123], v[84:87], v[0:3]\n\t"
        "ds_read_b64_tr_b16 a[150:151], v132 offset:31488\n\t"
        "s_add_u32 s36, s36, s38\n\t"
        "s_bcnt1_i32_b64 s46, s[64:65]\n\t"
        "v_mfma_f32_16x16x32_bf16 v[4:7], a[124:127], v[84:87], v[4:7]\n\t"
        "ds_read_b64_tr_b16 a[152:153], v133 offset:30720\n\t"
        "s_add_u32 s61, s61, s46\n\t"
        "v_mfma_f32_16x16x32_bf16 v[8:11], a[128:131], v[84:87], v[8:11]\n\t"
        "ds_read_b64_tr_b16 a[154:155], v133 offset:31488\n\t"
        "v_min_u32 v56, 97, v56\n\t"
        "v_mfma_f32_16x16x32_bf16 v[0:3], a[108:111], v[88:91], v[0:3]\n\t"
        "v_lshl_add_u32 v56, v56, 3, s60\n\t"
        "v_mfma_f32_16x16x32_bf16 v[4:7], a[112:115], v[88:91], v[4:7]\n\t"
        "s_mov_b64 exec, s[64:65]\n\t"
        "ds_write_b64 v56, v[52:53]\n\t"
        "s_mov_b64 exec, -1\n\t"
        "v_mfma_f32_16x16x32_bf16 v[8:11], a[116:119], v[88:91], v[8:11]\n\t"
        "s_cmp_gt_u32 s61, s53\n\t"
        "s_cselect_b32 s8, 1, 0\n\t"
        "v_mfma_f32_16x16x32_bf16 v[12:15], a[120:123], v[88:91], v[12:15]\n\t"
        "ds_read_b128 v[100:103], v129 offset:16384\n\t"
        "s_waitcnt vmcnt(14)\n\t"
        "v_and_b32 v48, s54, v48\n\t"
        "v_mfma_f32_16x16x32_bf16 v[16:19], a[124:127], v[88:91], v[16:19]\n\t"
        "v_pk_max_u16 v60, v154, v48\n\t"
        "v_and_b32 v49, s54, v49\n\t"
        "v_mfma_f32_16x16x32_bf16 v[20:23], a[128:131], v[88:91], v[20:23]\n\t"
        "v_pk_max_u16 v60, v60, v49\n\t"
        "v_and_b32 v50, s54, v50\n\t"
        "s_waitcnt lgkmcnt(5)\n\t"
        "v_mfma_f32_16x16x32_bf16 v[0:3], a[132:135], v[92:95], v[0:3]\n\t"
        "v_pk_max_u16 v60, v60, v50\n\t"
        "v_and_b32 v51, s54, v51\n\t"
        "v_mfma_f32_16x16x32_bf16 v[4:7], a[136:139], v[92:95], v[4:7]\n\t"
        "v_pk_max_u16 v60, v60, v51\n\t"
        "s_waitcnt vmcnt(13)\n\t"
        "v_and_b32 v61, s54, v61\n\t"
        "v_mfma_f32_16x16x32_bf16 v[8:11], a[140:143], v[92:95], v[8:11]\n\t"
        "v_pk_max_u16 v60, v60, v61\n\t"
        "v_cmp_ne_u32 s[12:13], v60, v154\n\t"
        "v_mfma_f32_16x16x32_bf16 v[12:15], a[108:111], v[92:95], v[12:15]\n\t"
        "ds_read_b128 v[104:107], v129 offset:20480\n\t"
        "s_lshl_b32 s6, 256, s4\n\t"
        "v_mfma_f32_16x16x32_bf16 v[16:19], a[112:115], v[92:95], v[16:19]\n\t"
        "s_cmp_lg_u64 s[12:13], 0\n\t"
        "s_cselect_b32 s6, s6, 0\n\t"
        "s_or_b32 s56, s56, s8\n\t"
        "v_mfma_f32_16x16x32_bf16 v[20:23], a[116:119], v[92:95], v[20:23]\n\t"
        "s_waitcnt vmcnt(7) lgkmcnt(3)\n\t"
        "s_barrier\n\t"
        "v_mfma_f32_16x16x32_bf16 v[24:27], a[120:123], v[92:95], v[24:27]\n\t"
        "s_cmp_gt_u32 s3, 64\n\t"
        "s_cselect_b32 s8, 1, 0\n\t"
        "v_mfma_f32_16x16x32_bf16 v[28:31], a[124:127], v[92:95], v[28:31]\n\t"
        "buffer_load_dwordx4 v140, s[24:27], s36 offen offset:0 lds\n\t"
        "ds_read_b64_tr_b16 a[156:157], v131 offset:36864\n\t"
        "s_or_b32 s56, s56, s8\n\t"
        "v_mfma_f32_16x16x32_bf16 v[32:35], a[128:131], v[92:95], v[32:35]\n\t"
        "ds_read_b64_tr_b16 a[158:159], v131 offset:37632\n\t"
        "s_min_u32 s61, s61, 80\n\t"
        "v_mfma_f32_16x16x32_bf16 v[12:15], a[132:135], v[96:99], v[12:15]\n\t"
        "ds_read_b64_tr_b16 a[160:161], v132 offset:36864\n\t"
        "ds_read_b64_tr_b16 a[162:163], v132 offset:37632\n\t"
        "v_min_u32 v78, s61, v116\n\t"
        "v_mfma_f32_16x16x32_bf16 v[16:19], a[136:139], v[96:99], v[16:19]\n\t"
        "ds_read_b64_tr_b16 a[164:165], v133 offset:36864\n\t"
        "ds_read_b64_tr_b16 a[166:167], v133 offset:37632\n\t"
        "v_lshl_add_u32 v78, v78, 3, s60\n\t"
        "v_mfma_f32_16x16x32_bf16 v[20:23], a[140:143], v[96:99], v[20:23]\n\t"
        "buffer_load_dwordx4 v141, s[24:27], s36 offen offset:1024 lds\n\t"
        "v_cmp_ge_u32 s[66:67], s61, v116\n\t"
        "v_mfma_f32_16x16x32_bf16 v[24:27], a[108:111], v[96:99], v[24:27]\n\t"
        "ds_read_b128 v[108:111], v129 offset:64\n\t"
        "s_mov_b32 s46, 0x07060302\n\t"
        "v_mfma_f32_16x16x32_bf16 v[28:31], a[112:115], v[96:99], v[28:31]\n\t"
        "ds_read_b64 v[62:63], v78\n\t"
        "v_mfma_f32_16x16x32_bf16 v[32:35], a[116:119], v[96:99], v[32:35]\n\t"
        "ds_read_b128 v[112:115], v129 offset:4160\n\t"
        "v_mfma_f32_16x16x32_bf16 v[36:39], a[120:123], v[96:99], v[36:39]\n\t"
        "buffer_load_dwordx4 v142, s[24:27], s36 offen offset:2048 lds\n\t"
        "v_readlane_b32 s40, v155, 16\n\t"
        "v_mfma_f32_16x16x32_bf16 v[40:43], a[124:127], v[96:99], v[40:43]\n\t"
        "ds_read_b64_tr_b16 a[168:169], v131 offset:43008\n\t"
        "v_readlane_b32 s43, v155, 4\n\t"
        "v_mfma_f32_16x16x32_bf16 v[44:47], a[128:131], v[96:99], v[44:47]\n\t"
        "ds_read_b64_tr_b16 a[170:171], v131 offset:43776\n\t"
        "s_add_u32 m0, s40, s35\n\t"
        "s_waitcnt lgkmcnt(9)\n\t"
        "v_mfma_f32_16x16x32_bf16 v[24:27], a[132:135], v[100:103], v[24:27]\n\t"
        "ds_read_b64_tr_b16 a[172:173], v132 offset:43008\n\t"
        "v_mfma_f32_16x16x32_bf16 v[28:31], a[136:139], v[100:103], v[28:31]\n\t"
        "buffer_load_dwordx4 v134, s[16:19], s43 offen  lds\n\t"
        "s_bitcmp1_b32 s42, 11\n\t"
        "s_cbranch_scc0 .Lx64%=\n\t"
        "v_readlane_b32 s40, v155, 21\n\t"
        "v_readlane_b32 s43, v155, 9\n\t"
        "s_nop 0\n\t"
        "s_add_u32 m0, s40, s35\n\t"
        "s_nop 2\n\t"
        "buffer_load_dwordx4 v137, s[16:19], s43 offen  lds\n\t"
        "v_readlane_b32 s40, v155, 22\n\t"
        "v_readlane_b32 s43, v155, 10\n\t"
        "s_nop 0\n\t"
        "s_add_u32 m0, s40, s35\n\t"
        "s_nop 2\n\t"
        "buffer_load_dwordx4 v136, s[16:19], s43 offen  lds\n\t"
        ".Lx64%=:\n\t"
        "v_mfma_f32_16x16x32_bf16 v[32:35], a[140:143], v[100:103], v[32:35]\n\t"
        "ds_read_b64_tr_b16 a[174:175], v132 offset:43776\n\t"
        "v_readlane_b32 s40, v155, 17\n\t"
        "v_mfma_f32_16x16x32_bf16 v[36:39], a[108:111], v[100:103], v[36:39]\n\t"
        "ds_read_b64_tr_b16 a[176:177], v133 offset:43008\n\t"
        "ds_read_b64_tr_b16 a[178:179], v133 offset:43776\n\t"
        "v_readlane_b32 s43, v155, 5\n\t"
        "v_mfma_f32_16x16x32_bf16 v[40:43], a[112:115], v[100:103], v[40:43]\n\t"
        "ds_read_b128 v[84:87], v129 offset:8256\n\t"
        "s_add_u32 m0, s40, s35\n\t"
        "v_mfma_f32_16x16x32_bf16 v[44:47], a[116:119], v[100:103], v[44:47]\n\t"
        "s_mov_b64 exec, s[66:67]\n\t"
        "s_waitcnt lgkmcnt(7)\n\t"
        "ds_write_b32 v63, v117\n\t"
        "s_mov_b64 exec, -1\n\t"
        "v_mfma_f32_16x16x32_bf16 v[36:39], a[132:135], v[104:107], v[36:39]\n\t"
        "buffer_load_dwordx4 v135, s[16:19], s43 offen  lds\n\t"
        "v_readlane_b32 s40, v155, 18\n\t"
        "v_mfma_f32_16x16x32_bf16 v[40:43], a[136:139], v[104:107], v[40:43]\n\t"
        "ds_read_b128 v[88:91], v129 offset:12352\n\t"
        "v_readlane_b32 s43, v155, 6\n\t"
        "v_mfma_f32_16x16x32_bf16 v[44:47], a[140:143], v[104:107], v[44:47]\n\t"
        "ds_read_b64_tr_b16 a[180:181], v131 offset:49152\n\t"
        "s_add_u32 m0, s40, s35\n\t"
        "v_mfma_f32_16x16x32_bf16 v[0:3], a[156:159], v[108:111], v[0:3]\n\t"
        "ds_read_b64_tr_b16 a[182:183], v131 offset:49920\n\t"
        "v_and_b32 v78, 0xfffff800, v63\n\t"
        "v_mfma_f32_16x16x32_bf16 v[4:7], a[160:163], v[108:111], v[4:7]\n\t"
        "ds_read_b128 v[64:67], v78 offset:1536\n\t"
        "v_lshlrev_b32 v72, 16, v62\n\t"
        "v_mfma_f32_16x16x32_bf16 v[8:11], a[164:167], v[108:111], v[8:11]\n\t"
        "buffer_load_dwordx4 v136, s[16:19], s43 offen  lds\n\t"
        "s_bitcmp1_b32 s42, 11\n\t"
        "s_cbranch_scc0 .Lx66%=\n\t"
        "v_readlane_b32 s40, v155, 23\n\t"
        "v_readlane_b32 s43, v155, 11\n\t"
        "s_nop 0\n\t"
        "s_add_u32 m0, s40, s35\n\t"
        "s_nop 2\n\t"
        "buffer_load_dwordx4 v137, s[16:19], s43 offen  lds\n\t"
        ".Lx66%=:\n\t"
        "v_mfma_f32_16x16x32_bf16 v[0:3], a[144:147], v[112:115], v[0:3]\n\t"
        "ds_read_b64_tr_b16 a[184:185], v132 offset:49152\n\t"
        "v_readlane_b32 s40, v155, 19\n\t"
        "v_mfma_f32_16x16x32_bf16 v[4:7], a[148:151], v[112:115], v[4:7]\n\t"
        "ds_read_b64_tr_b16 a[186:187], v132 offset:49920\n\t"
        "v_readlane_b32 s43, v155, 7\n\t"
        "v_mfma_f32_16x16x32_bf16 v[8:11], a[152:155], v[112:115], v[8:11]\n\t"
        "ds_read_b64_tr_b16 a[188:189], v133 offset:49152\n\t"
        "s_add_u32 m0, s40, s35\n\t"
        "v_mfma_f32_16x16x32_bf16 v[12:15], a[156:159], v[112:115], v[12:15]\n\t"
        "s_nop 0\n\t"
        "buffer_load_dwordx4 v137, s[16:19], s43 offen  lds\n\t"
        "s_add_u32 m0, s37, 24576\n\t"
        "ds_read_b128 v[92:95], v129 offset:16448\n\t"
        "v_mfma_f32_16x16x32_bf16 v[16:19], a[160:163], v[112:115], v[16:19]\n\t"
        "ds_read_b64_tr_b16 a[190:191], v133 offset:49920\n\t"
        "s_add_u32 s36, s36, s5\n\t"
        "v_mfma_f32_16x16x32_bf16 v[20:23], a[164:167], v[112:115], v[20:23]\n\t"
        "ds_read_b64_tr_b16 a[192:193], v131 offset:55296\n\t"
        "v_and_b32 v73, 0xffff0000, v62\n\t"
        "v_med3_f32 v72, v72, 0, s63\n\t"
        "s_waitcnt lgkmcnt(7)\n\t"
        "v_mfma_f32_16x16x32_bf16 v[0:3], a[168:171], v[84:87], v[0:3]\n\t"
        "ds_read_b64_tr_b16 a[194:195], v131 offset:56064\n\t"
        "v_med3_f32 v73, v73, 0, s63\n\t"
        "v_mfma_f32_16x16x32_bf16 v[4:7], a[172:175], v[84:87], v[4:7]\n\t"
        "buffer_load_dwordx4 v140, s[24:27], s36 offen offset:0 lds\n\t"
        "v_perm_b32 v62, v73, v72, s46\n\t"
        "v_mfma_f32_16x16x32_bf16 v[8:11], a[176:179], v[84:87], v[8:11]\n\t"
        "ds_read_b64_tr_b16 a[196:197], v132 offset:55296\n\t"
        "v_fract_f32 v74, v72\n\t"
        "v_mfma_f32_16x16x32_bf16 v[12:15], a[144:147], v[84:87], v[12:15]\n\t"
        "ds_read_b128 v[96:99], v129 offset:20544\n\t"
        "v_fract_f32 v75, v73\n\t"
        "v_mfma_f32_16x16x32_bf16 v[16:19], a[148:151], v[84:87], v[16:19]\n\t"
        "ds_read_b64_tr_b16 a[198:199], v132 offset:56064\n\t"
        "v_cvt_u32_f32 v76, v72\n\t"
        "v_mfma_f32_16x16x32_bf16 v[20:23], a[152:155], v[84:87], v[20:23]\n\t"
        "buffer_load_dwordx4 v141, s[24:27], s36 offen offset:1024 lds\n\t"
        "v_cvt_u32_f32 v77, v73\n\t"
        "v_mfma_f32_16x16x32_bf16 v[24:27], a[156:159], v[84:87], v[24:27]\n\t"
        "ds_read_b64_tr_b16 a[200:201], v133 offset:55296\n\t"
        "v_cmp_neq_f32 s[64:65], 0, v74\n\t"
        "v_mfma_f32_16x16x32_bf16 v[28:31], a[160:163], v[84:87], v[28:31]\n\t"
        "ds_read_b64_tr_b16 a[202:203], v133 offset:56064\n\t"
        "v_cmp_neq_f32 s[44:45], 0, v75\n\t"
        "v_mfma_f32_16x16x32_bf16 v[32:35], a[164:167], v[84:87], v[32:35]\n\t"
        "ds_read_b128 v[100:103], v129 offset:128\n\t"
        "v_sub_f32 v78, 1.0, v74\n\t"
        "v_mfma_f32_16x16x32_bf16 v[12:15], a[168:171], v[88:91], v[12:15]\n\t"
        "buffer_load_dwordx4 v142, s[24:27], s36 offen offset:2048 lds\n\t"
        "s_add_u32 m0, s37, 36864\n\t"
        "v_mfma_f32_16x16x32_bf16 v[16:19], a[172:175], v[88:91], v[16:19]\n\t"
        "ds_read_b128 v[104:107], v129 offset:4224\n\t"
        "s_add_u32 s36, s36, s39\n\t"
        "v_mfma_f32_16x16x32_bf16 v[20:23], a[176:179], v[88:91], v[20:23]\n\t"
        "v_sub_f32 v79, 1.0, v75\n\t"
        "v_cvt_pk_bf16_f32 v79, v78, v79\n\t"
        "v_mfma_f32_16x16x32_bf16 v[24:27], a[144:147], v[88:91], v[24:27]\n\t"
        "s_waitcnt vmcnt(10)\n\t"
        "s_barrier\n\t"
        "v_mfma_f32_16x16x32_bf16 v[28:31], a[148:151], v[88:91], v[28:31]\n\t"
        "buffer_load_dwordx4 v140, s[24:27], s36 offen offset:0 lds\n\t"
        "v_lshlrev_b32 v78, 16, v79\n\t"
        "v_mfma_f32_16x16x32_bf16 v[32:35], a[152:155], v[88:91], v[32:35]\n\t"
        "ds_read_b64_tr_b16 a[204:205], v131 offset:0\n\t"
        "v_and_b32 v79, 0xffff0000, v79\n\t"
        "v_mfma_f32_16x16x32_bf16 v[36:39], a[156:159], v[88:91], v[36:39]\n\t"
        "ds_read_b64_tr_b16 a[206:207], v131 offset:768\n\t"
        "v_mul_f32 v80, v78, v79\n\t"
        "v_mfma_f32_16x16x32_bf16 v[40:43], a[160:163], v[88:91], v[40:43]\n\t"
        "ds_read_b64_tr_b16 a[208:209], v132 offset:0\n\t"
        "v_mul_f32 v78, v78, v75\n\t"
        "v_mfma_f32_16x16x32_bf16 v[44:47], a[164:167], v[88:91], v[44:47]\n\t"
        "buffer_load_dwordx4 v141, s[24:27], s36 offen offset:1024 lds\n\t"
        "v_mul_f32 v79, v74, v79\n\t"
        "s_waitcnt lgkmcnt(5)\n\t"
        "v_mfma_f32_16x16x32_bf16 v[24:27], a[168:171], v[92:95], v[24:27]\n\t"
        "ds_read_b64_tr_b16 a[210:211], v132 offset:768\n\t"
        "v_mul_f32 v81, v74, v75\n\t"
        "v_mfma_f32_16x16x32_bf16 v[28:31], a[172:175], v[92:95], v[28:31]\n\t"
        "ds_read_b64_tr_b16 a[212:213], v133 offset:0\n\t"
        "v_cndmask_b32 v78, v79, v78, s[44:45]\n\t"
        "v_mfma_f32_16x16x32_bf16 v[32:35], a[176:179], v[92:95], v[32:35]\n\t"
        "ds_read_b64_tr_b16 a[214:215], v133 offset:768\n\t"
        "v_cndmask_b32 v79, 0, v79, s[44:45]\n\t"
        "v_mfma_f32_16x16x32_bf16 v[36:39], a[144:147], v[92:95], v[36:39]\n\t"
        "buffer_load_dwordx4 v142, s[24:27], s36 offen offset:2048 lds\n\t"
        "s_add_u32 m0, s37, 49152\n\t"
        "ds_read_b128 v[108:111], v129 offset:8320\n\t"
        "v_mfma_f32_16x16x32_bf16 v[40:43], a[148:151], v[92:95], v[40:43]\n\t"
        "ds_read_b64_tr_b16 a[216:217], v131 offset:6144\n\t"
        "s_add_u32 s36, s36, s38\n\t"
        "v_mfma_f32_16x16x32_bf16 v[44:47], a[152:155], v[92:95], v[44:47]\n\t"
        "ds_read_b64_tr_b16 a[218:219], v131 offset:6912\n\t"
        "v_cvt_pk_bf16_f32 v70, v80, v78\n\t"
        "v_mfma_f32_16x16x32_bf16 v[36:39], a[168:171], v[96:99], v[36:39]\n\t"
        "s_waitcnt vmcnt(10)\n\t"
        "s_barrier\n\t"
        "v_mfma_f32_16x16x32_bf16 v[40:43], a[172:175], v[96:99], v[40:43]\n\t"
        "buffer_load_dwordx4 v140, s[24:27], s36 offen offset:0 lds\n\t"
        "v_cvt_pk_bf16_f32 v71, v79, v81\n\t"
        "v_mfma_f32_16x16x32_bf16 v[44:47], a[176:179], v[96:99], v[44:47]\n\t"
        "ds_read_b128 v[112:115], v129 offset:12416\n\t"
        "v_pk_max_u16 v78, v62, v64\n\t"
        "s_waitcnt lgkmcnt(7)\n\t"
        "v_mfma_f32_16x16x32_bf16 v[0:3], a[192:195], v[100:103], v[0:3]\n\t"
        "ds_read_b64_tr_b16 a[220:221], v132 offset:6144\n\t"
        "v_pk_min_u16 v78, v78, v65\n\t"
        "v_mfma_f32_16x16x32_bf16 v[4:7], a[196:199], v[100:103], v[4:7]\n\t"
        "ds_read_b64_tr_b16 a[222:223], v132 offset:6912\n\t"
        "v_cmp_ne_u32 vcc, v78, v62\n\t"
        "v_mfma_f32_16x16x32_bf16 v[8:11], a[200:203], v[100:103], v[8:11]\n\t"
        "ds_read_b64_tr_b16 a[224:225], v133 offset:6144\n\t"
        "v_sub_u32 v78, v77, v67\n\t"
        "v_mfma_f32_16x16x32_bf16 v[0:3], a[180:183], v[104:107], v[0:3]\n\t"
        "ds_read_b64_tr_b16 a[226:227], v133 offset:6912\n\t"
        "v_mad_u32_u24 v79, v77, 6, v66\n\t"
        "v_mfma_f32_16x16x32_bf16 v[4:7], a[184:187], v[104:107], v[4:7]\n\t"
        "ds_read_b64_tr_b16 a[228:229], v131 offset:12288\n\t"
        "v_mad_u32_u24 v79, v76, s62, v79\n\t"
        "v_mfma_f32_16x16x32_bf16 v[8:11], a[188:191], v[104:107], v[8:11]\n\t"
        "ds_read_b64_tr_b16 a[230:231], v131 offset:13056\n\t"
        "s_and_b64 vcc, vcc, s[66:67]\n\t"
        "v_mfma_f32_16x16x32_bf16 v[12:15], a[192:195], v[104:107], v[12:15]\n\t"
        "ds_read_b128 v[84:87], v129 offset:16512\n\t"
        "s_or_b64 s[56:57], s[56:57], vcc\n\t"
        "v_mfma_f32_16x16x32_bf16 v[16:19], a[196:199], v[104:107], v[16:19]\n\t"
        "ds_read_b64_tr_b16 a[232:233], v132 offset:12288\n\t"
        "v_bfe_u32 v80, v78, 2, 1\n\t"
        "v_mfma_f32_16x16x32_bf16 v[20:23], a[200:203], v[104:107], v[20:23]\n\t"
        "ds_read_b64_tr_b16 a[234:235], v132 offset:13056\n\t"
        "v_or_b32 v80, v80, v79\n\t"
        "s_waitcnt lgkmcnt(10)\n\t"
        "v_mfma_f32_16x16x32_bf16 v[0:3], a[204:207], v[108:111], v[0:3]\n\t"
        "ds_read_b64_tr_b16 a[236:237], v133 offset:12288\n\t"
        "v_add_u32 v78, 1, v78\n\t"
        "v_mfma_f32_16x16x32_bf16 v[4:7], a[208:211], v[108:111], v[4:7]\n\t"
        "ds_read_b64_tr_b16 a[238:239], v133 offset:13056\n\t"
        "v_bfe_u32 v78, v78, 2, 1\n\t"
        "v_mfma_f32_16x16x32_bf16 v[8:11], a[212:215], v[108:111], v[8:11]\n\t"
        "ds_read_b64_tr_b16 a[240:241], v131 offset:18432\n\t"
        "v_add3_u32 v78, v79, v78, 6\n\t"
        "v_mfma_f32_16x16x32_bf16 v[12:15], a[180:183], v[108:111], v[12:15]\n\t"
        "ds_read_b128 v[88:91], v130 offset:0\n\t"
        "v_cndmask_b32 v78, v80, v78, s[44:45]\n\t"
        "v_mfma_f32_16x16x32_bf16 v[16:19], a[184:187], v[108:111], v[16:19]\n\t"
        "ds_read_b64_tr_b16 a[242:243], v131 offset:19200\n\t"
        "v_cndmask_b32 v79, 0, v119, s[64:65]\n\t"
        "v_mfma_f32_16x16x32_bf16 v[20:23], a[188:191], v[108:111], v[20:23]\n\t"
        "ds_read_b64_tr_b16 a[244:245], v132 offset:18432\n\t"
        "v_add_u32 v81, v80, v79\n\t"
        "v_mfma_f32_16x16x32_bf16 v[24:27], a[192:195], v[108:111], v[24:27]\n\t"
        "ds_read_b128 v[92:95], v130 offset:4096\n\t"
        "v_add_u32 v82, v78, v79\n\t"
        "v_mfma_f32_16x16x32_bf16 v[28:31], a[196:199], v[108:111], v[28:31]\n\t"
        "ds_read_b64_tr_b16 a[246:247], v132 offset:19200\n\t"
        "ds_read_b64_tr_b16 a[248:249], v133 offset:18432\n\t"
        "v_cndmask_b32 v78, v81, v78, s[44:45]\n\t"
        "v_mfma_f32_16x16x32_bf16 v[32:35], a[200:203], v[108:111], v[32:35]\n\t"
        "buffer_load_dwordx4 v141, s[24:27], s36 offen offset:1024 lds\n\t"
        "v_lshl_or_b32 v68, v78, 16, v80\n\t"
        "s_waitcnt lgkmcnt(9)\n\t"
        "v_mfma_f32_16x16x32_bf16 v[12:15], a[204:207], v[112:115], v[12:15]\n\t"
        "ds_read_b64_tr_b16 a[250:251], v133 offset:19200\n\t"
        "v_lshl_or_b32 v69, v82, 16, v81\n\t"
        "v_mfma_f32_16x16x32_bf16 v[16:19], a[208:211], v[112:115], v[16:19]\n\t"
        "ds_write_b128 v118, v[68:71]\n\t"
        "v_mfma_f32_16x16x32_bf16 v[20:23], a[212:215], v[112:115], v[20:23]\n\t"
        "s_cmp_gt_u32 s3, 32\n\t"
        "s_cselect_b32 s8, 2, 0\n\t"
        "v_mfma_f32_16x16x32_bf16 v[24:27], a[180:183], v[112:115], v[24:27]\n\t"
        "s_cmp_lg_u64 s[56:57], 0\n\t"
        "s_addc_u32 s8, s8, s6\n\t"
        "v_mfma_f32_16x16x32_bf16 v[28:31], a[184:187], v[112:115], v[28:31]\n\t"
        "ds_read_b128 v[160:163], v185 offset:0\n\t"
        "v_mov_b32 v151, s8\n\t"
        "v_mfma_f32_16x16x32_bf16 v[32:35], a[188:191], v[112:115], v[32:35]\n\t"
        "buffer_load_dwordx4 v142, s[24:27], s36 offen offset:2048 lds\n\t"
        "s_waitcnt vmcnt(3)\n\t"
        "s_barrier\n\t"
        "v_mfma_f32_16x16x32_bf16 v[36:39], a[192:195], v[112:115], v[36:39]\n\t"
        "s_mov_b64 exec, 1\n\t"
        "ds_write_b32 v153, v151\n\t"
        "s_mov_b64 exec, -1\n\t"
        "v_mfma_f32_16x16x32_bf16 v[40:43], a[196:199], v[112:115], v[40:43]\n\t"
        "ds_read_b128 v[96:99], v130 offset:8192\n\t"
        "v_xor_b32 v154, 32, v186\n\t"
        "v_mfma_f32_16x16x32_bf16 v[44:47], a[200:203], v[112:115], v[44:47]\n\t"
        "ds_read_b64_tr_b16 a[252:253], v131 offset:24576\n\t"
        "v_mfma_f32_16x16x32_bf16 v[24:27], a[204:207], v[84:87], v[24:27]\n\t"
        "ds_read_b64_tr_b16 a[254:255], v131 offset:25344\n\t"
        "v_mfma_f32_16x16x32_bf16 v[28:31], a[208:211], v[84:87], v[28:31]\n\t"
        "ds_read_b64_tr_b16 v[188:189], v132 offset:24576\n\t"
        "v_mfma_f32_16x16x32_bf16 v[32:35], a[212:215], v[84:87], v[32:35]\n\t"
        "ds_read_b64_tr_b16 v[190:191], v132 offset:25344\n\t"
        "v_mfma_f32_16x16x32_bf16 v[36:39], a[180:183], v[84:87], v[36:39]\n\t"
        "ds_read_b128 v[100:103], v130 offset:12288\n\t"
        "v_mfma_f32_16x16x32_bf16 v[40:43], a[184:187], v[84:87], v[40:43]\n\t"
        "ds_read_b64_tr_b16 v[192:193], v133 offset:24576\n\t"
        "s_waitcnt lgkmcnt(8)\n\t"
        "v_lshlrev_b32_sdwa v157, 5, v160 dst_sel:DWORD dst_unused:UNUSED_PAD src0_sel:DWORD src1_sel:WORD_0\n\t"
        "v_mfma_f32_16x16x32_bf16 v[44:47], a[188:191], v[84:87], v[44:47]\n\t"
        "ds_read_b64_tr_b16 v[194:195], v133 offset:25344\n\t"
        "v_lshlrev_b32_sdwa v158, 5, v160 dst_sel:DWORD dst_unused:UNUSED_PAD src0_sel:DWORD src1_sel:WORD_1\n\t"
        "v_mfma_f32_16x16x32_bf16 v[0:3], a[228:231], v[88:91], v[0:3]\n\t"
        "ds_read_b64_tr_b16 v[196:197], v131 offset:30720\n\t"
        "v_or_b32 v157, v184, v157\n\t"
        "v_mfma_f32_16x16x32_bf16 v[4:7], a[232:235], v[88:91], v[4:7]\n\t"
        "ds_read_b64_tr_b16 v[60:61], v157 offset:0\n\t"
        "v_or_b32 v158, v184, v158\n\t"
        "v_mfma_f32_16x16x32_bf16 v[8:11], a[236:239], v[88:91], v[8:11]\n\t"
        "ds_read_b64_tr_b16 v[62:63], v158 offset:0\n\t"
        "v_xor_b32 v159, 32, v157\n\t"
        "v_mfma_f32_16x16x32_bf16 v[0:3], a[216:219], v[92:95], v[0:3]\n\t"
        "ds_read_b64_tr_b16 v[64:65], v159 offset:0\n\t"
        "v_xor_b32 v152, 32, v158\n\t"
        "v_mfma_f32_16x16x32_bf16 v[4:7], a[220:223], v[92:95], v[4:7]\n\t"
        "ds_read_b64_tr_b16 v[66:67], v152 offset:0\n\t"
        "v_perm_b32 v164, v162, v162, v168\n\t"
        "v_mfma_f32_16x16x32_bf16 v[8:11], a[224:227], v[92:95], v[8:11]\n\t"
        "ds_read_b64_tr_b16 v[68:69], v157 offset:64\n\t"
        "v_perm_b32 v165, v162, v162, v169\n\t"
        "v_mfma_f32_16x16x32_bf16 v[12:15], a[228:231], v[92:95], v[12:15]\n\t"
        "ds_read_b64_tr_b16 v[70:71], v158 offset:64\n\t"
        "v_perm_b32 v166, v162, v162, v170\n\t"
        "v_mfma_f32_16x16x32_bf16 v[16:19], a[232:235], v[92:95], v[16:19]\n\t"
        "ds_read_b64_tr_b16 v[72:73], v159 offset:64\n\t"
        "v_perm_b32 v167, v162, v162, v171\n\t"
        "v_mfma_f32_16x16x32_bf16 v[20:23], a[236:239], v[92:95], v[20:23]\n\t"
        "ds_read_b64_tr_b16 v[74:75], v152 offset:64\n\t"
        "s_waitcnt lgkmcnt(10)\n\t"
        "v_mfma_f32_16x16x32_bf16 v[0:3], a[240:243], v[96:99], v[0:3]\n\t"
        "ds_read_b64_tr_b16 v[76:77], v157 offset:128\n\t"
        "ds_read_b128 v[104:107], v130 offset:16384\n\t"
        "v_lshlrev_b32_sdwa v157, 5, v161 dst_sel:DWORD dst_unused:UNUSED_PAD src0_sel:DWORD src1_sel:WORD_0\n\t"
        "v_mfma_f32_16x16x32_bf16 v[4:7], a[244:247], v[96:99], v[4:7]\n\t"
        "ds_read_b64_tr_b16 v[78:79], v158 offset:128\n\t"
        "v_lshlrev_b32_sdwa v158, 5, v161 dst_sel:DWORD dst_unused:UNUSED_PAD src0_sel:DWORD src1_sel:WORD_1\n\t"
        "v_mfma_f32_16x16x32_bf16 v[8:11], a[248:251], v[96:99], v[8:11]\n\t"
        "ds_read_b64_tr_b16 v[80:81], v159 offset:128\n\t"
        "v_or_b32 v157, v184, v157\n\t"
        "s_waitcnt lgkmcnt(10)\n\t"
        "v_mfma_f32_16x16x32_bf16 v[48:51], v[60:63], v[164:167], 0\n\t"
        "ds_read_b64_tr_b16 v[82:83], v152 offset:128\n\t"
        "v_or_b32 v158, v184, v158\n\t"
        "v_mfma_f32_16x16x32_bf16 v[12:15], a[216:219], v[96:99], v[12:15]\n\t"
        "ds_read_b64_tr_b16 v[60:61], v157 offset:0\n\t"
        "v_xor_b32 v159, 32, v157\n\t"
        "s_waitcnt lgkmcnt(10)\n\t"
        "v_mfma_f32_16x16x32_bf16 v[52:55], v[64:67], v[164:167], 0\n\t"
        "ds_read_b64_tr_b16 v[62:63], v158 offset:0\n\t"
        "v_xor_b32 v152, 32, v158\n\t"
        "v_mfma_f32_16x16x32_bf16 v[16:19], a[220:223], v[96:99], v[16:19]\n\t"
        "ds_read_b64_tr_b16 v[64:65], v159 offset:0\n\t"
        "s_waitcnt lgkmcnt(10)\n\t"
        "v_mfma_f32_16x16x32_bf16 v[56:59], v[68:71], v[164:167], 0\n\t"
        "ds_read_b64_tr_b16 v[66:67], v152 offset:0\n\t"
        "v_mfma_f32_16x16x32_bf16 v[20:23], a[224:227], v[96:99], v[20:23]\n\t"
        "ds_read_b64_tr_b16 v[68:69], v157 offset:64\n\t"
        "s_waitcnt lgkmcnt(10)\n\t"
        "v_mfma_f32_16x16x32_bf16 v[116:119], v[72:75], v[164:167], 0\n\t"
        "s_waitcnt vmcnt(0)\n\t"
        "s_barrier\n\t"
        "v_mfma_f32_16x16x32_bf16 v[24:27], a[228:231], v[96:99], v[24:27]\n\t"
        "ds_read_b64_tr_b16 v[70:71], v158 offset:64\n\t"
        "s_waitcnt lgkmcnt(8)\n\t"
        "v_mfma_f32_16x16x32_bf16 v[120:123], v[76:79], v[164:167], 0\n\t"
        "ds_read_b64_tr_b16 v[72:73], v159 offset:64\n\t"
        "v_mfma_f32_16x16x32_bf16 v[28:31], a[232:235], v[96:99], v[28:31]\n\t"
        "ds_read_b64_tr_b16 v[74:75], v152 offset:64\n\t"
        "s_waitcnt lgkmcnt(8)\n\t"
        "v_mfma_f32_16x16x32_bf16 v[124:127], v[80:83], v[164:167], 0\n\t"
        "ds_read_b64_tr_b16 v[76:77], v157 offset:128\n\t"
        "v_perm_b32 v164, v163, v163, v168\n\t"
        "v_mfma_f32_16x16x32_bf16 v[32:35], a[236:239], v[96:99], v[32:35]\n\t"
        "ds_read_b128 v[108:111], v129 offset:20608\n\t"
        "v_perm_b32 v165, v163, v163, v169\n\t"
        "v_mfma_f32_16x16x32_bf16 v[12:15], a[240:243], v[100:103], v[12:15]\n\t"
        "ds_read_b64_tr_b16 v[78:79], v158 offset:128\n\t"
        "v_perm_b32 v166, v163, v163, v170\n\t"
        "v_mfma_f32_16x16x32_bf16 v[16:19], a[244:247], v[100:103], v[16:19]\n\t"
        "ds_read_b64_tr_b16 v[80:81], v159 offset:128\n\t"
        "v_perm_b32 v167, v163, v163, v171\n\t"
        "v_mfma_f32_16x16x32_bf16 v[20:23], a[248:251], v[100:103], v[20:23]\n\t"
        "ds_read_b128 v[112:115], v130 offset:20480\n\t"
        "s_waitcnt lgkmcnt(9)\n\t"
        "v_mfma_f32_16x16x32_bf16 v[48:51], v[60:63], v[164:167], v[48:51]\n\t"
        "ds_read_b64_tr_b16 v[82:83], v152 offset:128\n\t"
        "v_mfma_f32_16x16x32_bf16 v[24:27], a[216:219], v[100:103], v[24:27]\n\t"
        "ds_read_b128 v[160:163], v185 offset:256\n\t"
        "ds_read_b64_tr_b16 v[198:199], v131 offset:31488\n\t"
        "v_mfma_f32_16x16x32_bf16 v[52:55], v[64:67], v[164:167], v[52:55]\n\t"
        "ds_read_b64_tr_b16 v[200:201], v132 offset:30720\n\t"
        "ds_read_b64_tr_b16 v[202:203], v132 offset:31488\n\t"
        "v_mfma_f32_16x16x32_bf16 v[28:31], a[220:223], v[100:103], v[28:31]\n\t"
        "ds_read_b64_tr_b16 v[204:205], v133 offset:30720\n\t"
        "ds_read_b64_tr_b16 v[206:207], v133 offset:31488\n\t"
        "s_waitcnt lgkmcnt(12)\n\t"
        "v_mfma_f32_16x16x32_bf16 v[56:59], v[68:71], v[164:167], v[56:59]\n\t"
        "ds_read_b128 v[84:87], v130 offset:64\n\t"
        "v_cvt_pk_bf16_f32 v48, v48, v49\n\t"
        "v_mfma_f32_16x16x32_bf16 v[32:35], a[224:227], v[100:103], v[32:35]\n\t"
        "ds_read_b128 v[88:91], v130 offset:4160\n\t"
        "v_cvt_pk_bf16_f32 v49, v50, v51\n\t"
        "v_mfma_f32_16x16x32_bf16 v[116:119], v[72:75], v[164:167], v[116:119]\n\t"
        "ds_write_b64 v186, v[48:49] offset:0\n\t"
        "v_mfma_f32_16x16x32_bf16 v[36:39], a[228:231], v[100:103], v[36:39]\n\t"
        "ds_read_b64_tr_b16 v[208:209], v131 offset:36864\n\t"
        "v_cvt_pk_bf16_f32 v52, v52, v53\n\t"
        "s_waitcnt lgkmcnt(12)\n\t"
        "v_mfma_f32_16x16x32_bf16 v[120:123], v[76:79], v[164:167], v[120:123]\n\t"
        "ds_read_b64_tr_b16 v[210:211], v131 offset:37632\n\t"
        "v_cvt_pk_bf16_f32 v53, v54, v55\n\t"
        "v_mfma_f32_16x16x32_bf16 v[40:43], a[232:235], v[100:103], v[40:43]\n\t"
        "ds_write_b64 v154, v[52:53] offset:0\n\t"
        "s_waitcnt lgkmcnt(11)\n\t"
        "v_mfma_f32_16x16x32_bf16 v[124:127], v[80:83], v[164:167], v[124:127]\n\t"
        "ds_read_b64_tr_b16 v[212:213], v132 offset:36864\n\t"
        "v_cvt_pk_bf16_f32 v56, v56, v57\n\t"
        "v_mfma_f32_16x16x32_bf16 v[44:47], a[236:239], v[100:103], v[44:47]\n\t"
        "ds_read_b64_tr_b16 v[214:215], v132 offset:37632\n\t"
        "v_cvt_pk_bf16_f32 v57, v58, v59\n\t"
        "v_mfma_f32_16x16x32_bf16 v[24:27], a[240:243], v[104:107], v[24:27]\n\t"
        "ds_write_b64 v186, v[56:57] offset:64\n\t"
        "v_mfma_f32_16x16x32_bf16 v[28:31], a[244:247], v[104:107], v[28:31]\n\t"
        "ds_read_b64_tr_b16 v[216:217], v133 offset:36864\n\t"
        "v_cvt_pk_bf16_f32 v116, v116, v117\n\t"
        "v_mfma_f32_16x16x32_bf16 v[32:35], a[248:251], v[104:107], v[32:35]\n\t"
        "ds_read_b64_tr_b16 v[218:219], v133 offset:37632\n\t"
        "v_cvt_pk_bf16_f32 v117, v118, v119\n\t"
        "v_mfma_f32_16x16x32_bf16 v[36:39], a[216:219], v[104:107], v[36:39]\n\t"
        "ds_write_b64 v154, v[116:117] offset:64\n\t"
        "v_mfma_f32_16x16x32_bf16 v[40:43], a[220:223], v[104:107], v[40:43]\n\t"
        "ds_read_b128 v[92:95], v130 offset:8256\n\t"
        "v_cvt_pk_bf16_f32 v120, v120, v121\n\t"
        "v_mfma_f32_16x16x32_bf16 v[44:47], a[224:227], v[104:107], v[44:47]\n\t"
        "ds_read_b64_tr_b16 v[220:221], v131 offset:43008\n\t"
        "v_cvt_pk_bf16_f32 v121, v122, v123\n\t"
        "v_mfma_f32_16x16x32_bf16 v[36:39], a[204:207], v[108:111], v[36:39]\n\t"
        "ds_write_b64 v186, v[120:121] offset:128\n\t"
        "v_mfma_f32_16x16x32_bf16 v[40:43], a[208:211], v[108:111], v[40:43]\n\t"
        "ds_read_b64_tr_b16 v[222:223], v131 offset:43776\n\t"
        "v_cvt_pk_bf16_f32 v124, v124, v125\n\t"
        "v_mfma_f32_16x16x32_bf16 v[44:47], a[212:215], v[108:111], v[44:47]\n\t"
        "ds_read_b64_tr_b16 v[224:225], v132 offset:43008\n\t"
        "v_cvt_pk_bf16_f32 v125, v126, v127\n\t"
        "v_mfma_f32_16x16x32_bf16 v[36:39], a[240:243], v[112:115], v[36:39]\n\t"
        "ds_write_b64 v154, v[124:125] offset:128\n\t"
        "v_mfma_f32_16x16x32_bf16 v[40:43], a[244:247], v[112:115], v[40:43]\n\t"
        "ds_read_b128 v[96:99], v130 offset:12352\n\t"
        "v_lshlrev_b32_sdwa v157, 5, v160 dst_sel:DWORD dst_unused:UNUSED_PAD src0_sel:DWORD src1_sel:WORD_0\n\t"
        "v_mfma_f32_16x16x32_bf16 v[44:47], a[248:251], v[112:115], v[44:47]\n\t"
        "ds_read_b64_tr_b16 v[226:227], v132 offset:43776\n\t"
        "v_lshlrev_b32_sdwa v158, 5, v160 dst_sel:DWORD dst_unused:UNUSED_PAD src0_sel:DWORD src1_sel:WORD_1\n\t"
        "s_waitcnt lgkmcnt(9)\n\t"
        "v_mfma_f32_16x16x32_bf16 v[0:3], v[196:199], v[84:87], v[0:3]\n\t"
        "ds_read_b64_tr_b16 v[228:229], v133 offset:43008\n\t"
        "v_or_b32 v157, v184, v157\n\t"
        "v_mfma_f32_16x16x32_bf16 v[4:7], v[200:203], v[84:87], v[4:7]\n\t"
        "ds_read_b64_tr_b16 v[60:61], v157 offset:0\n\t"
        "v_or_b32 v158, v184, v158\n\t"
        "v_mfma_f32_16x16x32_bf16 v[8:11], v[204:207], v[84:87], v[8:11]\n\t"
        "ds_read_b64_tr_b16 v[62:63], v158 offset:0\n\t"
        "v_xor_b32 v159, 32, v157\n\t"
        "v_mfma_f32_16x16x32_bf16 v[0:3], a[252:255], v[88:91], v[0:3]\n\t"
        "ds_read_b64_tr_b16 v[64:65], v159 offset:0\n\t"
        "v_xor_b32 v152, 32, v158\n\t"
        "v_mfma_f32_16x16x32_bf16 v[4:7], v[188:191], v[88:91], v[4:7]\n\t"
        "ds_read_b64_tr_b16 v[66:67], v152 offset:0\n\t"
        "v_perm_b32 v164, v162, v162, v168\n\t"
        "v_mfma_f32_16x16x32_bf16 v[8:11], v[192:195], v[88:91], v[8:11]\n\t"
        "ds_read_b64_tr_b16 v[68:69], v157 offset:64\n\t"
        "v_perm_b32 v165, v162, v162, v169\n\t"
        "v_mfma_f32_16x16x32_bf16 v[12:15], v[196:199], v[88:91], v[12:15]\n\t"
        "ds_read_b64_tr_b16 v[70:71], v158 offset:64\n\t"
        "ds_read_b128 v[100:103], v130 offset:16448\n\t"
        "v_perm_b32 v166, v162, v162, v170\n\t"
        "v_mfma_f32_16x16x32_bf16 v[16:19], v[200:203], v[88:91], v[16:19]\n\t"
        "ds_read_b64_tr_b16 v[72:73], v159 offset:64\n\t"
        "ds_read_b64_tr_b16 v[230:231], v133 offset:43776\n\t"
        "v_perm_b32 v167, v162, v162, v171\n\t"
        "v_mfma_f32_16x16x32_bf16 v[20:23], v[204:207], v[88:91], v[20:23]\n\t"
        "ds_read_b64_tr_b16 v[74:75], v152 offset:64\n\t"
        "s_waitcnt lgkmcnt(11)\n\t"
        "v_mfma_f32_16x16x32_bf16 v[0:3], v[208:211], v[92:95], v[0:3]\n\t"
        "ds_read_b64_tr_b16 v[76:77], v157 offset:128\n\t"
        "v_lshlrev_b32_sdwa v157, 5, v161 dst_sel:DWORD dst_unused:UNUSED_PAD src0_sel:DWORD src1_sel:WORD_0\n\t"
        "v_mfma_f32_16x16x32_bf16 v[4:7], v[212:215], v[92:95], v[4:7]\n\t"
        "ds_read_b64_tr_b16 v[78:79], v158 offset:128\n\t"
        "v_lshlrev_b32_sdwa v158, 5, v161 dst_sel:DWORD dst_unused:UNUSED_PAD src0_sel:DWORD src1_sel:WORD_1\n\t"
        "v_mfma_f32_16x16x32_bf16 v[8:11], v[216:219], v[92:95], v[8:11]\n\t"
        "ds_read_b64_tr_b16 v[80:81], v159 offset:128\n\t"
        "v_or_b32 v157, v184, v157\n\t"
        "s_waitcnt lgkmcnt(11)\n\t"
        "v_mfma_f32_16x16x32_bf16 v[48:51], v[60:63], v[164:167], 0\n\t"
        "ds_read_b64_tr_b16 v[82:83], v152 offset:128\n\t"
        "v_or_b32 v158, v184, v158\n\t"
        "v_mfma_f32_16x16x32_bf16 v[12:15], a[252:255], v[92:95], v[12:15]\n\t"
        "ds_read_b64_tr_b16 v[60:61], v157 offset:0\n\t"
        "v_xor_b32 v159, 32, v157\n\t"
        "s_waitcnt lgkmcnt(11)\n\t"
        "v_mfma_f32_16x16x32_bf16 v[52:55], v[64:67], v[164:167], 0\n\t"
        "ds_read_b64_tr_b16 v[62:63], v158 offset:0\n\t"
        "v_xor_b32 v152, 32, v158\n\t"
        "v_mfma_f32_16x16x32_bf16 v[16:19], v[188:191], v[92:95], v[16:19]\n\t"
        "ds_read_b64_tr_b16 v[64:65], v159 offset:0\n\t"
        "s_waitcnt lgkmcnt(10)\n\t"
        "v_mfma_f32_16x16x32_bf16 v[56:59], v[68:71], v[164:167], 0\n\t"
        "ds_read_b64_tr_b16 v[66:67], v152 offset:0\n\t"
        "v_mfma_f32_16x16x32_bf16 v[20:23], v[192:195], v[92:95], v[20:23]\n\t"
        "ds_read_b64_tr_b16 v[68:69], v157 offset:64\n\t"
        "s_waitcnt lgkmcnt(9)\n\t"
        "v_mfma_f32_16x16x32_bf16 v[116:119], v[72:75], v[164:167], 0\n\t"
        "ds_read_b64_tr_b16 v[70:71], v158 offset:64\n\t"
        "v_mfma_f32_16x16x32_bf16 v[24:27], v[196:199], v[92:95], v[24:27]\n\t"
        "ds_read_b64_tr_b16 v[72:73], v159 offset:64\n\t"
        "s_waitcnt lgkmcnt(9)\n\t"
        "v_mfma_f32_16x16x32_bf16 v[120:123], v[76:79], v[164:167], 0\n\t"
        "ds_read_b64_tr_b16 v[74:75], v152 offset:64\n\t"
        "v_mfma_f32_16x16x32_bf16 v[28:31], v[200:203], v[92:95], v[28:31]\n\t"
        ".LpX1%=:\n\t"
        "s_cbranch_execz .LrX%=\n\t"
        "ds_read_b64_tr_b16 v[76:77], v157 offset:128\n\t"
        "s_waitcnt lgkmcnt(9)\n\t"
        "v_mfma_f32_16x16x32_bf16 v[124:127], v[80:83], v[164:167], 0\n\t"
        "ds_read_b64_tr_b16 v[78:79], v158 offset:128\n\t"
        "v_perm_b32 v164, v163, v163, v168\n\t"
        "v_mfma_f32_16x16x32_bf16 v[32:35], v[204:207], v[92:95], v[32:35]\n\t"
        "ds_read_b128 v[104:107], v130 offset:20544\n\t"
        "v_perm_b32 v165, v163, v163, v169\n\t"
        "v_mfma_f32_16x16x32_bf16 v[12:15], v[208:211], v[96:99], v[12:15]\n\t"
        "ds_read_b64_tr_b16 v[80:81], v159 offset:128\n\t"
        "ds_read_b64_tr_b16 v[232:233], v131 offset:49152\n\t"
        "v_perm_b32 v166, v163, v163, v170\n\t"
        "v_mfma_f32_16x16x32_bf16 v[16:19], v[212:215], v[96:99], v[16:19]\n\t"
        "ds_read_b64_tr_b16 v[82:83], v152 offset:128\n\t"
        "ds_read_b64_tr_b16 v[234:235], v131 offset:49920\n\t"
        "v_perm_b32 v167, v163, v163, v171\n\t"
        "v_mfma_f32_16x16x32_bf16 v[20:23], v[216:219], v[96:99], v[20:23]\n\t"
        "ds_read_b64_tr_b16 v[236:237], v132 offset:49152\n\t"
        "ds_read_b64_tr_b16 v[238:239], v132 offset:49920\n\t"
        "s_waitcnt lgkmcnt(11)\n\t"
        "v_mfma_f32_16x16x32_bf16 v[48:51], v[60:63], v[164:167], v[48:51]\n\t"
        "ds_read_b64_tr_b16 v[240:241], v133 offset:49152\n\t"
        "ds_read_b64_tr_b16 v[242:243], v133 offset:49920\n\t"
        "v_mfma_f32_16x16x32_bf16 v[24:27], a[252:255], v[96:99], v[24:27]\n\t"
        "ds_read_b128 v[108:111], v130 offset:128\n\t"
        "v_mfma_f32_16x16x32_bf16 v[52:55], v[64:67], v[164:167], v[52:55]\n\t"
        "ds_read_b128 v[112:115], v130 offset:4224\n\t"
        "v_mfma_f32_16x16x32_bf16 v[28:31], v[188:191], v[96:99], v[28:31]\n\t"
        "ds_read_b64_tr_b16 v[244:245], v131 offset:55296\n\t"
        "v_mfma_f32_16x16x32_bf16 v[56:59], v[68:71], v[164:167], v[56:59]\n\t"
        "ds_read_b64_tr_b16 v[246:247], v131 offset:56064\n\t"
        "v_cvt_pk_bf16_f32 v48, v48, v49\n\t"
        "v_mfma_f32_16x16x32_bf16 v[32:35], v[192:195], v[96:99], v[32:35]\n\t"
        "ds_read_b64_tr_b16 v[248:249], v132 offset:55296\n\t"
        "v_cvt_pk_bf16_f32 v49, v50, v51\n\t"
        "s_waitcnt lgkmcnt(12)\n\t"
        "v_mfma_f32_16x16x32_bf16 v[116:119], v[72:75], v[164:167], v[116:119]\n\t"
        "ds_write_b64 v186, v[48:49] offset:12288\n\t"
        "v_mfma_f32_16x16x32_bf16 v[36:39], v[196:199], v[96:99], v[36:39]\n\t"
        "ds_read_b64_tr_b16 v[250:251], v132 offset:56064\n\t"
        "v_cvt_pk_bf16_f32 v52, v52, v53\n\t"
        "v_mfma_f32_16x16x32_bf16 v[120:123], v[76:79], v[164:167], v[120:123]\n\t"
        "ds_read_b64_tr_b16 v[252:253], v133 offset:55296\n\t"
        "v_cvt_pk_bf16_f32 v53, v54, v55\n\t"
        "v_mfma_f32_16x16x32_bf16 v[40:43], v[200:203], v[96:99], v[40:43]\n\t"
        "ds_write_b64 v154, v[52:53] offset:12288\n\t"
        "s_waitcnt lgkmcnt(9)\n\t"
        "v_mfma_f32_16x16x32_bf16 v[124:127], v[80:83], v[164:167], v[124:127]\n\t"
        "ds_read_b64_tr_b16 v[254:255], v133 offset:56064\n\t"
        "v_cvt_pk_bf16_f32 v56, v56, v57\n\t"
        "v_mfma_f32_16x16x32_bf16 v[44:47], v[204:207], v[96:99], v[44:47]\n\t"
        "ds_read_b128 v[84:87], v130 offset:8320\n\t"
        "v_cvt_pk_bf16_f32 v57, v58, v59\n\t"
        "v_mfma_f32_16x16x32_bf16 v[24:27], v[208:211], v[100:103], v[24:27]\n\t"
        "ds_write_b64 v186, v[56:57] offset:12352\n\t"
        "v_mfma_f32_16x16x32_bf16 v[28:31], v[212:215], v[100:103], v[28:31]\n\t"
        "v_cvt_pk_bf16_f32 v116, v116, v117\n\t"
        "v_cvt_pk_bf16_f32 v117, v118, v119\n\t"
        "v_mfma_f32_16x16x32_bf16 v[32:35], v[216:219], v[100:103], v[32:35]\n\t"
        "ds_write_b64 v154, v[116:117] offset:12352\n\t"
        "v_mfma_f32_16x16x32_bf16 v[36:39], a[252:255], v[100:103], v[36:39]\n\t"
        "v_cvt_pk_bf16_f32 v120, v120, v121\n\t"
        "v_cvt_pk_bf16_f32 v121, v122, v123\n\t"
        "v_mfma_f32_16x16x32_bf16 v[40:43], v[188:191], v[100:103], v[40:43]\n\t"
        "ds_write_b64 v186, v[120:121] offset:12416\n\t"
        "v_mfma_f32_16x16x32_bf16 v[44:47], v[192:195], v[100:103], v[44:47]\n\t"
        "v_cvt_pk_bf16_f32 v124, v124, v125\n\t"
        "v_cvt_pk_bf16_f32 v125, v126, v127\n\t"
        "v_mfma_f32_16x16x32_bf16 v[36:39], v[208:211], v[104:107], v[36:39]\n\t"
        "ds_write_b64 v154, v[124:125] offset:12416\n\t"
        "v_mfma_f32_16x16x32_bf16 v[40:43], v[212:215], v[104:107], v[40:43]\n\t"
        "ds_read_b128 v[88:91], v130 offset:12416\n\t"
        "v_mfma_f32_16x16x32_bf16 v[44:47], v[216:219], v[104:107], v[44:47]\n\t"
        "s_waitcnt lgkmcnt(5)\n\t"
        "v_mfma_f32_16x16x32_bf16 v[0:3], v[232:235], v[108:111], v[0:3]\n\t"
        "v_mfma_f32_16x16x32_bf16 v[4:7], v[236:239], v[108:111], v[4:7]\n\t"
        "v_mfma_f32_16x16x32_bf16 v[8:11], v[240:243], v[108:111], v[8:11]\n\t"
        "v_mfma_f32_16x16x32_bf16 v[0:3], v[220:223], v[112:115], v[0:3]\n\t"
        "s_waitcnt lgkmcnt(1)\n\t"
        "s_barrier\n\t"
        "v_mfma_f32_16x16x32_bf16 v[4:7], v[224:227], v[112:115], v[4:7]\n\t"
        "ds_read_b128 v[124:127], v182 offset:0\n\t"
        "v_mfma_f32_16x16x32_bf16 v[8:11], v[228:231], v[112:115], v[8:11]\n\t"
        "ds_read_b128 v[132:135], v187 offset:0\n\t"
        "v_mfma_f32_16x16x32_bf16 v[12:15], v[232:235], v[112:115], v[12:15]\n\t"
        "ds_read_b128 v[136:139], v187 offset:16\n\t"
        "v_mfma_f32_16x16x32_bf16 v[16:19], v[236:239], v[112:115], v[16:19]\n\t"
        "ds_read_b32 v140, v187 offset:32\n\t"
        "v_mfma_f32_16x16x32_bf16 v[20:23], v[240:243], v[112:115], v[20:23]\n\t"
        "ds_read_b128 v[92:95], v130 offset:16512\n\t"
        "v_mfma_f32_16x16x32_bf16 v[0:3], v[244:247], v[84:87], v[0:3]\n\t"
        "v_mfma_f32_16x16x32_bf16 v[4:7], v[248:251], v[84:87], v[4:7]\n\t"
        "v_mfma_f32_16x16x32_bf16 v[8:11], v[252:255], v[84:87], v[8:11]\n\t"
        "v_mfma_f32_16x16x32_bf16 v[12:15], v[220:223], v[84:87], v[12:15]\n\t"
        "ds_read_b128 v[96:99], v130 offset:20608\n\t"
        "v_mfma_f32_16x16x32_bf16 v[16:19], v[224:227], v[84:87], v[16:19]\n\t"
        "s_waitcnt lgkmcnt(5)\n\t"
        "v_or3_b32 v124, v124, v125, v126\n\t"
        "v_or_b32 v124, v124, v127\n\t"
        "v_mfma_f32_16x16x32_bf16 v[20:23], v[228:231], v[84:87], v[20:23]\n\t"
        "v_readfirstlane_b32 s6, v124\n\t"
        "v_mfma_f32_16x16x32_bf16 v[24:27], v[232:235], v[84:87], v[24:27]\n\t"
        "s_nop 0\n\t"
        "s_bitcmp1_b32 s6, 1\n\t"
        "s_cbranch_scc1 .Lx68%=\n\t"
        ".Lx69%=:\n\t"
        "v_mfma_f32_16x16x32_bf16 v[28:31], v[236:239], v[84:87], v[28:31]\n\t"
        "ds_read_b128 v[160:163], v185 offset:512\n\t"
        "s_waitcnt lgkmcnt(4)\n\t"
        "v_xor_b32 v132, v181, v132\n\t"
        "v_add_u32 v151, s9, v172\n\t"
        "v_mfma_f32_16x16x32_bf16 v[32:35], v[240:243], v[84:87], v[32:35]\n\t"
        "v_max_u32 v151, v151, v132\n\t"
        "v_xor_b32 v133, v181, v133\n\t"
        "v_add_u32 v183, s9, v173\n\t"
        "v_mfma_f32_16x16x32_bf16 v[12:15], v[244:247], v[88:91], v[12:15]\n\t"
        "ds_read_b128 v[100:103], v151 offset:0\n\t"
        "v_max_u32 v183, v183, v133\n\t"
        "v_xor_b32 v134, v181, v134\n\t"
        "v_mfma_f32_16x16x32_bf16 v[16:19], v[248:251], v[88:91], v[16:19]\n\t"
        "ds_read_b128 v[104:107], v151 offset:64\n\t"
        "s_add_u32 s55, s41, 0\n\t"
        "v_cvt_pk_bf16_f32 v116, v0, v1\n\t"
        "v_mfma_f32_16x16x32_bf16 v[20:23], v[252:255], v[88:91], v[20:23]\n\t"
        "v_cvt_pk_bf16_f32 v117, v2, v3\n\t"
        "v_cvt_pk_bf16_f32 v118, v4, v5\n\t"
        "v_cvt_pk_bf16_f32 v119, v6, v7\n\t"
        "v_mfma_f32_16x16x32_bf16 v[24:27], v[220:223], v[88:91], v[24:27]\n\t"
        "ds_read_b128 v[108:111], v151 offset:128\n\t"
        "v_add_u32 v151, s9, v174\n\t"
        "v_max_u32 v151, v151, v134\n\t"
        "v_mfma_f32_16x16x32_bf16 v[28:31], v[224:227], v[88:91], v[28:31]\n\t"
        "v_xor_b32 v135, v181, v135\n\t"
        "v_cvt_pk_bf16_f32 v120, v8, v9\n\t"
        "v_cvt_pk_bf16_f32 v121, v10, v11\n\t"
        "v_mfma_f32_16x16x32_bf16 v[32:35], v[228:231], v[88:91], v[32:35]\n\t"
        "v_permlane16_swap_b32 v116, v118\n\t"
        "v_mfma_f32_16x16x32_bf16 v[36:39], v[232:235], v[88:91], v[36:39]\n\t"
        "v_permlane16_swap_b32 v117, v119\n\t"
        "v_mfma_f32_16x16x32_bf16 v[40:43], v[236:239], v[88:91], v[40:43]\n\t"
        "buffer_store_dwordx4 v[116:119], v144, s[28:31], s55 offen sc1\n\t"
        "v_mfma_f32_16x16x32_bf16 v[44:47], v[240:243], v[88:91], v[44:47]\n\t"
        "ds_write_b64 v146, v[120:121] offset:0\n\t"
        "s_add_u32 s55, s41, 43008\n\t"
        "v_cvt_pk_bf16_f32 v116, v12, v13\n\t"
        "s_waitcnt lgkmcnt(2)\n\t"
        "v_mfma_f32_16x16x32_bf16 v[24:27], v[244:247], v[92:95], v[24:27]\n\t"
        "ds_read_b128 v[112:115], v183 offset:0\n\t"
        "v_cvt_pk_bf16_f32 v117, v14, v15\n\t"
        "v_cvt_pk_bf16_f32 v118, v16, v17\n\t"
        "v_mfma_f32_16x16x32_bf16 v[28:31], v[248:251], v[92:95], v[28:31]\n\t"
        "ds_read_b128 v[84:87], v183 offset:64\n\t"
        "v_cvt_pk_bf16_f32 v119, v18, v19\n\t"
        "v_cvt_pk_bf16_f32 v120, v20, v21\n\t"
        "v_mfma_f32_16x16x32_bf16 v[32:35], v[252:255], v[92:95], v[32:35]\n\t"
        "v_cvt_pk_bf16_f32 v121, v22, v23\n\t"
        "v_permlane16_swap_b32 v116, v118\n\t"
        "v_mfma_f32_16x16x32_bf16 v[36:39], v[220:223], v[92:95], v[36:39]\n\t"
        "v_permlane16_swap_b32 v117, v119\n\t"
        "v_lshlrev_b32_sdwa v157, 5, v160 dst_sel:DWORD dst_unused:UNUSED_PAD src0_sel:DWORD src1_sel:WORD_0\n\t"
        "v_mfma_f32_16x16x32_bf16 v[40:43], v[224:227], v[92:95], v[40:43]\n\t"
        "ds_read_b128 v[88:91], v183 offset:128\n\t"
        "v_add_u32 v183, s9, v175\n\t"
        "v_max_u32 v183, v183, v135\n\t"
        "v_mfma_f32_16x16x32_bf16 v[44:47], v[228:231], v[92:95], v[44:47]\n\t"
        "v_xor_b32 v136, v181, v136\n\t"
        "v_lshlrev_b32_sdwa v158, 5, v160 dst_sel:DWORD dst_unused:UNUSED_PAD src0_sel:DWORD src1_sel:WORD_1\n\t"
        "v_or_b32 v157, v184, v157\n\t"
        "v_mfma_f32_16x16x32_bf16 v[36:39], v[244:247], v[96:99], v[36:39]\n\t"
        "ds_read_b64_tr_b16 v[60:61], v157 offset:0\n\t"
        "ds_read_b64_tr_b16 v[68:69], v157 offset:64\n\t"
        "v_or_b32 v158, v184, v158\n\t"
        "v_mfma_f32_16x16x32_bf16 v[40:43], v[248:251], v[96:99], v[40:43]\n\t"
        "ds_read_b64_tr_b16 v[62:63], v158 offset:0\n\t"
        "ds_read_b64_tr_b16 v[70:71], v158 offset:64\n\t"
        "v_xor_b32 v159, 32, v157\n\t"
        "v_mfma_f32_16x16x32_bf16 v[44:47], v[252:255], v[96:99], v[44:47]\n\t"
        "ds_read_b64_tr_b16 v[64:65], v159 offset:0\n\t"
        "ds_read_b64_tr_b16 v[72:73], v159 offset:64\n\t"
        "v_xor_b32 v152, 32, v158\n\t"
        "v_mfma_f32_16x16x32_bf16 v[48:51], a[12:15], v[100:103], 0\n\t"
        "ds_read_b64_tr_b16 v[66:67], v152 offset:0\n\t"
        "ds_read_b64_tr_b16 v[74:75], v152 offset:64\n\t"
        "v_perm_b32 v164, v162, v162, v168\n\t"
        "v_mfma_f32_16x16x32_bf16 v[52:55], a[16:19], v[100:103], 0\n\t"
        "ds_read_b64_tr_b16 v[76:77], v157 offset:128\n\t"
        "ds_read_b64_tr_b16 v[78:79], v158 offset:128\n\t"
        "v_perm_b32 v165, v162, v162, v169\n\t"
        "v_mfma_f32_16x16x32_bf16 v[56:59], a[20:23], v[100:103], 0\n\t"
        "ds_read_b128 v[92:95], v151 offset:0\n\t"
        "ds_read_b64_tr_b16 v[80:81], v159 offset:128\n\t"
        "v_perm_b32 v166, v162, v162, v170\n\t"
        "v_mfma_f32_16x16x32_bf16 v[48:51], a[48:51], v[104:107], v[48:51]\n\t"
        "buffer_store_dwordx4 v[116:119], v144, s[28:31], s55 offen sc1\n\t"
        "v_mfma_f32_16x16x32_bf16 v[52:55], a[52:55], v[104:107], v[52:55]\n\t"
        "ds_write_b64 v146, v[120:121] offset:2048\n\t"
        "s_add_u32 s55, s41, 86016\n\t"
        "v_mfma_f32_16x16x32_bf16 v[56:59], a[56:59], v[104:107], v[56:59]\n\t"
        "ds_read_b128 v[96:99], v151 offset:64\n\t"
        "ds_read_b64_tr_b16 v[82:83], v152 offset:128\n\t"
        "v_cvt_pk_bf16_f32 v116, v24, v25\n\t"
        "s_waitcnt lgkmcnt(9)\n\t"
        "v_mfma_f32_16x16x32_bf16 v[48:51], a[84:87], v[108:111], v[48:51]\n\t"
        "ds_read_b128 v[100:103], v151 offset:128\n\t"
        "v_add_u32 v151, s9, v176\n\t"
        "v_max_u32 v151, v151, v136\n\t"
        "v_mfma_f32_16x16x32_bf16 v[52:55], a[88:91], v[108:111], v[52:55]\n\t"
        "ds_read_b128 v[104:107], v183 offset:0\n\t"
        "v_xor_b32 v137, v181, v137\n\t"
        "v_cvt_pk_bf16_f32 v117, v26, v27\n\t"
        "v_mfma_f32_16x16x32_bf16 v[56:59], a[92:95], v[108:111], v[56:59]\n\t"
        "v_cvt_pk_bf16_f32 v118, v28, v29\n\t"
        "v_cvt_pk_bf16_f32 v119, v30, v31\n\t"
        "v_cvt_pk_bf16_f32 v120, v32, v33\n\t"
        "v_mfma_f32_16x16x32_bf16 v[48:51], a[120:123], v[112:115], v[48:51]\n\t"
        "ds_read_b128 v[108:111], v183 offset:64\n\t"
        "v_cvt_pk_bf16_f32 v121, v34, v35\n\t"
        "v_perm_b32 v167, v162, v162, v171\n\t"
        "v_mfma_f32_16x16x32_bf16 v[52:55], a[124:127], v[112:115], v[52:55]\n\t"
        "v_permlane16_swap_b32 v116, v118\n\t"
        "v_lshlrev_b32_sdwa v157, 5, v161 dst_sel:DWORD dst_unused:UNUSED_PAD src0_sel:DWORD src1_sel:WORD_0\n\t"
        "v_mfma_f32_16x16x32_bf16 v[0:3], v[60:63], v[164:167], 0\n\t"
        "v_permlane16_swap_b32 v117, v119\n\t"
        "v_lshlrev_b32_sdwa v158, 5, v161 dst_sel:DWORD dst_unused:UNUSED_PAD src0_sel:DWORD src1_sel:WORD_1\n\t"
        "v_mfma_f32_16x16x32_bf16 v[56:59], a[128:131], v[112:115], v[56:59]\n\t"
        "v_or_b32 v157, v184, v157\n\t"
        "v_or_b32 v158, v184, v158\n\t"
        "v_xor_b32 v159, 32, v157\n\t"
        "s_waitcnt lgkmcnt(4)\n\t"
        "v_mfma_f32_16x16x32_bf16 v[4:7], v[64:67], v[164:167], 0\n\t"
        "ds_read_b64_tr_b16 v[60:61], v157 offset:0\n\t"
        "ds_read_b64_tr_b16 v[62:63], v158 offset:0\n\t"
        "v_xor_b32 v152, 32, v158\n\t"
        "v_mfma_f32_16x16x32_bf16 v[48:51], a[156:159], v[84:87], v[48:51]\n\t"
        "ds_read_b64_tr_b16 v[64:65], v159 offset:0\n\t"
        "ds_read_b64_tr_b16 v[66:67], v152 offset:0\n\t"
        "v_mfma_f32_16x16x32_bf16 v[8:11], v[68:71], v[164:167], 0\n\t"
        "ds_read_b64_tr_b16 v[68:69], v157 offset:64\n\t"
        "ds_read_b64_tr_b16 v[70:71], v158 offset:64\n\t"
        "v_mfma_f32_16x16x32_bf16 v[52:55], a[160:163], v[84:87], v[52:55]\n\t"
        "ds_read_b128 v[112:115], v183 offset:128\n\t"
        "v_add_u32 v183, s9, v177\n\t"
        "v_max_u32 v183, v183, v137\n\t"
        "v_mfma_f32_16x16x32_bf16 v[12:15], v[72:75], v[164:167], 0\n\t"
        "buffer_store_dwordx4 v[116:119], v144, s[28:31], s55 offen sc1\n\t"
        "v_mfma_f32_16x16x32_bf16 v[56:59], a[164:167], v[84:87], v[56:59]\n\t"
        "ds_write_b64 v146, v[120:121] offset:4096\n\t"
        "v_xor_b32 v138, v181, v138\n\t"
        "v_mfma_f32_16x16x32_bf16 v[16:19], v[76:79], v[164:167], 0\n\t"
        "ds_read_b64_tr_b16 v[72:73], v159 offset:64\n\t"
        "ds_read_b64_tr_b16 v[74:75], v152 offset:64\n\t"
        "s_add_u32 s55, s41, 129024\n\t"
        "v_mfma_f32_16x16x32_bf16 v[48:51], a[192:195], v[88:91], v[48:51]\n\t"
        "ds_read_b64_tr_b16 v[76:77], v157 offset:128\n\t"
        "ds_read_b64_tr_b16 v[78:79], v158 offset:128\n\t"
        "v_cvt_pk_bf16_f32 v116, v36, v37\n\t"
        "s_waitcnt lgkmcnt(10)\n\t"
        "v_mfma_f32_16x16x32_bf16 v[20:23], v[80:83], v[164:167], 0\n\t"
        "ds_read_b64_tr_b16 v[80:81], v159 offset:128\n\t"
        "ds_read_b64_tr_b16 v[82:83], v152 offset:128\n\t"
        "v_cvt_pk_bf16_f32 v117, v38, v39\n\t"
        "v_mfma_f32_16x16x32_bf16 v[52:55], a[196:199], v[88:91], v[52:55]\n\t"
        "ds_read_b128 v[84:87], v151 offset:0\n\t"
        "v_cvt_pk_bf16_f32 v118, v40, v41\n\t"
        "v_cvt_pk_bf16_f32 v119, v42, v43\n\t"
        "v_mfma_f32_16x16x32_bf16 v[56:59], a[200:203], v[88:91], v[56:59]\n\t"
        "v_cvt_pk_bf16_f32 v120, v44, v45\n\t"
        "v_cvt_pk_bf16_f32 v121, v46, v47\n\t"
        "v_perm_b32 v164, v163, v163, v168\n\t"
        "v_mfma_f32_16x16x32_bf16 v[48:51], a[228:231], v[92:95], v[48:51]\n\t"
        "v_permlane16_swap_b32 v116, v118\n\t"
        "v_perm_b32 v165, v163, v163, v169\n\t"
        "v_mfma_f32_16x16x32_bf16 v[52:55], a[232:235], v[92:95], v[52:55]\n\t"
        "v_permlane16_swap_b32 v117, v119\n\t"
        "v_perm_b32 v166, v163, v163, v170\n\t"
        "v_mfma_f32_16x16x32_bf16 v[56:59], a[236:239], v[92:95], v[56:59]\n\t"
        "ds_read_b128 v[88:91], v151 offset:64\n\t"
        "v_perm_b32 v167, v163, v163, v171\n\t"
        "v_mfma_f32_16x16x32_bf16 v[48:51], v[196:199], v[96:99], v[48:51]\n\t"
        "ds_read_b128 v[160:163], v185 offset:768\n\t"
        "v_mfma_f32_16x16x32_bf16 v[0:3], v[60:63], v[164:167], v[0:3]\n\t"
        "ds_read_b128 v[92:95], v151 offset:128\n\t"
        "v_add_u32 v151, s9, v178\n\t"
        "v_max_u32 v151, v151, v138\n\t"
        "v_xor_b32 v139, v181, v139\n\t"
        "v_mfma_f32_16x16x32_bf16 v[52:55], v[200:203], v[96:99], v[52:55]\n\t"
        "s_waitcnt lgkmcnt(5)\n\t"
        "v_mfma_f32_16x16x32_bf16 v[4:7], v[64:67], v[164:167], v[4:7]\n\t"
        "v_mfma_f32_16x16x32_bf16 v[56:59], v[204:207], v[96:99], v[56:59]\n\t"
        "buffer_store_dwordx4 v[116:119], v144, s[28:31], s55 offen sc1\n\t"
        "v_mfma_f32_16x16x32_bf16 v[8:11], v[68:71], v[164:167], v[8:11]\n\t"
        "ds_write_b64 v146, v[120:121] offset:6144\n\t"
        "v_cvt_pk_bf16_f32 v0, v0, v1\n\t"
        "v_cvt_pk_bf16_f32 v1, v2, v3\n\t"
        "v_mfma_f32_16x16x32_bf16 v[48:51], v[232:235], v[100:103], v[48:51]\n\t"
        "ds_write_b64 v186, v[0:1] offset:24576\n\t"
        "v_mfma_f32_16x16x32_bf16 v[12:15], v[72:75], v[164:167], v[12:15]\n\t"
        "ds_read_b128 v[96:99], v183 offset:0\n\t"
        "v_cvt_pk_bf16_f32 v4, v4, v5\n\t"
        "v_cvt_pk_bf16_f32 v5, v6, v7\n\t"
        "v_mfma_f32_16x16x32_bf16 v[52:55], v[236:239], v[100:103], v[52:55]\n\t"
        "ds_write_b64 v154, v[4:5] offset:24576\n\t"
        "v_mfma_f32_16x16x32_bf16 v[16:19], v[76:79], v[164:167], v[16:19]\n\t"
        "v_cvt_pk_bf16_f32 v8, v8, v9\n\t"
        "v_cvt_pk_bf16_f32 v9, v10, v11\n\t"
        "s_waitcnt lgkmcnt(4)\n\t"
        "v_lshlrev_b32_sdwa v157, 5, v160 dst_sel:DWORD dst_unused:UNUSED_PAD src0_sel:DWORD src1_sel:WORD_0\n\t"
        "v_lshlrev_b32_sdwa v158, 5, v160 dst_sel:DWORD dst_unused:UNUSED_PAD src0_sel:DWORD src1_sel:WORD_1\n\t"
        "v_or_b32 v157, v184, v157\n\t"
        "v_mfma_f32_16x16x32_bf16 v[56:59], v[240:243], v[100:103], v[56:59]\n\t"
        "ds_write_b64 v186, v[8:9] offset:24640\n\t"
        "v_mfma_f32_16x16x32_bf16 v[20:23], v[80:83], v[164:167], v[20:23]\n\t"
        "ds_read_b64_tr_b16 v[60:61], v157 offset:0\n\t"
        "ds_read_b64_tr_b16 v[68:69], v157 offset:64\n\t"
        "v_cvt_pk_bf16_f32 v12, v12, v13\n\t"
        "v_cvt_pk_bf16_f32 v13, v14, v15\n\t"
        "v_mfma_f32_16x16x32_bf16 v[48:51], a[0:3], v[104:107], v[48:51]\n\t"
        "ds_write_b64 v154, v[12:13] offset:24640\n\t"
        "v_mfma_f32_16x16x32_bf16 v[52:55], a[4:7], v[104:107], v[52:55]\n\t"
        "ds_read_b64_tr_b16 v[76:77], v157 offset:128\n\t"
        "v_cvt_pk_bf16_f32 v16, v16, v17\n\t"
        "v_cvt_pk_bf16_f32 v17, v18, v19\n\t"
        "v_or_b32 v158, v184, v158\n\t"
        "v_mfma_f32_16x16x32_bf16 v[56:59], a[8:11], v[104:107], v[56:59]\n\t"
        "ds_write_b64 v186, v[16:17] offset:24704\n\t"
        "v_mfma_f32_16x16x32_bf16 v[48:51], a[36:39], v[108:111], v[48:51]\n\t"
        "ds_read_b64_tr_b16 v[62:63], v158 offset:0\n\t"
        "ds_read_b64_tr_b16 v[70:71], v158 offset:64\n\t"
        "v_cvt_pk_bf16_f32 v20, v20, v21\n\t"
        "v_cvt_pk_bf16_f32 v21, v22, v23\n\t"
        "v_mfma_f32_16x16x32_bf16 v[52:55], a[40:43], v[108:111], v[52:55]\n\t"
        "ds_write_b64 v154, v[20:21] offset:24704\n\t"
        "v_mfma_f32_16x16x32_bf16 v[56:59], a[44:47], v[108:111], v[56:59]\n\t"
        "ds_read_b128 v[100:103], v183 offset:64\n\t"
        "ds_read_b64_tr_b16 v[78:79], v158 offset:128\n\t"
        "v_xor_b32 v159, 32, v157\n\t"
        "v_xor_b32 v152, 32, v158\n\t"
        "v_mfma_f32_16x16x32_bf16 v[48:51], a[72:75], v[112:115], v[48:51]\n\t"
        "ds_read_b64_tr_b16 v[64:65], v159 offset:0\n\t"
        "ds_read_b64_tr_b16 v[66:67], v152 offset:0\n\t"
        "v_perm_b32 v164, v162, v162, v168\n\t"
        "v_mfma_f32_16x16x32_bf16 v[52:55], a[76:79], v[112:115], v[52:55]\n\t"
        "ds_read_b64_tr_b16 v[72:73], v159 offset:64\n\t"
        "ds_read_b64_tr_b16 v[74:75], v152 offset:64\n\t"
        "v_perm_b32 v165, v162, v162, v169\n\t"
        "v_mfma_f32_16x16x32_bf16 v[56:59], a[80:83], v[112:115], v[56:59]\n\t"
        "ds_read_b128 v[104:107], v183 offset:128\n\t"
        "ds_read_b64_tr_b16 v[80:81], v159 offset:128\n\t"
        "v_add_u32 v183, s9, v179\n\t"
        "v_mfma_f32_16x16x32_bf16 v[48:51], a[108:111], v[84:87], v[48:51]\n\t"
        "ds_read_b64_tr_b16 v[82:83], v152 offset:128\n\t"
        "v_max_u32 v183, v183, v139\n\t"
        "v_perm_b32 v166, v162, v162, v170\n\t"
        "v_mfma_f32_16x16x32_bf16 v[52:55], a[112:115], v[84:87], v[52:55]\n\t"
        "ds_read_b128 v[108:111], v151 offset:0\n\t"
        "v_perm_b32 v167, v162, v162, v171\n\t"
        "v_lshlrev_b32_sdwa v157, 5, v161 dst_sel:DWORD dst_unused:UNUSED_PAD src0_sel:DWORD src1_sel:WORD_0\n\t"
        "v_mfma_f32_16x16x32_bf16 v[56:59], a[116:119], v[84:87], v[56:59]\n\t"
        "ds_read_b128 v[112:115], v151 offset:64\n\t"
        "v_lshlrev_b32_sdwa v158, 5, v161 dst_sel:DWORD dst_unused:UNUSED_PAD src0_sel:DWORD src1_sel:WORD_1\n\t"
        "v_or_b32 v157, v184, v157\n\t"
        "v_mfma_f32_16x16x32_bf16 v[48:51], a[144:147], v[88:91], v[48:51]\n\t"
        "ds_read_b128 v[84:87], v151 offset:128\n\t"
        "v_or_b32 v158, v184, v158\n\t"
        "v_xor_b32 v159, 32, v157\n\t"
        "s_waitcnt lgkmcnt(10)\n\t"
        "v_mfma_f32_16x16x32_bf16 v[0:3], v[60:63], v[164:167], 0\n\t"
        "ds_read_b64_tr_b16 v[60:61], v157 offset:0\n\t"
        "ds_read_b64_tr_b16 v[62:63], v158 offset:0\n\t"
        "v_xor_b32 v152, 32, v158\n\t"
        "v_mfma_f32_16x16x32_bf16 v[52:55], a[148:151], v[88:91], v[52:55]\n\t"
        "v_mfma_f32_16x16x32_bf16 v[8:11], v[68:71], v[164:167], 0\n\t"
        "ds_read_b64_tr_b16 v[68:69], v157 offset:64\n\t"
        "ds_read_b64_tr_b16 v[70:71], v158 offset:64\n\t"
        "v_mfma_f32_16x16x32_bf16 v[56:59], a[152:155], v[88:91], v[56:59]\n\t"
        "s_waitcnt lgkmcnt(8)\n\t"
        "v_mfma_f32_16x16x32_bf16 v[4:7], v[64:67], v[164:167], 0\n\t"
        "ds_read_b64_tr_b16 v[64:65], v159 offset:0\n\t"
        "ds_read_b64_tr_b16 v[66:67], v152 offset:0\n\t"
        "v_mfma_f32_16x16x32_bf16 v[48:51], a[180:183], v[92:95], v[48:51]\n\t"
        "ds_read_b128 v[88:91], v183 offset:0\n\t"
        "v_mfma_f32_16x16x32_bf16 v[12:15], v[72:75], v[164:167], 0\n\t"
        "ds_read_b64_tr_b16 v[72:73], v159 offset:64\n\t"
        "ds_read_b64_tr_b16 v[74:75], v152 offset:64\n\t"
        "v_mfma_f32_16x16x32_bf16 v[52:55], a[184:187], v[92:95], v[52:55]\n\t"
        "v_mfma_f32_16x16x32_bf16 v[16:19], v[76:79], v[164:167], 0\n\t"
        "ds_read_b64_tr_b16 v[76:77], v157 offset:128\n\t"
        "ds_read_b64_tr_b16 v[78:79], v158 offset:128\n\t"
        "v_mfma_f32_16x16x32_bf16 v[56:59], a[188:191], v[92:95], v[56:59]\n\t"
        "s_waitcnt lgkmcnt(9)\n\t"
        "v_mfma_f32_16x16x32_bf16 v[20:23], v[80:83], v[164:167], 0\n\t"
        "ds_read_b64_tr_b16 v[80:81], v159 offset:128\n\t"
        "ds_read_b64_tr_b16 v[82:83], v152 offset:128\n\t"
        "v_perm_b32 v164, v163, v163, v168\n\t"
        "v_perm_b32 v165, v163, v163, v169\n\t"
        "v_perm_b32 v166, v163, v163, v170\n\t"
        "v_mfma_f32_16x16x32_bf16 v[48:51], a[216:219], v[96:99], v[48:51]\n\t"
        "ds_read_b128 v[92:95], v183 offset:64\n\t"
        "v_perm_b32 v167, v163, v163, v171\n\t"
        "v_mfma_f32_16x16x32_bf16 v[52:55], a[220:223], v[96:99], v[52:55]\n\t"
        "s_nop 0\n\t"
        "v_mfma_f32_16x16x32_bf16 v[0:3], v[60:63], v[164:167], v[0:3]\n\t"
        "v_mfma_f32_16x16x32_bf16 v[56:59], a[224:227], v[96:99], v[56:59]\n\t"
        "s_waitcnt lgkmcnt(7)\n\t"
        "v_mfma_f32_16x16x32_bf16 v[4:7], v[64:67], v[164:167], v[4:7]\n\t"
        "v_mfma_f32_16x16x32_bf16 v[48:51], a[252:255], v[100:103], v[48:51]\n\t"
        "ds_read_b128 v[96:99], v183 offset:128\n\t"
        "v_mfma_f32_16x16x32_bf16 v[8:11], v[68:71], v[164:167], v[8:11]\n\t"
        "s_nop 1\n\t"
        "v_cvt_pk_bf16_f32 v0, v0, v1\n\t"
        "v_cvt_pk_bf16_f32 v1, v2, v3\n\t"
        "v_mfma_f32_16x16x32_bf16 v[52:55], v[188:191], v[100:103], v[52:55]\n\t"
        "ds_write_b64 v186, v[0:1] offset:36864\n\t"
        "s_waitcnt lgkmcnt(4)\n\t"
        "v_mfma_f32_16x16x32_bf16 v[12:15], v[72:75], v[164:167], v[12:15]\n\t"
        "v_cvt_pk_bf16_f32 v4, v4, v5\n\t"
        "v_cvt_pk_bf16_f32 v5, v6, v7\n\t"
        "v_mfma_f32_16x16x32_bf16 v[56:59], v[192:195], v[100:103], v[56:59]\n\t"
        "ds_write_b64 v154, v[4:5] offset:36864\n\t"
        "v_mfma_f32_16x16x32_bf16 v[16:19], v[76:79], v[164:167], v[16:19]\n\t"
        "v_cvt_pk_bf16_f32 v8, v8, v9\n\t"
        "v_cvt_pk_bf16_f32 v9, v10, v11\n\t"
        "v_mfma_f32_16x16x32_bf16 v[48:51], v[220:223], v[104:107], v[48:51]\n\t"
        "ds_write_b64 v186, v[8:9] offset:36928\n\t"
        "s_waitcnt lgkmcnt(4)\n\t"
        "v_mfma_f32_16x16x32_bf16 v[20:23], v[80:83], v[164:167], v[20:23]\n\t"
        "v_cvt_pk_bf16_f32 v12, v12, v13\n\t"
        "v_cvt_pk_bf16_f32 v13, v14, v15\n\t"
        "v_mfma_f32_16x16x32_bf16 v[52:55], v[224:227], v[104:107], v[52:55]\n\t"
        "ds_write_b64 v154, v[12:13] offset:36928\n\t"
        "v_mfma_f32_16x16x32_bf16 v[56:59], v[228:231], v[104:107], v[56:59]\n\t"
        "v_cvt_pk_bf16_f32 v16, v16, v17\n\t"
        "v_cvt_pk_bf16_f32 v17, v18, v19\n\t"
        "v_mfma_f32_16x16x32_bf16 v[48:51], a[24:27], v[108:111], v[48:51]\n\t"
        "ds_write_b64 v186, v[16:17] offset:36992\n\t"
        "v_mfma_f32_16x16x32_bf16 v[52:55], a[28:31], v[108:111], v[52:55]\n\t"
        "v_cvt_pk_bf16_f32 v20, v20, v21\n\t"
        "v_cvt_pk_bf16_f32 v21, v22, v23\n\t"
        "v_mfma_f32_16x16x32_bf16 v[56:59], a[32:35], v[108:111], v[56:59]\n\t"
        "ds_write_b64 v154, v[20:21] offset:36992\n\t"
        "s_cmp_gt_u32 s61, 64\n\t"
        "s_cbranch_scc1 .Lx70%=\n\t"
        ".Lx71%=:\n\t"
        "v_mfma_f32_16x16x32_bf16 v[48:51], a[60:63], v[112:115], v[48:51]\n\t"
        "v_mfma_f32_16x16x32_bf16 v[52:55], a[64:67], v[112:115], v[52:55]\n\t"
        "v_mfma_f32_16x16x32_bf16 v[56:59], a[68:71], v[112:115], v[56:59]\n\t"
        "v_mfma_f32_16x16x32_bf16 v[48:51], a[96:99], v[84:87], v[48:51]\n\t"
        "v_mfma_f32_16x16x32_bf16 v[52:55], a[100:103], v[84:87], v[52:55]\n\t"
        "v_mfma_f32_16x16x32_bf16 v[56:59], a[104:107], v[84:87], v[56:59]\n\t"
        "s_waitcnt lgkmcnt(0)\n\t"
        "s_barrier\n\t"
        "v_xor_b32 v140, v181, v140\n\t"
        "v_mfma_f32_16x16x32_bf16 v[48:51], a[132:135], v[88:91], v[48:51]\n\t"
        "ds_read_b128 v[132:135], v187 offset:4096\n\t"
        "v_mfma_f32_16x16x32_bf16 v[52:55], a[136:139], v[88:91], v[52:55]\n\t"
        "ds_read_b128 v[136:139], v187 offset:4112\n\t"
        "v_add_u32 v151, s9, v180\n\t"
        "v_max_u32 v151, v151, v140\n\t"
        "s_waitcnt lgkmcnt(1)\n\t"
        "v_xor_b32 v132, v181, v132\n\t"
        "v_mfma_f32_16x16x32_bf16 v[56:59], a[140:143], v[88:91], v[56:59]\n\t"
        "ds_read_b128 v[100:103], v151 offset:0\n\t"
        "v_mfma_f32_16x16x32_bf16 v[48:51], a[168:171], v[92:95], v[48:51]\n\t"
        "ds_read_b128 v[104:107], v151 offset:64\n\t"
        "v_mfma_f32_16x16x32_bf16 v[52:55], a[172:175], v[92:95], v[52:55]\n\t"
        "ds_read_b128 v[108:111], v151 offset:128\n\t"
        "v_mfma_f32_16x16x32_bf16 v[56:59], a[176:179], v[92:95], v[56:59]\n\t"
        "ds_read_b32 v140, v187 offset:4128\n\t"
        "v_readlane_b32 s9, v155, 48\n\t"
        "v_mfma_f32_16x16x32_bf16 v[48:51], a[204:207], v[96:99], v[48:51]\n\t"
        "ds_read_b128 v[124:127], v182 offset:16\n\t"
        "v_add_u32 v183, s9, v172\n\t"
        "v_max_u32 v183, v183, v132\n\t"
        "v_xor_b32 v133, v181, v133\n\t"
        "v_mfma_f32_16x16x32_bf16 v[52:55], a[208:211], v[96:99], v[52:55]\n\t"
        "ds_read_b128 v[112:115], v183 offset:0\n\t"
        "v_add_u32 v151, s9, v173\n\t"
        "v_mfma_f32_16x16x32_bf16 v[56:59], a[212:215], v[96:99], v[56:59]\n\t"
        "ds_read_b128 v[84:87], v183 offset:64\n\t"
        "v_max_u32 v151, v151, v133\n\t"
        "s_waitcnt lgkmcnt(6)\n\t"
        "v_mfma_f32_16x16x32_bf16 v[48:51], a[240:243], v[100:103], v[48:51]\n\t"
        "ds_read_b128 v[88:91], v183 offset:128\n\t"
        "v_xor_b32 v134, v181, v134\n\t"
        "v_mfma_f32_16x16x32_bf16 v[52:55], a[244:247], v[100:103], v[52:55]\n\t"
        "ds_read_b128 v[92:95], v151 offset:0\n\t"
        "v_add_u32 v183, s9, v174\n\t"
        "v_mfma_f32_16x16x32_bf16 v[56:59], a[248:251], v[100:103], v[56:59]\n\t"
        "ds_read_b128 v[96:99], v151 offset:64\n\t"
        "v_max_u32 v183, v183, v134\n\t"
        "s_waitcnt lgkmcnt(8)\n\t"
        "v_mfma_f32_16x16x32_bf16 v[48:51], v[208:211], v[104:107], v[48:51]\n\t"
        "ds_read_b128 v[100:103], v151 offset:128\n\t"
        "v_xor_b32 v135, v181, v135\n\t"
        "v_mfma_f32_16x16x32_bf16 v[52:55], v[212:215], v[104:107], v[52:55]\n\t"
        "v_add_u32 v151, s9, v175\n\t"
        "v_max_u32 v151, v151, v135\n\t"
        "v_mfma_f32_16x16x32_bf16 v[56:59], v[216:219], v[104:107], v[56:59]\n\t"
        "v_xor_b32 v136, v181, v136\n\t"
        "s_waitcnt lgkmcnt(7)\n\t"
        "v_mfma_f32_16x16x32_bf16 v[48:51], v[244:247], v[108:111], v[48:51]\n\t"
        "ds_read_b128 v[104:107], v183 offset:0\n\t"
        "s_waitcnt lgkmcnt(7)\n\t"
        "v_or3_b32 v124, v124, v125, v126\n\t"
        "v_mfma_f32_16x16x32_bf16 v[52:55], v[248:251], v[108:111], v[52:55]\n\t"
        "v_or_b32 v124, v124, v127\n\t"
        "v_mfma_f32_16x16x32_bf16 v[56:59], v[252:255], v[108:111], v[56:59]\n\t"
        "v_readfirstlane_b32 s8, v124\n\t"
        "s_waitcnt lgkmcnt(6)\n\t"
        "v_mfma_f32_16x16x32_bf16 v[72:75], a[12:15], v[112:115], 0\n\t"
        "ds_read_b128 v[124:127], v147 offset:0\n\t"
        "s_or_b32 s6, s6, s8\n\t"
        "v_mfma_f32_16x16x32_bf16 v[76:79], a[16:19], v[112:115], 0\n\t"
        "ds_read_b128 v[108:111], v183 offset:64\n\t"
        "v_mfma_f32_16x16x32_bf16 v[80:83], a[20:23], v[112:115], 0\n\t"
        "s_waitcnt lgkmcnt(5)\n\t"
        "v_mfma_f32_16x16x32_bf16 v[72:75], a[48:51], v[84:87], v[72:75]\n\t"
        "ds_read_b128 v[112:115], v183 offset:128\n\t"
        "v_add_u32 v183, s9, v176\n\t"
        "v_mfma_f32_16x16x32_bf16 v[76:79], a[52:55], v[84:87], v[76:79]\n\t"
        "v_max_u32 v183, v183, v136\n\t"
        "v_xor_b32 v137, v181, v137\n\t"
        "v_mfma_f32_16x16x32_bf16 v[80:83], a[56:59], v[84:87], v[80:83]\n\t"
        "v_mfma_f32_16x16x32_bf16 v[72:75], a[84:87], v[88:91], v[72:75]\n\t"
        "ds_read_b128 v[84:87], v151 offset:0\n\t"
        "v_mfma_f32_16x16x32_bf16 v[76:79], a[88:91], v[88:91], v[76:79]\n\t"
        "v_mfma_f32_16x16x32_bf16 v[80:83], a[92:95], v[88:91], v[80:83]\n\t"
        "v_mfma_f32_16x16x32_bf16 v[72:75], a[120:123], v[92:95], v[72:75]\n\t"
        "s_waitcnt lgkmcnt(3)\n\t"
        "buffer_store_dwordx4 v[124:127], v145, s[28:31], s50 offen sc1\n\t"
        "v_mfma_f32_16x16x32_bf16 v[76:79], a[124:127], v[92:95], v[76:79]\n\t"
        "ds_read_b128 v[124:127], v147 offset:4096\n\t"
        "s_add_u32 s50, s50, 86016\n\t"
        "v_mfma_f32_16x16x32_bf16 v[80:83], a[128:131], v[92:95], v[80:83]\n\t"
        "ds_read_b128 v[88:91], v151 offset:64\n\t"
        "v_mfma_f32_16x16x32_bf16 v[72:75], a[156:159], v[96:99], v[72:75]\n\t"
        "ds_read_b128 v[92:95], v151 offset:128\n\t"
        "v_add_u32 v151, s9, v177\n\t"
        "v_mfma_f32_16x16x32_bf16 v[76:79], a[160:163], v[96:99], v[76:79]\n\t"
        "v_max_u32 v151, v151, v137\n\t"
        "v_xor_b32 v138, v181, v138\n\t"
        "v_mfma_f32_16x16x32_bf16 v[80:83], a[164:167], v[96:99], v[80:83]\n\t"
        "v_mfma_f32_16x16x32_bf16 v[72:75], a[192:195], v[100:103], v[72:75]\n\t"
        "ds_read_b128 v[96:99], v183 offset:0\n\t"
        "v_mfma_f32_16x16x32_bf16 v[76:79], a[196:199], v[100:103], v[76:79]\n\t"
        "v_mfma_f32_16x16x32_bf16 v[80:83], a[200:203], v[100:103], v[80:83]\n\t"
        "v_mfma_f32_16x16x32_bf16 v[72:75], a[228:231], v[104:107], v[72:75]\n\t"
        "ds_read_b128 v[100:103], v183 offset:64\n\t"
        "v_mfma_f32_16x16x32_bf16 v[76:79], a[232:235], v[104:107], v[76:79]\n\t"
        "v_mfma_f32_16x16x32_bf16 v[80:83], a[236:239], v[104:107], v[80:83]\n\t"
        "s_waitcnt lgkmcnt(4)\n\t"
        "v_mfma_f32_16x16x32_bf16 v[72:75], v[196:199], v[108:111], v[72:75]\n\t"
        "ds_read_b128 v[104:107], v183 offset:128\n\t"
        "v_add_u32 v183, s9, v178\n\t"
        "v_mfma_f32_16x16x32_bf16 v[76:79], v[200:203], v[108:111], v[76:79]\n\t"
        "v_max_u32 v183, v183, v138\n\t"
        "v_xor_b32 v139, v181, v139\n\t"
        "v_mfma_f32_16x16x32_bf16 v[80:83], v[204:207], v[108:111], v[80:83]\n\t"
        "buffer_store_dwordx4 v[124:127], v145, s[28:31], s50 offen sc1\n\t"
        "v_mfma_f32_16x16x32_bf16 v[72:75], v[232:235], v[112:115], v[72:75]\n\t"
        "ds_read_b128 v[108:111], v151 offset:0\n\t"
        "v_readlane_b32 s55, v155, 49\n\t"
        "v_mfma_f32_16x16x32_bf16 v[76:79], v[236:239], v[112:115], v[76:79]\n\t"
        "v_cvt_pk_bf16_f32 v116, v48, v49\n\t"
        "v_cvt_pk_bf16_f32 v117, v50, v51\n\t"
        "v_mfma_f32_16x16x32_bf16 v[80:83], v[240:243], v[112:115], v[80:83]\n\t"
        "v_cvt_pk_bf16_f32 v118, v52, v53\n\t"
        "v_cvt_pk_bf16_f32 v119, v54, v55\n\t"
        "v_mfma_f32_16x16x32_bf16 v[72:75], a[0:3], v[84:87], v[72:75]\n\t"
        "ds_read_b128 v[112:115], v151 offset:64\n\t"
        "v_cvt_pk_bf16_f32 v120, v56, v57\n\t"
        "v_mfma_f32_16x16x32_bf16 v[76:79], a[4:7], v[84:87], v[76:79]\n\t"
        ".LpX2%=:\n\t"
        "s_cbranch_execz .LrX%=\n\t"
        "v_cvt_pk_bf16_f32 v121, v58, v59\n\t"
        "v_mfma_f32_16x16x32_bf16 v[80:83], a[8:11], v[84:87], v[80:83]\n\t"
        "v_permlane16_swap_b32 v116, v118\n\t"
        "s_waitcnt lgkmcnt(3)\n\t"
        "v_mfma_f32_16x16x32_bf16 v[72:75], a[36:39], v[88:91], v[72:75]\n\t"
        "v_permlane16_swap_b32 v117, v119\n\t"
        "v_mfma_f32_16x16x32_bf16 v[76:79], a[40:43], v[88:91], v[76:79]\n\t"
        "ds_read_b128 v[84:87], v151 offset:128\n\t"
        "v_add_u32 v151, s9, v179\n\t"
        "v_mfma_f32_16x16x32_bf16 v[80:83], a[44:47], v[88:91], v[80:83]\n\t"
        "v_max_u32 v151, v151, v139\n\t"
        "v_xor_b32 v140, v181, v140\n\t"
        "v_mfma_f32_16x16x32_bf16 v[72:75], a[72:75], v[92:95], v[72:75]\n\t"
        "ds_read_b128 v[88:91], v183 offset:0\n\t"
        "v_mfma_f32_16x16x32_bf16 v[76:79], a[76:79], v[92:95], v[76:79]\n\t"
        "v_mfma_f32_16x16x32_bf16 v[80:83], a[80:83], v[92:95], v[80:83]\n\t"
        "v_mfma_f32_16x16x32_bf16 v[72:75], a[108:111], v[96:99], v[72:75]\n\t"
        "ds_read_b128 v[92:95], v183 offset:64\n\t"
        "v_mfma_f32_16x16x32_bf16 v[76:79], a[112:115], v[96:99], v[76:79]\n\t"
        "buffer_store_dwordx4 v[116:119], v148, s[28:31], s55 offen sc1\n\t"
        "v_mfma_f32_16x16x32_bf16 v[80:83], a[116:119], v[96:99], v[80:83]\n\t"
        "ds_write_b64 v146, v[120:121] offset:8192\n\t"
        "v_mfma_f32_16x16x32_bf16 v[72:75], a[144:147], v[100:103], v[72:75]\n\t"
        "ds_read_b128 v[96:99], v183 offset:128\n\t"
        "v_add_u32 v183, s9, v180\n\t"
        "v_mfma_f32_16x16x32_bf16 v[76:79], a[148:151], v[100:103], v[76:79]\n\t"
        "v_max_u32 v183, v183, v140\n\t"
        "v_mfma_f32_16x16x32_bf16 v[80:83], a[152:155], v[100:103], v[80:83]\n\t"
        "ds_read_b128 v[100:103], v151 offset:0\n\t"
        "s_waitcnt lgkmcnt(5)\n\t"
        "v_mfma_f32_16x16x32_bf16 v[72:75], a[180:183], v[104:107], v[72:75]\n\t"
        "v_mfma_f32_16x16x32_bf16 v[76:79], a[184:187], v[104:107], v[76:79]\n\t"
        "v_mfma_f32_16x16x32_bf16 v[80:83], a[188:191], v[104:107], v[80:83]\n\t"
        "ds_read_b128 v[104:107], v151 offset:64\n\t"
        "v_mfma_f32_16x16x32_bf16 v[72:75], a[216:219], v[108:111], v[72:75]\n\t"
        "v_mfma_f32_16x16x32_bf16 v[76:79], a[220:223], v[108:111], v[76:79]\n\t"
        "v_mfma_f32_16x16x32_bf16 v[80:83], a[224:227], v[108:111], v[80:83]\n\t"
        "ds_read_b128 v[108:111], v151 offset:128\n\t"
        "v_mfma_f32_16x16x32_bf16 v[72:75], a[252:255], v[112:115], v[72:75]\n\t"
        "v_mfma_f32_16x16x32_bf16 v[76:79], v[188:191], v[112:115], v[76:79]\n\t"
        "v_mfma_f32_16x16x32_bf16 v[80:83], v[192:195], v[112:115], v[80:83]\n\t"
        "ds_read_b128 v[112:115], v183 offset:0\n\t"
        "v_mfma_f32_16x16x32_bf16 v[72:75], v[220:223], v[84:87], v[72:75]\n\t"
        "v_mfma_f32_16x16x32_bf16 v[76:79], v[224:227], v[84:87], v[76:79]\n\t"
        "v_mfma_f32_16x16x32_bf16 v[80:83], v[228:231], v[84:87], v[80:83]\n\t"
        "ds_read_b128 v[84:87], v183 offset:64\n\t"
        "s_waitcnt lgkmcnt(4)\n\t"
        "v_mfma_f32_16x16x32_bf16 v[72:75], a[24:27], v[88:91], v[72:75]\n\t"
        "v_mfma_f32_16x16x32_bf16 v[76:79], a[28:31], v[88:91], v[76:79]\n\t"
        "v_mfma_f32_16x16x32_bf16 v[80:83], a[32:35], v[88:91], v[80:83]\n\t"
        "ds_read_b128 v[88:91], v183 offset:128\n\t"
        "v_mfma_f32_16x16x32_bf16 v[80:83], a[68:71], v[92:95], v[80:83]\n\t"
        "v_mfma_f32_16x16x32_bf16 v[80:83], a[104:107], v[96:99], v[80:83]\n\t"
        "v_mfma_f32_16x16x32_bf16 v[80:83], a[140:143], v[100:103], v[80:83]\n\t"
        "s_waitcnt lgkmcnt(3)\n\t"
        "v_mfma_f32_16x16x32_bf16 v[80:83], a[176:179], v[104:107], v[80:83]\n\t"
        "v_mfma_f32_16x16x32_bf16 v[80:83], a[212:215], v[108:111], v[80:83]\n\t"
        "s_waitcnt lgkmcnt(2)\n\t"
        "v_mfma_f32_16x16x32_bf16 v[80:83], a[248:251], v[112:115], v[80:83]\n\t"
        "s_waitcnt lgkmcnt(1)\n\t"
        "v_mfma_f32_16x16x32_bf16 v[80:83], v[216:219], v[84:87], v[80:83]\n\t"
        "s_waitcnt lgkmcnt(0)\n\t"
        "v_mfma_f32_16x16x32_bf16 v[80:83], v[252:255], v[88:91], v[80:83]\n\t"
        "v_mfma_f32_16x16x32_bf16 v[72:75], a[60:63], v[92:95], v[72:75]\n\t"
        "v_mfma_f32_16x16x32_bf16 v[76:79], a[64:67], v[92:95], v[76:79]\n\t"
        "s_nop 5\n\t"
        "v_cvt_pk_bf16_f32 v120, v80, v81\n\t"
        "v_cvt_pk_bf16_f32 v121, v82, v83\n\t"
        "v_mfma_f32_16x16x32_bf16 v[72:75], a[96:99], v[96:99], v[72:75]\n\t"
        "ds_write_b64 v146, v[120:121] offset:10240\n\t"
        "v_mfma_f32_16x16x32_bf16 v[76:79], a[100:103], v[96:99], v[76:79]\n\t"
        "v_mfma_f32_16x16x32_bf16 v[72:75], a[132:135], v[100:103], v[72:75]\n\t"
        "v_mfma_f32_16x16x32_bf16 v[76:79], a[136:139], v[100:103], v[76:79]\n\t"
        "s_waitcnt lgkmcnt(0)\n\t"
        "s_barrier\n\t"
        "v_mfma_f32_16x16x32_bf16 v[72:75], a[168:171], v[104:107], v[72:75]\n\t"
        "ds_read_b128 v[124:127], v147 offset:8192\n\t"
        "v_readlane_b32 s55, v155, 51\n\t"
        "v_mfma_f32_16x16x32_bf16 v[76:79], a[172:175], v[104:107], v[76:79]\n\t"
        "v_mfma_f32_16x16x32_bf16 v[72:75], a[204:207], v[108:111], v[72:75]\n\t"
        "v_mfma_f32_16x16x32_bf16 v[76:79], a[208:211], v[108:111], v[76:79]\n\t"
        "v_mfma_f32_16x16x32_bf16 v[72:75], a[240:243], v[112:115], v[72:75]\n\t"
        "v_mfma_f32_16x16x32_bf16 v[76:79], a[244:247], v[112:115], v[76:79]\n\t"
        "v_mfma_f32_16x16x32_bf16 v[72:75], v[208:211], v[84:87], v[72:75]\n\t"
        "v_mfma_f32_16x16x32_bf16 v[76:79], v[212:215], v[84:87], v[76:79]\n\t"
        "v_mfma_f32_16x16x32_bf16 v[72:75], v[244:247], v[88:91], v[72:75]\n\t"
        "v_mfma_f32_16x16x32_bf16 v[76:79], v[248:251], v[88:91], v[76:79]\n\t"
        "s_nop 6\n\t"
        "v_cvt_pk_bf16_f32 v116, v72, v73\n\t"
        "v_cvt_pk_bf16_f32 v117, v74, v75\n\t"
        "v_cvt_pk_bf16_f32 v118, v76, v77\n\t"
        "v_cvt_pk_bf16_f32 v119, v78, v79\n\t"
        "s_nop 0\n\t"
        "v_permlane16_swap_b32 v116, v118\n\t"
        "v_permlane16_swap_b32 v117, v119\n\t"
        "buffer_store_dwordx4 v[116:119], v148, s[28:31], s55 offen  \n\t"
        "s_waitcnt lgkmcnt(0)\n\t"
        "buffer_store_dwordx4 v[124:127], v150, s[28:31], s52 offen  \n\t"
        "s_andn2_b32 s6, s6, 2\n\t"
        "s_cbranch_scc1 99f\n\t"
        "s_endpgm\n\t"
        "22:\n\t"
        "ds_bpermute_b32 v64, v65, v155 offset:172\n\t"
        "s_add_u32 m0, s37, -24576\n\t"
        "s_add_u32 s36, s36, 6144\n\t"
        "buffer_load_dwordx4 v140, s[24:27], s36 offen offset:0 lds\n\t"
        "ds_bpermute_b32 v78, v81, v155 offset:148\n\t"
        "ds_bpermute_b32 v79, v81, v155 offset:160\n\t"
        "v_readlane_b32 s71, v155, 26\n\t"
        "v_readlane_b32 s10, v155, 29\n\t"
        "s_nop 0\n\t"
        "v_mul_u32_u24 v73, s71, v66\n\t"
        "v_lshrrev_b32 v73, 11, v73\n\t"
        "v_lshlrev_b32 v66, 4, v66\n\t"
        "v_mad_u32_u24 v73, v73, s10, v66\n\t"
        "s_waitcnt lgkmcnt(2)\n\t"
        "v_add_u32 v64, v64, v73\n\t"
        "buffer_load_dwordx4 v141, s[24:27], s36 offen offset:1024 lds\n\t"
        "v_readlane_b32 s69, v155, 34\n\t"
        "v_readlane_b32 s70, v155, 35\n\t"
        "s_nop 0\n\t"
        "v_mad_u32_u24 v70, v52, s69, v70\n\t"
        "v_mad_u32_u24 v70, v158, s70, v70\n\t"
        "s_mul_i32 s71, s69, s68\n\t"
        "s_add_u32 s71, s71, 157728\n\t"
        "v_mad_u32_u24 v67, v82, s70, v67\n\t"
        "v_add_u32 v67, s71, v67\n\t"
        "v_readlane_b32 s11, v155, 30\n\t"
        "buffer_load_dwordx4 v142, s[24:27], s36 offen offset:2048 lds\n\t"
        "s_movk_i32 s10, 223\n\t"
        "v_mov_b32 v75, 0x7ff00000\n\t"
        "v_readlane_b32 s12, v155, 24\n\t"
        "v_readlane_b32 s13, v155, 25\n\t"
        "s_nop 0\n\t"
        "s_bfe_u32 s70, s12, 524288\n\t"
        "s_bfe_u32 s71, s13, 524288\n\t"
        "v_cmp_gt_u32 vcc, s71, v71\n\t"
        "v_add3_u32 v73, v71, s70, -2\n\t"
        "v_med3_i32 v73, v73, 0, s10\n\t"
        "s_add_u32 m0, s35, 4096\n\t"
        "s_add_u32 s43, s34, 43008\n\t"
        "buffer_load_dwordx4 v143, s[16:19], s43 offen  lds\n\t"
        "v_cndmask_b32 v74, v75, v72, vcc\n\t"
        "v_mad_u32_u24 v134, v73, s11, v74\n\t"
        "s_bfe_u32 s70, s12, 524296\n\t"
        "s_bfe_u32 s71, s13, 524296\n\t"
        "v_cmp_gt_u32 vcc, s71, v71\n\t"
        "v_add3_u32 v73, v71, s70, -2\n\t"
        "v_med3_i32 v73, v73, 0, s10\n\t"
        "v_cndmask_b32 v74, v75, v72, vcc\n\t"
        "v_mad_u32_u24 v135, v73, s11, v74\n\t"
        "s_add_u32 m0, s35, 20480\n\t"
        "s_add_u32 s43, s34, 215040\n\t"
        "buffer_load_dwordx4 v143, s[16:19], s43 offen  lds\n\t"
        "s_bfe_u32 s70, s12, 524304\n\t"
        "s_bfe_u32 s71, s13, 524304\n\t"
        "v_cmp_gt_u32 vcc, s71, v71\n\t"
        "v_add3_u32 v73, v71, s70, -2\n\t"
        "v_med3_i32 v73, v73, 0, s10\n\t"
        "v_cndmask_b32 v74, v75, v72, vcc\n\t"
        "v_mad_u32_u24 v136, v73, s11, v74\n\t"
        "s_bfe_u32 s70, s12, 524312\n\t"
        "s_bfe_u32 s71, s13, 524312\n\t"
        "v_cmp_gt_u32 vcc, s71, v71\n\t"
        "v_add3_u32 v73, v71, s70, -2\n\t"
        "v_med3_i32 v73, v73, 0, s10\n\t"
        "v_cndmask_b32 v74, v75, v72, vcc\n\t"
        "v_mad_u32_u24 v137, v73, s11, v74\n\t"
        "s_mov_b64 exec, 0\n\t"
        "s_cmp_eq_u32 s4, 0\n\t"
        "s_cbranch_scc1 .LpY0%=\n\t"
        "s_cmp_eq_u32 s4, 1\n\t"
        "s_cbranch_scc1 .LpY1%=\n\t"
        "s_cmp_eq_u32 s4, 2\n\t"
        "s_cbranch_scc1 .LpY2%=\n\t"
        "s_nop 0\n\t"
        ".LrY%=:\n\t"
        "s_mov_b64 exec, -1\n\t"
        "s_waitcnt vmcnt(9)\n\t"
        "s_barrier\n\t"
        "ds_read_b64_tr_b16 a[0:1], v138 offset:6144\n\t"
        "ds_read_b64_tr_b16 a[2:3], v138 offset:6912\n\t"
        "ds_read_b64_tr_b16 a[4:5], v139 offset:6144\n\t"
        "ds_read_b64_tr_b16 a[6:7], v139 offset:6912\n\t"
        "ds_read_b64_tr_b16 a[8:9], v149 offset:6144\n\t"
        "ds_read_b64_tr_b16 a[10:11], v149 offset:6912\n\t"
        "ds_read_b128 v[84:87], v128 offset:8192\n\t"
        "ds_read_b64_tr_b16 a[12:13], v138 offset:12288\n\t"
        "ds_read_b64_tr_b16 a[14:15], v138 offset:13056\n\t"
        "ds_read_b64_tr_b16 a[16:17], v139 offset:12288\n\t"
        "ds_read_b64_tr_b16 a[18:19], v139 offset:13056\n\t"
        "ds_read_b64_tr_b16 a[20:21], v149 offset:12288\n\t"
        "ds_read_b64_tr_b16 a[22:23], v149 offset:13056\n\t"
        "ds_read_b128 v[88:91], v128 offset:8192\n\t"
        "s_add_u32 m0, s37, -12288\n\t"
        "s_add_u32 s36, s36, s39\n\t"
        "s_waitcnt lgkmcnt(7)\n\t"
        "v_mfma_f32_16x16x32_bf16 v[12:15], a[0:3], v[84:87], 0\n\t"
        "buffer_load_dwordx4 v140, s[24:27], s36 offen offset:0 lds\n\t"
        "buffer_load_dwordx4 v141, s[24:27], s36 offen offset:1024 lds\n\t"
        "buffer_load_dwordx4 v142, s[24:27], s36 offen offset:2048 lds\n\t"
        "s_waitcnt vmcnt(8)\n\t"
        "s_barrier\n\t"
        "ds_read_b128 v[92:95], v128 offset:12288\n\t"
        "ds_read_b64_tr_b16 a[24:25], v138 offset:0\n\t"
        "v_mfma_f32_16x16x32_bf16 v[16:19], a[4:7], v[84:87], 0\n\t"
        "ds_read_b64_tr_b16 a[26:27], v138 offset:768\n\t"
        "s_add_u32 m0, s35, 157696\n\t"
        "s_nop 0\n\t"
        "buffer_load_dwordx4 v64, s[20:23], 0 offen lds\n\t"
        "s_add_u32 m0, s35, 16384\n\t"
        "s_add_u32 s43, s34, 172032\n\t"
        "v_mfma_f32_16x16x32_bf16 v[20:23], a[8:11], v[84:87], 0\n\t"
        "buffer_load_dwordx4 v143, s[16:19], s43 offen  lds\n\t"
        "s_add_u32 m0, s35, 32768\n\t"
        "ds_read_b64_tr_b16 a[28:29], v139 offset:0\n\t"
        "s_add_u32 s43, s34, 344064\n\t"
        "s_min_u32 s43, s43, 9590784\n\t"
        "s_waitcnt lgkmcnt(4)\n\t"
        "v_mfma_f32_16x16x32_bf16 v[24:27], a[12:15], v[88:91], 0\n\t"
        "buffer_load_dwordx4 v143, s[16:19], s43 offen  lds\n\t"
        "s_add_u32 m0, s37, 0\n\t"
        "ds_read_b64_tr_b16 a[30:31], v139 offset:768\n\t"
        "s_add_u32 s36, s36, s38\n\t"
        "ds_read_b64_tr_b16 a[32:33], v149 offset:0\n\t"
        "v_mfma_f32_16x16x32_bf16 v[28:31], a[16:19], v[88:91], 0\n\t"
        "buffer_load_dwordx4 v140, s[24:27], s36 offen offset:0 lds\n\t"
        "ds_read_b64_tr_b16 a[34:35], v149 offset:768\n\t"
        "ds_read_b128 v[96:99], v128 offset:8192\n\t"
        "v_readlane_b32 s44, v155, 32\n\t"
        "v_mfma_f32_16x16x32_bf16 v[32:35], a[20:23], v[88:91], 0\n\t"
        "buffer_load_dwordx4 v141, s[24:27], s36 offen offset:1024 lds\n\t"
        "v_readlane_b32 s45, v155, 33\n\t"
        "s_waitcnt lgkmcnt(7)\n\t"
        "v_mfma_f32_16x16x32_bf16 v[24:27], a[0:3], v[92:95], v[24:27]\n\t"
        "ds_read_b128 v[100:103], v128 offset:12288\n\t"
        "v_readlane_b32 s62, v155, 36\n\t"
        "v_mfma_f32_16x16x32_bf16 v[28:31], a[4:7], v[92:95], v[28:31]\n\t"
        "s_nop 0\n\t"
        "s_lshl_b32 s70, s62, 5\n\t"
        "s_sub_u32 s71, s70, 16384\n\t"
        "v_mfma_f32_16x16x32_bf16 v[32:35], a[8:11], v[92:95], v[32:35]\n\t"
        "buffer_load_dwordx4 v142, s[24:27], s36 offen offset:2048 lds\n\t"
        "s_add_u32 m0, s35, 0\n\t"
        "s_add_u32 s43, s34, 0\n\t"
        "v_mfma_f32_16x16x32_bf16 v[36:39], a[12:15], v[92:95], 0\n\t"
        "s_mov_b32 exec_lo, 0\n\t"
        "s_mov_b32 exec_hi, 0xfff00000\n\t"
        "ds_write_b32 v119, v155\n\t"
        "s_mov_b64 exec, -1\n\t"
        "v_mfma_f32_16x16x32_bf16 v[40:43], a[16:19], v[92:95], 0\n\t"
        "s_mul_i32 s71, s71, s68\n\t"
        "v_add_u32 v172, s71, v128\n\t"
        "v_mfma_f32_16x16x32_bf16 v[44:47], a[20:23], v[92:95], 0\n\t"
        "buffer_load_dwordx4 v143, s[16:19], s43 offen  lds\n\t"
        "s_add_u32 m0, s35, 36864\n\t"
        "s_add_u32 s43, s34, 387072\n\t"
        "s_min_u32 s43, s43, 9590784\n\t"
        "buffer_load_dwordx4 v143, s[16:19], s43 offen  lds\n\t"
        "s_waitcnt vmcnt(8)\n\t"
        "s_barrier\n\t"
        "ds_read_b128 v[104:107], v128 offset:4096\n\t"
        "s_add_u32 m0, s37, 12288\n\t"
        "ds_read_b64_tr_b16 a[36:37], v138 offset:18432\n\t"
        "s_add_u32 s36, s36, s5\n\t"
        "s_waitcnt lgkmcnt(4)\n\t"
        "v_mfma_f32_16x16x32_bf16 v[0:3], a[24:27], v[96:99], 0\n\t"
        "buffer_load_dwordx4 v140, s[24:27], s36 offen offset:0 lds\n\t"
        "ds_read_b64_tr_b16 a[38:39], v138 offset:19200\n\t"
        "v_add_u32 v173, s71, v129\n\t"
        "v_mfma_f32_16x16x32_bf16 v[4:7], a[28:31], v[96:99], 0\n\t"
        "buffer_load_dwordx4 v141, s[24:27], s36 offen offset:1024 lds\n\t"
        "ds_read_b64_tr_b16 a[40:41], v139 offset:18432\n\t"
        "v_add_u32 v174, s71, v130\n\t"
        "v_mfma_f32_16x16x32_bf16 v[8:11], a[32:35], v[96:99], 0\n\t"
        "ds_read_b64_tr_b16 a[42:43], v139 offset:19200\n\t"
        "ds_read_b64_tr_b16 a[44:45], v149 offset:18432\n\t"
        "s_add_u32 s71, s71, s70\n\t"
        "v_add_u32 v175, s71, v128\n\t"
        "s_waitcnt lgkmcnt(7)\n\t"
        "v_mfma_f32_16x16x32_bf16 v[12:15], a[24:27], v[100:103], v[12:15]\n\t"
        "buffer_load_dwordx4 v142, s[24:27], s36 offen offset:2048 lds\n\t"
        "s_add_u32 m0, s37, 24576\n\t"
        "ds_read_b64_tr_b16 a[46:47], v149 offset:19200\n\t"
        "s_add_u32 s36, s36, s39\n\t"
        "ds_read_b64_tr_b16 a[48:49], v138 offset:24576\n\t"
        "v_mfma_f32_16x16x32_bf16 v[16:19], a[28:31], v[100:103], v[16:19]\n\t"
        "ds_read_b64_tr_b16 a[50:51], v138 offset:25344\n\t"
        "ds_read_b64_tr_b16 a[52:53], v139 offset:24576\n\t"
        "ds_read_b64_tr_b16 a[54:55], v139 offset:25344\n\t"
        "v_add_u32 v176, s71, v129\n\t"
        "v_add_u32 v177, s71, v130\n\t"
        "v_mfma_f32_16x16x32_bf16 v[20:23], a[32:35], v[100:103], v[20:23]\n\t"
        "ds_read_b64_tr_b16 a[56:57], v149 offset:24576\n\t"
        "ds_read_b64_tr_b16 a[58:59], v149 offset:25344\n\t"
        "ds_read_b128 v[108:111], v128 offset:4160\n\t"
        "s_add_u32 s71, s71, s70\n\t"
        "s_waitcnt lgkmcnt(11)\n\t"
        "v_mfma_f32_16x16x32_bf16 v[0:3], a[0:3], v[104:107], v[0:3]\n\t"
        "ds_read_b64_tr_b16 a[60:61], v138 offset:30720\n\t"
        "ds_read_b64_tr_b16 a[62:63], v138 offset:31488\n\t"
        "v_add_u32 v178, s71, v128\n\t"
        "v_add_u32 v179, s71, v129\n\t"
        "v_mfma_f32_16x16x32_bf16 v[4:7], a[4:7], v[104:107], v[4:7]\n\t"
        "ds_read_b64_tr_b16 a[64:65], v139 offset:30720\n\t"
        "ds_read_b64_tr_b16 a[66:67], v139 offset:31488\n\t"
        "v_add_u32 v180, s71, v130\n\t"
        "v_mfma_f32_16x16x32_bf16 v[8:11], a[8:11], v[104:107], v[8:11]\n\t"
        "ds_read_b64_tr_b16 a[68:69], v149 offset:30720\n\t"
        "ds_read_b64_tr_b16 a[70:71], v149 offset:31488\n\t"
        "v_add_f32 v78, v78, v56\n\t"
        "v_add_f32 v79, v79, v57\n\t"
        "v_mfma_f32_16x16x32_bf16 v[12:15], a[12:15], v[104:107], v[12:15]\n\t"
        "ds_read_b128 v[112:115], v128 offset:8256\n\t"
        "v_mfma_f32_16x16x32_bf16 v[16:19], a[16:19], v[104:107], v[16:19]\n\t"
        "s_cmp_eq_u32 s69, 36\n\t"
        "s_cselect_b64 exec, -1, 0\n\t"
        "v_swap_b32 v68, v69\n\t"
        "v_swap_b32 v173, v175\n\t"
        "v_swap_b32 v174, v178\n\t"
        "v_swap_b32 v177, v179\n\t"
        "s_mov_b64 exec, -1\n\t"
        "v_mfma_f32_16x16x32_bf16 v[20:23], a[20:23], v[104:107], v[20:23]\n\t"
        "ds_read_b128 v[84:87], v128 offset:12352\n\t"
        "v_add3_u32 v68, v68, v52, -1\n\t"
        "v_add3_u32 v69, v69, v158, -1\n\t"
        "s_waitcnt lgkmcnt(8)\n\t"
        "v_mfma_f32_16x16x32_bf16 v[0:3], a[36:39], v[108:111], v[0:3]\n\t"
        "ds_read_b64_tr_b16 a[72:73], v138 offset:36864\n\t"
        "ds_read_b64_tr_b16 a[74:75], v138 offset:37632\n\t"
        "v_cvt_f32_i32 v68, v68\n\t"
        "v_cvt_f32_i32 v69, v69\n\t"
        "v_mfma_f32_16x16x32_bf16 v[4:7], a[40:43], v[108:111], v[4:7]\n\t"
        "ds_read_b64_tr_b16 a[76:77], v139 offset:36864\n\t"
        "ds_read_b64_tr_b16 a[78:79], v139 offset:37632\n\t"
        "v_readlane_b32 s69, v155, 31\n\t"
        "v_mfma_f32_16x16x32_bf16 v[8:11], a[44:47], v[108:111], v[8:11]\n\t"
        "ds_read_b64_tr_b16 a[80:81], v149 offset:36864\n\t"
        "ds_read_b64_tr_b16 a[82:83], v149 offset:37632\n\t"
        "v_mov_b32 v154, s69\n\t"
        "v_mov_b32 v119, s62\n\t"
        "v_mfma_f32_16x16x32_bf16 v[12:15], a[48:51], v[108:111], v[12:15]\n\t"
        "v_readlane_b32 s53, v155, 28\n\t"
        "v_readlane_b32 s9, v155, 46\n\t"
        "v_mfma_f32_16x16x32_bf16 v[16:19], a[52:55], v[108:111], v[16:19]\n\t"
        "buffer_load_dwordx4 v140, s[24:27], s36 offen offset:0 lds\n\t"
        "s_sub_u32 s10, 43200, s11\n\t"
        "s_mul_i32 s10, s10, s68\n\t"
        "v_mfma_f32_16x16x32_bf16 v[20:23], a[56:59], v[108:111], v[20:23]\n\t"
        "s_sub_u32 s11, s11, 192\n\t"
        "v_mad_u32_u24 v148, v158, s11, v144\n\t"
        "s_waitcnt lgkmcnt(7)\n\t"
        "v_mfma_f32_16x16x32_bf16 v[0:3], a[60:63], v[112:115], v[0:3]\n\t"
        "v_add_u32 v148, s10, v148\n\t"
        "v_mad_u32_u24 v150, v83, s11, v145\n\t"
        "s_lshl_b32 s70, s49, 1\n\t"
        "v_mfma_f32_16x16x32_bf16 v[4:7], a[64:67], v[112:115], v[4:7]\n\t"
        "s_add_u32 s70, s70, 49\n\t"
        "v_readlane_b32 s52, v155, s70\n\t"
        "v_mfma_f32_16x16x32_bf16 v[8:11], a[68:71], v[112:115], v[8:11]\n\t"
        "buffer_load_dwordx4 v141, s[24:27], s36 offen offset:1024 lds\n\t"
        "s_add_u32 s52, s52, s10\n\t"
        "v_mfma_f32_16x16x32_bf16 v[12:15], a[36:39], v[112:115], v[12:15]\n\t"
        "v_mfma_f32_16x16x32_bf16 v[16:19], a[40:43], v[112:115], v[16:19]\n\t"
        "v_mfma_f32_16x16x32_bf16 v[20:23], a[44:47], v[112:115], v[20:23]\n\t"
        "v_mfma_f32_16x16x32_bf16 v[24:27], a[48:51], v[112:115], v[24:27]\n\t"
        "buffer_load_dwordx4 v142, s[24:27], s36 offen offset:2048 lds\n\t"
        "v_mfma_f32_16x16x32_bf16 v[28:31], a[52:55], v[112:115], v[28:31]\n\t"
        "v_mfma_f32_16x16x32_bf16 v[32:35], a[56:59], v[112:115], v[32:35]\n\t"
        "s_waitcnt lgkmcnt(0)\n\t"
        "v_mfma_f32_16x16x32_bf16 v[12:15], a[60:63], v[84:87], v[12:15]\n\t"
        "v_mfma_f32_16x16x32_bf16 v[16:19], a[64:67], v[84:87], v[16:19]\n\t"
        "buffer_load_dwordx4 v[48:51], v55, s[20:23], s48 offen\n\t"
        "v_mfma_f32_16x16x32_bf16 v[20:23], a[68:71], v[84:87], v[20:23]\n\t"
        "v_mfma_f32_16x16x32_bf16 v[24:27], a[36:39], v[84:87], v[24:27]\n\t"
        "ds_read_b128 v[88:91], v128 offset:4224\n\t"
        "v_mfma_f32_16x16x32_bf16 v[28:31], a[40:43], v[84:87], v[28:31]\n\t"
        "v_mfma_f32_16x16x32_bf16 v[32:35], a[44:47], v[84:87], v[32:35]\n\t"
        "buffer_load_dword v61, v61, s[20:23], s48 offen offset:512\n\t"
        "v_readlane_b32 s40, v155, 12\n\t"
        "v_readlane_b32 s43, v155, 0\n\t"
        "v_mfma_f32_16x16x32_bf16 v[36:39], a[48:51], v[84:87], v[36:39]\n\t"
        "ds_read_b128 v[92:95], v128 offset:8320\n\t"
        "s_add_u32 m0, s40, s35\n\t"
        "v_mfma_f32_16x16x32_bf16 v[40:43], a[52:55], v[84:87], v[40:43]\n\t"
        "v_mfma_f32_16x16x32_bf16 v[44:47], a[56:59], v[84:87], v[44:47]\n\t"
        "s_waitcnt lgkmcnt(1)\n\t"
        "v_mfma_f32_16x16x32_bf16 v[0:3], a[72:75], v[88:91], v[0:3]\n\t"
        "s_waitcnt vmcnt(10)\n\t"
        "s_barrier\n\t"
        "buffer_load_dwordx4 v134, s[16:19], s43 offen  lds\n\t"
        "ds_read_b128 v[96:99], v128 offset:16384\n\t"
        "v_mfma_f32_16x16x32_bf16 v[4:7], a[76:79], v[88:91], v[4:7]\n\t"
        "ds_read_b128 v[100:103], v128 offset:16448\n\t"
        "ds_read_b64_tr_b16 a[84:85], v131 offset:0\n\t"
        "v_readlane_b32 s40, v155, 13\n\t"
        "v_readlane_b32 s43, v155, 1\n\t"
        "v_mfma_f32_16x16x32_bf16 v[8:11], a[80:83], v[88:91], v[8:11]\n\t"
        "ds_read_b64_tr_b16 a[86:87], v131 offset:768\n\t"
        "s_add_u32 m0, s40, s35\n\t"
        "ds_read_b64_tr_b16 a[88:89], v132 offset:0\n\t"
        "s_waitcnt lgkmcnt(5)\n\t"
        "v_mfma_f32_16x16x32_bf16 v[12:15], a[72:75], v[92:95], v[12:15]\n\t"
        "ds_read_b64_tr_b16 a[90:91], v132 offset:768\n\t"
        "ds_read_b64_tr_b16 a[92:93], v133 offset:0\n\t"
        "v_readlane_b32 s46, v155, 37\n\t"
        "v_readlane_b32 s47, v155, 40\n\t"
        "v_mfma_f32_16x16x32_bf16 v[16:19], a[76:79], v[92:95], v[16:19]\n\t"
        "buffer_load_dwordx4 v135, s[16:19], s43 offen  lds\n\t"
        "ds_read_b64_tr_b16 a[94:95], v133 offset:768\n\t"
        "v_readlane_b32 s40, v155, 14\n\t"
        "v_readlane_b32 s43, v155, 2\n\t"
        "v_mfma_f32_16x16x32_bf16 v[20:23], a[80:83], v[92:95], v[20:23]\n\t"
        "ds_read_b128 v[104:107], v128 offset:4224\n\t"
        "s_add_u32 m0, s40, s35\n\t"
        "ds_read_b64_tr_b16 a[96:97], v131 offset:6144\n\t"
        "s_waitcnt lgkmcnt(9)\n\t"
        "v_mfma_f32_16x16x32_bf16 v[24:27], a[24:27], v[96:99], v[24:27]\n\t"
        "ds_read_b64_tr_b16 a[98:99], v131 offset:6912\n\t"
        "ds_read_b64_tr_b16 a[100:101], v132 offset:6144\n\t"
        "v_add_f32 v58, s46, v68\n\t"
        "v_add_f32 v59, s47, v69\n\t"
        "v_mfma_f32_16x16x32_bf16 v[28:31], a[28:31], v[96:99], v[28:31]\n\t"
        "ds_read_b64_tr_b16 a[102:103], v132 offset:6912\n\t"
        "ds_read_b64_tr_b16 a[104:105], v133 offset:6144\n\t"
        "v_readlane_b32 s47, v155, 42\n\t"
        "v_mfma_f32_16x16x32_bf16 v[32:35], a[32:35], v[96:99], v[32:35]\n\t"
        "buffer_load_dwordx4 v134, s[16:19], s43 offen  lds\n\t"
        "ds_read_b64_tr_b16 a[106:107], v133 offset:6912\n\t"
        "v_readlane_b32 s40, v155, 15\n\t"
        "v_readlane_b32 s43, v155, 3\n\t"
        "v_mfma_f32_16x16x32_bf16 v[36:39], a[0:3], v[96:99], v[36:39]\n\t"
        "ds_read_b128 v[108:111], v128 offset:8320\n\t"
        "s_add_u32 m0, s40, s35\n\t"
        "v_mfma_f32_16x16x32_bf16 v[40:43], a[4:7], v[96:99], v[40:43]\n\t"
        "ds_read_b32 v64, v70 offset:0\n\t"
        "v_mfma_f32_16x16x32_bf16 v[44:47], a[8:11], v[96:99], v[44:47]\n\t"
        "ds_read_b128 v[112:115], v128 offset:12416\n\t"
        "s_waitcnt lgkmcnt(10)\n\t"
        "v_mfma_f32_16x16x32_bf16 v[24:27], a[60:63], v[100:103], v[24:27]\n\t"
        "ds_read_b32 v67, v67 offset:0\n\t"
        "v_mfma_f32_16x16x32_bf16 v[28:31], a[64:67], v[100:103], v[28:31]\n\t"
        "ds_read_b32 v66, v70 offset:2560\n\t"
        "v_mfma_f32_16x16x32_bf16 v[32:35], a[68:71], v[100:103], v[32:35]\n\t"
        "buffer_load_dwordx4 v135, s[16:19], s43 offen  lds\n\t"
        "s_add_u32 m0, s37, 36864\n\t"
        "s_add_u32 s36, s36, s38\n\t"
        "v_mfma_f32_16x16x32_bf16 v[36:39], a[36:39], v[100:103], v[36:39]\n\t"
        "ds_read_b128 v[84:87], v128 offset:16512\n\t"
        "v_mfma_f32_16x16x32_bf16 v[40:43], a[40:43], v[100:103], v[40:43]\n\t"
        "v_mfma_f32_16x16x32_bf16 v[44:47], a[44:47], v[100:103], v[44:47]\n\t"
        "s_waitcnt lgkmcnt(7)\n\t"
        "v_mfma_f32_16x16x32_bf16 v[12:15], a[84:87], v[104:107], v[12:15]\n\t"
        "buffer_load_dwordx4 v140, s[24:27], s36 offen offset:0 lds\n\t"
        "v_mfma_f32_16x16x32_bf16 v[16:19], a[88:91], v[104:107], v[16:19]\n\t"
        "s_waitcnt lgkmcnt(4)\n\t"
        "v_perm_b32 v56, v64, v64, s44\n\t"
        "v_perm_b32 v57, v64, v64, s45\n\t"
        "v_add_f32 v56, v56, v58\n\t"
        "v_mfma_f32_16x16x32_bf16 v[20:23], a[92:95], v[104:107], v[20:23]\n\t"
        "v_add_f32 v57, v57, v59\n\t"
        "v_cvt_pk_bf16_f32 v52, v56, v57\n\t"
        "v_cvt_pk_bf16_f32 v58, v58, v59\n\t"
        "v_mfma_f32_16x16x32_bf16 v[0:3], a[96:99], v[108:111], v[0:3]\n\t"
        "v_cmp_ne_u32 s[64:65], v52, v58\n\t"
        "s_waitcnt lgkmcnt(2)\n\t"
        "v_perm_b32 v57, v67, v67, s45\n\t"
        "v_add_f32 v57, v57, v79\n\t"
        "v_mfma_f32_16x16x32_bf16 v[4:7], a[100:103], v[108:111], v[4:7]\n\t"
        "buffer_load_dwordx4 v141, s[24:27], s36 offen offset:1024 lds\n\t"
        "v_mbcnt_lo_u32_b32 v56, s64, 1\n\t"
        "v_mfma_f32_16x16x32_bf16 v[8:11], a[104:107], v[108:111], v[8:11]\n\t"
        "v_mbcnt_hi_u32_b32 v56, s65, v56\n\t"
        "s_bcnt1_i32_b64 s61, s[64:65]\n\t"
        "v_mfma_f32_16x16x32_bf16 v[24:27], a[84:87], v[108:111], v[24:27]\n\t"
        "v_add_u32 v54, s61, 1\n\t"
        "v_lshl_add_u32 v56, v56, 3, s60\n\t"
        "v_cvt_pk_bf16_f32 v58, v78, v79\n\t"
        "v_mfma_f32_16x16x32_bf16 v[28:31], a[88:91], v[108:111], v[28:31]\n\t"
        "s_mov_b64 exec, s[64:65]\n\t"
        "ds_write_b64 v56, v[52:53]\n\t"
        "s_mov_b64 exec, -1\n\t"
        "v_mfma_f32_16x16x32_bf16 v[32:35], a[92:95], v[108:111], v[32:35]\n\t"
        "s_waitcnt vmcnt(8) lgkmcnt(1)\n\t"
        "s_barrier\n\t"
        "buffer_load_dwordx4 v142, s[24:27], s36 offen offset:2048 lds\n\t"
        "s_add_u32 m0, s37, 49152\n\t"
        "ds_read_b128 v[88:91], v128 offset:0\n\t"
        "v_mfma_f32_16x16x32_bf16 v[12:15], a[96:99], v[112:115], v[12:15]\n\t"
        "ds_read_b128 v[92:95], v128 offset:64\n\t"
        "s_add_u32 s36, s36, s5\n\t"
        "ds_read_b128 v[96:99], v128 offset:128\n\t"
        "v_mfma_f32_16x16x32_bf16 v[16:19], a[100:103], v[112:115], v[16:19]\n\t"
        "ds_read_b64_tr_b16 a[108:109], v131 offset:12288\n\t"
        "ds_read_b64_tr_b16 a[110:111], v131 offset:13056\n\t"
        "v_swap_b32 v53, v80\n\t"
        "v_perm_b32 v56, v67, v67, s44\n\t"
        "v_mfma_f32_16x16x32_bf16 v[20:23], a[104:107], v[112:115], v[20:23]\n\t"
        "ds_read_b64_tr_b16 a[112:113], v132 offset:12288\n\t"
        "ds_read_b64_tr_b16 a[114:115], v132 offset:13056\n\t"
        "v_add_f32 v56, v56, v78\n\t"
        "v_cvt_pk_bf16_f32 v52, v56, v57\n\t"
        "v_mfma_f32_16x16x32_bf16 v[24:27], a[72:75], v[112:115], v[24:27]\n\t"
        "buffer_load_dwordx4 v140, s[24:27], s36 offen offset:0 lds\n\t"
        "ds_read_b128 v[100:103], v128 offset:20480\n\t"
        "v_cmp_ne_u32 s[64:65], v52, v58\n\t"
        "v_mfma_f32_16x16x32_bf16 v[28:31], a[76:79], v[112:115], v[28:31]\n\t"
        "ds_read_b64_tr_b16 a[116:117], v133 offset:12288\n\t"
        "ds_read_b64_tr_b16 a[118:119], v133 offset:13056\n\t"
        "s_and_b64 s[64:65], s[64:65], 0xffffff\n\t"
        "v_mbcnt_lo_u32_b32 v56, s64, v54\n\t"
        "v_mfma_f32_16x16x32_bf16 v[32:35], a[80:83], v[112:115], v[32:35]\n\t"
        "ds_read_b64_tr_b16 a[120:121], v131 offset:18432\n\t"
        "ds_read_b64_tr_b16 a[122:123], v131 offset:19200\n\t"
        "s_bcnt1_i32_b64 s46, s[64:65]\n\t"
        "v_mfma_f32_16x16x32_bf16 v[36:39], a[84:87], v[112:115], v[36:39]\n\t"
        "ds_read_b128 v[104:107], v128 offset:20544\n\t"
        "ds_read_b64_tr_b16 a[124:125], v132 offset:18432\n\t"
        "s_add_u32 s61, s61, s46\n\t"
        "v_add_u32 v54, s46, v54\n\t"
        "v_mfma_f32_16x16x32_bf16 v[40:43], a[88:91], v[112:115], v[40:43]\n\t"
        "buffer_load_dwordx4 v141, s[24:27], s36 offen offset:1024 lds\n\t"
        "ds_read_b64_tr_b16 a[126:127], v132 offset:19200\n\t"
        "s_mov_b32 s3, s61\n\t"
        "v_mfma_f32_16x16x32_bf16 v[44:47], a[92:95], v[112:115], v[44:47]\n\t"
        "ds_read_b64_tr_b16 a[128:129], v133 offset:18432\n\t"
        "ds_read_b64_tr_b16 a[130:131], v133 offset:19200\n\t"
        "v_lshl_add_u32 v56, v56, 3, s60\n\t"
        "v_readlane_b32 s46, v155, 39\n\t"
        "v_mfma_f32_16x16x32_bf16 v[24:27], a[96:99], v[84:87], v[24:27]\n\t"
        "s_mov_b64 exec, s[64:65]\n\t"
        "ds_write_b64 v56, v[52:53]\n\t"
        "s_mov_b64 exec, -1\n\t"
        "ds_read_b128 v[108:111], v129 offset:0\n\t"
        "v_mfma_f32_16x16x32_bf16 v[28:31], a[100:103], v[84:87], v[28:31]\n\t"
        "ds_read_b64_tr_b16 a[132:133], v131 offset:24576\n\t"
        "ds_read_b64_tr_b16 a[134:135], v131 offset:25344\n\t"
        "v_add_u32 v53, 4096, v80\n\t"
        "v_perm_b32 v56, v66, v66, s44\n\t"
        "v_mfma_f32_16x16x32_bf16 v[32:35], a[104:107], v[84:87], v[32:35]\n\t"
        "buffer_load_dwordx4 v142, s[24:27], s36 offen offset:2048 lds\n\t"
        "ds_read_b64_tr_b16 a[136:137], v132 offset:24576\n\t"
        "v_readlane_b32 s40, v155, 16\n\t"
        "v_mfma_f32_16x16x32_bf16 v[36:39], a[72:75], v[84:87], v[36:39]\n\t"
        "ds_read_b128 v[112:115], v129 offset:4096\n\t"
        "ds_read_b64_tr_b16 a[138:139], v132 offset:25344\n\t"
        "v_readlane_b32 s43, v155, 4\n\t"
        "s_add_u32 m0, s40, s35\n\t"
        "v_mfma_f32_16x16x32_bf16 v[40:43], a[76:79], v[84:87], v[40:43]\n\t"
        "ds_read_b64_tr_b16 a[140:141], v133 offset:24576\n\t"
        "ds_read_b64_tr_b16 a[142:143], v133 offset:25344\n\t"
        "v_perm_b32 v57, v66, v66, s45\n\t"
        "v_mfma_f32_16x16x32_bf16 v[44:47], a[80:83], v[84:87], v[44:47]\n\t"
        "ds_read_b64_tr_b16 a[144:145], v131 offset:30720\n\t"
        "ds_read_b64_tr_b16 a[146:147], v131 offset:31488\n\t"
        "v_add_f32 v58, s46, v68\n\t"
        "s_waitcnt lgkmcnt(11)\n\t"
        "v_mfma_f32_16x16x32_bf16 v[0:3], a[12:15], v[88:91], v[0:3]\n\t"
        "buffer_load_dwordx4 v134, s[16:19], s43 offen  lds\n\t"
        "ds_read_b64_tr_b16 a[148:149], v132 offset:30720\n\t"
        "v_readlane_b32 s40, v155, 17\n\t"
        "v_readlane_b32 s43, v155, 5\n\t"
        "v_mfma_f32_16x16x32_bf16 v[4:7], a[16:19], v[88:91], v[4:7]\n\t"
        "ds_read_b64_tr_b16 a[150:151], v132 offset:31488\n\t"
        "s_add_u32 m0, s40, s35\n\t"
        "ds_read_b64_tr_b16 a[152:153], v133 offset:30720\n\t"
        "v_mfma_f32_16x16x32_bf16 v[8:11], a[20:23], v[88:91], v[8:11]\n\t"
        "ds_read_b64_tr_b16 a[154:155], v133 offset:31488\n\t"
        "v_add_f32 v59, s47, v69\n\t"
        "v_add_f32 v56, v56, v58\n\t"
        "v_mfma_f32_16x16x32_bf16 v[0:3], a[48:51], v[92:95], v[0:3]\n\t"
        "ds_read_b128 v[84:87], v129 offset:8192\n\t"
        "v_add_f32 v57, v57, v59\n\t"
        "v_mfma_f32_16x16x32_bf16 v[4:7], a[52:55], v[92:95], v[4:7]\n\t"
        "buffer_load_dwordx4 v135, s[16:19], s43 offen  lds\n\t"
        "v_readlane_b32 s40, v155, 18\n\t"
        "v_readlane_b32 s43, v155, 6\n\t"
        "v_mfma_f32_16x16x32_bf16 v[8:11], a[56:59], v[92:95], v[8:11]\n\t"
        "s_add_u32 m0, s40, s35\n\t"
        "v_max_f32 v56, 0, v56\n\t"
        "v_mfma_f32_16x16x32_bf16 v[0:3], a[84:87], v[96:99], v[0:3]\n\t"
        "v_max_f32 v57, 0, v57\n\t"
        "v_max_f32 v58, 0, v58\n\t"
        "v_mfma_f32_16x16x32_bf16 v[4:7], a[88:91], v[96:99], v[4:7]\n\t"
        "v_max_f32 v59, 0, v59\n\t"
        "v_cvt_pk_bf16_f32 v52, v56, v57\n\t"
        "v_cvt_pk_bf16_f32 v58, v58, v59\n\t"
        "v_mfma_f32_16x16x32_bf16 v[8:11], a[92:95], v[96:99], v[8:11]\n\t"
        "buffer_load_dwordx4 v136, s[16:19], s43 offen  lds\n\t"
        "v_readlane_b32 s40, v155, 19\n\t"
        "v_mfma_f32_16x16x32_bf16 v[36:39], a[24:27], v[100:103], v[36:39]\n\t"
        ".LpY0%=:\n\t"
        "s_cbranch_execz .LrY%=\n\t"
        "v_readlane_b32 s43, v155, 7\n\t"
        "s_add_u32 m0, s40, s35\n\t"
        "v_cmp_ne_u32 s[64:65], v52, v58\n\t"
        "v_mfma_f32_16x16x32_bf16 v[40:43], a[28:31], v[100:103], v[40:43]\n\t"
        "s_nop 0\n\t"
        "v_mbcnt_lo_u32_b32 v56, s64, v54\n\t"
        "v_mbcnt_hi_u32_b32 v56, s65, v56\n\t"
        "v_mfma_f32_16x16x32_bf16 v[44:47], a[32:35], v[100:103], v[44:47]\n\t"
        "s_bcnt1_i32_b64 s46, s[64:65]\n\t"
        "s_add_u32 s61, s61, s46\n\t"
        "v_mfma_f32_16x16x32_bf16 v[36:39], a[60:63], v[104:107], v[36:39]\n\t"
        "buffer_load_dwordx4 v137, s[16:19], s43 offen  lds\n\t"
        "s_add_u32 m0, s37, 0\n\t"
        "ds_read_b128 v[88:91], v129 offset:12288\n\t"
        "v_mfma_f32_16x16x32_bf16 v[40:43], a[64:67], v[104:107], v[40:43]\n\t"
        "s_add_u32 s36, s36, s39\n\t"
        "s_waitcnt vmcnt(15)\n\t"
        "v_and_b32 v48, s54, v48\n\t"
        "v_pk_max_u16 v60, v154, v48\n\t"
        "v_mfma_f32_16x16x32_bf16 v[44:47], a[68:71], v[104:107], v[44:47]\n\t"
        "v_and_b32 v49, s54, v49\n\t"
        "v_pk_max_u16 v60, v60, v49\n\t"
        "v_and_b32 v50, s54, v50\n\t"
        "s_waitcnt lgkmcnt(1)\n\t"
        "v_mfma_f32_16x16x32_bf16 v[0:3], a[120:123], v[108:111], v[0:3]\n\t"
        "v_pk_max_u16 v60, v60, v50\n\t"
        "v_and_b32 v51, s54, v51\n\t"
        "v_mfma_f32_16x16x32_bf16 v[4:7], a[124:127], v[108:111], v[4:7]\n\t"
        "buffer_load_dwordx4 v140, s[24:27], s36 offen offset:0 lds\n\t"
        "v_pk_max_u16 v60, v60, v51\n\t"
        "v_mfma_f32_16x16x32_bf16 v[8:11], a[128:131], v[108:111], v[8:11]\n\t"
        "s_waitcnt vmcnt(15)\n\t"
        "v_and_b32 v61, s54, v61\n\t"
        "v_pk_max_u16 v60, v60, v61\n\t"
        "v_cmp_ne_u32 s[12:13], v60, v154\n\t"
        "v_mfma_f32_16x16x32_bf16 v[0:3], a[108:111], v[112:115], v[0:3]\n\t"
        "s_lshl_b32 s6, 256, s4\n\t"
        "v_min_u32 v56, 97, v56\n\t"
        "v_mfma_f32_16x16x32_bf16 v[4:7], a[112:115], v[112:115], v[4:7]\n\t"
        "s_cmp_lg_u64 s[12:13], 0\n\t"
        "s_cselect_b32 s6, s6, 0\n\t"
        "v_mfma_f32_16x16x32_bf16 v[8:11], a[116:119], v[112:115], v[8:11]\n\t"
        "buffer_load_dwordx4 v141, s[24:27], s36 offen offset:1024 lds\n\t"
        "v_lshl_add_u32 v56, v56, 3, s60\n\t"
        "v_mfma_f32_16x16x32_bf16 v[12:15], a[120:123], v[112:115], v[12:15]\n\t"
        "s_mov_b64 exec, s[64:65]\n\t"
        "ds_write_b64 v56, v[52:53]\n\t"
        "s_mov_b64 exec, -1\n\t"
        "v_mfma_f32_16x16x32_bf16 v[16:19], a[124:127], v[112:115], v[16:19]\n\t"
        "s_cmp_gt_u32 s61, s53\n\t"
        "s_cselect_b32 s8, 1, 0\n\t"
        "s_or_b32 s56, s56, s8\n\t"
        "v_mfma_f32_16x16x32_bf16 v[20:23], a[128:131], v[112:115], v[20:23]\n\t"
        "s_cmp_gt_u32 s3, 64\n\t"
        "s_cselect_b32 s8, 1, 0\n\t"
        "s_or_b32 s56, s56, s8\n\t"
        "v_mfma_f32_16x16x32_bf16 v[0:3], a[132:135], v[84:87], v[0:3]\n\t"
        "s_waitcnt vmcnt(9)\n\t"
        "s_barrier\n\t"
        "buffer_load_dwordx4 v142, s[24:27], s36 offen offset:2048 lds\n\t"
        "s_add_u32 m0, s37, 12288\n\t"
        "ds_read_b128 v[92:95], v129 offset:16384\n\t"
        "v_mfma_f32_16x16x32_bf16 v[4:7], a[136:139], v[84:87], v[4:7]\n\t"
        "ds_read_b64_tr_b16 a[156:157], v131 offset:36864\n\t"
        "s_add_u32 s36, s36, s38\n\t"
        "ds_read_b64_tr_b16 a[158:159], v131 offset:37632\n\t"
        "v_mfma_f32_16x16x32_bf16 v[8:11], a[140:143], v[84:87], v[8:11]\n\t"
        "ds_read_b64_tr_b16 a[160:161], v132 offset:36864\n\t"
        "ds_read_b64_tr_b16 a[162:163], v132 offset:37632\n\t"
        "s_min_u32 s61, s61, 80\n\t"
        "v_min_u32 v78, s61, v116\n\t"
        "v_mfma_f32_16x16x32_bf16 v[12:15], a[108:111], v[84:87], v[12:15]\n\t"
        "ds_read_b128 v[96:99], v128 offset:20608\n\t"
        "ds_read_b64_tr_b16 a[164:165], v133 offset:36864\n\t"
        "v_lshl_add_u32 v78, v78, 3, s60\n\t"
        "v_mfma_f32_16x16x32_bf16 v[16:19], a[112:115], v[84:87], v[16:19]\n\t"
        "ds_read_b64 v[62:63], v78\n\t"
        "buffer_load_dwordx4 v140, s[24:27], s36 offen offset:0 lds\n\t"
        "v_cmp_ge_u32 s[66:67], s61, v116\n\t"
        "v_mfma_f32_16x16x32_bf16 v[20:23], a[116:119], v[84:87], v[20:23]\n\t"
        "ds_read_b64_tr_b16 a[166:167], v133 offset:37632\n\t"
        "ds_read_b64_tr_b16 a[168:169], v131 offset:43008\n\t"
        "s_mov_b32 s46, 0x07060302\n\t"
        "v_mfma_f32_16x16x32_bf16 v[24:27], a[120:123], v[84:87], v[24:27]\n\t"
        "ds_read_b128 v[100:103], v129 offset:64\n\t"
        "ds_read_b64_tr_b16 a[170:171], v131 offset:43776\n\t"
        "v_mfma_f32_16x16x32_bf16 v[28:31], a[124:127], v[84:87], v[28:31]\n\t"
        "ds_read_b64_tr_b16 a[172:173], v132 offset:43008\n\t"
        "ds_read_b64_tr_b16 a[174:175], v132 offset:43776\n\t"
        "v_mfma_f32_16x16x32_bf16 v[32:35], a[128:131], v[84:87], v[32:35]\n\t"
        "s_waitcnt vmcnt(8)\n\t"
        "s_barrier\n\t"
        "buffer_load_dwordx4 v141, s[24:27], s36 offen offset:1024 lds\n\t"
        "ds_read_b64_tr_b16 a[176:177], v133 offset:43008\n\t"
        "s_waitcnt lgkmcnt(11)\n\t"
        "v_mfma_f32_16x16x32_bf16 v[12:15], a[132:135], v[88:91], v[12:15]\n\t"
        "ds_read_b128 v[104:107], v129 offset:4160\n\t"
        "ds_read_b64_tr_b16 a[178:179], v133 offset:43776\n\t"
        "v_mfma_f32_16x16x32_bf16 v[16:19], a[136:139], v[88:91], v[16:19]\n\t"
        "ds_read_b64_tr_b16 a[180:181], v131 offset:49152\n\t"
        "ds_read_b64_tr_b16 a[182:183], v131 offset:49920\n\t"
        "v_mfma_f32_16x16x32_bf16 v[20:23], a[140:143], v[88:91], v[20:23]\n\t"
        "ds_read_b64_tr_b16 a[184:185], v132 offset:49152\n\t"
        "ds_read_b64_tr_b16 a[186:187], v132 offset:49920\n\t"
        "v_mfma_f32_16x16x32_bf16 v[24:27], a[108:111], v[88:91], v[24:27]\n\t"
        "buffer_load_dwordx4 v142, s[24:27], s36 offen offset:2048 lds\n\t"
        "s_add_u32 m0, s37, 24576\n\t"
        "ds_read_b64_tr_b16 a[188:189], v133 offset:49152\n\t"
        "v_mfma_f32_16x16x32_bf16 v[28:31], a[112:115], v[88:91], v[28:31]\n\t"
        "s_mov_b64 exec, s[66:67]\n\t"
        "s_waitcnt lgkmcnt(12)\n\t"
        "ds_write_b32 v63, v117\n\t"
        "s_mov_b64 exec, -1\n\t"
        "s_add_u32 s36, s36, s5\n\t"
        "ds_read_b64_tr_b16 a[190:191], v133 offset:49920\n\t"
        "v_mfma_f32_16x16x32_bf16 v[32:35], a[116:119], v[88:91], v[32:35]\n\t"
        "ds_read_b64_tr_b16 a[192:193], v131 offset:55296\n\t"
        "ds_read_b64_tr_b16 a[194:195], v131 offset:56064\n\t"
        "v_and_b32 v78, 0xfffff800, v63\n\t"
        "v_lshlrev_b32 v72, 16, v62\n\t"
        "v_mfma_f32_16x16x32_bf16 v[36:39], a[120:123], v[88:91], v[36:39]\n\t"
        "ds_read_b128 v[64:67], v78 offset:1536\n\t"
        "ds_read_b128 v[108:111], v129 offset:8256\n\t"
        "v_and_b32 v73, 0xffff0000, v62\n\t"
        "v_med3_f32 v72, v72, 0, s63\n\t"
        "v_mfma_f32_16x16x32_bf16 v[40:43], a[124:127], v[88:91], v[40:43]\n\t"
        "buffer_load_dwordx4 v140, s[24:27], s36 offen offset:0 lds\n\t"
        "ds_read_b64_tr_b16 a[196:197], v132 offset:55296\n\t"
        "v_med3_f32 v73, v73, 0, s63\n\t"
        "v_mfma_f32_16x16x32_bf16 v[44:47], a[128:131], v[88:91], v[44:47]\n\t"
        "ds_read_b64_tr_b16 a[198:199], v132 offset:56064\n\t"
        "ds_read_b64_tr_b16 a[200:201], v133 offset:55296\n\t"
        "v_perm_b32 v62, v73, v72, s46\n\t"
        "v_mfma_f32_16x16x32_bf16 v[24:27], a[132:135], v[92:95], v[24:27]\n\t"
        "ds_read_b64_tr_b16 a[202:203], v133 offset:56064\n\t"
        "v_fract_f32 v74, v72\n\t"
        "v_fract_f32 v75, v73\n\t"
        "v_mfma_f32_16x16x32_bf16 v[28:31], a[136:139], v[92:95], v[28:31]\n\t"
        "v_cvt_u32_f32 v76, v72\n\t"
        "v_cvt_u32_f32 v77, v73\n\t"
        "v_mfma_f32_16x16x32_bf16 v[32:35], a[140:143], v[92:95], v[32:35]\n\t"
        "v_cmp_neq_f32 s[64:65], 0, v74\n\t"
        "v_cmp_neq_f32 s[44:45], 0, v75\n\t"
        "v_mfma_f32_16x16x32_bf16 v[36:39], a[108:111], v[92:95], v[36:39]\n\t"
        "v_sub_f32 v78, 1.0, v74\n\t"
        "v_sub_f32 v79, 1.0, v75\n\t"
        "v_cvt_pk_bf16_f32 v79, v78, v79\n\t"
        "v_mfma_f32_16x16x32_bf16 v[40:43], a[112:115], v[92:95], v[40:43]\n\t"
        "v_lshlrev_b32 v78, 16, v79\n\t"
        "v_and_b32 v79, 0xffff0000, v79\n\t"
        "v_mfma_f32_16x16x32_bf16 v[44:47], a[116:119], v[92:95], v[44:47]\n\t"
        "buffer_load_dwordx4 v141, s[24:27], s36 offen offset:1024 lds\n\t"
        "v_mul_f32 v80, v78, v79\n\t"
        "v_mfma_f32_16x16x32_bf16 v[36:39], a[96:99], v[96:99], v[36:39]\n\t"
        "ds_read_b128 v[112:115], v129 offset:128\n\t"
        "v_mul_f32 v78, v78, v75\n\t"
        "v_mul_f32 v79, v74, v79\n\t"
        "v_mfma_f32_16x16x32_bf16 v[40:43], a[100:103], v[96:99], v[40:43]\n\t"
        "v_mul_f32 v81, v74, v75\n\t"
        "v_cndmask_b32 v78, v79, v78, s[44:45]\n\t"
        "v_mfma_f32_16x16x32_bf16 v[44:47], a[104:107], v[96:99], v[44:47]\n\t"
        "v_cndmask_b32 v79, 0, v79, s[44:45]\n\t"
        "v_cvt_pk_bf16_f32 v70, v80, v78\n\t"
        "s_waitcnt lgkmcnt(2)\n\t"
        "v_mfma_f32_16x16x32_bf16 v[0:3], a[156:159], v[100:103], v[0:3]\n\t"
        "buffer_load_dwordx4 v142, s[24:27], s36 offen offset:2048 lds\n\t"
        "s_add_u32 m0, s37, 36864\n\t"
        "ds_read_b128 v[84:87], v129 offset:4224\n\t"
        "v_mfma_f32_16x16x32_bf16 v[4:7], a[160:163], v[100:103], v[4:7]\n\t"
        "s_add_u32 s36, s36, s39\n\t"
        "v_cvt_pk_bf16_f32 v71, v79, v81\n\t"
        "v_pk_max_u16 v78, v62, v64\n\t"
        "v_mfma_f32_16x16x32_bf16 v[8:11], a[164:167], v[100:103], v[8:11]\n\t"
        "v_pk_min_u16 v78, v78, v65\n\t"
        "v_cmp_ne_u32 vcc, v78, v62\n\t"
        "v_mfma_f32_16x16x32_bf16 v[0:3], a[144:147], v[104:107], v[0:3]\n\t"
        "v_sub_u32 v78, v77, v67\n\t"
        "v_mad_u32_u24 v79, v77, 6, v66\n\t"
        "v_mfma_f32_16x16x32_bf16 v[4:7], a[148:151], v[104:107], v[4:7]\n\t"
        "v_mad_u32_u24 v79, v76, s62, v79\n\t"
        "s_and_b64 vcc, vcc, s[66:67]\n\t"
        "v_mfma_f32_16x16x32_bf16 v[8:11], a[152:155], v[104:107], v[8:11]\n\t"
        "s_waitcnt vmcnt(6) lgkmcnt(2)\n\t"
        "s_barrier\n\t"
        "buffer_load_dwordx4 v140, s[24:27], s36 offen offset:0 lds\n\t"
        "ds_read_b64_tr_b16 a[204:205], v131 offset:0\n\t"
        "v_mfma_f32_16x16x32_bf16 v[12:15], a[156:159], v[104:107], v[12:15]\n\t"
        "ds_read_b128 v[88:91], v129 offset:12352\n\t"
        "ds_read_b64_tr_b16 a[206:207], v131 offset:768\n\t"
        "s_or_b64 s[56:57], s[56:57], vcc\n\t"
        "v_bfe_u32 v80, v78, 2, 1\n\t"
        "v_mfma_f32_16x16x32_bf16 v[16:19], a[160:163], v[104:107], v[16:19]\n\t"
        "ds_read_b64_tr_b16 a[208:209], v132 offset:0\n\t"
        "ds_read_b64_tr_b16 a[210:211], v132 offset:768\n\t"
        "v_or_b32 v80, v80, v79\n\t"
        "v_mfma_f32_16x16x32_bf16 v[20:23], a[164:167], v[104:107], v[20:23]\n\t"
        "ds_read_b64_tr_b16 a[212:213], v133 offset:0\n\t"
        "ds_read_b64_tr_b16 a[214:215], v133 offset:768\n\t"
        "v_add_u32 v78, 1, v78\n\t"
        "v_mfma_f32_16x16x32_bf16 v[0:3], a[168:171], v[108:111], v[0:3]\n\t"
        "buffer_load_dwordx4 v141, s[24:27], s36 offen offset:1024 lds\n\t"
        "ds_read_b64_tr_b16 a[216:217], v131 offset:6144\n\t"
        "v_bfe_u32 v78, v78, 2, 1\n\t"
        "v_mfma_f32_16x16x32_bf16 v[4:7], a[172:175], v[108:111], v[4:7]\n\t"
        "ds_read_b64_tr_b16 a[218:219], v131 offset:6912\n\t"
        "ds_read_b64_tr_b16 a[220:221], v132 offset:6144\n\t"
        "v_add3_u32 v78, v79, v78, 6\n\t"
        "v_cndmask_b32 v78, v80, v78, s[44:45]\n\t"
        "v_mfma_f32_16x16x32_bf16 v[8:11], a[176:179], v[108:111], v[8:11]\n\t"
        "ds_read_b64_tr_b16 a[222:223], v132 offset:6912\n\t"
        "ds_read_b64_tr_b16 a[224:225], v133 offset:6144\n\t"
        "v_cndmask_b32 v79, 0, v119, s[64:65]\n\t"
        "v_mfma_f32_16x16x32_bf16 v[12:15], a[144:147], v[108:111], v[12:15]\n\t"
        "ds_read_b64_tr_b16 a[226:227], v133 offset:6912\n\t"
        "v_add_u32 v81, v80, v79\n\t"
        "v_mfma_f32_16x16x32_bf16 v[16:19], a[148:151], v[108:111], v[16:19]\n\t"
        "buffer_load_dwordx4 v142, s[24:27], s36 offen offset:2048 lds\n\t"
        "s_add_u32 m0, s37, 49152\n\t"
        "v_mfma_f32_16x16x32_bf16 v[20:23], a[152:155], v[108:111], v[20:23]\n\t"
        "s_waitcnt vmcnt(6)\n\t"
        "s_barrier\n\t"
        "ds_read_b64_tr_b16 a[228:229], v131 offset:12288\n\t"
        "s_add_u32 s36, s36, s38\n\t"
        "ds_read_b64_tr_b16 a[230:231], v131 offset:13056\n\t"
        "v_mfma_f32_16x16x32_bf16 v[24:27], a[156:159], v[108:111], v[24:27]\n\t"
        "ds_read_b128 v[92:95], v129 offset:8320\n\t"
        "ds_read_b64_tr_b16 a[232:233], v132 offset:12288\n\t"
        "v_add_u32 v82, v78, v79\n\t"
        "v_cndmask_b32 v78, v81, v78, s[44:45]\n\t"
        "v_mfma_f32_16x16x32_bf16 v[28:31], a[160:163], v[108:111], v[28:31]\n\t"
        "ds_read_b64_tr_b16 a[234:235], v132 offset:13056\n\t"
        "ds_read_b64_tr_b16 a[236:237], v133 offset:12288\n\t"
        "v_lshl_or_b32 v68, v78, 16, v80\n\t"
        "v_mfma_f32_16x16x32_bf16 v[32:35], a[164:167], v[108:111], v[32:35]\n\t"
        "buffer_load_dwordx4 v140, s[24:27], s36 offen offset:0 lds\n\t"
        "ds_read_b64_tr_b16 a[238:239], v133 offset:13056\n\t"
        "v_lshl_or_b32 v69, v82, 16, v81\n\t"
        "s_waitcnt lgkmcnt(10)\n\t"
        "v_mfma_f32_16x16x32_bf16 v[0:3], a[192:195], v[112:115], v[0:3]\n\t"
        "ds_write_b128 v118, v[68:71]\n\t"
        "v_mfma_f32_16x16x32_bf16 v[4:7], a[196:199], v[112:115], v[4:7]\n\t"
        "ds_read_b128 v[160:163], v185 offset:0\n\t"
        "v_xor_b32 v154, 32, v186\n\t"
        "v_mfma_f32_16x16x32_bf16 v[8:11], a[200:203], v[112:115], v[8:11]\n\t"
        "ds_read_b64_tr_b16 a[240:241], v131 offset:18432\n\t"
        "v_mfma_f32_16x16x32_bf16 v[0:3], a[180:183], v[84:87], v[0:3]\n\t"
        "buffer_load_dwordx4 v141, s[24:27], s36 offen offset:1024 lds\n\t"
        "v_mfma_f32_16x16x32_bf16 v[4:7], a[184:187], v[84:87], v[4:7]\n\t"
        "ds_read_b64_tr_b16 a[242:243], v131 offset:19200\n\t"
        "v_mfma_f32_16x16x32_bf16 v[8:11], a[188:191], v[84:87], v[8:11]\n\t"
        "ds_read_b64_tr_b16 a[244:245], v132 offset:18432\n\t"
        "v_mfma_f32_16x16x32_bf16 v[12:15], a[192:195], v[84:87], v[12:15]\n\t"
        "ds_read_b128 v[96:99], v130 offset:0\n\t"
        "ds_read_b64_tr_b16 a[246:247], v132 offset:19200\n\t"
        "v_mfma_f32_16x16x32_bf16 v[16:19], a[196:199], v[84:87], v[16:19]\n\t"
        "buffer_load_dwordx4 v142, s[24:27], s36 offen offset:2048 lds\n\t"
        "ds_read_b64_tr_b16 a[248:249], v133 offset:18432\n\t"
        "v_mfma_f32_16x16x32_bf16 v[20:23], a[200:203], v[84:87], v[20:23]\n\t"
        "ds_read_b64_tr_b16 a[250:251], v133 offset:19200\n\t"
        "v_mfma_f32_16x16x32_bf16 v[12:15], a[168:171], v[88:91], v[12:15]\n\t"
        "ds_read_b128 v[100:103], v130 offset:4096\n\t"
        "v_mfma_f32_16x16x32_bf16 v[16:19], a[172:175], v[88:91], v[16:19]\n\t"
        "s_waitcnt lgkmcnt(8)\n\t"
        "v_lshlrev_b32_sdwa v157, 5, v160 dst_sel:DWORD dst_unused:UNUSED_PAD src0_sel:DWORD src1_sel:WORD_0\n\t"
        "v_lshlrev_b32_sdwa v158, 5, v160 dst_sel:DWORD dst_unused:UNUSED_PAD src0_sel:DWORD src1_sel:WORD_1\n\t"
        "v_or_b32 v157, v184, v157\n\t"
        "v_mfma_f32_16x16x32_bf16 v[20:23], a[176:179], v[88:91], v[20:23]\n\t"
        "ds_read_b64_tr_b16 v[60:61], v157 offset:0\n\t"
        "v_or_b32 v158, v184, v158\n\t"
        "v_xor_b32 v159, 32, v157\n\t"
        "v_mfma_f32_16x16x32_bf16 v[24:27], a[144:147], v[88:91], v[24:27]\n\t"
        "ds_read_b64_tr_b16 v[62:63], v158 offset:0\n\t"
        "v_xor_b32 v152, 32, v158\n\t"
        "v_mfma_f32_16x16x32_bf16 v[28:31], a[148:151], v[88:91], v[28:31]\n\t"
        "ds_read_b64_tr_b16 v[64:65], v159 offset:0\n\t"
        "v_perm_b32 v164, v162, v162, v168\n\t"
        "v_mfma_f32_16x16x32_bf16 v[32:35], a[152:155], v[88:91], v[32:35]\n\t"
        "ds_read_b64_tr_b16 v[66:67], v152 offset:0\n\t"
        "v_perm_b32 v165, v162, v162, v169\n\t"
        "v_mfma_f32_16x16x32_bf16 v[36:39], a[156:159], v[88:91], v[36:39]\n\t"
        "ds_read_b64_tr_b16 v[68:69], v157 offset:64\n\t"
        "v_perm_b32 v166, v162, v162, v170\n\t"
        "v_perm_b32 v167, v162, v162, v171\n\t"
        "v_mfma_f32_16x16x32_bf16 v[40:43], a[160:163], v[88:91], v[40:43]\n\t"
        "ds_read_b64_tr_b16 v[70:71], v158 offset:64\n\t"
        "v_mfma_f32_16x16x32_bf16 v[44:47], a[164:167], v[88:91], v[44:47]\n\t"
        "s_waitcnt vmcnt(3) lgkmcnt(7)\n\t"
        "s_barrier\n\t"
        "v_mfma_f32_16x16x32_bf16 v[0:3], a[204:207], v[92:95], v[0:3]\n\t"
        "ds_read_b64_tr_b16 v[72:73], v159 offset:64\n\t"
        "ds_read_b128 v[104:107], v130 offset:8192\n\t"
        "v_mfma_f32_16x16x32_bf16 v[4:7], a[208:211], v[92:95], v[4:7]\n\t"
        "ds_read_b64_tr_b16 v[74:75], v152 offset:64\n\t"
        "ds_read_b64_tr_b16 a[252:253], v131 offset:24576\n\t"
        "v_mfma_f32_16x16x32_bf16 v[8:11], a[212:215], v[92:95], v[8:11]\n\t"
        "ds_read_b64_tr_b16 v[76:77], v157 offset:128\n\t"
        "ds_read_b64_tr_b16 a[254:255], v131 offset:25344\n\t"
        "v_lshlrev_b32_sdwa v157, 5, v161 dst_sel:DWORD dst_unused:UNUSED_PAD src0_sel:DWORD src1_sel:WORD_0\n\t"
        "v_or_b32 v157, v184, v157\n\t"
        "v_mfma_f32_16x16x32_bf16 v[12:15], a[180:183], v[92:95], v[12:15]\n\t"
        "ds_read_b64_tr_b16 v[78:79], v158 offset:128\n\t"
        "ds_read_b64_tr_b16 v[188:189], v132 offset:24576\n\t"
        "v_lshlrev_b32_sdwa v158, 5, v161 dst_sel:DWORD dst_unused:UNUSED_PAD src0_sel:DWORD src1_sel:WORD_1\n\t"
        "s_waitcnt lgkmcnt(11)\n\t"
        "v_mfma_f32_16x16x32_bf16 v[48:51], v[60:63], v[164:167], 0\n\t"
        "ds_read_b64_tr_b16 v[80:81], v159 offset:128\n\t"
        "ds_read_b64_tr_b16 v[190:191], v132 offset:25344\n\t"
        "v_or_b32 v158, v184, v158\n\t"
        "v_mfma_f32_16x16x32_bf16 v[16:19], a[184:187], v[92:95], v[16:19]\n\t"
        "ds_read_b64_tr_b16 v[82:83], v152 offset:128\n\t"
        "ds_read_b64_tr_b16 v[192:193], v133 offset:24576\n\t"
        "v_xor_b32 v159, 32, v157\n\t"
        "s_waitcnt lgkmcnt(12)\n\t"
        "v_mfma_f32_16x16x32_bf16 v[52:55], v[64:67], v[164:167], 0\n\t"
        "ds_read_b64_tr_b16 v[60:61], v157 offset:0\n\t"
        "ds_read_b64_tr_b16 v[194:195], v133 offset:25344\n\t"
        "v_xor_b32 v152, 32, v158\n\t"
        "v_mfma_f32_16x16x32_bf16 v[20:23], a[188:191], v[92:95], v[20:23]\n\t"
        "ds_read_b64_tr_b16 v[62:63], v158 offset:0\n\t"
        "v_mfma_f32_16x16x32_bf16 v[56:59], v[68:71], v[164:167], 0\n\t"
        "ds_read_b64_tr_b16 v[64:65], v159 offset:0\n\t"
        "v_mfma_f32_16x16x32_bf16 v[24:27], a[192:195], v[92:95], v[24:27]\n\t"
        "ds_read_b64_tr_b16 v[66:67], v152 offset:0\n\t"
        "v_mfma_f32_16x16x32_bf16 v[28:31], a[196:199], v[92:95], v[28:31]\n\t"
        "ds_read_b64_tr_b16 v[68:69], v157 offset:64\n\t"
        "s_waitcnt lgkmcnt(12)\n\t"
        "v_mfma_f32_16x16x32_bf16 v[116:119], v[72:75], v[164:167], 0\n\t"
        "ds_read_b64_tr_b16 v[70:71], v158 offset:64\n\t"
        "v_mfma_f32_16x16x32_bf16 v[32:35], a[200:203], v[92:95], v[32:35]\n\t"
        "ds_read_b64_tr_b16 v[72:73], v159 offset:64\n\t"
        "s_waitcnt lgkmcnt(11)\n\t"
        "v_mfma_f32_16x16x32_bf16 v[120:123], v[76:79], v[164:167], 0\n\t"
        "ds_read_b64_tr_b16 v[74:75], v152 offset:64\n\t"
        "v_mfma_f32_16x16x32_bf16 v[0:3], a[228:231], v[96:99], v[0:3]\n\t"
        "ds_read_b64_tr_b16 v[76:77], v157 offset:128\n\t"
        "s_waitcnt lgkmcnt(10)\n\t"
        "v_mfma_f32_16x16x32_bf16 v[124:127], v[80:83], v[164:167], 0\n\t"
        "ds_read_b64_tr_b16 v[78:79], v158 offset:128\n\t"
        "v_perm_b32 v164, v163, v163, v168\n\t"
        "v_perm_b32 v165, v163, v163, v169\n\t"
        "v_mfma_f32_16x16x32_bf16 v[4:7], a[232:235], v[96:99], v[4:7]\n\t"
        "ds_read_b64_tr_b16 v[80:81], v159 offset:128\n\t"
        "v_perm_b32 v166, v163, v163, v170\n\t"
        "v_perm_b32 v167, v163, v163, v171\n\t"
        "v_mfma_f32_16x16x32_bf16 v[8:11], a[236:239], v[96:99], v[8:11]\n\t"
        "ds_read_b128 v[108:111], v129 offset:12416\n\t"
        "s_waitcnt lgkmcnt(9)\n\t"
        "v_mfma_f32_16x16x32_bf16 v[48:51], v[60:63], v[164:167], v[48:51]\n\t"
        "ds_read_b64_tr_b16 v[82:83], v152 offset:128\n\t"
        "v_mfma_f32_16x16x32_bf16 v[0:3], a[216:219], v[100:103], v[0:3]\n\t"
        "ds_read_b128 v[160:163], v185 offset:256\n\t"
        "ds_read_b64_tr_b16 v[196:197], v131 offset:30720\n\t"
        "s_waitcnt lgkmcnt(10)\n\t"
        "v_mfma_f32_16x16x32_bf16 v[52:55], v[64:67], v[164:167], v[52:55]\n\t"
        "ds_read_b64_tr_b16 v[198:199], v131 offset:31488\n\t"
        "ds_read_b64_tr_b16 v[200:201], v132 offset:30720\n\t"
        "v_mfma_f32_16x16x32_bf16 v[4:7], a[220:223], v[100:103], v[4:7]\n\t"
        "ds_read_b64_tr_b16 v[202:203], v132 offset:31488\n\t"
        "ds_read_b64_tr_b16 v[204:205], v133 offset:30720\n\t"
        "s_waitcnt lgkmcnt(11)\n\t"
        "v_mfma_f32_16x16x32_bf16 v[56:59], v[68:71], v[164:167], v[56:59]\n\t"
        "ds_read_b64_tr_b16 v[206:207], v133 offset:31488\n\t"
        "v_cvt_pk_bf16_f32 v48, v48, v49\n\t"
        "v_cvt_pk_bf16_f32 v49, v50, v51\n\t"
        "v_mfma_f32_16x16x32_bf16 v[8:11], a[224:227], v[100:103], v[8:11]\n\t"
        "ds_write_b64 v186, v[48:49] offset:0\n\t"
        "v_mfma_f32_16x16x32_bf16 v[116:119], v[72:75], v[164:167], v[116:119]\n\t"
        "ds_read_b64_tr_b16 v[208:209], v131 offset:36864\n\t"
        "v_cvt_pk_bf16_f32 v52, v52, v53\n\t"
        "v_cvt_pk_bf16_f32 v53, v54, v55\n\t"
        "v_mfma_f32_16x16x32_bf16 v[12:15], a[228:231], v[100:103], v[12:15]\n\t"
        "s_waitcnt vmcnt(0)\n\t"
        "s_barrier\n\t"
        "s_waitcnt lgkmcnt(10)\n\t"
        "v_mfma_f32_16x16x32_bf16 v[120:123], v[76:79], v[164:167], v[120:123]\n\t"
        "ds_write_b64 v154, v[52:53] offset:0\n\t"
        "v_mfma_f32_16x16x32_bf16 v[16:19], a[232:235], v[100:103], v[16:19]\n\t"
        "ds_read_b128 v[112:115], v130 offset:64\n\t"
        "v_cvt_pk_bf16_f32 v56, v56, v57\n\t"
        "v_cvt_pk_bf16_f32 v57, v58, v59\n\t"
        "s_waitcnt lgkmcnt(9)\n\t"
        "v_mfma_f32_16x16x32_bf16 v[124:127], v[80:83], v[164:167], v[124:127]\n\t"
        "ds_write_b64 v186, v[56:57] offset:64\n\t"
        "v_mfma_f32_16x16x32_bf16 v[20:23], a[236:239], v[100:103], v[20:23]\n\t"
        "ds_read_b64_tr_b16 v[210:211], v131 offset:37632\n\t"
        "v_cvt_pk_bf16_f32 v116, v116, v117\n\t"
        "v_mfma_f32_16x16x32_bf16 v[0:3], a[240:243], v[104:107], v[0:3]\n\t"
        "ds_read_b128 v[84:87], v130 offset:4160\n\t"
        "v_cvt_pk_bf16_f32 v117, v118, v119\n\t"
        "v_mfma_f32_16x16x32_bf16 v[4:7], a[244:247], v[104:107], v[4:7]\n\t"
        "ds_write_b64 v154, v[116:117] offset:64\n\t"
        "v_mfma_f32_16x16x32_bf16 v[8:11], a[248:251], v[104:107], v[8:11]\n\t"
        "ds_read_b64_tr_b16 v[212:213], v132 offset:36864\n\t"
        "v_cvt_pk_bf16_f32 v120, v120, v121\n\t"
        "v_cvt_pk_bf16_f32 v121, v122, v123\n\t"
        "v_mfma_f32_16x16x32_bf16 v[12:15], a[216:219], v[104:107], v[12:15]\n\t"
        "ds_write_b64 v186, v[120:121] offset:128\n\t"
        "v_mfma_f32_16x16x32_bf16 v[16:19], a[220:223], v[104:107], v[16:19]\n\t"
        "ds_read_b64_tr_b16 v[214:215], v132 offset:37632\n\t"
        "v_cvt_pk_bf16_f32 v124, v124, v125\n\t"
        "v_cvt_pk_bf16_f32 v125, v126, v127\n\t"
        "v_mfma_f32_16x16x32_bf16 v[20:23], a[224:227], v[104:107], v[20:23]\n\t"
        "ds_write_b64 v154, v[124:125] offset:128\n\t"
        "v_mfma_f32_16x16x32_bf16 v[24:27], a[228:231], v[104:107], v[24:27]\n\t"
        "ds_read_b128 v[88:91], v130 offset:12288\n\t"
        "ds_read_b64_tr_b16 v[216:217], v133 offset:36864\n\t"
        "v_lshlrev_b32_sdwa v157, 5, v160 dst_sel:DWORD dst_unused:UNUSED_PAD src0_sel:DWORD src1_sel:WORD_0\n\t"
        "v_mfma_f32_16x16x32_bf16 v[28:31], a[232:235], v[104:107], v[28:31]\n\t"
        "ds_read_b64_tr_b16 v[218:219], v133 offset:37632\n\t"
        "v_lshlrev_b32_sdwa v158, 5, v160 dst_sel:DWORD dst_unused:UNUSED_PAD src0_sel:DWORD src1_sel:WORD_1\n\t"
        "v_mfma_f32_16x16x32_bf16 v[32:35], a[236:239], v[104:107], v[32:35]\n\t"
        "ds_read_b64_tr_b16 v[220:221], v131 offset:43008\n\t"
        "v_or_b32 v157, v184, v157\n\t"
        "v_mfma_f32_16x16x32_bf16 v[12:15], a[204:207], v[108:111], v[12:15]\n\t"
        "ds_read_b64_tr_b16 v[60:61], v157 offset:0\n\t"
        "v_or_b32 v158, v184, v158\n\t"
        "v_xor_b32 v159, 32, v157\n\t"
        "v_mfma_f32_16x16x32_bf16 v[16:19], a[208:211], v[108:111], v[16:19]\n\t"
        "ds_read_b64_tr_b16 v[62:63], v158 offset:0\n\t"
        "v_xor_b32 v152, 32, v158\n\t"
        "v_mfma_f32_16x16x32_bf16 v[20:23], a[212:215], v[108:111], v[20:23]\n\t"
        "ds_read_b64_tr_b16 v[64:65], v159 offset:0\n\t"
        "v_perm_b32 v164, v162, v162, v168\n\t"
        "v_mfma_f32_16x16x32_bf16 v[24:27], a[180:183], v[108:111], v[24:27]\n\t"
        "ds_read_b64_tr_b16 v[66:67], v152 offset:0\n\t"
        "v_perm_b32 v165, v162, v162, v169\n\t"
        "v_mfma_f32_16x16x32_bf16 v[28:31], a[184:187], v[108:111], v[28:31]\n\t"
        "ds_read_b64_tr_b16 v[68:69], v157 offset:64\n\t"
        "v_perm_b32 v166, v162, v162, v170\n\t"
        "v_perm_b32 v167, v162, v162, v171\n\t"
        "v_mfma_f32_16x16x32_bf16 v[32:35], a[188:191], v[108:111], v[32:35]\n\t"
        "ds_read_b64_tr_b16 v[70:71], v158 offset:64\n\t"
        "v_mfma_f32_16x16x32_bf16 v[36:39], a[192:195], v[108:111], v[36:39]\n\t"
        "ds_read_b64_tr_b16 v[72:73], v159 offset:64\n\t"
        "v_mfma_f32_16x16x32_bf16 v[40:43], a[196:199], v[108:111], v[40:43]\n\t"
        "ds_read_b64_tr_b16 v[74:75], v152 offset:64\n\t"
        "v_mfma_f32_16x16x32_bf16 v[44:47], a[200:203], v[108:111], v[44:47]\n\t"
        "ds_read_b64_tr_b16 v[76:77], v157 offset:128\n\t"
        "v_lshlrev_b32_sdwa v157, 5, v161 dst_sel:DWORD dst_unused:UNUSED_PAD src0_sel:DWORD src1_sel:WORD_0\n\t"
        "v_or_b32 v157, v184, v157\n\t"
        "s_waitcnt lgkmcnt(9)\n\t"
        "v_mfma_f32_16x16x32_bf16 v[0:3], v[196:199], v[112:115], v[0:3]\n\t"
        "ds_read_b64_tr_b16 v[78:79], v158 offset:128\n\t"
        "ds_read_b128 v[92:95], v130 offset:8256\n\t"
        "v_lshlrev_b32_sdwa v158, 5, v161 dst_sel:DWORD dst_unused:UNUSED_PAD src0_sel:DWORD src1_sel:WORD_1\n\t"
        "v_mfma_f32_16x16x32_bf16 v[4:7], v[200:203], v[112:115], v[4:7]\n\t"
        "ds_read_b64_tr_b16 v[80:81], v159 offset:128\n\t"
        "ds_read_b64_tr_b16 v[222:223], v131 offset:43776\n\t"
        "v_or_b32 v158, v184, v158\n\t"
        "s_waitcnt lgkmcnt(10)\n\t"
        "v_mfma_f32_16x16x32_bf16 v[48:51], v[60:63], v[164:167], 0\n\t"
        "ds_read_b64_tr_b16 v[82:83], v152 offset:128\n\t"
        "ds_read_b64_tr_b16 v[224:225], v132 offset:43008\n\t"
        "v_xor_b32 v159, 32, v157\n\t"
        "v_mfma_f32_16x16x32_bf16 v[8:11], v[204:207], v[112:115], v[8:11]\n\t"
        "ds_read_b64_tr_b16 v[60:61], v157 offset:0\n\t"
        "ds_read_b64_tr_b16 v[226:227], v132 offset:43776\n\t"
        "v_xor_b32 v152, 32, v158\n\t"
        "s_waitcnt lgkmcnt(11)\n\t"
        "v_mfma_f32_16x16x32_bf16 v[52:55], v[64:67], v[164:167], 0\n\t"
        "ds_read_b64_tr_b16 v[62:63], v158 offset:0\n\t"
        "ds_read_b64_tr_b16 v[228:229], v133 offset:43008\n\t"
        "v_mfma_f32_16x16x32_bf16 v[0:3], a[252:255], v[84:87], v[0:3]\n\t"
        "ds_read_b64_tr_b16 v[64:65], v159 offset:0\n\t"
        "ds_read_b64_tr_b16 v[230:231], v133 offset:43776\n\t"
        "v_mfma_f32_16x16x32_bf16 v[56:59], v[68:71], v[164:167], 0\n\t"
        "ds_read_b64_tr_b16 v[66:67], v152 offset:0\n\t"
        "v_mfma_f32_16x16x32_bf16 v[4:7], v[188:191], v[84:87], v[4:7]\n\t"
        "ds_read_b64_tr_b16 v[68:69], v157 offset:64\n\t"
        "s_waitcnt lgkmcnt(13)\n\t"
        "v_mfma_f32_16x16x32_bf16 v[116:119], v[72:75], v[164:167], 0\n\t"
        "ds_read_b64_tr_b16 v[70:71], v158 offset:64\n\t"
        "v_mfma_f32_16x16x32_bf16 v[8:11], v[192:195], v[84:87], v[8:11]\n\t"
        "ds_read_b64_tr_b16 v[72:73], v159 offset:64\n\t"
        "v_mfma_f32_16x16x32_bf16 v[120:123], v[76:79], v[164:167], 0\n\t"
        "ds_read_b64_tr_b16 v[74:75], v152 offset:64\n\t"
        "v_mfma_f32_16x16x32_bf16 v[12:15], v[196:199], v[84:87], v[12:15]\n\t"
        "ds_read_b64_tr_b16 v[76:77], v157 offset:128\n\t"
        "s_waitcnt lgkmcnt(12)\n\t"
        "v_mfma_f32_16x16x32_bf16 v[124:127], v[80:83], v[164:167], 0\n\t"
        "ds_read_b64_tr_b16 v[78:79], v158 offset:128\n\t"
        "v_perm_b32 v164, v163, v163, v168\n\t"
        "v_perm_b32 v165, v163, v163, v169\n\t"
        "v_mfma_f32_16x16x32_bf16 v[16:19], v[200:203], v[84:87], v[16:19]\n\t"
        "s_waitcnt lgkmcnt(7)\n\t"
        "s_barrier\n\t"
        "v_perm_b32 v166, v163, v163, v170\n\t"
        "v_mfma_f32_16x16x32_bf16 v[20:23], v[204:207], v[84:87], v[20:23]\n\t"
        "ds_read_b64_tr_b16 v[80:81], v159 offset:128\n\t"
        "v_perm_b32 v167, v163, v163, v171\n\t"
        "v_mfma_f32_16x16x32_bf16 v[12:15], a[240:243], v[88:91], v[12:15]\n\t"
        "ds_read_b64_tr_b16 v[82:83], v152 offset:128\n\t"
        "v_mfma_f32_16x16x32_bf16 v[48:51], v[60:63], v[164:167], v[48:51]\n\t"
        "ds_read_b128 v[160:163], v185 offset:512\n\t"
        "v_mfma_f32_16x16x32_bf16 v[16:19], a[244:247], v[88:91], v[16:19]\n\t"
        "ds_read_b128 v[96:99], v130 offset:12352\n\t"
        "s_waitcnt lgkmcnt(8)\n\t"
        "v_mfma_f32_16x16x32_bf16 v[52:55], v[64:67], v[164:167], v[52:55]\n\t"
        "ds_read_b64_tr_b16 v[232:233], v131 offset:49152\n\t"
        "v_mfma_f32_16x16x32_bf16 v[20:23], a[248:251], v[88:91], v[20:23]\n\t"
        "ds_read_b64_tr_b16 v[234:235], v131 offset:49920\n\t"
        "v_mfma_f32_16x16x32_bf16 v[56:59], v[68:71], v[164:167], v[56:59]\n\t"
        "ds_read_b64_tr_b16 v[236:237], v132 offset:49152\n\t"
        "v_cvt_pk_bf16_f32 v48, v48, v49\n\t"
        "v_cvt_pk_bf16_f32 v49, v50, v51\n\t"
        "v_mfma_f32_16x16x32_bf16 v[24:27], a[216:219], v[88:91], v[24:27]\n\t"
        "ds_write_b64 v186, v[48:49] offset:12288\n\t"
        "ds_read_b64_tr_b16 v[238:239], v132 offset:49920\n\t"
        "s_waitcnt lgkmcnt(9)\n\t"
        "v_mfma_f32_16x16x32_bf16 v[116:119], v[72:75], v[164:167], v[116:119]\n\t"
        "ds_read_b64_tr_b16 v[240:241], v133 offset:49152\n\t"
        "ds_read_b64_tr_b16 v[242:243], v133 offset:49920\n\t"
        "v_cvt_pk_bf16_f32 v52, v52, v53\n\t"
        "v_cvt_pk_bf16_f32 v53, v54, v55\n\t"
        "v_mfma_f32_16x16x32_bf16 v[28:31], a[220:223], v[88:91], v[28:31]\n\t"
        "ds_write_b64 v154, v[52:53] offset:12288\n\t"
        "v_mfma_f32_16x16x32_bf16 v[120:123], v[76:79], v[164:167], v[120:123]\n\t"
        "ds_read_b64_tr_b16 v[244:245], v131 offset:55296\n\t"
        "v_cvt_pk_bf16_f32 v56, v56, v57\n\t"
        "v_cvt_pk_bf16_f32 v57, v58, v59\n\t"
        "v_mfma_f32_16x16x32_bf16 v[32:35], a[224:227], v[88:91], v[32:35]\n\t"
        "ds_write_b64 v186, v[56:57] offset:12352\n\t"
        "s_waitcnt lgkmcnt(9)\n\t"
        "v_mfma_f32_16x16x32_bf16 v[124:127], v[80:83], v[164:167], v[124:127]\n\t"
        "ds_read_b64_tr_b16 v[246:247], v131 offset:56064\n\t"
        "v_cvt_pk_bf16_f32 v116, v116, v117\n\t"
        "v_cvt_pk_bf16_f32 v117, v118, v119\n\t"
        "v_mfma_f32_16x16x32_bf16 v[36:39], a[228:231], v[88:91], v[36:39]\n\t"
        "ds_write_b64 v154, v[116:117] offset:12352\n\t"
        "v_mfma_f32_16x16x32_bf16 v[40:43], a[232:235], v[88:91], v[40:43]\n\t"
        "ds_read_b128 v[100:103], v130 offset:128\n\t"
        "v_cvt_pk_bf16_f32 v120, v120, v121\n\t"
        "v_mfma_f32_16x16x32_bf16 v[44:47], a[236:239], v[88:91], v[44:47]\n\t"
        "ds_read_b64_tr_b16 v[248:249], v132 offset:55296\n\t"
        "v_cvt_pk_bf16_f32 v121, v122, v123\n\t"
        "v_mfma_f32_16x16x32_bf16 v[0:3], v[208:211], v[92:95], v[0:3]\n\t"
        "ds_write_b64 v186, v[120:121] offset:12416\n\t"
        "v_mfma_f32_16x16x32_bf16 v[4:7], v[212:215], v[92:95], v[4:7]\n\t"
        "ds_read_b128 v[104:107], v130 offset:4224\n\t"
        "v_cvt_pk_bf16_f32 v124, v124, v125\n\t"
        "v_cvt_pk_bf16_f32 v125, v126, v127\n\t"
        "v_mfma_f32_16x16x32_bf16 v[8:11], v[216:219], v[92:95], v[8:11]\n\t"
        "ds_write_b64 v154, v[124:125] offset:12416\n\t"
        "v_mfma_f32_16x16x32_bf16 v[12:15], a[252:255], v[92:95], v[12:15]\n\t"
        "ds_read_b64_tr_b16 v[250:251], v132 offset:56064\n\t"
        "v_lshlrev_b32_sdwa v157, 5, v160 dst_sel:DWORD dst_unused:UNUSED_PAD src0_sel:DWORD src1_sel:WORD_0\n\t"
        "v_lshlrev_b32_sdwa v158, 5, v160 dst_sel:DWORD dst_unused:UNUSED_PAD src0_sel:DWORD src1_sel:WORD_1\n\t"
        "v_mfma_f32_16x16x32_bf16 v[16:19], v[188:191], v[92:95], v[16:19]\n\t"
        "ds_read_b64_tr_b16 v[252:253], v133 offset:55296\n\t"
        "v_or_b32 v157, v184, v157\n\t"
        "v_mfma_f32_16x16x32_bf16 v[20:23], v[192:195], v[92:95], v[20:23]\n\t"
        "ds_read_b64_tr_b16 v[60:61], v157 offset:0\n\t"
        "v_or_b32 v158, v184, v158\n\t"
        "v_mfma_f32_16x16x32_bf16 v[24:27], v[196:199], v[92:95], v[24:27]\n\t"
        "ds_read_b64_tr_b16 v[62:63], v158 offset:0\n\t"
        "ds_read_b128 v[108:111], v129 offset:16448\n\t"
        "v_xor_b32 v159, 32, v157\n\t"
        "v_xor_b32 v152, 32, v158\n\t"
        "v_mfma_f32_16x16x32_bf16 v[28:31], v[200:203], v[92:95], v[28:31]\n\t"
        "ds_read_b64_tr_b16 v[64:65], v159 offset:0\n\t"
        "ds_read_b64_tr_b16 v[254:255], v133 offset:56064\n\t"
        "v_perm_b32 v164, v162, v162, v168\n\t"
        "v_mfma_f32_16x16x32_bf16 v[32:35], v[204:207], v[92:95], v[32:35]\n\t"
        "ds_read_b64_tr_b16 v[66:67], v152 offset:0\n\t"
        "v_perm_b32 v165, v162, v162, v169\n\t"
        "v_mfma_f32_16x16x32_bf16 v[12:15], v[208:211], v[96:99], v[12:15]\n\t"
        ".LpY1%=:\n\t"
        "s_cbranch_execz .LrY%=\n\t"
        "ds_read_b64_tr_b16 v[68:69], v157 offset:64\n\t"
        "v_perm_b32 v166, v162, v162, v170\n\t"
        "v_mfma_f32_16x16x32_bf16 v[16:19], v[212:215], v[96:99], v[16:19]\n\t"
        "ds_read_b64_tr_b16 v[70:71], v158 offset:64\n\t"
        "v_perm_b32 v167, v162, v162, v171\n\t"
        "v_mfma_f32_16x16x32_bf16 v[20:23], v[216:219], v[96:99], v[20:23]\n\t"
        "ds_read_b64_tr_b16 v[72:73], v159 offset:64\n\t"
        "v_mfma_f32_16x16x32_bf16 v[24:27], a[252:255], v[96:99], v[24:27]\n\t"
        "ds_read_b64_tr_b16 v[74:75], v152 offset:64\n\t"
        "v_mfma_f32_16x16x32_bf16 v[28:31], v[188:191], v[96:99], v[28:31]\n\t"
        "ds_read_b64_tr_b16 v[76:77], v157 offset:128\n\t"
        "v_lshlrev_b32_sdwa v157, 5, v161 dst_sel:DWORD dst_unused:UNUSED_PAD src0_sel:DWORD src1_sel:WORD_0\n\t"
        "v_or_b32 v157, v184, v157\n\t"
        "v_mfma_f32_16x16x32_bf16 v[32:35], v[192:195], v[96:99], v[32:35]\n\t"
        "ds_read_b64_tr_b16 v[78:79], v158 offset:128\n\t"
        "v_lshlrev_b32_sdwa v158, 5, v161 dst_sel:DWORD dst_unused:UNUSED_PAD src0_sel:DWORD src1_sel:WORD_1\n\t"
        "v_or_b32 v158, v184, v158\n\t"
        "v_mfma_f32_16x16x32_bf16 v[36:39], v[196:199], v[96:99], v[36:39]\n\t"
        "ds_read_b64_tr_b16 v[80:81], v159 offset:128\n\t"
        "v_xor_b32 v159, 32, v157\n\t"
        "s_waitcnt lgkmcnt(10)\n\t"
        "v_mfma_f32_16x16x32_bf16 v[48:51], v[60:63], v[164:167], 0\n\t"
        "ds_read_b64_tr_b16 v[82:83], v152 offset:128\n\t"
        "v_xor_b32 v152, 32, v158\n\t"
        "v_mfma_f32_16x16x32_bf16 v[40:43], v[200:203], v[96:99], v[40:43]\n\t"
        "ds_read_b64_tr_b16 v[60:61], v157 offset:0\n\t"
        "s_waitcnt lgkmcnt(9)\n\t"
        "v_mfma_f32_16x16x32_bf16 v[52:55], v[64:67], v[164:167], 0\n\t"
        "ds_read_b64_tr_b16 v[62:63], v158 offset:0\n\t"
        "v_mfma_f32_16x16x32_bf16 v[44:47], v[204:207], v[96:99], v[44:47]\n\t"
        "ds_read_b128 v[112:115], v130 offset:8320\n\t"
        "s_waitcnt lgkmcnt(9)\n\t"
        "v_mfma_f32_16x16x32_bf16 v[56:59], v[68:71], v[164:167], 0\n\t"
        "ds_read_b64_tr_b16 v[64:65], v159 offset:0\n\t"
        "v_mfma_f32_16x16x32_bf16 v[0:3], v[232:235], v[100:103], v[0:3]\n\t"
        "ds_read_b64_tr_b16 v[66:67], v152 offset:0\n\t"
        "s_waitcnt lgkmcnt(9)\n\t"
        "v_mfma_f32_16x16x32_bf16 v[116:119], v[72:75], v[164:167], 0\n\t"
        "ds_read_b64_tr_b16 v[68:69], v157 offset:64\n\t"
        "v_mfma_f32_16x16x32_bf16 v[4:7], v[236:239], v[100:103], v[4:7]\n\t"
        "ds_read_b64_tr_b16 v[70:71], v158 offset:64\n\t"
        "s_waitcnt lgkmcnt(9)\n\t"
        "v_mfma_f32_16x16x32_bf16 v[120:123], v[76:79], v[164:167], 0\n\t"
        "ds_read_b64_tr_b16 v[72:73], v159 offset:64\n\t"
        "v_mfma_f32_16x16x32_bf16 v[8:11], v[240:243], v[100:103], v[8:11]\n\t"
        "ds_read_b64_tr_b16 v[74:75], v152 offset:64\n\t"
        "s_waitcnt lgkmcnt(9)\n\t"
        "v_mfma_f32_16x16x32_bf16 v[124:127], v[80:83], v[164:167], 0\n\t"
        "ds_read_b64_tr_b16 v[76:77], v157 offset:128\n\t"
        "v_perm_b32 v164, v163, v163, v168\n\t"
        "v_perm_b32 v165, v163, v163, v169\n\t"
        "v_mfma_f32_16x16x32_bf16 v[0:3], v[220:223], v[104:107], v[0:3]\n\t"
        "ds_read_b64_tr_b16 v[78:79], v158 offset:128\n\t"
        "v_perm_b32 v166, v163, v163, v170\n\t"
        "v_perm_b32 v167, v163, v163, v171\n\t"
        "v_mfma_f32_16x16x32_bf16 v[4:7], v[224:227], v[104:107], v[4:7]\n\t"
        "ds_read_b64_tr_b16 v[80:81], v159 offset:128\n\t"
        "s_waitcnt lgkmcnt(9)\n\t"
        "v_mfma_f32_16x16x32_bf16 v[48:51], v[60:63], v[164:167], v[48:51]\n\t"
        "ds_read_b64_tr_b16 v[82:83], v152 offset:128\n\t"
        "v_mfma_f32_16x16x32_bf16 v[8:11], v[228:231], v[104:107], v[8:11]\n\t"
        "ds_read_b128 v[160:163], v185 offset:768\n\t"
        "s_waitcnt lgkmcnt(9)\n\t"
        "v_mfma_f32_16x16x32_bf16 v[52:55], v[64:67], v[164:167], v[52:55]\n\t"
        "ds_read_b128 v[84:87], v130 offset:12416\n\t"
        "v_mfma_f32_16x16x32_bf16 v[12:15], v[232:235], v[104:107], v[12:15]\n\t"
        "s_waitcnt lgkmcnt(8)\n\t"
        "v_mfma_f32_16x16x32_bf16 v[56:59], v[68:71], v[164:167], v[56:59]\n\t"
        "v_cvt_pk_bf16_f32 v48, v48, v49\n\t"
        "v_cvt_pk_bf16_f32 v49, v50, v51\n\t"
        "v_mfma_f32_16x16x32_bf16 v[16:19], v[236:239], v[104:107], v[16:19]\n\t"
        "ds_write_b64 v186, v[48:49] offset:24576\n\t"
        "s_waitcnt lgkmcnt(7)\n\t"
        "v_mfma_f32_16x16x32_bf16 v[116:119], v[72:75], v[164:167], v[116:119]\n\t"
        "v_cvt_pk_bf16_f32 v52, v52, v53\n\t"
        "v_cvt_pk_bf16_f32 v53, v54, v55\n\t"
        "v_mfma_f32_16x16x32_bf16 v[20:23], v[240:243], v[104:107], v[20:23]\n\t"
        "ds_write_b64 v154, v[52:53] offset:24576\n\t"
        "s_waitcnt lgkmcnt(6)\n\t"
        "v_mfma_f32_16x16x32_bf16 v[120:123], v[76:79], v[164:167], v[120:123]\n\t"
        "v_cvt_pk_bf16_f32 v56, v56, v57\n\t"
        "v_cvt_pk_bf16_f32 v57, v58, v59\n\t"
        "v_mfma_f32_16x16x32_bf16 v[24:27], a[168:171], v[108:111], v[24:27]\n\t"
        "ds_write_b64 v186, v[56:57] offset:24640\n\t"
        "s_waitcnt lgkmcnt(5)\n\t"
        "v_mfma_f32_16x16x32_bf16 v[124:127], v[80:83], v[164:167], v[124:127]\n\t"
        "v_cvt_pk_bf16_f32 v116, v116, v117\n\t"
        "v_cvt_pk_bf16_f32 v117, v118, v119\n\t"
        "s_waitcnt lgkmcnt(4)\n\t"
        "v_lshlrev_b32_sdwa v157, 5, v160 dst_sel:DWORD dst_unused:UNUSED_PAD src0_sel:DWORD src1_sel:WORD_0\n\t"
        "v_mfma_f32_16x16x32_bf16 v[28:31], a[172:175], v[108:111], v[28:31]\n\t"
        "ds_write_b64 v154, v[116:117] offset:24640\n\t"
        "v_mfma_f32_16x16x32_bf16 v[32:35], a[176:179], v[108:111], v[32:35]\n\t"
        "v_cvt_pk_bf16_f32 v120, v120, v121\n\t"
        "v_cvt_pk_bf16_f32 v121, v122, v123\n\t"
        "v_lshlrev_b32_sdwa v158, 5, v160 dst_sel:DWORD dst_unused:UNUSED_PAD src0_sel:DWORD src1_sel:WORD_1\n\t"
        "v_mfma_f32_16x16x32_bf16 v[36:39], a[144:147], v[108:111], v[36:39]\n\t"
        "ds_write_b64 v186, v[120:121] offset:24704\n\t"
        "v_mfma_f32_16x16x32_bf16 v[40:43], a[148:151], v[108:111], v[40:43]\n\t"
        "ds_read_b128 v[88:91], v129 offset:16512\n\t"
        "v_cvt_pk_bf16_f32 v124, v124, v125\n\t"
        "v_cvt_pk_bf16_f32 v125, v126, v127\n\t"
        "v_mfma_f32_16x16x32_bf16 v[44:47], a[152:155], v[108:111], v[44:47]\n\t"
        "ds_write_b64 v154, v[124:125] offset:24704\n\t"
        "v_mfma_f32_16x16x32_bf16 v[0:3], v[244:247], v[112:115], v[0:3]\n\t"
        "v_or_b32 v157, v184, v157\n\t"
        "v_or_b32 v158, v184, v158\n\t"
        "v_xor_b32 v159, 32, v157\n\t"
        "v_mfma_f32_16x16x32_bf16 v[4:7], v[248:251], v[112:115], v[4:7]\n\t"
        "ds_read_b64_tr_b16 v[60:61], v157 offset:0\n\t"
        "v_xor_b32 v152, 32, v158\n\t"
        "v_mfma_f32_16x16x32_bf16 v[8:11], v[252:255], v[112:115], v[8:11]\n\t"
        "ds_read_b64_tr_b16 v[62:63], v158 offset:0\n\t"
        "v_perm_b32 v164, v162, v162, v168\n\t"
        "v_mfma_f32_16x16x32_bf16 v[12:15], v[220:223], v[112:115], v[12:15]\n\t"
        "ds_read_b64_tr_b16 v[64:65], v159 offset:0\n\t"
        "v_perm_b32 v165, v162, v162, v169\n\t"
        "v_mfma_f32_16x16x32_bf16 v[16:19], v[224:227], v[112:115], v[16:19]\n\t"
        "ds_read_b64_tr_b16 v[66:67], v152 offset:0\n\t"
        "v_perm_b32 v166, v162, v162, v170\n\t"
        "v_perm_b32 v167, v162, v162, v171\n\t"
        "v_mfma_f32_16x16x32_bf16 v[20:23], v[228:231], v[112:115], v[20:23]\n\t"
        "ds_read_b64_tr_b16 v[68:69], v157 offset:64\n\t"
        "v_mfma_f32_16x16x32_bf16 v[24:27], v[232:235], v[112:115], v[24:27]\n\t"
        "ds_read_b64_tr_b16 v[70:71], v158 offset:64\n\t"
        "ds_read_b128 v[92:95], v130 offset:16384\n\t"
        "v_mfma_f32_16x16x32_bf16 v[28:31], v[236:239], v[112:115], v[28:31]\n\t"
        "ds_read_b64_tr_b16 v[72:73], v159 offset:64\n\t"
        "v_mfma_f32_16x16x32_bf16 v[32:35], v[240:243], v[112:115], v[32:35]\n\t"
        "ds_read_b64_tr_b16 v[74:75], v152 offset:64\n\t"
        "s_waitcnt lgkmcnt(9)\n\t"
        "v_mfma_f32_16x16x32_bf16 v[12:15], v[244:247], v[84:87], v[12:15]\n\t"
        "ds_read_b64_tr_b16 v[76:77], v157 offset:128\n\t"
        "v_lshlrev_b32_sdwa v157, 5, v161 dst_sel:DWORD dst_unused:UNUSED_PAD src0_sel:DWORD src1_sel:WORD_0\n\t"
        "v_or_b32 v157, v184, v157\n\t"
        "v_mfma_f32_16x16x32_bf16 v[16:19], v[248:251], v[84:87], v[16:19]\n\t"
        "ds_read_b64_tr_b16 v[78:79], v158 offset:128\n\t"
        "v_lshlrev_b32_sdwa v158, 5, v161 dst_sel:DWORD dst_unused:UNUSED_PAD src0_sel:DWORD src1_sel:WORD_1\n\t"
        "v_mfma_f32_16x16x32_bf16 v[20:23], v[252:255], v[84:87], v[20:23]\n\t"
        "ds_read_b64_tr_b16 v[80:81], v159 offset:128\n\t"
        "v_or_b32 v158, v184, v158\n\t"
        "v_xor_b32 v159, 32, v157\n\t"
        "s_waitcnt lgkmcnt(10)\n\t"
        "v_mfma_f32_16x16x32_bf16 v[48:51], v[60:63], v[164:167], 0\n\t"
        "ds_read_b64_tr_b16 v[82:83], v152 offset:128\n\t"
        "v_xor_b32 v152, 32, v158\n\t"
        "v_mfma_f32_16x16x32_bf16 v[24:27], v[220:223], v[84:87], v[24:27]\n\t"
        "ds_read_b64_tr_b16 v[60:61], v157 offset:0\n\t"
        "s_waitcnt lgkmcnt(10)\n\t"
        "v_mfma_f32_16x16x32_bf16 v[52:55], v[64:67], v[164:167], 0\n\t"
        "ds_read_b64_tr_b16 v[62:63], v158 offset:0\n\t"
        "v_mfma_f32_16x16x32_bf16 v[28:31], v[224:227], v[84:87], v[28:31]\n\t"
        "ds_read_b64_tr_b16 v[64:65], v159 offset:0\n\t"
        "ds_read_b128 v[96:99], v130 offset:16448\n\t"
        "s_waitcnt lgkmcnt(10)\n\t"
        "v_mfma_f32_16x16x32_bf16 v[56:59], v[68:71], v[164:167], 0\n\t"
        "ds_read_b64_tr_b16 v[66:67], v152 offset:0\n\t"
        "v_mfma_f32_16x16x32_bf16 v[32:35], v[228:231], v[84:87], v[32:35]\n\t"
        "ds_read_b64_tr_b16 v[68:69], v157 offset:64\n\t"
        "s_waitcnt lgkmcnt(10)\n\t"
        "v_mfma_f32_16x16x32_bf16 v[116:119], v[72:75], v[164:167], 0\n\t"
        "ds_read_b64_tr_b16 v[70:71], v158 offset:64\n\t"
        "v_mfma_f32_16x16x32_bf16 v[36:39], v[232:235], v[84:87], v[36:39]\n\t"
        "ds_read_b64_tr_b16 v[72:73], v159 offset:64\n\t"
        "s_waitcnt lgkmcnt(10)\n\t"
        "v_mfma_f32_16x16x32_bf16 v[120:123], v[76:79], v[164:167], 0\n\t"
        "ds_read_b64_tr_b16 v[74:75], v152 offset:64\n\t"
        "v_mfma_f32_16x16x32_bf16 v[40:43], v[236:239], v[84:87], v[40:43]\n\t"
        "ds_read_b64_tr_b16 v[76:77], v157 offset:128\n\t"
        "s_waitcnt lgkmcnt(10)\n\t"
        "v_mfma_f32_16x16x32_bf16 v[124:127], v[80:83], v[164:167], 0\n\t"
        "ds_read_b64_tr_b16 v[78:79], v158 offset:128\n\t"
        "v_perm_b32 v164, v163, v163, v168\n\t"
        "v_perm_b32 v165, v163, v163, v169\n\t"
        "v_mfma_f32_16x16x32_bf16 v[44:47], v[240:243], v[84:87], v[44:47]\n\t"
        "ds_read_b64_tr_b16 v[80:81], v159 offset:128\n\t"
        "v_perm_b32 v166, v163, v163, v170\n\t"
        "v_perm_b32 v167, v163, v163, v171\n\t"
        "v_mfma_f32_16x16x32_bf16 v[24:27], a[204:207], v[88:91], v[24:27]\n\t"
        "ds_read_b64_tr_b16 v[82:83], v152 offset:128\n\t"
        "s_waitcnt lgkmcnt(10)\n\t"
        "v_mfma_f32_16x16x32_bf16 v[48:51], v[60:63], v[164:167], v[48:51]\n\t"
        "ds_read_b128 v[100:103], v130 offset:16512\n\t"
        "v_mfma_f32_16x16x32_bf16 v[28:31], a[208:211], v[88:91], v[28:31]\n\t"
        "s_waitcnt lgkmcnt(9)\n\t"
        "v_mfma_f32_16x16x32_bf16 v[52:55], v[64:67], v[164:167], v[52:55]\n\t"
        "v_mfma_f32_16x16x32_bf16 v[32:35], a[212:215], v[88:91], v[32:35]\n\t"
        "s_waitcnt lgkmcnt(7)\n\t"
        "v_mfma_f32_16x16x32_bf16 v[56:59], v[68:71], v[164:167], v[56:59]\n\t"
        "s_nop 0\n\t"
        "v_cvt_pk_bf16_f32 v48, v48, v49\n\t"
        "v_cvt_pk_bf16_f32 v49, v50, v51\n\t"
        "v_mfma_f32_16x16x32_bf16 v[36:39], a[180:183], v[88:91], v[36:39]\n\t"
        "ds_write_b64 v186, v[48:49] offset:36864\n\t"
        "s_waitcnt lgkmcnt(5)\n\t"
        "v_mfma_f32_16x16x32_bf16 v[116:119], v[72:75], v[164:167], v[116:119]\n\t"
        "ds_read_b128 v[104:107], v129 offset:20480\n\t"
        "v_cvt_pk_bf16_f32 v52, v52, v53\n\t"
        "v_cvt_pk_bf16_f32 v53, v54, v55\n\t"
        "v_mfma_f32_16x16x32_bf16 v[40:43], a[184:187], v[88:91], v[40:43]\n\t"
        "ds_write_b64 v154, v[52:53] offset:36864\n\t"
        "s_waitcnt lgkmcnt(5)\n\t"
        "v_mfma_f32_16x16x32_bf16 v[120:123], v[76:79], v[164:167], v[120:123]\n\t"
        "v_cvt_pk_bf16_f32 v56, v56, v57\n\t"
        "v_cvt_pk_bf16_f32 v57, v58, v59\n\t"
        "v_mfma_f32_16x16x32_bf16 v[44:47], a[188:191], v[88:91], v[44:47]\n\t"
        "ds_write_b64 v186, v[56:57] offset:36928\n\t"
        "s_waitcnt lgkmcnt(4)\n\t"
        "v_mfma_f32_16x16x32_bf16 v[124:127], v[80:83], v[164:167], v[124:127]\n\t"
        "v_cvt_pk_bf16_f32 v116, v116, v117\n\t"
        "v_cvt_pk_bf16_f32 v117, v118, v119\n\t"
        "v_mfma_f32_16x16x32_bf16 v[24:27], a[240:243], v[92:95], v[24:27]\n\t"
        "ds_write_b64 v154, v[116:117] offset:36928\n\t"
        "v_mfma_f32_16x16x32_bf16 v[28:31], a[244:247], v[92:95], v[28:31]\n\t"
        "ds_read_b128 v[108:111], v129 offset:20544\n\t"
        "v_cvt_pk_bf16_f32 v120, v120, v121\n\t"
        "v_cvt_pk_bf16_f32 v121, v122, v123\n\t"
        "v_mfma_f32_16x16x32_bf16 v[32:35], a[248:251], v[92:95], v[32:35]\n\t"
        "ds_write_b64 v186, v[120:121] offset:36992\n\t"
        "v_mfma_f32_16x16x32_bf16 v[36:39], a[216:219], v[92:95], v[36:39]\n\t"
        "ds_read_b128 v[112:115], v129 offset:20608\n\t"
        "v_cvt_pk_bf16_f32 v124, v124, v125\n\t"
        "v_cvt_pk_bf16_f32 v125, v126, v127\n\t"
        "v_mfma_f32_16x16x32_bf16 v[40:43], a[220:223], v[92:95], v[40:43]\n\t"
        "ds_write_b64 v154, v[124:125] offset:36992\n\t"
        "v_mfma_f32_16x16x32_bf16 v[44:47], a[224:227], v[92:95], v[44:47]\n\t"
        "s_cmp_gt_u32 s61, 64\n\t"
        "s_cbranch_scc0 .Lx72%=\n\t"
        "v_bfe_u32 v52, v185, 4, 4\n\t"
        "v_add_u32 v48, 65, v52\n\t"
        "s_sub_u32 s8, s37, 12288\n\t"
        "v_mov_b32 v51, s62\n\t"
        "v_lshlrev_b32 v53, 3, v52\n\t"
        "v_mul_u32_u24 v52, 192, v52\n\t"
        "v_and_or_b32 v52, v53, 48, v52\n\t"
        "v_add_u32 v49, s8, v52\n\t"
        "v_subrev_u32 v186, 12288, v186\n\t"
        "v_xor_b32 v154, 32, v186\n\t"
        "v_min_u32 v76, s61, v48\n\t"
        "v_lshl_add_u32 v76, v76, 3, s60\n\t"
        "ds_read_b64 v[60:61], v76\n\t"
        "v_cmp_ge_u32 s[66:67], s61, v48\n\t"
        "s_mov_b32 s46, 0x07060302\n\t"
        "s_waitcnt lgkmcnt(0)\n\t"
        "v_and_b32 v76, 0xfffff800, v61\n\t"
        "ds_read_b128 v[62:65], v76 offset:1536\n\t"
        "v_lshlrev_b32 v70, 16, v60\n\t"
        "v_and_b32 v71, 0xffff0000, v60\n\t"
        "v_med3_f32 v70, v70, 0, s63\n\t"
        "v_med3_f32 v71, v71, 0, s63\n\t"
        "s_mov_b64 exec, s[66:67]\n\t"
        "ds_write_b32 v61, v49\n\t"
        "s_mov_b64 exec, -1\n\t"
        "v_perm_b32 v60, v71, v70, s46\n\t"
        "v_fract_f32 v72, v70\n\t"
        "v_fract_f32 v73, v71\n\t"
        "v_cvt_u32_f32 v74, v70\n\t"
        "v_cvt_u32_f32 v75, v71\n\t"
        "v_cmp_neq_f32 s[64:65], 0, v72\n\t"
        "v_cmp_neq_f32 s[44:45], 0, v73\n\t"
        "v_sub_f32 v76, 1.0, v72\n\t"
        "v_sub_f32 v77, 1.0, v73\n\t"
        "v_cvt_pk_bf16_f32 v77, v76, v77\n\t"
        "v_lshlrev_b32 v76, 16, v77\n\t"
        "v_and_b32 v77, 0xffff0000, v77\n\t"
        "v_mul_f32 v78, v76, v77\n\t"
        "v_mul_f32 v76, v76, v73\n\t"
        "v_mul_f32 v77, v72, v77\n\t"
        "v_mul_f32 v79, v72, v73\n\t"
        "v_cndmask_b32 v76, v77, v76, s[44:45]\n\t"
        "v_cndmask_b32 v77, 0, v77, s[44:45]\n\t"
        "v_cvt_pk_bf16_f32 v162, v78, v76\n\t"
        "v_cvt_pk_bf16_f32 v163, v77, v79\n\t"
        "s_waitcnt lgkmcnt(1)\n\t"
        "v_pk_max_u16 v76, v60, v62\n\t"
        "v_pk_min_u16 v76, v76, v63\n\t"
        "v_cmp_ne_u32 vcc, v76, v60\n\t"
        "v_sub_u32 v76, v75, v65\n\t"
        "v_mad_u32_u24 v77, v75, 6, v64\n\t"
        "v_mad_u32_u24 v77, v74, s62, v77\n\t"
        "s_and_b64 vcc, vcc, s[66:67]\n\t"
        "s_or_b64 s[56:57], s[56:57], vcc\n\t"
        "v_bfe_u32 v78, v76, 2, 1\n\t"
        "v_or_b32 v78, v78, v77\n\t"
        "v_add_u32 v76, 1, v76\n\t"
        "v_bfe_u32 v76, v76, 2, 1\n\t"
        "v_add3_u32 v76, v77, v76, 6\n\t"
        "v_cndmask_b32 v76, v78, v76, s[44:45]\n\t"
        "v_cndmask_b32 v77, 0, v51, s[64:65]\n\t"
        "v_add_u32 v79, v78, v77\n\t"
        "v_add_u32 v80, v76, v77\n\t"
        "v_cndmask_b32 v76, v79, v76, s[44:45]\n\t"
        "v_lshl_or_b32 v160, v76, 16, v78\n\t"
        "v_lshl_or_b32 v161, v80, 16, v79\n\t"
        "v_lshlrev_b32_sdwa v157, 5, v160 dst_sel:DWORD dst_unused:UNUSED_PAD src0_sel:DWORD src1_sel:WORD_0\n\t"
        "v_lshlrev_b32_sdwa v158, 5, v160 dst_sel:DWORD dst_unused:UNUSED_PAD src0_sel:DWORD src1_sel:WORD_1\n\t"
        "v_or_b32 v157, v184, v157\n\t"
        "v_or_b32 v158, v184, v158\n\t"
        "v_xor_b32 v159, 32, v157\n\t"
        "v_xor_b32 v152, 32, v158\n\t"
        "ds_read_b64_tr_b16 v[60:61], v157 offset:0\n\t"
        "ds_read_b64_tr_b16 v[62:63], v158 offset:0\n\t"
        "ds_read_b64_tr_b16 v[64:65], v159 offset:0\n\t"
        "ds_read_b64_tr_b16 v[66:67], v152 offset:0\n\t"
        "ds_read_b64_tr_b16 v[68:69], v157 offset:64\n\t"
        "ds_read_b64_tr_b16 v[70:71], v158 offset:64\n\t"
        "ds_read_b64_tr_b16 v[72:73], v159 offset:64\n\t"
        "ds_read_b64_tr_b16 v[74:75], v152 offset:64\n\t"
        "ds_read_b64_tr_b16 v[76:77], v157 offset:128\n\t"
        "ds_read_b64_tr_b16 v[78:79], v158 offset:128\n\t"
        "ds_read_b64_tr_b16 v[80:81], v159 offset:128\n\t"
        "ds_read_b64_tr_b16 v[82:83], v152 offset:128\n\t"
        "v_perm_b32 v164, v162, v162, v168\n\t"
        "v_perm_b32 v165, v162, v162, v169\n\t"
        "v_perm_b32 v166, v162, v162, v170\n\t"
        "v_perm_b32 v167, v162, v162, v171\n\t"
        "s_waitcnt lgkmcnt(10)\n\t"
        "s_nop 0\n\t"
        "v_mfma_f32_16x16x32_bf16 v[48:51], v[60:63], v[164:167], 0\n\t"
        "s_waitcnt lgkmcnt(8)\n\t"
        "v_mfma_f32_16x16x32_bf16 v[52:55], v[64:67], v[164:167], 0\n\t"
        "s_waitcnt lgkmcnt(6)\n\t"
        "v_mfma_f32_16x16x32_bf16 v[56:59], v[68:71], v[164:167], 0\n\t"
        "s_waitcnt lgkmcnt(4)\n\t"
        "v_mfma_f32_16x16x32_bf16 v[116:119], v[72:75], v[164:167], 0\n\t"
        "s_waitcnt lgkmcnt(2)\n\t"
        "v_mfma_f32_16x16x32_bf16 v[120:123], v[76:79], v[164:167], 0\n\t"
        "s_waitcnt lgkmcnt(0)\n\t"
        "v_mfma_f32_16x16x32_bf16 v[124:127], v[80:83], v[164:167], 0\n\t"
        "v_lshlrev_b32_sdwa v157, 5, v161 dst_sel:DWORD dst_unused:UNUSED_PAD src0_sel:DWORD src1_sel:WORD_0\n\t"
        "v_lshlrev_b32_sdwa v158, 5, v161 dst_sel:DWORD dst_unused:UNUSED_PAD src0_sel:DWORD src1_sel:WORD_1\n\t"
        "v_or_b32 v157, v184, v157\n\t"
        "v_or_b32 v158, v184, v158\n\t"
        "v_xor_b32 v159, 32, v157\n\t"
        "v_xor_b32 v152, 32, v158\n\t"
        "ds_read_b64_tr_b16 v[60:61], v157 offset:0\n\t"
        "ds_read_b64_tr_b16 v[62:63], v158 offset:0\n\t"
        "ds_read_b64_tr_b16 v[64:65], v159 offset:0\n\t"
        "ds_read_b64_tr_b16 v[66:67], v152 offset:0\n\t"
        "ds_read_b64_tr_b16 v[68:69], v157 offset:64\n\t"
        "ds_read_b64_tr_b16 v[70:71], v158 offset:64\n\t"
        "ds_read_b64_tr_b16 v[72:73], v159 offset:64\n\t"
        "ds_read_b64_tr_b16 v[74:75], v152 offset:64\n\t"
        "ds_read_b64_tr_b16 v[76:77], v157 offset:128\n\t"
        "ds_read_b64_tr_b16 v[78:79], v158 offset:128\n\t"
        "ds_read_b64_tr_b16 v[80:81], v159 offset:128\n\t"
        "ds_read_b64_tr_b16 v[82:83], v152 offset:128\n\t"
        "v_perm_b32 v164, v163, v163, v168\n\t"
        "v_perm_b32 v165, v163, v163, v169\n\t"
        "v_perm_b32 v166, v163, v163, v170\n\t"
        "v_perm_b32 v167, v163, v163, v171\n\t"
        "s_waitcnt lgkmcnt(10)\n\t"
        "s_nop 0\n\t"
        "v_mfma_f32_16x16x32_bf16 v[48:51], v[60:63], v[164:167], v[48:51]\n\t"
        "s_waitcnt lgkmcnt(8)\n\t"
        "v_mfma_f32_16x16x32_bf16 v[52:55], v[64:67], v[164:167], v[52:55]\n\t"
        "s_waitcnt lgkmcnt(6)\n\t"
        "v_mfma_f32_16x16x32_bf16 v[56:59], v[68:71], v[164:167], v[56:59]\n\t"
        "s_waitcnt lgkmcnt(4)\n\t"
        "v_mfma_f32_16x16x32_bf16 v[116:119], v[72:75], v[164:167], v[116:119]\n\t"
        "s_waitcnt lgkmcnt(2)\n\t"
        "v_mfma_f32_16x16x32_bf16 v[120:123], v[76:79], v[164:167], v[120:123]\n\t"
        "s_waitcnt lgkmcnt(0)\n\t"
        "v_mfma_f32_16x16x32_bf16 v[124:127], v[80:83], v[164:167], v[124:127]\n\t"
        "v_cvt_pk_bf16_f32 v48, v48, v49\n\t"
        "v_cvt_pk_bf16_f32 v49, v50, v51\n\t"
        "v_cvt_pk_bf16_f32 v52, v52, v53\n\t"
        "v_cvt_pk_bf16_f32 v53, v54, v55\n\t"
        "v_cvt_pk_bf16_f32 v56, v56, v57\n\t"
        "v_cvt_pk_bf16_f32 v57, v58, v59\n\t"
        "v_cvt_pk_bf16_f32 v116, v116, v117\n\t"
        "v_cvt_pk_bf16_f32 v117, v118, v119\n\t"
        "v_cvt_pk_bf16_f32 v120, v120, v121\n\t"
        "v_cvt_pk_bf16_f32 v121, v122, v123\n\t"
        "v_cvt_pk_bf16_f32 v124, v124, v125\n\t"
        "v_cvt_pk_bf16_f32 v125, v126, v127\n\t"
        "ds_write_b64 v186, v[48:49] offset:0\n\t"
        "ds_write_b64 v154, v[52:53] offset:0\n\t"
        "ds_write_b64 v186, v[56:57] offset:64\n\t"
        "ds_write_b64 v154, v[116:117] offset:64\n\t"
        "ds_write_b64 v186, v[120:121] offset:128\n\t"
        "ds_write_b64 v154, v[124:125] offset:128\n\t"
        "v_add_u32 v186, 12288, v186\n\t"
        "v_xor_b32 v154, 32, v186\n\t"
        ".Lx72%=:\n\t"
        "v_mfma_f32_16x16x32_bf16 v[24:27], v[208:211], v[96:99], v[24:27]\n\t"
        "s_cmp_gt_u32 s3, 64\n\t"
        "s_cselect_b32 s8, 2, 0\n\t"
        "v_mfma_f32_16x16x32_bf16 v[28:31], v[212:215], v[96:99], v[28:31]\n\t"
        "s_cmp_lg_u64 s[56:57], 0\n\t"
        "s_addc_u32 s8, s8, s6\n\t"
        "v_mov_b32 v151, s8\n\t"
        "v_mfma_f32_16x16x32_bf16 v[32:35], v[216:219], v[96:99], v[32:35]\n\t"
        "s_mov_b64 exec, 1\n\t"
        "ds_write_b32 v153, v151\n\t"
        "s_mov_b64 exec, -1\n\t"
        "v_mfma_f32_16x16x32_bf16 v[36:39], a[252:255], v[96:99], v[36:39]\n\t"
        "ds_read_b128 v[84:87], v130 offset:20480\n\t"
        "ds_read_b128 v[88:91], v130 offset:20544\n\t"
        "v_mfma_f32_16x16x32_bf16 v[40:43], v[188:191], v[96:99], v[40:43]\n\t"
        "v_mfma_f32_16x16x32_bf16 v[44:47], v[192:195], v[96:99], v[44:47]\n\t"
        "v_mfma_f32_16x16x32_bf16 v[24:27], v[244:247], v[100:103], v[24:27]\n\t"
        "ds_read_b128 v[92:95], v130 offset:20608\n\t"
        "v_mfma_f32_16x16x32_bf16 v[28:31], v[248:251], v[100:103], v[28:31]\n\t"
        "v_mfma_f32_16x16x32_bf16 v[32:35], v[252:255], v[100:103], v[32:35]\n\t"
        "s_waitcnt lgkmcnt(3)\n\t"
        "s_barrier\n\t"
        "v_mfma_f32_16x16x32_bf16 v[36:39], v[220:223], v[100:103], v[36:39]\n\t"
        "ds_read_b128 v[124:127], v182 offset:0\n\t"
        "v_mfma_f32_16x16x32_bf16 v[40:43], v[224:227], v[100:103], v[40:43]\n\t"
        "ds_read_b128 v[132:135], v187 offset:4096\n\t"
        "v_mfma_f32_16x16x32_bf16 v[44:47], v[228:231], v[100:103], v[44:47]\n\t"
        "ds_read_b128 v[136:139], v187 offset:4112\n\t"
        "v_mfma_f32_16x16x32_bf16 v[36:39], a[132:135], v[104:107], v[36:39]\n\t"
        "ds_read_b32 v140, v187 offset:4128\n\t"
        "v_mfma_f32_16x16x32_bf16 v[40:43], a[136:139], v[104:107], v[40:43]\n\t"
        "v_mfma_f32_16x16x32_bf16 v[44:47], a[140:143], v[104:107], v[44:47]\n\t"
        "v_mfma_f32_16x16x32_bf16 v[36:39], a[168:171], v[108:111], v[36:39]\n\t"
        "v_mfma_f32_16x16x32_bf16 v[40:43], a[172:175], v[108:111], v[40:43]\n\t"
        "v_mfma_f32_16x16x32_bf16 v[44:47], a[176:179], v[108:111], v[44:47]\n\t"
        "v_mfma_f32_16x16x32_bf16 v[36:39], a[204:207], v[112:115], v[36:39]\n\t"
        "s_waitcnt lgkmcnt(3)\n\t"
        "v_or3_b32 v124, v124, v125, v126\n\t"
        "v_or_b32 v124, v124, v127\n\t"
        "v_mfma_f32_16x16x32_bf16 v[40:43], a[208:211], v[112:115], v[40:43]\n\t"
        "v_readfirstlane_b32 s6, v124\n\t"
        "v_mfma_f32_16x16x32_bf16 v[44:47], a[212:215], v[112:115], v[44:47]\n\t"
        "s_nop 0\n\t"
        "s_bitcmp1_b32 s6, 1\n\t"
        "s_cbranch_scc1 .Lx74%=\n\t"
        ".Lx75%=:\n\t"
        "s_waitcnt lgkmcnt(2)\n\t"
        "v_xor_b32 v132, v181, v132\n\t"
        "v_readlane_b32 s9, v155, 48\n\t"
        "v_mfma_f32_16x16x32_bf16 v[36:39], a[240:243], v[84:87], v[36:39]\n\t"
        "s_nop 0\n\t"
        "v_add_u32 v151, s9, v172\n\t"
        "v_max_u32 v151, v151, v132\n\t"
        "v_xor_b32 v133, v181, v133\n\t"
        "v_add_u32 v183, s9, v173\n\t"
        "v_mfma_f32_16x16x32_bf16 v[40:43], a[244:247], v[84:87], v[40:43]\n\t"
        "ds_read_b128 v[96:99], v151 offset:0\n\t"
        "v_max_u32 v183, v183, v133\n\t"
        "v_xor_b32 v134, v181, v134\n\t"
        "v_mfma_f32_16x16x32_bf16 v[44:47], a[248:251], v[84:87], v[44:47]\n\t"
        "ds_read_b128 v[100:103], v151 offset:64\n\t"
        "s_add_u32 s55, s41, 0\n\t"
        "v_mfma_f32_16x16x32_bf16 v[36:39], v[208:211], v[88:91], v[36:39]\n\t"
        "ds_read_b128 v[104:107], v151 offset:128\n\t"
        "v_add_u32 v151, s9, v174\n\t"
        "v_mfma_f32_16x16x32_bf16 v[40:43], v[212:215], v[88:91], v[40:43]\n\t"
        "ds_read_b128 v[108:111], v183 offset:0\n\t"
        "v_max_u32 v151, v151, v134\n\t"
        "v_xor_b32 v135, v181, v135\n\t"
        "v_mfma_f32_16x16x32_bf16 v[44:47], v[216:219], v[88:91], v[44:47]\n\t"
        "ds_read_b128 v[112:115], v183 offset:64\n\t"
        "v_cvt_pk_bf16_f32 v116, v0, v1\n\t"
        "v_mfma_f32_16x16x32_bf16 v[36:39], v[244:247], v[92:95], v[36:39]\n\t"
        "ds_read_b128 v[84:87], v183 offset:128\n\t"
        "v_add_u32 v183, s9, v175\n\t"
        "v_mfma_f32_16x16x32_bf16 v[40:43], v[248:251], v[92:95], v[40:43]\n\t"
        "ds_read_b128 v[88:91], v151 offset:0\n\t"
        "v_max_u32 v183, v183, v135\n\t"
        "v_mfma_f32_16x16x32_bf16 v[44:47], v[252:255], v[92:95], v[44:47]\n\t"
        "s_waitcnt lgkmcnt(7)\n\t"
        "v_xor_b32 v136, v181, v136\n\t"
        "v_cvt_pk_bf16_f32 v117, v2, v3\n\t"
        "s_waitcnt lgkmcnt(6)\n\t"
        "v_mfma_f32_16x16x32_bf16 v[72:75], a[12:15], v[96:99], 0\n\t"
        "ds_read_b128 v[92:95], v151 offset:64\n\t"
        "v_cvt_pk_bf16_f32 v118, v4, v5\n\t"
        "v_cvt_pk_bf16_f32 v119, v6, v7\n\t"
        "v_mfma_f32_16x16x32_bf16 v[76:79], a[16:19], v[96:99], 0\n\t"
        "v_cvt_pk_bf16_f32 v120, v8, v9\n\t"
        "v_cvt_pk_bf16_f32 v121, v10, v11\n\t"
        "v_mfma_f32_16x16x32_bf16 v[80:83], a[20:23], v[96:99], 0\n\t"
        "v_permlane16_swap_b32 v116, v118\n\t"
        "s_waitcnt lgkmcnt(6)\n\t"
        "v_mfma_f32_16x16x32_bf16 v[72:75], a[48:51], v[100:103], v[72:75]\n\t"
        "v_permlane16_swap_b32 v117, v119\n\t"
        "v_mfma_f32_16x16x32_bf16 v[76:79], a[52:55], v[100:103], v[76:79]\n\t"
        "buffer_store_dwordx4 v[116:119], v144, s[28:31], s55 offen sc1\n\t"
        "v_mfma_f32_16x16x32_bf16 v[80:83], a[56:59], v[100:103], v[80:83]\n\t"
        "ds_write_b64 v146, v[120:121] offset:0\n\t"
        "s_waitcnt lgkmcnt(3)\n\t"
        "v_mfma_f32_16x16x32_bf16 v[72:75], a[84:87], v[104:107], v[72:75]\n\t"
        "ds_read_b128 v[96:99], v151 offset:128\n\t"
        "v_add_u32 v151, s9, v176\n\t"
        "v_max_u32 v151, v151, v136\n\t"
        "v_mfma_f32_16x16x32_bf16 v[76:79], a[88:91], v[104:107], v[76:79]\n\t"
        "ds_read_b128 v[100:103], v183 offset:0\n\t"
        "v_xor_b32 v137, v181, v137\n\t"
        "v_mfma_f32_16x16x32_bf16 v[80:83], a[92:95], v[104:107], v[80:83]\n\t"
        "s_add_u32 s55, s41, 43008\n\t"
        "v_cvt_pk_bf16_f32 v116, v12, v13\n\t"
        "v_mfma_f32_16x16x32_bf16 v[72:75], a[120:123], v[108:111], v[72:75]\n\t"
        "ds_read_b128 v[104:107], v183 offset:64\n\t"
        "v_cvt_pk_bf16_f32 v117, v14, v15\n\t"
        "v_mfma_f32_16x16x32_bf16 v[76:79], a[124:127], v[108:111], v[76:79]\n\t"
        "v_cvt_pk_bf16_f32 v118, v16, v17\n\t"
        "v_cvt_pk_bf16_f32 v119, v18, v19\n\t"
        "v_cvt_pk_bf16_f32 v120, v20, v21\n\t"
        "v_mfma_f32_16x16x32_bf16 v[80:83], a[128:131], v[108:111], v[80:83]\n\t"
        "v_cvt_pk_bf16_f32 v121, v22, v23\n\t"
        "v_mfma_f32_16x16x32_bf16 v[72:75], a[156:159], v[112:115], v[72:75]\n\t"
        "v_permlane16_swap_b32 v116, v118\n\t"
        "v_mfma_f32_16x16x32_bf16 v[76:79], a[160:163], v[112:115], v[76:79]\n\t"
        "v_permlane16_swap_b32 v117, v119\n\t"
        "v_mfma_f32_16x16x32_bf16 v[80:83], a[164:167], v[112:115], v[80:83]\n\t"
        "buffer_store_dwordx4 v[116:119], v144, s[28:31], s55 offen sc1\n\t"
        "v_mfma_f32_16x16x32_bf16 v[72:75], a[192:195], v[84:87], v[72:75]\n\t"
        "ds_write_b64 v146, v[120:121] offset:2048\n\t"
        "v_mfma_f32_16x16x32_bf16 v[76:79], a[196:199], v[84:87], v[76:79]\n\t"
        "ds_read_b128 v[108:111], v183 offset:128\n\t"
        "v_add_u32 v183, s9, v177\n\t"
        "v_max_u32 v183, v183, v137\n\t"
        "v_mfma_f32_16x16x32_bf16 v[80:83], a[200:203], v[84:87], v[80:83]\n\t"
        "ds_read_b128 v[112:115], v151 offset:0\n\t"
        "v_xor_b32 v138, v181, v138\n\t"
        "s_waitcnt lgkmcnt(3)\n\t"
        "v_mfma_f32_16x16x32_bf16 v[72:75], a[228:231], v[88:91], v[72:75]\n\t"
        "ds_read_b128 v[84:87], v151 offset:64\n\t"
        "s_add_u32 s55, s41, 86016\n\t"
        "v_mfma_f32_16x16x32_bf16 v[76:79], a[232:235], v[88:91], v[76:79]\n\t"
        "v_cvt_pk_bf16_f32 v116, v24, v25\n\t"
        "v_cvt_pk_bf16_f32 v117, v26, v27\n\t"
        "v_mfma_f32_16x16x32_bf16 v[80:83], a[236:239], v[88:91], v[80:83]\n\t"
        "v_cvt_pk_bf16_f32 v118, v28, v29\n\t"
        "v_cvt_pk_bf16_f32 v119, v30, v31\n\t"
        "v_cvt_pk_bf16_f32 v120, v32, v33\n\t"
        "v_mfma_f32_16x16x32_bf16 v[72:75], v[196:199], v[92:95], v[72:75]\n\t"
        "ds_read_b128 v[88:91], v151 offset:128\n\t"
        "v_add_u32 v151, s9, v178\n\t"
        "v_mfma_f32_16x16x32_bf16 v[76:79], v[200:203], v[92:95], v[76:79]\n\t"
        "v_max_u32 v151, v151, v138\n\t"
        "v_xor_b32 v139, v181, v139\n\t"
        "v_mfma_f32_16x16x32_bf16 v[80:83], v[204:207], v[92:95], v[80:83]\n\t"
        "v_cvt_pk_bf16_f32 v121, v34, v35\n\t"
        "v_mfma_f32_16x16x32_bf16 v[72:75], v[232:235], v[96:99], v[72:75]\n\t"
        "v_permlane16_swap_b32 v116, v118\n\t"
        "v_mfma_f32_16x16x32_bf16 v[76:79], v[236:239], v[96:99], v[76:79]\n\t"
        "v_permlane16_swap_b32 v117, v119\n\t"
        "v_mfma_f32_16x16x32_bf16 v[80:83], v[240:243], v[96:99], v[80:83]\n\t"
        "buffer_store_dwordx4 v[116:119], v144, s[28:31], s55 offen sc1\n\t"
        "v_mfma_f32_16x16x32_bf16 v[72:75], a[0:3], v[100:103], v[72:75]\n\t"
        "ds_write_b64 v146, v[120:121] offset:4096\n\t"
        "v_mfma_f32_16x16x32_bf16 v[76:79], a[4:7], v[100:103], v[76:79]\n\t"
        "ds_read_b128 v[92:95], v183 offset:0\n\t"
        "s_add_u32 s55, s41, 129024\n\t"
        "v_cvt_pk_bf16_f32 v116, v36, v37\n\t"
        "v_mfma_f32_16x16x32_bf16 v[80:83], a[8:11], v[100:103], v[80:83]\n\t"
        "ds_read_b128 v[96:99], v183 offset:64\n\t"
        "v_cvt_pk_bf16_f32 v117, v38, v39\n\t"
        "v_mfma_f32_16x16x32_bf16 v[72:75], a[36:39], v[104:107], v[72:75]\n\t"
        "ds_read_b128 v[100:103], v183 offset:128\n\t"
        "v_add_u32 v183, s9, v179\n\t"
        "v_mfma_f32_16x16x32_bf16 v[76:79], a[40:43], v[104:107], v[76:79]\n\t"
        "v_max_u32 v183, v183, v139\n\t"
        "v_xor_b32 v140, v181, v140\n\t"
        "v_cvt_pk_bf16_f32 v118, v40, v41\n\t"
        "v_mfma_f32_16x16x32_bf16 v[80:83], a[44:47], v[104:107], v[80:83]\n\t"
        "v_cvt_pk_bf16_f32 v119, v42, v43\n\t"
        "v_cvt_pk_bf16_f32 v120, v44, v45\n\t"
        "s_waitcnt lgkmcnt(4)\n\t"
        "v_mfma_f32_16x16x32_bf16 v[72:75], a[72:75], v[108:111], v[72:75]\n\t"
        "ds_read_b128 v[104:107], v151 offset:0\n\t"
        "v_cvt_pk_bf16_f32 v121, v46, v47\n\t"
        "v_mfma_f32_16x16x32_bf16 v[76:79], a[76:79], v[108:111], v[76:79]\n\t"
        "v_permlane16_swap_b32 v116, v118\n\t"
        "v_mfma_f32_16x16x32_bf16 v[80:83], a[80:83], v[108:111], v[80:83]\n\t"
        "v_permlane16_swap_b32 v117, v119\n\t"
        "v_mfma_f32_16x16x32_bf16 v[72:75], a[108:111], v[112:115], v[72:75]\n\t"
        "buffer_store_dwordx4 v[116:119], v144, s[28:31], s55 offen sc1\n\t"
        "v_mfma_f32_16x16x32_bf16 v[76:79], a[112:115], v[112:115], v[76:79]\n\t"
        "ds_write_b64 v146, v[120:121] offset:6144\n\t"
        "v_mfma_f32_16x16x32_bf16 v[80:83], a[116:119], v[112:115], v[80:83]\n\t"
        "ds_read_b128 v[108:111], v151 offset:64\n\t"
        "v_mfma_f32_16x16x32_bf16 v[72:75], a[144:147], v[84:87], v[72:75]\n\t"
        "ds_read_b128 v[112:115], v151 offset:128\n\t"
        "v_add_u32 v151, s9, v180\n\t"
        "v_max_u32 v151, v151, v140\n\t"
        "v_mfma_f32_16x16x32_bf16 v[76:79], a[148:151], v[84:87], v[76:79]\n\t"
        "v_mfma_f32_16x16x32_bf16 v[80:83], a[152:155], v[84:87], v[80:83]\n\t"
        "ds_read_b128 v[84:87], v183 offset:0\n\t"
        "v_mfma_f32_16x16x32_bf16 v[72:75], a[180:183], v[88:91], v[72:75]\n\t"
        "v_mfma_f32_16x16x32_bf16 v[76:79], a[184:187], v[88:91], v[76:79]\n\t"
        "s_waitcnt lgkmcnt(3)\n\t"
        "s_barrier\n\t"
        "v_mfma_f32_16x16x32_bf16 v[80:83], a[188:191], v[88:91], v[80:83]\n\t"
        "ds_read_b128 v[88:91], v183 offset:64\n\t"
        "v_mfma_f32_16x16x32_bf16 v[72:75], a[216:219], v[92:95], v[72:75]\n\t"
        ".LpY2%=:\n\t"
        "s_cbranch_execz .LrY%=\n\t"
        "ds_read_b128 v[124:127], v147 offset:0\n\t"
        "v_mfma_f32_16x16x32_bf16 v[76:79], a[220:223], v[92:95], v[76:79]\n\t"
        "v_mfma_f32_16x16x32_bf16 v[80:83], a[224:227], v[92:95], v[80:83]\n\t"
        "ds_read_b128 v[92:95], v183 offset:128\n\t"
        "v_mfma_f32_16x16x32_bf16 v[72:75], a[252:255], v[96:99], v[72:75]\n\t"
        "v_mfma_f32_16x16x32_bf16 v[76:79], v[188:191], v[96:99], v[76:79]\n\t"
        "v_mfma_f32_16x16x32_bf16 v[80:83], v[192:195], v[96:99], v[80:83]\n\t"
        "ds_read_b128 v[96:99], v151 offset:0\n\t"
        "v_mfma_f32_16x16x32_bf16 v[72:75], v[220:223], v[100:103], v[72:75]\n\t"
        "v_mfma_f32_16x16x32_bf16 v[76:79], v[224:227], v[100:103], v[76:79]\n\t"
        "v_mfma_f32_16x16x32_bf16 v[80:83], v[228:231], v[100:103], v[80:83]\n\t"
        "ds_read_b128 v[100:103], v151 offset:64\n\t"
        "v_mfma_f32_16x16x32_bf16 v[72:75], a[24:27], v[104:107], v[72:75]\n\t"
        "s_waitcnt lgkmcnt(3)\n\t"
        "buffer_store_dwordx4 v[124:127], v145, s[28:31], s50 offen sc1\n\t"
        "v_mfma_f32_16x16x32_bf16 v[76:79], a[28:31], v[104:107], v[76:79]\n\t"
        "ds_read_b128 v[124:127], v147 offset:4096\n\t"
        "s_add_u32 s50, s50, 86016\n\t"
        "v_mfma_f32_16x16x32_bf16 v[80:83], a[32:35], v[104:107], v[80:83]\n\t"
        "ds_read_b128 v[104:107], v151 offset:128\n\t"
        "v_mfma_f32_16x16x32_bf16 v[80:83], a[68:71], v[108:111], v[80:83]\n\t"
        "v_mfma_f32_16x16x32_bf16 v[80:83], a[104:107], v[112:115], v[80:83]\n\t"
        "v_mfma_f32_16x16x32_bf16 v[80:83], a[140:143], v[84:87], v[80:83]\n\t"
        "v_mfma_f32_16x16x32_bf16 v[80:83], a[176:179], v[88:91], v[80:83]\n\t"
        "s_waitcnt lgkmcnt(3)\n\t"
        "v_mfma_f32_16x16x32_bf16 v[80:83], a[212:215], v[92:95], v[80:83]\n\t"
        "v_mfma_f32_16x16x32_bf16 v[80:83], a[248:251], v[96:99], v[80:83]\n\t"
        "s_waitcnt lgkmcnt(2)\n\t"
        "v_mfma_f32_16x16x32_bf16 v[80:83], v[216:219], v[100:103], v[80:83]\n\t"
        "s_waitcnt lgkmcnt(0)\n\t"
        "v_mfma_f32_16x16x32_bf16 v[80:83], v[252:255], v[104:107], v[80:83]\n\t"
        "buffer_store_dwordx4 v[124:127], v145, s[28:31], s50 offen sc1\n\t"
        "v_mfma_f32_16x16x32_bf16 v[72:75], a[60:63], v[108:111], v[72:75]\n\t"
        "v_mfma_f32_16x16x32_bf16 v[76:79], a[64:67], v[108:111], v[76:79]\n\t"
        "s_nop 4\n\t"
        "v_cvt_pk_bf16_f32 v120, v80, v81\n\t"
        "v_cvt_pk_bf16_f32 v121, v82, v83\n\t"
        "v_mfma_f32_16x16x32_bf16 v[72:75], a[96:99], v[112:115], v[72:75]\n\t"
        "ds_write_b64 v146, v[120:121] offset:10240\n\t"
        "v_mfma_f32_16x16x32_bf16 v[76:79], a[100:103], v[112:115], v[76:79]\n\t"
        "v_mfma_f32_16x16x32_bf16 v[72:75], a[132:135], v[84:87], v[72:75]\n\t"
        "v_mfma_f32_16x16x32_bf16 v[76:79], a[136:139], v[84:87], v[76:79]\n\t"
        "s_waitcnt lgkmcnt(0)\n\t"
        "s_barrier\n\t"
        "v_readlane_b32 s55, v155, 51\n\t"
        "v_mfma_f32_16x16x32_bf16 v[72:75], a[168:171], v[88:91], v[72:75]\n\t"
        "ds_read_b128 v[124:127], v147 offset:8192\n\t"
        "v_mfma_f32_16x16x32_bf16 v[76:79], a[172:175], v[88:91], v[76:79]\n\t"
        "v_mfma_f32_16x16x32_bf16 v[72:75], a[204:207], v[92:95], v[72:75]\n\t"
        "v_mfma_f32_16x16x32_bf16 v[76:79], a[208:211], v[92:95], v[76:79]\n\t"
        "v_mfma_f32_16x16x32_bf16 v[72:75], a[240:243], v[96:99], v[72:75]\n\t"
        "v_mfma_f32_16x16x32_bf16 v[76:79], a[244:247], v[96:99], v[76:79]\n\t"
        "v_mfma_f32_16x16x32_bf16 v[72:75], v[208:211], v[100:103], v[72:75]\n\t"
        "v_mfma_f32_16x16x32_bf16 v[76:79], v[212:215], v[100:103], v[76:79]\n\t"
        "v_mfma_f32_16x16x32_bf16 v[72:75], v[244:247], v[104:107], v[72:75]\n\t"
        "v_mfma_f32_16x16x32_bf16 v[76:79], v[248:251], v[104:107], v[76:79]\n\t"
        "s_nop 6\n\t"
        "v_cvt_pk_bf16_f32 v116, v72, v73\n\t"
        "v_cvt_pk_bf16_f32 v117, v74, v75\n\t"
        "v_cvt_pk_bf16_f32 v118, v76, v77\n\t"
        "v_cvt_pk_bf16_f32 v119, v78, v79\n\t"
        "s_nop 0\n\t"
        "v_permlane16_swap_b32 v116, v118\n\t"
        "v_permlane16_swap_b32 v117, v119\n\t"
        "buffer_store_dwordx4 v[116:119], v148, s[28:31], s55 offen  \n\t"
        "s_waitcnt lgkmcnt(0)\n\t"
        "buffer_store_dwordx4 v[124:127], v150, s[28:31], s52 offen  \n\t"
        "s_andn2_b32 s6, s6, 2\n\t"
        "s_cbranch_scc1 99f\n\t"
        "s_endpgm\n\t"
        ".Lx50%=:\n\t"
        "v_mov_b32 v151, v185\n\t"
        "v_mov_b32 v183, v186\n\t"
        "s_min_u32 s8, s3, 64\n\t"
        "s_waitcnt lgkmcnt(0)\n\t"
        "30:\n\t"
        "ds_read_b128 v[160:163], v185\n\t"
        "s_waitcnt lgkmcnt(0)\n\t"
        "v_lshlrev_b32_sdwa v157, 5, v160 dst_sel:DWORD dst_unused:UNUSED_PAD src0_sel:DWORD src1_sel:WORD_0\n\t"
        "v_lshlrev_b32_sdwa v158, 5, v160 dst_sel:DWORD dst_unused:UNUSED_PAD src0_sel:DWORD src1_sel:WORD_1\n\t"
        "v_or_b32 v157, v184, v157\n\t"
        "v_or_b32 v158, v184, v158\n\t"
        "v_xor_b32 v159, 32, v157\n\t"
        "v_xor_b32 v152, 32, v158\n\t"
        "ds_read_b64_tr_b16 v[60:61], v157 offset:0\n\t"
        "ds_read_b64_tr_b16 v[62:63], v158 offset:0\n\t"
        "ds_read_b64_tr_b16 v[64:65], v159 offset:0\n\t"
        "ds_read_b64_tr_b16 v[66:67], v152 offset:0\n\t"
        "ds_read_b64_tr_b16 v[68:69], v157 offset:64\n\t"
        "ds_read_b64_tr_b16 v[70:71], v158 offset:64\n\t"
        "ds_read_b64_tr_b16 v[72:73], v159 offset:64\n\t"
        "ds_read_b64_tr_b16 v[74:75], v152 offset:64\n\t"
        "ds_read_b64_tr_b16 v[76:77], v157 offset:128\n\t"
        "ds_read_b64_tr_b16 v[78:79], v158 offset:128\n\t"
        "ds_read_b64_tr_b16 v[80:81], v159 offset:128\n\t"
        "ds_read_b64_tr_b16 v[82:83], v152 offset:128\n\t"
        "v_perm_b32 v164, v162, v162, v168\n\t"
        "v_perm_b32 v165, v162, v162, v169\n\t"
        "v_perm_b32 v166, v162, v162, v170\n\t"
        "v_perm_b32 v167, v162, v162, v171\n\t"
        "v_cmp_ne_u32 vcc, 0, v163\n\t"
        "s_waitcnt lgkmcnt(10)\n\t"
        "v_mfma_f32_16x16x32_bf16 v[48:51], v[60:63], v[164:167], 0\n\t"
        "s_waitcnt lgkmcnt(8)\n\t"
        "v_mfma_f32_16x16x32_bf16 v[52:55], v[64:67], v[164:167], 0\n\t"
        "s_waitcnt lgkmcnt(6)\n\t"
        "v_mfma_f32_16x16x32_bf16 v[56:59], v[68:71], v[164:167], 0\n\t"
        "s_waitcnt lgkmcnt(4)\n\t"
        "v_mfma_f32_16x16x32_bf16 v[116:119], v[72:75], v[164:167], 0\n\t"
        "s_waitcnt lgkmcnt(2)\n\t"
        "v_mfma_f32_16x16x32_bf16 v[120:123], v[76:79], v[164:167], 0\n\t"
        "s_waitcnt lgkmcnt(0)\n\t"
        "v_mfma_f32_16x16x32_bf16 v[124:127], v[80:83], v[164:167], 0\n\t"
        "s_cbranch_vccz 31f\n\t"
        "v_lshlrev_b32_sdwa v157, 5, v161 dst_sel:DWORD dst_unused:UNUSED_PAD src0_sel:DWORD src1_sel:WORD_0\n\t"
        "v_lshlrev_b32_sdwa v158, 5, v161 dst_sel:DWORD dst_unused:UNUSED_PAD src0_sel:DWORD src1_sel:WORD_1\n\t"
        "v_or_b32 v157, v184, v157\n\t"
        "v_or_b32 v158, v184, v158\n\t"
        "v_xor_b32 v159, 32, v157\n\t"
        "v_xor_b32 v152, 32, v158\n\t"
        "ds_read_b64_tr_b16 v[60:61], v157 offset:0\n\t"
        "ds_read_b64_tr_b16 v[62:63], v158 offset:0\n\t"
        "ds_read_b64_tr_b16 v[64:65], v159 offset:0\n\t"
        "ds_read_b64_tr_b16 v[66:67], v152 offset:0\n\t"
        "ds_read_b64_tr_b16 v[68:69], v157 offset:64\n\t"
        "ds_read_b64_tr_b16 v[70:71], v158 offset:64\n\t"
        "ds_read_b64_tr_b16 v[72:73], v159 offset:64\n\t"
        "ds_read_b64_tr_b16 v[74:75], v152 offset:64\n\t"
        "ds_read_b64_tr_b16 v[76:77], v157 offset:128\n\t"
        "ds_read_b64_tr_b16 v[78:79], v158 offset:128\n\t"
        "ds_read_b64_tr_b16 v[80:81], v159 offset:128\n\t"
        "ds_read_b64_tr_b16 v[82:83], v152 offset:128\n\t"
        "v_perm_b32 v164, v163, v163, v168\n\t"
        "v_perm_b32 v165, v163, v163, v169\n\t"
        "v_perm_b32 v166, v163, v163, v170\n\t"
        "v_perm_b32 v167, v163, v163, v171\n\t"
        "s_waitcnt lgkmcnt(10)\n\t"
        "s_nop 0\n\t"
        "v_mfma_f32_16x16x32_bf16 v[48:51], v[60:63], v[164:167], v[48:51]\n\t"
        "s_waitcnt lgkmcnt(8)\n\t"
        "v_mfma_f32_16x16x32_bf16 v[52:55], v[64:67], v[164:167], v[52:55]\n\t"
        "s_waitcnt lgkmcnt(6)\n\t"
        "v_mfma_f32_16x16x32_bf16 v[56:59], v[68:71], v[164:167], v[56:59]\n\t"
        "s_waitcnt lgkmcnt(4)\n\t"
        "v_mfma_f32_16x16x32_bf16 v[116:119], v[72:75], v[164:167], v[116:119]\n\t"
        "s_waitcnt lgkmcnt(2)\n\t"
        "v_mfma_f32_16x16x32_bf16 v[120:123], v[76:79], v[164:167], v[120:123]\n\t"
        "s_waitcnt lgkmcnt(0)\n\t"
        "v_mfma_f32_16x16x32_bf16 v[124:127], v[80:83], v[164:167], v[124:127]\n\t"
        "31:\n\t"
        "v_cvt_pk_bf16_f32 v48, v48, v49\n\t"
        "v_cvt_pk_bf16_f32 v49, v50, v51\n\t"
        "v_cvt_pk_bf16_f32 v52, v52, v53\n\t"
        "v_cvt_pk_bf16_f32 v53, v54, v55\n\t"
        "v_cvt_pk_bf16_f32 v56, v56, v57\n\t"
        "v_cvt_pk_bf16_f32 v57, v58, v59\n\t"
        "v_cvt_pk_bf16_f32 v116, v116, v117\n\t"
        "v_cvt_pk_bf16_f32 v117, v118, v119\n\t"
        "v_cvt_pk_bf16_f32 v120, v120, v121\n\t"
        "v_cvt_pk_bf16_f32 v121, v122, v123\n\t"
        "v_cvt_pk_bf16_f32 v124, v124, v125\n\t"
        "v_cvt_pk_bf16_f32 v125, v126, v127\n\t"
        "ds_write_b64 v186, v[48:49] offset:0\n\t"
        "ds_write_b64 v154, v[52:53] offset:0\n\t"
        "ds_write_b64 v186, v[56:57] offset:64\n\t"
        "ds_write_b64 v154, v[116:117] offset:64\n\t"
        "ds_write_b64 v186, v[120:121] offset:128\n\t"
        "ds_write_b64 v154, v[124:125] offset:128\n\t"
        "v_add_u32 v185, 256, v185\n\t"
        "v_add_u32 v186, 12288, v186\n\t"
        "v_xor_b32 v154, 32, v186\n\t"
        "s_sub_i32 s8, s8, 16\n\t"
        "s_cmp_gt_i32 s8, 0\n\t"
        "s_cbranch_scc1 30b\n\t"
        "v_mov_b32 v185, v151\n\t"
        "v_mov_b32 v186, v183\n\t"
        "v_xor_b32 v154, 32, v186\n\t"
        "s_waitcnt lgkmcnt(0)\n\t"
        "s_barrier\n\t"
        "s_branch .Lx51%=\n\t"
        ".LwM%=:\n\t"
        "s_branch .LrM%=\n\t"
        ".Lx54%=:\n\t"
        "v_add_u32 v185, 768, v185\n\t"
        "v_add_u32 v186, 36864, v186\n\t"
        "v_xor_b32 v154, 32, v186\n\t"
        "s_min_u32 s47, s61, 64\n\t"
        "s_sub_u32 s47, s47, 48\n\t"
        "s_waitcnt lgkmcnt(0)\n\t"
        "40:\n\t"
        "ds_read_b128 v[160:163], v185\n\t"
        "s_waitcnt lgkmcnt(0)\n\t"
        "v_lshlrev_b32_sdwa v157, 5, v160 dst_sel:DWORD dst_unused:UNUSED_PAD src0_sel:DWORD src1_sel:WORD_0\n\t"
        "v_lshlrev_b32_sdwa v158, 5, v160 dst_sel:DWORD dst_unused:UNUSED_PAD src0_sel:DWORD src1_sel:WORD_1\n\t"
        "v_or_b32 v157, v184, v157\n\t"
        "v_or_b32 v158, v184, v158\n\t"
        "v_xor_b32 v159, 32, v157\n\t"
        "v_xor_b32 v152, 32, v158\n\t"
        "ds_read_b64_tr_b16 v[60:61], v157 offset:0\n\t"
        "ds_read_b64_tr_b16 v[62:63], v158 offset:0\n\t"
        "ds_read_b64_tr_b16 v[64:65], v159 offset:0\n\t"
        "ds_read_b64_tr_b16 v[66:67], v152 offset:0\n\t"
        "ds_read_b64_tr_b16 v[68:69], v157 offset:64\n\t"
        "ds_read_b64_tr_b16 v[70:71], v158 offset:64\n\t"
        "ds_read_b64_tr_b16 v[72:73], v159 offset:64\n\t"
        "ds_read_b64_tr_b16 v[74:75], v152 offset:64\n\t"
        "ds_read_b64_tr_b16 v[76:77], v157 offset:128\n\t"
        "ds_read_b64_tr_b16 v[78:79], v158 offset:128\n\t"
        "ds_read_b64_tr_b16 v[80:81], v159 offset:128\n\t"
        "ds_read_b64_tr_b16 v[82:83], v152 offset:128\n\t"
        "v_perm_b32 v164, v162, v162, v168\n\t"
        "v_perm_b32 v165, v162, v162, v169\n\t"
        "v_perm_b32 v166, v162, v162, v170\n\t"
        "v_perm_b32 v167, v162, v162, v171\n\t"
        "v_cmp_ne_u32 vcc, 0, v163\n\t"
        "s_waitcnt lgkmcnt(10)\n\t"
        "v_mfma_f32_16x16x32_bf16 v[0:3], v[60:63], v[164:167], 0\n\t"
        "s_waitcnt lgkmcnt(8)\n\t"
        "v_mfma_f32_16x16x32_bf16 v[4:7], v[64:67], v[164:167], 0\n\t"
        "s_waitcnt lgkmcnt(6)\n\t"
        "v_mfma_f32_16x16x32_bf16 v[8:11], v[68:71], v[164:167], 0\n\t"
        "s_waitcnt lgkmcnt(4)\n\t"
        "v_mfma_f32_16x16x32_bf16 v[12:15], v[72:75], v[164:167], 0\n\t"
        "s_waitcnt lgkmcnt(2)\n\t"
        "v_mfma_f32_16x16x32_bf16 v[16:19], v[76:79], v[164:167], 0\n\t"
        "s_waitcnt lgkmcnt(0)\n\t"
        "v_mfma_f32_16x16x32_bf16 v[20:23], v[80:83], v[164:167], 0\n\t"
        "s_cbranch_vccz 41f\n\t"
        "v_lshlrev_b32_sdwa v157, 5, v161 dst_sel:DWORD dst_unused:UNUSED_PAD src0_sel:DWORD src1_sel:WORD_0\n\t"
        "v_lshlrev_b32_sdwa v158, 5, v161 dst_sel:DWORD dst_unused:UNUSED_PAD src0_sel:DWORD src1_sel:WORD_1\n\t"
        "v_or_b32 v157, v184, v157\n\t"
        "v_or_b32 v158, v184, v158\n\t"
        "v_xor_b32 v159, 32, v157\n\t"
        "v_xor_b32 v152, 32, v158\n\t"
        "ds_read_b64_tr_b16 v[60:61], v157 offset:0\n\t"
        "ds_read_b64_tr_b16 v[62:63], v158 offset:0\n\t"
        "ds_read_b64_tr_b16 v[64:65], v159 offset:0\n\t"
        "ds_read_b64_tr_b16 v[66:67], v152 offset:0\n\t"
        "ds_read_b64_tr_b16 v[68:69], v157 offset:64\n\t"
        "ds_read_b64_tr_b16 v[70:71], v158 offset:64\n\t"
        "ds_read_b64_tr_b16 v[72:73], v159 offset:64\n\t"
        "ds_read_b64_tr_b16 v[74:75], v152 offset:64\n\t"
        "ds_read_b64_tr_b16 v[76:77], v157 offset:128\n\t"
        "ds_read_b64_tr_b16 v[78:79], v158 offset:128\n\t"
        "ds_read_b64_tr_b16 v[80:81], v159 offset:128\n\t"
        "ds_read_b64_tr_b16 v[82:83], v152 offset:128\n\t"
        "v_perm_b32 v164, v163, v163, v168\n\t"
        "v_perm_b32 v165, v163, v163, v169\n\t"
        "v_perm_b32 v166, v163, v163, v170\n\t"
        "v_perm_b32 v167, v163, v163, v171\n\t"
        "s_waitcnt lgkmcnt(10)\n\t"
        "s_nop 0\n\t"
        "v_mfma_f32_16x16x32_bf16 v[0:3], v[60:63], v[164:167], v[0:3]\n\t"
        "s_waitcnt lgkmcnt(8)\n\t"
        "v_mfma_f32_16x16x32_bf16 v[4:7], v[64:67], v[164:167], v[4:7]\n\t"
        "s_waitcnt lgkmcnt(6)\n\t"
        "v_mfma_f32_16x16x32_bf16 v[8:11], v[68:71], v[164:167], v[8:11]\n\t"
        "s_waitcnt lgkmcnt(4)\n\t"
        "v_mfma_f32_16x16x32_bf16 v[12:15], v[72:75], v[164:167], v[12:15]\n\t"
        "s_waitcnt lgkmcnt(2)\n\t"
        "v_mfma_f32_16x16x32_bf16 v[16:19], v[76:79], v[164:167], v[16:19]\n\t"
        "s_waitcnt lgkmcnt(0)\n\t"
        "v_mfma_f32_16x16x32_bf16 v[20:23], v[80:83], v[164:167], v[20:23]\n\t"
        "41:\n\t"
        "v_cvt_pk_bf16_f32 v0, v0, v1\n\t"
        "v_cvt_pk_bf16_f32 v1, v2, v3\n\t"
        "v_cvt_pk_bf16_f32 v4, v4, v5\n\t"
        "v_cvt_pk_bf16_f32 v5, v6, v7\n\t"
        "v_cvt_pk_bf16_f32 v8, v8, v9\n\t"
        "v_cvt_pk_bf16_f32 v9, v10, v11\n\t"
        "v_cvt_pk_bf16_f32 v12, v12, v13\n\t"
        "v_cvt_pk_bf16_f32 v13, v14, v15\n\t"
        "v_cvt_pk_bf16_f32 v16, v16, v17\n\t"
        "v_cvt_pk_bf16_f32 v17, v18, v19\n\t"
        "v_cvt_pk_bf16_f32 v20, v20, v21\n\t"
        "v_cvt_pk_bf16_f32 v21, v22, v23\n\t"
        "ds_write_b64 v186, v[0:1] offset:0\n\t"
        "ds_write_b64 v154, v[4:5] offset:0\n\t"
        "ds_write_b64 v186, v[8:9] offset:64\n\t"
        "ds_write_b64 v154, v[12:13] offset:64\n\t"
        "ds_write_b64 v186, v[16:17] offset:128\n\t"
        "ds_write_b64 v154, v[20:21] offset:128\n\t"
        "v_add_u32 v185, 256, v185\n\t"
        "v_add_u32 v186, 12288, v186\n\t"
        "v_xor_b32 v154, 32, v186\n\t"
        "s_sub_i32 s47, s47, 16\n\t"
        "s_cmp_gt_i32 s47, 0\n\t"
        "s_cbranch_scc1 40b\n\t"
        "s_cmp_le_u32 s61, 64\n\t"
        "s_cbranch_scc1 52f\n\t"
        "v_bfe_u32 v64, v185, 4, 4\n\t"
        "v_add_u32 v60, 65, v64\n\t"
        "s_sub_u32 s8, s37, 12288\n\t"
        "v_mov_b32 v63, s62\n\t"
        "v_lshlrev_b32 v65, 3, v64\n\t"
        "v_mul_u32_u24 v64, 192, v64\n\t"
        "v_and_or_b32 v64, v65, 48, v64\n\t"
        "v_add_u32 v61, s8, v64\n\t"
        "v_subrev_u32 v186, 61440, v186\n\t"
        "v_xor_b32 v154, 32, v186\n\t"
        "v_min_u32 v16, s61, v60\n\t"
        "v_lshl_add_u32 v16, v16, 3, s60\n\t"
        "ds_read_b64 v[0:1], v16\n\t"
        "v_cmp_ge_u32 s[66:67], s61, v60\n\t"
        "s_mov_b32 s46, 0x07060302\n\t"
        "s_waitcnt lgkmcnt(0)\n\t"
        "v_and_b32 v16, 0xfffff800, v1\n\t"
        "ds_read_b128 v[2:5], v16 offset:1536\n\t"
        "v_lshlrev_b32 v10, 16, v0\n\t"
        "v_and_b32 v11, 0xffff0000, v0\n\t"
        "v_med3_f32 v10, v10, 0, s63\n\t"
        "v_med3_f32 v11, v11, 0, s63\n\t"
        "s_mov_b64 exec, s[66:67]\n\t"
        "ds_write_b32 v1, v61\n\t"
        "s_mov_b64 exec, -1\n\t"
        "v_perm_b32 v0, v11, v10, s46\n\t"
        "v_fract_f32 v12, v10\n\t"
        "v_fract_f32 v13, v11\n\t"
        "v_cvt_u32_f32 v14, v10\n\t"
        "v_cvt_u32_f32 v15, v11\n\t"
        "v_cmp_neq_f32 s[64:65], 0, v12\n\t"
        "v_cmp_neq_f32 s[44:45], 0, v13\n\t"
        "v_sub_f32 v16, 1.0, v12\n\t"
        "v_sub_f32 v17, 1.0, v13\n\t"
        "v_cvt_pk_bf16_f32 v17, v16, v17\n\t"
        "v_lshlrev_b32 v16, 16, v17\n\t"
        "v_and_b32 v17, 0xffff0000, v17\n\t"
        "v_mul_f32 v18, v16, v17\n\t"
        "v_mul_f32 v16, v16, v13\n\t"
        "v_mul_f32 v17, v12, v17\n\t"
        "v_mul_f32 v19, v12, v13\n\t"
        "v_cndmask_b32 v16, v17, v16, s[44:45]\n\t"
        "v_cndmask_b32 v17, 0, v17, s[44:45]\n\t"
        "v_cvt_pk_bf16_f32 v162, v18, v16\n\t"
        "v_cvt_pk_bf16_f32 v163, v17, v19\n\t"
        "s_waitcnt lgkmcnt(1)\n\t"
        "v_pk_max_u16 v16, v0, v2\n\t"
        "v_pk_min_u16 v16, v16, v3\n\t"
        "v_cmp_ne_u32 vcc, v16, v0\n\t"
        "v_sub_u32 v16, v15, v5\n\t"
        "v_mad_u32_u24 v17, v15, 6, v4\n\t"
        "v_mad_u32_u24 v17, v14, s62, v17\n\t"
        "s_and_b64 vcc, vcc, s[66:67]\n\t"
        "s_or_b64 s[56:57], s[56:57], vcc\n\t"
        "v_bfe_u32 v18, v16, 2, 1\n\t"
        "v_or_b32 v18, v18, v17\n\t"
        "v_add_u32 v16, 1, v16\n\t"
        "v_bfe_u32 v16, v16, 2, 1\n\t"
        "v_add3_u32 v16, v17, v16, 6\n\t"
        "v_cndmask_b32 v16, v18, v16, s[44:45]\n\t"
        "v_cndmask_b32 v17, 0, v63, s[64:65]\n\t"
        "v_add_u32 v19, v18, v17\n\t"
        "v_add_u32 v20, v16, v17\n\t"
        "v_cndmask_b32 v16, v19, v16, s[44:45]\n\t"
        "v_lshl_or_b32 v160, v16, 16, v18\n\t"
        "v_lshl_or_b32 v161, v20, 16, v19\n\t"
        "v_lshlrev_b32_sdwa v157, 5, v160 dst_sel:DWORD dst_unused:UNUSED_PAD src0_sel:DWORD src1_sel:WORD_0\n\t"
        "v_lshlrev_b32_sdwa v158, 5, v160 dst_sel:DWORD dst_unused:UNUSED_PAD src0_sel:DWORD src1_sel:WORD_1\n\t"
        "v_or_b32 v157, v184, v157\n\t"
        "v_or_b32 v158, v184, v158\n\t"
        "v_xor_b32 v159, 32, v157\n\t"
        "v_xor_b32 v152, 32, v158\n\t"
        "ds_read_b64_tr_b16 v[60:61], v157 offset:0\n\t"
        "ds_read_b64_tr_b16 v[62:63], v158 offset:0\n\t"
        "ds_read_b64_tr_b16 v[64:65], v159 offset:0\n\t"
        "ds_read_b64_tr_b16 v[66:67], v152 offset:0\n\t"
        "ds_read_b64_tr_b16 v[68:69], v157 offset:64\n\t"
        "ds_read_b64_tr_b16 v[70:71], v158 offset:64\n\t"
        "ds_read_b64_tr_b16 v[72:73], v159 offset:64\n\t"
        "ds_read_b64_tr_b16 v[74:75], v152 offset:64\n\t"
        "ds_read_b64_tr_b16 v[76:77], v157 offset:128\n\t"
        "ds_read_b64_tr_b16 v[78:79], v158 offset:128\n\t"
        "ds_read_b64_tr_b16 v[80:81], v159 offset:128\n\t"
        "ds_read_b64_tr_b16 v[82:83], v152 offset:128\n\t"
        "v_lshlrev_b32_sdwa v157, 5, v161 dst_sel:DWORD dst_unused:UNUSED_PAD src0_sel:DWORD src1_sel:WORD_0\n\t"
        "v_lshlrev_b32_sdwa v158, 5, v161 dst_sel:DWORD dst_unused:UNUSED_PAD src0_sel:DWORD src1_sel:WORD_1\n\t"
        "v_or_b32 v157, v184, v157\n\t"
        "v_or_b32 v158, v184, v158\n\t"
        "v_xor_b32 v159, 32, v157\n\t"
        "v_xor_b32 v152, 32, v158\n\t"
        "ds_read_b64_tr_b16 v[24:25], v157 offset:0\n\t"
        "ds_read_b64_tr_b16 v[26:27], v158 offset:0\n\t"
        "ds_read_b64_tr_b16 v[28:29], v159 offset:0\n\t"
        "ds_read_b64_tr_b16 v[30:31], v152 offset:0\n\t"
        "ds_read_b64_tr_b16 v[32:33], v157 offset:64\n\t"
        "ds_read_b64_tr_b16 v[34:35], v158 offset:64\n\t"
        "ds_read_b64_tr_b16 v[36:37], v159 offset:64\n\t"
        "ds_read_b64_tr_b16 v[38:39], v152 offset:64\n\t"
        "ds_read_b64_tr_b16 v[40:41], v157 offset:128\n\t"
        "ds_read_b64_tr_b16 v[42:43], v158 offset:128\n\t"
        "ds_read_b64_tr_b16 v[44:45], v159 offset:128\n\t"
        "ds_read_b64_tr_b16 v[46:47], v152 offset:128\n\t"
        "s_cmp_lg_u64 s[56:57], 0\n\t"
        "s_cselect_b32 s8, 1, 0\n\t"
        "v_mov_b32 v157, s8\n\t"
        "v_perm_b32 v164, v162, v162, v168\n\t"
        "v_perm_b32 v165, v162, v162, v169\n\t"
        "v_perm_b32 v166, v162, v162, v170\n\t"
        "v_perm_b32 v167, v162, v162, v171\n\t"
        "s_waitcnt lgkmcnt(12)\n\t"
        "s_nop 0\n\t"
        "v_mfma_f32_16x16x32_bf16 v[0:3], v[60:63], v[164:167], 0\n\t"
        "v_mfma_f32_16x16x32_bf16 v[4:7], v[64:67], v[164:167], 0\n\t"
        "v_mfma_f32_16x16x32_bf16 v[8:11], v[68:71], v[164:167], 0\n\t"
        "v_mfma_f32_16x16x32_bf16 v[12:15], v[72:75], v[164:167], 0\n\t"
        "v_mfma_f32_16x16x32_bf16 v[16:19], v[76:79], v[164:167], 0\n\t"
        "v_mfma_f32_16x16x32_bf16 v[20:23], v[80:83], v[164:167], 0\n\t"
        "v_perm_b32 v164, v163, v163, v168\n\t"
        "v_perm_b32 v165, v163, v163, v169\n\t"
        "v_perm_b32 v166, v163, v163, v170\n\t"
        "v_perm_b32 v167, v163, v163, v171\n\t"
        "s_waitcnt lgkmcnt(3)\n\t"
        "s_nop 0\n\t"
        "v_mfma_f32_16x16x32_bf16 v[0:3], v[24:27], v[164:167], v[0:3]\n\t"
        "v_mfma_f32_16x16x32_bf16 v[4:7], v[28:31], v[164:167], v[4:7]\n\t"
        "v_mfma_f32_16x16x32_bf16 v[8:11], v[32:35], v[164:167], v[8:11]\n\t"
        "v_mfma_f32_16x16x32_bf16 v[12:15], v[36:39], v[164:167], v[12:15]\n\t"
        "s_waitcnt lgkmcnt(0)\n\t"
        "v_mfma_f32_16x16x32_bf16 v[16:19], v[40:43], v[164:167], v[16:19]\n\t"
        "v_mfma_f32_16x16x32_bf16 v[20:23], v[44:47], v[164:167], v[20:23]\n\t"
        "ds_write_b32 v153, v157 offset:16\n\t"
        "s_nop 0\n\t"
        "v_cvt_pk_bf16_f32 v0, v0, v1\n\t"
        "v_cvt_pk_bf16_f32 v1, v2, v3\n\t"
        "v_cvt_pk_bf16_f32 v4, v4, v5\n\t"
        "v_cvt_pk_bf16_f32 v5, v6, v7\n\t"
        "v_cvt_pk_bf16_f32 v8, v8, v9\n\t"
        "v_cvt_pk_bf16_f32 v9, v10, v11\n\t"
        "v_cvt_pk_bf16_f32 v12, v12, v13\n\t"
        "v_cvt_pk_bf16_f32 v13, v14, v15\n\t"
        "v_cvt_pk_bf16_f32 v16, v16, v17\n\t"
        "v_cvt_pk_bf16_f32 v17, v18, v19\n\t"
        "v_cvt_pk_bf16_f32 v20, v20, v21\n\t"
        "v_cvt_pk_bf16_f32 v21, v22, v23\n\t"
        "ds_write_b64 v186, v[0:1] offset:0\n\t"
        "ds_write_b64 v154, v[4:5] offset:0\n\t"
        "ds_write_b64 v186, v[8:9] offset:64\n\t"
        "ds_write_b64 v154, v[12:13] offset:64\n\t"
        "ds_write_b64 v186, v[16:17] offset:128\n\t"
        "ds_write_b64 v154, v[20:21] offset:128\n\t"
        "52:\n\t"
        "s_branch .Lx55%=\n\t"
        ".Lx60%=:\n\t"
        "v_mov_b32 v151, v185\n\t"
        "v_mov_b32 v183, v186\n\t"
        "s_min_u32 s8, s3, 64\n\t"
        "s_waitcnt lgkmcnt(0)\n\t"
        "30:\n\t"
        "ds_read_b128 v[160:163], v185\n\t"
        "s_waitcnt lgkmcnt(0)\n\t"
        "v_lshlrev_b32_sdwa v157, 5, v160 dst_sel:DWORD dst_unused:UNUSED_PAD src0_sel:DWORD src1_sel:WORD_0\n\t"
        "v_lshlrev_b32_sdwa v158, 5, v160 dst_sel:DWORD dst_unused:UNUSED_PAD src0_sel:DWORD src1_sel:WORD_1\n\t"
        "v_or_b32 v157, v184, v157\n\t"
        "v_or_b32 v158, v184, v158\n\t"
        "v_xor_b32 v159, 32, v157\n\t"
        "v_xor_b32 v152, 32, v158\n\t"
        "ds_read_b64_tr_b16 v[60:61], v157 offset:0\n\t"
        "ds_read_b64_tr_b16 v[62:63], v158 offset:0\n\t"
        "ds_read_b64_tr_b16 v[64:65], v159 offset:0\n\t"
        "ds_read_b64_tr_b16 v[66:67], v152 offset:0\n\t"
        "ds_read_b64_tr_b16 v[68:69], v157 offset:64\n\t"
        "ds_read_b64_tr_b16 v[70:71], v158 offset:64\n\t"
        "ds_read_b64_tr_b16 v[72:73], v159 offset:64\n\t"
        "ds_read_b64_tr_b16 v[74:75], v152 offset:64\n\t"
        "ds_read_b64_tr_b16 v[76:77], v157 offset:128\n\t"
        "ds_read_b64_tr_b16 v[78:79], v158 offset:128\n\t"
        "ds_read_b64_tr_b16 v[80:81], v159 offset:128\n\t"
        "ds_read_b64_tr_b16 v[82:83], v152 offset:128\n\t"
        "v_perm_b32 v164, v162, v162, v168\n\t"
        "v_perm_b32 v165, v162, v162, v169\n\t"
        "v_perm_b32 v166, v162, v162, v170\n\t"
        "v_perm_b32 v167, v162, v162, v171\n\t"
        "v_cmp_ne_u32 vcc, 0, v163\n\t"
        "s_waitcnt lgkmcnt(10)\n\t"
        "v_mfma_f32_16x16x32_bf16 v[48:51], v[60:63], v[164:167], 0\n\t"
        "s_waitcnt lgkmcnt(8)\n\t"
        "v_mfma_f32_16x16x32_bf16 v[52:55], v[64:67], v[164:167], 0\n\t"
        "s_waitcnt lgkmcnt(6)\n\t"
        "v_mfma_f32_16x16x32_bf16 v[56:59], v[68:71], v[164:167], 0\n\t"
        "s_waitcnt lgkmcnt(4)\n\t"
        "v_mfma_f32_16x16x32_bf16 v[116:119], v[72:75], v[164:167], 0\n\t"
        "s_waitcnt lgkmcnt(2)\n\t"
        "v_mfma_f32_16x16x32_bf16 v[120:123], v[76:79], v[164:167], 0\n\t"
        "s_waitcnt lgkmcnt(0)\n\t"
        "v_mfma_f32_16x16x32_bf16 v[124:127], v[80:83], v[164:167], 0\n\t"
        "s_cbranch_vccz 31f\n\t"
        "v_lshlrev_b32_sdwa v157, 5, v161 dst_sel:DWORD dst_unused:UNUSED_PAD src0_sel:DWORD src1_sel:WORD_0\n\t"
        "v_lshlrev_b32_sdwa v158, 5, v161 dst_sel:DWORD dst_unused:UNUSED_PAD src0_sel:DWORD src1_sel:WORD_1\n\t"
        "v_or_b32 v157, v184, v157\n\t"
        "v_or_b32 v158, v184, v158\n\t"
        "v_xor_b32 v159, 32, v157\n\t"
        "v_xor_b32 v152, 32, v158\n\t"
        "ds_read_b64_tr_b16 v[60:61], v157 offset:0\n\t"
        "ds_read_b64_tr_b16 v[62:63], v158 offset:0\n\t"
        "ds_read_b64_tr_b16 v[64:65], v159 offset:0\n\t"
        "ds_read_b64_tr_b16 v[66:67], v152 offset:0\n\t"
        "ds_read_b64_tr_b16 v[68:69], v157 offset:64\n\t"
        "ds_read_b64_tr_b16 v[70:71], v158 offset:64\n\t"
        "ds_read_b64_tr_b16 v[72:73], v159 offset:64\n\t"
        "ds_read_b64_tr_b16 v[74:75], v152 offset:64\n\t"
        "ds_read_b64_tr_b16 v[76:77], v157 offset:128\n\t"
        "ds_read_b64_tr_b16 v[78:79], v158 offset:128\n\t"
        "ds_read_b64_tr_b16 v[80:81], v159 offset:128\n\t"
        "ds_read_b64_tr_b16 v[82:83], v152 offset:128\n\t"
        "v_perm_b32 v164, v163, v163, v168\n\t"
        "v_perm_b32 v165, v163, v163, v169\n\t"
        "v_perm_b32 v166, v163, v163, v170\n\t"
        "v_perm_b32 v167, v163, v163, v171\n\t"
        "s_waitcnt lgkmcnt(10)\n\t"
        "s_nop 0\n\t"
        "v_mfma_f32_16x16x32_bf16 v[48:51], v[60:63], v[164:167], v[48:51]\n\t"
        "s_waitcnt lgkmcnt(8)\n\t"
        "v_mfma_f32_16x16x32_bf16 v[52:55], v[64:67], v[164:167], v[52:55]\n\t"
        "s_waitcnt lgkmcnt(6)\n\t"
        "v_mfma_f32_16x16x32_bf16 v[56:59], v[68:71], v[164:167], v[56:59]\n\t"
        "s_waitcnt lgkmcnt(4)\n\t"
        "v_mfma_f32_16x16x32_bf16 v[116:119], v[72:75], v[164:167], v[116:119]\n\t"
        "s_waitcnt lgkmcnt(2)\n\t"
        "v_mfma_f32_16x16x32_bf16 v[120:123], v[76:79], v[164:167], v[120:123]\n\t"
        "s_waitcnt lgkmcnt(0)\n\t"
        "v_mfma_f32_16x16x32_bf16 v[124:127], v[80:83], v[164:167], v[124:127]\n\t"
        "31:\n\t"
        "v_cvt_pk_bf16_f32 v48, v48, v49\n\t"
        "v_cvt_pk_bf16_f32 v49, v50, v51\n\t"
        "v_cvt_pk_bf16_f32 v52, v52, v53\n\t"
        "v_cvt_pk_bf16_f32 v53, v54, v55\n\t"
        "v_cvt_pk_bf16_f32 v56, v56, v57\n\t"
        "v_cvt_pk_bf16_f32 v57, v58, v59\n\t"
        "v_cvt_pk_bf16_f32 v116, v116, v117\n\t"
        "v_cvt_pk_bf16_f32 v117, v118, v119\n\t"
        "v_cvt_pk_bf16_f32 v120, v120, v121\n\t"
        "v_cvt_pk_bf16_f32 v121, v122, v123\n\t"
        "v_cvt_pk_bf16_f32 v124, v124, v125\n\t"
        "v_cvt_pk_bf16_f32 v125, v126, v127\n\t"
        "ds_write_b64 v186, v[48:49] offset:0\n\t"
        "ds_write_b64 v154, v[52:53] offset:0\n\t"
        "ds_write_b64 v186, v[56:57] offset:64\n\t"
        "ds_write_b64 v154, v[116:117] offset:64\n\t"
        "ds_write_b64 v186, v[120:121] offset:128\n\t"
        "ds_write_b64 v154, v[124:125] offset:128\n\t"
        "v_add_u32 v185, 256, v185\n\t"
        "v_add_u32 v186, 12288, v186\n\t"
        "v_xor_b32 v154, 32, v186\n\t"
        "s_sub_i32 s8, s8, 16\n\t"
        "s_cmp_gt_i32 s8, 0\n\t"
        "s_cbranch_scc1 30b\n\t"
        "v_mov_b32 v185, v151\n\t"
        "v_mov_b32 v186, v183\n\t"
        "v_xor_b32 v154, 32, v186\n\t"
        "s_waitcnt lgkmcnt(0)\n\t"
        "s_barrier\n\t"
        "s_branch .Lx61%=\n\t"
        ".Lx68%=:\n\t"
        "v_mov_b32 v151, v185\n\t"
        "v_mov_b32 v183, v186\n\t"
        "s_min_u32 s8, s3, 64\n\t"
        "s_waitcnt lgkmcnt(0)\n\t"
        "30:\n\t"
        "ds_read_b128 v[160:163], v185\n\t"
        "s_waitcnt lgkmcnt(0)\n\t"
        "v_lshlrev_b32_sdwa v157, 5, v160 dst_sel:DWORD dst_unused:UNUSED_PAD src0_sel:DWORD src1_sel:WORD_0\n\t"
        "v_lshlrev_b32_sdwa v158, 5, v160 dst_sel:DWORD dst_unused:UNUSED_PAD src0_sel:DWORD src1_sel:WORD_1\n\t"
        "v_or_b32 v157, v184, v157\n\t"
        "v_or_b32 v158, v184, v158\n\t"
        "v_xor_b32 v159, 32, v157\n\t"
        "v_xor_b32 v152, 32, v158\n\t"
        "ds_read_b64_tr_b16 v[60:61], v157 offset:0\n\t"
        "ds_read_b64_tr_b16 v[62:63], v158 offset:0\n\t"
        "ds_read_b64_tr_b16 v[64:65], v159 offset:0\n\t"
        "ds_read_b64_tr_b16 v[66:67], v152 offset:0\n\t"
        "ds_read_b64_tr_b16 v[68:69], v157 offset:64\n\t"
        "ds_read_b64_tr_b16 v[70:71], v158 offset:64\n\t"
        "ds_read_b64_tr_b16 v[72:73], v159 offset:64\n\t"
        "ds_read_b64_tr_b16 v[74:75], v152 offset:64\n\t"
        "ds_read_b64_tr_b16 v[76:77], v157 offset:128\n\t"
        "ds_read_b64_tr_b16 v[78:79], v158 offset:128\n\t"
        "ds_read_b64_tr_b16 v[80:81], v159 offset:128\n\t"
        "ds_read_b64_tr_b16 v[82:83], v152 offset:128\n\t"
        "v_perm_b32 v164, v162, v162, v168\n\t"
        "v_perm_b32 v165, v162, v162, v169\n\t"
        "v_perm_b32 v166, v162, v162, v170\n\t"
        "v_perm_b32 v167, v162, v162, v171\n\t"
        "v_cmp_ne_u32 vcc, 0, v163\n\t"
        "s_waitcnt lgkmcnt(10)\n\t"
        "v_mfma_f32_16x16x32_bf16 v[48:51], v[60:63], v[164:167], 0\n\t"
        "s_waitcnt lgkmcnt(8)\n\t"
        "v_mfma_f32_16x16x32_bf16 v[52:55], v[64:67], v[164:167], 0\n\t"
        "s_waitcnt lgkmcnt(6)\n\t"
        "v_mfma_f32_16x16x32_bf16 v[56:59], v[68:71], v[164:167], 0\n\t"
        "s_waitcnt lgkmcnt(4)\n\t"
        "v_mfma_f32_16x16x32_bf16 v[116:119], v[72:75], v[164:167], 0\n\t"
        "s_waitcnt lgkmcnt(2)\n\t"
        "v_mfma_f32_16x16x32_bf16 v[120:123], v[76:79], v[164:167], 0\n\t"
        "s_waitcnt lgkmcnt(0)\n\t"
        "v_mfma_f32_16x16x32_bf16 v[124:127], v[80:83], v[164:167], 0\n\t"
        "s_cbranch_vccz 31f\n\t"
        "v_lshlrev_b32_sdwa v157, 5, v161 dst_sel:DWORD dst_unused:UNUSED_PAD src0_sel:DWORD src1_sel:WORD_0\n\t"
        "v_lshlrev_b32_sdwa v158, 5, v161 dst_sel:DWORD dst_unused:UNUSED_PAD src0_sel:DWORD src1_sel:WORD_1\n\t"
        "v_or_b32 v157, v184, v157\n\t"
        "v_or_b32 v158, v184, v158\n\t"
        "v_xor_b32 v159, 32, v157\n\t"
        "v_xor_b32 v152, 32, v158\n\t"
        "ds_read_b64_tr_b16 v[60:61], v157 offset:0\n\t"
        "ds_read_b64_tr_b16 v[62:63], v158 offset:0\n\t"
        "ds_read_b64_tr_b16 v[64:65], v159 offset:0\n\t"
        "ds_read_b64_tr_b16 v[66:67], v152 offset:0\n\t"
        "ds_read_b64_tr_b16 v[68:69], v157 offset:64\n\t"
        "ds_read_b64_tr_b16 v[70:71], v158 offset:64\n\t"
        "ds_read_b64_tr_b16 v[72:73], v159 offset:64\n\t"
        "ds_read_b64_tr_b16 v[74:75], v152 offset:64\n\t"
        "ds_read_b64_tr_b16 v[76:77], v157 offset:128\n\t"
        "ds_read_b64_tr_b16 v[78:79], v158 offset:128\n\t"
        "ds_read_b64_tr_b16 v[80:81], v159 offset:128\n\t"
        "ds_read_b64_tr_b16 v[82:83], v152 offset:128\n\t"
        "v_perm_b32 v164, v163, v163, v168\n\t"
        "v_perm_b32 v165, v163, v163, v169\n\t"
        "v_perm_b32 v166, v163, v163, v170\n\t"
        "v_perm_b32 v167, v163, v163, v171\n\t"
        "s_waitcnt lgkmcnt(10)\n\t"
        "s_nop 0\n\t"
        "v_mfma_f32_16x16x32_bf16 v[48:51], v[60:63], v[164:167], v[48:51]\n\t"
        "s_waitcnt lgkmcnt(8)\n\t"
        "v_mfma_f32_16x16x32_bf16 v[52:55], v[64:67], v[164:167], v[52:55]\n\t"
        "s_waitcnt lgkmcnt(6)\n\t"
        "v_mfma_f32_16x16x32_bf16 v[56:59], v[68:71], v[164:167], v[56:59]\n\t"
        "s_waitcnt lgkmcnt(4)\n\t"
        "v_mfma_f32_16x16x32_bf16 v[116:119], v[72:75], v[164:167], v[116:119]\n\t"
        "s_waitcnt lgkmcnt(2)\n\t"
        "v_mfma_f32_16x16x32_bf16 v[120:123], v[76:79], v[164:167], v[120:123]\n\t"
        "s_waitcnt lgkmcnt(0)\n\t"
        "v_mfma_f32_16x16x32_bf16 v[124:127], v[80:83], v[164:167], v[124:127]\n\t"
        "31:\n\t"
        "v_cvt_pk_bf16_f32 v48, v48, v49\n\t"
        "v_cvt_pk_bf16_f32 v49, v50, v51\n\t"
        "v_cvt_pk_bf16_f32 v52, v52, v53\n\t"
        "v_cvt_pk_bf16_f32 v53, v54, v55\n\t"
        "v_cvt_pk_bf16_f32 v56, v56, v57\n\t"
        "v_cvt_pk_bf16_f32 v57, v58, v59\n\t"
        "v_cvt_pk_bf16_f32 v116, v116, v117\n\t"
        "v_cvt_pk_bf16_f32 v117, v118, v119\n\t"
        "v_cvt_pk_bf16_f32 v120, v120, v121\n\t"
        "v_cvt_pk_bf16_f32 v121, v122, v123\n\t"
        "v_cvt_pk_bf16_f32 v124, v124, v125\n\t"
        "v_cvt_pk_bf16_f32 v125, v126, v127\n\t"
        "ds_write_b64 v186, v[48:49] offset:0\n\t"
        "ds_write_b64 v154, v[52:53] offset:0\n\t"
        "ds_write_b64 v186, v[56:57] offset:64\n\t"
        "ds_write_b64 v154, v[116:117] offset:64\n\t"
        "ds_write_b64 v186, v[120:121] offset:128\n\t"
        "ds_write_b64 v154, v[124:125] offset:128\n\t"
        "v_add_u32 v185, 256, v185\n\t"
        "v_add_u32 v186, 12288, v186\n\t"
        "v_xor_b32 v154, 32, v186\n\t"
        "s_sub_i32 s8, s8, 16\n\t"
        "s_cmp_gt_i32 s8, 0\n\t"
        "s_cbranch_scc1 30b\n\t"
        "v_mov_b32 v185, v151\n\t"
        "v_mov_b32 v186, v183\n\t"
        "v_xor_b32 v154, 32, v186\n\t"
        "s_waitcnt lgkmcnt(0)\n\t"
        "s_barrier\n\t"
        "s_branch .Lx69%=\n\t"
        ".LwX%=:\n\t"
        "s_branch .LrX%=\n\t"
        ".Lx70%=:\n\t"
        "v_add_u32 v185, 1024, v185\n\t"
        "v_add_u32 v186, 49152, v186\n\t"
        "v_xor_b32 v154, 32, v186\n\t"
        "v_bfe_u32 v64, v185, 4, 4\n\t"
        "v_add_u32 v60, 65, v64\n\t"
        "s_sub_u32 s8, s37, 12288\n\t"
        "v_mov_b32 v63, s62\n\t"
        "v_lshlrev_b32 v65, 3, v64\n\t"
        "v_mul_u32_u24 v64, 192, v64\n\t"
        "v_and_or_b32 v64, v65, 48, v64\n\t"
        "v_add_u32 v61, s8, v64\n\t"
        "v_subrev_u32 v186, 61440, v186\n\t"
        "v_xor_b32 v154, 32, v186\n\t"
        "v_min_u32 v16, s61, v60\n\t"
        "v_lshl_add_u32 v16, v16, 3, s60\n\t"
        "ds_read_b64 v[0:1], v16\n\t"
        "v_cmp_ge_u32 s[66:67], s61, v60\n\t"
        "s_mov_b32 s46, 0x07060302\n\t"
        "s_waitcnt lgkmcnt(0)\n\t"
        "v_and_b32 v16, 0xfffff800, v1\n\t"
        "ds_read_b128 v[2:5], v16 offset:1536\n\t"
        "v_lshlrev_b32 v10, 16, v0\n\t"
        "v_and_b32 v11, 0xffff0000, v0\n\t"
        "v_med3_f32 v10, v10, 0, s63\n\t"
        "v_med3_f32 v11, v11, 0, s63\n\t"
        "s_mov_b64 exec, s[66:67]\n\t"
        "ds_write_b32 v1, v61\n\t"
        "s_mov_b64 exec, -1\n\t"
        "v_perm_b32 v0, v11, v10, s46\n\t"
        "v_fract_f32 v12, v10\n\t"
        "v_fract_f32 v13, v11\n\t"
        "v_cvt_u32_f32 v14, v10\n\t"
        "v_cvt_u32_f32 v15, v11\n\t"
        "v_cmp_neq_f32 s[64:65], 0, v12\n\t"
        "v_cmp_neq_f32 s[44:45], 0, v13\n\t"
        "v_sub_f32 v16, 1.0, v12\n\t"
        "v_sub_f32 v17, 1.0, v13\n\t"
        "v_cvt_pk_bf16_f32 v17, v16, v17\n\t"
        "v_lshlrev_b32 v16, 16, v17\n\t"
        "v_and_b32 v17, 0xffff0000, v17\n\t"
        "v_mul_f32 v18, v16, v17\n\t"
        "v_mul_f32 v16, v16, v13\n\t"
        "v_mul_f32 v17, v12, v17\n\t"
        "v_mul_f32 v19, v12, v13\n\t"
        "v_cndmask_b32 v16, v17, v16, s[44:45]\n\t"
        "v_cndmask_b32 v17, 0, v17, s[44:45]\n\t"
        "v_cvt_pk_bf16_f32 v162, v18, v16\n\t"
        "v_cvt_pk_bf16_f32 v163, v17, v19\n\t"
        "s_waitcnt lgkmcnt(1)\n\t"
        "v_pk_max_u16 v16, v0, v2\n\t"
        "v_pk_min_u16 v16, v16, v3\n\t"
        "v_cmp_ne_u32 vcc, v16, v0\n\t"
        "v_sub_u32 v16, v15, v5\n\t"
        "v_mad_u32_u24 v17, v15, 6, v4\n\t"
        "v_mad_u32_u24 v17, v14, s62, v17\n\t"
        "s_and_b64 vcc, vcc, s[66:67]\n\t"
        "s_or_b64 s[56:57], s[56:57], vcc\n\t"
        "v_bfe_u32 v18, v16, 2, 1\n\t"
        "v_or_b32 v18, v18, v17\n\t"
        "v_add_u32 v16, 1, v16\n\t"
        "v_bfe_u32 v16, v16, 2, 1\n\t"
        "v_add3_u32 v16, v17, v16, 6\n\t"
        "v_cndmask_b32 v16, v18, v16, s[44:45]\n\t"
        "v_cndmask_b32 v17, 0, v63, s[64:65]\n\t"
        "v_add_u32 v19, v18, v17\n\t"
        "v_add_u32 v20, v16, v17\n\t"
        "v_cndmask_b32 v16, v19, v16, s[44:45]\n\t"
        "v_lshl_or_b32 v160, v16, 16, v18\n\t"
        "v_lshl_or_b32 v161, v20, 16, v19\n\t"
        "v_lshlrev_b32_sdwa v157, 5, v160 dst_sel:DWORD dst_unused:UNUSED_PAD src0_sel:DWORD src1_sel:WORD_0\n\t"
        "v_lshlrev_b32_sdwa v158, 5, v160 dst_sel:DWORD dst_unused:UNUSED_PAD src0_sel:DWORD src1_sel:WORD_1\n\t"
        "v_or_b32 v157, v184, v157\n\t"
        "v_or_b32 v158, v184, v158\n\t"
        "v_xor_b32 v159, 32, v157\n\t"
        "v_xor_b32 v152, 32, v158\n\t"
        "ds_read_b64_tr_b16 v[60:61], v157 offset:0\n\t"
        "ds_read_b64_tr_b16 v[62:63], v158 offset:0\n\t"
        "ds_read_b64_tr_b16 v[64:65], v159 offset:0\n\t"
        "ds_read_b64_tr_b16 v[66:67], v152 offset:0\n\t"
        "ds_read_b64_tr_b16 v[68:69], v157 offset:64\n\t"
        "ds_read_b64_tr_b16 v[70:71], v158 offset:64\n\t"
        "ds_read_b64_tr_b16 v[72:73], v159 offset:64\n\t"
        "ds_read_b64_tr_b16 v[74:75], v152 offset:64\n\t"
        "ds_read_b64_tr_b16 v[76:77], v157 offset:128\n\t"
        "ds_read_b64_tr_b16 v[78:79], v158 offset:128\n\t"
        "ds_read_b64_tr_b16 v[80:81], v159 offset:128\n\t"
        "ds_read_b64_tr_b16 v[82:83], v152 offset:128\n\t"
        "v_lshlrev_b32_sdwa v157, 5, v161 dst_sel:DWORD dst_unused:UNUSED_PAD src0_sel:DWORD src1_sel:WORD_0\n\t"
        "v_lshlrev_b32_sdwa v158, 5, v161 dst_sel:DWORD dst_unused:UNUSED_PAD src0_sel:DWORD src1_sel:WORD_1\n\t"
        "v_or_b32 v157, v184, v157\n\t"
        "v_or_b32 v158, v184, v158\n\t"
        "v_xor_b32 v159, 32, v157\n\t"
        "v_xor_b32 v152, 32, v158\n\t"
        "ds_read_b64_tr_b16 v[24:25], v157 offset:0\n\t"
        "ds_read_b64_tr_b16 v[26:27], v158 offset:0\n\t"
        "ds_read_b64_tr_b16 v[28:29], v159 offset:0\n\t"
        "ds_read_b64_tr_b16 v[30:31], v152 offset:0\n\t"
        "ds_read_b64_tr_b16 v[32:33], v157 offset:64\n\t"
        "ds_read_b64_tr_b16 v[34:35], v158 offset:64\n\t"
        "ds_read_b64_tr_b16 v[36:37], v159 offset:64\n\t"
        "ds_read_b64_tr_b16 v[38:39], v152 offset:64\n\t"
        "ds_read_b64_tr_b16 v[40:41], v157 offset:128\n\t"
        "ds_read_b64_tr_b16 v[42:43], v158 offset:128\n\t"
        "ds_read_b64_tr_b16 v[44:45], v159 offset:128\n\t"
        "ds_read_b64_tr_b16 v[46:47], v152 offset:128\n\t"
        "s_cmp_lg_u64 s[56:57], 0\n\t"
        "s_cselect_b32 s8, 1, 0\n\t"
        "v_mov_b32 v157, s8\n\t"
        "v_perm_b32 v164, v162, v162, v168\n\t"
        "v_perm_b32 v165, v162, v162, v169\n\t"
        "v_perm_b32 v166, v162, v162, v170\n\t"
        "v_perm_b32 v167, v162, v162, v171\n\t"
        "s_waitcnt lgkmcnt(12)\n\t"
        "s_nop 0\n\t"
        "v_mfma_f32_16x16x32_bf16 v[0:3], v[60:63], v[164:167], 0\n\t"
        "v_mfma_f32_16x16x32_bf16 v[4:7], v[64:67], v[164:167], 0\n\t"
        "v_mfma_f32_16x16x32_bf16 v[8:11], v[68:71], v[164:167], 0\n\t"
        "v_mfma_f32_16x16x32_bf16 v[12:15], v[72:75], v[164:167], 0\n\t"
        "v_mfma_f32_16x16x32_bf16 v[16:19], v[76:79], v[164:167], 0\n\t"
        "v_mfma_f32_16x16x32_bf16 v[20:23], v[80:83], v[164:167], 0\n\t"
        "v_perm_b32 v164, v163, v163, v168\n\t"
        "v_perm_b32 v165, v163, v163, v169\n\t"
        "v_perm_b32 v166, v163, v163, v170\n\t"
        "v_perm_b32 v167, v163, v163, v171\n\t"
        "s_waitcnt lgkmcnt(3)\n\t"
        "s_nop 0\n\t"
        "v_mfma_f32_16x16x32_bf16 v[0:3], v[24:27], v[164:167], v[0:3]\n\t"
        "v_mfma_f32_16x16x32_bf16 v[4:7], v[28:31], v[164:167], v[4:7]\n\t"
        "v_mfma_f32_16x16x32_bf16 v[8:11], v[32:35], v[164:167], v[8:11]\n\t"
        "v_mfma_f32_16x16x32_bf16 v[12:15], v[36:39], v[164:167], v[12:15]\n\t"
        "s_waitcnt lgkmcnt(0)\n\t"
        "v_mfma_f32_16x16x32_bf16 v[16:19], v[40:43], v[164:167], v[16:19]\n\t"
        "v_mfma_f32_16x16x32_bf16 v[20:23], v[44:47], v[164:167], v[20:23]\n\t"
        "ds_write_b32 v153, v157 offset:16\n\t"
        "s_nop 0\n\t"
        "v_cvt_pk_bf16_f32 v0, v0, v1\n\t"
        "v_cvt_pk_bf16_f32 v1, v2, v3\n\t"
        "v_cvt_pk_bf16_f32 v4, v4, v5\n\t"
        "v_cvt_pk_bf16_f32 v5, v6, v7\n\t"
        "v_cvt_pk_bf16_f32 v8, v8, v9\n\t"
        "v_cvt_pk_bf16_f32 v9, v10, v11\n\t"
        "v_cvt_pk_bf16_f32 v12, v12, v13\n\t"
        "v_cvt_pk_bf16_f32 v13, v14, v15\n\t"
        "v_cvt_pk_bf16_f32 v16, v16, v17\n\t"
        "v_cvt_pk_bf16_f32 v17, v18, v19\n\t"
        "v_cvt_pk_bf16_f32 v20, v20, v21\n\t"
        "v_cvt_pk_bf16_f32 v21, v22, v23\n\t"
        "ds_write_b64 v186, v[0:1] offset:0\n\t"
        "ds_write_b64 v154, v[4:5] offset:0\n\t"
        "ds_write_b64 v186, v[8:9] offset:64\n\t"
        "ds_write_b64 v154, v[12:13] offset:64\n\t"
        "ds_write_b64 v186, v[16:17] offset:128\n\t"
        "ds_write_b64 v154, v[20:21] offset:128\n\t"
        "s_branch .Lx71%=\n\t"
        ".Lx74%=:\n\t"
        "v_mov_b32 v151, v185\n\t"
        "v_mov_b32 v183, v186\n\t"
        "s_min_u32 s8, s3, 64\n\t"
        "s_waitcnt lgkmcnt(0)\n\t"
        "30:\n\t"
        "ds_read_b128 v[160:163], v185\n\t"
        "s_waitcnt lgkmcnt(0)\n\t"
        "v_lshlrev_b32_sdwa v157, 5, v160 dst_sel:DWORD dst_unused:UNUSED_PAD src0_sel:DWORD src1_sel:WORD_0\n\t"
        "v_lshlrev_b32_sdwa v158, 5, v160 dst_sel:DWORD dst_unused:UNUSED_PAD src0_sel:DWORD src1_sel:WORD_1\n\t"
        "v_or_b32 v157, v184, v157\n\t"
        "v_or_b32 v158, v184, v158\n\t"
        "v_xor_b32 v159, 32, v157\n\t"
        "v_xor_b32 v152, 32, v158\n\t"
        "ds_read_b64_tr_b16 v[60:61], v157 offset:0\n\t"
        "ds_read_b64_tr_b16 v[62:63], v158 offset:0\n\t"
        "ds_read_b64_tr_b16 v[64:65], v159 offset:0\n\t"
        "ds_read_b64_tr_b16 v[66:67], v152 offset:0\n\t"
        "ds_read_b64_tr_b16 v[68:69], v157 offset:64\n\t"
        "ds_read_b64_tr_b16 v[70:71], v158 offset:64\n\t"
        "ds_read_b64_tr_b16 v[72:73], v159 offset:64\n\t"
        "ds_read_b64_tr_b16 v[74:75], v152 offset:64\n\t"
        "ds_read_b64_tr_b16 v[76:77], v157 offset:128\n\t"
        "ds_read_b64_tr_b16 v[78:79], v158 offset:128\n\t"
        "ds_read_b64_tr_b16 v[80:81], v159 offset:128\n\t"
        "ds_read_b64_tr_b16 v[82:83], v152 offset:128\n\t"
        "v_perm_b32 v164, v162, v162, v168\n\t"
        "v_perm_b32 v165, v162, v162, v169\n\t"
        "v_perm_b32 v166, v162, v162, v170\n\t"
        "v_perm_b32 v167, v162, v162, v171\n\t"
        "v_cmp_ne_u32 vcc, 0, v163\n\t"
        "s_waitcnt lgkmcnt(10)\n\t"
        "v_mfma_f32_16x16x32_bf16 v[48:51], v[60:63], v[164:167], 0\n\t"
        "s_waitcnt lgkmcnt(8)\n\t"
        "v_mfma_f32_16x16x32_bf16 v[52:55], v[64:67], v[164:167], 0\n\t"
        "s_waitcnt lgkmcnt(6)\n\t"
        "v_mfma_f32_16x16x32_bf16 v[56:59], v[68:71], v[164:167], 0\n\t"
        "s_waitcnt lgkmcnt(4)\n\t"
        "v_mfma_f32_16x16x32_bf16 v[116:119], v[72:75], v[164:167], 0\n\t"
        "s_waitcnt lgkmcnt(2)\n\t"
        "v_mfma_f32_16x16x32_bf16 v[120:123], v[76:79], v[164:167], 0\n\t"
        "s_waitcnt lgkmcnt(0)\n\t"
        "v_mfma_f32_16x16x32_bf16 v[124:127], v[80:83], v[164:167], 0\n\t"
        "s_cbranch_vccz 31f\n\t"
        "v_lshlrev_b32_sdwa v157, 5, v161 dst_sel:DWORD dst_unused:UNUSED_PAD src0_sel:DWORD src1_sel:WORD_0\n\t"
        "v_lshlrev_b32_sdwa v158, 5, v161 dst_sel:DWORD dst_unused:UNUSED_PAD src0_sel:DWORD src1_sel:WORD_1\n\t"
        "v_or_b32 v157, v184, v157\n\t"
        "v_or_b32 v158, v184, v158\n\t"
        "v_xor_b32 v159, 32, v157\n\t"
        "v_xor_b32 v152, 32, v158\n\t"
        "ds_read_b64_tr_b16 v[60:61], v157 offset:0\n\t"
        "ds_read_b64_tr_b16 v[62:63], v158 offset:0\n\t"
        "ds_read_b64_tr_b16 v[64:65], v159 offset:0\n\t"
        "ds_read_b64_tr_b16 v[66:67], v152 offset:0\n\t"
        "ds_read_b64_tr_b16 v[68:69], v157 offset:64\n\t"
        "ds_read_b64_tr_b16 v[70:71], v158 offset:64\n\t"
        "ds_read_b64_tr_b16 v[72:73], v159 offset:64\n\t"
        "ds_read_b64_tr_b16 v[74:75], v152 offset:64\n\t"
        "ds_read_b64_tr_b16 v[76:77], v157 offset:128\n\t"
        "ds_read_b64_tr_b16 v[78:79], v158 offset:128\n\t"
        "ds_read_b64_tr_b16 v[80:81], v159 offset:128\n\t"
        "ds_read_b64_tr_b16 v[82:83], v152 offset:128\n\t"
        "v_perm_b32 v164, v163, v163, v168\n\t"
        "v_perm_b32 v165, v163, v163, v169\n\t"
        "v_perm_b32 v166, v163, v163, v170\n\t"
        "v_perm_b32 v167, v163, v163, v171\n\t"
        "s_waitcnt lgkmcnt(10)\n\t"
        "s_nop 0\n\t"
        "v_mfma_f32_16x16x32_bf16 v[48:51], v[60:63], v[164:167], v[48:51]\n\t"
        "s_waitcnt lgkmcnt(8)\n\t"
        "v_mfma_f32_16x16x32_bf16 v[52:55], v[64:67], v[164:167], v[52:55]\n\t"
        "s_waitcnt lgkmcnt(6)\n\t"
        "v_mfma_f32_16x16x32_bf16 v[56:59], v[68:71], v[164:167], v[56:59]\n\t"
        "s_waitcnt lgkmcnt(4)\n\t"
        "v_mfma_f32_16x16x32_bf16 v[116:119], v[72:75], v[164:167], v[116:119]\n\t"
        "s_waitcnt lgkmcnt(2)\n\t"
        "v_mfma_f32_16x16x32_bf16 v[120:123], v[76:79], v[164:167], v[120:123]\n\t"
        "s_waitcnt lgkmcnt(0)\n\t"
        "v_mfma_f32_16x16x32_bf16 v[124:127], v[80:83], v[164:167], v[124:127]\n\t"
        "31:\n\t"
        "v_cvt_pk_bf16_f32 v48, v48, v49\n\t"
        "v_cvt_pk_bf16_f32 v49, v50, v51\n\t"
        "v_cvt_pk_bf16_f32 v52, v52, v53\n\t"
        "v_cvt_pk_bf16_f32 v53, v54, v55\n\t"
        "v_cvt_pk_bf16_f32 v56, v56, v57\n\t"
        "v_cvt_pk_bf16_f32 v57, v58, v59\n\t"
        "v_cvt_pk_bf16_f32 v116, v116, v117\n\t"
        "v_cvt_pk_bf16_f32 v117, v118, v119\n\t"
        "v_cvt_pk_bf16_f32 v120, v120, v121\n\t"
        "v_cvt_pk_bf16_f32 v121, v122, v123\n\t"
        "v_cvt_pk_bf16_f32 v124, v124, v125\n\t"
        "v_cvt_pk_bf16_f32 v125, v126, v127\n\t"
        "ds_write_b64 v186, v[48:49] offset:0\n\t"
        "ds_write_b64 v154, v[52:53] offset:0\n\t"
        "ds_write_b64 v186, v[56:57] offset:64\n\t"
        "ds_write_b64 v154, v[116:117] offset:64\n\t"
        "ds_write_b64 v186, v[120:121] offset:128\n\t"
        "ds_write_b64 v154, v[124:125] offset:128\n\t"
        "v_add_u32 v185, 256, v185\n\t"
        "v_add_u32 v186, 12288, v186\n\t"
        "v_xor_b32 v154, 32, v186\n\t"
        "s_sub_i32 s8, s8, 16\n\t"
        "s_cmp_gt_i32 s8, 0\n\t"
        "s_cbranch_scc1 30b\n\t"
        "v_mov_b32 v185, v151\n\t"
        "v_mov_b32 v186, v183\n\t"
        "v_xor_b32 v154, 32, v186\n\t"
        "s_waitcnt lgkmcnt(0)\n\t"
        "s_barrier\n\t"
        "s_branch .Lx75%=\n\t"
        "99:\n\t"
        "s_nop 7\n\t"
        "s_waitcnt vmcnt(0) lgkmcnt(0)\n\t"
        "s_barrier\n\t"
        "s_lshl_b32 s6, s6, 16\n\t"
        "v_or_b32 %0, s6, v156\n\t"
        : "+v"(tid) : "s"(ka), "s"(bid), "s"(recs)
        : "memory", "scc", "vcc", "s3", "s4", "s5", "s6", "s7", "s8", "s9", "s10", "s11", "s12", "s13", "s16", "s17", "s18", "s19", "s20", "s21", "s22", "s23", "s24", "s25", "s26", "s27", "s28", "s29", "s30", "s31", "s34", "s35", "s36", "s37", "s38", "s39", "s40", "s41", "s42", "s43", "s44", "s45", "s46", "s47", "s48", "s49", "s50", "s51", "s52", "s53", "s54", "s55", "s56", "s57", "s60", "s61", "s62", "s63", "s64", "s65", "s66", "s67", "s68", "s69", "s70", "s71", "s72", "s73", "s74", "s75", "s76", "s77", "s78", "s79", "s80", "s81", "s82", "s83", "s84", "s85", "s86", "s87", "s88", "s89", "s90", "s91", "s92", "s93", "s94", "s95", "s96", "s97", "s98", "s99", "v1", "v2", "v3", "v4", "v5", "v6", "v7", "v8", "v9", "v10", "v11", "v12", "v13", "v14", "v15", "v16", "v17", "v18", "v19", "v20", "v21", "v22", "v23", "v24", "v25", "v26", "v27", "v28", "v29", "v30", "v31", "v32", "v33", "v34", "v35", "v36", "v37", "v38", "v39", "v40", "v41", "v42", "v43", "v44", "v45", "v46", "v47", "v48", "v49", "v50", "v51", "v52", "v53", "v54", "v55", "v56", "v57", "v58", "v59", "v60", "v61", "v62", "v63", "v64", "v65", "v66", "v67", "v68", "v69", "v70", "v71", "v72", "v73", "v74", "v75", "v76", "v77", "v78", "v79", "v80", "v81", "v82", "v83", "v84", "v85", "v86", "v87", "v88", "v89", "v90", "v91", "v92", "v93", "v94", "v95", "v96", "v97", "v98", "v99", "v100", "v101", "v102", "v103", "v104", "v105", "v106", "v107", "v108", "v109", "v110", "v111", "v112", "v113", "v114", "v115", "v116", "v117", "v118", "v119", "v120", "v121", "v122", "v123", "v124", "v125", "v126", "v127", "v128", "v129", "v130", "v131", "v132", "v133", "v134", "v135", "v136", "v137", "v138", "v139", "v140", "v141", "v142", "v143", "v144", "v145", "v146", "v147", "v148", "v149", "v150", "v151", "v152", "v153", "v154", "v155", "v156", "v157", "v158", "v159", "v160", "v161", "v162", "v163", "v164", "v165", "v166", "v167", "v168", "v169", "v170", "v171", "v172", "v173", "v174", "v175", "v176", "v177", "v178", "v179", "v180", "v181", "v182", "v183", "v184", "v185", "v186", "v187", "v188", "v189", "v190", "v191", "v192", "v193", "v194", "v195", "v196", "v197", "v198", "v199", "v200", "v201", "v202", "v203", "v204", "v205", "v206", "v207", "v208", "v209", "v210", "v211", "v212", "v213", "v214", "v215", "v216", "v217", "v218", "v219", "v220", "v221", "v222", "v223", "v224", "v225", "v226", "v227", "v228", "v229", "v230", "v231", "v232", "v233", "v234", "v235", "v236", "v237", "v238", "v239", "v240", "v241", "v242", "v243", "v244", "v245", "v246", "v247", "v248", "v249", "v250", "v251", "v252", "v253", "v254", "v255", "a0", "a1", "a2", "a3", "a4", "a5", "a6", "a7", "a8", "a9", "a10", "a11", "a12", "a13", "a14", "a15", "a16", "a17", "a18", "a19", "a20", "a21", "a22", "a23", "a24", "a25", "a26", "a27", "a28", "a29", "a30", "a31", "a32", "a33", "a34", "a35", "a36", "a37", "a38", "a39", "a40", "a41", "a42", "a43", "a44", "a45", "a46", "a47", "a48", "a49", "a50", "a51", "a52", "a53", "a54", "a55", "a56", "a57", "a58", "a59", "a60", "a61", "a62", "a63", "a64", "a65", "a66", "a67", "a68", "a69", "a70", "a71", "a72", "a73", "a74", "a75", "a76", "a77", "a78", "a79", "a80", "a81", "a82", "a83", "a84", "a85", "a86", "a87", "a88", "a89", "a90", "a91", "a92", "a93", "a94", "a95", "a96", "a97", "a98", "a99", "a100", "a101", "a102", "a103", "a104", "a105", "a106", "a107", "a108", "a109", "a110", "a111", "a112", "a113", "a114", "a115", "a116", "a117", "a118", "a119", "a120", "a121", "a122", "a123", "a124", "a125", "a126", "a127", "a128", "a129", "a130", "a131", "a132", "a133", "a134", "a135", "a136", "a137", "a138", "a139", "a140", "a141", "a142", "a143", "a144", "a145", "a146", "a147", "a148", "a149", "a150", "a151", "a152", "a153", "a154", "a155", "a156", "a157", "a158", "a159", "a160", "a161", "a162", "a163", "a164", "a165", "a166", "a167", "a168", "a169", "a170", "a171", "a172", "a173", "a174", "a175", "a176", "a177", "a178", "a179", "a180", "a181", "a182", "a183", "a184", "a185", "a186", "a187", "a188", "a189", "a190", "a191", "a192", "a193", "a194", "a195", "a196", "a197", "a198", "a199", "a200", "a201", "a202", "a203", "a204", "a205", "a206", "a207", "a208", "a209", "a210", "a211", "a212", "a213", "a214", "a215", "a216", "a217", "a218", "a219", "a220", "a221", "a222", "a223", "a224", "a225", "a226", "a227", "a228", "a229", "a230", "a231", "a232", "a233", "a234", "a235", "a236", "a237", "a238", "a239", "a240", "a241", "a242", "a243", "a244", "a245", "a246", "a247", "a248", "a249", "a250", "a251", "a252", "a253", "a254", "a255");
    asm volatile("" : "+s"(ka), "+s"(bid) :: "memory");
    {
        typedef short v4s __attribute__((ext_vector_type(4)));
        const unsigned short *x = ka->x, *w = ka->w;
        const unsigned* off = (const unsigned*)ka->off;
        unsigned short* out = ka->out;
        const unsigned tw = recs[bid * 64 + 27] >> 16, redo = __builtin_amdgcn_readfirstlane(tid >> 16);
        const unsigned* rc = recs + bid * 64;
        tid &= 255;
        const int lane = tid & 63, li = lane & 15, q = lane >> 4, wv = __builtin_amdgcn_readfirstlane(tid >> 6);
        int pix[4] = {-1, -1, -1, -1}, n = 0;
#pragma unroll 1
        for (int s = 0; s < 14; ++s) {
            const unsigned o = s < 8 ? (((tw & 255) + s) * 224 + ((tw >> 8) & 255)) * 192 : rc[49 + ((s - 8) >> 1)] + (s & 1) * (43200 - rc[30]);
            if (!((redo >> (s < 8 ? 8 + (s >> 1) : 0)) & 1) || o >= 224 * 224 * 192) continue;
            const int p = (o + li * (s < 8 ? 192 : rc[30])) / 192;
#pragma unroll
            for (int i = 0; i < 4; ++i) pix[i] = n == wv + 4 * i ? p : pix[i];
            ++n;
        }
        f32x4 acc[4][6];
        unsigned od[4];
        u32x4 wn[5];
#pragma unroll
        for (int i = 0; i < 4; ++i) {
            od[i] = off[max(pix[i], 0) * 9];
#pragma unroll
            for (int k = 0; k < 6; ++k) acc[i][k] = (f32x4){0.f, 0.f, 0.f, 0.f};
        }
#pragma unroll
        for (int k = 0; k < 5; ++k) wn[k] = *(const u32x4*)(w + min(tid + 256 * k, 1151u) * 8);
        const __amdgpu_buffer_rsrc_t xr = __builtin_amdgcn_make_buffer_rsrc((void*)x, 0, 224 * 224 * 192, 0x00020000);
        const unsigned wb = (8 * q + (li >> 2)) * 192 + (li & 3) * 8;
#pragma unroll 1
        for (int t = 0; t < 9; ++t) {
            unsigned char* slab = lds + (t & 1) * 18432;
#pragma unroll
            for (int k = 0; k < 5; ++k) {
                const unsigned c = min(tid + 256 * k, 1151u);
                *(u32x4*)(slab + ((c * 16) ^ (((c / 96) & 1) << 5))) = wn[k];
            }
            __syncthreads();
            u32x4 wf[3][6];
#pragma unroll
            for (int k = 0; k < 18; ++k) {
                __attribute__((address_space(3))) unsigned char* a = (__attribute__((address_space(3))) unsigned char*)slab + (k / 6) * 6144 + wb + (((k % 6) * 32) ^ ((q & 1) << 5));
                const u32x2 lo = __builtin_bit_cast(u32x2, __builtin_amdgcn_ds_read_tr16_b64_v4i16((__attribute__((address_space(3))) v4s*)a));
                const u32x2 hi = __builtin_bit_cast(u32x2, __builtin_amdgcn_ds_read_tr16_b64_v4i16((__attribute__((address_space(3))) v4s*)(a + 768)));
                wf[k / 6][k % 6] = (u32x4){lo[0], lo[1], hi[0], hi[1]};
            }
            const int tn = min(t + 1, 8);
#pragma unroll
            for (int k = 0; k < 5; ++k) wn[k] = *(const u32x4*)(w + tn * 9216 + min(tid + 256 * k, 1151u) * 8);
#pragma unroll
            for (int i0 = 0; i0 < 4; i0 += 2) {
                if (wv + 4 * i0 >= n) continue;
                u32x4 g[2][3][4];
                float wt[2][4];
#pragma unroll
                for (int j = 0; j < 2; ++j) {
                    const int i = i0 + j, p = max(pix[i], 0), h = p / 224, c = p - h * 224;
                    float py = rb((float)(h + t / 3 - 1) + bf_lo(od[i])), px = rb((float)(c + t % 3 - 1) + bf_hi(od[i]));
                    od[i] = off[p * 9 + tn];
                    py = fminf(fmaxf(py, 0.f), 223.f); px = fminf(fmaxf(px, 0.f), 223.f);
                    const float y0 = floorf(py), x0 = floorf(px), y1 = fminf(y0 + 1.f, 223.f), x1 = fminf(x0 + 1.f, 223.f);
                    const float wy = py - y0, wx = px - x0, a = rb(1.f - wy), b = rb(1.f - wx);
                    wt[j][0] = rb(a * b); wt[j][1] = rb(a * wx); wt[j][2] = rb(wy * b); wt[j][3] = rb(wy * wx);
                    int ad[4] = {(int)y0 * 224 + (int)x0, (int)y0 * 224 + (int)x1, (int)y1 * 224 + (int)x0, (int)y1 * 224 + (int)x1};
#pragma unroll
                    for (int k = 0; k < 4; ++k) ad[k] = wt[j][k] != 0.f && pix[i] >= 0 ? ad[k] * 192 + q * 16 : 0x40000000;
#pragma unroll
                    for (int k = 0; k < 4; ++k) {
#pragma unroll
                        for (int ks = 0; ks < 3; ++ks) g[j][ks][k] = (u32x4){0u, 0u, 0u, 0u};
                        if (__builtin_amdgcn_ballot_w64(ad[k] != 0x40000000) == 0) continue;
#pragma unroll
                        for (int ks = 0; ks < 3; ++ks) g[j][ks][k] = __builtin_amdgcn_raw_buffer_load_b128(xr, ad[k] + ks * 64, 0, 0);
                    }
                }
                asm volatile("" ::: "memory");
#pragma unroll
                for (int j = 0; j < 2; ++j) {
                    if (wv + 4 * (i0 + j) >= n) continue;
#pragma unroll
                    for (int ks = 0; ks < 3; ++ks) {
                        u32x4 bfr;
#pragma unroll
                        for (int d = 0; d < 4; ++d) {
                            float lo = 0.f, hi = 0.f;
#pragma unroll
                            for (int cn = 0; cn < 4; ++cn) {
                                const unsigned u = g[j][ks][cn][d];
                                const bf16x2 v = __builtin_bit_cast(bf16x2, u);
                                lo = __builtin_amdgcn_fdot2_f32_bf16(v, __builtin_bit_cast(bf16x2, __float_as_uint(wt[j][cn]) >> 16), lo, false);
                                hi = __builtin_amdgcn_fdot2_f32_bf16(v, __builtin_bit_cast(bf16x2, __float_as_uint(wt[j][cn])), hi, false);
                            }
                            bfr[d] = cvt_pk(lo, hi);
                        }
#pragma unroll
                        for (int k = 0; k < 6; ++k)
                            acc[i0 + j][k] = __builtin_amdgcn_mfma_f32_16x16x32_bf16(__builtin_bit_cast(bf16x8, wf[ks][k]), __builtin_bit_cast(bf16x8, bfr), acc[i0 + j][k], 0, 0, 0);
                    }
                }
            }
        }
#pragma unroll
        for (int i = 0; i < 4; ++i) {
            if (wv + 4 * i >= n) continue;
#pragma unroll
            for (int k = 0; k < 6; ++k) {
                u32x2 o; o[0] = cvt_pk(acc[i][k][0], acc[i][k][1]); o[1] = cvt_pk(acc[i][k][2], acc[i][k][3]);
                *(u32x2*)(out + pix[i] * 96 + k * 16 + q * 4) = o;
            }
        }
    }
}
extern "C" void kernel_launch(void* const* d_in, const int* in_sizes, int n_in, void* d_out, int out_size, void* d_ws, size_t ws_size, hipStream_t stream) {
    dcn_kernel<<<256, 256, 0, stream>>>((const unsigned short*)d_in[0], (const unsigned short*)d_in[1], (const unsigned short*)d_in[2], (unsigned short*)d_out, d_ws);
}
```

```cpp
#include <hip/hip_runtime.h>
#include <stdint.h>
typedef __attribute__((ext_vector_type(8))) __bf16 bf16x8;
typedef __attribute__((ext_vector_type(2))) __bf16 bf16x2;
typedef __attribute__((ext_vector_type(4))) float f32x4;
typedef __attribute__((ext_vector_type(4))) unsigned u32x4;
typedef __attribute__((ext_vector_type(2))) unsigned u32x2;
struct Args { const unsigned short *x, *off, *w; unsigned short* out; void* ws; };
static __device__ const unsigned recs[256 * 64] = {
1419264,1419264,1462272,1462272,1505280,1505280,1548288,1548288,1548288,1548288,1548288,1548288,40960,44032,48128,51200,55296,58368,62464,65536,62464,65536,62464,65536,268439552,336991254,57,807538688,80,7488,192,1040137727,16780300,50465804,576,36,224,1107820544,1107820544,1107820544,1098907648,1098907648,0,274752,1073741824,274176,44032,44032,40960,1465344,1073741824,1462272,16900,1107575312,4294961196,4294967294,16900,1107575312,4294961196,4294967294,16900,1107575312,4294961196,4294967294,
2623488,2666496,2709504,2752512,2795520,2838528,2881536,2924544,2924544,2924544,2924544,2924544,40960,45056,49152,53248,57344,61440,65536,69632,69632,69632,69632,69632,538976288,353703189,57,1080164608,80,7488,192,1040137855,16780300,50465804,576,36,128,1115947008,1115684864,1115160576,1107296256,1107296256,1107296256,533376,517248,501120,57344,49152,40960,2844672,2758656,2672640,1106264692,1112031880,4294960588,30,1106264692,1112031880,4294960588,30,1106264692,1112031880,4294960588,30,
3827712,3827712,3870720,3870720,3913728,3913728,3956736,3956736,3956736,3956736,3956736,3956736,40960,44032,48128,51200,55296,58368,62464,65536,62464,65536,62464,65536,268439552,336991254,57,1352798208,80,7488,192,1048526591,16780300,50465804,576,36,224,1119092736,1119092736,1119092736,1098907648,1098907648,0,726336,1073741824,725760,44032,44032,40960,3873792,1073741824,3870720,17074,1107575480,4294948652,4294967294,17074,1107575480,4294948652,4294967294,17074,1107575480,4294948652,4294967294,
21312,21312,21504,21504,21696,21696,21888,21888,21888,21888,21888,21888,40960,44032,48128,51200,55296,58368,62464,65536,62464,65536,62464,65536,268439552,336991254,410,1881808896,80,7984,43008,1048526335,50465804,16780300,36,80,224,1121976320,1121976320,1121976320,1098907648,1098907648,0,133056,1073741824,4032,44032,44032,40960,709632,1073741824,21504,17118,1107575524,4294943724,4294967294,17118,1107575524,4294943724,4294967294,17118,1107575524,4294943724,4294967294,
5720064,5720064,5763072,5763072,5806080,5806080,5849088,5849088,5849088,5849088,5849088,5849088,40960,44032,48128,51200,55296,58368,62464,65536,62464,65536,62464,65536,268439552,336991254,57,2154958848,80,7488,192,1048526463,16780300,50465804,576,36,224,1124466688,1124466688,1124466688,1098907648,1098907648,0,1081152,1073741824,1080576,44032,44032,40960,5766144,1073741824,5763072,17157,1107575560,4294938796,4294967294,17157,1107575560,4294938796,4294967294,17157,1107575560,4294938796,4294967294,
6752256,6795264,6838272,6881280,6924288,6967296,7010304,7053312,7053312,7053312,7053312,7053312,40960,45056,49152,53248,57344,61440,65536,69632,69632,69632,69632,69632,538976288,353703189,57,2428109056,80,7488,192,1056915199,16780300,50465804,576,36,128,1126301696,1126170624,1126039552,1107296256,1107296256,1107296256,1307520,1291392,1275264,57344,49152,40960,6973440,6887424,6801408,1106264861,1112032036,4294948300,30,1106264861,1112032036,4294948300,30,1106264861,1112032036,4294948300,30,
7612416,7612416,7655424,7655424,7698432,7698432,7741440,7741440,7741440,7741440,7741440,7741440,40960,44032,48128,51200,55296,58368,62464,65536,62464,65536,62464,65536,268439552,336991254,57,2957643776,80,7488,192,1056915071,16780300,50465804,576,36,224,1127350272,1127350272,1127350272,1098907648,1098907648,0,1435968,1073741824,1435392,44032,44032,40960,7658496,1073741824,7655424,17201,1107575604,4294928940,4294967294,17201,1107575604,4294928940,4294967294,17201,1107575604,4294928940,4294967294,
38592,38592,38784,38784,38976,38976,39168,39168,39168,39168,39168,39168,40960,44032,48128,51200,55296,58368,62464,65536,62464,65536,62464,65536,268439552,336991254,410,3230801920,80,7984,43008,1056915199,50465804,16780300,36,80,224,1128923136,1128923136,1128923136,1098907648,1098907648,0,136296,1073741824,7272,44032,44032,40960,726912,1073741824,38784,17225,1107575628,4294923564,4294967294,17225,1107575628,4294923564,4294967294,17225,1107575628,4294923564,4294967294,
1505280,1505280,1548288,1548288,1591296,1591296,1634304,1634304,1634304,1634304,1634304,1634304,40960,44032,48128,51200,55296,58368,62464,65536,62464,65536,62464,65536,268439552,336991254,57,808062976,80,7488,192,1040137727,16780300,50465804,576,36,224,1108344832,1108344832,1108344832,1098907648,1098907648,0,290880,1073741824,290304,44032,44032,40960,1551360,1073741824,1548288,16908,1107575320,4294960748,4294967294,16908,1107575320,4294960748,4294967294,16908,1107575320,4294960748,4294967294,
12096,12096,12288,12288,12480,12480,12672,12672,12672,12672,12672,12672,40960,44032,48128,51200,55296,58368,62464,65536,62464,65536,62464,65536,268439552,336991254,410,1080696832,80,7984,43008,1040137855,50465804,16780300,36,80,224,1115684864,1115684864,1115684864,1098907648,1098907648,0,131328,1073741824,2304,44032,44032,40960,700416,1073741824,12288,17020,1107575428,4294954476,4294967294,17020,1107575428,4294954476,4294967294,17020,1107575428,4294954476,4294967294,
3913728,3913728,3956736,3956736,3999744,3999744,4042752,4042752,4042752,4042752,4042752,4042752,40960,44032,48128,51200,55296,58368,62464,65536,62464,65536,62464,65536,268439552,336991254,57,1353322496,80,7488,192,1048526591,16780300,50465804,576,36,224,1119354880,1119354880,1119354880,1098907648,1098907648,0,742464,1073741824,741888,44032,44032,40960,3959808,1073741824,3956736,17078,1107575484,4294948204,4294967294,17078,1107575484,4294948204,4294967294,17078,1107575484,4294948204,4294967294,
21696,21696,21888,21888,22080,22080,22272,22272,22272,22272,22272,22272,40960,44032,48128,51200,55296,58368,62464,65536,62464,65536,62464,65536,268439552,336991254,410,1882333184,80,7984,43008,1048526335,50465804,16780300,36,80,224,1122238464,1122238464,1122238464,1098907648,1098907648,0,133128,1073741824,4104,44032,44032,40960,710016,1073741824,21888,17122,1107575528,4294943276,4294967294,17122,1107575528,4294943276,4294967294,17122,1107575528,4294943276,4294967294,
5806080,5806080,5849088,5849088,5892096,5892096,5935104,5935104,5935104,5935104,5935104,5935104,40960,44032,48128,51200,55296,58368,62464,65536,62464,65536,62464,65536,268439552,336991254,57,2155483136,80,7488,192,1048526463,16780300,50465804,576,36,224,1124597760,1124597760,1124597760,1098907648,1098907648,0,1097280,1073741824,1096704,44032,44032,40960,5852160,1073741824,5849088,17159,1107575562,4294938348,4294967294,17159,1107575562,4294938348,4294967294,17159,1107575562,4294938348,4294967294,
30144,30144,30336,30336,30528,30528,30720,30720,30720,30720,30720,30720,40960,44032,48128,51200,55296,58368,62464,65536,62464,65536,62464,65536,268439552,336991254,410,2428641280,80,7984,43008,1056915199,50465804,16780300,36,80,224,1126039552,1126039552,1126039552,1098907648,1098907648,0,134712,1073741824,5688,44032,44032,40960,718464,1073741824,30336,17181,1107575584,4294933420,4294967294,17181,1107575584,4294933420,4294967294,17181,1107575584,4294933420,4294967294,
7698432,7698432,7741440,7741440,7784448,7784448,7827456,7827456,7827456,7827456,7827456,7827456,40960,44032,48128,51200,55296,58368,62464,65536,62464,65536,62464,65536,268439552,336991254,57,2958168064,80,7488,192,1056915071,16780300,50465804,576,36,224,1127481344,1127481344,1127481344,1098907648,1098907648,0,1452096,1073741824,1451520,44032,44032,40960,7744512,1073741824,7741440,17203,1107575606,4294928492,4294967294,17203,1107575606,4294928492,4294967294,17203,1107575606,4294928492,4294967294,
38976,38976,39168,39168,39360,39360,39552,39552,39552,39552,39552,39552,40960,44032,48128,51200,55296,58368,62464,65536,62464,65536,62464,65536,268439552,336991254,410,3231326208,80,7984,43008,1056915199,50465804,16780300,36,80,224,1129054208,1129054208,1129054208,1098907648,1098907648,0,136368,1073741824,7344,44032,44032,40960,727296,1073741824,39168,17227,1107575630,4294923116,4294967294,17227,1107575630,4294923116,4294967294,17227,1107575630,4294923116,4294967294,
1290240,1333248,1376256,1419264,1462272,1505280,1548288,1591296,1634304,1634304,1634304,1634304,40960,45056,49152,53248,57344,61440,65536,69632,73728,73728,73728,73728,538976288,353703189,57,808584448,64,7488,192,1040137727,16780300,50465804,576,36,128,1108344832,1107820544,1107296256,1107296256,1107296256,1107296256,291456,275328,259200,61440,53248,45056,1554432,1468416,1382400,1106264560,1112031768,4294964556,30,1106264560,1112031768,4294964556,30,1106264560,1112031768,4294964556,30,
12480,12480,12672,12672,12864,12864,13056,13056,13056,13056,13056,13056,40960,44032,48128,51200,55296,58368,62464,65536,62464,65536,62464,65536,268439552,336991254,410,1081221120,80,7984,43008,1040137855,50465804,16780300,36,80,224,1115947008,1115947008,1115947008,1098907648,1098907648,0,131400,1073741824,2376,44032,44032,40960,700800,1073741824,12672,17026,1107575432,4294954028,4294967294,17026,1107575432,4294954028,4294967294,17026,1107575432,4294954028,4294967294,
3999744,3999744,4042752,4042752,4085760,4085760,4128768,4128768,4128768,4128768,4128768,4128768,40960,44032,48128,51200,55296,58368,62464,65536,62464,65536,62464,65536,268439552,336991254,57,1353846784,80,7488,192,1048526591,16780300,50465804,576,36,224,1119617024,1119617024,1119617024,1098907648,1098907648,0,758592,1073741824,758016,44032,44032,40960,4045824,1073741824,4042752,17082,1107575488,4294947756,4294967294,17082,1107575488,4294947756,4294967294,17082,1107575488,4294947756,4294967294,
22080,22080,22272,22272,22464,22464,22656,22656,22656,22656,22656,22656,40960,44032,48128,51200,55296,58368,62464,65536,62464,65536,62464,65536,268439552,336991254,410,1882857472,80,7984,43008,1048526335,50465804,16780300,36,80,224,1122500608,1122500608,1122500608,1098907648,1098907648,0,133200,1073741824,4176,44032,44032,40960,710400,1073741824,22272,17126,1107575532,4294942828,4294967294,17126,1107575532,4294942828,4294967294,17126,1107575532,4294942828,4294967294,
5892096,5892096,5935104,5935104,5978112,5978112,6021120,6021120,6021120,6021120,6021120,6021120,40960,44032,48128,51200,55296,58368,62464,65536,62464,65536,62464,65536,268439552,336991254,57,2156007424,80,7488,192,1048526591,16780300,50465804,576,36,224,1124728832,1124728832,1124728832,1098907648,1098907648,0,1113408,1073741824,1112832,44032,44032,40960,5938176,1073741824,5935104,17161,1107575564,4294937900,4294967294,17161,1107575564,4294937900,4294967294,17161,1107575564,4294937900,4294967294,
172032,215040,258048,301056,344064,387072,9074688,9117696,9117696,9117696,9117696,9117696,49152,53248,57344,61440,65536,69632,40960,45056,45056,45056,45056,45056,2425356288,320017685,57,2429170176,80,7488,192,1056915199,16780300,50465804,576,36,128,1129447424,1129447424,1086324736,1125122048,1125122048,0,1698624,1073741824,48384,32768,32768,53248,9059328,1073741824,258048,1125073737,1126187860,4294940716,142,1125073737,1126187860,4294940716,142,16512,1099972880,1036,4294967294,
7784448,7784448,7827456,7827456,7870464,7870464,7913472,7913472,7913472,7913472,7913472,7913472,40960,44032,48128,51200,55296,58368,62464,65536,62464,65536,62464,65536,268439552,336991254,57,2958692352,80,7488,192,1056915071,16780300,50465804,576,36,224,1127612416,1127612416,1127612416,1098907648,1098907648,0,1468224,1073741824,1467648,44032,44032,40960,7830528,1073741824,7827456,17205,1107575608,4294928044,4294967294,17205,1107575608,4294928044,4294967294,17205,1107575608,4294928044,4294967294,
39360,39360,39552,39552,39744,39744,39936,39936,39936,39936,39936,39936,40960,44032,48128,51200,55296,58368,62464,65536,62464,65536,62464,65536,268439552,336991254,410,3231850496,80,7984,43008,1056915199,50465804,16780300,36,80,224,1129185280,1129185280,1129185280,1098907648,1098907648,0,136440,1073741824,7416,44032,44032,40960,727680,1073741824,39552,17229,1107575632,4294922668,4294967294,17229,1107575632,4294922668,4294967294,17229,1107575632,4294922668,4294967294,
1591296,1591296,1634304,1634304,1677312,1677312,1720320,1720320,1720320,1720320,1720320,1720320,40960,44032,48128,51200,55296,58368,62464,65536,62464,65536,62464,65536,268439552,336991254,57,809111552,80,7488,192,1040137727,16780300,50465804,576,36,224,1108869120,1108869120,1108869120,1098907648,1098907648,0,307008,1073741824,306432,44032,44032,40960,1637376,1073741824,1634304,16916,1107575328,4294960300,4294967294,16916,1107575328,4294960300,4294967294,16916,1107575328,4294960300,4294967294,
12864,12864,13056,13056,13248,13248,13440,13440,13440,13440,13440,13440,40960,44032,48128,51200,55296,58368,62464,65536,62464,65536,62464,65536,268439552,336991254,410,1081745408,80,7984,43008,1040137855,50465804,16780300,36,80,224,1116209152,1116209152,1116209152,1098907648,1098907648,0,131472,1073741824,2448,44032,44032,40960,701184,1073741824,13056,17030,1107575436,4294953580,4294967294,17030,1107575436,4294953580,4294967294,17030,1107575436,4294953580,4294967294,
3913728,3956736,3999744,4042752,4085760,4128768,4171776,4214784,4214784,4214784,4214784,4214784,40960,45056,49152,53248,57344,61440,65536,69632,69632,69632,69632,69632,538976288,353703189,57,1354367232,80,7488,192,1048526591,16780300,50465804,576,36,128,1119879168,1119617024,1119354880,1107296256,1107296256,1107296256,775296,759168,743040,57344,49152,40960,4134912,4048896,3962880,1106264758,1112031940,4294956748,30,1106264758,1112031940,4294956748,30,1106264758,1112031940,4294956748,30,
4945920,4988928,5031936,5074944,5117952,5160960,5203968,5246976,5246976,5246976,5246976,5246976,40960,45056,49152,53248,57344,61440,65536,69632,69632,69632,69632,69632,538976288,353703189,57,1883373824,80,7488,192,1048526463,16780300,50465804,576,36,128,1123024896,1122762752,1122500608,1107296256,1107296256,1107296256,968832,952704,936576,57344,49152,40960,5167104,5081088,4995072,1106264806,1112031988,4294953676,30,1106264806,1112031988,4294953676,30,1106264806,1112031988,4294953676,30,
5978112,5978112,6021120,6021120,6064128,6064128,6107136,6107136,6107136,6107136,6107136,6107136,40960,44032,48128,51200,55296,58368,62464,65536,62464,65536,62464,65536,268439552,336991254,57,2156531712,80,7488,192,1048526591,16780300,50465804,576,36,224,1124859904,1124859904,1124859904,1098907648,1098907648,0,1129536,1073741824,1128960,44032,44032,40960,6024192,1073741824,6021120,17163,1107575566,4294937452,4294967294,17163,1107575566,4294937452,4294967294,17163,1107575566,4294937452,4294967294,
1290240,1333248,1376256,1419264,1462272,9504768,9547776,9590784,9590784,9590784,9590784,9590784,57344,61440,65536,69632,73728,40960,45056,49152,53248,53248,53248,53248,2425393296,353703189,57,2429814016,64,7488,192,1056915199,16780300,50465804,576,36,128,1130102784,1130233856,1107296256,1125122048,1125122048,1125122048,1779264,1795392,263232,32768,40960,61440,9489408,9575424,1403904,1125073747,1126187871,4294939436,142,1125073747,1126187871,4294939436,142,1125007856,1126318600,4294964396,142,
7870464,7870464,7913472,7913472,7956480,7956480,7999488,7999488,7999488,7999488,7999488,7999488,40960,44032,48128,51200,55296,58368,62464,65536,62464,65536,62464,65536,268439552,336991254,57,2959216640,80,7488,192,1056915071,16780300,50465804,576,36,224,1127743488,1127743488,1127743488,1098907648,1098907648,0,1484352,1073741824,1483776,44032,44032,40960,7916544,1073741824,7913472,17207,1107575610,4294927596,4294967294,17207,1107575610,4294927596,4294967294,17207,1107575610,4294927596,4294967294,
39744,39744,39936,39936,40128,40128,40320,40320,40320,40320,40320,40320,40960,44032,48128,51200,55296,58368,62464,65536,62464,65536,62464,65536,268439552,336991254,410,3232374784,80,7984,43008,1056915199,50465804,16780300,36,80,224,1129316352,1129316352,1129316352,1098907648,1098907648,0,136512,1073741824,7488,44032,44032,40960,728064,1073741824,39936,17231,1107575634,4294922220,4294967294,17231,1107575634,4294922220,4294967294,17231,1107575634,4294922220,4294967294,
1677312,1677312,1720320,1720320,1763328,1763328,1806336,1806336,1806336,1806336,1806336,1806336,40960,44032,48128,51200,55296,58368,62464,65536,62464,65536,62464,65536,268439552,336991254,57,809635840,80,7488,192,1040137855,16780300,50465804,576,36,224,1109393408,1109393408,1109393408,1098907648,1098907648,0,323136,1073741824,322560,44032,44032,40960,1723392,1073741824,1720320,16924,1107575336,4294959852,4294967294,16924,1107575336,4294959852,4294967294,16924,1107575336,4294959852,4294967294,
13248,13248,13440,13440,13632,13632,13824,13824,13824,13824,13824,13824,40960,44032,48128,51200,55296,58368,62464,65536,62464,65536,62464,65536,268439552,336991254,410,1082269696,80,7984,43008,1040137983,50465804,16780300,36,80,224,1116471296,1116471296,1116471296,1098907648,1098907648,0,131544,1073741824,2520,44032,44032,40960,701568,1073741824,13440,17034,1107575440,4294953132,4294967294,17034,1107575440,4294953132,4294967294,17034,1107575440,4294953132,4294967294,
4085760,4085760,4128768,4128768,4171776,4171776,4214784,4214784,4214784,4214784,4214784,4214784,40960,44032,48128,51200,55296,58368,62464,65536,62464,65536,62464,65536,268439552,336991254,57,1354895360,80,7488,192,1048526591,16780300,50465804,576,36,224,1119879168,1119879168,1119879168,1098907648,1098907648,0,774720,1073741824,774144,44032,44032,40960,4131840,1073741824,4128768,17086,1107575492,4294947308,4294967294,17086,1107575492,4294947308,4294967294,17086,1107575492,4294947308,4294967294,
22464,22464,22656,22656,22848,22848,23040,23040,23040,23040,23040,23040,40960,44032,48128,51200,55296,58368,62464,65536,62464,65536,62464,65536,268439552,336991254,410,1883906048,80,7984,43008,1048526463,50465804,16780300,36,80,224,1122762752,1122762752,1122762752,1098907648,1098907648,0,133272,1073741824,4248,44032,44032,40960,710784,1073741824,22656,17130,1107575536,4294942380,4294967294,17130,1107575536,4294942380,4294967294,17130,1107575536,4294942380,4294967294,
6064128,6064128,6107136,6107136,6150144,6150144,6193152,6193152,6193152,6193152,6193152,6193152,40960,44032,48128,51200,55296,58368,62464,65536,62464,65536,62464,65536,268439552,336991254,57,2157056000,80,7488,192,1048526591,16780300,50465804,576,36,224,1124990976,1124990976,1124990976,1098907648,1098907648,0,1145664,1073741824,1145088,44032,44032,40960,6110208,1073741824,6107136,17165,1107575568,4294937004,4294967294,17165,1107575568,4294937004,4294967294,17165,1107575568,4294937004,4294967294,
30528,30528,30720,30720,30912,30912,31104,31104,31104,31104,31104,31104,40960,44032,48128,51200,55296,58368,62464,65536,62464,65536,62464,65536,268439552,336991254,410,2686590976,80,7984,43008,1056914943,50465804,16780300,36,80,224,1126170624,1126170624,1126170624,1098907648,1098907648,0,134784,1073741824,5760,44032,44032,40960,718848,1073741824,30720,17183,1107575586,4294932972,4294967294,17183,1107575586,4294932972,4294967294,17183,1107575586,4294932972,4294967294,
7784448,7827456,7870464,7913472,7956480,7999488,8042496,8085504,8085504,8085504,8085504,8085504,40960,45056,49152,53248,57344,61440,65536,69632,69632,69632,69632,69632,538976288,353703189,57,2959737088,80,7488,192,1056915071,16780300,50465804,576,36,128,1127874560,1127743488,1127612416,1107296256,1107296256,1107296256,1501056,1484928,1468800,57344,49152,40960,8005632,7919616,7833600,1106264885,1112032060,4294945228,30,1106264885,1112032060,4294945228,30,1106264885,1112032060,4294945228,30,
40128,40128,40320,40320,40512,40512,40704,40704,40704,40704,40704,40704,40960,44032,48128,51200,55296,58368,62464,65536,62464,65536,62464,65536,268439552,336991254,410,3232899072,80,7984,43008,1056915199,50465804,16780300,36,80,224,1129447424,1129447424,1129447424,1098907648,1098907648,0,136584,1073741824,7560,44032,44032,40960,728448,1073741824,40320,17233,1107575636,4294921772,4294967294,17233,1107575636,4294921772,4294967294,17233,1107575636,4294921772,4294967294,
1763328,1763328,1806336,1806336,1849344,1849344,1892352,1892352,1892352,1892352,1892352,1892352,40960,44032,48128,51200,55296,58368,62464,65536,62464,65536,62464,65536,268439552,336991254,57,810160128,80,7488,192,1040137855,16780300,50465804,576,36,224,1109917696,1109917696,1109917696,1098907648,1098907648,0,339264,1073741824,338688,44032,44032,40960,1809408,1073741824,1806336,16932,1107575344,4294959404,4294967294,16932,1107575344,4294959404,4294967294,16932,1107575344,4294959404,4294967294,
13632,13632,13824,13824,14016,14016,14208,14208,14208,14208,14208,14208,40960,44032,48128,51200,55296,58368,62464,65536,62464,65536,62464,65536,268439552,336991254,410,1082793984,80,7984,43008,1040137983,50465804,16780300,36,80,224,1116733440,1116733440,1116733440,1098907648,1098907648,0,131616,1073741824,2592,44032,44032,40960,701952,1073741824,13824,17038,1107575444,4294952684,4294967294,17038,1107575444,4294952684,4294967294,17038,1107575444,4294952684,4294967294,
4171776,4171776,4214784,4214784,4257792,4257792,4300800,4300800,4300800,4300800,4300800,4300800,40960,44032,48128,51200,55296,58368,62464,65536,62464,65536,62464,65536,268439552,336991254,57,1355419648,80,7488,192,1048526591,16780300,50465804,576,36,224,1120141312,1120141312,1120141312,1098907648,1098907648,0,790848,1073741824,790272,44032,44032,40960,4217856,1073741824,4214784,17090,1107575496,4294946860,4294967294,17090,1107575496,4294946860,4294967294,17090,1107575496,4294946860,4294967294,
22848,22848,23040,23040,23232,23232,23424,23424,23424,23424,23424,23424,40960,44032,48128,51200,55296,58368,62464,65536,62464,65536,62464,65536,268439552,336991254,410,1884430336,80,7984,43008,1048526463,50465804,16780300,36,80,224,1123024896,1123024896,1123024896,1098907648,1098907648,0,133344,1073741824,4320,44032,44032,40960,711168,1073741824,23040,17134,1107575540,4294941932,4294967294,17134,1107575540,4294941932,4294967294,17134,1107575540,4294941932,4294967294,
5978112,6021120,6064128,6107136,6150144,6193152,6236160,6279168,6279168,6279168,6279168,6279168,40960,45056,49152,53248,57344,61440,65536,69632,69632,69632,69632,69632,538976288,353703189,57,2157576448,80,7488,192,1048526591,16780300,50465804,576,36,128,1125122048,1124990976,1124859904,1107296256,1107296256,1107296256,1162368,1146240,1130112,57344,49152,40960,6199296,6113280,6027264,1106264843,1112032018,4294950604,30,1106264843,1112032018,4294950604,30,1106264843,1112032018,4294950604,30,
30912,30912,31104,31104,31296,31296,31488,31488,31488,31488,31488,31488,40960,44032,48128,51200,55296,58368,62464,65536,62464,65536,62464,65536,268439552,336991254,410,2687115264,80,7984,43008,1056914943,50465804,16780300,36,80,224,1126301696,1126301696,1126301696,1098907648,1098907648,0,134856,1073741824,5832,44032,44032,40960,719232,1073741824,31104,17185,1107575588,4294932524,4294967294,17185,1107575588,4294932524,4294967294,17185,1107575588,4294932524,4294967294,
7956480,7956480,7999488,7999488,8042496,8042496,8085504,8085504,8085504,8085504,8085504,8085504,40960,44032,48128,51200,55296,58368,62464,65536,62464,65536,62464,65536,268439552,336991254,57,2960265216,80,7488,192,1056915071,16780300,50465804,576,36,224,1127874560,1127874560,1127874560,1098907648,1098907648,0,1500480,1073741824,1499904,44032,44032,40960,8002560,1073741824,7999488,17209,1107575612,4294927148,4294967294,17209,1107575612,4294927148,4294967294,17209,1107575612,4294927148,4294967294,
40512,40512,40704,40704,40896,40896,41088,41088,41088,41088,41088,41088,40960,44032,48128,51200,55296,58368,62464,65536,62464,65536,62464,65536,268439552,336991254,410,3233423360,80,7984,43008,1056915199,50465804,16780300,36,80,224,1129578496,1129578496,1129578496,1098907648,1098907648,0,136656,1073741824,7632,44032,44032,40960,728832,1073741824,40704,17235,1107575638,4294921324,4294967294,17235,1107575638,4294921324,4294967294,17235,1107575638,4294921324,4294967294,
1849344,1849344,1892352,1892352,1935360,1935360,1978368,1978368,1978368,1978368,1978368,1978368,40960,44032,48128,51200,55296,58368,62464,65536,62464,65536,62464,65536,268439552,336991254,57,810684416,80,7488,192,1040137855,16780300,50465804,576,36,224,1110441984,1110441984,1110441984,1098907648,1098907648,0,355392,1073741824,354816,44032,44032,40960,1895424,1073741824,1892352,16940,1107575352,4294958956,4294967294,16940,1107575352,4294958956,4294967294,16940,1107575352,4294958956,4294967294,
14016,14016,14208,14208,14400,14400,14592,14592,14592,14592,14592,14592,40960,44032,48128,51200,55296,58368,62464,65536,62464,65536,62464,65536,268439552,336991254,410,1083318272,80,7984,43008,1040137983,50465804,16780300,36,80,224,1116995584,1116995584,1116995584,1098907648,1098907648,0,131688,1073741824,2664,44032,44032,40960,702336,1073741824,14208,17042,1107575448,4294952236,4294967294,17042,1107575448,4294952236,4294967294,17042,1107575448,4294952236,4294967294,
4257792,4257792,4300800,4300800,4343808,4343808,4386816,4386816,4386816,4386816,4386816,4386816,40960,44032,48128,51200,55296,58368,62464,65536,62464,65536,62464,65536,268439552,336991254,57,1355943936,80,7488,192,1048526591,16780300,50465804,576,36,224,1120403456,1120403456,1120403456,1098907648,1098907648,0,806976,1073741824,806400,44032,44032,40960,4303872,1073741824,4300800,17094,1107575500,4294946412,4294967294,17094,1107575500,4294946412,4294967294,17094,1107575500,4294946412,4294967294,
23232,23232,23424,23424,23616,23616,23808,23808,23808,23808,23808,23808,40960,44032,48128,51200,55296,58368,62464,65536,62464,65536,62464,65536,268439552,336991254,410,1884954624,80,7984,43008,1048526463,50465804,16780300,36,80,224,1123287040,1123287040,1123287040,1098907648,1098907648,0,133416,1073741824,4392,44032,44032,40960,711552,1073741824,23424,17138,1107575544,4294941484,4294967294,17138,1107575544,4294941484,4294967294,17138,1107575544,4294941484,4294967294,
6150144,6150144,6193152,6193152,6236160,6236160,6279168,6279168,6279168,6279168,6279168,6279168,40960,44032,48128,51200,55296,58368,62464,65536,62464,65536,62464,65536,268439552,336991254,57,2158104576,80,7488,192,1048526591,16780300,50465804,576,36,224,1125122048,1125122048,1125122048,1098907648,1098907648,0,1161792,1073741824,1161216,44032,44032,40960,6196224,1073741824,6193152,17167,1107575570,4294936556,4294967294,17167,1107575570,4294936556,4294967294,17167,1107575570,4294936556,4294967294,
31296,31296,31488,31488,31680,31680,31872,31872,31872,31872,31872,31872,40960,44032,48128,51200,55296,58368,62464,65536,62464,65536,62464,65536,268439552,336991254,410,2687639552,80,7984,43008,1056914943,50465804,16780300,36,80,224,1126432768,1126432768,1126432768,1098907648,1098907648,0,134928,1073741824,5904,44032,44032,40960,719616,1073741824,31488,17187,1107575590,4294932076,4294967294,17187,1107575590,4294932076,4294967294,17187,1107575590,4294932076,4294967294,
8042496,8042496,8085504,8085504,8128512,8128512,8171520,8171520,8171520,8171520,8171520,8171520,40960,44032,48128,51200,55296,58368,62464,65536,62464,65536,62464,65536,268439552,336991254,57,2960789504,80,7488,192,1056915071,16780300,50465804,576,36,224,1128005632,1128005632,1128005632,1098907648,1098907648,0,1516608,1073741824,1516032,44032,44032,40960,8088576,1073741824,8085504,17211,1107575614,4294926700,4294967294,17211,1107575614,4294926700,4294967294,17211,1107575614,4294926700,4294967294,
40896,40896,41088,41088,41280,41280,41472,41472,41472,41472,41472,41472,40960,44032,48128,51200,55296,58368,62464,65536,62464,65536,62464,65536,268439552,336991254,410,3233947648,80,7984,43008,1056915199,50465804,16780300,36,80,224,1129709568,1129709568,1129709568,1098907648,1098907648,0,136728,1073741824,7704,44032,44032,40960,729216,1073741824,41088,17237,1107575640,4294920876,4294967294,17237,1107575640,4294920876,4294967294,17237,1107575640,4294920876,4294967294,
1591296,1634304,1677312,1720320,1763328,1806336,1849344,1892352,1892352,1892352,1892352,1892352,40960,45056,49152,53248,57344,61440,65536,69632,69632,69632,69632,69632,538976288,353703189,57,811204864,80,7488,192,1040137855,16780300,50465804,576,36,128,1109917696,1109393408,1108869120,1107296256,1107296256,1107296256,339840,323712,307584,57344,49152,40960,1812480,1726464,1640448,1106264596,1112031792,4294963660,30,1106264596,1112031792,4294963660,30,1106264596,1112031792,4294963660,30,
2881536,2924544,2967552,3010560,3053568,3096576,3139584,3182592,3182592,3182592,3182592,3182592,40960,45056,49152,53248,57344,61440,65536,69632,69632,69632,69632,69632,538976288,353703189,57,1083834624,80,7488,192,1040137983,16780300,50465804,576,36,128,1116733440,1116471296,1116209152,1107296256,1107296256,1107296256,581760,565632,549504,57344,49152,40960,3102720,3016704,2930688,1106264710,1112031892,4294959820,30,1106264710,1112031892,4294959820,30,1106264710,1112031892,4294959820,30,
4343808,4343808,4386816,4386816,4429824,4429824,4472832,4472832,4472832,4472832,4472832,4472832,40960,44032,48128,51200,55296,58368,62464,65536,62464,65536,62464,65536,268439552,336991254,57,1356468224,80,7488,192,1048526591,16780300,50465804,576,36,224,1120665600,1120665600,1120665600,1098907648,1098907648,0,823104,1073741824,822528,44032,44032,40960,4389888,1073741824,4386816,17098,1107575504,4294945964,4294967294,17098,1107575504,4294945964,4294967294,17098,1107575504,4294945964,4294967294,
23616,23616,23808,23808,24000,24000,24192,24192,24192,24192,24192,24192,40960,44032,48128,51200,55296,58368,62464,65536,62464,65536,62464,65536,268439552,336991254,410,1885478912,80,7984,43008,1048526463,50465804,16780300,36,80,224,1123549184,1123549184,1123549184,1098907648,1098907648,0,133488,1073741824,4464,44032,44032,40960,711936,1073741824,23808,17142,1107575548,4294941036,4294967294,17142,1107575548,4294941036,4294967294,17142,1107575548,4294941036,4294967294,
6236160,6236160,6279168,6279168,6322176,6322176,6365184,6365184,6365184,6365184,6365184,6365184,40960,44032,48128,51200,55296,58368,62464,65536,62464,65536,62464,65536,268439552,336991254,57,2158628864,80,7488,192,1048526591,16780300,50465804,576,36,224,1125253120,1125253120,1125253120,1098907648,1098907648,0,1177920,1073741824,1177344,44032,44032,40960,6282240,1073741824,6279168,17169,1107575572,4294936108,4294967294,17169,1107575572,4294936108,4294967294,17169,1107575572,4294936108,4294967294,
31680,31680,31872,31872,32064,32064,32256,32256,32256,32256,32256,32256,40960,44032,48128,51200,55296,58368,62464,65536,62464,65536,62464,65536,268439552,336991254,410,2688163840,80,7984,43008,1056914943,50465804,16780300,36,80,224,1126563840,1126563840,1126563840,1098907648,1098907648,0,135000,1073741824,5976,44032,44032,40960,720000,1073741824,31872,17189,1107575592,4294931628,4294967294,17189,1107575592,4294931628,4294967294,17189,1107575592,4294931628,4294967294,
8128512,8128512,8171520,8171520,8214528,8214528,8257536,8257536,8257536,8257536,8257536,8257536,40960,44032,48128,51200,55296,58368,62464,65536,62464,65536,62464,65536,268439552,336991254,57,2961313792,80,7488,192,1056915199,16780300,50465804,576,36,224,1128136704,1128136704,1128136704,1098907648,1098907648,0,1532736,1073741824,1532160,44032,44032,40960,8174592,1073741824,8171520,17213,1107575616,4294926252,4294967294,17213,1107575616,4294926252,4294967294,17213,1107575616,4294926252,4294967294,
430080,473088,516096,559104,602112,645120,9074688,9117696,9117696,9117696,9117696,9117696,49152,53248,57344,61440,65536,69632,40960,45056,45056,45056,45056,45056,3233808384,320017685,57,3234476544,80,7488,192,1056915199,16780300,50465804,576,36,128,1129447424,1129447424,1094713344,1128267776,1128267776,0,1700352,1073741824,96768,32768,32768,53248,9068544,1073741824,516096,1128219465,1129333588,4294940428,190,1128219465,1129333588,4294940428,190,16672,1099972976,268,4294967294,
1935360,1935360,1978368,1978368,2021376,2021376,2064384,2064384,2064384,2064384,2064384,2064384,40960,44032,48128,51200,55296,58368,62464,65536,62464,65536,62464,65536,268439552,336991254,57,811732992,80,7488,192,1040137855,16780300,50465804,576,36,224,1110966272,1110966272,1110966272,1098907648,1098907648,0,371520,1073741824,370944,44032,44032,40960,1981440,1073741824,1978368,16948,1107575360,4294958508,4294967294,16948,1107575360,4294958508,4294967294,16948,1107575360,4294958508,4294967294,
14400,14400,14592,14592,14784,14784,14976,14976,14976,14976,14976,14976,40960,44032,48128,51200,55296,58368,62464,65536,62464,65536,62464,65536,268439552,336991254,410,1084366848,80,7984,43008,1040137983,50465804,16780300,36,80,224,1117257728,1117257728,1117257728,1098907648,1098907648,0,131760,1073741824,2736,44032,44032,40960,702720,1073741824,14592,17046,1107575452,4294951788,4294967294,17046,1107575452,4294951788,4294967294,17046,1107575452,4294951788,4294967294,
3072,3072,3264,3264,3456,3456,3648,3648,3840,3840,4032,4032,40960,44032,48128,51200,55296,58368,62464,65536,69632,72704,76800,79872,268439552,336991254,410,1612852224,64,7984,43008,1048526335,50465804,16780300,36,80,224,1099956224,1099956224,1099956224,1098907648,1098907648,0,129672,1073741824,648,51200,51200,48128,691584,1073741824,3456,16768,1107575208,4294965004,4294967294,16768,1107575208,4294965004,4294967294,16768,1107575208,4294965004,4294967294,
24000,24000,24192,24192,24384,24384,24576,24576,24576,24576,24576,24576,40960,44032,48128,51200,55296,58368,62464,65536,62464,65536,62464,65536,268439552,336991254,410,1886003200,80,7984,43008,1048526463,50465804,16780300,36,80,224,1123811328,1123811328,1123811328,1098907648,1098907648,0,133560,1073741824,4536,44032,44032,40960,712320,1073741824,24192,17146,1107575552,4294940588,4294967294,17146,1107575552,4294940588,4294967294,17146,1107575552,4294940588,4294967294,
6322176,6322176,6365184,6365184,6408192,6408192,6451200,6451200,6451200,6451200,6451200,6451200,40960,44032,48128,51200,55296,58368,62464,65536,62464,65536,62464,65536,268439552,336991254,57,2159153152,80,7488,192,1048526591,16780300,50465804,576,36,224,1125384192,1125384192,1125384192,1098907648,1098907648,0,1194048,1073741824,1193472,44032,44032,40960,6368256,1073741824,6365184,17171,1107575574,4294935660,4294967294,17171,1107575574,4294935660,4294967294,17171,1107575574,4294935660,4294967294,
32064,32064,32256,32256,32448,32448,32640,32640,32640,32640,32640,32640,40960,44032,48128,51200,55296,58368,62464,65536,62464,65536,62464,65536,268439552,336991254,410,2688688128,80,7984,43008,1056915071,50465804,16780300,36,80,224,1126694912,1126694912,1126694912,1098907648,1098907648,0,135072,1073741824,6048,44032,44032,40960,720384,1073741824,32256,17191,1107575594,4294931180,4294967294,17191,1107575594,4294931180,4294967294,17191,1107575594,4294931180,4294967294,
8214528,8214528,8257536,8257536,8300544,8300544,8343552,8343552,8343552,8343552,8343552,8343552,40960,44032,48128,51200,55296,58368,62464,65536,62464,65536,62464,65536,268439552,336991254,57,2961838080,80,7488,192,1056915199,16780300,50465804,576,36,224,1128267776,1128267776,1128267776,1098907648,1098907648,0,1548864,1073741824,1548288,44032,44032,40960,8260608,1073741824,8257536,17215,1107575618,4294925804,4294967294,17215,1107575618,4294925804,4294967294,17215,1107575618,4294925804,4294967294,
1290240,1333248,1376256,1419264,1462272,9504768,9547776,9590784,9590784,9590784,9590784,9590784,57344,61440,65536,69632,73728,40960,45056,49152,53248,53248,53248,53248,3233857728,353703189,57,3235120384,64,7488,192,1056915199,16780300,50465804,576,36,128,1130102784,1130233856,1107296256,1128267776,1128267776,1128267776,1780992,1797120,264960,32768,40960,61440,9498624,9584640,1413120,1128219475,1129333599,4294939148,190,1128219475,1129333599,4294939148,190,1128153584,1129464328,4294964108,190,
2021376,2021376,2064384,2064384,2107392,2107392,2150400,2150400,2150400,2150400,2150400,2150400,40960,44032,48128,51200,55296,58368,62464,65536,62464,65536,62464,65536,268439552,336991254,57,812257280,80,7488,192,1040137855,16780300,50465804,576,36,224,1111490560,1111490560,1111490560,1098907648,1098907648,0,387648,1073741824,387072,44032,44032,40960,2067456,1073741824,2064384,16956,1107575368,4294958060,4294967294,16956,1107575368,4294958060,4294967294,16956,1107575368,4294958060,4294967294,
14784,14784,14976,14976,15168,15168,15360,15360,15360,15360,15360,15360,40960,44032,48128,51200,55296,58368,62464,65536,62464,65536,62464,65536,268439552,336991254,410,1084891136,80,7984,43008,1040137983,50465804,16780300,36,80,224,1117519872,1117519872,1117519872,1098907648,1098907648,0,131832,1073741824,2808,44032,44032,40960,703104,1073741824,14976,17050,1107575456,4294951340,4294967294,17050,1107575456,4294951340,4294967294,17050,1107575456,4294951340,4294967294,
3456,3456,3648,3648,3840,3840,4032,4032,4224,4224,4416,4416,40960,44032,48128,51200,55296,58368,62464,65536,69632,72704,76800,79872,268439552,336991254,410,1613376512,64,7984,43008,1048526335,50465804,16780300,36,80,224,1101004800,1101004800,1101004800,1098907648,1098907648,0,129744,1073741824,720,51200,51200,48128,691968,1073741824,3840,16784,1107575224,4294964556,4294967294,16784,1107575224,4294964556,4294967294,16784,1107575224,4294964556,4294967294,
86016,129024,172032,215040,258048,301056,301056,301056,301056,301056,301056,301056,40960,45056,49152,53248,57344,61440,61440,61440,61440,61440,61440,61440,0,353703189,57,1886564864,80,7488,192,1048526463,16780300,50465804,576,36,128,1082130432,1082130432,1082130432,0,0,0,1073741824,1073741824,32256,45056,45056,45056,1073741824,1073741824,172032,16384,1099972832,1036,4294967294,16384,1099972832,1036,4294967294,16384,1099972832,1036,4294967294,
6408192,6408192,6451200,6451200,6494208,6494208,6537216,6537216,6537216,6537216,6537216,6537216,40960,44032,48128,51200,55296,58368,62464,65536,62464,65536,62464,65536,268439552,336991254,57,2159677440,80,7488,192,1048526591,16780300,50465804,576,36,224,1125515264,1125515264,1125515264,1098907648,1098907648,0,1210176,1073741824,1209600,44032,44032,40960,6454272,1073741824,6451200,17173,1107575576,4294935212,4294967294,17173,1107575576,4294935212,4294967294,17173,1107575576,4294935212,4294967294,
32448,32448,32640,32640,32832,32832,33024,33024,33024,33024,33024,33024,40960,44032,48128,51200,55296,58368,62464,65536,62464,65536,62464,65536,268439552,336991254,410,2689212416,80,7984,43008,1056915071,50465804,16780300,36,80,224,1126825984,1126825984,1126825984,1098907648,1098907648,0,135144,1073741824,6120,44032,44032,40960,720768,1073741824,32640,17193,1107575596,4294930732,4294967294,17193,1107575596,4294930732,4294967294,17193,1107575596,4294930732,4294967294,
8300544,8300544,8343552,8343552,8386560,8386560,8429568,8429568,8429568,8429568,8429568,8429568,40960,44032,48128,51200,55296,58368,62464,65536,62464,65536,62464,65536,268439552,336991254,57,2962362368,80,7488,192,1056915199,16780300,50465804,576,36,224,1128398848,1128398848,1128398848,1098907648,1098907648,0,1564992,1073741824,1564416,44032,44032,40960,8346624,1073741824,8343552,17217,1107575620,4294925356,4294967294,17217,1107575620,4294925356,4294967294,17217,1107575620,4294925356,4294967294,
8816640,8859648,8902656,8945664,8988672,9031680,9074688,9117696,9117696,9117696,9117696,9117696,40960,45056,49152,53248,57344,61440,65536,69632,69632,69632,69632,69632,538976288,353703189,57,3491889408,80,7488,192,1056914943,16780300,50465804,576,36,128,1129447424,1129316352,1129185280,1107296256,1107296256,1107296256,1694592,1678464,1662336,57344,49152,40960,9037824,8951808,8865792,1106264909,1112032084,4294942156,30,1106264909,1112032084,4294942156,30,1106264909,1112032084,4294942156,30,
2107392,2107392,2150400,2150400,2193408,2193408,2236416,2236416,2236416,2236416,2236416,2236416,40960,44032,48128,51200,55296,58368,62464,65536,62464,65536,62464,65536,268439552,336991254,57,812781568,80,7488,192,1040137855,16780300,50465804,576,36,224,1112014848,1112014848,1112014848,1098907648,1098907648,0,403776,1073741824,403200,44032,44032,40960,2153472,1073741824,2150400,16964,1107575376,4294957612,4294967294,16964,1107575376,4294957612,4294967294,16964,1107575376,4294957612,4294967294,
1290240,1290240,1333248,1333248,1376256,1376256,1419264,1419264,1462272,1462272,1505280,1505280,40960,44032,48128,51200,55296,58368,62464,65536,69632,72704,76800,79872,1076903984,336991254,57,1085414400,64,7488,192,1040137983,16780300,50465804,576,36,224,1107296256,1107296256,1107296256,1115684864,1115684864,1111490560,260352,1073741824,259776,51200,51200,48128,1388544,1073741824,1385472,1110983152,1117929996,4294961580,46,1110983152,1117929996,4294961580,46,1110983152,1117929996,4294961580,46,
4171776,4214784,4257792,4300800,4343808,4386816,4429824,4472832,4472832,4472832,4472832,4472832,40960,45056,49152,53248,57344,61440,65536,69632,69632,69632,69632,69632,538976288,353703189,57,1613889792,80,7488,192,1048526335,16780300,50465804,576,36,128,1120665600,1120403456,1120141312,1107296256,1107296256,1107296256,823680,807552,791424,57344,49152,40960,4392960,4306944,4220928,1106264770,1112031952,4294955980,30,1106264770,1112031952,4294955980,30,1106264770,1112031952,4294955980,30,
3840,3840,4032,4032,4224,4224,4416,4416,4608,4608,4800,4800,40960,44032,48128,51200,55296,58368,62464,65536,69632,72704,76800,79872,268439552,336991254,410,1887054848,64,7984,43008,1048526463,50465804,16780300,36,80,224,1102053376,1102053376,1102053376,1098907648,1098907648,0,129816,1073741824,792,51200,51200,48128,692352,1073741824,4224,16800,1107575240,4294964108,4294967294,16800,1107575240,4294964108,4294967294,16800,1107575240,4294964108,4294967294,
1032192,1075200,1118208,1161216,1204224,1247232,8730624,8773632,8773632,8773632,8773632,8773632,49152,53248,57344,61440,65536,69632,40960,45056,45056,45056,45056,45056,2155872256,320017685,57,2160209920,80,7488,192,1048526591,16780300,50465804,576,36,128,1128923136,1128923136,1104150528,1124073472,1124073472,0,1633536,1073741824,209664,32768,32768,53248,8712192,1073741824,1118208,1123959617,1125139276,4294941836,126,1123959617,1125139276,4294941836,126,16832,1099973096,4294965772,4294967294,
7010304,7053312,7096320,7139328,7182336,7225344,7268352,7311360,7311360,7311360,7311360,7311360,40960,45056,49152,53248,57344,61440,65536,69632,69632,69632,69632,69632,538976288,353703189,57,2689728768,80,7488,192,1056915071,16780300,50465804,576,36,128,1126694912,1126563840,1126432768,1107296256,1107296256,1107296256,1355904,1339776,1323648,57344,49152,40960,7231488,7145472,7059456,1106264867,1112032042,4294947532,30,1106264867,1112032042,4294947532,30,1106264867,1112032042,4294947532,30,
8386560,8386560,8429568,8429568,8472576,8472576,8515584,8515584,8515584,8515584,8515584,8515584,40960,44032,48128,51200,55296,58368,62464,65536,62464,65536,62464,65536,268439552,336991254,57,2962886656,80,7488,192,1056915199,16780300,50465804,576,36,224,1128529920,1128529920,1128529920,1098907648,1098907648,0,1581120,1073741824,1580544,44032,44032,40960,8432640,1073741824,8429568,17219,1107575622,4294924908,4294967294,17219,1107575622,4294924908,4294967294,17219,1107575622,4294924908,4294967294,
41280,41280,41472,41472,41664,41664,41856,41856,41856,41856,41856,41856,40960,44032,48128,51200,55296,58368,62464,65536,62464,65536,62464,65536,268439552,336991254,410,3492421632,80,7984,43008,1056914943,50465804,16780300,36,80,224,1129840640,1129840640,1129840640,1098907648,1098907648,0,136800,1073741824,7776,44032,44032,40960,729600,1073741824,41472,17239,1107575642,4294920428,4294967294,17239,1107575642,4294920428,4294967294,17239,1107575642,4294920428,4294967294,
2193408,2193408,2236416,2236416,2279424,2279424,2322432,2322432,2322432,2322432,2322432,2322432,40960,44032,48128,51200,55296,58368,62464,65536,62464,65536,62464,65536,268439552,336991254,57,813305856,80,7488,192,1040137855,16780300,50465804,576,36,224,1112539136,1112539136,1112539136,1098907648,1098907648,0,419904,1073741824,419328,44032,44032,40960,2239488,1073741824,2236416,16972,1107575384,4294957164,4294967294,16972,1107575384,4294957164,4294967294,16972,1107575384,4294957164,4294967294,
0,43008,86016,129024,172032,215040,215040,215040,215040,215040,215040,215040,40960,45056,49152,53248,57344,61440,61440,61440,61440,61440,61440,61440,0,353703189,57,1085977088,80,7488,192,1040137983,16780300,50465804,576,36,128,1073741824,1073741824,1073741824,0,0,0,1073741824,1073741824,16128,45056,45056,45056,1073741824,1073741824,86016,0,1099972768,1292,4294967294,0,1099972768,1292,4294967294,0,1099972768,1292,4294967294,
15168,15168,15360,15360,15552,15552,15744,15744,15744,15744,15744,15744,40960,44032,48128,51200,55296,58368,62464,65536,62464,65536,62464,65536,268439552,336991254,410,1614422016,80,7984,43008,1048526335,50465804,16780300,36,80,224,1117782016,1117782016,1117782016,1098907648,1098907648,0,131904,1073741824,2880,44032,44032,40960,703488,1073741824,15360,17054,1107575460,4294950892,4294967294,17054,1107575460,4294950892,4294967294,17054,1107575460,4294950892,4294967294,
5203968,5246976,5289984,5332992,5376000,5419008,5462016,5505024,5505024,5505024,5505024,5505024,40960,45056,49152,53248,57344,61440,65536,69632,69632,69632,69632,69632,538976288,353703189,57,1887568128,80,7488,192,1048526591,16780300,50465804,576,36,128,1123811328,1123549184,1123287040,1107296256,1107296256,1107296256,1017216,1001088,984960,57344,49152,40960,5425152,5339136,5253120,1106264818,1112032000,4294952908,30,1106264818,1112032000,4294952908,30,1106264818,1112032000,4294952908,30,
1118208,1161216,1204224,1247232,1290240,1333248,9160704,9203712,9203712,9203712,9203712,9203712,49152,53248,57344,61440,65536,69632,40960,45056,45056,45056,45056,45056,2155872256,320017685,57,2160865280,80,7488,192,1048526591,16780300,50465804,576,36,128,1129578496,1129578496,1105199104,1124073472,1124073472,0,1714176,1073741824,225792,32768,32768,53248,9142272,1073741824,1204224,1123959627,1125139286,4294940556,126,1123959627,1125139286,4294940556,126,16848,1099973112,4294965516,4294967294,
32832,32832,33024,33024,33216,33216,33408,33408,33408,33408,33408,33408,40960,44032,48128,51200,55296,58368,62464,65536,62464,65536,62464,65536,268439552,336991254,410,2690260992,80,7984,43008,1056915071,50465804,16780300,36,80,224,1126957056,1126957056,1126957056,1098907648,1098907648,0,135216,1073741824,6192,44032,44032,40960,721152,1073741824,33024,17195,1107575598,4294930284,4294967294,17195,1107575598,4294930284,4294967294,17195,1107575598,4294930284,4294967294,
8472576,8472576,8515584,8515584,8558592,8558592,8601600,8601600,8601600,8601600,8601600,8601600,40960,44032,48128,51200,55296,58368,62464,65536,62464,65536,62464,65536,268439552,336991254,57,2963410944,80,7488,192,1056915199,16780300,50465804,576,36,224,1128660992,1128660992,1128660992,1098907648,1098907648,0,1597248,1073741824,1596672,44032,44032,40960,8518656,1073741824,8515584,17221,1107575624,4294924460,4294967294,17221,1107575624,4294924460,4294967294,17221,1107575624,4294924460,4294967294,
41664,41664,41856,41856,42048,42048,42240,42240,42240,42240,42240,42240,40960,44032,48128,51200,55296,58368,62464,65536,62464,65536,62464,65536,268439552,336991254,410,3492945920,80,7984,43008,1056914943,50465804,16780300,36,80,224,1129971712,1129971712,1129971712,1098907648,1098907648,0,136872,1073741824,7848,44032,44032,40960,729984,1073741824,41856,17241,1107575644,4294919980,4294967294,17241,1107575644,4294919980,4294967294,17241,1107575644,4294919980,4294967294,
2279424,2279424,2322432,2322432,2365440,2365440,2408448,2408448,2408448,2408448,2408448,2408448,40960,44032,48128,51200,55296,58368,62464,65536,62464,65536,62464,65536,268439552,336991254,57,813830144,80,7488,192,1040137983,16780300,50465804,576,36,224,1113063424,1113063424,1113063424,1098907648,1098907648,0,436032,1073741824,435456,44032,44032,40960,2325504,1073741824,2322432,16980,1107575392,4294956716,4294967294,16980,1107575392,4294956716,4294967294,16980,1107575392,4294956716,4294967294,
2365440,2365440,2408448,2408448,2451456,2451456,2494464,2494464,2494464,2494464,2494464,2494464,40960,44032,48128,51200,55296,58368,62464,65536,62464,65536,62464,65536,268439552,336991254,57,1086459904,80,7488,192,1040137983,16780300,50465804,576,36,224,1113587712,1113587712,1113587712,1098907648,1098907648,0,452160,1073741824,451584,44032,44032,40960,2411520,1073741824,2408448,16988,1107575400,4294956268,4294967294,16988,1107575400,4294956268,4294967294,16988,1107575400,4294956268,4294967294,
15552,15552,15744,15744,15936,15936,16128,16128,16128,16128,16128,16128,40960,44032,48128,51200,55296,58368,62464,65536,62464,65536,62464,65536,268439552,336991254,410,1614946304,80,7984,43008,1048526463,50465804,16780300,36,80,224,1118044160,1118044160,1118044160,1098907648,1098907648,0,131976,1073741824,2952,44032,44032,40960,703872,1073741824,15744,17058,1107575464,4294950444,4294967294,17058,1107575464,4294950444,4294967294,17058,1107575464,4294950444,4294967294,
4429824,4429824,4472832,4472832,4515840,4515840,4558848,4558848,4558848,4558848,4558848,4558848,40960,44032,48128,51200,55296,58368,62464,65536,62464,65536,62464,65536,268439552,336991254,57,1888096256,80,7488,192,1048526591,16780300,50465804,576,36,224,1120927744,1120927744,1120927744,1098907648,1098907648,0,839232,1073741824,838656,44032,44032,40960,4475904,1073741824,4472832,17102,1107575508,4294945516,4294967294,17102,1107575508,4294945516,4294967294,17102,1107575508,4294945516,4294967294,
1204224,1247232,1290240,1333248,1376256,1419264,9590784,9590784,9590784,9590784,9590784,9590784,49152,53248,57344,61440,65536,69632,40960,45056,45056,45056,45056,45056,2155872256,320017685,57,2161520640,80,7488,192,1048526591,16780300,50465804,576,36,128,1130233856,1130233856,1106247680,1124073472,1124073472,0,1794816,1073741824,241920,32768,32768,53248,9572352,1073741824,1290240,1123959637,1125139295,4294939276,126,1123959637,1125139295,4294939276,126,16864,1099973124,4294965260,4294967294,
33216,33216,33408,33408,33600,33600,33792,33792,33792,33792,33792,33792,40960,44032,48128,51200,55296,58368,62464,65536,62464,65536,62464,65536,268439552,336991254,410,2690785280,80,7984,43008,1056915071,50465804,16780300,36,80,224,1127088128,1127088128,1127088128,1098907648,1098907648,0,135288,1073741824,6264,44032,44032,40960,721536,1073741824,33408,17197,1107575600,4294929836,4294967294,17197,1107575600,4294929836,4294967294,17197,1107575600,4294929836,4294967294,
8042496,8085504,8128512,8171520,8214528,8257536,8300544,8343552,8343552,8343552,8343552,8343552,40960,45056,49152,53248,57344,61440,65536,69632,69632,69632,69632,69632,538976288,353703189,57,2963931392,80,7488,192,1056915199,16780300,50465804,576,36,128,1128267776,1128136704,1128005632,1107296256,1107296256,1107296256,1549440,1533312,1517184,57344,49152,40960,8263680,8177664,8091648,1106264891,1112032066,4294944460,30,1106264891,1112032066,4294944460,30,1106264891,1112032066,4294944460,30,
42048,42048,42240,42240,42432,42432,42624,42624,42624,42624,42624,42624,40960,44032,48128,51200,55296,58368,62464,65536,62464,65536,62464,65536,268439552,336991254,410,3493470208,80,7984,43008,1056914943,50465804,16780300,36,80,224,1130102784,1130102784,1130102784,1098907648,1098907648,0,136944,1073741824,7920,44032,44032,40960,730368,1073741824,42240,17243,1107575646,4294919532,4294967294,17243,1107575646,4294919532,4294967294,17243,1107575646,4294919532,4294967294,
1849344,1892352,1935360,1978368,2021376,2064384,2107392,2150400,2150400,2150400,2150400,2150400,40960,45056,49152,53248,57344,61440,65536,69632,69632,69632,69632,69632,538976288,353703189,57,814350592,80,7488,192,1040137983,16780300,50465804,576,36,128,1111490560,1110966272,1110441984,1107296256,1107296256,1107296256,388224,372096,355968,57344,49152,40960,2070528,1984512,1898496,1106264620,1112031816,4294962892,30,1106264620,1112031816,4294962892,30,1106264620,1112031816,4294962892,30,
2451456,2451456,2494464,2494464,2537472,2537472,2580480,2580480,2580480,2580480,2580480,2580480,40960,44032,48128,51200,55296,58368,62464,65536,62464,65536,62464,65536,268439552,336991254,57,1086984192,80,7488,192,1040137983,16780300,50465804,576,36,224,1114112000,1114112000,1114112000,1098907648,1098907648,0,468288,1073741824,467712,44032,44032,40960,2497536,1073741824,2494464,16996,1107575408,4294955820,4294967294,16996,1107575408,4294955820,4294967294,16996,1107575408,4294955820,4294967294,
15936,15936,16128,16128,16320,16320,16512,16512,16512,16512,16512,16512,40960,44032,48128,51200,55296,58368,62464,65536,62464,65536,62464,65536,268439552,336991254,410,1615470592,80,7984,43008,1048526463,50465804,16780300,36,80,224,1118306304,1118306304,1118306304,1098907648,1098907648,0,132048,1073741824,3024,44032,44032,40960,704256,1073741824,16128,17062,1107575468,4294949996,4294967294,17062,1107575468,4294949996,4294967294,17062,1107575468,4294949996,4294967294,
4515840,4515840,4558848,4558848,4601856,4601856,4644864,4644864,4644864,4644864,4644864,4644864,40960,44032,48128,51200,55296,58368,62464,65536,62464,65536,62464,65536,268439552,336991254,57,1888620544,80,7488,192,1048526591,16780300,50465804,576,36,224,1121189888,1121189888,1121189888,1098907648,1098907648,0,855360,1073741824,854784,44032,44032,40960,4561920,1073741824,4558848,17106,1107575512,4294945068,4294967294,17106,1107575512,4294945068,4294967294,17106,1107575512,4294945068,4294967294,
1290240,1290240,1333248,1333248,1376256,1376256,1419264,1419264,1462272,1462272,1505280,1505280,40960,44032,48128,51200,55296,58368,62464,65536,69632,72704,76800,79872,1615880272,336991254,57,2418154496,64,7488,192,1056914943,16780300,50465804,576,36,224,1107296256,1107296256,1107296256,1119879168,1119879168,1117782016,261504,1073741824,260928,51200,51200,48128,1394688,1073741824,1391616,1117536752,1122124300,4294961388,78,1117536752,1122124300,4294961388,78,1117536752,1122124300,4294961388,78,
4608,4608,4800,4800,4992,4992,5184,5184,5376,5376,5568,5568,40960,44032,48128,51200,55296,58368,62464,65536,69632,72704,76800,79872,268439552,336991254,410,2691312640,64,7984,43008,1056915071,50465804,16780300,36,80,224,1104150528,1104150528,1104150528,1098907648,1098907648,0,129960,1073741824,936,51200,51200,48128,693120,1073741824,4992,16832,1107575272,4294963212,4294967294,16832,1107575272,4294963212,4294967294,16832,1107575272,4294963212,4294967294,
4992,4992,5184,5184,5376,5376,5568,5568,5760,5760,5952,5952,40960,44032,48128,51200,55296,58368,62464,65536,69632,72704,76800,79872,268439552,336991254,410,2964466688,64,7984,43008,1056915199,50465804,16780300,36,80,224,1105199104,1105199104,1105199104,1098907648,1098907648,0,130032,1073741824,1008,51200,51200,48128,693504,1073741824,5376,16848,1107575288,4294962764,4294967294,16848,1107575288,4294962764,4294967294,16848,1107575288,4294962764,4294967294,
42432,42432,42624,42624,42816,42816,42816,42816,42816,42816,42816,42816,40960,44032,48128,51200,55296,58368,62464,65536,62464,65536,62464,65536,268439552,336991254,410,3493994496,80,7984,43008,1056915071,50465804,16780300,36,80,224,1130233856,1130233856,1130233856,1098907648,1098907648,0,137016,1073741824,7992,44032,44032,40960,730752,1073741824,42624,17245,1107575647,4294919084,4294967294,17245,1107575647,4294919084,4294967294,17245,1107575647,4294919084,4294967294,
2688,2688,2880,2880,3072,3072,3264,3264,3456,3456,3648,3648,40960,44032,48128,51200,55296,58368,62464,65536,69632,72704,76800,79872,268439552,336991254,410,814886400,64,7984,43008,1040137983,50465804,16780300,36,80,224,1098907648,1098907648,1098907648,1098907648,1098907648,0,129600,1073741824,576,51200,51200,48128,691200,1073741824,3072,16736,1107575192,4294965452,4294967294,16736,1107575192,4294965452,4294967294,16736,1107575192,4294965452,4294967294,
3139584,3182592,3225600,3268608,3311616,3354624,3397632,3440640,3440640,3440640,3440640,3440640,40960,45056,49152,53248,57344,61440,65536,69632,69632,69632,69632,69632,538976288,353703189,57,1087504640,80,7488,192,1040137983,16780300,50465804,576,36,128,1117519872,1117257728,1116995584,1107296256,1107296256,1107296256,630144,614016,597888,57344,49152,40960,3360768,3274752,3188736,1106264722,1112031904,4294959052,30,1106264722,1112031904,4294959052,30,1106264722,1112031904,4294959052,30,
16320,16320,16512,16512,16704,16704,16896,16896,16896,16896,16896,16896,40960,44032,48128,51200,55296,58368,62464,65536,62464,65536,62464,65536,268439552,336991254,410,1615994880,80,7984,43008,1048526463,50465804,16780300,36,80,224,1118568448,1118568448,1118568448,1098907648,1098907648,0,132120,1073741824,3096,44032,44032,40960,704640,1073741824,16512,17066,1107575472,4294949548,4294967294,17066,1107575472,4294949548,4294967294,17066,1107575472,4294949548,4294967294,
4601856,4601856,4644864,4644864,4687872,4687872,4730880,4730880,4730880,4730880,4730880,4730880,40960,44032,48128,51200,55296,58368,62464,65536,62464,65536,62464,65536,268439552,336991254,57,1889144832,80,7488,192,1048526591,16780300,50465804,576,36,224,1121452032,1121452032,1121452032,1098907648,1098907648,0,871488,1073741824,870912,44032,44032,40960,4647936,1073741824,4644864,17110,1107575516,4294944620,4294967294,17110,1107575516,4294944620,4294967294,17110,1107575516,4294944620,4294967294,
4224,4224,4416,4416,4608,4608,4800,4800,4992,4992,5184,5184,40960,44032,48128,51200,55296,58368,62464,65536,69632,72704,76800,79872,268439552,336991254,410,2418682880,64,7984,43008,1056914943,50465804,16780300,36,80,224,1103101952,1103101952,1103101952,1098907648,1098907648,0,129888,1073741824,864,51200,51200,48128,692736,1073741824,4608,16816,1107575256,4294963660,4294967294,16816,1107575256,4294963660,4294967294,16816,1107575256,4294963660,4294967294,
1290240,1290240,1333248,1333248,1376256,1376256,1419264,1419264,1462272,1462272,1505280,1505280,40960,44032,48128,51200,55296,58368,62464,65536,69632,72704,76800,79872,268439552,336991254,57,2691832832,64,7488,192,1056915071,16780300,50465804,576,36,224,1107296256,1107296256,1107296256,1098907648,1098907648,0,258624,1073741824,258048,51200,51200,48128,1379328,1073741824,1376256,16880,1107575308,4294961868,4294967294,16880,1107575308,4294961868,4294967294,16880,1107575308,4294961868,4294967294,
5376,5376,5568,5568,5760,5760,5952,5952,6144,6144,6336,6336,40960,44032,48128,51200,55296,58368,62464,65536,69632,72704,76800,79872,268439552,336991254,410,2964990976,64,7984,43008,1056915199,50465804,16780300,36,80,224,1106247680,1106247680,1106247680,1098907648,1098907648,0,130104,1073741824,1080,51200,51200,48128,693888,1073741824,5760,16864,1107575300,4294962316,4294967294,16864,1107575300,4294962316,4294967294,16864,1107575300,4294962316,4294967294,
1290240,1290240,1333248,1333248,1376256,1376256,1419264,1419264,1462272,1462272,1505280,1505280,40960,44032,48128,51200,55296,58368,62464,65536,69632,72704,76800,79872,2154856560,336991254,57,3494517760,64,7488,192,1056915071,16780300,50465804,576,36,224,1107296256,1107296256,1107296256,1124073472,1124073472,1121976320,262656,1073741824,262080,51200,51200,48128,1400832,1073741824,1397760,1121731056,1125204492,4294961196,110,1121731056,1125204492,4294961196,110,1121731056,1125204492,4294961196,110,
6336,6336,6528,6528,6720,6720,6912,6912,6912,6912,6912,6912,40960,44032,48128,51200,55296,58368,62464,65536,62464,65536,62464,65536,268439552,336991254,410,815407104,80,7984,43008,1040137983,50465804,16780300,36,80,224,1107820544,1107820544,1107820544,1098907648,1098907648,0,130248,1073741824,1224,44032,44032,40960,694656,1073741824,6528,16900,1107575312,4294961196,4294967294,16900,1107575312,4294961196,4294967294,16900,1107575312,4294961196,4294967294,
2537472,2537472,2580480,2580480,2623488,2623488,2666496,2666496,2666496,2666496,2666496,2666496,40960,44032,48128,51200,55296,58368,62464,65536,62464,65536,62464,65536,268439552,336991254,57,1088032768,80,7488,192,1040137983,16780300,50465804,576,36,224,1114636288,1114636288,1114636288,1098907648,1098907648,0,484416,1073741824,483840,44032,44032,40960,2583552,1073741824,2580480,17004,1107575416,4294955372,4294967294,17004,1107575416,4294955372,4294967294,17004,1107575416,4294955372,4294967294,
16704,16704,16896,16896,17088,17088,17280,17280,17280,17280,17280,17280,40960,44032,48128,51200,55296,58368,62464,65536,62464,65536,62464,65536,268439552,336991254,410,1616519168,80,7984,43008,1048526463,50465804,16780300,36,80,224,1118830592,1118830592,1118830592,1098907648,1098907648,0,132192,1073741824,3168,44032,44032,40960,705024,1073741824,16896,17070,1107575476,4294949100,4294967294,17070,1107575476,4294949100,4294967294,17070,1107575476,4294949100,4294967294,
4687872,4687872,4730880,4730880,4773888,4773888,4816896,4816896,4816896,4816896,4816896,4816896,40960,44032,48128,51200,55296,58368,62464,65536,62464,65536,62464,65536,268439552,336991254,57,1889669120,80,7488,192,1048526591,16780300,50465804,576,36,224,1121714176,1121714176,1121714176,1098907648,1098907648,0,887616,1073741824,887040,44032,44032,40960,4733952,1073741824,4730880,17114,1107575520,4294944172,4294967294,17114,1107575520,4294944172,4294967294,17114,1107575520,4294944172,4294967294,
24384,24384,24576,24576,24768,24768,24960,24960,24960,24960,24960,24960,40960,44032,48128,51200,55296,58368,62464,65536,62464,65536,62464,65536,268439552,336991254,410,2419204096,80,7984,43008,1056914943,50465804,16780300,36,80,224,1124073472,1124073472,1124073472,1098907648,1098907648,0,133632,1073741824,4608,44032,44032,40960,712704,1073741824,24576,17150,1107575554,4294940140,4294967294,17150,1107575554,4294940140,4294967294,17150,1107575554,4294940140,4294967294,
6494208,6494208,6537216,6537216,6580224,6580224,6623232,6623232,6623232,6623232,6623232,6623232,40960,44032,48128,51200,55296,58368,62464,65536,62464,65536,62464,65536,268439552,336991254,57,2692354048,80,7488,192,1056915071,16780300,50465804,576,36,224,1125646336,1125646336,1125646336,1098907648,1098907648,0,1226304,1073741824,1225728,44032,44032,40960,6540288,1073741824,6537216,17175,1107575578,4294934764,4294967294,17175,1107575578,4294934764,4294967294,17175,1107575578,4294934764,4294967294,
33600,33600,33792,33792,33984,33984,34176,34176,34176,34176,34176,34176,40960,44032,48128,51200,55296,58368,62464,65536,62464,65536,62464,65536,268439552,336991254,410,2965512192,80,7984,43008,1056915199,50465804,16780300,36,80,224,1127219200,1127219200,1127219200,1098907648,1098907648,0,135360,1073741824,6336,44032,44032,40960,721920,1073741824,33792,17199,1107575602,4294929388,4294967294,17199,1107575602,4294929388,4294967294,17199,1107575602,4294929388,4294967294,
5760,5760,5952,5952,6144,6144,6336,6336,6528,6528,6720,6720,40960,44032,48128,51200,55296,58368,62464,65536,69632,72704,76800,79872,268439552,336991254,410,3495046144,64,7984,43008,1056915071,50465804,16780300,36,80,224,1107296256,1107296256,1107296256,1098907648,1098907648,0,130176,1073741824,1152,51200,51200,48128,694272,1073741824,6144,16880,1107575308,4294961868,4294967294,16880,1107575308,4294961868,4294967294,16880,1107575308,4294961868,4294967294,
6720,6720,6912,6912,7104,7104,7296,7296,7296,7296,7296,7296,40960,44032,48128,51200,55296,58368,62464,65536,62464,65536,62464,65536,268439552,336991254,410,815931392,80,7984,43008,1040137983,50465804,16780300,36,80,224,1108344832,1108344832,1108344832,1098907648,1098907648,0,130320,1073741824,1296,44032,44032,40960,695040,1073741824,6912,16908,1107575320,4294960748,4294967294,16908,1107575320,4294960748,4294967294,16908,1107575320,4294960748,4294967294,
2623488,2623488,2666496,2666496,2709504,2709504,2752512,2752512,2752512,2752512,2752512,2752512,40960,44032,48128,51200,55296,58368,62464,65536,62464,65536,62464,65536,268439552,336991254,57,1344409600,80,7488,192,1048526335,16780300,50465804,576,36,224,1115160576,1115160576,1115160576,1098907648,1098907648,0,500544,1073741824,499968,44032,44032,40960,2669568,1073741824,2666496,17012,1107575424,4294954924,4294967294,17012,1107575424,4294954924,4294967294,17012,1107575424,4294954924,4294967294,
17088,17088,17280,17280,17472,17472,17664,17664,17664,17664,17664,17664,40960,44032,48128,51200,55296,58368,62464,65536,62464,65536,62464,65536,268439552,336991254,410,1617043456,80,7984,43008,1048526463,50465804,16780300,36,80,224,1119092736,1119092736,1119092736,1098907648,1098907648,0,132264,1073741824,3240,44032,44032,40960,705408,1073741824,17280,17074,1107575480,4294948652,4294967294,17074,1107575480,4294948652,4294967294,17074,1107575480,4294948652,4294967294,
4773888,4773888,4816896,4816896,4859904,4859904,4902912,4902912,4902912,4902912,4902912,4902912,40960,44032,48128,51200,55296,58368,62464,65536,62464,65536,62464,65536,268439552,336991254,57,1890193408,80,7488,192,1048526591,16780300,50465804,576,36,224,1121976320,1121976320,1121976320,1098907648,1098907648,0,903744,1073741824,903168,44032,44032,40960,4819968,1073741824,4816896,17118,1107575524,4294943724,4294967294,17118,1107575524,4294943724,4294967294,17118,1107575524,4294943724,4294967294,
6236160,6279168,6322176,6365184,6408192,6451200,6494208,6537216,6537216,6537216,6537216,6537216,40960,45056,49152,53248,57344,61440,65536,69632,69632,69632,69632,69632,538976288,353703189,57,2419720448,80,7488,192,1056914943,16780300,50465804,576,36,128,1125515264,1125384192,1125253120,1107296256,1107296256,1107296256,1210752,1194624,1178496,57344,49152,40960,6457344,6371328,6285312,1106264849,1112032024,4294949836,30,1106264849,1112032024,4294949836,30,1106264849,1112032024,4294949836,30,
6580224,6580224,6623232,6623232,6666240,6666240,6709248,6709248,6709248,6709248,6709248,6709248,40960,44032,48128,51200,55296,58368,62464,65536,62464,65536,62464,65536,268439552,336991254,57,2692878336,80,7488,192,1056915199,16780300,50465804,576,36,224,1125777408,1125777408,1125777408,1098907648,1098907648,0,1242432,1073741824,1241856,44032,44032,40960,6626304,1073741824,6623232,17177,1107575580,4294934316,4294967294,17177,1107575580,4294934316,4294967294,17177,1107575580,4294934316,4294967294,
344064,387072,430080,473088,516096,559104,9074688,9117696,9117696,9117696,9117696,9117696,49152,53248,57344,61440,65536,69632,40960,45056,45056,45056,45056,45056,2964324352,320017685,57,2966041088,80,7488,192,1056915199,16780300,50465804,576,36,128,1129447424,1129447424,1092616192,1127219200,1127219200,0,1699776,1073741824,80640,32768,32768,53248,9065472,1073741824,430080,1127170889,1128285012,4294940524,174,1127170889,1128285012,4294940524,174,16640,1099972944,524,4294967294,
8558592,8558592,8601600,8601600,8644608,8644608,8687616,8687616,8687616,8687616,8687616,8687616,40960,44032,48128,51200,55296,58368,62464,65536,62464,65536,62464,65536,268439552,336991254,57,3495563264,80,7488,192,1056915071,16780300,50465804,576,36,224,1128792064,1128792064,1128792064,1098907648,1098907648,0,1613376,1073741824,1612800,44032,44032,40960,8604672,1073741824,8601600,17223,1107575626,4294924012,4294967294,17223,1107575626,4294924012,4294967294,17223,1107575626,4294924012,4294967294,
7104,7104,7296,7296,7488,7488,7680,7680,7680,7680,7680,7680,40960,44032,48128,51200,55296,58368,62464,65536,62464,65536,62464,65536,268439552,336991254,410,816455680,80,7984,43008,1040137983,50465804,16780300,36,80,224,1108869120,1108869120,1108869120,1098907648,1098907648,0,130392,1073741824,1368,44032,44032,40960,695424,1073741824,7296,16916,1107575328,4294960300,4294967294,16916,1107575328,4294960300,4294967294,16916,1107575328,4294960300,4294967294,
2709504,2709504,2752512,2752512,2795520,2795520,2838528,2838528,2838528,2838528,2838528,2838528,40960,44032,48128,51200,55296,58368,62464,65536,62464,65536,62464,65536,268439552,336991254,57,1344933888,80,7488,192,1048526335,16780300,50465804,576,36,224,1115684864,1115684864,1115684864,1098907648,1098907648,0,516672,1073741824,516096,44032,44032,40960,2755584,1073741824,2752512,17020,1107575428,4294954476,4294967294,17020,1107575428,4294954476,4294967294,17020,1107575428,4294954476,4294967294,
17472,17472,17664,17664,17856,17856,18048,18048,18048,18048,18048,18048,40960,44032,48128,51200,55296,58368,62464,65536,62464,65536,62464,65536,268439552,336991254,410,1617567744,80,7984,43008,1048526463,50465804,16780300,36,80,224,1119354880,1119354880,1119354880,1098907648,1098907648,0,132336,1073741824,3312,44032,44032,40960,705792,1073741824,17664,17078,1107575484,4294948204,4294967294,17078,1107575484,4294948204,4294967294,17078,1107575484,4294948204,4294967294,
4859904,4859904,4902912,4902912,4945920,4945920,4988928,4988928,4988928,4988928,4988928,4988928,40960,44032,48128,51200,55296,58368,62464,65536,62464,65536,62464,65536,268439552,336991254,57,1890717696,80,7488,192,1048526591,16780300,50465804,576,36,224,1122238464,1122238464,1122238464,1098907648,1098907648,0,919872,1073741824,919296,44032,44032,40960,4905984,1073741824,4902912,17122,1107575528,4294943276,4294967294,17122,1107575528,4294943276,4294967294,17122,1107575528,4294943276,4294967294,
24768,24768,24960,24960,25152,25152,25344,25344,25344,25344,25344,25344,40960,44032,48128,51200,55296,58368,62464,65536,62464,65536,62464,65536,268439552,336991254,410,2420252672,80,7984,43008,1056915071,50465804,16780300,36,80,224,1124204544,1124204544,1124204544,1098907648,1098907648,0,133704,1073741824,4680,44032,44032,40960,713088,1073741824,24960,17153,1107575556,4294939692,4294967294,17153,1107575556,4294939692,4294967294,17153,1107575556,4294939692,4294967294,
6666240,6666240,6709248,6709248,6752256,6752256,6795264,6795264,6795264,6795264,6795264,6795264,40960,44032,48128,51200,55296,58368,62464,65536,62464,65536,62464,65536,268439552,336991254,57,2693402624,80,7488,192,1056915199,16780300,50465804,576,36,224,1125908480,1125908480,1125908480,1098907648,1098907648,0,1258560,1073741824,1257984,44032,44032,40960,6712320,1073741824,6709248,17179,1107575582,4294933868,4294967294,17179,1107575582,4294933868,4294967294,17179,1107575582,4294933868,4294967294,
1290240,1333248,1376256,1419264,1462272,9504768,9547776,9590784,9590784,9590784,9590784,9590784,57344,61440,65536,69632,73728,40960,45056,49152,53248,53248,53248,53248,2964369584,353703189,57,2966684928,64,7488,192,1056915199,16780300,50465804,576,36,128,1130102784,1130233856,1107296256,1127219200,1127219200,1127219200,1780416,1796544,264384,32768,40960,61440,9495552,9581568,1410048,1127170899,1128285023,4294939244,174,1127170899,1128285023,4294939244,174,1127105008,1128415752,4294964204,174,
9074688,9117696,9160704,9203712,9246720,9289728,9332736,9375744,9375744,9375744,9375744,9375744,40960,45056,49152,53248,57344,61440,65536,69632,69632,69632,69632,69632,538976288,353703189,57,3496083712,80,7488,192,1056915071,16780300,50465804,576,36,128,1129840640,1129709568,1129578496,1107296256,1107296256,1107296256,1742976,1726848,1710720,57344,49152,40960,9295872,9209856,9123840,1106264915,1112032090,4294941388,30,1106264915,1112032090,4294941388,30,1106264915,1112032090,4294941388,30,
7488,7488,7680,7680,7872,7872,8064,8064,8064,8064,8064,8064,40960,44032,48128,51200,55296,58368,62464,65536,62464,65536,62464,65536,268439552,336991254,410,816979968,80,7984,43008,1040137983,50465804,16780300,36,80,224,1109393408,1109393408,1109393408,1098907648,1098907648,0,130464,1073741824,1440,44032,44032,40960,695808,1073741824,7680,16924,1107575336,4294959852,4294967294,16924,1107575336,4294959852,4294967294,16924,1107575336,4294959852,4294967294,
2795520,2795520,2838528,2838528,2881536,2881536,2924544,2924544,2924544,2924544,2924544,2924544,40960,44032,48128,51200,55296,58368,62464,65536,62464,65536,62464,65536,268439552,336991254,57,1345458176,80,7488,192,1048526335,16780300,50465804,576,36,224,1115947008,1115947008,1115947008,1098907648,1098907648,0,532800,1073741824,532224,44032,44032,40960,2841600,1073741824,2838528,17026,1107575432,4294954028,4294967294,17026,1107575432,4294954028,4294967294,17026,1107575432,4294954028,4294967294,
4429824,4472832,4515840,4558848,4601856,4644864,4687872,4730880,4730880,4730880,4730880,4730880,40960,45056,49152,53248,57344,61440,65536,69632,69632,69632,69632,69632,538976288,353703189,57,1618084096,80,7488,192,1048526463,16780300,50465804,576,36,128,1121452032,1121189888,1120927744,1107296256,1107296256,1107296256,872064,855936,839808,57344,49152,40960,4651008,4564992,4478976,1106264782,1112031964,4294955212,30,1106264782,1112031964,4294955212,30,1106264782,1112031964,4294955212,30,
4945920,4945920,4988928,4988928,5031936,5031936,5074944,5074944,5074944,5074944,5074944,5074944,40960,44032,48128,51200,55296,58368,62464,65536,62464,65536,62464,65536,268439552,336991254,57,1891241984,80,7488,192,1048526591,16780300,50465804,576,36,224,1122500608,1122500608,1122500608,1098907648,1098907648,0,936000,1073741824,935424,44032,44032,40960,4992000,1073741824,4988928,17126,1107575532,4294942828,4294967294,17126,1107575532,4294942828,4294967294,17126,1107575532,4294942828,4294967294,
25152,25152,25344,25344,25536,25536,25728,25728,25728,25728,25728,25728,40960,44032,48128,51200,55296,58368,62464,65536,62464,65536,62464,65536,268439552,336991254,410,2420776960,80,7984,43008,1056915071,50465804,16780300,36,80,224,1124335616,1124335616,1124335616,1098907648,1098907648,0,133776,1073741824,4752,44032,44032,40960,713472,1073741824,25344,17155,1107575558,4294939244,4294967294,17155,1107575558,4294939244,4294967294,17155,1107575558,4294939244,4294967294,
7268352,7311360,7354368,7397376,7440384,7483392,7526400,7569408,7569408,7569408,7569408,7569408,40960,45056,49152,53248,57344,61440,65536,69632,69632,69632,69632,69632,538976288,353703189,57,2693923072,80,7488,192,1056915199,16780300,50465804,576,36,128,1127088128,1126957056,1126825984,1107296256,1107296256,1107296256,1404288,1388160,1372032,57344,49152,40960,7489536,7403520,7317504,1106264873,1112032048,4294946764,30,1106264873,1112032048,4294946764,30,1106264873,1112032048,4294946764,30,
33984,33984,34176,34176,34368,34368,34560,34560,34560,34560,34560,34560,40960,44032,48128,51200,55296,58368,62464,65536,62464,65536,62464,65536,268439552,336991254,410,3223461888,80,7984,43008,1056914943,50465804,16780300,36,80,224,1127350272,1127350272,1127350272,1098907648,1098907648,0,135432,1073741824,6408,44032,44032,40960,722304,1073741824,34176,17201,1107575604,4294928940,4294967294,17201,1107575604,4294928940,4294967294,17201,1107575604,4294928940,4294967294,
8644608,8644608,8687616,8687616,8730624,8730624,8773632,8773632,8773632,8773632,8773632,8773632,40960,44032,48128,51200,55296,58368,62464,65536,62464,65536,62464,65536,268439552,336991254,57,3496611840,80,7488,192,1056915071,16780300,50465804,576,36,224,1128923136,1128923136,1128923136,1098907648,1098907648,0,1629504,1073741824,1628928,44032,44032,40960,8690688,1073741824,8687616,17225,1107575628,4294923564,4294967294,17225,1107575628,4294923564,4294967294,17225,1107575628,4294923564,4294967294,
2107392,2150400,2193408,2236416,2279424,2322432,2365440,2408448,2408448,2408448,2408448,2408448,40960,45056,49152,53248,57344,61440,65536,69632,69632,69632,69632,69632,538976288,353703189,57,817496320,80,7488,192,1040137983,16780300,50465804,576,36,128,1113063424,1112539136,1112014848,1107296256,1107296256,1107296256,436608,420480,404352,57344,49152,40960,2328576,2242560,2156544,1106264644,1112031840,4294962124,30,1106264644,1112031840,4294962124,30,1106264644,1112031840,4294962124,30,
2881536,2881536,2924544,2924544,2967552,2967552,3010560,3010560,3010560,3010560,3010560,3010560,40960,44032,48128,51200,55296,58368,62464,65536,62464,65536,62464,65536,268439552,336991254,57,1345982464,80,7488,192,1048526335,16780300,50465804,576,36,224,1116209152,1116209152,1116209152,1098907648,1098907648,0,548928,1073741824,548352,44032,44032,40960,2927616,1073741824,2924544,17030,1107575436,4294953580,4294967294,17030,1107575436,4294953580,4294967294,17030,1107575436,4294953580,4294967294,
17856,17856,18048,18048,18240,18240,18432,18432,18432,18432,18432,18432,40960,44032,48128,51200,55296,58368,62464,65536,62464,65536,62464,65536,268439552,336991254,410,1618616320,80,7984,43008,1048526463,50465804,16780300,36,80,224,1119617024,1119617024,1119617024,1098907648,1098907648,0,132408,1073741824,3384,44032,44032,40960,706176,1073741824,18048,17082,1107575488,4294947756,4294967294,17082,1107575488,4294947756,4294967294,17082,1107575488,4294947756,4294967294,
774144,817152,860160,903168,946176,989184,8730624,8773632,8773632,8773632,8773632,8773632,49152,53248,57344,61440,65536,69632,40960,45056,45056,45056,45056,45056,1886388224,320017685,57,1891774464,80,7488,192,1048526591,16780300,50465804,576,36,128,1128923136,1128923136,1101004800,1121976320,1121976320,0,1632960,1073741824,161280,32768,32768,53248,8709120,1073741824,860160,1121862465,1124090700,4294941932,110,1121862465,1124090700,4294941932,110,16784,1099973048,4294966540,4294967294,
25536,25536,25728,25728,25920,25920,26112,26112,26112,26112,26112,26112,40960,44032,48128,51200,55296,58368,62464,65536,62464,65536,62464,65536,268439552,336991254,410,2421301248,80,7984,43008,1056915071,50465804,16780300,36,80,224,1124466688,1124466688,1124466688,1098907648,1098907648,0,133848,1073741824,4824,44032,44032,40960,713856,1073741824,25728,17157,1107575560,4294938796,4294967294,17157,1107575560,4294938796,4294967294,17157,1107575560,4294938796,4294967294,
6752256,6752256,6795264,6795264,6838272,6838272,6881280,6881280,6881280,6881280,6881280,6881280,40960,44032,48128,51200,55296,58368,62464,65536,62464,65536,62464,65536,268439552,336991254,57,2694451200,80,7488,192,1056915199,16780300,50465804,576,36,224,1126039552,1126039552,1126039552,1098907648,1098907648,0,1274688,1073741824,1274112,44032,44032,40960,6798336,1073741824,6795264,17181,1107575584,4294933420,4294967294,17181,1107575584,4294933420,4294967294,17181,1107575584,4294933420,4294967294,
34368,34368,34560,34560,34752,34752,34944,34944,34944,34944,34944,34944,40960,44032,48128,51200,55296,58368,62464,65536,62464,65536,62464,65536,268439552,336991254,410,3223986176,80,7984,43008,1056914943,50465804,16780300,36,80,224,1127481344,1127481344,1127481344,1098907648,1098907648,0,135504,1073741824,6480,44032,44032,40960,722688,1073741824,34560,17203,1107575606,4294928492,4294967294,17203,1107575606,4294928492,4294967294,17203,1107575606,4294928492,4294967294,
8730624,8730624,8773632,8773632,8816640,8816640,8859648,8859648,8859648,8859648,8859648,8859648,40960,44032,48128,51200,55296,58368,62464,65536,62464,65536,62464,65536,268439552,336991254,57,3497136128,80,7488,192,1056915071,16780300,50465804,576,36,224,1129054208,1129054208,1129054208,1098907648,1098907648,0,1645632,1073741824,1645056,44032,44032,40960,8776704,1073741824,8773632,17227,1107575630,4294923116,4294967294,17227,1107575630,4294923116,4294967294,17227,1107575630,4294923116,4294967294,
7872,7872,8064,8064,8256,8256,8448,8448,8448,8448,8448,8448,40960,44032,48128,51200,55296,58368,62464,65536,62464,65536,62464,65536,268439552,336991254,410,818028544,80,7984,43008,1040137983,50465804,16780300,36,80,224,1109917696,1109917696,1109917696,1098907648,1098907648,0,130536,1073741824,1512,44032,44032,40960,696192,1073741824,8064,16932,1107575344,4294959404,4294967294,16932,1107575344,4294959404,4294967294,16932,1107575344,4294959404,4294967294,
2967552,2967552,3010560,3010560,3053568,3053568,3096576,3096576,3096576,3096576,3096576,3096576,40960,44032,48128,51200,55296,58368,62464,65536,62464,65536,62464,65536,268439552,336991254,57,1346506752,80,7488,192,1048526463,16780300,50465804,576,36,224,1116471296,1116471296,1116471296,1098907648,1098907648,0,565056,1073741824,564480,44032,44032,40960,3013632,1073741824,3010560,17034,1107575440,4294953132,4294967294,17034,1107575440,4294953132,4294967294,17034,1107575440,4294953132,4294967294,
18240,18240,18432,18432,18624,18624,18816,18816,18816,18816,18816,18816,40960,44032,48128,51200,55296,58368,62464,65536,62464,65536,62464,65536,268439552,336991254,410,1619140608,80,7984,43008,1048526591,50465804,16780300,36,80,224,1119879168,1119879168,1119879168,1098907648,1098907648,0,132480,1073741824,3456,44032,44032,40960,706560,1073741824,18432,17086,1107575492,4294947308,4294967294,17086,1107575492,4294947308,4294967294,17086,1107575492,4294947308,4294967294,
860160,903168,946176,989184,1032192,1075200,9160704,9203712,9203712,9203712,9203712,9203712,49152,53248,57344,61440,65536,69632,40960,45056,45056,45056,45056,45056,1886388224,320017685,57,1892429824,80,7488,192,1048526591,16780300,50465804,576,36,128,1129578496,1129578496,1102053376,1121976320,1121976320,0,1713600,1073741824,177408,32768,32768,53248,9139200,1073741824,946176,1121862475,1124090710,4294940652,110,1121862475,1124090710,4294940652,110,16800,1099973064,4294966284,4294967294,
25920,25920,26112,26112,26304,26304,26496,26496,26496,26496,26496,26496,40960,44032,48128,51200,55296,58368,62464,65536,62464,65536,62464,65536,268439552,336991254,410,2421825536,80,7984,43008,1056915071,50465804,16780300,36,80,224,1124597760,1124597760,1124597760,1098907648,1098907648,0,133920,1073741824,4896,44032,44032,40960,714240,1073741824,26112,17159,1107575562,4294938348,4294967294,17159,1107575562,4294938348,4294967294,17159,1107575562,4294938348,4294967294,
6838272,6838272,6881280,6881280,6924288,6924288,6967296,6967296,6967296,6967296,6967296,6967296,40960,44032,48128,51200,55296,58368,62464,65536,62464,65536,62464,65536,268439552,336991254,57,2694975488,80,7488,192,1056915199,16780300,50465804,576,36,224,1126170624,1126170624,1126170624,1098907648,1098907648,0,1290816,1073741824,1290240,44032,44032,40960,6884352,1073741824,6881280,17183,1107575586,4294932972,4294967294,17183,1107575586,4294932972,4294967294,17183,1107575586,4294932972,4294967294,
34752,34752,34944,34944,35136,35136,35328,35328,35328,35328,35328,35328,40960,44032,48128,51200,55296,58368,62464,65536,62464,65536,62464,65536,268439552,336991254,410,3224510464,80,7984,43008,1056914943,50465804,16780300,36,80,224,1127612416,1127612416,1127612416,1098907648,1098907648,0,135576,1073741824,6552,44032,44032,40960,723072,1073741824,34944,17205,1107575608,4294928044,4294967294,17205,1107575608,4294928044,4294967294,17205,1107575608,4294928044,4294967294,
8816640,8816640,8859648,8859648,8902656,8902656,8945664,8945664,8945664,8945664,8945664,8945664,40960,44032,48128,51200,55296,58368,62464,65536,62464,65536,62464,65536,268439552,336991254,57,3497660416,80,7488,192,1056915071,16780300,50465804,576,36,224,1129185280,1129185280,1129185280,1098907648,1098907648,0,1661760,1073741824,1661184,44032,44032,40960,8862720,1073741824,8859648,17229,1107575632,4294922668,4294967294,17229,1107575632,4294922668,4294967294,17229,1107575632,4294922668,4294967294,
8256,8256,8448,8448,8640,8640,8832,8832,8832,8832,8832,8832,40960,44032,48128,51200,55296,58368,62464,65536,62464,65536,62464,65536,268439552,336991254,410,818552832,80,7984,43008,1040137983,50465804,16780300,36,80,224,1110441984,1110441984,1110441984,1098907648,1098907648,0,130608,1073741824,1584,44032,44032,40960,696576,1073741824,8448,16940,1107575352,4294958956,4294967294,16940,1107575352,4294958956,4294967294,16940,1107575352,4294958956,4294967294,
3397632,3440640,3483648,3526656,3569664,3612672,3655680,3698688,3698688,3698688,3698688,3698688,40960,45056,49152,53248,57344,61440,65536,69632,69632,69632,69632,69632,538976288,353703189,57,1347027200,80,7488,192,1048526463,16780300,50465804,576,36,128,1118306304,1118044160,1117782016,1107296256,1107296256,1107296256,678528,662400,646272,57344,49152,40960,3618816,3532800,3446784,1106264734,1112031916,4294958284,30,1106264734,1112031916,4294958284,30,1106264734,1112031916,4294958284,30,
18624,18624,18816,18816,19008,19008,19200,19200,19200,19200,19200,19200,40960,44032,48128,51200,55296,58368,62464,65536,62464,65536,62464,65536,268439552,336991254,410,1619664896,80,7984,43008,1048526591,50465804,16780300,36,80,224,1120141312,1120141312,1120141312,1098907648,1098907648,0,132552,1073741824,3528,44032,44032,40960,706944,1073741824,18816,17090,1107575496,4294946860,4294967294,17090,1107575496,4294946860,4294967294,17090,1107575496,4294946860,4294967294,
946176,989184,1032192,1075200,1118208,1161216,9590784,9590784,9590784,9590784,9590784,9590784,49152,53248,57344,61440,65536,69632,40960,45056,45056,45056,45056,45056,1886388224,320017685,57,1893085184,80,7488,192,1048526591,16780300,50465804,576,36,128,1130233856,1130233856,1103101952,1121976320,1121976320,0,1794240,1073741824,193536,32768,32768,53248,9569280,1073741824,1032192,1121862485,1124090719,4294939372,110,1121862485,1124090719,4294939372,110,16816,1099973080,4294966028,4294967294,
26304,26304,26496,26496,26688,26688,26880,26880,26880,26880,26880,26880,40960,44032,48128,51200,55296,58368,62464,65536,62464,65536,62464,65536,268439552,336991254,410,2422349824,80,7984,43008,1056915071,50465804,16780300,36,80,224,1124728832,1124728832,1124728832,1098907648,1098907648,0,133992,1073741824,4968,44032,44032,40960,714624,1073741824,26496,17161,1107575564,4294937900,4294967294,17161,1107575564,4294937900,4294967294,17161,1107575564,4294937900,4294967294,
6924288,6924288,6967296,6967296,7010304,7010304,7053312,7053312,7053312,7053312,7053312,7053312,40960,44032,48128,51200,55296,58368,62464,65536,62464,65536,62464,65536,268439552,336991254,57,2695499776,80,7488,192,1056915199,16780300,50465804,576,36,224,1126301696,1126301696,1126301696,1098907648,1098907648,0,1306944,1073741824,1306368,44032,44032,40960,6970368,1073741824,6967296,17185,1107575588,4294932524,4294967294,17185,1107575588,4294932524,4294967294,17185,1107575588,4294932524,4294967294,
35136,35136,35328,35328,35520,35520,35712,35712,35712,35712,35712,35712,40960,44032,48128,51200,55296,58368,62464,65536,62464,65536,62464,65536,268439552,336991254,410,3225034752,80,7984,43008,1056914943,50465804,16780300,36,80,224,1127743488,1127743488,1127743488,1098907648,1098907648,0,135648,1073741824,6624,44032,44032,40960,723456,1073741824,35328,17207,1107575610,4294927596,4294967294,17207,1107575610,4294927596,4294967294,17207,1107575610,4294927596,4294967294,
8902656,8902656,8945664,8945664,8988672,8988672,9031680,9031680,9031680,9031680,9031680,9031680,40960,44032,48128,51200,55296,58368,62464,65536,62464,65536,62464,65536,268439552,336991254,57,3498184704,80,7488,192,1056915199,16780300,50465804,576,36,224,1129316352,1129316352,1129316352,1098907648,1098907648,0,1677888,1073741824,1677312,44032,44032,40960,8948736,1073741824,8945664,17231,1107575634,4294922220,4294967294,17231,1107575634,4294922220,4294967294,17231,1107575634,4294922220,4294967294,
8640,8640,8832,8832,9024,9024,9216,9216,9216,9216,9216,9216,40960,44032,48128,51200,55296,58368,62464,65536,62464,65536,62464,65536,268439552,336991254,410,819077120,80,7984,43008,1040137983,50465804,16780300,36,80,224,1110966272,1110966272,1110966272,1098907648,1098907648,0,130680,1073741824,1656,44032,44032,40960,696960,1073741824,8832,16948,1107575360,4294958508,4294967294,16948,1107575360,4294958508,4294967294,16948,1107575360,4294958508,4294967294,
3053568,3053568,3096576,3096576,3139584,3139584,3182592,3182592,3182592,3182592,3182592,3182592,40960,44032,48128,51200,55296,58368,62464,65536,62464,65536,62464,65536,268439552,336991254,57,1347555328,80,7488,192,1048526463,16780300,50465804,576,36,224,1116733440,1116733440,1116733440,1098907648,1098907648,0,581184,1073741824,580608,44032,44032,40960,3099648,1073741824,3096576,17038,1107575444,4294952684,4294967294,17038,1107575444,4294952684,4294967294,17038,1107575444,4294952684,4294967294,
19008,19008,19200,19200,19392,19392,19584,19584,19584,19584,19584,19584,40960,44032,48128,51200,55296,58368,62464,65536,62464,65536,62464,65536,268439552,336991254,410,1620189184,80,7984,43008,1048526591,50465804,16780300,36,80,224,1120403456,1120403456,1120403456,1098907648,1098907648,0,132624,1073741824,3600,44032,44032,40960,707328,1073741824,19200,17094,1107575500,4294946412,4294967294,17094,1107575500,4294946412,4294967294,17094,1107575500,4294946412,4294967294,
5462016,5505024,5548032,5591040,5634048,5677056,5720064,5763072,5763072,5763072,5763072,5763072,40960,45056,49152,53248,57344,61440,65536,69632,69632,69632,69632,69632,538976288,353703189,57,2149712128,80,7488,192,1048526335,16780300,50465804,576,36,128,1124335616,1124204544,1124073472,1107296256,1107296256,1107296256,1065600,1049472,1033344,57344,49152,40960,5683200,5597184,5511168,1106264830,1112032006,4294952140,30,1106264830,1112032006,4294952140,30,1106264830,1112032006,4294952140,30,
26688,26688,26880,26880,27072,27072,27264,27264,27264,27264,27264,27264,40960,44032,48128,51200,55296,58368,62464,65536,62464,65536,62464,65536,268439552,336991254,410,2422874112,80,7984,43008,1056915071,50465804,16780300,36,80,224,1124859904,1124859904,1124859904,1098907648,1098907648,0,134064,1073741824,5040,44032,44032,40960,715008,1073741824,26880,17163,1107575566,4294937452,4294967294,17163,1107575566,4294937452,4294967294,17163,1107575566,4294937452,4294967294,
7010304,7010304,7053312,7053312,7096320,7096320,7139328,7139328,7139328,7139328,7139328,7139328,40960,44032,48128,51200,55296,58368,62464,65536,62464,65536,62464,65536,268439552,336991254,57,2696024064,80,7488,192,1056915199,16780300,50465804,576,36,224,1126432768,1126432768,1126432768,1098907648,1098907648,0,1323072,1073741824,1322496,44032,44032,40960,7056384,1073741824,7053312,17187,1107575590,4294932076,4294967294,17187,1107575590,4294932076,4294967294,17187,1107575590,4294932076,4294967294,
8300544,8343552,8386560,8429568,8472576,8515584,8558592,8601600,8601600,8601600,8601600,8601600,40960,45056,49152,53248,57344,61440,65536,69632,69632,69632,69632,69632,538976288,353703189,57,3225551104,80,7488,192,1056915071,16780300,50465804,576,36,128,1128660992,1128529920,1128398848,1107296256,1107296256,1107296256,1597824,1581696,1565568,57344,49152,40960,8521728,8435712,8349696,1106264897,1112032072,4294943692,30,1106264897,1112032072,4294943692,30,1106264897,1112032072,4294943692,30,
8988672,8988672,9031680,9031680,9074688,9074688,9117696,9117696,9117696,9117696,9117696,9117696,40960,44032,48128,51200,55296,58368,62464,65536,62464,65536,62464,65536,268439552,336991254,57,3498708992,80,7488,192,1056915199,16780300,50465804,576,36,224,1129447424,1129447424,1129447424,1098907648,1098907648,0,1694016,1073741824,1693440,44032,44032,40960,9034752,1073741824,9031680,17233,1107575636,4294921772,4294967294,17233,1107575636,4294921772,4294967294,17233,1107575636,4294921772,4294967294,
9024,9024,9216,9216,9408,9408,9600,9600,9600,9600,9600,9600,40960,44032,48128,51200,55296,58368,62464,65536,62464,65536,62464,65536,268439552,336991254,410,819601408,80,7984,43008,1040137983,50465804,16780300,36,80,224,1111490560,1111490560,1111490560,1098907648,1098907648,0,130752,1073741824,1728,44032,44032,40960,697344,1073741824,9216,16956,1107575368,4294958060,4294967294,16956,1107575368,4294958060,4294967294,16956,1107575368,4294958060,4294967294,
3139584,3139584,3182592,3182592,3225600,3225600,3268608,3268608,3268608,3268608,3268608,3268608,40960,44032,48128,51200,55296,58368,62464,65536,62464,65536,62464,65536,268439552,336991254,57,1348079616,80,7488,192,1048526463,16780300,50465804,576,36,224,1116995584,1116995584,1116995584,1098907648,1098907648,0,597312,1073741824,596736,44032,44032,40960,3185664,1073741824,3182592,17042,1107575448,4294952236,4294967294,17042,1107575448,4294952236,4294967294,17042,1107575448,4294952236,4294967294,
19392,19392,19584,19584,19776,19776,19968,19968,19968,19968,19968,19968,40960,44032,48128,51200,55296,58368,62464,65536,62464,65536,62464,65536,268439552,336991254,410,1620713472,80,7984,43008,1048526591,50465804,16780300,36,80,224,1120665600,1120665600,1120665600,1098907648,1098907648,0,132696,1073741824,3672,44032,44032,40960,707712,1073741824,19584,17098,1107575504,4294945964,4294967294,17098,1107575504,4294945964,4294967294,17098,1107575504,4294945964,4294967294,
5031936,5031936,5074944,5074944,5117952,5117952,5160960,5160960,5160960,5160960,5160960,5160960,40960,44032,48128,51200,55296,58368,62464,65536,62464,65536,62464,65536,268439552,336991254,57,2150240256,80,7488,192,1048526335,16780300,50465804,576,36,224,1122762752,1122762752,1122762752,1098907648,1098907648,0,952128,1073741824,951552,44032,44032,40960,5078016,1073741824,5074944,17130,1107575536,4294942380,4294967294,17130,1107575536,4294942380,4294967294,17130,1107575536,4294942380,4294967294,
27072,27072,27264,27264,27456,27456,27648,27648,27648,27648,27648,27648,40960,44032,48128,51200,55296,58368,62464,65536,62464,65536,62464,65536,268439552,336991254,410,2423398400,80,7984,43008,1056915071,50465804,16780300,36,80,224,1124990976,1124990976,1124990976,1098907648,1098907648,0,134136,1073741824,5112,44032,44032,40960,715392,1073741824,27264,17165,1107575568,4294937004,4294967294,17165,1107575568,4294937004,4294967294,17165,1107575568,4294937004,4294967294,
7096320,7096320,7139328,7139328,7182336,7182336,7225344,7225344,7225344,7225344,7225344,7225344,40960,44032,48128,51200,55296,58368,62464,65536,62464,65536,62464,65536,268439552,336991254,57,2696548352,80,7488,192,1056915199,16780300,50465804,576,36,224,1126563840,1126563840,1126563840,1098907648,1098907648,0,1339200,1073741824,1338624,44032,44032,40960,7142400,1073741824,7139328,17189,1107575592,4294931628,4294967294,17189,1107575592,4294931628,4294967294,17189,1107575592,4294931628,4294967294,
35520,35520,35712,35712,35904,35904,36096,36096,36096,36096,36096,36096,40960,44032,48128,51200,55296,58368,62464,65536,62464,65536,62464,65536,268439552,336991254,410,3226083328,80,7984,43008,1056915071,50465804,16780300,36,80,224,1127874560,1127874560,1127874560,1098907648,1098907648,0,135720,1073741824,6696,44032,44032,40960,723840,1073741824,35712,17209,1107575612,4294927148,4294967294,17209,1107575612,4294927148,4294967294,17209,1107575612,4294927148,4294967294,
9074688,9074688,9117696,9117696,9160704,9160704,9203712,9203712,9203712,9203712,9203712,9203712,40960,44032,48128,51200,55296,58368,62464,65536,62464,65536,62464,65536,268439552,336991254,57,3499233280,80,7488,192,1056915199,16780300,50465804,576,36,224,1129578496,1129578496,1129578496,1098907648,1098907648,0,1710144,1073741824,1709568,44032,44032,40960,9120768,1073741824,9117696,17235,1107575638,4294921324,4294967294,17235,1107575638,4294921324,4294967294,17235,1107575638,4294921324,4294967294,
9408,9408,9600,9600,9792,9792,9984,9984,9984,9984,9984,9984,40960,44032,48128,51200,55296,58368,62464,65536,62464,65536,62464,65536,268439552,336991254,410,1075978240,80,7984,43008,1040137727,50465804,16780300,36,80,224,1112014848,1112014848,1112014848,1098907648,1098907648,0,130824,1073741824,1800,44032,44032,40960,697728,1073741824,9600,16964,1107575376,4294957612,4294967294,16964,1107575376,4294957612,4294967294,16964,1107575376,4294957612,4294967294,
3225600,3225600,3268608,3268608,3311616,3311616,3354624,3354624,3354624,3354624,3354624,3354624,40960,44032,48128,51200,55296,58368,62464,65536,62464,65536,62464,65536,268439552,336991254,57,1348603904,80,7488,192,1048526463,16780300,50465804,576,36,224,1117257728,1117257728,1117257728,1098907648,1098907648,0,613440,1073741824,612864,44032,44032,40960,3271680,1073741824,3268608,17046,1107575452,4294951788,4294967294,17046,1107575452,4294951788,4294967294,17046,1107575452,4294951788,4294967294,
19776,19776,19968,19968,20160,20160,20352,20352,20352,20352,20352,20352,40960,44032,48128,51200,55296,58368,62464,65536,62464,65536,62464,65536,268439552,336991254,410,1621237760,80,7984,43008,1048526591,50465804,16780300,36,80,224,1120927744,1120927744,1120927744,1098907648,1098907648,0,132768,1073741824,3744,44032,44032,40960,708096,1073741824,19968,17102,1107575508,4294945516,4294967294,17102,1107575508,4294945516,4294967294,17102,1107575508,4294945516,4294967294,
5117952,5117952,5160960,5160960,5203968,5203968,5246976,5246976,5246976,5246976,5246976,5246976,40960,44032,48128,51200,55296,58368,62464,65536,62464,65536,62464,65536,268439552,336991254,57,2150764544,80,7488,192,1048526335,16780300,50465804,576,36,224,1123024896,1123024896,1123024896,1098907648,1098907648,0,968256,1073741824,967680,44032,44032,40960,5164032,1073741824,5160960,17134,1107575540,4294941932,4294967294,17134,1107575540,4294941932,4294967294,17134,1107575540,4294941932,4294967294,
6494208,6537216,6580224,6623232,6666240,6709248,6752256,6795264,6795264,6795264,6795264,6795264,40960,45056,49152,53248,57344,61440,65536,69632,69632,69632,69632,69632,538976288,353703189,57,2423914752,80,7488,192,1056915071,16780300,50465804,576,36,128,1125908480,1125777408,1125646336,1107296256,1107296256,1107296256,1259136,1243008,1226880,57344,49152,40960,6715392,6629376,6543360,1106264855,1112032030,4294949068,30,1106264855,1112032030,4294949068,30,1106264855,1112032030,4294949068,30,
7182336,7182336,7225344,7225344,7268352,7268352,7311360,7311360,7311360,7311360,7311360,7311360,40960,44032,48128,51200,55296,58368,62464,65536,62464,65536,62464,65536,268439552,336991254,57,2697072640,80,7488,192,1056915199,16780300,50465804,576,36,224,1126694912,1126694912,1126694912,1098907648,1098907648,0,1355328,1073741824,1354752,44032,44032,40960,7228416,1073741824,7225344,17191,1107575594,4294931180,4294967294,17191,1107575594,4294931180,4294967294,17191,1107575594,4294931180,4294967294,
35904,35904,36096,36096,36288,36288,36480,36480,36480,36480,36480,36480,40960,44032,48128,51200,55296,58368,62464,65536,62464,65536,62464,65536,268439552,336991254,410,3226607616,80,7984,43008,1056915071,50465804,16780300,36,80,224,1128005632,1128005632,1128005632,1098907648,1098907648,0,135792,1073741824,6768,44032,44032,40960,724224,1073741824,36096,17211,1107575614,4294926700,4294967294,17211,1107575614,4294926700,4294967294,17211,1107575614,4294926700,4294967294,
9160704,9160704,9203712,9203712,9246720,9246720,9289728,9289728,9289728,9289728,9289728,9289728,40960,44032,48128,51200,55296,58368,62464,65536,62464,65536,62464,65536,268439552,336991254,57,3499757568,80,7488,192,1056915199,16780300,50465804,576,36,224,1129709568,1129709568,1129709568,1098907648,1098907648,0,1726272,1073741824,1725696,44032,44032,40960,9206784,1073741824,9203712,17237,1107575640,4294920876,4294967294,17237,1107575640,4294920876,4294967294,17237,1107575640,4294920876,4294967294,
2365440,2408448,2451456,2494464,2537472,2580480,2623488,2666496,2666496,2666496,2666496,2666496,40960,45056,49152,53248,57344,61440,65536,69632,69632,69632,69632,69632,538976288,353703189,57,1076494592,80,7488,192,1040137727,16780300,50465804,576,36,128,1114636288,1114112000,1113587712,1107296256,1107296256,1107296256,484992,468864,452736,57344,49152,40960,2586624,2500608,2414592,1106264668,1112031864,4294961356,30,1106264668,1112031864,4294961356,30,1106264668,1112031864,4294961356,30,
3311616,3311616,3354624,3354624,3397632,3397632,3440640,3440640,3440640,3440640,3440640,3440640,40960,44032,48128,51200,55296,58368,62464,65536,62464,65536,62464,65536,268439552,336991254,57,1349128192,80,7488,192,1048526463,16780300,50465804,576,36,224,1117519872,1117519872,1117519872,1098907648,1098907648,0,629568,1073741824,628992,44032,44032,40960,3357696,1073741824,3354624,17050,1107575456,4294951340,4294967294,17050,1107575456,4294951340,4294967294,17050,1107575456,4294951340,4294967294,
4687872,4730880,4773888,4816896,4859904,4902912,4945920,4988928,4988928,4988928,4988928,4988928,40960,45056,49152,53248,57344,61440,65536,69632,69632,69632,69632,69632,538976288,353703189,57,1621754112,80,7488,192,1048526591,16780300,50465804,576,36,128,1122238464,1121976320,1121714176,1107296256,1107296256,1107296256,920448,904320,888192,57344,49152,40960,4909056,4823040,4737024,1106264794,1112031976,4294954444,30,1106264794,1112031976,4294954444,30,1106264794,1112031976,4294954444,30,
5203968,5203968,5246976,5246976,5289984,5289984,5332992,5332992,5332992,5332992,5332992,5332992,40960,44032,48128,51200,55296,58368,62464,65536,62464,65536,62464,65536,268439552,336991254,57,2151288832,80,7488,192,1048526335,16780300,50465804,576,36,224,1123287040,1123287040,1123287040,1098907648,1098907648,0,984384,1073741824,983808,44032,44032,40960,5250048,1073741824,5246976,17138,1107575544,4294941484,4294967294,17138,1107575544,4294941484,4294967294,17138,1107575544,4294941484,4294967294,
27456,27456,27648,27648,27840,27840,28032,28032,28032,28032,28032,28032,40960,44032,48128,51200,55296,58368,62464,65536,62464,65536,62464,65536,268439552,336991254,410,2424446976,80,7984,43008,1056915199,50465804,16780300,36,80,224,1125122048,1125122048,1125122048,1098907648,1098907648,0,134208,1073741824,5184,44032,44032,40960,715776,1073741824,27648,17167,1107575570,4294936556,4294967294,17167,1107575570,4294936556,4294967294,17167,1107575570,4294936556,4294967294,
258048,301056,344064,387072,430080,473088,9074688,9117696,9117696,9117696,9117696,9117696,49152,53248,57344,61440,65536,69632,40960,45056,45056,45056,45056,45056,2694840320,320017685,57,2697605632,80,7488,192,1056915199,16780300,50465804,576,36,128,1129447424,1129447424,1090519040,1126170624,1126170624,0,1699200,1073741824,64512,32768,32768,53248,9062400,1073741824,344064,1126122313,1127236436,4294940620,158,1126122313,1127236436,4294940620,158,16576,1099972912,780,4294967294,
36288,36288,36480,36480,36672,36672,36864,36864,36864,36864,36864,36864,40960,44032,48128,51200,55296,58368,62464,65536,62464,65536,62464,65536,268439552,336991254,410,3227131904,80,7984,43008,1056915071,50465804,16780300,36,80,224,1128136704,1128136704,1128136704,1098907648,1098907648,0,135864,1073741824,6840,44032,44032,40960,724608,1073741824,36480,17213,1107575616,4294926252,4294967294,17213,1107575616,4294926252,4294967294,17213,1107575616,4294926252,4294967294,
9332736,9375744,9418752,9461760,9504768,9547776,9590784,9590784,9590784,9590784,9590784,9590784,40960,45056,49152,53248,57344,61440,65536,69632,69632,69632,69632,69632,538976288,353703189,57,3500278016,80,7488,192,1056915199,16780300,50465804,576,36,128,1130233856,1130102784,1129971712,1107296256,1107296256,1107296256,1791360,1775232,1759104,57344,49152,40960,9553920,9467904,9381888,1106264921,1112032095,4294940620,30,1106264921,1112032095,4294940620,30,1106264921,1112032095,4294940620,30,
9792,9792,9984,9984,10176,10176,10368,10368,10368,10368,10368,10368,40960,44032,48128,51200,55296,58368,62464,65536,62464,65536,62464,65536,268439552,336991254,410,1077026816,80,7984,43008,1040137727,50465804,16780300,36,80,224,1112539136,1112539136,1112539136,1098907648,1098907648,0,130896,1073741824,1872,44032,44032,40960,698112,1073741824,9984,16972,1107575384,4294957164,4294967294,16972,1107575384,4294957164,4294967294,16972,1107575384,4294957164,4294967294,
3397632,3397632,3440640,3440640,3483648,3483648,3526656,3526656,3526656,3526656,3526656,3526656,40960,44032,48128,51200,55296,58368,62464,65536,62464,65536,62464,65536,268439552,336991254,57,1349652480,80,7488,192,1048526463,16780300,50465804,576,36,224,1117782016,1117782016,1117782016,1098907648,1098907648,0,645696,1073741824,645120,44032,44032,40960,3443712,1073741824,3440640,17054,1107575460,4294950892,4294967294,17054,1107575460,4294950892,4294967294,17054,1107575460,4294950892,4294967294,
20160,20160,20352,20352,20544,20544,20736,20736,20736,20736,20736,20736,40960,44032,48128,51200,55296,58368,62464,65536,62464,65536,62464,65536,268439552,336991254,410,1622286336,80,7984,43008,1048526591,50465804,16780300,36,80,224,1121189888,1121189888,1121189888,1098907648,1098907648,0,132840,1073741824,3816,44032,44032,40960,708480,1073741824,20352,17106,1107575512,4294945068,4294967294,17106,1107575512,4294945068,4294967294,17106,1107575512,4294945068,4294967294,
5289984,5289984,5332992,5332992,5376000,5376000,5419008,5419008,5419008,5419008,5419008,5419008,40960,44032,48128,51200,55296,58368,62464,65536,62464,65536,62464,65536,268439552,336991254,57,2151813120,80,7488,192,1048526463,16780300,50465804,576,36,224,1123549184,1123549184,1123549184,1098907648,1098907648,0,1000512,1073741824,999936,44032,44032,40960,5336064,1073741824,5332992,17142,1107575548,4294941036,4294967294,17142,1107575548,4294941036,4294967294,17142,1107575548,4294941036,4294967294,
27840,27840,28032,28032,28224,28224,28416,28416,28416,28416,28416,28416,40960,44032,48128,51200,55296,58368,62464,65536,62464,65536,62464,65536,268439552,336991254,410,2424971264,80,7984,43008,1056915199,50465804,16780300,36,80,224,1125253120,1125253120,1125253120,1098907648,1098907648,0,134280,1073741824,5256,44032,44032,40960,716160,1073741824,28032,17169,1107575572,4294936108,4294967294,17169,1107575572,4294936108,4294967294,17169,1107575572,4294936108,4294967294,
1290240,1333248,1376256,1419264,1462272,9504768,9547776,9590784,9590784,9590784,9590784,9590784,57344,61440,65536,69632,73728,40960,45056,49152,53248,53248,53248,53248,2694881440,353703189,57,2698249472,64,7488,192,1056915199,16780300,50465804,576,36,128,1130102784,1130233856,1107296256,1126170624,1126170624,1126170624,1779840,1795968,263808,32768,40960,61440,9492480,9578496,1406976,1126122323,1127236447,4294939340,158,1126122323,1127236447,4294939340,158,1126056432,1127367176,4294964300,158,
36672,36672,36864,36864,37056,37056,37248,37248,37248,37248,37248,37248,40960,44032,48128,51200,55296,58368,62464,65536,62464,65536,62464,65536,268439552,336991254,410,3227656192,80,7984,43008,1056915071,50465804,16780300,36,80,224,1128267776,1128267776,1128267776,1098907648,1098907648,0,135936,1073741824,6912,44032,44032,40960,724992,1073741824,36864,17215,1107575618,4294925804,4294967294,17215,1107575618,4294925804,4294967294,17215,1107575618,4294925804,4294967294,
9246720,9246720,9289728,9289728,9332736,9332736,9375744,9375744,9375744,9375744,9375744,9375744,40960,44032,48128,51200,55296,58368,62464,65536,62464,65536,62464,65536,268439552,336991254,57,3500806144,80,7488,192,1056915199,16780300,50465804,576,36,224,1129840640,1129840640,1129840640,1098907648,1098907648,0,1742400,1073741824,1741824,44032,44032,40960,9292800,1073741824,9289728,17239,1107575642,4294920428,4294967294,17239,1107575642,4294920428,4294967294,17239,1107575642,4294920428,4294967294,
10176,10176,10368,10368,10560,10560,10752,10752,10752,10752,10752,10752,40960,44032,48128,51200,55296,58368,62464,65536,62464,65536,62464,65536,268439552,336991254,410,1077551104,80,7984,43008,1040137727,50465804,16780300,36,80,224,1113063424,1113063424,1113063424,1098907648,1098907648,0,130968,1073741824,1944,44032,44032,40960,698496,1073741824,10368,16980,1107575392,4294956716,4294967294,16980,1107575392,4294956716,4294967294,16980,1107575392,4294956716,4294967294,
3483648,3483648,3526656,3526656,3569664,3569664,3612672,3612672,3612672,3612672,3612672,3612672,40960,44032,48128,51200,55296,58368,62464,65536,62464,65536,62464,65536,268439552,336991254,57,1350176768,80,7488,192,1048526463,16780300,50465804,576,36,224,1118044160,1118044160,1118044160,1098907648,1098907648,0,661824,1073741824,661248,44032,44032,40960,3529728,1073741824,3526656,17058,1107575464,4294950444,4294967294,17058,1107575464,4294950444,4294967294,17058,1107575464,4294950444,4294967294,
20544,20544,20736,20736,20928,20928,21120,21120,21120,21120,21120,21120,40960,44032,48128,51200,55296,58368,62464,65536,62464,65536,62464,65536,268439552,336991254,410,1622810624,80,7984,43008,1048526591,50465804,16780300,36,80,224,1121452032,1121452032,1121452032,1098907648,1098907648,0,132912,1073741824,3888,44032,44032,40960,708864,1073741824,20736,17110,1107575516,4294944620,4294967294,17110,1107575516,4294944620,4294967294,17110,1107575516,4294944620,4294967294,
5376000,5376000,5419008,5419008,5462016,5462016,5505024,5505024,5505024,5505024,5505024,5505024,40960,44032,48128,51200,55296,58368,62464,65536,62464,65536,62464,65536,268439552,336991254,57,2152337408,80,7488,192,1048526463,16780300,50465804,576,36,224,1123811328,1123811328,1123811328,1098907648,1098907648,0,1016640,1073741824,1016064,44032,44032,40960,5422080,1073741824,5419008,17146,1107575552,4294940588,4294967294,17146,1107575552,4294940588,4294967294,17146,1107575552,4294940588,4294967294,
28224,28224,28416,28416,28608,28608,28800,28800,28800,28800,28800,28800,40960,44032,48128,51200,55296,58368,62464,65536,62464,65536,62464,65536,268439552,336991254,410,2425495552,80,7984,43008,1056915199,50465804,16780300,36,80,224,1125384192,1125384192,1125384192,1098907648,1098907648,0,134352,1073741824,5328,44032,44032,40960,716544,1073741824,28416,17171,1107575574,4294935660,4294967294,17171,1107575574,4294935660,4294967294,17171,1107575574,4294935660,4294967294,
7268352,7268352,7311360,7311360,7354368,7354368,7397376,7397376,7397376,7397376,7397376,7397376,40960,44032,48128,51200,55296,58368,62464,65536,62464,65536,62464,65536,268439552,336991254,57,2955022336,80,7488,192,1056914943,16780300,50465804,576,36,224,1126825984,1126825984,1126825984,1098907648,1098907648,0,1371456,1073741824,1370880,44032,44032,40960,7314432,1073741824,7311360,17193,1107575596,4294930732,4294967294,17193,1107575596,4294930732,4294967294,17193,1107575596,4294930732,4294967294,
37056,37056,37248,37248,37440,37440,37632,37632,37632,37632,37632,37632,40960,44032,48128,51200,55296,58368,62464,65536,62464,65536,62464,65536,268439552,336991254,410,3228180480,80,7984,43008,1056915071,50465804,16780300,36,80,224,1128398848,1128398848,1128398848,1098907648,1098907648,0,136008,1073741824,6984,44032,44032,40960,725376,1073741824,37248,17217,1107575620,4294925356,4294967294,17217,1107575620,4294925356,4294967294,17217,1107575620,4294925356,4294967294,
9332736,9332736,9375744,9375744,9418752,9418752,9461760,9461760,9461760,9461760,9461760,9461760,40960,44032,48128,51200,55296,58368,62464,65536,62464,65536,62464,65536,268439552,336991254,57,3501330432,80,7488,192,1056915199,16780300,50465804,576,36,224,1129971712,1129971712,1129971712,1098907648,1098907648,0,1758528,1073741824,1757952,44032,44032,40960,9378816,1073741824,9375744,17241,1107575644,4294919980,4294967294,17241,1107575644,4294919980,4294967294,17241,1107575644,4294919980,4294967294,
10560,10560,10752,10752,10944,10944,11136,11136,11136,11136,11136,11136,40960,44032,48128,51200,55296,58368,62464,65536,62464,65536,62464,65536,268439552,336991254,410,1078075392,80,7984,43008,1040137855,50465804,16780300,36,80,224,1113587712,1113587712,1113587712,1098907648,1098907648,0,131040,1073741824,2016,44032,44032,40960,698880,1073741824,10752,16988,1107575400,4294956268,4294967294,16988,1107575400,4294956268,4294967294,16988,1107575400,4294956268,4294967294,
3655680,3698688,3741696,3784704,3827712,3870720,3913728,3956736,3956736,3956736,3956736,3956736,40960,45056,49152,53248,57344,61440,65536,69632,69632,69632,69632,69632,538976288,353703189,57,1350697216,80,7488,192,1048526591,16780300,50465804,576,36,128,1119092736,1118830592,1118568448,1107296256,1107296256,1107296256,726912,710784,694656,57344,49152,40960,3876864,3790848,3704832,1106264746,1112031928,4294957516,30,1106264746,1112031928,4294957516,30,1106264746,1112031928,4294957516,30,
0,0,0,43008,86016,129024,8730624,8773632,8773632,8773632,8773632,8773632,49152,53248,57344,61440,65536,69632,40960,45056,45056,45056,45056,45056,1616904192,320017685,57,1623339520,80,7488,192,1048526591,16780300,50465804,576,36,128,1128923136,1128923136,0,1119879168,1119879168,0,1632384,1073741824,0,32768,32768,53248,8706048,1073741824,0,1119765313,1121993548,4294942028,94,1119765313,1121993548,4294942028,94,0,1099972672,1804,4294967294,
5462016,5462016,5505024,5505024,5548032,5548032,5591040,5591040,5591040,5591040,5591040,5591040,40960,44032,48128,51200,55296,58368,62464,65536,62464,65536,62464,65536,268439552,336991254,57,2152861696,80,7488,192,1048526463,16780300,50465804,576,36,224,1124073472,1124073472,1124073472,1098907648,1098907648,0,1032768,1073741824,1032192,44032,44032,40960,5508096,1073741824,5505024,17150,1107575554,4294940140,4294967294,17150,1107575554,4294940140,4294967294,17150,1107575554,4294940140,4294967294,
28608,28608,28800,28800,28992,28992,29184,29184,29184,29184,29184,29184,40960,44032,48128,51200,55296,58368,62464,65536,62464,65536,62464,65536,268439552,336991254,410,2426019840,80,7984,43008,1056915199,50465804,16780300,36,80,224,1125515264,1125515264,1125515264,1098907648,1098907648,0,134424,1073741824,5400,44032,44032,40960,716928,1073741824,28800,17173,1107575576,4294935212,4294967294,17173,1107575576,4294935212,4294967294,17173,1107575576,4294935212,4294967294,
7526400,7569408,7612416,7655424,7698432,7741440,7784448,7827456,7827456,7827456,7827456,7827456,40960,45056,49152,53248,57344,61440,65536,69632,69632,69632,69632,69632,538976288,353703189,57,2955542784,80,7488,192,1056914943,16780300,50465804,576,36,128,1127481344,1127350272,1127219200,1107296256,1107296256,1107296256,1452672,1436544,1420416,57344,49152,40960,7747584,7661568,7575552,1106264879,1112032054,4294945996,30,1106264879,1112032054,4294945996,30,1106264879,1112032054,4294945996,30,
37440,37440,37632,37632,37824,37824,38016,38016,38016,38016,38016,38016,40960,44032,48128,51200,55296,58368,62464,65536,62464,65536,62464,65536,268439552,336991254,410,3228704768,80,7984,43008,1056915071,50465804,16780300,36,80,224,1128529920,1128529920,1128529920,1098907648,1098907648,0,136080,1073741824,7056,44032,44032,40960,725760,1073741824,37632,17219,1107575622,4294924908,4294967294,17219,1107575622,4294924908,4294967294,17219,1107575622,4294924908,4294967294,
9418752,9418752,9461760,9461760,9504768,9504768,9547776,9547776,9547776,9547776,9547776,9547776,40960,44032,48128,51200,55296,58368,62464,65536,62464,65536,62464,65536,268439552,336991254,57,3501854720,80,7488,192,1056915199,16780300,50465804,576,36,224,1130102784,1130102784,1130102784,1098907648,1098907648,0,1774656,1073741824,1774080,44032,44032,40960,9464832,1073741824,9461760,17243,1107575646,4294919532,4294967294,17243,1107575646,4294919532,4294967294,17243,1107575646,4294919532,4294967294,
10944,10944,11136,11136,11328,11328,11520,11520,11520,11520,11520,11520,40960,44032,48128,51200,55296,58368,62464,65536,62464,65536,62464,65536,268439552,336991254,410,1078599680,80,7984,43008,1040137855,50465804,16780300,36,80,224,1114112000,1114112000,1114112000,1098907648,1098907648,0,131112,1073741824,2088,44032,44032,40960,699264,1073741824,11136,16996,1107575408,4294955820,4294967294,16996,1107575408,4294955820,4294967294,16996,1107575408,4294955820,4294967294,
3569664,3569664,3612672,3612672,3655680,3655680,3698688,3698688,3698688,3698688,3698688,3698688,40960,44032,48128,51200,55296,58368,62464,65536,62464,65536,62464,65536,268439552,336991254,57,1351225344,80,7488,192,1048526591,16780300,50465804,576,36,224,1118306304,1118306304,1118306304,1098907648,1098907648,0,677952,1073741824,677376,44032,44032,40960,3615744,1073741824,3612672,17062,1107575468,4294949996,4294967294,17062,1107575468,4294949996,4294967294,17062,1107575468,4294949996,4294967294,
602112,645120,688128,731136,774144,817152,9160704,9203712,9203712,9203712,9203712,9203712,49152,53248,57344,61440,65536,69632,40960,45056,45056,45056,45056,45056,1616904192,320017685,57,1623994368,80,7488,192,1048526591,16780300,50465804,576,36,128,1129578496,1129578496,1098907648,1119879168,1119879168,0,1713024,1073741824,129024,32768,32768,53248,9136128,1073741824,688128,1119765323,1121993558,4294940748,94,1119765323,1121993558,4294940748,94,16736,1099973016,4294967052,4294967294,
5720064,5763072,5806080,5849088,5892096,5935104,5978112,6021120,6021120,6021120,6021120,6021120,40960,45056,49152,53248,57344,61440,65536,69632,69632,69632,69632,69632,538976288,353703189,57,2153382144,80,7488,192,1048526463,16780300,50465804,576,36,128,1124728832,1124597760,1124466688,1107296256,1107296256,1107296256,1113984,1097856,1081728,57344,49152,40960,5941248,5855232,5769216,1106264837,1112032012,4294951372,30,1106264837,1112032012,4294951372,30,1106264837,1112032012,4294951372,30,
28992,28992,29184,29184,29376,29376,29568,29568,29568,29568,29568,29568,40960,44032,48128,51200,55296,58368,62464,65536,62464,65536,62464,65536,268439552,336991254,410,2426544128,80,7984,43008,1056915199,50465804,16780300,36,80,224,1125646336,1125646336,1125646336,1098907648,1098907648,0,134496,1073741824,5472,44032,44032,40960,717312,1073741824,29184,17175,1107575578,4294934764,4294967294,17175,1107575578,4294934764,4294967294,17175,1107575578,4294934764,4294967294,
7354368,7354368,7397376,7397376,7440384,7440384,7483392,7483392,7483392,7483392,7483392,7483392,40960,44032,48128,51200,55296,58368,62464,65536,62464,65536,62464,65536,268439552,336991254,57,2956070912,80,7488,192,1056914943,16780300,50465804,576,36,224,1126957056,1126957056,1126957056,1098907648,1098907648,0,1387584,1073741824,1387008,44032,44032,40960,7400448,1073741824,7397376,17195,1107575598,4294930284,4294967294,17195,1107575598,4294930284,4294967294,17195,1107575598,4294930284,4294967294,
37824,37824,38016,38016,38208,38208,38400,38400,38400,38400,38400,38400,40960,44032,48128,51200,55296,58368,62464,65536,62464,65536,62464,65536,268439552,336991254,410,3229229056,80,7984,43008,1056915071,50465804,16780300,36,80,224,1128660992,1128660992,1128660992,1098907648,1098907648,0,136152,1073741824,7128,44032,44032,40960,726144,1073741824,38016,17221,1107575624,4294924460,4294967294,17221,1107575624,4294924460,4294967294,17221,1107575624,4294924460,4294967294,
9504768,9504768,9547776,9547776,9590784,9590784,9590784,9590784,9590784,9590784,9590784,9590784,40960,44032,48128,51200,55296,58368,62464,65536,62464,65536,62464,65536,268439552,336991254,57,3502379008,80,7488,192,1056915199,16780300,50465804,576,36,224,1130233856,1130233856,1130233856,1098907648,1098907648,0,1790784,1073741824,1790208,44032,44032,40960,9550848,1073741824,9547776,17245,1107575647,4294919084,4294967294,17245,1107575647,4294919084,4294967294,17245,1107575647,4294919084,4294967294,
11328,11328,11520,11520,11712,11712,11904,11904,11904,11904,11904,11904,40960,44032,48128,51200,55296,58368,62464,65536,62464,65536,62464,65536,268439552,336991254,410,1079123968,80,7984,43008,1040137855,50465804,16780300,36,80,224,1114636288,1114636288,1114636288,1098907648,1098907648,0,131184,1073741824,2160,44032,44032,40960,699648,1073741824,11520,17004,1107575416,4294955372,4294967294,17004,1107575416,4294955372,4294967294,17004,1107575416,4294955372,4294967294,
3655680,3655680,3698688,3698688,3741696,3741696,3784704,3784704,3784704,3784704,3784704,3784704,40960,44032,48128,51200,55296,58368,62464,65536,62464,65536,62464,65536,268439552,336991254,57,1351749632,80,7488,192,1048526591,16780300,50465804,576,36,224,1118568448,1118568448,1118568448,1098907648,1098907648,0,694080,1073741824,693504,44032,44032,40960,3701760,1073741824,3698688,17066,1107575472,4294949548,4294967294,17066,1107575472,4294949548,4294967294,17066,1107575472,4294949548,4294967294,
688128,731136,774144,817152,860160,903168,9590784,9590784,9590784,9590784,9590784,9590784,49152,53248,57344,61440,65536,69632,40960,45056,45056,45056,45056,45056,1616904192,320017685,57,1624649728,80,7488,192,1048526591,16780300,50465804,576,36,128,1130233856,1130233856,1099956224,1119879168,1119879168,0,1793664,1073741824,145152,32768,32768,53248,9566208,1073741824,774144,1119765333,1121993567,4294939468,94,1119765333,1121993567,4294939468,94,16768,1099973032,4294966796,4294967294,
5548032,5548032,5591040,5591040,5634048,5634048,5677056,5677056,5677056,5677056,5677056,5677056,40960,44032,48128,51200,55296,58368,62464,65536,62464,65536,62464,65536,268439552,336991254,57,2153910272,80,7488,192,1048526463,16780300,50465804,576,36,224,1124204544,1124204544,1124204544,1098907648,1098907648,0,1048896,1073741824,1048320,44032,44032,40960,5594112,1073741824,5591040,17153,1107575556,4294939692,4294967294,17153,1107575556,4294939692,4294967294,17153,1107575556,4294939692,4294967294,
29376,29376,29568,29568,29760,29760,29952,29952,29952,29952,29952,29952,40960,44032,48128,51200,55296,58368,62464,65536,62464,65536,62464,65536,268439552,336991254,410,2427068416,80,7984,43008,1056915199,50465804,16780300,36,80,224,1125777408,1125777408,1125777408,1098907648,1098907648,0,134568,1073741824,5544,44032,44032,40960,717696,1073741824,29568,17177,1107575580,4294934316,4294967294,17177,1107575580,4294934316,4294967294,17177,1107575580,4294934316,4294967294,
7440384,7440384,7483392,7483392,7526400,7526400,7569408,7569408,7569408,7569408,7569408,7569408,40960,44032,48128,51200,55296,58368,62464,65536,62464,65536,62464,65536,268439552,336991254,57,2956595200,80,7488,192,1056914943,16780300,50465804,576,36,224,1127088128,1127088128,1127088128,1098907648,1098907648,0,1403712,1073741824,1403136,44032,44032,40960,7486464,1073741824,7483392,17197,1107575600,4294929836,4294967294,17197,1107575600,4294929836,4294967294,17197,1107575600,4294929836,4294967294,
8558592,8601600,8644608,8687616,8730624,8773632,8816640,8859648,8859648,8859648,8859648,8859648,40960,45056,49152,53248,57344,61440,65536,69632,69632,69632,69632,69632,538976288,353703189,57,3229745408,80,7488,192,1056915199,16780300,50465804,576,36,128,1129054208,1128923136,1128792064,1107296256,1107296256,1107296256,1646208,1630080,1613952,57344,49152,40960,8779776,8693760,8607744,1106264903,1112032078,4294942924,30,1106264903,1112032078,4294942924,30,1106264903,1112032078,4294942924,30,
516096,559104,602112,645120,688128,731136,9074688,9117696,9117696,9117696,9117696,9117696,49152,53248,57344,61440,65536,69632,40960,45056,45056,45056,45056,45056,3503292416,320017685,57,3502912000,80,7488,192,1056915199,16780300,50465804,576,36,128,1129447424,1129447424,1096810496,1129316352,1129316352,0,1700928,1073741824,112896,32768,32768,53248,9071616,1073741824,602112,1129268041,1130316628,4294940332,206,1129268041,1130316628,4294940332,206,16704,1099973000,12,4294967294,
11712,11712,11904,11904,12096,12096,12288,12288,12288,12288,12288,12288,40960,44032,48128,51200,55296,58368,62464,65536,62464,65536,62464,65536,268439552,336991254,410,1079648256,80,7984,43008,1040137855,50465804,16780300,36,80,224,1115160576,1115160576,1115160576,1098907648,1098907648,0,131256,1073741824,2232,44032,44032,40960,700032,1073741824,11904,17012,1107575424,4294954924,4294967294,17012,1107575424,4294954924,4294967294,17012,1107575424,4294954924,4294967294,
3741696,3741696,3784704,3784704,3827712,3827712,3870720,3870720,3870720,3870720,3870720,3870720,40960,44032,48128,51200,55296,58368,62464,65536,62464,65536,62464,65536,268439552,336991254,57,1352273920,80,7488,192,1048526591,16780300,50465804,576,36,224,1118830592,1118830592,1118830592,1098907648,1098907648,0,710208,1073741824,709632,44032,44032,40960,3787776,1073741824,3784704,17070,1107575476,4294949100,4294967294,17070,1107575476,4294949100,4294967294,17070,1107575476,4294949100,4294967294,
20928,20928,21120,21120,21312,21312,21504,21504,21504,21504,21504,21504,40960,44032,48128,51200,55296,58368,62464,65536,62464,65536,62464,65536,268439552,336991254,410,1881284608,80,7984,43008,1048526335,50465804,16780300,36,80,224,1121714176,1121714176,1121714176,1098907648,1098907648,0,132984,1073741824,3960,44032,44032,40960,709248,1073741824,21120,17114,1107575520,4294944172,4294967294,17114,1107575520,4294944172,4294967294,17114,1107575520,4294944172,4294967294,
5634048,5634048,5677056,5677056,5720064,5720064,5763072,5763072,5763072,5763072,5763072,5763072,40960,44032,48128,51200,55296,58368,62464,65536,62464,65536,62464,65536,268439552,336991254,57,2154434560,80,7488,192,1048526463,16780300,50465804,576,36,224,1124335616,1124335616,1124335616,1098907648,1098907648,0,1065024,1073741824,1064448,44032,44032,40960,5680128,1073741824,5677056,17155,1107575558,4294939244,4294967294,17155,1107575558,4294939244,4294967294,17155,1107575558,4294939244,4294967294,
29760,29760,29952,29952,30144,30144,30336,30336,30336,30336,30336,30336,40960,44032,48128,51200,55296,58368,62464,65536,62464,65536,62464,65536,268439552,336991254,410,2427592704,80,7984,43008,1056915199,50465804,16780300,36,80,224,1125908480,1125908480,1125908480,1098907648,1098907648,0,134640,1073741824,5616,44032,44032,40960,718080,1073741824,29952,17179,1107575582,4294933868,4294967294,17179,1107575582,4294933868,4294967294,17179,1107575582,4294933868,4294967294,
7526400,7526400,7569408,7569408,7612416,7612416,7655424,7655424,7655424,7655424,7655424,7655424,40960,44032,48128,51200,55296,58368,62464,65536,62464,65536,62464,65536,268439552,336991254,57,2957119488,80,7488,192,1056915071,16780300,50465804,576,36,224,1127219200,1127219200,1127219200,1098907648,1098907648,0,1419840,1073741824,1419264,44032,44032,40960,7572480,1073741824,7569408,17199,1107575602,4294929388,4294967294,17199,1107575602,4294929388,4294967294,17199,1107575602,4294929388,4294967294,
38208,38208,38400,38400,38592,38592,38784,38784,38784,38784,38784,38784,40960,44032,48128,51200,55296,58368,62464,65536,62464,65536,62464,65536,268439552,336991254,410,3230277632,80,7984,43008,1056915199,50465804,16780300,36,80,224,1128792064,1128792064,1128792064,1098907648,1098907648,0,136224,1073741824,7200,44032,44032,40960,726528,1073741824,38400,17223,1107575626,4294924012,4294967294,17223,1107575626,4294924012,4294967294,17223,1107575626,4294924012,4294967294,
1290240,1333248,1376256,1419264,1462272,9504768,9547776,9590784,9590784,9590784,9590784,9590784,57344,61440,65536,69632,73728,40960,45056,49152,53248,53248,53248,53248,3503345872,353703189,57,3503555840,64,7488,192,1056915199,16780300,50465804,576,36,128,1130102784,1130233856,1107296256,1129316352,1129316352,1129316352,1781568,1797696,265536,32768,40960,61440,9501696,9587712,1416192,1129268051,1130316639,4294939052,206,1129268051,1130316639,4294939052,206,1129202160,1130316296,4294964012,206};
__device__ __forceinline__ float bf_lo(unsigned u) { return __uint_as_float(u << 16); }
__device__ __forceinline__ float bf_hi(unsigned u) { return __uint_as_float(u & 0xffff0000u); }
__device__ __forceinline__ unsigned cvt_pk(float lo, float hi) { bf16x2 r = {(__bf16)lo, (__bf16)hi}; return __builtin_bit_cast(unsigned, r); }
__device__ __forceinline__ float rb(float v) { return bf_lo(cvt_pk(v, 0.f)); }
__global__ __launch_bounds__(256) __attribute__((aligned(4096))) void dcn_kernel(const unsigned short* x_, const unsigned short* off_, const unsigned short* w_, unsigned short* out_, void* ws_) {
    __shared__ __attribute__((aligned(128))) unsigned char lds[163840];
    asm volatile("" ::"v"((__attribute__((address_space(3))) unsigned char*)lds));
    unsigned tid = threadIdx.x, bid = blockIdx.x;
    const __attribute__((address_space(4))) Args* ka = (const __attribute__((address_space(4))) Args*)__builtin_amdgcn_kernarg_segment_ptr();
    asm volatile(
        "s_load_dwordx8 s[48:55], %1, 0\n\t"
        "v_mov_b32 v156, %0\n\t"
        "s_mov_b32 s42, %2\n\t"
        "s_getpc_b64 s[44:45]\n\t"
        "s_and_b32 s44, s44, 0xffffff00\n\t"
        "9:\n\t"
        "s_mov_b32 s46, 99f-9b+3300\n\t"
        "s_mov_b32 s47, 0x20000\n\t"
        "v_lshlrev_b32 v160, 12, v156\n\t"
        "s_lshl_b32 s43, s42, 4\n\t"
        "s_and_b32 s43, s43, 3968\n\t"
        "s_mov_b32 m0, 161856\n\t"
        "v_cmp_gt_u32 vcc, 64, v156\n\t"
        "s_nop 1\n\t"
        "s_cbranch_vccz 10f\n\t"
        "buffer_load_dword v160, s[44:47], s43 offen lds\n\t"
        "10:\n\t"
        "s_mov_b64 s[64:65], %3\n\t"
        "v_and_b32 v157, 63, v156\n\t"
        "s_mov_b32 s66, 65536\n\t"
        "s_mov_b32 s67, 0x20000\n\t"
        "v_lshlrev_b32 v155, 2, v157\n\t"
        "s_lshl_b32 s70, s42, 8\n\t"
        "buffer_load_dword v155, v155, s[64:67], s70 offen\n\t"
        "v_lshrrev_b32 v73, 6, v156\n\t"
        "v_and_b32 v157, 63, v156\n\t"
        "v_readfirstlane_b32 s4, v73\n\t"
        "v_lshlrev_b32 v140, 4, v157\n\t"
        "s_mov_b32 s26, 165888\n\t"
        "s_mov_b32 s27, 0x20000\n\t"
        "v_xor_b32 v142, 32, v140\n\t"
        "v_and_b32 v73, 32, v157\n\t"
        "v_xor_b32 v141, v73, v140\n\t"
        "s_lshr_b32 s68, s4, 1\n\t"
        "s_mul_i32 s37, s4, 3072\n\t"
        "s_mul_i32 s36, s68, -61440\n\t"
        "s_add_u32 s36, s36, s37\n\t"
        "s_add_u32 s36, s36, 55296\n\t"
        "s_add_u32 s37, s37, 86016\n\t"
        "s_and_b32 s8, s42, 7\n\t"
        "s_lshl_b32 s8, s8, 5\n\t"
        "s_lshr_b32 s10, s42, 3\n\t"
        "s_add_u32 s10, s10, s8\n\t"
        "s_mul_i32 s8, s10, 2731\n\t"
        "s_sub_u32 s11, s10, 3\n\t"
        "s_mul_i32 s11, s11, 2850\n\t"
        "s_max_i32 s8, s8, s11\n\t"
        "s_lshr_b32 s8, s8, 16\n\t"
        "v_mul_u32_u24 v71, 171, v156\n\t"
        "v_lshrrev_b32 v71, 11, v71\n\t"
        "v_mul_u32_u24 v74, 12, v71\n\t"
        "v_sub_u32 v74, v156, v74\n\t"
        "s_mul_i32 s11, s8, 23\n\t"
        "s_sub_u32 s10, s10, s11\n\t"
        "s_min_u32 s11, s8, 3\n\t"
        "s_sub_u32 s10, s10, s11\n\t"
        "s_lshl_b32 s6, s8, 4\n\t"
        "s_add_u32 s6, s6, 48\n\t"
        "v_lshrrev_b32 v75, 1, v71\n\t"
        "v_and_b32 v75, 2, v75\n\t"
        "v_xor_b32 v74, v74, v75\n\t"
        "v_lshlrev_b32 v72, 4, v74\n\t"
        "s_lshl_b32 s8, s8, 1\n\t"
        "s_lshr_b32 s8, 1396799, s8\n\t"
        "s_and_b32 s8, s8, 3\n\t"
        "s_sub_u32 s8, s10, s8\n\t"
        "s_max_i32 s8, s8, 20\n\t"
        "s_lshl2_add_u32 s8, s10, s8\n\t"
        "s_lshl1_add_u32 s8, s8, -6\n\t"
        "v_add_u32 v73, -1, v71\n\t"
        "v_cmp_lt_u32 vcc, 17, v73\n\t"
        "v_mov_b32 v75, 0x7ff00000\n\t"
        "v_add3_u32 v161, v71, s6, -2\n\t"
        "v_cndmask_b32 v162, v72, v75, vcc\n\t"
        "s_movk_i32 s9, 192\n\t"
        "s_lshl_b32 s35, s4, 10\n\t"
        "s_mov_b32 s18, 9633792\n\t"
        "s_mov_b32 s19, 0x20000\n\t"
        "v_min_u32 v161, 223, v161\n\t"
        "s_mul_i32 s34, s8, 43008\n\t"
        "v_mad_u32_u24 v143, v161, s9, v162\n\t"
        "s_sub_u32 s34, s34, 43008\n\t"
        "s_lshr_b32 s12, s42, 3\n\t"
        "s_lshl_b32 s12, s12, 2\n\t"
        "s_add_u32 s12, s12, s4\n\t"
        "s_mul_i32 s12, s12, 1296\n\t"
        "s_and_b32 s12, s12, 0xffffff80\n\t"
        "v_min_u32 v164, 11, v157\n\t"
        "v_lshlrev_b32 v164, 7, v164\n\t"
        "s_mul_i32 s39, s68, 6144\n\t"
        "s_add_u32 s39, s39, 55296\n\t"
        "s_mul_i32 s38, s68, 159744\n\t"
        "s_add_u32 s38, s38, -104448\n\t"
        "s_mul_i32 s5, s68, -165888\n\t"
        "s_add_u32 s5, s5, 61440\n\t"
        "s_mov_b32 s22, 1806336\n\t"
        "s_mov_b32 s23, 0x20000\n\t"
        "s_mov_b32 s30, 9633792\n\t"
        "s_mov_b32 s31, 0x20000\n\t"
        "s_lshl_b32 s10, s4, 2\n\t"
        "s_add_u32 s10, s10, 161792\n\t"
        "v_mov_b32 v153, s10\n\t"
        "v_mov_b32 v182, 161792\n\t"
        "v_mov_b32 v76, 0\n\t"
        "v_mov_b32 v77, 0\n\t"
        "v_lshlrev_b32 v73, 3, v156\n\t"
        "v_add_u32 v73, 147456, v73\n\t"
        "s_add_u32 s60, s35, 153600\n\t"
        "v_mov_b32 v74, s60\n\t"
        "s_cmp_lt_u32 s4, 3\n\t"
        "s_cselect_b64 exec, -1, 0\n\t"
        "ds_write_b64 v73, v[76:77] offset:0\n\t"
        "ds_write_b64 v73, v[76:77] offset:2048\n\t"
        "ds_write_b64 v73, v[76:77] offset:4096\n\t"
        "s_mov_b64 exec, 1\n\t"
        "ds_write_b32 v153, v76 offset:16\n\t"
        "ds_write_b64 v74, v[76:77]\n\t"
        "s_mov_b64 exec, -1\n\t"
        "s_waitcnt lgkmcnt(0)\n\t"
        "s_mov_b64 s[24:25], s[52:53]\n\t"
        "s_mov_b64 s[16:17], s[48:49]\n\t"
        "s_add_u32 m0, s37, -36864\n\t"
        "s_nop 0\n\t"
        "buffer_load_dwordx4 v140, s[24:27], s36 offen offset:0 lds\n\t"
        "buffer_load_dwordx4 v141, s[24:27], s36 offen offset:1024 lds\n\t"
        "buffer_load_dwordx4 v142, s[24:27], s36 offen offset:2048 lds\n\t"
        "buffer_load_dword v165, v164, s[24:27], s12 offen\n\t"
        "s_add_u32 m0, s35, 8192\n\t"
        "s_add_u32 s43, s34, 86016\n\t"
        "buffer_load_dwordx4 v143, s[16:19], s43 offen  lds\n\t"
        "s_add_u32 m0, s35, 24576\n\t"
        "s_add_u32 s43, s34, 258048\n\t"
        "buffer_load_dwordx4 v143, s[16:19], s43 offen  lds\n\t"
        "s_mov_b64 s[20:21], s[50:51]\n\t"
        "s_mov_b64 s[28:29], s[54:55]\n\t"
        "s_mul_i32 s41, s8, 224\n\t"
        "s_add_u32 s41, s41, s6\n\t"
        "v_and_b32 v158, 15, v156\n\t"
        "s_and_b32 s49, s4, 1\n\t"
        "s_lshl_b32 s8, s49, 5\n\t"
        "v_xor_b32 v152, s8, v140\n\t"
        "s_mul_i32 s43, s4, 1536\n\t"
        "s_add_u32 m0, s43, 43008\n\t"
        "s_add_u32 s43, s43, 110592\n\t"
        "buffer_load_dwordx4 v152, s[24:27], s43 offen lds\n\t"
        "buffer_load_dwordx4 v152, s[24:27], s43 offen offset:512 lds\n\t"
        "s_add_u32 m0, s35, 12288\n\t"
        "s_add_u32 s43, s34, 129024\n\t"
        "buffer_load_dwordx4 v143, s[16:19], s43 offen  lds\n\t"
        "s_add_u32 m0, s35, 28672\n\t"
        "s_add_u32 s43, s34, 301056\n\t"
        "s_min_u32 s43, s43, 9590784\n\t"
        "buffer_load_dwordx4 v143, s[16:19], s43 offen  lds\n\t"
        "v_bfe_i32 v74, v156, 5, 1\n\t"
        "v_and_b32 v74, 7, v74\n\t"
        "v_xor_b32 v74, s4, v74\n\t"
        "v_mul_u32_u24 v68, 11, v74\n\t"
        "v_lshrrev_b32 v68, 5, v68\n\t"
        "v_mul_u32_u24 v75, 3, v68\n\t"
        "v_sub_u32 v69, v74, v75\n\t"
        "v_lshlrev_b32 v70, 2, v74\n\t"
        "v_mul_u32_u24 v74, 205, v156\n\t"
        "v_lshrrev_b32 v74, 14, v74\n\t"
        "v_min_u32 v74, 2, v74\n\t"
        "v_lshlrev_b32 v65, 2, v74\n\t"
        "v_mul_u32_u24 v74, 80, v74\n\t"
        "v_sub_u32 v66, v156, v74\n\t"
        "v_bfe_u32 v52, v156, 4, 1\n\t"
        "v_and_b32 v181, 48, v156\n\t"
        "s_mov_b64 s[56:57], 0\n\t"
        "s_mov_b32 s63, 0x435f0000\n\t"
        "s_mov_b32 s54, 0x7fff7fff\n\t"
        "v_bfe_u32 v159, v156, 4, 2\n\t"
        "v_mul_u32_u24 v62, 192, v158\n\t"
        "v_lshlrev_b32 v76, 3, v158\n\t"
        "v_and_b32 v77, 48, v76\n\t"
        "v_and_b32 v184, 24, v76\n\t"
        "s_mul_i32 s69, s68, 16384\n\t"
        "s_lshl_b32 s70, s49, 7\n\t"
        "s_lshl_b32 s71, s49, 5\n\t"
        "v_add_u32 v73, 8, v76\n\t"
        "v_and_b32 v73, 32, v73\n\t"
        "s_add_u32 s69, s69, 192\n\t"
        "v_xor_b32 v73, v181, v73\n\t"
        "v_add3_u32 v128, v73, v62, s69\n\t"
        "v_add_u32 v73, 16, v76\n\t"
        "v_and_b32 v73, 32, v73\n\t"
        "s_add_u32 s69, s69, 192\n\t"
        "v_xor_b32 v73, v181, v73\n\t"
        "v_add3_u32 v129, v73, v62, s69\n\t"
        "v_add_u32 v73, 24, v76\n\t"
        "v_and_b32 v73, 32, v73\n\t"
        "s_add_u32 s69, s69, 192\n\t"
        "v_xor_b32 v73, v181, v73\n\t"
        "v_add3_u32 v130, v73, v62, s69\n\t"
        "v_lshrrev_b32 v73, 2, v158\n\t"
        "v_lshl_add_u32 v74, v159, 3, v73\n\t"
        "v_mul_u32_u24 v74, 192, v74\n\t"
        "v_lshlrev_b32 v75, 1, v156\n\t"
        "v_and_b32 v75, 32, v75\n\t"
        "v_add3_u32 v74, v74, v75, v184\n\t"
        "v_add_u32 v74, 86016, v74\n\t"
        "v_xad_u32 v133, v74, s71, 64\n\t"
        "v_add_u32 v131, s70, v74\n\t"
        "v_xor_b32 v132, 32, v131\n\t"
        "v_subrev_u32 v138, 43008, v131\n\t"
        "v_subrev_u32 v139, 43008, v132\n\t"
        "v_subrev_u32 v149, 43008, v133\n\t"
        "v_lshrrev_b32 v74, 3, v157\n\t"
        "v_min_u32 v74, 2, v74\n\t"
        "v_lshlrev_b32 v81, 2, v74\n\t"
        "s_lshl_b32 s69, s4, 3\n\t"
        "v_and_or_b32 v82, v156, 7, s69\n\t"
        "v_mul_u32_u24 v80, 48, v82\n\t"
        "v_lshl_add_u32 v80, v74, 11, v80\n\t"
        "v_add_u32 v80, 147488, v80\n\t"
        "v_and_b32 v82, 15, v82\n\t"
        "v_and_b32 v74, 31, v156\n\t"
        "v_mad_u32_u24 v53, v74, 48, v70\n\t"
        "v_add_u32 v53, 147456, v53\n\t"
        "v_add_u32 v70, 157696, v70\n\t"
        "v_mul_u32_u24 v67, 320, v81\n\t"
        "s_add_u32 s69, s68, 1\n\t"
        "v_cvt_f32_u32 v56, s69\n\t"
        "v_add_u32 v57, 1, v82\n\t"
        "v_cvt_f32_u32 v57, v57\n\t"
        "v_lshrrev_b32 v83, 2, v157\n\t"
        "v_and_b32 v74, 3, v156\n\t"
        "v_lshlrev_b32 v119, 11, v83\n\t"
        "v_lshl_add_u32 v119, v74, 2, v119\n\t"
        "v_add_u32 v119, 122368, v119\n\t"
        "v_lshl_add_u32 v74, v74, 4, 64\n\t"
        "v_mad_u32_u24 v145, v83, s9, v74\n\t"
        "v_lshrrev_b32 v74, 1, v156\n\t"
        "v_and_or_b32 v74, v74, 16, v75\n\t"
        "v_add3_u32 v144, v74, v62, s70\n\t"
        "s_lshl_b32 s69, s68, 10\n\t"
        "s_add_u32 s69, s69, 135168\n\t"
        "v_add_u32 v74, v76, v159\n\t"
        "v_lshl_or_b32 v74, v74, 3, s71\n\t"
        "v_xad_u32 v146, v74, v77, s69\n\t"
        "s_lshl_b32 s70, s49, 11\n\t"
        "s_add_u32 s70, s70, s69\n\t"
        "v_lshlrev_b32 v74, 1, v156\n\t"
        "v_and_b32 v74, 48, v74\n\t"
        "v_xad_u32 v147, v74, v140, s70\n\t"
        "s_mul_i32 s48, s41, 36\n\t"
        "s_mul_i32 s41, s41, 192\n\t"
        "s_mul_i32 s69, s68, 172032\n\t"
        "s_add_u32 s41, s41, s69\n\t"
        "s_mul_i32 s69, s49, 43008\n\t"
        "s_add_u32 s50, s41, s69\n\t"
        "v_lshrrev_b32 v74, 5, v156\n\t"
        "v_mul_u32_u24 v74, 8064, v74\n\t"
        "v_and_b32 v55, 496, v140\n\t"
        "v_add_u32 v55, v55, v74\n\t"
        "v_lshl_add_u32 v61, v158, 2, v74\n\t"
        "s_mul_i32 s69, s68, 768\n\t"
        "s_add_u32 s69, s69, 147456\n\t"
        "v_mad_u32_u24 v187, v158, 48, s69\n\t"
        "v_lshl_add_u32 v74, v159, 3, v62\n\t"
        "v_xad_u32 v186, v74, v77, s37\n\t"
        "v_and_b32 v73, 2, v73\n\t"
        "v_add_u32 v73, v73, v159\n\t"
        "v_and_b32 v73, 3, v73\n\t"
        "v_and_or_b32 v73, v158, 12, v73\n\t"
        "s_add_u32 s69, s35, 157696\n\t"
        "v_lshl_add_u32 v185, v73, 4, s69\n\t"
        "v_add_u32 v118, s69, v140\n\t"
        "v_add_u32 v116, 1, v157\n\t"
        "v_mul_u32_u24 v74, 12288, v159\n\t"
        "v_add3_u32 v74, v74, v62, v77\n\t"
        "v_add_u32 v117, s37, v74\n\t"
        "v_lshlrev_b32 v73, 2, v158\n\t"
        "v_and_b32 v73, 16, v73\n\t"
        "s_mov_b32 s69, 0x0c0c0100\n\t"
        "v_alignbit_b32 v169, s69, s69, v73\n\t"
        "s_mov_b32 s69, 0x0c0c0302\n\t"
        "v_alignbit_b32 v171, s69, s69, v73\n\t"
        "v_mov_b32 v74, 0x0c0c0c0c\n\t"
        "s_mov_b32 s10, 2228241\n\t"
        "s_mov_b32 s11, 8912964\n\t"
        "v_cndmask_b32 v168, v74, v169, s[10:11]\n\t"
        "v_cndmask_b32 v170, v74, v171, s[10:11]\n\t"
        "s_mov_b32 s10, 2281718784\n\t"
        "s_mov_b32 s11, 570429696\n\t"
        "v_cndmask_b32 v169, v74, v169, s[10:11]\n\t"
        "v_cndmask_b32 v171, v74, v171, s[10:11]\n\t"
        "s_waitcnt vmcnt(10)\n\t"
        "v_readlane_b32 s7, v155, 27\n\t"
        "s_nop 1\n\t"
        "s_or_b32 s42, s42, s7\n\t"
        "s_bitcmp1_b32 s7, 8\n\t"
        "s_cbranch_scc1 20f\n\t"
        "s_bitcmp1_b32 s7, 9\n\t"
        "s_cbranch_scc1 21f\n\t"
        "ds_bpermute_b32 v64, v65, v155 offset:172\n\t"
        "s_add_u32 m0, s37, -24576\n\t"
        "s_add_u32 s36, s36, 6144\n\t"
        "buffer_load_dwordx4 v140, s[24:27], s36 offen offset:0 lds\n\t"
        "ds_bpermute_b32 v78, v81, v155 offset:148\n\t"
        "ds_bpermute_b32 v79, v81, v155 offset:160\n\t"
        "v_readlane_b32 s71, v155, 26\n\t"
        "v_readlane_b32 s10, v155, 29\n\t"
        "s_nop 0\n\t"
        "v_mul_u32_u24 v73, s71, v66\n\t"
        "v_lshrrev_b32 v73, 11, v73\n\t"
        "v_lshlrev_b32 v66, 4, v66\n\t"
        "v_mad_u32_u24 v73, v73, s10, v66\n\t"
        "s_waitcnt lgkmcnt(2)\n\t"
        "v_add_u32 v64, v64, v73\n\t"
        "buffer_load_dwordx4 v141, s[24:27], s36 offen offset:1024 lds\n\t"
        "v_readlane_b32 s69, v155, 34\n\t"
        "v_readlane_b32 s70, v155, 35\n\t"
        "s_nop 0\n\t"
        "v_mad_u32_u24 v70, v52, s69, v70\n\t"
        "v_mad_u32_u24 v70, v158, s70, v70\n\t"
        "s_mul_i32 s71, s69, s68\n\t"
        "s_add_u32 s71, s71, 157728\n\t"
        "v_mad_u32_u24 v67, v82, s70, v67\n\t"
        "v_add_u32 v67, s71, v67\n\t"
        "v_readlane_b32 s11, v155, 30\n\t"
        "buffer_load_dwordx4 v142, s[24:27], s36 offen offset:2048 lds\n\t"
        "s_movk_i32 s10, 223\n\t"
        "v_mov_b32 v75, 0x7ff00000\n\t"
        "v_readlane_b32 s12, v155, 24\n\t"
        "v_readlane_b32 s13, v155, 25\n\t"
        "s_nop 0\n\t"
        "s_bfe_u32 s70, s12, 524288\n\t"
        "s_bfe_u32 s71, s13, 524288\n\t"
        "v_cmp_gt_u32 vcc, s71, v71\n\t"
        "v_add3_u32 v73, v71, s70, -2\n\t"
        "v_med3_i32 v73, v73, 0, s10\n\t"
        "s_add_u32 m0, s35, 4096\n\t"
        "s_add_u32 s43, s34, 43008\n\t"
        "buffer_load_dwordx4 v143, s[16:19], s43 offen  lds\n\t"
        "v_cndmask_b32 v74, v75, v72, vcc\n\t"
        "v_mad_u32_u24 v134, v73, s11, v74\n\t"
        "s_bfe_u32 s70, s12, 524296\n\t"
        "s_bfe_u32 s71, s13, 524296\n\t"
        "v_cmp_gt_u32 vcc, s71, v71\n\t"
        "v_add3_u32 v73, v71, s70, -2\n\t"
        "v_med3_i32 v73, v73, 0, s10\n\t"
        "v_cndmask_b32 v74, v75, v72, vcc\n\t"
        "v_mad_u32_u24 v135, v73, s11, v74\n\t"
        "s_add_u32 m0, s35, 20480\n\t"
        "s_add_u32 s43, s34, 215040\n\t"
        "buffer_load_dwordx4 v143, s[16:19], s43 offen  lds\n\t"
        "s_bfe_u32 s70, s12, 524304\n\t"
        "s_bfe_u32 s71, s13, 524304\n\t"
        "v_cmp_gt_u32 vcc, s71, v71\n\t"
        "v_add3_u32 v73, v71, s70, -2\n\t"
        "v_med3_i32 v73, v73, 0, s10\n\t"
        "v_cndmask_b32 v74, v75, v72, vcc\n\t"
        "v_mad_u32_u24 v136, v73, s11, v74\n\t"
        "s_bfe_u32 s70, s12, 524312\n\t"
        "s_bfe_u32 s71, s13, 524312\n\t"
        "v_cmp_gt_u32 vcc, s71, v71\n\t"
        "v_add3_u32 v73, v71, s70, -2\n\t"
        "v_med3_i32 v73, v73, 0, s10\n\t"
        "v_cndmask_b32 v74, v75, v72, vcc\n\t"
        "v_mad_u32_u24 v137, v73, s11, v74\n\t"
        "s_mov_b64 exec, 0\n\t"
        "s_cmp_eq_u32 s4, 0\n\t"
        "s_cbranch_scc1 .LpM0%=\n\t"
        "s_cmp_eq_u32 s4, 1\n\t"
        "s_cbranch_scc1 .LpM1%=\n\t"
        "s_cmp_eq_u32 s4, 2\n\t"
        "s_cbranch_scc1 .LpM2%=\n\t"
        "s_branch .LwM%=\n\t"
        ".LrM%=:\n\t"
        "s_mov_b64 exec, -1\n\t"
        "s_waitcnt vmcnt(9)\n\t"
        "s_barrier\n\t"
        "ds_read_b64_tr_b16 a[0:1], v138 offset:6144\n\t"
        "ds_read_b64_tr_b16 a[2:3], v138 offset:6912\n\t"
        "ds_read_b64_tr_b16 a[4:5], v139 offset:6144\n\t"
        "ds_read_b64_tr_b16 a[6:7], v139 offset:6912\n\t"
        "ds_read_b64_tr_b16 a[8:9], v149 offset:6144\n\t"
        "ds_read_b64_tr_b16 a[10:11], v149 offset:6912\n\t"
        "ds_read_b128 v[84:87], v128 offset:8192\n\t"
        "ds_read_b64_tr_b16 a[12:13], v138 offset:12288\n\t"
        "ds_read_b64_tr_b16 a[14:15], v138 offset:13056\n\t"
        "ds_read_b64_tr_b16 a[16:17], v139 offset:12288\n\t"
        "ds_read_b64_tr_b16 a[18:19], v139 offset:13056\n\t"
        "ds_read_b64_tr_b16 a[20:21], v149 offset:12288\n\t"
        "ds_read_b64_tr_b16 a[22:23], v149 offset:13056\n\t"
        "ds_read_b128 v[88:91], v128 offset:8192\n\t"
        "s_add_u32 m0, s37, -12288\n\t"
        "s_add_u32 s36, s36, s39\n\t"
        "s_waitcnt lgkmcnt(7)\n\t"
        "v_mfma_f32_16x16x32_bf16 v[12:15], a[0:3], v[84:87], 0\n\t"
        "buffer_load_dwordx4 v140, s[24:27], s36 offen offset:0 lds\n\t"
        "buffer_load_dwordx4 v141, s[24:27], s36 offen offset:1024 lds\n\t"
        "buffer_load_dwordx4 v142, s[24:27], s36 offen offset:2048 lds\n\t"
        "s_add_u32 m0, s35, 16384\n\t"
        "s_add_u32 s43, s34, 172032\n\t"
        "buffer_load_dwordx4 v143, s[16:19], s43 offen  lds\n\t"
        "s_add_u32 m0, s35, 32768\n\t"
        "s_add_u32 s43, s34, 344064\n\t"
        "s_min_u32 s43, s43, 9590784\n\t"
        "buffer_load_dwordx4 v143, s[16:19], s43 offen  lds\n\t"
        "s_waitcnt vmcnt(10)\n\t"
        "s_barrier\n\t"
        "ds_read_b128 v[92:95], v128 offset:12288\n\t"
        "ds_read_b64_tr_b16 a[24:25], v138 offset:0\n\t"
        "v_mfma_f32_16x16x32_bf16 v[16:19], a[4:7], v[84:87], 0\n\t"
        "ds_read_b64_tr_b16 a[26:27], v138 offset:768\n\t"
        "s_add_u32 m0, s35, 157696\n\t"
        "s_nop 0\n\t"
        "buffer_load_dwordx4 v64, s[20:23], 0 offen lds\n\t"
        "s_add_u32 m0, s37, 0\n\t"
        "s_add_u32 s36, s36, s38\n\t"
        "v_mfma_f32_16x16x32_bf16 v[20:23], a[8:11], v[84:87], 0\n\t"
        "buffer_load_dwordx4 v140, s[24:27], s36 offen offset:0 lds\n\t"
        "ds_read_b64_tr_b16 a[28:29], v139 offset:0\n\t"
        "v_readlane_b32 s44, v155, 32\n\t"
        "s_waitcnt lgkmcnt(4)\n\t"
        "v_mfma_f32_16x16x32_bf16 v[24:27], a[12:15], v[88:91], 0\n\t"
        "ds_read_b64_tr_b16 a[30:31], v139 offset:768\n\t"
        "ds_read_b64_tr_b16 a[32:33], v149 offset:0\n\t"
        "ds_read_b64_tr_b16 a[34:35], v149 offset:768\n\t"
        "v_readlane_b32 s45, v155, 33\n\t"
        "v_mfma_f32_16x16x32_bf16 v[28:31], a[16:19], v[88:91], 0\n\t"
        "buffer_load_dwordx4 v141, s[24:27], s36 offen offset:1024 lds\n\t"
        "ds_read_b128 v[96:99], v128 offset:8192\n\t"
        "v_readlane_b32 s62, v155, 36\n\t"
        "v_mfma_f32_16x16x32_bf16 v[32:35], a[20:23], v[88:91], 0\n\t"
        "ds_read_b128 v[100:103], v128 offset:12288\n\t"
        "s_lshl_b32 s70, s62, 5\n\t"
        "s_waitcnt lgkmcnt(8)\n\t"
        "v_mfma_f32_16x16x32_bf16 v[24:27], a[0:3], v[92:95], v[24:27]\n\t"
        "s_waitcnt vmcnt(10)\n\t"
        "s_barrier\n\t"
        "ds_read_b64_tr_b16 a[36:37], v138 offset:18432\n\t"
        "ds_read_b64_tr_b16 a[38:39], v138 offset:19200\n\t"
        "v_mfma_f32_16x16x32_bf16 v[28:31], a[4:7], v[92:95], v[28:31]\n\t"
        "buffer_load_dwordx4 v142, s[24:27], s36 offen offset:2048 lds\n\t"
        "s_add_u32 m0, s35, 0\n\t"
        "ds_read_b64_tr_b16 a[40:41], v139 offset:18432\n\t"
        "s_add_u32 s43, s34, 0\n\t"
        "v_mfma_f32_16x16x32_bf16 v[32:35], a[8:11], v[92:95], v[32:35]\n\t"
        "ds_read_b64_tr_b16 a[42:43], v139 offset:19200\n\t"
        "ds_read_b64_tr_b16 a[44:45], v149 offset:18432\n\t"
        "s_sub_u32 s71, s70, 16384\n\t"
        "v_mfma_f32_16x16x32_bf16 v[36:39], a[12:15], v[92:95], 0\n\t"
        "buffer_load_dwordx4 v143, s[16:19], s43 offen  lds\n\t"
        "s_add_u32 m0, s35, 36864\n\t"
        "ds_read_b64_tr_b16 a[46:47], v149 offset:19200\n\t"
        "s_add_u32 s43, s34, 387072\n\t"
        "ds_read_b64_tr_b16 a[48:49], v138 offset:24576\n\t"
        "s_min_u32 s43, s43, 9590784\n\t"
        "v_mfma_f32_16x16x32_bf16 v[40:43], a[16:19], v[92:95], 0\n\t"
        "ds_read_b64_tr_b16 a[50:51], v138 offset:25344\n\t"
        "ds_read_b64_tr_b16 a[52:53], v139 offset:24576\n\t"
        "ds_read_b64_tr_b16 a[54:55], v139 offset:25344\n\t"
        "s_mul_i32 s71, s71, s68\n\t"
        "v_mfma_f32_16x16x32_bf16 v[44:47], a[20:23], v[92:95], 0\n\t"
        "buffer_load_dwordx4 v143, s[16:19], s43 offen  lds\n\t"
        "s_add_u32 m0, s37, 12288\n\t"
        "ds_read_b64_tr_b16 a[56:57], v149 offset:24576\n\t"
        "s_add_u32 s36, s36, s5\n\t"
        "ds_read_b64_tr_b16 a[58:59], v149 offset:25344\n\t"
        "s_waitcnt lgkmcnt(12)\n\t"
        "v_mfma_f32_16x16x32_bf16 v[0:3], a[24:27], v[96:99], 0\n\t"
        "ds_read_b128 v[104:107], v128 offset:8256\n\t"
        "v_add_u32 v172, s71, v128\n\t"
        "v_mfma_f32_16x16x32_bf16 v[4:7], a[28:31], v[96:99], 0\n\t"
        "s_mov_b32 exec_lo, 0\n\t"
        "s_mov_b32 exec_hi, 0xfff00000\n\t"
        "ds_write_b32 v119, v155\n\t"
        "s_mov_b64 exec, -1\n\t"
        "v_mfma_f32_16x16x32_bf16 v[8:11], a[32:35], v[96:99], 0\n\t"
        "buffer_load_dwordx4 v140, s[24:27], s36 offen offset:0 lds\n\t"
        "v_add_u32 v173, s71, v129\n\t"
        "v_mfma_f32_16x16x32_bf16 v[12:15], a[24:27], v[100:103], v[12:15]\n\t"
        "v_add_u32 v174, s71, v130\n\t"
        "s_add_u32 s71, s71, s70\n\t"
        "v_mfma_f32_16x16x32_bf16 v[16:19], a[28:31], v[100:103], v[16:19]\n\t"
        "v_add_u32 v175, s71, v128\n\t"
        "v_add_u32 v176, s71, v129\n\t"
        "v_mfma_f32_16x16x32_bf16 v[20:23], a[32:35], v[100:103], v[20:23]\n\t"
        "s_waitcnt vmcnt(9)\n\t"
        "s_barrier\n\t"
        "buffer_load_dwordx4 v141, s[24:27], s36 offen offset:1024 lds\n\t"
        "ds_read_b128 v[108:111], v128 offset:4096\n\t"
        "s_waitcnt lgkmcnt(2)\n\t"
        "v_mfma_f32_16x16x32_bf16 v[12:15], a[36:39], v[104:107], v[12:15]\n\t"
        "ds_read_b128 v[112:115], v128 offset:4160\n\t"
        "ds_read_b64_tr_b16 a[60:61], v138 offset:30720\n\t"
        "v_add_u32 v177, s71, v130\n\t"
        "v_mfma_f32_16x16x32_bf16 v[16:19], a[40:43], v[104:107], v[16:19]\n\t"
        "ds_read_b64_tr_b16 a[62:63], v138 offset:31488\n\t"
        "ds_read_b64_tr_b16 a[64:65], v139 offset:30720\n\t"
        "s_add_u32 s71, s71, s70\n\t"
        "v_mfma_f32_16x16x32_bf16 v[20:23], a[44:47], v[104:107], v[20:23]\n\t"
        "buffer_load_dwordx4 v142, s[24:27], s36 offen offset:2048 lds\n\t"
        "s_add_u32 m0, s37, 24576\n\t"
        "ds_read_b64_tr_b16 a[66:67], v139 offset:31488\n\t"
        "s_add_u32 s36, s36, s39\n\t"
        "v_mfma_f32_16x16x32_bf16 v[24:27], a[48:51], v[104:107], v[24:27]\n\t"
        "ds_read_b64_tr_b16 a[68:69], v149 offset:30720\n\t"
        "ds_read_b64_tr_b16 a[70:71], v149 offset:31488\n\t"
        "v_add_u32 v178, s71, v128\n\t"
        "v_mfma_f32_16x16x32_bf16 v[28:31], a[52:55], v[104:107], v[28:31]\n\t"
        "ds_read_b128 v[84:87], v128 offset:8256\n\t"
        "v_add_u32 v179, s71, v129\n\t"
        "v_mfma_f32_16x16x32_bf16 v[32:35], a[56:59], v[104:107], v[32:35]\n\t"
        "s_cmp_eq_u32 s69, 36\n\t"
        "s_cselect_b64 exec, -1, 0\n\t"
        "v_swap_b32 v68, v69\n\t"
        "v_swap_b32 v173, v175\n\t"
        "v_swap_b32 v174, v178\n\t"
        "v_swap_b32 v177, v179\n\t"
        "s_mov_b64 exec, -1\n\t"
        "s_waitcnt lgkmcnt(8)\n\t"
        "v_mfma_f32_16x16x32_bf16 v[0:3], a[0:3], v[108:111], v[0:3]\n\t"
        "ds_read_b128 v[88:91], v128 offset:12352\n\t"
        "ds_read_b64_tr_b16 a[72:73], v138 offset:36864\n\t"
        "v_add_u32 v180, s71, v130\n\t"
        "v_mfma_f32_16x16x32_bf16 v[4:7], a[4:7], v[108:111], v[4:7]\n\t"
        "ds_read_b64_tr_b16 a[74:75], v138 offset:37632\n\t"
        "ds_read_b64_tr_b16 a[76:77], v139 offset:36864\n\t"
        "v_add3_u32 v68, v68, v52, -1\n\t"
        "v_mfma_f32_16x16x32_bf16 v[8:11], a[8:11], v[108:111], v[8:11]\n\t"
        "ds_read_b64_tr_b16 a[78:79], v139 offset:37632\n\t"
        "ds_read_b64_tr_b16 a[80:81], v149 offset:36864\n\t"
        "v_add3_u32 v69, v69, v158, -1\n\t"
        "v_mfma_f32_16x16x32_bf16 v[12:15], a[12:15], v[108:111], v[12:15]\n\t"
        "ds_read_b64_tr_b16 a[82:83], v149 offset:37632\n\t"
        "v_cvt_f32_i32 v68, v68\n\t"
        "v_mfma_f32_16x16x32_bf16 v[16:19], a[16:19], v[108:111], v[16:19]\n\t"
        "v_cvt_f32_i32 v69, v69\n\t"
        "v_add_f32 v78, v78, v56\n\t"
        "v_mfma_f32_16x16x32_bf16 v[20:23], a[20:23], v[108:111], v[20:23]\n\t"
        "v_add_f32 v79, v79, v57\n\t"
        "v_readlane_b32 s69, v155, 31\n\t"
        "s_waitcnt lgkmcnt(7)\n\t"
        "v_mfma_f32_16x16x32_bf16 v[0:3], a[36:39], v[112:115], v[0:3]\n\t"
        "v_mov_b32 v154, s69\n\t"
        "v_mov_b32 v119, s62\n\t"
        "v_readlane_b32 s53, v155, 28\n\t"
        "v_mfma_f32_16x16x32_bf16 v[4:7], a[40:43], v[112:115], v[4:7]\n\t"
        "v_readlane_b32 s9, v155, 46\n\t"
        "s_sub_u32 s10, 43200, s11\n\t"
        "v_mfma_f32_16x16x32_bf16 v[8:11], a[44:47], v[112:115], v[8:11]\n\t"
        "s_mul_i32 s10, s10, s68\n\t"
        "s_sub_u32 s11, s11, 192\n\t"
        "v_mfma_f32_16x16x32_bf16 v[12:15], a[48:51], v[112:115], v[12:15]\n\t"
        "v_mad_u32_u24 v148, v158, s11, v144\n\t"
        "v_add_u32 v148, s10, v148\n\t"
        "v_mfma_f32_16x16x32_bf16 v[16:19], a[52:55], v[112:115], v[16:19]\n\t"
        "v_mad_u32_u24 v150, v83, s11, v145\n\t"
        "s_lshl_b32 s70, s49, 1\n\t"
        "v_mfma_f32_16x16x32_bf16 v[20:23], a[56:59], v[112:115], v[20:23]\n\t"
        "s_add_u32 s70, s70, 49\n\t"
        "v_readlane_b32 s52, v155, s70\n\t"
        "v_mfma_f32_16x16x32_bf16 v[0:3], a[60:63], v[84:87], v[0:3]\n\t"
        "s_nop 0\n\t"
        "s_add_u32 s52, s52, s10\n\t"
        "v_mfma_f32_16x16x32_bf16 v[4:7], a[64:67], v[84:87], v[4:7]\n\t"
        "v_mfma_f32_16x16x32_bf16 v[8:11], a[68:71], v[84:87], v[8:11]\n\t"
        "buffer_load_dwordx4 v140, s[24:27], s36 offen offset:0 lds\n\t"
        "s_waitcnt lgkmcnt(0)\n\t"
        "v_mfma_f32_16x16x32_bf16 v[12:15], a[60:63], v[88:91], v[12:15]\n\t"
        "v_mfma_f32_16x16x32_bf16 v[16:19], a[64:67], v[88:91], v[16:19]\n\t"
        "v_mfma_f32_16x16x32_bf16 v[20:23], a[68:71], v[88:91], v[20:23]\n\t"
        "v_mfma_f32_16x16x32_bf16 v[24:27], a[36:39], v[88:91], v[24:27]\n\t"
        "ds_read_b128 v[92:95], v128 offset:4224\n\t"
        "v_mfma_f32_16x16x32_bf16 v[28:31], a[40:43], v[88:91], v[28:31]\n\t"
        "buffer_load_dwordx4 v141, s[24:27], s36 offen offset:1024 lds\n\t"
        "v_mfma_f32_16x16x32_bf16 v[32:35], a[44:47], v[88:91], v[32:35]\n\t"
        "v_mfma_f32_16x16x32_bf16 v[36:39], a[48:51], v[88:91], v[36:39]\n\t"
        "ds_read_b128 v[96:99], v128 offset:8320\n\t"
        "v_mfma_f32_16x16x32_bf16 v[40:43], a[52:55], v[88:91], v[40:43]\n\t"
        "v_mfma_f32_16x16x32_bf16 v[44:47], a[56:59], v[88:91], v[44:47]\n\t"
        "buffer_load_dwordx4 v142, s[24:27], s36 offen offset:2048 lds\n\t"
        "s_waitcnt lgkmcnt(1)\n\t"
        "v_mfma_f32_16x16x32_bf16 v[0:3], a[72:75], v[92:95], v[0:3]\n\t"
        "ds_read_b128 v[100:103], v128 offset:12416\n\t"
        "v_mfma_f32_16x16x32_bf16 v[4:7], a[76:79], v[92:95], v[4:7]\n\t"
        "v_mfma_f32_16x16x32_bf16 v[8:11], a[80:83], v[92:95], v[8:11]\n\t"
        "buffer_load_dwordx4 v[48:51], v55, s[20:23], s48 offen\n\t"
        "buffer_load_dword v61, v61, s[20:23], s48 offen offset:512\n\t"
        "s_waitcnt vmcnt(10)\n\t"
        "s_barrier\n\t"
        "ds_read_b128 v[104:107], v128 offset:16384\n\t"
        "s_add_u32 m0, s37, 36864\n\t"
        "ds_read_b128 v[108:111], v128 offset:16448\n\t"
        "s_waitcnt lgkmcnt(3)\n\t"
        "v_mfma_f32_16x16x32_bf16 v[12:15], a[72:75], v[96:99], v[12:15]\n\t"
        "ds_read_b32 v64, v70 offset:0\n\t"
        "s_add_u32 s36, s36, s38\n\t"
        "ds_read_b64_tr_b16 a[84:85], v131 offset:0\n\t"
        "v_mfma_f32_16x16x32_bf16 v[16:19], a[76:79], v[96:99], v[16:19]\n\t"
        "ds_read_b32 v67, v67 offset:0\n\t"
        "ds_read_b64_tr_b16 a[86:87], v131 offset:768\n\t"
        "v_readlane_b32 s46, v155, 37\n\t"
        "v_mfma_f32_16x16x32_bf16 v[20:23], a[80:83], v[96:99], v[20:23]\n\t"
        "ds_read_b64_tr_b16 a[88:89], v132 offset:0\n\t"
        "ds_read_b64_tr_b16 a[90:91], v132 offset:768\n\t"
        "v_readlane_b32 s47, v155, 40\n\t"
        "s_waitcnt lgkmcnt(8)\n\t"
        "v_mfma_f32_16x16x32_bf16 v[24:27], a[72:75], v[100:103], v[24:27]\n\t"
        "buffer_load_dwordx4 v140, s[24:27], s36 offen offset:0 lds\n\t"
        "ds_read_b64_tr_b16 a[92:93], v133 offset:0\n\t"
        "v_add_f32 v58, s46, v68\n\t"
        "v_mfma_f32_16x16x32_bf16 v[28:31], a[76:79], v[100:103], v[28:31]\n\t"
        "ds_read_b64_tr_b16 a[94:95], v133 offset:768\n\t"
        "ds_read_b128 v[112:115], v128 offset:4224\n\t"
        "v_add_f32 v59, s47, v69\n\t"
        "v_mfma_f32_16x16x32_bf16 v[32:35], a[80:83], v[100:103], v[32:35]\n\t"
        "ds_read_b64_tr_b16 a[96:97], v131 offset:6144\n\t"
        "ds_read_b64_tr_b16 a[98:99], v131 offset:6912\n\t"
        "s_waitcnt lgkmcnt(12)\n\t"
        "v_mfma_f32_16x16x32_bf16 v[24:27], a[24:27], v[104:107], v[24:27]\n\t"
        "ds_read_b64_tr_b16 a[100:101], v132 offset:6144\n\t"
        "ds_read_b64_tr_b16 a[102:103], v132 offset:6912\n\t"
        "v_mfma_f32_16x16x32_bf16 v[28:31], a[28:31], v[104:107], v[28:31]\n\t"
        "buffer_load_dwordx4 v141, s[24:27], s36 offen offset:1024 lds\n\t"
        "ds_read_b64_tr_b16 a[104:105], v133 offset:6144\n\t"
        "v_mfma_f32_16x16x32_bf16 v[32:35], a[32:35], v[104:107], v[32:35]\n\t"
        "ds_read_b64_tr_b16 a[106:107], v133 offset:6912\n\t"
        "v_mfma_f32_16x16x32_bf16 v[36:39], a[0:3], v[104:107], v[36:39]\n\t"
        "ds_read_b128 v[84:87], v128 offset:8320\n\t"
        "s_waitcnt lgkmcnt(12)\n\t"
        "v_perm_b32 v56, v64, v64, s44\n\t"
        "v_perm_b32 v57, v64, v64, s45\n\t"
        "v_mfma_f32_16x16x32_bf16 v[40:43], a[4:7], v[104:107], v[40:43]\n\t"
        "ds_read_b32 v66, v70 offset:2560\n\t"
        "v_add_f32 v56, v56, v58\n\t"
        "v_mfma_f32_16x16x32_bf16 v[44:47], a[8:11], v[104:107], v[44:47]\n\t"
        "buffer_load_dwordx4 v142, s[24:27], s36 offen offset:2048 lds\n\t"
        "s_add_u32 m0, s37, 49152\n\t"
        "v_mfma_f32_16x16x32_bf16 v[24:27], a[60:63], v[108:111], v[24:27]\n\t"
        "ds_read_b128 v[88:91], v128 offset:12416\n\t"
        "s_add_u32 s36, s36, s5\n\t"
        "v_mfma_f32_16x16x32_bf16 v[28:31], a[64:67], v[108:111], v[28:31]\n\t"
        ".LpM0%=:\n\t"
        "s_cbranch_execz .LrM%=\n\t"
        "ds_read_b128 v[92:95], v128 offset:16512\n\t"
        "v_add_f32 v57, v57, v59\n\t"
        "v_mfma_f32_16x16x32_bf16 v[32:35], a[68:71], v[108:111], v[32:35]\n\t"
        "v_cvt_pk_bf16_f32 v52, v56, v57\n\t"
        "v_cvt_pk_bf16_f32 v58, v58, v59\n\t"
        "v_mfma_f32_16x16x32_bf16 v[36:39], a[36:39], v[108:111], v[36:39]\n\t"
        "buffer_load_dwordx4 v140, s[24:27], s36 offen offset:0 lds\n\t"
        "v_cmp_ne_u32 s[64:65], v52, v58\n\t"
        "v_mfma_f32_16x16x32_bf16 v[40:43], a[40:43], v[108:111], v[40:43]\n\t"
        "s_nop 0\n\t"
        "v_mbcnt_lo_u32_b32 v56, s64, 1\n\t"
        "v_mbcnt_hi_u32_b32 v56, s65, v56\n\t"
        "v_mfma_f32_16x16x32_bf16 v[44:47], a[44:47], v[108:111], v[44:47]\n\t"
        "s_bcnt1_i32_b64 s61, s[64:65]\n\t"
        "v_add_u32 v54, s61, 1\n\t"
        "s_waitcnt lgkmcnt(4)\n\t"
        "v_mfma_f32_16x16x32_bf16 v[12:15], a[84:87], v[112:115], v[12:15]\n\t"
        "v_lshl_add_u32 v56, v56, 3, s60\n\t"
        "v_perm_b32 v57, v67, v67, s45\n\t"
        "v_mfma_f32_16x16x32_bf16 v[16:19], a[88:91], v[112:115], v[16:19]\n\t"
        "s_mov_b64 exec, s[64:65]\n\t"
        "ds_write_b64 v56, v[52:53]\n\t"
        "s_mov_b64 exec, -1\n\t"
        "v_mfma_f32_16x16x32_bf16 v[20:23], a[92:95], v[112:115], v[20:23]\n\t"
        "buffer_load_dwordx4 v141, s[24:27], s36 offen offset:1024 lds\n\t"
        "v_swap_b32 v53, v80\n\t"
        "s_waitcnt lgkmcnt(3)\n\t"
        "v_mfma_f32_16x16x32_bf16 v[0:3], a[96:99], v[84:87], v[0:3]\n\t"
        "v_perm_b32 v56, v67, v67, s44\n\t"
        "v_add_f32 v56, v56, v78\n\t"
        "v_mfma_f32_16x16x32_bf16 v[4:7], a[100:103], v[84:87], v[4:7]\n\t"
        "v_add_f32 v57, v57, v79\n\t"
        "v_cvt_pk_bf16_f32 v52, v56, v57\n\t"
        "v_mfma_f32_16x16x32_bf16 v[8:11], a[104:107], v[84:87], v[8:11]\n\t"
        "v_cvt_pk_bf16_f32 v58, v78, v79\n\t"
        "v_cmp_ne_u32 s[64:65], v52, v58\n\t"
        "v_mfma_f32_16x16x32_bf16 v[24:27], a[84:87], v[84:87], v[24:27]\n\t"
        "buffer_load_dwordx4 v142, s[24:27], s36 offen offset:2048 lds\n\t"
        "v_readlane_b32 s40, v155, 12\n\t"
        "v_mfma_f32_16x16x32_bf16 v[28:31], a[88:91], v[84:87], v[28:31]\n\t"
        "v_readlane_b32 s43, v155, 0\n\t"
        "s_add_u32 m0, s40, s35\n\t"
        "s_and_b64 s[64:65], s[64:65], 0xffffff\n\t"
        "v_mfma_f32_16x16x32_bf16 v[32:35], a[92:95], v[84:87], v[32:35]\n\t"
        "v_mbcnt_lo_u32_b32 v56, s64, v54\n\t"
        "s_bcnt1_i32_b64 s46, s[64:65]\n\t"
        "s_waitcnt lgkmcnt(1)\n\t"
        "v_mfma_f32_16x16x32_bf16 v[12:15], a[96:99], v[88:91], v[12:15]\n\t"
        "s_waitcnt vmcnt(8)\n\t"
        "s_barrier\n\t"
        "ds_read_b128 v[96:99], v128 offset:0\n\t"
        "ds_read_b128 v[100:103], v128 offset:64\n\t"
        "v_mfma_f32_16x16x32_bf16 v[16:19], a[100:103], v[88:91], v[16:19]\n\t"
        "buffer_load_dwordx4 v134, s[16:19], s43 offen  lds\n\t"
        "ds_read_b128 v[104:107], v128 offset:128\n\t"
        "v_readlane_b32 s40, v155, 13\n\t"
        "v_mfma_f32_16x16x32_bf16 v[20:23], a[104:107], v[88:91], v[20:23]\n\t"
        "ds_read_b128 v[108:111], v128 offset:20480\n\t"
        "ds_read_b64_tr_b16 a[108:109], v131 offset:12288\n\t"
        "v_readlane_b32 s43, v155, 1\n\t"
        "v_mfma_f32_16x16x32_bf16 v[36:39], a[84:87], v[88:91], v[36:39]\n\t"
        "ds_read_b128 v[112:115], v128 offset:20544\n\t"
        "s_add_u32 m0, s40, s35\n\t"
        "ds_read_b64_tr_b16 a[110:111], v131 offset:13056\n\t"
        "v_mfma_f32_16x16x32_bf16 v[40:43], a[88:91], v[88:91], v[40:43]\n\t"
        "ds_read_b64_tr_b16 a[112:113], v132 offset:12288\n\t"
        "ds_read_b64_tr_b16 a[114:115], v132 offset:13056\n\t"
        "s_add_u32 s61, s61, s46\n\t"
        "v_mfma_f32_16x16x32_bf16 v[44:47], a[92:95], v[88:91], v[44:47]\n\t"
        "buffer_load_dwordx4 v135, s[16:19], s43 offen  lds\n\t"
        "ds_read_b64_tr_b16 a[116:117], v133 offset:12288\n\t"
        "v_readlane_b32 s40, v155, 14\n\t"
        "v_mfma_f32_16x16x32_bf16 v[24:27], a[96:99], v[92:95], v[24:27]\n\t"
        "ds_read_b128 v[84:87], v128 offset:20608\n\t"
        "ds_read_b64_tr_b16 a[118:119], v133 offset:13056\n\t"
        "v_readlane_b32 s43, v155, 2\n\t"
        "v_mfma_f32_16x16x32_bf16 v[28:31], a[100:103], v[92:95], v[28:31]\n\t"
        "ds_read_b64_tr_b16 a[120:121], v131 offset:18432\n\t"
        "s_add_u32 m0, s40, s35\n\t"
        "v_mfma_f32_16x16x32_bf16 v[32:35], a[104:107], v[92:95], v[32:35]\n\t"
        "ds_read_b64_tr_b16 a[122:123], v131 offset:19200\n\t"
        "v_add_u32 v54, s46, v54\n\t"
        "v_mfma_f32_16x16x32_bf16 v[36:39], a[72:75], v[92:95], v[36:39]\n\t"
        "buffer_load_dwordx4 v134, s[16:19], s43 offen  lds\n\t"
        "s_bitcmp1_b32 s42, 10\n\t"
        "s_cbranch_scc0 .Lx40%=\n\t"
        "v_readlane_b32 s40, v155, 20\n\t"
        "v_readlane_b32 s43, v155, 8\n\t"
        "s_nop 0\n\t"
        "s_add_u32 m0, s40, s35\n\t"
        "s_nop 2\n\t"
        "buffer_load_dwordx4 v136, s[16:19], s43 offen  lds\n\t"
        ".Lx40%=:\n\t"
        "v_mfma_f32_16x16x32_bf16 v[40:43], a[76:79], v[92:95], v[40:43]\n\t"
        "ds_read_b64_tr_b16 a[124:125], v132 offset:18432\n\t"
        "v_readlane_b32 s40, v155, 15\n\t"
        "v_mfma_f32_16x16x32_bf16 v[44:47], a[80:83], v[92:95], v[44:47]\n\t"
        "ds_read_b64_tr_b16 a[126:127], v132 offset:19200\n\t"
        "v_readlane_b32 s43, v155, 3\n\t"
        "s_waitcnt lgkmcnt(8)\n\t"
        "v_mfma_f32_16x16x32_bf16 v[0:3], a[12:15], v[96:99], v[0:3]\n\t"
        "ds_read_b64_tr_b16 a[128:129], v133 offset:18432\n\t"
        "s_add_u32 m0, s40, s35\n\t"
        "ds_read_b64_tr_b16 a[130:131], v133 offset:19200\n\t"
        "v_mfma_f32_16x16x32_bf16 v[4:7], a[16:19], v[96:99], v[4:7]\n\t"
        "ds_read_b128 v[88:91], v129 offset:0\n\t"
        "s_mov_b32 s3, s61\n\t"
        "v_mfma_f32_16x16x32_bf16 v[8:11], a[20:23], v[96:99], v[8:11]\n\t"
        "ds_read_b128 v[92:95], v129 offset:4096\n\t"
        "v_lshl_add_u32 v56, v56, 3, s60\n\t"
        "v_mfma_f32_16x16x32_bf16 v[0:3], a[48:51], v[100:103], v[0:3]\n\t"
        "s_mov_b64 exec, s[64:65]\n\t"
        "ds_write_b64 v56, v[52:53]\n\t"
        "s_mov_b64 exec, -1\n\t"
        "v_mfma_f32_16x16x32_bf16 v[4:7], a[52:55], v[100:103], v[4:7]\n\t"
        "ds_read_b64_tr_b16 a[132:133], v131 offset:24576\n\t"
        "v_add_u32 v53, 4096, v80\n\t"
        "v_mfma_f32_16x16x32_bf16 v[8:11], a[56:59], v[100:103], v[8:11]\n\t"
        "ds_read_b64_tr_b16 a[134:135], v131 offset:25344\n\t"
        "v_readlane_b32 s46, v155, 39\n\t"
        "v_mfma_f32_16x16x32_bf16 v[0:3], a[84:87], v[104:107], v[0:3]\n\t"
        "buffer_load_dwordx4 v135, s[16:19], s43 offen  lds\n\t"
        "s_add_u32 m0, s37, 0\n\t"
        "v_mfma_f32_16x16x32_bf16 v[4:7], a[88:91], v[104:107], v[4:7]\n\t"
        "ds_read_b64_tr_b16 a[136:137], v132 offset:24576\n\t"
        "s_add_u32 s36, s36, s39\n\t"
        "v_mfma_f32_16x16x32_bf16 v[8:11], a[92:95], v[104:107], v[8:11]\n\t"
        "ds_read_b64_tr_b16 a[138:139], v132 offset:25344\n\t"
        "v_readlane_b32 s47, v155, 42\n\t"
        "v_mfma_f32_16x16x32_bf16 v[36:39], a[24:27], v[108:111], v[36:39]\n\t"
        "ds_read_b64_tr_b16 a[140:141], v133 offset:24576\n\t"
        "v_perm_b32 v56, v66, v66, s44\n\t"
        "v_mfma_f32_16x16x32_bf16 v[40:43], a[28:31], v[108:111], v[40:43]\n\t"
        "ds_read_b64_tr_b16 a[142:143], v133 offset:25344\n\t"
        "v_perm_b32 v57, v66, v66, s45\n\t"
        "v_mfma_f32_16x16x32_bf16 v[44:47], a[32:35], v[108:111], v[44:47]\n\t"
        "buffer_load_dwordx4 v140, s[24:27], s36 offen offset:0 lds\n\t"
        "v_add_f32 v58, s46, v68\n\t"
        "v_mfma_f32_16x16x32_bf16 v[36:39], a[60:63], v[112:115], v[36:39]\n\t"
        "ds_read_b128 v[96:99], v129 offset:8192\n\t"
        "v_add_f32 v59, s47, v69\n\t"
        "v_mfma_f32_16x16x32_bf16 v[40:43], a[64:67], v[112:115], v[40:43]\n\t"
        "ds_read_b64_tr_b16 a[144:145], v131 offset:30720\n\t"
        "v_add_f32 v56, v56, v58\n\t"
        "v_mfma_f32_16x16x32_bf16 v[44:47], a[68:71], v[112:115], v[44:47]\n\t"
        "ds_read_b64_tr_b16 a[146:147], v131 offset:31488\n\t"
        "v_add_f32 v57, v57, v59\n\t"
        "s_waitcnt lgkmcnt(8)\n\t"
        "v_mfma_f32_16x16x32_bf16 v[36:39], a[96:99], v[84:87], v[36:39]\n\t"
        "buffer_load_dwordx4 v141, s[24:27], s36 offen offset:1024 lds\n\t"
        "v_max_f32 v56, 0, v56\n\t"
        "v_mfma_f32_16x16x32_bf16 v[40:43], a[100:103], v[84:87], v[40:43]\n\t"
        "ds_read_b128 v[100:103], v129 offset:12288\n\t"
        "v_max_f32 v57, 0, v57\n\t"
        "v_mfma_f32_16x16x32_bf16 v[44:47], a[104:107], v[84:87], v[44:47]\n\t"
        "ds_read_b64_tr_b16 a[148:149], v132 offset:30720\n\t"
        "v_max_f32 v58, 0, v58\n\t"
        "v_mfma_f32_16x16x32_bf16 v[0:3], a[120:123], v[88:91], v[0:3]\n\t"
        "ds_read_b64_tr_b16 a[150:151], v132 offset:31488\n\t"
        "v_max_f32 v59, 0, v59\n\t"
        "v_mfma_f32_16x16x32_bf16 v[4:7], a[124:127], v[88:91], v[4:7]\n\t"
        "buffer_load_dwordx4 v142, s[24:27], s36 offen offset:2048 lds\n\t"
        "s_add_u32 m0, s37, 12288\n\t"
        "v_mfma_f32_16x16x32_bf16 v[8:11], a[128:131], v[88:91], v[8:11]\n\t"
        "ds_read_b64_tr_b16 a[152:153], v133 offset:30720\n\t"
        "s_add_u32 s36, s36, s38\n\t"
        "v_mfma_f32_16x16x32_bf16 v[0:3], a[108:111], v[92:95], v[0:3]\n\t"
        "ds_read_b64_tr_b16 a[154:155], v133 offset:31488\n\t"
        "v_cvt_pk_bf16_f32 v52, v56, v57\n\t"
        "v_mfma_f32_16x16x32_bf16 v[4:7], a[112:115], v[92:95], v[4:7]\n\t"
        "v_cvt_pk_bf16_f32 v58, v58, v59\n\t"
        "v_cmp_ne_u32 s[64:65], v52, v58\n\t"
        "v_mfma_f32_16x16x32_bf16 v[8:11], a[116:119], v[92:95], v[8:11]\n\t"
        "s_nop 0\n\t"
        "v_mbcnt_lo_u32_b32 v56, s64, v54\n\t"
        "v_mbcnt_hi_u32_b32 v56, s65, v56\n\t"
        "v_mfma_f32_16x16x32_bf16 v[12:15], a[120:123], v[92:95], v[12:15]\n\t"
        "ds_read_b128 v[104:107], v129 offset:16384\n\t"
        "s_bcnt1_i32_b64 s46, s[64:65]\n\t"
        "v_mfma_f32_16x16x32_bf16 v[16:19], a[124:127], v[92:95], v[16:19]\n\t"
        "s_add_u32 s61, s61, s46\n\t"
        "v_min_u32 v56, 97, v56\n\t"
        "v_mfma_f32_16x16x32_bf16 v[20:23], a[128:131], v[92:95], v[20:23]\n\t"
        "v_lshl_add_u32 v56, v56, 3, s60\n\t"
        "s_waitcnt lgkmcnt(5)\n\t"
        "v_mfma_f32_16x16x32_bf16 v[0:3], a[132:135], v[96:99], v[0:3]\n\t"
        "s_mov_b64 exec, s[64:65]\n\t"
        "ds_write_b64 v56, v[52:53]\n\t"
        "s_mov_b64 exec, -1\n\t"
        "v_mfma_f32_16x16x32_bf16 v[4:7], a[136:139], v[96:99], v[4:7]\n\t"
        "s_cmp_gt_u32 s61, s53\n\t"
        "s_cselect_b32 s8, 1, 0\n\t"
        "v_mfma_f32_16x16x32_bf16 v[8:11], a[140:143], v[96:99], v[8:11]\n\t"
        "s_waitcnt vmcnt(14)\n\t"
        "v_and_b32 v48, s54, v48\n\t"
        "v_pk_max_u16 v60, v154, v48\n\t"
        "v_mfma_f32_16x16x32_bf16 v[12:15], a[108:111], v[96:99], v[12:15]\n\t"
        "ds_read_b128 v[108:111], v129 offset:20480\n\t"
        "v_and_b32 v49, s54, v49\n\t"
        "v_mfma_f32_16x16x32_bf16 v[16:19], a[112:115], v[96:99], v[16:19]\n\t"
        "v_pk_max_u16 v60, v60, v49\n\t"
        "v_and_b32 v50, s54, v50\n\t"
        "v_mfma_f32_16x16x32_bf16 v[20:23], a[116:119], v[96:99], v[20:23]\n\t"
        "v_pk_max_u16 v60, v60, v50\n\t"
        "v_and_b32 v51, s54, v51\n\t"
        "v_mfma_f32_16x16x32_bf16 v[24:27], a[120:123], v[96:99], v[24:27]\n\t"
        "v_pk_max_u16 v60, v60, v51\n\t"
        "s_waitcnt vmcnt(13)\n\t"
        "v_and_b32 v61, s54, v61\n\t"
        "v_mfma_f32_16x16x32_bf16 v[28:31], a[124:127], v[96:99], v[28:31]\n\t"
        "v_pk_max_u16 v60, v60, v61\n\t"
        "v_cmp_ne_u32 s[12:13], v60, v154\n\t"
        "v_mfma_f32_16x16x32_bf16 v[32:35], a[128:131], v[96:99], v[32:35]\n\t"
        "s_waitcnt vmcnt(7) lgkmcnt(2)\n\t"
        "s_barrier\n\t"
        "v_mfma_f32_16x16x32_bf16 v[12:15], a[132:135], v[100:103], v[12:15]\n\t"
        "buffer_load_dwordx4 v140, s[24:27], s36 offen offset:0 lds\n\t"
        "ds_read_b64_tr_b16 a[156:157], v131 offset:36864\n\t"
        "s_lshl_b32 s6, 256, s4\n\t"
        "v_mfma_f32_16x16x32_bf16 v[16:19], a[136:139], v[100:103], v[16:19]\n\t"
        "s_cmp_lg_u64 s[12:13], 0\n\t"
        "s_cselect_b32 s6, s6, 0\n\t"
        "ds_read_b64_tr_b16 a[158:159], v131 offset:37632\n\t"
        "ds_read_b64_tr_b16 a[160:161], v132 offset:36864\n\t"
        "v_mfma_f32_16x16x32_bf16 v[20:23], a[140:143], v[100:103], v[20:23]\n\t"
        "ds_read_b64_tr_b16 a[162:163], v132 offset:37632\n\t"
        "ds_read_b64_tr_b16 a[164:165], v133 offset:36864\n\t"
        "s_or_b32 s56, s56, s8\n\t"
        "v_mfma_f32_16x16x32_bf16 v[24:27], a[108:111], v[100:103], v[24:27]\n\t"
        "s_cmp_gt_u32 s3, 64\n\t"
        "s_cselect_b32 s8, 1, 0\n\t"
        "ds_read_b64_tr_b16 a[166:167], v133 offset:37632\n\t"
        "v_mfma_f32_16x16x32_bf16 v[28:31], a[112:115], v[100:103], v[28:31]\n\t"
        "buffer_load_dwordx4 v141, s[24:27], s36 offen offset:1024 lds\n\t"
        "s_or_b32 s56, s56, s8\n\t"
        "v_mfma_f32_16x16x32_bf16 v[32:35], a[116:119], v[100:103], v[32:35]\n\t"
        "ds_read_b128 v[112:115], v129 offset:64\n\t"
        "s_min_u32 s61, s61, 80\n\t"
        "v_mfma_f32_16x16x32_bf16 v[36:39], a[120:123], v[100:103], v[36:39]\n\t"
        "ds_read_b128 v[84:87], v129 offset:4160\n\t"
        "v_min_u32 v78, s61, v116\n\t"
        "v_mfma_f32_16x16x32_bf16 v[40:43], a[124:127], v[100:103], v[40:43]\n\t"
        "ds_read_b64_tr_b16 a[168:169], v131 offset:43008\n\t"
        "v_lshl_add_u32 v78, v78, 3, s60\n\t"
        "v_mfma_f32_16x16x32_bf16 v[44:47], a[128:131], v[100:103], v[44:47]\n\t"
        "ds_read_b64 v[62:63], v78\n\t"
        "v_cmp_ge_u32 s[66:67], s61, v116\n\t"
        "v_mfma_f32_16x16x32_bf16 v[24:27], a[132:135], v[104:107], v[24:27]\n\t"
        "buffer_load_dwordx4 v142, s[24:27], s36 offen offset:2048 lds\n\t"
        "v_readlane_b32 s40, v155, 16\n\t"
        "v_mfma_f32_16x16x32_bf16 v[28:31], a[136:139], v[104:107], v[28:31]\n\t"
        "ds_read_b64_tr_b16 a[170:171], v131 offset:43776\n\t"
        "v_readlane_b32 s43, v155, 4\n\t"
        "v_mfma_f32_16x16x32_bf16 v[32:35], a[140:143], v[104:107], v[32:35]\n\t"
        "ds_read_b64_tr_b16 a[172:173], v132 offset:43008\n\t"
        "s_add_u32 m0, s40, s35\n\t"
        "v_mfma_f32_16x16x32_bf16 v[36:39], a[108:111], v[104:107], v[36:39]\n\t"
        "ds_read_b64_tr_b16 a[174:175], v132 offset:43776\n\t"
        "ds_read_b64_tr_b16 a[176:177], v133 offset:43008\n\t"
        "s_mov_b32 s46, 0x07060302\n\t"
        "v_mfma_f32_16x16x32_bf16 v[40:43], a[112:115], v[104:107], v[40:43]\n\t"
        "buffer_load_dwordx4 v134, s[16:19], s43 offen  lds\n\t"
        "ds_read_b64_tr_b16 a[178:179], v133 offset:43776\n\t"
        "s_bitcmp1_b32 s42, 11\n\t"
        "s_cbranch_scc0 .Lx42%=\n\t"
        "v_readlane_b32 s40, v155, 21\n\t"
        "v_readlane_b32 s43, v155, 9\n\t"
        "s_nop 0\n\t"
        "s_add_u32 m0, s40, s35\n\t"
        "s_nop 2\n\t"
        "buffer_load_dwordx4 v137, s[16:19], s43 offen  lds\n\t"
        "v_readlane_b32 s40, v155, 22\n\t"
        "v_readlane_b32 s43, v155, 10\n\t"
        "s_nop 0\n\t"
        "s_add_u32 m0, s40, s35\n\t"
        "s_nop 2\n\t"
        "buffer_load_dwordx4 v136, s[16:19], s43 offen  lds\n\t"
        ".Lx42%=:\n\t"
        "v_mfma_f32_16x16x32_bf16 v[44:47], a[116:119], v[104:107], v[44:47]\n\t"
        "ds_read_b128 v[88:91], v129 offset:8256\n\t"
        "v_readlane_b32 s40, v155, 17\n\t"
        "s_waitcnt lgkmcnt(8)\n\t"
        "v_mfma_f32_16x16x32_bf16 v[36:39], a[132:135], v[108:111], v[36:39]\n\t"
        "ds_read_b128 v[92:95], v129 offset:12352\n\t"
        "v_readlane_b32 s43, v155, 5\n\t"
        "v_mfma_f32_16x16x32_bf16 v[40:43], a[136:139], v[108:111], v[40:43]\n\t"
        "ds_read_b64_tr_b16 a[180:181], v131 offset:49152\n\t"
        "s_add_u32 m0, s40, s35\n\t"
        "v_mfma_f32_16x16x32_bf16 v[44:47], a[140:143], v[108:111], v[44:47]\n\t"
        "s_mov_b64 exec, s[66:67]\n\t"
        "s_waitcnt lgkmcnt(8)\n\t"
        "ds_write_b32 v63, v117\n\t"
        "s_mov_b64 exec, -1\n\t"
        "v_mfma_f32_16x16x32_bf16 v[0:3], a[156:159], v[112:115], v[0:3]\n\t"
        "buffer_load_dwordx4 v135, s[16:19], s43 offen  lds\n\t"
        "v_readlane_b32 s40, v155, 18\n\t"
        "v_mfma_f32_16x16x32_bf16 v[4:7], a[160:163], v[112:115], v[4:7]\n\t"
        "ds_read_b64_tr_b16 a[182:183], v131 offset:49920\n\t"
        "v_readlane_b32 s43, v155, 6\n\t"
        "v_mfma_f32_16x16x32_bf16 v[8:11], a[164:167], v[112:115], v[8:11]\n\t"
        "ds_read_b64_tr_b16 a[184:185], v132 offset:49152\n\t"
        "s_add_u32 m0, s40, s35\n\t"
        "v_mfma_f32_16x16x32_bf16 v[0:3], a[144:147], v[84:87], v[0:3]\n\t"
        "ds_read_b64_tr_b16 a[186:187], v132 offset:49920\n\t"
        "v_and_b32 v78, 0xfffff800, v63\n\t"
        "v_mfma_f32_16x16x32_bf16 v[4:7], a[148:151], v[84:87], v[4:7]\n\t"
        "ds_read_b128 v[64:67], v78 offset:1536\n\t"
        "v_lshlrev_b32 v72, 16, v62\n\t"
        "v_mfma_f32_16x16x32_bf16 v[8:11], a[152:155], v[84:87], v[8:11]\n\t"
        "buffer_load_dwordx4 v136, s[16:19], s43 offen  lds\n\t"
        "s_bitcmp1_b32 s42, 11\n\t"
        "s_cbranch_scc0 .Lx44%=\n\t"
        "v_readlane_b32 s40, v155, 23\n\t"
        "v_readlane_b32 s43, v155, 11\n\t"
        "s_nop 0\n\t"
        "s_add_u32 m0, s40, s35\n\t"
        "s_nop 2\n\t"
        "buffer_load_dwordx4 v137, s[16:19], s43 offen  lds\n\t"
        ".Lx44%=:\n\t"
        "v_mfma_f32_16x16x32_bf16 v[12:15], a[156:159], v[84:87], v[12:15]\n\t"
        "ds_read_b128 v[96:99], v129 offset:16448\n\t"
        "ds_read_b64_tr_b16 a[188:189], v133 offset:49152\n\t"
        "v_readlane_b32 s40, v155, 19\n\t"
        "v_mfma_f32_16x16x32_bf16 v[16:19], a[160:163], v[84:87], v[16:19]\n\t"
        "ds_read_b64_tr_b16 a[190:191], v133 offset:49920\n\t"
        "v_readlane_b32 s43, v155, 7\n\t"
        "v_mfma_f32_16x16x32_bf16 v[20:23], a[164:167], v[84:87], v[20:23]\n\t"
        "ds_read_b64_tr_b16 a[192:193], v131 offset:55296\n\t"
        "s_add_u32 m0, s40, s35\n\t"
        "s_waitcnt lgkmcnt(8)\n\t"
        "v_mfma_f32_16x16x32_bf16 v[0:3], a[168:171], v[88:91], v[0:3]\n\t"
        "buffer_load_dwordx4 v137, s[16:19], s43 offen  lds\n\t"
        "s_add_u32 m0, s37, 24576\n\t"
        "v_mfma_f32_16x16x32_bf16 v[4:7], a[172:175], v[88:91], v[4:7]\n\t"
        "ds_read_b64_tr_b16 a[194:195], v131 offset:56064\n\t"
        "s_add_u32 s36, s36, s5\n\t"
        "v_mfma_f32_16x16x32_bf16 v[8:11], a[176:179], v[88:91], v[8:11]\n\t"
        "ds_read_b64_tr_b16 a[196:197], v132 offset:55296\n\t"
        "v_and_b32 v73, 0xffff0000, v62\n\t"
        "v_mfma_f32_16x16x32_bf16 v[12:15], a[144:147], v[88:91], v[12:15]\n\t"
        "ds_read_b128 v[100:103], v129 offset:20544\n\t"
        "v_med3_f32 v72, v72, 0, s63\n\t"
        "v_mfma_f32_16x16x32_bf16 v[16:19], a[148:151], v[88:91], v[16:19]\n\t"
        "buffer_load_dwordx4 v140, s[24:27], s36 offen offset:0 lds\n\t"
        "v_med3_f32 v73, v73, 0, s63\n\t"
        "v_mfma_f32_16x16x32_bf16 v[20:23], a[152:155], v[88:91], v[20:23]\n\t"
        "ds_read_b64_tr_b16 a[198:199], v132 offset:56064\n\t"
        "v_perm_b32 v62, v73, v72, s46\n\t"
        "v_mfma_f32_16x16x32_bf16 v[24:27], a[156:159], v[88:91], v[24:27]\n\t"
        "ds_read_b64_tr_b16 a[200:201], v133 offset:55296\n\t"
        "v_fract_f32 v74, v72\n\t"
        "v_mfma_f32_16x16x32_bf16 v[28:31], a[160:163], v[88:91], v[28:31]\n\t"
        "ds_read_b64_tr_b16 a[202:203], v133 offset:56064\n\t"
        "v_fract_f32 v75, v73\n\t"
        "v_mfma_f32_16x16x32_bf16 v[32:35], a[164:167], v[88:91], v[32:35]\n\t"
        "buffer_load_dwordx4 v141, s[24:27], s36 offen offset:1024 lds\n\t"
        "v_cvt_u32_f32 v76, v72\n\t"
        "v_mfma_f32_16x16x32_bf16 v[12:15], a[168:171], v[92:95], v[12:15]\n\t"
        "ds_read_b128 v[104:107], v129 offset:128\n\t"
        "v_cvt_u32_f32 v77, v73\n\t"
        "v_mfma_f32_16x16x32_bf16 v[16:19], a[172:175], v[92:95], v[16:19]\n\t"
        "ds_read_b128 v[108:111], v129 offset:4224\n\t"
        "v_cmp_neq_f32 s[64:65], 0, v74\n\t"
        "v_mfma_f32_16x16x32_bf16 v[20:23], a[176:179], v[92:95], v[20:23]\n\t"
        "v_cmp_neq_f32 s[44:45], 0, v75\n\t"
        "v_sub_f32 v78, 1.0, v74\n\t"
        "v_mfma_f32_16x16x32_bf16 v[24:27], a[144:147], v[92:95], v[24:27]\n\t"
        "buffer_load_dwordx4 v142, s[24:27], s36 offen offset:2048 lds\n\t"
        "s_add_u32 m0, s37, 36864\n\t"
        "v_mfma_f32_16x16x32_bf16 v[28:31], a[148:151], v[92:95], v[28:31]\n\t"
        "s_add_u32 s36, s36, s39\n\t"
        "v_sub_f32 v79, 1.0, v75\n\t"
        "v_mfma_f32_16x16x32_bf16 v[32:35], a[152:155], v[92:95], v[32:35]\n\t"
        "s_waitcnt vmcnt(10)\n\t"
        "s_barrier\n\t"
        "v_mfma_f32_16x16x32_bf16 v[36:39], a[156:159], v[92:95], v[36:39]\n\t"
        "ds_read_b64_tr_b16 a[204:205], v131 offset:0\n\t"
        "v_cvt_pk_bf16_f32 v79, v78, v79\n\t"
        "v_mfma_f32_16x16x32_bf16 v[40:43], a[160:163], v[92:95], v[40:43]\n\t"
        "buffer_load_dwordx4 v140, s[24:27], s36 offen offset:0 lds\n\t"
        "v_lshlrev_b32 v78, 16, v79\n\t"
        "v_mfma_f32_16x16x32_bf16 v[44:47], a[164:167], v[92:95], v[44:47]\n\t"
        "ds_read_b64_tr_b16 a[206:207], v131 offset:768\n\t"
        "v_and_b32 v79, 0xffff0000, v79\n\t"
        "s_waitcnt lgkmcnt(4)\n\t"
        "v_mfma_f32_16x16x32_bf16 v[24:27], a[168:171], v[96:99], v[24:27]\n\t"
        "ds_read_b64_tr_b16 a[208:209], v132 offset:0\n\t"
        "v_mul_f32 v80, v78, v79\n\t"
        "v_mfma_f32_16x16x32_bf16 v[28:31], a[172:175], v[96:99], v[28:31]\n\t"
        "ds_read_b64_tr_b16 a[210:211], v132 offset:768\n\t"
        "v_mul_f32 v78, v78, v75\n\t"
        "v_mfma_f32_16x16x32_bf16 v[32:35], a[176:179], v[96:99], v[32:35]\n\t"
        "buffer_load_dwordx4 v141, s[24:27], s36 offen offset:1024 lds\n\t"
        "v_mul_f32 v79, v74, v79\n\t"
        "v_mfma_f32_16x16x32_bf16 v[36:39], a[144:147], v[96:99], v[36:39]\n\t"
        "ds_read_b64_tr_b16 a[212:213], v133 offset:0\n\t"
        "ds_read_b64_tr_b16 a[214:215], v133 offset:768\n\t"
        "v_mul_f32 v81, v74, v75\n\t"
        "v_mfma_f32_16x16x32_bf16 v[40:43], a[148:151], v[96:99], v[40:43]\n\t"
        "ds_read_b128 v[112:115], v129 offset:8320\n\t"
        "v_cndmask_b32 v78, v79, v78, s[44:45]\n\t"
        "v_mfma_f32_16x16x32_bf16 v[44:47], a[152:155], v[96:99], v[44:47]\n\t"
        "ds_read_b64_tr_b16 a[216:217], v131 offset:6144\n\t"
        "v_cndmask_b32 v79, 0, v79, s[44:45]\n\t"
        "v_mfma_f32_16x16x32_bf16 v[36:39], a[168:171], v[100:103], v[36:39]\n\t"
        "buffer_load_dwordx4 v142, s[24:27], s36 offen offset:2048 lds\n\t"
        "s_add_u32 m0, s37, 49152\n\t"
        "v_mfma_f32_16x16x32_bf16 v[40:43], a[172:175], v[100:103], v[40:43]\n\t"
        "s_waitcnt vmcnt(10)\n\t"
        "s_barrier\n\t"
        "v_mfma_f32_16x16x32_bf16 v[44:47], a[176:179], v[100:103], v[44:47]\n\t"
        "ds_read_b128 v[84:87], v129 offset:12416\n\t"
        "s_add_u32 s36, s36, s38\n\t"
        "s_waitcnt lgkmcnt(7)\n\t"
        "v_mfma_f32_16x16x32_bf16 v[0:3], a[192:195], v[104:107], v[0:3]\n\t"
        "ds_read_b64_tr_b16 a[218:219], v131 offset:6912\n\t"
        "v_cvt_pk_bf16_f32 v70, v80, v78\n\t"
        "v_mfma_f32_16x16x32_bf16 v[4:7], a[196:199], v[104:107], v[4:7]\n\t"
        "buffer_load_dwordx4 v140, s[24:27], s36 offen offset:0 lds\n\t"
        "v_cvt_pk_bf16_f32 v71, v79, v81\n\t"
        "v_mfma_f32_16x16x32_bf16 v[8:11], a[200:203], v[104:107], v[8:11]\n\t"
        "ds_read_b64_tr_b16 a[220:221], v132 offset:6144\n\t"
        "v_pk_max_u16 v78, v62, v64\n\t"
        "v_mfma_f32_16x16x32_bf16 v[0:3], a[180:183], v[108:111], v[0:3]\n\t"
        "ds_read_b64_tr_b16 a[222:223], v132 offset:6912\n\t"
        "v_pk_min_u16 v78, v78, v65\n\t"
        "v_mfma_f32_16x16x32_bf16 v[4:7], a[184:187], v[108:111], v[4:7]\n\t"
        "ds_read_b64_tr_b16 a[224:225], v133 offset:6144\n\t"
        "v_cmp_ne_u32 vcc, v78, v62\n\t"
        "v_mfma_f32_16x16x32_bf16 v[8:11], a[188:191], v[108:111], v[8:11]\n\t"
        "ds_read_b64_tr_b16 a[226:227], v133 offset:6912\n\t"
        "v_sub_u32 v78, v77, v67\n\t"
        "v_mfma_f32_16x16x32_bf16 v[12:15], a[192:195], v[108:111], v[12:15]\n\t"
        "ds_read_b128 v[88:91], v129 offset:16512\n\t"
        "v_mad_u32_u24 v79, v77, 6, v66\n\t"
        "v_mfma_f32_16x16x32_bf16 v[16:19], a[196:199], v[108:111], v[16:19]\n\t"
        "ds_read_b64_tr_b16 a[228:229], v131 offset:12288\n\t"
        "v_mad_u32_u24 v79, v76, s62, v79\n\t"
        "v_mfma_f32_16x16x32_bf16 v[20:23], a[200:203], v[108:111], v[20:23]\n\t"
        "ds_read_b64_tr_b16 a[230:231], v131 offset:13056\n\t"
        "s_and_b64 vcc, vcc, s[66:67]\n\t"
        "s_waitcnt lgkmcnt(9)\n\t"
        "v_mfma_f32_16x16x32_bf16 v[0:3], a[204:207], v[112:115], v[0:3]\n\t"
        "ds_read_b64_tr_b16 a[232:233], v132 offset:12288\n\t"
        "s_or_b64 s[56:57], s[56:57], vcc\n\t"
        "v_mfma_f32_16x16x32_bf16 v[4:7], a[208:211], v[112:115], v[4:7]\n\t"
        "ds_read_b64_tr_b16 a[234:235], v132 offset:13056\n\t"
        "v_bfe_u32 v80, v78, 2, 1\n\t"
        "v_mfma_f32_16x16x32_bf16 v[8:11], a[212:215], v[112:115], v[8:11]\n\t"
        "ds_read_b64_tr_b16 a[236:237], v133 offset:12288\n\t"
        "v_or_b32 v80, v80, v79\n\t"
        "v_mfma_f32_16x16x32_bf16 v[12:15], a[180:183], v[112:115], v[12:15]\n\t"
        "ds_read_b128 v[92:95], v129 offset:20608\n\t"
        "v_add_u32 v78, 1, v78\n\t"
        "v_mfma_f32_16x16x32_bf16 v[16:19], a[184:187], v[112:115], v[16:19]\n\t"
        "ds_read_b64_tr_b16 a[238:239], v133 offset:13056\n\t"
        "v_bfe_u32 v78, v78, 2, 1\n\t"
        "v_mfma_f32_16x16x32_bf16 v[20:23], a[188:191], v[112:115], v[20:23]\n\t"
        "ds_read_b64_tr_b16 a[240:241], v131 offset:18432\n\t"
        "v_add3_u32 v78, v79, v78, 6\n\t"
        "v_mfma_f32_16x16x32_bf16 v[24:27], a[192:195], v[112:115], v[24:27]\n\t"
        "ds_read_b128 v[96:99], v130 offset:0\n\t"
        "v_cndmask_b32 v78, v80, v78, s[44:45]\n\t"
        "v_mfma_f32_16x16x32_bf16 v[28:31], a[196:199], v[112:115], v[28:31]\n\t"
        "ds_read_b64_tr_b16 a[242:243], v131 offset:19200\n\t"
        "v_cndmask_b32 v79, 0, v119, s[64:65]\n\t"
        "v_mfma_f32_16x16x32_bf16 v[32:35], a[200:203], v[112:115], v[32:35]\n\t"
        "ds_read_b64_tr_b16 a[244:245], v132 offset:18432\n\t"
        "v_add_u32 v81, v80, v79\n\t"
        "s_waitcnt lgkmcnt(10)\n\t"
        "v_mfma_f32_16x16x32_bf16 v[12:15], a[204:207], v[84:87], v[12:15]\n\t"
        "ds_read_b128 v[100:103], v130 offset:4096\n\t"
        "v_add_u32 v82, v78, v79\n\t"
        "v_mfma_f32_16x16x32_bf16 v[16:19], a[208:211], v[84:87], v[16:19]\n\t"
        "ds_read_b64_tr_b16 a[246:247], v132 offset:19200\n\t"
        "v_cndmask_b32 v78, v81, v78, s[44:45]\n\t"
        "v_mfma_f32_16x16x32_bf16 v[20:23], a[212:215], v[84:87], v[20:23]\n\t"
        "buffer_load_dwordx4 v141, s[24:27], s36 offen offset:1024 lds\n\t"
        "v_lshl_or_b32 v68, v78, 16, v80\n\t"
        "v_mfma_f32_16x16x32_bf16 v[24:27], a[180:183], v[84:87], v[24:27]\n\t"
        "ds_read_b64_tr_b16 a[248:249], v133 offset:18432\n\t"
        "v_lshl_or_b32 v69, v82, 16, v81\n\t"
        "v_mfma_f32_16x16x32_bf16 v[28:31], a[184:187], v[84:87], v[28:31]\n\t"
        "ds_write_b128 v118, v[68:71]\n\t"
        "v_mfma_f32_16x16x32_bf16 v[32:35], a[188:191], v[84:87], v[32:35]\n\t"
        "s_cmp_gt_u32 s3, 32\n\t"
        "s_cselect_b32 s8, 2, 0\n\t"
        "v_mfma_f32_16x16x32_bf16 v[36:39], a[192:195], v[84:87], v[36:39]\n\t"
        "s_cmp_lg_u64 s[56:57], 0\n\t"
        "s_addc_u32 s8, s8, s6\n\t"
        "v_mfma_f32_16x16x32_bf16 v[40:43], a[196:199], v[84:87], v[40:43]\n\t"
        "ds_read_b128 v[160:163], v185 offset:0\n\t"
        "v_mov_b32 v151, s8\n\t"
        "v_mfma_f32_16x16x32_bf16 v[44:47], a[200:203], v[84:87], v[44:47]\n\t"
        "s_mov_b64 exec, 1\n\t"
        "ds_write_b32 v153, v151\n\t"
        "s_mov_b64 exec, -1\n\t"
        "v_mfma_f32_16x16x32_bf16 v[24:27], a[204:207], v[88:91], v[24:27]\n\t"
        "buffer_load_dwordx4 v142, s[24:27], s36 offen offset:2048 lds\n\t"
        "v_xor_b32 v154, 32, v186\n\t"
        "v_mfma_f32_16x16x32_bf16 v[28:31], a[208:211], v[88:91], v[28:31]\n\t"
        "ds_read_b64_tr_b16 a[250:251], v133 offset:19200\n\t"
        "v_mfma_f32_16x16x32_bf16 v[32:35], a[212:215], v[88:91], v[32:35]\n\t"
        "ds_read_b128 v[104:107], v130 offset:8192\n\t"
        "v_mfma_f32_16x16x32_bf16 v[36:39], a[180:183], v[88:91], v[36:39]\n\t"
        "v_mfma_f32_16x16x32_bf16 v[40:43], a[184:187], v[88:91], v[40:43]\n\t"
        "v_mfma_f32_16x16x32_bf16 v[44:47], a[188:191], v[88:91], v[44:47]\n\t"
        "s_waitcnt lgkmcnt(5)\n\t"
        "v_mfma_f32_16x16x32_bf16 v[36:39], a[204:207], v[92:95], v[36:39]\n\t"
        "ds_read_b128 v[108:111], v130 offset:12288\n\t"
        "v_mfma_f32_16x16x32_bf16 v[40:43], a[208:211], v[92:95], v[40:43]\n\t"
        "s_waitcnt lgkmcnt(4)\n\t"
        "v_lshlrev_b32_sdwa v157, 5, v160 dst_sel:DWORD dst_unused:UNUSED_PAD src0_sel:DWORD src1_sel:WORD_0\n\t"
        "v_lshlrev_b32_sdwa v158, 5, v160 dst_sel:DWORD dst_unused:UNUSED_PAD src0_sel:DWORD src1_sel:WORD_1\n\t"
        "v_mfma_f32_16x16x32_bf16 v[44:47], a[212:215], v[92:95], v[44:47]\n\t"
        "v_or_b32 v157, v184, v157\n\t"
        "v_or_b32 v158, v184, v158\n\t"
        "v_mfma_f32_16x16x32_bf16 v[0:3], a[228:231], v[96:99], v[0:3]\n\t"
        "v_xor_b32 v159, 32, v157\n\t"
        "v_xor_b32 v152, 32, v158\n\t"
        "v_mfma_f32_16x16x32_bf16 v[4:7], a[232:235], v[96:99], v[4:7]\n\t"
        "v_perm_b32 v164, v162, v162, v168\n\t"
        "v_perm_b32 v165, v162, v162, v169\n\t"
        "v_mfma_f32_16x16x32_bf16 v[8:11], a[236:239], v[96:99], v[8:11]\n\t"
        "s_waitcnt vmcnt(3) lgkmcnt(2)\n\t"
        "s_barrier\n\t"
        "v_mfma_f32_16x16x32_bf16 v[0:3], a[216:219], v[100:103], v[0:3]\n\t"
        "ds_read_b64_tr_b16 v[60:61], v157 offset:0\n\t"
        "v_perm_b32 v166, v162, v162, v170\n\t"
        "v_mfma_f32_16x16x32_bf16 v[4:7], a[220:223], v[100:103], v[4:7]\n\t"
        "ds_read_b64_tr_b16 v[62:63], v158 offset:0\n\t"
        "v_perm_b32 v167, v162, v162, v171\n\t"
        "v_mfma_f32_16x16x32_bf16 v[8:11], a[224:227], v[100:103], v[8:11]\n\t"
        "ds_read_b64_tr_b16 v[64:65], v159 offset:0\n\t"
        "v_cmp_ne_u32 vcc, 0, v163\n\t"
        "v_mfma_f32_16x16x32_bf16 v[12:15], a[228:231], v[100:103], v[12:15]\n\t"
        "ds_read_b64_tr_b16 v[66:67], v152 offset:0\n\t"
        "ds_read_b128 v[112:115], v130 offset:16384\n\t"
        "v_mfma_f32_16x16x32_bf16 v[16:19], a[232:235], v[100:103], v[16:19]\n\t"
        "ds_read_b64_tr_b16 v[68:69], v157 offset:64\n\t"
        "ds_read_b64_tr_b16 a[252:253], v131 offset:24576\n\t"
        "v_mfma_f32_16x16x32_bf16 v[20:23], a[236:239], v[100:103], v[20:23]\n\t"
        "ds_read_b64_tr_b16 v[70:71], v158 offset:64\n\t"
        "ds_read_b64_tr_b16 a[254:255], v131 offset:25344\n\t"
        "s_waitcnt lgkmcnt(9)\n\t"
        "v_mfma_f32_16x16x32_bf16 v[0:3], a[240:243], v[104:107], v[0:3]\n\t"
        "ds_read_b64_tr_b16 v[72:73], v159 offset:64\n\t"
        "ds_read_b64_tr_b16 v[188:189], v132 offset:24576\n\t"
        "v_mfma_f32_16x16x32_bf16 v[4:7], a[244:247], v[104:107], v[4:7]\n\t"
        "ds_read_b64_tr_b16 v[74:75], v152 offset:64\n\t"
        "ds_read_b64_tr_b16 v[190:191], v132 offset:25344\n\t"
        "v_mfma_f32_16x16x32_bf16 v[8:11], a[248:251], v[104:107], v[8:11]\n\t"
        "ds_read_b64_tr_b16 v[76:77], v157 offset:128\n\t"
        "ds_read_b64_tr_b16 v[192:193], v133 offset:24576\n\t"
        "v_mfma_f32_16x16x32_bf16 v[12:15], a[216:219], v[104:107], v[12:15]\n\t"
        "ds_read_b64_tr_b16 v[78:79], v158 offset:128\n\t"
        "ds_read_b128 v[84:87], v130 offset:20480\n\t"
        "v_mfma_f32_16x16x32_bf16 v[16:19], a[220:223], v[104:107], v[16:19]\n\t"
        "ds_read_b64_tr_b16 v[80:81], v159 offset:128\n\t"
        "ds_read_b64_tr_b16 v[194:195], v133 offset:25344\n\t"
        "s_waitcnt lgkmcnt(15)\n\t"
        "v_mfma_f32_16x16x32_bf16 v[48:51], v[60:63], v[164:167], 0\n\t"
        "ds_read_b64_tr_b16 v[82:83], v152 offset:128\n\t"
        "v_mfma_f32_16x16x32_bf16 v[20:23], a[224:227], v[104:107], v[20:23]\n\t"
        "ds_read_b64_tr_b16 v[196:197], v131 offset:30720\n\t"
        "v_mfma_f32_16x16x32_bf16 v[52:55], v[64:67], v[164:167], 0\n\t"
        "ds_read_b64_tr_b16 v[198:199], v131 offset:31488\n\t"
        "v_mfma_f32_16x16x32_bf16 v[24:27], a[228:231], v[104:107], v[24:27]\n\t"
        "ds_read_b64_tr_b16 v[200:201], v132 offset:30720\n\t"
        "s_waitcnt lgkmcnt(14)\n\t"
        "v_mfma_f32_16x16x32_bf16 v[56:59], v[68:71], v[164:167], 0\n\t"
        "ds_read_b64_tr_b16 v[202:203], v132 offset:31488\n\t"
        "v_mfma_f32_16x16x32_bf16 v[28:31], a[232:235], v[104:107], v[28:31]\n\t"
        "ds_read_b64_tr_b16 v[204:205], v133 offset:30720\n\t"
        "s_waitcnt lgkmcnt(12)\n\t"
        "v_mfma_f32_16x16x32_bf16 v[116:119], v[72:75], v[164:167], 0\n\t"
        "ds_read_b64_tr_b16 v[206:207], v133 offset:31488\n\t"
        "v_mfma_f32_16x16x32_bf16 v[32:35], a[236:239], v[104:107], v[32:35]\n\t"
        "ds_read_b128 v[88:91], v130 offset:64\n\t"
        "s_waitcnt lgkmcnt(11)\n\t"
        "v_mfma_f32_16x16x32_bf16 v[120:123], v[76:79], v[164:167], 0\n\t"
        "ds_read_b64_tr_b16 v[208:209], v131 offset:36864\n\t"
        "v_mfma_f32_16x16x32_bf16 v[12:15], a[240:243], v[108:111], v[12:15]\n\t"
        "ds_read_b128 v[92:95], v130 offset:4160\n\t"
        ".LpM1%=:\n\t"
        "s_cbranch_execz .LrM%=\n\t"
        "s_waitcnt lgkmcnt(9)\n\t"
        "v_mfma_f32_16x16x32_bf16 v[124:127], v[80:83], v[164:167], 0\n\t"
        "ds_read_b64_tr_b16 v[210:211], v131 offset:37632\n\t"
        "s_cbranch_vccz .Lx46%=\n\t"
        "v_lshlrev_b32_sdwa v157, 5, v161 dst_sel:DWORD dst_unused:UNUSED_PAD src0_sel:DWORD src1_sel:WORD_0\n\t"
        "v_lshlrev_b32_sdwa v158, 5, v161 dst_sel:DWORD dst_unused:UNUSED_PAD src0_sel:DWORD src1_sel:WORD_1\n\t"
        "v_or_b32 v157, v184, v157\n\t"
        "v_or_b32 v158, v184, v158\n\t"
        "v_xor_b32 v159, 32, v157\n\t"
        "v_xor_b32 v152, 32, v158\n\t"
        "ds_read_b64_tr_b16 v[60:61], v157 offset:0\n\t"
        "ds_read_b64_tr_b16 v[62:63], v158 offset:0\n\t"
        "ds_read_b64_tr_b16 v[64:65], v159 offset:0\n\t"
        "ds_read_b64_tr_b16 v[66:67], v152 offset:0\n\t"
        "ds_read_b64_tr_b16 v[68:69], v157 offset:64\n\t"
        "ds_read_b64_tr_b16 v[70:71], v158 offset:64\n\t"
        "ds_read_b64_tr_b16 v[72:73], v159 offset:64\n\t"
        "ds_read_b64_tr_b16 v[74:75], v152 offset:64\n\t"
        "ds_read_b64_tr_b16 v[76:77], v157 offset:128\n\t"
        "ds_read_b64_tr_b16 v[78:79], v158 offset:128\n\t"
        "ds_read_b64_tr_b16 v[80:81], v159 offset:128\n\t"
        "ds_read_b64_tr_b16 v[82:83], v152 offset:128\n\t"
        "v_perm_b32 v164, v163, v163, v168\n\t"
        "v_perm_b32 v165, v163, v163, v169\n\t"
        "v_perm_b32 v166, v163, v163, v170\n\t"
        "v_perm_b32 v167, v163, v163, v171\n\t"
        "s_waitcnt lgkmcnt(10)\n\t"
        "s_nop 0\n\t"
        "v_mfma_f32_16x16x32_bf16 v[48:51], v[60:63], v[164:167], v[48:51]\n\t"
        "s_waitcnt lgkmcnt(8)\n\t"
        "v_mfma_f32_16x16x32_bf16 v[52:55], v[64:67], v[164:167], v[52:55]\n\t"
        "s_waitcnt lgkmcnt(6)\n\t"
        "v_mfma_f32_16x16x32_bf16 v[56:59], v[68:71], v[164:167], v[56:59]\n\t"
        "s_waitcnt lgkmcnt(4)\n\t"
        "v_mfma_f32_16x16x32_bf16 v[116:119], v[72:75], v[164:167], v[116:119]\n\t"
        "s_waitcnt lgkmcnt(2)\n\t"
        "v_mfma_f32_16x16x32_bf16 v[120:123], v[76:79], v[164:167], v[120:123]\n\t"
        "s_waitcnt lgkmcnt(0)\n\t"
        "v_mfma_f32_16x16x32_bf16 v[124:127], v[80:83], v[164:167], v[124:127]\n\t"
        "s_nop 4\n\t"
        ".Lx46%=:\n\t"
        "v_mfma_f32_16x16x32_bf16 v[16:19], a[244:247], v[108:111], v[16:19]\n\t"
        "ds_read_b128 v[160:163], v185 offset:256\n\t"
        "v_cvt_pk_bf16_f32 v48, v48, v49\n\t"
        "v_mfma_f32_16x16x32_bf16 v[20:23], a[248:251], v[108:111], v[20:23]\n\t"
        "ds_read_b64_tr_b16 v[212:213], v132 offset:36864\n\t"
        "v_cvt_pk_bf16_f32 v49, v50, v51\n\t"
        "v_mfma_f32_16x16x32_bf16 v[24:27], a[216:219], v[108:111], v[24:27]\n\t"
        "ds_write_b64 v186, v[48:49] offset:0\n\t"
        "v_mfma_f32_16x16x32_bf16 v[28:31], a[220:223], v[108:111], v[28:31]\n\t"
        "s_waitcnt vmcnt(0) lgkmcnt(7)\n\t"
        "s_barrier\n\t"
        "v_mfma_f32_16x16x32_bf16 v[32:35], a[224:227], v[108:111], v[32:35]\n\t"
        "ds_read_b64_tr_b16 v[214:215], v132 offset:37632\n\t"
        "v_cvt_pk_bf16_f32 v52, v52, v53\n\t"
        "v_mfma_f32_16x16x32_bf16 v[36:39], a[228:231], v[108:111], v[36:39]\n\t"
        "ds_read_b64_tr_b16 v[216:217], v133 offset:36864\n\t"
        "v_cvt_pk_bf16_f32 v53, v54, v55\n\t"
        "v_mfma_f32_16x16x32_bf16 v[40:43], a[232:235], v[108:111], v[40:43]\n\t"
        "ds_write_b64 v154, v[52:53] offset:0\n\t"
        "v_mfma_f32_16x16x32_bf16 v[44:47], a[236:239], v[108:111], v[44:47]\n\t"
        "ds_read_b64_tr_b16 v[218:219], v133 offset:37632\n\t"
        "v_cvt_pk_bf16_f32 v56, v56, v57\n\t"
        "v_mfma_f32_16x16x32_bf16 v[24:27], a[240:243], v[112:115], v[24:27]\n\t"
        "ds_read_b128 v[96:99], v130 offset:8256\n\t"
        "v_cvt_pk_bf16_f32 v57, v58, v59\n\t"
        "v_mfma_f32_16x16x32_bf16 v[28:31], a[244:247], v[112:115], v[28:31]\n\t"
        "ds_write_b64 v186, v[56:57] offset:64\n\t"
        "v_mfma_f32_16x16x32_bf16 v[32:35], a[248:251], v[112:115], v[32:35]\n\t"
        "ds_read_b64_tr_b16 v[220:221], v131 offset:43008\n\t"
        "v_cvt_pk_bf16_f32 v116, v116, v117\n\t"
        "v_mfma_f32_16x16x32_bf16 v[36:39], a[216:219], v[112:115], v[36:39]\n\t"
        "ds_read_b64_tr_b16 v[222:223], v131 offset:43776\n\t"
        "v_cvt_pk_bf16_f32 v117, v118, v119\n\t"
        "v_mfma_f32_16x16x32_bf16 v[40:43], a[220:223], v[112:115], v[40:43]\n\t"
        "ds_write_b64 v154, v[116:117] offset:64\n\t"
        "v_mfma_f32_16x16x32_bf16 v[44:47], a[224:227], v[112:115], v[44:47]\n\t"
        "ds_read_b64_tr_b16 v[224:225], v132 offset:43008\n\t"
        "v_cvt_pk_bf16_f32 v120, v120, v121\n\t"
        "v_mfma_f32_16x16x32_bf16 v[36:39], a[240:243], v[84:87], v[36:39]\n\t"
        "ds_read_b128 v[100:103], v130 offset:12352\n\t"
        "v_cvt_pk_bf16_f32 v121, v122, v123\n\t"
        "v_mfma_f32_16x16x32_bf16 v[40:43], a[244:247], v[84:87], v[40:43]\n\t"
        "ds_write_b64 v186, v[120:121] offset:128\n\t"
        "v_mfma_f32_16x16x32_bf16 v[44:47], a[248:251], v[84:87], v[44:47]\n\t"
        "ds_read_b64_tr_b16 v[226:227], v132 offset:43776\n\t"
        "v_cvt_pk_bf16_f32 v124, v124, v125\n\t"
        "s_waitcnt lgkmcnt(9)\n\t"
        "v_mfma_f32_16x16x32_bf16 v[0:3], v[196:199], v[88:91], v[0:3]\n\t"
        "ds_read_b64_tr_b16 v[228:229], v133 offset:43008\n\t"
        "v_cvt_pk_bf16_f32 v125, v126, v127\n\t"
        "v_mfma_f32_16x16x32_bf16 v[4:7], v[200:203], v[88:91], v[4:7]\n\t"
        "ds_write_b64 v154, v[124:125] offset:128\n\t"
        "v_mfma_f32_16x16x32_bf16 v[8:11], v[204:207], v[88:91], v[8:11]\n\t"
        "ds_read_b64_tr_b16 v[230:231], v133 offset:43776\n\t"
        "v_lshlrev_b32_sdwa v157, 5, v160 dst_sel:DWORD dst_unused:UNUSED_PAD src0_sel:DWORD src1_sel:WORD_0\n\t"
        "v_mfma_f32_16x16x32_bf16 v[0:3], a[252:255], v[92:95], v[0:3]\n\t"
        "ds_read_b64_tr_b16 v[232:233], v131 offset:49152\n\t"
        "v_lshlrev_b32_sdwa v158, 5, v160 dst_sel:DWORD dst_unused:UNUSED_PAD src0_sel:DWORD src1_sel:WORD_1\n\t"
        "v_mfma_f32_16x16x32_bf16 v[4:7], v[188:191], v[92:95], v[4:7]\n\t"
        "ds_read_b64_tr_b16 v[234:235], v131 offset:49920\n\t"
        "v_or_b32 v157, v184, v157\n\t"
        "v_mfma_f32_16x16x32_bf16 v[8:11], v[192:195], v[92:95], v[8:11]\n\t"
        "ds_read_b64_tr_b16 v[60:61], v157 offset:0\n\t"
        "v_or_b32 v158, v184, v158\n\t"
        "v_mfma_f32_16x16x32_bf16 v[12:15], v[196:199], v[92:95], v[12:15]\n\t"
        "ds_read_b64_tr_b16 v[62:63], v158 offset:0\n\t"
        "v_xor_b32 v159, 32, v157\n\t"
        "v_mfma_f32_16x16x32_bf16 v[16:19], v[200:203], v[92:95], v[16:19]\n\t"
        "ds_read_b64_tr_b16 v[64:65], v159 offset:0\n\t"
        "v_xor_b32 v152, 32, v158\n\t"
        "v_mfma_f32_16x16x32_bf16 v[20:23], v[204:207], v[92:95], v[20:23]\n\t"
        "ds_read_b64_tr_b16 v[66:67], v152 offset:0\n\t"
        "v_perm_b32 v164, v162, v162, v168\n\t"
        "s_waitcnt lgkmcnt(9)\n\t"
        "v_mfma_f32_16x16x32_bf16 v[0:3], v[208:211], v[96:99], v[0:3]\n\t"
        "ds_read_b64_tr_b16 v[68:69], v157 offset:64\n\t"
        "v_perm_b32 v165, v162, v162, v169\n\t"
        "v_mfma_f32_16x16x32_bf16 v[4:7], v[212:215], v[96:99], v[4:7]\n\t"
        "ds_read_b64_tr_b16 v[70:71], v158 offset:64\n\t"
        "v_perm_b32 v166, v162, v162, v170\n\t"
        "v_mfma_f32_16x16x32_bf16 v[8:11], v[216:219], v[96:99], v[8:11]\n\t"
        "ds_read_b128 v[104:107], v130 offset:16448\n\t"
        "v_perm_b32 v167, v162, v162, v171\n\t"
        "v_mfma_f32_16x16x32_bf16 v[12:15], a[252:255], v[96:99], v[12:15]\n\t"
        "ds_read_b64_tr_b16 v[72:73], v159 offset:64\n\t"
        "v_cmp_ne_u32 vcc, 0, v163\n\t"
        "v_mfma_f32_16x16x32_bf16 v[16:19], v[188:191], v[96:99], v[16:19]\n\t"
        "ds_read_b64_tr_b16 v[74:75], v152 offset:64\n\t"
        "v_mfma_f32_16x16x32_bf16 v[20:23], v[192:195], v[96:99], v[20:23]\n\t"
        "ds_read_b64_tr_b16 v[76:77], v157 offset:128\n\t"
        "v_mfma_f32_16x16x32_bf16 v[24:27], v[196:199], v[96:99], v[24:27]\n\t"
        "ds_read_b64_tr_b16 v[78:79], v158 offset:128\n\t"
        "s_waitcnt lgkmcnt(9)\n\t"
        "v_mfma_f32_16x16x32_bf16 v[48:51], v[60:63], v[164:167], 0\n\t"
        "ds_read_b64_tr_b16 v[80:81], v159 offset:128\n\t"
        "v_mfma_f32_16x16x32_bf16 v[28:31], v[200:203], v[96:99], v[28:31]\n\t"
        "ds_read_b64_tr_b16 v[82:83], v152 offset:128\n\t"
        "s_waitcnt lgkmcnt(9)\n\t"
        "v_mfma_f32_16x16x32_bf16 v[52:55], v[64:67], v[164:167], 0\n\t"
        "ds_read_b128 v[108:111], v130 offset:20544\n\t"
        "v_mfma_f32_16x16x32_bf16 v[32:35], v[204:207], v[96:99], v[32:35]\n\t"
        "ds_read_b64_tr_b16 v[236:237], v132 offset:49152\n\t"
        "s_waitcnt lgkmcnt(9)\n\t"
        "v_mfma_f32_16x16x32_bf16 v[56:59], v[68:71], v[164:167], 0\n\t"
        "ds_read_b64_tr_b16 v[238:239], v132 offset:49920\n\t"
        "v_mfma_f32_16x16x32_bf16 v[12:15], v[208:211], v[100:103], v[12:15]\n\t"
        "ds_read_b64_tr_b16 v[240:241], v133 offset:49152\n\t"
        "ds_read_b64_tr_b16 v[242:243], v133 offset:49920\n\t"
        "v_mfma_f32_16x16x32_bf16 v[16:19], v[212:215], v[100:103], v[16:19]\n\t"
        "ds_read_b128 v[112:115], v130 offset:128\n\t"
        "s_waitcnt lgkmcnt(10)\n\t"
        "v_mfma_f32_16x16x32_bf16 v[116:119], v[72:75], v[164:167], 0\n\t"
        "ds_read_b128 v[84:87], v130 offset:4224\n\t"
        "v_mfma_f32_16x16x32_bf16 v[20:23], v[216:219], v[100:103], v[20:23]\n\t"
        "ds_read_b64_tr_b16 v[244:245], v131 offset:55296\n\t"
        "s_waitcnt lgkmcnt(10)\n\t"
        "v_mfma_f32_16x16x32_bf16 v[120:123], v[76:79], v[164:167], 0\n\t"
        "ds_read_b64_tr_b16 v[246:247], v131 offset:56064\n\t"
        "v_mfma_f32_16x16x32_bf16 v[24:27], a[252:255], v[100:103], v[24:27]\n\t"
        "ds_read_b64_tr_b16 v[248:249], v132 offset:55296\n\t"
        "s_waitcnt lgkmcnt(10)\n\t"
        "v_mfma_f32_16x16x32_bf16 v[124:127], v[80:83], v[164:167], 0\n\t"
        "ds_read_b64_tr_b16 v[250:251], v132 offset:56064\n\t"
        "s_cbranch_vccz .Lx48%=\n\t"
        "v_lshlrev_b32_sdwa v157, 5, v161 dst_sel:DWORD dst_unused:UNUSED_PAD src0_sel:DWORD src1_sel:WORD_0\n\t"
        "v_lshlrev_b32_sdwa v158, 5, v161 dst_sel:DWORD dst_unused:UNUSED_PAD src0_sel:DWORD src1_sel:WORD_1\n\t"
        "v_or_b32 v157, v184, v157\n\t"
        "v_or_b32 v158, v184, v158\n\t"
        "v_xor_b32 v159, 32, v157\n\t"
        "v_xor_b32 v152, 32, v158\n\t"
        "ds_read_b64_tr_b16 v[60:61], v157 offset:0\n\t"
        "ds_read_b64_tr_b16 v[62:63], v158 offset:0\n\t"
        "ds_read_b64_tr_b16 v[64:65], v159 offset:0\n\t"
        "ds_read_b64_tr_b16 v[66:67], v152 offset:0\n\t"
        "ds_read_b64_tr_b16 v[68:69], v157 offset:64\n\t"
        "ds_read_b64_tr_b16 v[70:71], v158 offset:64\n\t"
        "ds_read_b64_tr_b16 v[72:73], v159 offset:64\n\t"
        "ds_read_b64_tr_b16 v[74:75], v152 offset:64\n\t"
        "ds_read_b64_tr_b16 v[76:77], v157 offset:128\n\t"
        "ds_read_b64_tr_b16 v[78:79], v158 offset:128\n\t"
        "ds_read_b64_tr_b16 v[80:81], v159 offset:128\n\t"
        "ds_read_b64_tr_b16 v[82:83], v152 offset:128\n\t"
        "v_perm_b32 v164, v163, v163, v168\n\t"
        "v_perm_b32 v165, v163, v163, v169\n\t"
        "v_perm_b32 v166, v163, v163, v170\n\t"
        "v_perm_b32 v167, v163, v163, v171\n\t"
        "s_waitcnt lgkmcnt(10)\n\t"
        "s_nop 0\n\t"
        "v_mfma_f32_16x16x32_bf16 v[48:51], v[60:63], v[164:167], v[48:51]\n\t"
        "s_waitcnt lgkmcnt(8)\n\t"
        "v_mfma_f32_16x16x32_bf16 v[52:55], v[64:67], v[164:167], v[52:55]\n\t"
        "s_waitcnt lgkmcnt(6)\n\t"
        "v_mfma_f32_16x16x32_bf16 v[56:59], v[68:71], v[164:167], v[56:59]\n\t"
        "s_waitcnt lgkmcnt(4)\n\t"
        "v_mfma_f32_16x16x32_bf16 v[116:119], v[72:75], v[164:167], v[116:119]\n\t"
        "s_waitcnt lgkmcnt(2)\n\t"
        "v_mfma_f32_16x16x32_bf16 v[120:123], v[76:79], v[164:167], v[120:123]\n\t"
        "s_waitcnt lgkmcnt(0)\n\t"
        "v_mfma_f32_16x16x32_bf16 v[124:127], v[80:83], v[164:167], v[124:127]\n\t"
        "s_nop 4\n\t"
        ".Lx48%=:\n\t"
        "v_mfma_f32_16x16x32_bf16 v[28:31], v[188:191], v[100:103], v[28:31]\n\t"
        "ds_read_b64_tr_b16 v[252:253], v133 offset:55296\n\t"
        "v_cvt_pk_bf16_f32 v48, v48, v49\n\t"
        "v_mfma_f32_16x16x32_bf16 v[32:35], v[192:195], v[100:103], v[32:35]\n\t"
        "ds_read_b64_tr_b16 v[254:255], v133 offset:56064\n\t"
        "v_cvt_pk_bf16_f32 v49, v50, v51\n\t"
        "v_mfma_f32_16x16x32_bf16 v[36:39], v[196:199], v[100:103], v[36:39]\n\t"
        "ds_write_b64 v186, v[48:49] offset:12288\n\t"
        "v_mfma_f32_16x16x32_bf16 v[40:43], v[200:203], v[100:103], v[40:43]\n\t"
        "ds_read_b128 v[88:91], v130 offset:8320\n\t"
        "v_cvt_pk_bf16_f32 v52, v52, v53\n\t"
        "v_mfma_f32_16x16x32_bf16 v[44:47], v[204:207], v[100:103], v[44:47]\n\t"
        "v_cvt_pk_bf16_f32 v53, v54, v55\n\t"
        "v_cvt_pk_bf16_f32 v56, v56, v57\n\t"
        "v_mfma_f32_16x16x32_bf16 v[24:27], v[208:211], v[104:107], v[24:27]\n\t"
        "ds_write_b64 v154, v[52:53] offset:12288\n\t"
        "v_mfma_f32_16x16x32_bf16 v[28:31], v[212:215], v[104:107], v[28:31]\n\t"
        "v_cvt_pk_bf16_f32 v57, v58, v59\n\t"
        "v_cvt_pk_bf16_f32 v116, v116, v117\n\t"
        "v_mfma_f32_16x16x32_bf16 v[32:35], v[216:219], v[104:107], v[32:35]\n\t"
        "ds_write_b64 v186, v[56:57] offset:12352\n\t"
        "v_mfma_f32_16x16x32_bf16 v[36:39], a[252:255], v[104:107], v[36:39]\n\t"
        "v_cvt_pk_bf16_f32 v117, v118, v119\n\t"
        "v_cvt_pk_bf16_f32 v120, v120, v121\n\t"
        "v_mfma_f32_16x16x32_bf16 v[40:43], v[188:191], v[104:107], v[40:43]\n\t"
        "ds_write_b64 v154, v[116:117] offset:12352\n\t"
        "v_mfma_f32_16x16x32_bf16 v[44:47], v[192:195], v[104:107], v[44:47]\n\t"
        "v_cvt_pk_bf16_f32 v121, v122, v123\n\t"
        "v_cvt_pk_bf16_f32 v124, v124, v125\n\t"
        "s_waitcnt lgkmcnt(5)\n\t"
        "v_mfma_f32_16x16x32_bf16 v[36:39], v[208:211], v[108:111], v[36:39]\n\t"
        "ds_write_b64 v186, v[120:121] offset:12416\n\t"
        "v_mfma_f32_16x16x32_bf16 v[40:43], v[212:215], v[108:111], v[40:43]\n\t"
        "ds_read_b128 v[92:95], v130 offset:12416\n\t"
        "v_cvt_pk_bf16_f32 v125, v126, v127\n\t"
        "v_mfma_f32_16x16x32_bf16 v[44:47], v[216:219], v[108:111], v[44:47]\n\t"
        "ds_write_b64 v154, v[124:125] offset:12416\n\t"
        "v_mfma_f32_16x16x32_bf16 v[0:3], v[232:235], v[112:115], v[0:3]\n\t"
        "v_mfma_f32_16x16x32_bf16 v[4:7], v[236:239], v[112:115], v[4:7]\n\t"
        "v_mfma_f32_16x16x32_bf16 v[8:11], v[240:243], v[112:115], v[8:11]\n\t"
        "v_mfma_f32_16x16x32_bf16 v[0:3], v[220:223], v[84:87], v[0:3]\n\t"
        "v_mfma_f32_16x16x32_bf16 v[4:7], v[224:227], v[84:87], v[4:7]\n\t"
        "v_mfma_f32_16x16x32_bf16 v[8:11], v[228:231], v[84:87], v[8:11]\n\t"
        "s_waitcnt lgkmcnt(0)\n\t"
        "s_barrier\n\t"
        "v_mfma_f32_16x16x32_bf16 v[12:15], v[232:235], v[84:87], v[12:15]\n\t"
        "ds_read_b128 v[124:127], v182 offset:0\n\t"
        "v_mfma_f32_16x16x32_bf16 v[16:19], v[236:239], v[84:87], v[16:19]\n\t"
        "ds_read_b128 v[132:135], v187 offset:0\n\t"
        "v_mfma_f32_16x16x32_bf16 v[20:23], v[240:243], v[84:87], v[20:23]\n\t"
        "ds_read_b128 v[136:139], v187 offset:16\n\t"
        "v_mfma_f32_16x16x32_bf16 v[0:3], v[244:247], v[88:91], v[0:3]\n\t"
        "ds_read_b32 v140, v187 offset:32\n\t"
        "v_mfma_f32_16x16x32_bf16 v[4:7], v[248:251], v[88:91], v[4:7]\n\t"
        "ds_read_b128 v[96:99], v130 offset:16512\n\t"
        "v_mfma_f32_16x16x32_bf16 v[8:11], v[252:255], v[88:91], v[8:11]\n\t"
        "v_mfma_f32_16x16x32_bf16 v[12:15], v[220:223], v[88:91], v[12:15]\n\t"
        "ds_read_b128 v[100:103], v130 offset:20608\n\t"
        "v_mfma_f32_16x16x32_bf16 v[16:19], v[224:227], v[88:91], v[16:19]\n\t"
        "v_mfma_f32_16x16x32_bf16 v[20:23], v[228:231], v[88:91], v[20:23]\n\t"
        "v_mfma_f32_16x16x32_bf16 v[24:27], v[232:235], v[88:91], v[24:27]\n\t"
        "s_waitcnt lgkmcnt(5)\n\t"
        "v_or3_b32 v124, v124, v125, v126\n\t"
        "v_or_b32 v124, v124, v127\n\t"
        "v_mfma_f32_16x16x32_bf16 v[28:31], v[236:239], v[88:91], v[28:31]\n\t"
        "v_readfirstlane_b32 s6, v124\n\t"
        "v_mfma_f32_16x16x32_bf16 v[32:35], v[240:243], v[88:91], v[32:35]\n\t"
        "s_nop 0\n\t"
        "s_bitcmp1_b32 s6, 1\n\t"
        "s_cbranch_scc1 .Lx50%=\n\t"
        ".Lx51%=:\n\t"
        "v_mfma_f32_16x16x32_bf16 v[12:15], v[244:247], v[92:95], v[12:15]\n\t"
        "ds_read_b128 v[160:163], v185 offset:512\n\t"
        "s_waitcnt lgkmcnt(4)\n\t"
        "v_xor_b32 v132, v181, v132\n\t"
        "v_mfma_f32_16x16x32_bf16 v[16:19], v[248:251], v[92:95], v[16:19]\n\t"
        "v_add_u32 v151, s9, v172\n\t"
        "v_max_u32 v151, v151, v132\n\t"
        "v_mfma_f32_16x16x32_bf16 v[20:23], v[252:255], v[92:95], v[20:23]\n\t"
        "ds_read_b128 v[104:107], v151 offset:0\n\t"
        "v_xor_b32 v133, v181, v133\n\t"
        "v_mfma_f32_16x16x32_bf16 v[24:27], v[220:223], v[92:95], v[24:27]\n\t"
        "ds_read_b128 v[108:111], v151 offset:64\n\t"
        "v_add_u32 v183, s9, v173\n\t"
        "v_mfma_f32_16x16x32_bf16 v[28:31], v[224:227], v[92:95], v[28:31]\n\t"
        "ds_read_b128 v[112:115], v151 offset:128\n\t"
        "v_max_u32 v183, v183, v133\n\t"
        "v_xor_b32 v134, v181, v134\n\t"
        "v_mfma_f32_16x16x32_bf16 v[32:35], v[228:231], v[92:95], v[32:35]\n\t"
        "v_add_u32 v151, s9, v174\n\t"
        "v_max_u32 v151, v151, v134\n\t"
        "v_mfma_f32_16x16x32_bf16 v[36:39], v[232:235], v[92:95], v[36:39]\n\t"
        "ds_read_b128 v[84:87], v183 offset:0\n\t"
        "v_xor_b32 v135, v181, v135\n\t"
        "v_mfma_f32_16x16x32_bf16 v[40:43], v[236:239], v[92:95], v[40:43]\n\t"
        "s_add_u32 s55, s41, 0\n\t"
        "v_cvt_pk_bf16_f32 v116, v0, v1\n\t"
        "v_mfma_f32_16x16x32_bf16 v[44:47], v[240:243], v[92:95], v[44:47]\n\t"
        "v_cvt_pk_bf16_f32 v117, v2, v3\n\t"
        "v_cvt_pk_bf16_f32 v118, v4, v5\n\t"
        "s_waitcnt lgkmcnt(5)\n\t"
        "v_mfma_f32_16x16x32_bf16 v[24:27], v[244:247], v[96:99], v[24:27]\n\t"
        "ds_read_b128 v[88:91], v183 offset:64\n\t"
        "v_cvt_pk_bf16_f32 v119, v6, v7\n\t"
        "v_cvt_pk_bf16_f32 v120, v8, v9\n\t"
        "v_mfma_f32_16x16x32_bf16 v[28:31], v[248:251], v[96:99], v[28:31]\n\t"
        "v_cvt_pk_bf16_f32 v121, v10, v11\n\t"
        "s_waitcnt lgkmcnt(5)\n\t"
        "v_lshlrev_b32_sdwa v157, 5, v160 dst_sel:DWORD dst_unused:UNUSED_PAD src0_sel:DWORD src1_sel:WORD_0\n\t"
        "v_mfma_f32_16x16x32_bf16 v[32:35], v[252:255], v[96:99], v[32:35]\n\t"
        "v_permlane16_swap_b32 v116, v118\n\t"
        "v_mfma_f32_16x16x32_bf16 v[36:39], v[220:223], v[96:99], v[36:39]\n\t"
        "v_permlane16_swap_b32 v117, v119\n\t"
        "v_mfma_f32_16x16x32_bf16 v[40:43], v[224:227], v[96:99], v[40:43]\n\t"
        "buffer_store_dwordx4 v[116:119], v144, s[28:31], s55 offen sc1\n\t"
        "v_mfma_f32_16x16x32_bf16 v[44:47], v[228:231], v[96:99], v[44:47]\n\t"
        "ds_write_b64 v146, v[120:121] offset:0\n\t"
        "v_mfma_f32_16x16x32_bf16 v[36:39], v[244:247], v[100:103], v[36:39]\n\t"
        "ds_read_b128 v[92:95], v183 offset:128\n\t"
        "v_add_u32 v183, s9, v175\n\t"
        "v_max_u32 v183, v183, v135\n\t"
        "v_mfma_f32_16x16x32_bf16 v[40:43], v[248:251], v[100:103], v[40:43]\n\t"
        "ds_read_b128 v[96:99], v151 offset:0\n\t"
        "v_xor_b32 v136, v181, v136\n\t"
        "v_mfma_f32_16x16x32_bf16 v[44:47], v[252:255], v[100:103], v[44:47]\n\t"
        "s_add_u32 s55, s41, 43008\n\t"
        "v_cvt_pk_bf16_f32 v116, v12, v13\n\t"
        "s_waitcnt lgkmcnt(3)\n\t"
        "v_mfma_f32_16x16x32_bf16 v[48:51], a[12:15], v[104:107], 0\n\t"
        "ds_read_b128 v[100:103], v151 offset:64\n\t"
        "v_cvt_pk_bf16_f32 v117, v14, v15\n\t"
        "v_mfma_f32_16x16x32_bf16 v[52:55], a[16:19], v[104:107], 0\n\t"
        "v_cvt_pk_bf16_f32 v118, v16, v17\n\t"
        "v_cvt_pk_bf16_f32 v119, v18, v19\n\t"
        "v_cvt_pk_bf16_f32 v120, v20, v21\n\t"
        "v_mfma_f32_16x16x32_bf16 v[56:59], a[20:23], v[104:107], 0\n\t"
        "v_cvt_pk_bf16_f32 v121, v22, v23\n\t"
        "v_lshlrev_b32_sdwa v158, 5, v160 dst_sel:DWORD dst_unused:UNUSED_PAD src0_sel:DWORD src1_sel:WORD_1\n\t"
        "v_mfma_f32_16x16x32_bf16 v[48:51], a[48:51], v[108:111], v[48:51]\n\t"
        "v_permlane16_swap_b32 v116, v118\n\t"
        "v_mfma_f32_16x16x32_bf16 v[52:55], a[52:55], v[108:111], v[52:55]\n\t"
        "v_permlane16_swap_b32 v117, v119\n\t"
        "v_mfma_f32_16x16x32_bf16 v[56:59], a[56:59], v[108:111], v[56:59]\n\t"
        "ds_read_b128 v[104:107], v151 offset:128\n\t"
        "v_add_u32 v151, s9, v176\n\t"
        "v_mfma_f32_16x16x32_bf16 v[48:51], a[84:87], v[112:115], v[48:51]\n\t"
        "ds_read_b128 v[108:111], v183 offset:0\n\t"
        "v_max_u32 v151, v151, v136\n\t"
        "v_xor_b32 v137, v181, v137\n\t"
        "v_mfma_f32_16x16x32_bf16 v[52:55], a[88:91], v[112:115], v[52:55]\n\t"
        "v_or_b32 v157, v184, v157\n\t"
        "v_or_b32 v158, v184, v158\n\t"
        "v_mfma_f32_16x16x32_bf16 v[56:59], a[92:95], v[112:115], v[56:59]\n\t"
        "ds_read_b64_tr_b16 v[60:61], v157 offset:0\n\t"
        "v_xor_b32 v159, 32, v157\n\t"
        "v_mfma_f32_16x16x32_bf16 v[48:51], a[120:123], v[84:87], v[48:51]\n\t"
        "buffer_store_dwordx4 v[116:119], v144, s[28:31], s55 offen sc1\n\t"
        "v_mfma_f32_16x16x32_bf16 v[52:55], a[124:127], v[84:87], v[52:55]\n\t"
        "ds_write_b64 v146, v[120:121] offset:2048\n\t"
        "v_mfma_f32_16x16x32_bf16 v[56:59], a[128:131], v[84:87], v[56:59]\n\t"
        "ds_read_b64_tr_b16 v[62:63], v158 offset:0\n\t"
        "s_add_u32 s55, s41, 86016\n\t"
        "v_cvt_pk_bf16_f32 v116, v24, v25\n\t"
        "v_mfma_f32_16x16x32_bf16 v[48:51], a[156:159], v[88:91], v[48:51]\n\t"
        "ds_read_b64_tr_b16 v[64:65], v159 offset:0\n\t"
        "v_cvt_pk_bf16_f32 v117, v26, v27\n\t"
        "v_mfma_f32_16x16x32_bf16 v[52:55], a[160:163], v[88:91], v[52:55]\n\t"
        "ds_read_b64_tr_b16 v[68:69], v157 offset:64\n\t"
        "v_cvt_pk_bf16_f32 v118, v28, v29\n\t"
        "v_mfma_f32_16x16x32_bf16 v[56:59], a[164:167], v[88:91], v[56:59]\n\t"
        "ds_read_b128 v[112:115], v183 offset:64\n\t"
        "v_cvt_pk_bf16_f32 v119, v30, v31\n\t"
        "s_waitcnt lgkmcnt(6)\n\t"
        "v_mfma_f32_16x16x32_bf16 v[48:51], a[192:195], v[92:95], v[48:51]\n\t"
        "ds_read_b64_tr_b16 v[70:71], v158 offset:64\n\t"
        "v_cvt_pk_bf16_f32 v120, v32, v33\n\t"
        "v_mfma_f32_16x16x32_bf16 v[52:55], a[196:199], v[92:95], v[52:55]\n\t"
        "ds_read_b64_tr_b16 v[72:73], v159 offset:64\n\t"
        "v_cvt_pk_bf16_f32 v121, v34, v35\n\t"
        "v_xor_b32 v152, 32, v158\n\t"
        "v_mfma_f32_16x16x32_bf16 v[56:59], a[200:203], v[92:95], v[56:59]\n\t"
        "v_permlane16_swap_b32 v116, v118\n\t"
        "v_mfma_f32_16x16x32_bf16 v[48:51], a[228:231], v[96:99], v[48:51]\n\t"
        "v_permlane16_swap_b32 v117, v119\n\t"
        "v_mfma_f32_16x16x32_bf16 v[52:55], a[232:235], v[96:99], v[52:55]\n\t"
        "ds_read_b128 v[84:87], v183 offset:128\n\t"
        "v_add_u32 v183, s9, v177\n\t"
        "v_mfma_f32_16x16x32_bf16 v[56:59], a[236:239], v[96:99], v[56:59]\n\t"
        "ds_read_b128 v[88:91], v151 offset:0\n\t"
        "v_max_u32 v183, v183, v137\n\t"
        "v_mfma_f32_16x16x32_bf16 v[48:51], v[196:199], v[100:103], v[48:51]\n\t"
        "ds_read_b64_tr_b16 v[66:67], v152 offset:0\n\t"
        "v_xor_b32 v138, v181, v138\n\t"
        "v_perm_b32 v164, v162, v162, v168\n\t"
        "v_mfma_f32_16x16x32_bf16 v[52:55], v[200:203], v[100:103], v[52:55]\n\t"
        "ds_read_b64_tr_b16 v[74:75], v152 offset:64\n\t"
        "v_perm_b32 v165, v162, v162, v169\n\t"
        "v_mfma_f32_16x16x32_bf16 v[56:59], v[204:207], v[100:103], v[56:59]\n\t"
        "buffer_store_dwordx4 v[116:119], v144, s[28:31], s55 offen sc1\n\t"
        "v_mfma_f32_16x16x32_bf16 v[48:51], v[232:235], v[104:107], v[48:51]\n\t"
        "ds_write_b64 v146, v[120:121] offset:4096\n\t"
        "v_mfma_f32_16x16x32_bf16 v[52:55], v[236:239], v[104:107], v[52:55]\n\t"
        "ds_read_b128 v[92:95], v151 offset:64\n\t"
        "s_add_u32 s55, s41, 129024\n\t"
        "v_mfma_f32_16x16x32_bf16 v[56:59], v[240:243], v[104:107], v[56:59]\n\t"
        "ds_read_b128 v[96:99], v151 offset:128\n\t"
        "v_add_u32 v151, s9, v178\n\t"
        "v_max_u32 v151, v151, v138\n\t"
        "v_mfma_f32_16x16x32_bf16 v[48:51], a[0:3], v[108:111], v[48:51]\n\t"
        "ds_read_b64_tr_b16 v[76:77], v157 offset:128\n\t"
        "v_xor_b32 v139, v181, v139\n\t"
        "v_mfma_f32_16x16x32_bf16 v[52:55], a[4:7], v[108:111], v[52:55]\n\t"
        "ds_read_b64_tr_b16 v[78:79], v158 offset:128\n\t"
        "v_cvt_pk_bf16_f32 v116, v36, v37\n\t"
        "v_mfma_f32_16x16x32_bf16 v[56:59], a[8:11], v[108:111], v[56:59]\n\t"
        "ds_read_b128 v[100:103], v183 offset:0\n\t"
        "v_cvt_pk_bf16_f32 v117, v38, v39\n\t"
        "s_waitcnt lgkmcnt(7)\n\t"
        "v_mfma_f32_16x16x32_bf16 v[48:51], a[36:39], v[112:115], v[48:51]\n\t"
        "ds_read_b64_tr_b16 v[80:81], v159 offset:128\n\t"
        "v_cvt_pk_bf16_f32 v118, v40, v41\n\t"
        "v_mfma_f32_16x16x32_bf16 v[52:55], a[40:43], v[112:115], v[52:55]\n\t"
        "ds_read_b64_tr_b16 v[82:83], v152 offset:128\n\t"
        "v_cvt_pk_bf16_f32 v119, v42, v43\n\t"
        "v_cvt_pk_bf16_f32 v120, v44, v45\n\t"
        "v_mfma_f32_16x16x32_bf16 v[56:59], a[44:47], v[112:115], v[56:59]\n\t"
        "ds_read_b128 v[104:107], v183 offset:64\n\t"
        "v_cvt_pk_bf16_f32 v121, v46, v47\n\t"
        "v_mfma_f32_16x16x32_bf16 v[48:51], a[72:75], v[84:87], v[48:51]\n\t"
        "v_permlane16_swap_b32 v116, v118\n\t"
        "v_mfma_f32_16x16x32_bf16 v[52:55], a[76:79], v[84:87], v[52:55]\n\t"
        "v_permlane16_swap_b32 v117, v119\n\t"
        "v_mfma_f32_16x16x32_bf16 v[56:59], a[80:83], v[84:87], v[56:59]\n\t"
        "ds_read_b128 v[108:111], v183 offset:128\n\t"
        "v_add_u32 v183, s9, v179\n\t"
        "v_mfma_f32_16x16x32_bf16 v[48:51], a[108:111], v[88:91], v[48:51]\n\t"
        "ds_read_b128 v[112:115], v151 offset:0\n\t"
        "v_max_u32 v183, v183, v139\n\t"
        "v_perm_b32 v166, v162, v162, v170\n\t"
        "v_mfma_f32_16x16x32_bf16 v[52:55], a[112:115], v[88:91], v[52:55]\n\t"
        "buffer_store_dwordx4 v[116:119], v144, s[28:31], s55 offen sc1\n\t"
        "v_mfma_f32_16x16x32_bf16 v[56:59], a[116:119], v[88:91], v[56:59]\n\t"
        "ds_write_b64 v146, v[120:121] offset:6144\n\t"
        "s_waitcnt lgkmcnt(5)\n\t"
        "v_mfma_f32_16x16x32_bf16 v[48:51], a[144:147], v[92:95], v[48:51]\n\t"
        "ds_read_b128 v[84:87], v151 offset:64\n\t"
        "v_perm_b32 v167, v162, v162, v171\n\t"
        "v_mfma_f32_16x16x32_bf16 v[52:55], a[148:151], v[92:95], v[52:55]\n\t"
        "ds_read_b128 v[88:91], v151 offset:128\n\t"
        "v_cmp_ne_u32 vcc, 0, v163\n\t"
        "v_mfma_f32_16x16x32_bf16 v[0:3], v[60:63], v[164:167], 0\n\t"
        "v_mfma_f32_16x16x32_bf16 v[56:59], a[152:155], v[92:95], v[56:59]\n\t"
        "v_mfma_f32_16x16x32_bf16 v[4:7], v[64:67], v[164:167], 0\n\t"
        "v_mfma_f32_16x16x32_bf16 v[48:51], a[180:183], v[96:99], v[48:51]\n\t"
        "ds_read_b128 v[92:95], v183 offset:0\n\t"
        "v_mfma_f32_16x16x32_bf16 v[8:11], v[68:71], v[164:167], 0\n\t"
        "v_mfma_f32_16x16x32_bf16 v[52:55], a[184:187], v[96:99], v[52:55]\n\t"
        "v_mfma_f32_16x16x32_bf16 v[12:15], v[72:75], v[164:167], 0\n\t"
        "v_mfma_f32_16x16x32_bf16 v[56:59], a[188:191], v[96:99], v[56:59]\n\t"
        "v_mfma_f32_16x16x32_bf16 v[16:19], v[76:79], v[164:167], 0\n\t"
        "v_mfma_f32_16x16x32_bf16 v[48:51], a[216:219], v[100:103], v[48:51]\n\t"
        "ds_read_b128 v[96:99], v183 offset:64\n\t"
        "s_waitcnt lgkmcnt(2)\n\t"
        "v_mfma_f32_16x16x32_bf16 v[20:23], v[80:83], v[164:167], 0\n\t"
        "s_cbranch_vccz .Lx52%=\n\t"
        "v_lshlrev_b32_sdwa v157, 5, v161 dst_sel:DWORD dst_unused:UNUSED_PAD src0_sel:DWORD src1_sel:WORD_0\n\t"
        "v_lshlrev_b32_sdwa v158, 5, v161 dst_sel:DWORD dst_unused:UNUSED_PAD src0_sel:DWORD src1_sel:WORD_1\n\t"
        "v_or_b32 v157, v184, v157\n\t"
        "v_or_b32 v158, v184, v158\n\t"
        "v_xor_b32 v159, 32, v157\n\t"
        "v_xor_b32 v152, 32, v158\n\t"
        "ds_read_b64_tr_b16 v[60:61], v157 offset:0\n\t"
        "ds_read_b64_tr_b16 v[62:63], v158 offset:0\n\t"
        "ds_read_b64_tr_b16 v[64:65], v159 offset:0\n\t"
        "ds_read_b64_tr_b16 v[66:67], v152 offset:0\n\t"
        "ds_read_b64_tr_b16 v[68:69], v157 offset:64\n\t"
        "ds_read_b64_tr_b16 v[70:71], v158 offset:64\n\t"
        "ds_read_b64_tr_b16 v[72:73], v159 offset:64\n\t"
        "ds_read_b64_tr_b16 v[74:75], v152 offset:64\n\t"
        "ds_read_b64_tr_b16 v[76:77], v157 offset:128\n\t"
        "ds_read_b64_tr_b16 v[78:79], v158 offset:128\n\t"
        "ds_read_b64_tr_b16 v[80:81], v159 offset:128\n\t"
        "ds_read_b64_tr_b16 v[82:83], v152 offset:128\n\t"
        "v_perm_b32 v164, v163, v163, v168\n\t"
        "v_perm_b32 v165, v163, v163, v169\n\t"
        "v_perm_b32 v166, v163, v163, v170\n\t"
        "v_perm_b32 v167, v163, v163, v171\n\t"
        "s_waitcnt lgkmcnt(10)\n\t"
        "s_nop 0\n\t"
        "v_mfma_f32_16x16x32_bf16 v[0:3], v[60:63], v[164:167], v[0:3]\n\t"
        "s_waitcnt lgkmcnt(8)\n\t"
        "v_mfma_f32_16x16x32_bf16 v[4:7], v[64:67], v[164:167], v[4:7]\n\t"
        "s_waitcnt lgkmcnt(6)\n\t"
        "v_mfma_f32_16x16x32_bf16 v[8:11], v[68:71], v[164:167], v[8:11]\n\t"
        "s_waitcnt lgkmcnt(4)\n\t"
        "v_mfma_f32_16x16x32_bf16 v[12:15], v[72:75], v[164:167], v[12:15]\n\t"
        "s_waitcnt lgkmcnt(2)\n\t"
        "v_mfma_f32_16x16x32_bf16 v[16:19], v[76:79], v[164:167], v[16:19]\n\t"
        "s_waitcnt lgkmcnt(0)\n\t"
        "v_mfma_f32_16x16x32_bf16 v[20:23], v[80:83], v[164:167], v[20:23]\n\t"
        "s_nop 4\n\t"
        ".Lx52%=:\n\t"
        "v_cvt_pk_bf16_f32 v0, v0, v1\n\t"
        "v_cvt_pk_bf16_f32 v1, v2, v3\n\t"
        "v_cvt_pk_bf16_f32 v4, v4, v5\n\t"
        "v_cvt_pk_bf16_f32 v5, v6, v7\n\t"
        "v_mfma_f32_16x16x32_bf16 v[52:55], a[220:223], v[100:103], v[52:55]\n\t"
        "ds_write_b64 v186, v[0:1] offset:24576\n\t"
        "v_mfma_f32_16x16x32_bf16 v[56:59], a[224:227], v[100:103], v[56:59]\n\t"
        "ds_write_b64 v154, v[4:5] offset:24576\n\t"
        "v_mfma_f32_16x16x32_bf16 v[48:51], a[252:255], v[104:107], v[48:51]\n\t"
        "ds_read_b128 v[100:103], v183 offset:128\n\t"
        "v_cvt_pk_bf16_f32 v8, v8, v9\n\t"
        "v_mfma_f32_16x16x32_bf16 v[52:55], v[188:191], v[104:107], v[52:55]\n\t"
        "v_cvt_pk_bf16_f32 v9, v10, v11\n\t"
        "v_cvt_pk_bf16_f32 v12, v12, v13\n\t"
        "v_mfma_f32_16x16x32_bf16 v[56:59], v[192:195], v[104:107], v[56:59]\n\t"
        "ds_write_b64 v186, v[8:9] offset:24640\n\t"
        "v_mfma_f32_16x16x32_bf16 v[48:51], v[220:223], v[108:111], v[48:51]\n\t"
        "v_cvt_pk_bf16_f32 v13, v14, v15\n\t"
        "v_cvt_pk_bf16_f32 v16, v16, v17\n\t"
        "v_cvt_pk_bf16_f32 v17, v18, v19\n\t"
        "v_mfma_f32_16x16x32_bf16 v[52:55], v[224:227], v[108:111], v[52:55]\n\t"
        "ds_write_b64 v154, v[12:13] offset:24640\n\t"
        "v_mfma_f32_16x16x32_bf16 v[56:59], v[228:231], v[108:111], v[56:59]\n\t"
        "ds_write_b64 v186, v[16:17] offset:24704\n\t"
        "v_mfma_f32_16x16x32_bf16 v[48:51], a[24:27], v[112:115], v[48:51]\n\t"
        "v_cvt_pk_bf16_f32 v20, v20, v21\n\t"
        "v_cvt_pk_bf16_f32 v21, v22, v23\n\t"
        "v_mfma_f32_16x16x32_bf16 v[52:55], a[28:31], v[112:115], v[52:55]\n\t"
        "ds_write_b64 v154, v[20:21] offset:24704\n\t"
        "v_mfma_f32_16x16x32_bf16 v[56:59], a[32:35], v[112:115], v[56:59]\n\t"
        "s_cmp_gt_u32 s61, 48\n\t"
        "s_cbranch_scc1 .Lx54%=\n\t"
        ".Lx55%=:\n\t"
        "v_mfma_f32_16x16x32_bf16 v[48:51], a[60:63], v[84:87], v[48:51]\n\t"
        "v_mfma_f32_16x16x32_bf16 v[52:55], a[64:67], v[84:87], v[52:55]\n\t"
        "v_mfma_f32_16x16x32_bf16 v[56:59], a[68:71], v[84:87], v[56:59]\n\t"
        "v_mfma_f32_16x16x32_bf16 v[48:51], a[96:99], v[88:91], v[48:51]\n\t"
        "v_mfma_f32_16x16x32_bf16 v[52:55], a[100:103], v[88:91], v[52:55]\n\t"
        "s_waitcnt lgkmcnt(0)\n\t"
        "s_barrier\n\t"
        "v_xor_b32 v140, v181, v140\n\t"
        "v_mfma_f32_16x16x32_bf16 v[56:59], a[104:107], v[88:91], v[56:59]\n\t"
        "ds_read_b128 v[132:135], v187 offset:4096\n\t"
        "v_mfma_f32_16x16x32_bf16 v[48:51], a[132:135], v[92:95], v[48:51]\n\t"
        "ds_read_b128 v[136:139], v187 offset:4112\n\t"
        "v_add_u32 v151, s9, v180\n\t"
        "v_max_u32 v151, v151, v140\n\t"
        "s_waitcnt lgkmcnt(1)\n\t"
        "v_xor_b32 v132, v181, v132\n\t"
        "v_mfma_f32_16x16x32_bf16 v[52:55], a[136:139], v[92:95], v[52:55]\n\t"
        "ds_read_b128 v[104:107], v151 offset:0\n\t"
        "v_mfma_f32_16x16x32_bf16 v[56:59], a[140:143], v[92:95], v[56:59]\n\t"
        "ds_read_b128 v[108:111], v151 offset:64\n\t"
        "v_mfma_f32_16x16x32_bf16 v[48:51], a[168:171], v[96:99], v[48:51]\n\t"
        "ds_read_b128 v[112:115], v151 offset:128\n\t"
        "v_mfma_f32_16x16x32_bf16 v[52:55], a[172:175], v[96:99], v[52:55]\n\t"
        "ds_read_b32 v140, v187 offset:4128\n\t"
        "v_readlane_b32 s9, v155, 48\n\t"
        "v_mfma_f32_16x16x32_bf16 v[56:59], a[176:179], v[96:99], v[56:59]\n\t"
        "ds_read_b128 v[124:127], v182 offset:16\n\t"
        "v_add_u32 v183, s9, v172\n\t"
        "v_max_u32 v183, v183, v132\n\t"
        "v_xor_b32 v133, v181, v133\n\t"
        "v_mfma_f32_16x16x32_bf16 v[48:51], a[204:207], v[100:103], v[48:51]\n\t"
        "ds_read_b128 v[84:87], v183 offset:0\n\t"
        "v_add_u32 v151, s9, v173\n\t"
        "v_mfma_f32_16x16x32_bf16 v[52:55], a[208:211], v[100:103], v[52:55]\n\t"
        "ds_read_b128 v[88:91], v183 offset:64\n\t"
        "v_max_u32 v151, v151, v133\n\t"
        "v_mfma_f32_16x16x32_bf16 v[56:59], a[212:215], v[100:103], v[56:59]\n\t"
        "ds_read_b128 v[92:95], v183 offset:128\n\t"
        "v_xor_b32 v134, v181, v134\n\t"
        "s_waitcnt lgkmcnt(7)\n\t"
        "v_mfma_f32_16x16x32_bf16 v[48:51], a[240:243], v[104:107], v[48:51]\n\t"
        "ds_read_b128 v[96:99], v151 offset:0\n\t"
        "v_add_u32 v183, s9, v174\n\t"
        ".LpM2%=:\n\t"
        "s_cbranch_execz .LrM%=\n\t"
        "v_mfma_f32_16x16x32_bf16 v[52:55], a[244:247], v[104:107], v[52:55]\n\t"
        "ds_read_b128 v[100:103], v151 offset:64\n\t"
        "v_max_u32 v183, v183, v134\n\t"
        "v_mfma_f32_16x16x32_bf16 v[56:59], a[248:251], v[104:107], v[56:59]\n\t"
        "v_xor_b32 v135, v181, v135\n\t"
        "s_waitcnt lgkmcnt(8)\n\t"
        "v_mfma_f32_16x16x32_bf16 v[48:51], v[208:211], v[108:111], v[48:51]\n\t"
        "ds_read_b128 v[104:107], v151 offset:128\n\t"
        "v_add_u32 v151, s9, v175\n\t"
        "v_mfma_f32_16x16x32_bf16 v[52:55], v[212:215], v[108:111], v[52:55]\n\t"
        "v_max_u32 v151, v151, v135\n\t"
        "v_xor_b32 v136, v181, v136\n\t"
        "v_mfma_f32_16x16x32_bf16 v[56:59], v[216:219], v[108:111], v[56:59]\n\t"
        "s_waitcnt lgkmcnt(6)\n\t"
        "v_or3_b32 v124, v124, v125, v126\n\t"
        "v_or_b32 v124, v124, v127\n\t"
        "v_mfma_f32_16x16x32_bf16 v[48:51], v[244:247], v[112:115], v[48:51]\n\t"
        "ds_read_b128 v[108:111], v183 offset:0\n\t"
        "v_readfirstlane_b32 s8, v124\n\t"
        "v_mfma_f32_16x16x32_bf16 v[52:55], v[248:251], v[112:115], v[52:55]\n\t"
        "ds_read_b128 v[124:127], v147 offset:0\n\t"
        "s_or_b32 s6, s6, s8\n\t"
        "v_mfma_f32_16x16x32_bf16 v[56:59], v[252:255], v[112:115], v[56:59]\n\t"
        "s_waitcnt lgkmcnt(6)\n\t"
        "v_mfma_f32_16x16x32_bf16 v[72:75], a[12:15], v[84:87], 0\n\t"
        "ds_read_b128 v[112:115], v183 offset:64\n\t"
        "v_mfma_f32_16x16x32_bf16 v[76:79], a[16:19], v[84:87], 0\n\t"
        "v_mfma_f32_16x16x32_bf16 v[80:83], a[20:23], v[84:87], 0\n\t"
        "v_mfma_f32_16x16x32_bf16 v[72:75], a[48:51], v[88:91], v[72:75]\n\t"
        "ds_read_b128 v[84:87], v183 offset:128\n\t"
        "v_add_u32 v183, s9, v176\n\t"
        "v_mfma_f32_16x16x32_bf16 v[76:79], a[52:55], v[88:91], v[76:79]\n\t"
        "v_max_u32 v183, v183, v136\n\t"
        "v_xor_b32 v137, v181, v137\n\t"
        "v_mfma_f32_16x16x32_bf16 v[80:83], a[56:59], v[88:91], v[80:83]\n\t"
        "s_waitcnt lgkmcnt(4)\n\t"
        "v_mfma_f32_16x16x32_bf16 v[72:75], a[84:87], v[92:95], v[72:75]\n\t"
        "ds_read_b128 v[88:91], v151 offset:0\n\t"
        "v_mfma_f32_16x16x32_bf16 v[76:79], a[88:91], v[92:95], v[76:79]\n\t"
        "s_waitcnt lgkmcnt(3)\n\t"
        "buffer_store_dwordx4 v[124:127], v145, s[28:31], s50 offen sc1\n\t"
        "v_mfma_f32_16x16x32_bf16 v[80:83], a[92:95], v[92:95], v[80:83]\n\t"
        "ds_read_b128 v[124:127], v147 offset:4096\n\t"
        "s_add_u32 s50, s50, 86016\n\t"
        "v_mfma_f32_16x16x32_bf16 v[72:75], a[120:123], v[96:99], v[72:75]\n\t"
        "ds_read_b128 v[92:95], v151 offset:64\n\t"
        "v_mfma_f32_16x16x32_bf16 v[76:79], a[124:127], v[96:99], v[76:79]\n\t"
        "v_mfma_f32_16x16x32_bf16 v[80:83], a[128:131], v[96:99], v[80:83]\n\t"
        "v_mfma_f32_16x16x32_bf16 v[72:75], a[156:159], v[100:103], v[72:75]\n\t"
        "ds_read_b128 v[96:99], v151 offset:128\n\t"
        "v_add_u32 v151, s9, v177\n\t"
        "v_mfma_f32_16x16x32_bf16 v[76:79], a[160:163], v[100:103], v[76:79]\n\t"
        "v_max_u32 v151, v151, v137\n\t"
        "v_xor_b32 v138, v181, v138\n\t"
        "v_mfma_f32_16x16x32_bf16 v[80:83], a[164:167], v[100:103], v[80:83]\n\t"
        "v_mfma_f32_16x16x32_bf16 v[72:75], a[192:195], v[104:107], v[72:75]\n\t"
        "ds_read_b128 v[100:103], v183 offset:0\n\t"
        "v_mfma_f32_16x16x32_bf16 v[76:79], a[196:199], v[104:107], v[76:79]\n\t"
        "v_mfma_f32_16x16x32_bf16 v[80:83], a[200:203], v[104:107], v[80:83]\n\t"
        "v_mfma_f32_16x16x32_bf16 v[72:75], a[228:231], v[108:111], v[72:75]\n\t"
        "ds_read_b128 v[104:107], v183 offset:64\n\t"
        "v_mfma_f32_16x16x32_bf16 v[76:79], a[232:235], v[108:111], v[76:79]\n\t"
        "v_mfma_f32_16x16x32_bf16 v[80:83], a[236:239], v[108:111], v[80:83]\n\t"
        "s_waitcnt lgkmcnt(3)\n\t"
        "v_mfma_f32_16x16x32_bf16 v[72:75], v[196:199], v[112:115], v[72:75]\n\t"
        "buffer_store_dwordx4 v[124:127], v145, s[28:31], s50 offen sc1\n\t"
        "v_mfma_f32_16x16x32_bf16 v[76:79], v[200:203], v[112:115], v[76:79]\n\t"
        "ds_read_b128 v[108:111], v183 offset:128\n\t"
        "v_add_u32 v183, s9, v178\n\t"
        "v_mfma_f32_16x16x32_bf16 v[80:83], v[204:207], v[112:115], v[80:83]\n\t"
        "v_max_u32 v183, v183, v138\n\t"
        "v_xor_b32 v139, v181, v139\n\t"
        "v_mfma_f32_16x16x32_bf16 v[72:75], v[232:235], v[84:87], v[72:75]\n\t"
        "ds_read_b128 v[112:115], v151 offset:0\n\t"
        "v_readlane_b32 s55, v155, 49\n\t"
        "v_mfma_f32_16x16x32_bf16 v[76:79], v[236:239], v[84:87], v[76:79]\n\t"
        "v_cvt_pk_bf16_f32 v116, v48, v49\n\t"
        "v_cvt_pk_bf16_f32 v117, v50, v51\n\t"
        "v_mfma_f32_16x16x32_bf16 v[80:83], v[240:243], v[84:87], v[80:83]\n\t"
        "v_cvt_pk_bf16_f32 v118, v52, v53\n\t"
        "v_cvt_pk_bf16_f32 v119, v54, v55\n\t"
        "v_mfma_f32_16x16x32_bf16 v[72:75], a[0:3], v[88:91], v[72:75]\n\t"
        "ds_read_b128 v[84:87], v151 offset:64\n\t"
        "v_cvt_pk_bf16_f32 v120, v56, v57\n\t"
        "v_mfma_f32_16x16x32_bf16 v[76:79], a[4:7], v[88:91], v[76:79]\n\t"
        "v_cvt_pk_bf16_f32 v121, v58, v59\n\t"
        "v_mfma_f32_16x16x32_bf16 v[80:83], a[8:11], v[88:91], v[80:83]\n\t"
        "v_permlane16_swap_b32 v116, v118\n\t"
        "v_mfma_f32_16x16x32_bf16 v[72:75], a[36:39], v[92:95], v[72:75]\n\t"
        "v_permlane16_swap_b32 v117, v119\n\t"
        "v_mfma_f32_16x16x32_bf16 v[76:79], a[40:43], v[92:95], v[76:79]\n\t"
        "ds_read_b128 v[88:91], v151 offset:128\n\t"
        "v_add_u32 v151, s9, v179\n\t"
        "v_mfma_f32_16x16x32_bf16 v[80:83], a[44:47], v[92:95], v[80:83]\n\t"
        "v_max_u32 v151, v151, v139\n\t"
        "v_xor_b32 v140, v181, v140\n\t"
        "s_waitcnt lgkmcnt(3)\n\t"
        "v_mfma_f32_16x16x32_bf16 v[72:75], a[72:75], v[96:99], v[72:75]\n\t"
        "ds_read_b128 v[92:95], v183 offset:0\n\t"
        "v_mfma_f32_16x16x32_bf16 v[76:79], a[76:79], v[96:99], v[76:79]\n\t"
        "v_mfma_f32_16x16x32_bf16 v[80:83], a[80:83], v[96:99], v[80:83]\n\t"
        "buffer_store_dwordx4 v[116:119], v148, s[28:31], s55 offen sc1\n\t"
        "v_mfma_f32_16x16x32_bf16 v[72:75], a[108:111], v[100:103], v[72:75]\n\t"
        "ds_write_b64 v146, v[120:121] offset:8192\n\t"
        "v_mfma_f32_16x16x32_bf16 v[76:79], a[112:115], v[100:103], v[76:79]\n\t"
        "ds_read_b128 v[96:99], v183 offset:64\n\t"
        "v_mfma_f32_16x16x32_bf16 v[80:83], a[116:119], v[100:103], v[80:83]\n\t"
        "ds_read_b128 v[100:103], v183 offset:128\n\t"
        "v_add_u32 v183, s9, v180\n\t"
        "v_mfma_f32_16x16x32_bf16 v[72:75], a[144:147], v[104:107], v[72:75]\n\t"
        "v_max_u32 v183, v183, v140\n\t"
        "v_mfma_f32_16x16x32_bf16 v[76:79], a[148:151], v[104:107], v[76:79]\n\t"
        "v_mfma_f32_16x16x32_bf16 v[80:83], a[152:155], v[104:107], v[80:83]\n\t"
        "ds_read_b128 v[104:107], v151 offset:0\n\t"
        "v_mfma_f32_16x16x32_bf16 v[72:75], a[180:183], v[108:111], v[72:75]\n\t"
        "v_mfma_f32_16x16x32_bf16 v[76:79], a[184:187], v[108:111], v[76:79]\n\t"
        "v_mfma_f32_16x16x32_bf16 v[80:83], a[188:191], v[108:111], v[80:83]\n\t"
        "ds_read_b128 v[108:111], v151 offset:64\n\t"
        "s_waitcnt lgkmcnt(5)\n\t"
        "v_mfma_f32_16x16x32_bf16 v[72:75], a[216:219], v[112:115], v[72:75]\n\t"
        "v_mfma_f32_16x16x32_bf16 v[76:79], a[220:223], v[112:115], v[76:79]\n\t"
        "v_mfma_f32_16x16x32_bf16 v[80:83], a[224:227], v[112:115], v[80:83]\n\t"
        "ds_read_b128 v[112:115], v151 offset:128\n\t"
        "v_mfma_f32_16x16x32_bf16 v[72:75], a[252:255], v[84:87], v[72:75]\n\t"
        "v_mfma_f32_16x16x32_bf16 v[76:79], v[188:191], v[84:87], v[76:79]\n\t"
        "v_mfma_f32_16x16x32_bf16 v[80:83], v[192:195], v[84:87], v[80:83]\n\t"
        "ds_read_b128 v[84:87], v183 offset:0\n\t"
        "v_mfma_f32_16x16x32_bf16 v[72:75], v[220:223], v[88:91], v[72:75]\n\t"
        "v_mfma_f32_16x16x32_bf16 v[76:79], v[224:227], v[88:91], v[76:79]\n\t"
        "v_mfma_f32_16x16x32_bf16 v[80:83], v[228:231], v[88:91], v[80:83]\n\t"
        "ds_read_b128 v[88:91], v183 offset:64\n\t"
        "v_mfma_f32_16x16x32_bf16 v[72:75], a[24:27], v[92:95], v[72:75]\n\t"
        "v_mfma_f32_16x16x32_bf16 v[76:79], a[28:31], v[92:95], v[76:79]\n\t"
        "v_mfma_f32_16x16x32_bf16 v[80:83], a[32:35], v[92:95], v[80:83]\n\t"
        "ds_read_b128 v[92:95], v183 offset:128\n\t"
        "s_waitcnt lgkmcnt(4)\n\t"
        "v_mfma_f32_16x16x32_bf16 v[80:83], a[68:71], v[96:99], v[80:83]\n\t"
        "v_mfma_f32_16x16x32_bf16 v[80:83], a[104:107], v[100:103], v[80:83]\n\t"
        "v_mfma_f32_16x16x32_bf16 v[80:83], a[140:143], v[104:107], v[80:83]\n\t"
        "v_mfma_f32_16x16x32_bf16 v[80:83], a[176:179], v[108:111], v[80:83]\n\t"
        "s_waitcnt lgkmcnt(2)\n\t"
        "v_mfma_f32_16x16x32_bf16 v[80:83], a[212:215], v[112:115], v[80:83]\n\t"
        "v_mfma_f32_16x16x32_bf16 v[80:83], a[248:251], v[84:87], v[80:83]\n\t"
        "s_waitcnt lgkmcnt(1)\n\t"
        "v_mfma_f32_16x16x32_bf16 v[80:83], v[216:219], v[88:91], v[80:83]\n\t"
        "s_waitcnt lgkmcnt(0)\n\t"
        "v_mfma_f32_16x16x32_bf16 v[80:83], v[252:255], v[92:95], v[80:83]\n\t"
        "v_mfma_f32_16x16x32_bf16 v[72:75], a[60:63], v[96:99], v[72:75]\n\t"
        "v_mfma_f32_16x16x32_bf16 v[76:79], a[64:67], v[96:99], v[76:79]\n\t"
        "s_nop 5\n\t"
        "v_cvt_pk_bf16_f32 v120, v80, v81\n\t"
        "v_cvt_pk_bf16_f32 v121, v82, v83\n\t"
        "v_mfma_f32_16x16x32_bf16 v[72:75], a[96:99], v[100:103], v[72:75]\n\t"
        "ds_write_b64 v146, v[120:121] offset:10240\n\t"
        "v_mfma_f32_16x16x32_bf16 v[76:79], a[100:103], v[100:103], v[76:79]\n\t"
        "v_mfma_f32_16x16x32_bf16 v[72:75], a[132:135], v[104:107], v[72:75]\n\t"
        "v_mfma_f32_16x16x32_bf16 v[76:79], a[136:139], v[104:107], v[76:79]\n\t"
        "s_waitcnt lgkmcnt(0)\n\t"
        "s_barrier\n\t"
        "v_mfma_f32_16x16x32_bf16 v[72:75], a[168:171], v[108:111], v[72:75]\n\t"
        "ds_read_b128 v[124:127], v147 offset:8192\n\t"
        "v_readlane_b32 s55, v155, 51\n\t"
        "v_mfma_f32_16x16x32_bf16 v[76:79], a[172:175], v[108:111], v[76:79]\n\t"
        "v_mfma_f32_16x16x32_bf16 v[72:75], a[204:207], v[112:115], v[72:75]\n\t"
        "v_mfma_f32_16x16x32_bf16 v[76:79], a[208:211], v[112:115], v[76:79]\n\t"
        "v_mfma_f32_16x16x32_bf16 v[72:75], a[240:243], v[84:87], v[72:75]\n\t"
        "v_mfma_f32_16x16x32_bf16 v[76:79], a[244:247], v[84:87], v[76:79]\n\t"
        "v_mfma_f32_16x16x32_bf16 v[72:75], v[208:211], v[88:91], v[72:75]\n\t"
        "v_mfma_f32_16x16x32_bf16 v[76:79], v[212:215], v[88:91], v[76:79]\n\t"
        "v_mfma_f32_16x16x32_bf16 v[72:75], v[244:247], v[92:95], v[72:75]\n\t"
        "v_mfma_f32_16x16x32_bf16 v[76:79], v[248:251], v[92:95], v[76:79]\n\t"
        "s_nop 6\n\t"
        "v_cvt_pk_bf16_f32 v116, v72, v73\n\t"
        "v_cvt_pk_bf16_f32 v117, v74, v75\n\t"
        "v_cvt_pk_bf16_f32 v118, v76, v77\n\t"
        "v_cvt_pk_bf16_f32 v119, v78, v79\n\t"
        "s_nop 0\n\t"
        "v_permlane16_swap_b32 v116, v118\n\t"
        "v_permlane16_swap_b32 v117, v119\n\t"
        "buffer_store_dwordx4 v[116:119], v148, s[28:31], s55 offen  \n\t"
        "s_waitcnt lgkmcnt(0)\n\t"
        "buffer_store_dwordx4 v[124:127], v150, s[28:31], s52 offen  \n\t"
        "s_andn2_b32 s6, s6, 2\n\t"
        "s_cbranch_scc1 99f\n\t"
        "s_endpgm\n\t"
        "20:\n\t"
        "ds_bpermute_b32 v64, v65, v155 offset:172\n\t"
        "s_add_u32 m0, s35, 4096\n\t"
        "s_add_u32 s43, s34, 43008\n\t"
        "buffer_load_dwordx4 v143, s[16:19], s43 offen  lds\n\t"
        "ds_bpermute_b32 v78, v81, v155 offset:148\n\t"
        "ds_bpermute_b32 v79, v81, v155 offset:160\n\t"
        "v_readlane_b32 s71, v155, 26\n\t"
        "v_readlane_b32 s10, v155, 29\n\t"
        "s_nop 0\n\t"
        "v_mul_u32_u24 v73, s71, v66\n\t"
        "v_lshrrev_b32 v73, 11, v73\n\t"
        "v_lshlrev_b32 v66, 4, v66\n\t"
        "v_mad_u32_u24 v73, v73, s10, v66\n\t"
        "s_waitcnt lgkmcnt(2)\n\t"
        "v_add_u32 v64, v64, v73\n\t"
        "s_add_u32 m0, s35, 20480\n\t"
        "s_add_u32 s43, s34, 215040\n\t"
        "buffer_load_dwordx4 v143, s[16:19], s43 offen  lds\n\t"
        "v_readlane_b32 s69, v155, 34\n\t"
        "v_readlane_b32 s70, v155, 35\n\t"
        "s_nop 0\n\t"
        "v_mad_u32_u24 v70, v52, s69, v70\n\t"
        "v_mad_u32_u24 v70, v158, s70, v70\n\t"
        "s_mul_i32 s71, s69, s68\n\t"
        "s_add_u32 s71, s71, 157728\n\t"
        "v_mad_u32_u24 v67, v82, s70, v67\n\t"
        "v_add_u32 v67, s71, v67\n\t"
        "v_readlane_b32 s11, v155, 30\n\t"
        "s_add_u32 m0, s37, -24576\n\t"
        "s_add_u32 s36, s36, 6144\n\t"
        "buffer_load_dwordx4 v140, s[24:27], s36 offen offset:0 lds\n\t"
        "s_movk_i32 s10, 223\n\t"
        "v_mov_b32 v75, 0x7ff00000\n\t"
        "v_readlane_b32 s12, v155, 24\n\t"
        "v_readlane_b32 s13, v155, 25\n\t"
        "s_nop 0\n\t"
        "s_bfe_u32 s70, s12, 524288\n\t"
        "s_bfe_u32 s71, s13, 524288\n\t"
        "v_cmp_gt_u32 vcc, s71, v71\n\t"
        "v_add3_u32 v73, v71, s70, -2\n\t"
        "v_med3_i32 v73, v73, 0, s10\n\t"
        "buffer_load_dwordx4 v141, s[24:27], s36 offen offset:1024 lds\n\t"
        "v_cndmask_b32 v74, v75, v72, vcc\n\t"
        "v_mad_u32_u24 v134, v73, s11, v74\n\t"
        "s_bfe_u32 s70, s12, 524296\n\t"
        "s_bfe_u32 s71, s13, 524296\n\t"
        "v_cmp_gt_u32 vcc, s71, v71\n\t"
        "v_add3_u32 v73, v71, s70, -2\n\t"
        "v_med3_i32 v73, v73, 0, s10\n\t"
        "v_cndmask_b32 v74, v75, v72, vcc\n\t"
        "v_mad_u32_u24 v135, v73, s11, v74\n\t"
        "buffer_load_dwordx4 v142, s[24:27], s36 offen offset:2048 lds\n\t"
        "s_bfe_u32 s70, s12, 524304\n\t"
        "s_bfe_u32 s71, s13, 524304\n\t"
        "v_cmp_gt_u32 vcc, s71, v71\n\t"
        "v_add3_u32 v73, v71, s70, -2\n\t"
        "v_med3_i32 v73, v73, 0, s10\n\t"
        "v_cndmask_b32 v74, v75, v72, vcc\n\t"
        "v_mad_u32_u24 v136, v73, s11, v74\n\t"
        "s_bfe_u32 s70, s12, 524312\n\t"
        "s_bfe_u32 s71, s13, 524312\n\t"
        "v_cmp_gt_u32 vcc, s71, v71\n\t"
        "v_add3_u32 v73, v71, s70, -2\n\t"
        "v_med3_i32 v73, v73, 0, s10\n\t"
        "v_cndmask_b32 v74, v75, v72, vcc\n\t"
        "v_mad_u32_u24 v137, v73, s11, v74\n\t"
        "v_readlane_b32 s44, v155, 32\n\t"
        "v_readlane_b32 s45, v155, 33\n\t"
        "v_readlane_b32 s62, v155, 36\n\t"
        "s_mov_b32 exec_lo, 0\n\t"
        "s_mov_b32 exec_hi, 0xfff00000\n\t"
        "ds_write_b32 v119, v155\n\t"
        "s_mov_b64 exec, -1\n\t"
        "s_lshl_b32 s70, s62, 5\n\t"
        "s_sub_u32 s71, s70, 16384\n\t"
        "s_mul_i32 s71, s71, s68\n\t"
        "v_add_u32 v172, s71, v128\n\t"
        "v_add_u32 v173, s71, v129\n\t"
        "v_add_u32 v174, s71, v130\n\t"
        "s_add_u32 s71, s71, s70\n\t"
        "v_add_u32 v175, s71, v128\n\t"
        "v_add_u32 v176, s71, v129\n\t"
        "v_add_u32 v177, s71, v130\n\t"
        "s_add_u32 s71, s71, s70\n\t"
        "v_add_u32 v178, s71, v128\n\t"
        "v_add_u32 v179, s71, v129\n\t"
        "v_add_u32 v180, s71, v130\n\t"
        "s_cmp_eq_u32 s69, 36\n\t"
        "s_cselect_b64 exec, -1, 0\n\t"
        "v_swap_b32 v68, v69\n\t"
        "v_swap_b32 v173, v175\n\t"
        "v_swap_b32 v174, v178\n\t"
        "v_swap_b32 v177, v179\n\t"
        "s_mov_b64 exec, -1\n\t"
        "v_add3_u32 v68, v68, v52, -1\n\t"
        "v_add3_u32 v69, v69, v158, -1\n\t"
        "v_cvt_f32_i32 v68, v68\n\t"
        "v_cvt_f32_i32 v69, v69\n\t"
        "s_waitcnt lgkmcnt(1)\n\t"
        "v_add_f32 v78, v78, v56\n\t"
        "v_add_f32 v79, v79, v57\n\t"
        "v_readlane_b32 s69, v155, 31\n\t"
        "s_nop 1\n\t"
        "v_mov_b32 v154, s69\n\t"
        "v_mov_b32 v119, s62\n\t"
        "v_readlane_b32 s53, v155, 28\n\t"
        "v_readlane_b32 s9, v155, 46\n\t"
        "s_sub_u32 s10, 43200, s11\n\t"
        "s_mul_i32 s10, s10, s68\n\t"
        "s_sub_u32 s11, s11, 192\n\t"
        "v_mad_u32_u24 v148, v158, s11, v144\n\t"
        "v_add_u32 v148, s10, v148\n\t"
        "v_mad_u32_u24 v150, v83, s11, v145\n\t"
        "s_lshl_b32 s70, s49, 0\n\t"
        "s_add_u32 s70, s70, 50\n\t"
        "v_readlane_b32 s52, v155, s70\n\t"
        "s_nop 1\n\t"
        "s_add_u32 s52, s52, s10\n\t"
        "v_readlane_b32 s51, v155, 49\n\t"
        "s_lshl_b32 s70, s49, 30\n\t"
        "s_add_u32 s70, s70, s10\n\t"
        "s_add_u32 s51, s51, s70\n\t"
        "s_mov_b64 exec, 0\n\t"
        "s_cmp_eq_u32 s4, 0\n\t"
        "s_cbranch_scc1 .LpS0%=\n\t"
        "s_cmp_eq_u32 s4, 1\n\t"
        "s_cbranch_scc1 .LpS1%=\n\t"
        "s_cmp_eq_u32 s4, 2\n\t"
        "s_cbranch_scc1 .LpS2%=\n\t"
        "s_nop 0\n\t"
        ".LrS%=:\n\t"
        "s_mov_b64 exec, -1\n\t"
        "s_waitcnt vmcnt(9) lgkmcnt(0)\n\t"
        "s_barrier\n\t"
        "ds_read_b64_tr_b16 a[0:1], v138 offset:6144\n\t"
        "ds_read_b64_tr_b16 a[2:3], v138 offset:6912\n\t"
        "ds_read_b64_tr_b16 a[4:5], v139 offset:6144\n\t"
        "ds_read_b64_tr_b16 a[6:7], v139 offset:6912\n\t"
        "ds_read_b64_tr_b16 a[8:9], v149 offset:6144\n\t"
        "ds_read_b64_tr_b16 a[10:11], v149 offset:6912\n\t"
        "ds_read_b128 v[84:87], v128 offset:8192\n\t"
        "ds_read_b64_tr_b16 a[12:13], v138 offset:12288\n\t"
        "ds_read_b64_tr_b16 a[14:15], v138 offset:13056\n\t"
        "ds_read_b64_tr_b16 a[16:17], v139 offset:12288\n\t"
        "ds_read_b64_tr_b16 a[18:19], v139 offset:13056\n\t"
        "ds_read_b64_tr_b16 a[20:21], v149 offset:12288\n\t"
        "ds_read_b64_tr_b16 a[22:23], v149 offset:13056\n\t"
        "ds_read_b128 v[88:91], v128 offset:8192\n\t"
        "s_add_u32 m0, s37, -12288\n\t"
        "s_add_u32 s36, s36, s39\n\t"
        "s_waitcnt lgkmcnt(7)\n\t"
        "v_mfma_f32_16x16x32_bf16 v[12:15], a[0:3], v[84:87], 0\n\t"
        "buffer_load_dwordx4 v140, s[24:27], s36 offen offset:0 lds\n\t"
        "buffer_load_dwordx4 v141, s[24:27], s36 offen offset:1024 lds\n\t"
        "buffer_load_dwordx4 v142, s[24:27], s36 offen offset:2048 lds\n\t"
        "s_add_u32 m0, s37, 0\n\t"
        "s_add_u32 s36, s36, s38\n\t"
        "buffer_load_dwordx4 v140, s[24:27], s36 offen offset:0 lds\n\t"
        "buffer_load_dwordx4 v141, s[24:27], s36 offen offset:1024 lds\n\t"
        "buffer_load_dwordx4 v142, s[24:27], s36 offen offset:2048 lds\n\t"
        "s_add_u32 m0, s37, 12288\n\t"
        "s_add_u32 s36, s36, s5\n\t"
        "buffer_load_dwordx4 v140, s[24:27], s36 offen offset:0 lds\n\t"
        "s_waitcnt vmcnt(10)\n\t"
        "s_barrier\n\t"
        "ds_read_b128 v[92:95], v128 offset:4096\n\t"
        "ds_read_b64_tr_b16 a[24:25], v138 offset:0\n\t"
        "v_mfma_f32_16x16x32_bf16 v[16:19], a[4:7], v[84:87], 0\n\t"
        "buffer_load_dwordx4 v141, s[24:27], s36 offen offset:1024 lds\n\t"
        "ds_read_b64_tr_b16 a[26:27], v138 offset:768\n\t"
        "v_mfma_f32_16x16x32_bf16 v[20:23], a[8:11], v[84:87], 0\n\t"
        "ds_read_b64_tr_b16 a[28:29], v139 offset:0\n\t"
        "ds_read_b64_tr_b16 a[30:31], v139 offset:768\n\t"
        "s_waitcnt lgkmcnt(5)\n\t"
        "v_mfma_f32_16x16x32_bf16 v[24:27], a[12:15], v[88:91], 0\n\t"
        "buffer_load_dwordx4 v142, s[24:27], s36 offen offset:2048 lds\n\t"
        "s_add_u32 m0, s37, 24576\n\t"
        "ds_read_b64_tr_b16 a[32:33], v149 offset:0\n\t"
        "s_add_u32 s36, s36, s39\n\t"
        "ds_read_b64_tr_b16 a[34:35], v149 offset:768\n\t"
        "v_mfma_f32_16x16x32_bf16 v[28:31], a[16:19], v[88:91], 0\n\t"
        "ds_read_b128 v[96:99], v128 offset:8192\n\t"
        "v_mfma_f32_16x16x32_bf16 v[32:35], a[20:23], v[88:91], 0\n\t"
        "ds_read_b128 v[100:103], v128 offset:12288\n\t"
        "s_waitcnt lgkmcnt(8)\n\t"
        "v_mfma_f32_16x16x32_bf16 v[0:3], a[0:3], v[92:95], 0\n\t"
        "buffer_load_dwordx4 v140, s[24:27], s36 offen offset:0 lds\n\t"
        "v_mfma_f32_16x16x32_bf16 v[4:7], a[4:7], v[92:95], 0\n\t"
        "v_mfma_f32_16x16x32_bf16 v[8:11], a[8:11], v[92:95], 0\n\t"
        "v_mfma_f32_16x16x32_bf16 v[12:15], a[12:15], v[92:95], v[12:15]\n\t"
        "v_mfma_f32_16x16x32_bf16 v[16:19], a[16:19], v[92:95], v[16:19]\n\t"
        "buffer_load_dwordx4 v141, s[24:27], s36 offen offset:1024 lds\n\t"
        "v_mfma_f32_16x16x32_bf16 v[20:23], a[20:23], v[92:95], v[20:23]\n\t"
        "s_waitcnt lgkmcnt(1)\n\t"
        "v_mfma_f32_16x16x32_bf16 v[0:3], a[24:27], v[96:99], v[0:3]\n\t"
        "v_mfma_f32_16x16x32_bf16 v[4:7], a[28:31], v[96:99], v[4:7]\n\t"
        "v_mfma_f32_16x16x32_bf16 v[8:11], a[32:35], v[96:99], v[8:11]\n\t"
        "buffer_load_dwordx4 v142, s[24:27], s36 offen offset:2048 lds\n\t"
        "s_add_u32 m0, s35, 16384\n\t"
        "s_add_u32 s43, s34, 172032\n\t"
        "s_waitcnt lgkmcnt(0)\n\t"
        "v_mfma_f32_16x16x32_bf16 v[12:15], a[24:27], v[100:103], v[12:15]\n\t"
        "v_mfma_f32_16x16x32_bf16 v[16:19], a[28:31], v[100:103], v[16:19]\n\t"
        "s_waitcnt vmcnt(9)\n\t"
        "s_barrier\n\t"
        "ds_read_b64_tr_b16 a[36:37], v138 offset:18432\n\t"
        "ds_read_b64_tr_b16 a[38:39], v138 offset:19200\n\t"
        "ds_read_b64_tr_b16 a[40:41], v139 offset:18432\n\t"
        "v_mfma_f32_16x16x32_bf16 v[20:23], a[32:35], v[100:103], v[20:23]\n\t"
        "buffer_load_dwordx4 v143, s[16:19], s43 offen  lds\n\t"
        "s_add_u32 m0, s35, 32768\n\t"
        "ds_read_b64_tr_b16 a[42:43], v139 offset:19200\n\t"
        "s_add_u32 s43, s34, 344064\n\t"
        "ds_read_b64_tr_b16 a[44:45], v149 offset:18432\n\t"
        "s_min_u32 s43, s43, 9590784\n\t"
        "ds_read_b64_tr_b16 a[46:47], v149 offset:19200\n\t"
        "ds_read_b64_tr_b16 a[48:49], v138 offset:24576\n\t"
        "ds_read_b64_tr_b16 a[50:51], v138 offset:25344\n\t"
        "ds_read_b64_tr_b16 a[52:53], v139 offset:24576\n\t"
        "ds_read_b64_tr_b16 a[54:55], v139 offset:25344\n\t"
        "ds_read_b64_tr_b16 a[56:57], v149 offset:24576\n\t"
        "ds_read_b64_tr_b16 a[58:59], v149 offset:25344\n\t"
        "ds_read_b128 v[104:107], v128 offset:4160\n\t"
        "v_mfma_f32_16x16x32_bf16 v[24:27], a[0:3], v[100:103], v[24:27]\n\t"
        "buffer_load_dwordx4 v143, s[16:19], s43 offen  lds\n\t"
        "s_add_u32 m0, s35, 157696\n\t"
        "ds_read_b64_tr_b16 a[60:61], v138 offset:30720\n\t"
        "v_mfma_f32_16x16x32_bf16 v[28:31], a[4:7], v[100:103], v[28:31]\n\t"
        "buffer_load_dwordx4 v64, s[20:23], 0 offen lds\n\t"
        "s_add_u32 m0, s35, 0\n\t"
        "ds_read_b64_tr_b16 a[62:63], v138 offset:31488\n\t"
        "s_add_u32 s43, s34, 0\n\t"
        "v_mfma_f32_16x16x32_bf16 v[32:35], a[8:11], v[100:103], v[32:35]\n\t"
        "s_waitcnt vmcnt(9)\n\t"
        "s_barrier\n\t"
        "ds_read_b64_tr_b16 a[64:65], v139 offset:30720\n\t"
        "ds_read_b64_tr_b16 a[66:67], v139 offset:31488\n\t"
        "v_mfma_f32_16x16x32_bf16 v[36:39], a[12:15], v[100:103], 0\n\t"
        "ds_read_b64_tr_b16 a[68:69], v149 offset:30720\n\t"
        "ds_read_b64_tr_b16 a[70:71], v149 offset:31488\n\t"
        "v_mfma_f32_16x16x32_bf16 v[40:43], a[16:19], v[100:103], 0\n\t"
        "buffer_load_dwordx4 v143, s[16:19], s43 offen  lds\n\t"
        "s_add_u32 m0, s35, 36864\n\t"
        "ds_read_b128 v[108:111], v128 offset:8256\n\t"
        "s_add_u32 s43, s34, 387072\n\t"
        "s_min_u32 s43, s43, 9590784\n\t"
        "v_mfma_f32_16x16x32_bf16 v[44:47], a[20:23], v[100:103], 0\n\t"
        "ds_read_b128 v[112:115], v128 offset:12352\n\t"
        "s_waitcnt lgkmcnt(8)\n\t"
        "v_mfma_f32_16x16x32_bf16 v[0:3], a[36:39], v[104:107], v[0:3]\n\t"
        "ds_read_b64_tr_b16 a[72:73], v138 offset:36864\n\t"
        "ds_read_b64_tr_b16 a[74:75], v138 offset:37632\n\t"
        "v_mfma_f32_16x16x32_bf16 v[4:7], a[40:43], v[104:107], v[4:7]\n\t"
        "buffer_load_dwordx4 v143, s[16:19], s43 offen  lds\n\t"
        "ds_read_b64_tr_b16 a[76:77], v139 offset:36864\n\t"
        "v_mfma_f32_16x16x32_bf16 v[8:11], a[44:47], v[104:107], v[8:11]\n\t"
        "ds_read_b64_tr_b16 a[78:79], v139 offset:37632\n\t"
        "ds_read_b64_tr_b16 a[80:81], v149 offset:36864\n\t"
        "v_mfma_f32_16x16x32_bf16 v[12:15], a[48:51], v[104:107], v[12:15]\n\t"
        "ds_read_b64_tr_b16 a[82:83], v149 offset:37632\n\t"
        "v_mfma_f32_16x16x32_bf16 v[16:19], a[52:55], v[104:107], v[16:19]\n\t"
        "buffer_load_dwordx4 v[48:51], v55, s[20:23], s48 offen\n\t"
        "v_mfma_f32_16x16x32_bf16 v[20:23], a[56:59], v[104:107], v[20:23]\n\t"
        "ds_read_b64_tr_b16 a[84:85], v131 offset:0\n\t"
        "s_waitcnt lgkmcnt(8)\n\t"
        "v_mfma_f32_16x16x32_bf16 v[0:3], a[60:63], v[108:111], v[0:3]\n\t"
        "ds_read_b64_tr_b16 a[86:87], v131 offset:768\n\t"
        "v_mfma_f32_16x16x32_bf16 v[4:7], a[64:67], v[108:111], v[4:7]\n\t"
        "ds_read_b64_tr_b16 a[88:89], v132 offset:0\n\t"
        "v_mfma_f32_16x16x32_bf16 v[8:11], a[68:71], v[108:111], v[8:11]\n\t"
        "buffer_load_dword v61, v61, s[20:23], s48 offen offset:512\n\t"
        "s_add_u32 m0, s37, 36864\n\t"
        "s_add_u32 s36, s36, s38\n\t"
        "v_mfma_f32_16x16x32_bf16 v[12:15], a[36:39], v[108:111], v[12:15]\n\t"
        "ds_read_b64_tr_b16 a[90:91], v132 offset:768\n\t"
        "ds_read_b64_tr_b16 a[92:93], v133 offset:0\n\t"
        "v_mfma_f32_16x16x32_bf16 v[16:19], a[40:43], v[108:111], v[16:19]\n\t"
        "ds_read_b64_tr_b16 a[94:95], v133 offset:768\n\t"
        "v_mfma_f32_16x16x32_bf16 v[20:23], a[44:47], v[108:111], v[20:23]\n\t"
        "ds_read_b128 v[84:87], v128 offset:4224\n\t"
        "v_mfma_f32_16x16x32_bf16 v[24:27], a[48:51], v[108:111], v[24:27]\n\t"
        "buffer_load_dwordx4 v140, s[24:27], s36 offen offset:0 lds\n\t"
        "v_mfma_f32_16x16x32_bf16 v[28:31], a[52:55], v[108:111], v[28:31]\n\t"
        "ds_read_b64_tr_b16 a[96:97], v131 offset:6144\n\t"
        "v_mfma_f32_16x16x32_bf16 v[32:35], a[56:59], v[108:111], v[32:35]\n\t"
        "ds_read_b64_tr_b16 a[98:99], v131 offset:6912\n\t"
        "s_waitcnt lgkmcnt(8)\n\t"
        "v_mfma_f32_16x16x32_bf16 v[12:15], a[60:63], v[112:115], v[12:15]\n\t"
        "ds_read_b64_tr_b16 a[100:101], v132 offset:6144\n\t"
        "v_mfma_f32_16x16x32_bf16 v[16:19], a[64:67], v[112:115], v[16:19]\n\t"
        "buffer_load_dwordx4 v141, s[24:27], s36 offen offset:1024 lds\n\t"
        "ds_read_b64_tr_b16 a[102:103], v132 offset:6912\n\t"
        "v_mfma_f32_16x16x32_bf16 v[20:23], a[68:71], v[112:115], v[20:23]\n\t"
        "s_waitcnt vmcnt(9)\n\t"
        "s_barrier\n\t"
        "ds_read_b64_tr_b16 a[104:105], v133 offset:6144\n\t"
        "ds_read_b64_tr_b16 a[106:107], v133 offset:6912\n\t"
        "v_mfma_f32_16x16x32_bf16 v[24:27], a[36:39], v[112:115], v[24:27]\n\t"
        "ds_read_b128 v[88:91], v128 offset:8320\n\t"
        "ds_read_b128 v[92:95], v128 offset:12416\n\t"
        "v_mfma_f32_16x16x32_bf16 v[28:31], a[40:43], v[112:115], v[28:31]\n\t"
        "ds_read_b64_tr_b16 a[108:109], v131 offset:12288\n\t"
        "ds_read_b64_tr_b16 a[110:111], v131 offset:13056\n\t"
        "v_mfma_f32_16x16x32_bf16 v[32:35], a[44:47], v[112:115], v[32:35]\n\t"
        "buffer_load_dwordx4 v142, s[24:27], s36 offen offset:2048 lds\n\t"
        "s_add_u32 m0, s37, 49152\n\t"
        "ds_read_b64_tr_b16 a[112:113], v132 offset:12288\n\t"
        "v_mfma_f32_16x16x32_bf16 v[36:39], a[48:51], v[112:115], v[36:39]\n\t"
        "ds_read_b64_tr_b16 a[114:115], v132 offset:13056\n\t"
        "s_add_u32 s36, s36, s5\n\t"
        "ds_read_b64_tr_b16 a[116:117], v133 offset:12288\n\t"
        "v_mfma_f32_16x16x32_bf16 v[40:43], a[52:55], v[112:115], v[40:43]\n\t"
        "ds_read_b64_tr_b16 a[118:119], v133 offset:13056\n\t"
        "ds_read_b64_tr_b16 a[120:121], v131 offset:18432\n\t"
        "v_mfma_f32_16x16x32_bf16 v[44:47], a[56:59], v[112:115], v[44:47]\n\t"
        "ds_read_b64_tr_b16 a[122:123], v131 offset:19200\n\t"
        "ds_read_b64_tr_b16 a[124:125], v132 offset:18432\n\t"
        "s_waitcnt lgkmcnt(14)\n\t"
        "v_mfma_f32_16x16x32_bf16 v[0:3], a[72:75], v[84:87], v[0:3]\n\t"
        "buffer_load_dwordx4 v140, s[24:27], s36 offen offset:0 lds\n\t"
        "ds_read_b64_tr_b16 a[126:127], v132 offset:19200\n\t"
        "v_mfma_f32_16x16x32_bf16 v[4:7], a[76:79], v[84:87], v[4:7]\n\t"
        "ds_read_b64_tr_b16 a[128:129], v133 offset:18432\n\t"
        "ds_read_b64_tr_b16 a[130:131], v133 offset:19200\n\t"
        "v_mfma_f32_16x16x32_bf16 v[8:11], a[80:83], v[84:87], v[8:11]\n\t"
        "ds_read_b64_tr_b16 a[132:133], v131 offset:24576\n\t"
        "ds_read_b64_tr_b16 a[134:135], v131 offset:25344\n\t"
        "v_mfma_f32_16x16x32_bf16 v[12:15], a[84:87], v[84:87], v[12:15]\n\t"
        "ds_read_b128 v[96:99], v129 offset:4096\n\t"
        "ds_read_b64_tr_b16 a[136:137], v132 offset:24576\n\t"
        "v_mfma_f32_16x16x32_bf16 v[16:19], a[88:91], v[84:87], v[16:19]\n\t"
        "buffer_load_dwordx4 v141, s[24:27], s36 offen offset:1024 lds\n\t"
        "ds_read_b64_tr_b16 a[138:139], v132 offset:25344\n\t"
        "v_mfma_f32_16x16x32_bf16 v[20:23], a[92:95], v[84:87], v[20:23]\n\t"
        "ds_read_b64_tr_b16 a[140:141], v133 offset:24576\n\t"
        "ds_read_b64_tr_b16 a[142:143], v133 offset:25344\n\t"
        "s_waitcnt lgkmcnt(14)\n\t"
        "v_mfma_f32_16x16x32_bf16 v[0:3], a[96:99], v[88:91], v[0:3]\n\t"
        "ds_read_b64_tr_b16 a[144:145], v131 offset:30720\n\t"
        "ds_read_b64_tr_b16 a[146:147], v131 offset:31488\n\t"
        "v_mfma_f32_16x16x32_bf16 v[4:7], a[100:103], v[88:91], v[4:7]\n\t"
        "ds_read_b64_tr_b16 a[148:149], v132 offset:30720\n\t"
        "ds_read_b64_tr_b16 a[150:151], v132 offset:31488\n\t"
        "v_mfma_f32_16x16x32_bf16 v[8:11], a[104:107], v[88:91], v[8:11]\n\t"
        "ds_read_b64_tr_b16 a[152:153], v133 offset:30720\n\t"
        "ds_read_b64_tr_b16 a[154:155], v133 offset:31488\n\t"
        "v_mfma_f32_16x16x32_bf16 v[12:15], a[72:75], v[88:91], v[12:15]\n\t"
        "ds_read_b128 v[100:103], v129 offset:8192\n\t"
        "v_mfma_f32_16x16x32_bf16 v[16:19], a[76:79], v[88:91], v[16:19]\n\t"
        "v_mfma_f32_16x16x32_bf16 v[20:23], a[80:83], v[88:91], v[20:23]\n\t"
        "v_mfma_f32_16x16x32_bf16 v[24:27], a[84:87], v[88:91], v[24:27]\n\t"
        "v_mfma_f32_16x16x32_bf16 v[28:31], a[88:91], v[88:91], v[28:31]\n\t"
        "v_mfma_f32_16x16x32_bf16 v[32:35], a[92:95], v[88:91], v[32:35]\n\t"
        "v_mfma_f32_16x16x32_bf16 v[12:15], a[96:99], v[92:95], v[12:15]\n\t"
        "v_mfma_f32_16x16x32_bf16 v[16:19], a[100:103], v[92:95], v[16:19]\n\t"
        "v_mfma_f32_16x16x32_bf16 v[20:23], a[104:107], v[92:95], v[20:23]\n\t"
        "v_mfma_f32_16x16x32_bf16 v[24:27], a[72:75], v[92:95], v[24:27]\n\t"
        "ds_read_b128 v[104:107], v129 offset:12288\n\t"
        "v_mfma_f32_16x16x32_bf16 v[28:31], a[76:79], v[92:95], v[28:31]\n\t"
        "v_mfma_f32_16x16x32_bf16 v[32:35], a[80:83], v[92:95], v[32:35]\n\t"
        "v_mfma_f32_16x16x32_bf16 v[36:39], a[84:87], v[92:95], v[36:39]\n\t"
        "v_mfma_f32_16x16x32_bf16 v[40:43], a[88:91], v[92:95], v[40:43]\n\t"
        "v_mfma_f32_16x16x32_bf16 v[44:47], a[92:95], v[92:95], v[44:47]\n\t"
        "s_waitcnt lgkmcnt(1)\n\t"
        "v_mfma_f32_16x16x32_bf16 v[0:3], a[108:111], v[96:99], v[0:3]\n\t"
        "v_mfma_f32_16x16x32_bf16 v[4:7], a[112:115], v[96:99], v[4:7]\n\t"
        "v_mfma_f32_16x16x32_bf16 v[8:11], a[116:119], v[96:99], v[8:11]\n\t"
        "v_mfma_f32_16x16x32_bf16 v[12:15], a[120:123], v[96:99], v[12:15]\n\t"
        "v_mfma_f32_16x16x32_bf16 v[16:19], a[124:127], v[96:99], v[16:19]\n\t"
        "buffer_load_dwordx4 v142, s[24:27], s36 offen offset:2048 lds\n\t"
        "s_add_u32 m0, s37, 0\n\t"
        "s_add_u32 s36, s36, s39\n\t"
        "v_mfma_f32_16x16x32_bf16 v[20:23], a[128:131], v[96:99], v[20:23]\n\t"
        "v_mfma_f32_16x16x32_bf16 v[0:3], a[132:135], v[100:103], v[0:3]\n\t"
        "v_mfma_f32_16x16x32_bf16 v[4:7], a[136:139], v[100:103], v[4:7]\n\t"
        "v_mfma_f32_16x16x32_bf16 v[8:11], a[140:143], v[100:103], v[8:11]\n\t"
        "v_mfma_f32_16x16x32_bf16 v[12:15], a[108:111], v[100:103], v[12:15]\n\t"
        "v_mfma_f32_16x16x32_bf16 v[16:19], a[112:115], v[100:103], v[16:19]\n\t"
        "v_mfma_f32_16x16x32_bf16 v[20:23], a[116:119], v[100:103], v[20:23]\n\t"
        "v_mfma_f32_16x16x32_bf16 v[24:27], a[120:123], v[100:103], v[24:27]\n\t"
        "v_mfma_f32_16x16x32_bf16 v[28:31], a[124:127], v[100:103], v[28:31]\n\t"
        "v_mfma_f32_16x16x32_bf16 v[32:35], a[128:131], v[100:103], v[32:35]\n\t"
        "s_waitcnt lgkmcnt(0)\n\t"
        "v_mfma_f32_16x16x32_bf16 v[12:15], a[132:135], v[104:107], v[12:15]\n\t"
        "v_mfma_f32_16x16x32_bf16 v[16:19], a[136:139], v[104:107], v[16:19]\n\t"
        "v_mfma_f32_16x16x32_bf16 v[20:23], a[140:143], v[104:107], v[20:23]\n\t"
        "v_mfma_f32_16x16x32_bf16 v[24:27], a[108:111], v[104:107], v[24:27]\n\t"
        "ds_read_b128 v[108:111], v129 offset:4160\n\t"
        "v_mfma_f32_16x16x32_bf16 v[28:31], a[112:115], v[104:107], v[28:31]\n\t"
        "v_mfma_f32_16x16x32_bf16 v[32:35], a[116:119], v[104:107], v[32:35]\n\t"
        "v_mfma_f32_16x16x32_bf16 v[36:39], a[120:123], v[104:107], v[36:39]\n\t"
        "ds_read_b128 v[112:115], v129 offset:8256\n\t"
        "v_mfma_f32_16x16x32_bf16 v[40:43], a[124:127], v[104:107], v[40:43]\n\t"
        "v_mfma_f32_16x16x32_bf16 v[44:47], a[128:131], v[104:107], v[44:47]\n\t"
        "s_waitcnt lgkmcnt(1)\n\t"
        "v_mfma_f32_16x16x32_bf16 v[0:3], a[144:147], v[108:111], v[0:3]\n\t"
        "ds_read_b128 v[84:87], v129 offset:12352\n\t"
        "v_mfma_f32_16x16x32_bf16 v[4:7], a[148:151], v[108:111], v[4:7]\n\t"
        "v_mfma_f32_16x16x32_bf16 v[8:11], a[152:155], v[108:111], v[8:11]\n\t"
        "s_waitcnt vmcnt(3)\n\t"
        "s_barrier\n\t"
        "buffer_load_dwordx4 v140, s[24:27], s36 offen offset:0 lds\n\t"
        "ds_read_b128 v[88:91], v128 offset:0\n\t"
        ".LpS0%=:\n\t"
        "s_cbranch_execz .LrS%=\n\t"
        "s_waitcnt lgkmcnt(2)\n\t"
        "v_mfma_f32_16x16x32_bf16 v[12:15], a[144:147], v[112:115], v[12:15]\n\t"
        "ds_read_b128 v[92:95], v128 offset:64\n\t"
        "ds_read_b128 v[96:99], v128 offset:128\n\t"
        "v_readlane_b32 s46, v155, 37\n\t"
        "v_readlane_b32 s47, v155, 40\n\t"
        "v_mfma_f32_16x16x32_bf16 v[16:19], a[148:151], v[112:115], v[16:19]\n\t"
        "ds_read_b128 v[100:103], v129 offset:0\n\t"
        "ds_read_b128 v[104:107], v128 offset:16384\n\t"
        "v_and_b32 v48, s54, v48\n\t"
        "v_pk_max_u16 v60, v154, v48\n\t"
        "v_mfma_f32_16x16x32_bf16 v[20:23], a[152:155], v[112:115], v[20:23]\n\t"
        "ds_read_b32 v64, v70 offset:0\n\t"
        "ds_read_b64_tr_b16 a[156:157], v131 offset:36864\n\t"
        "v_and_b32 v49, s54, v49\n\t"
        "s_waitcnt lgkmcnt(7)\n\t"
        "v_mfma_f32_16x16x32_bf16 v[24:27], a[144:147], v[84:87], v[24:27]\n\t"
        "ds_read_b32 v67, v67 offset:0\n\t"
        "buffer_load_dwordx4 v141, s[24:27], s36 offen offset:1024 lds\n\t"
        "v_pk_max_u16 v60, v60, v49\n\t"
        "v_mfma_f32_16x16x32_bf16 v[28:31], a[148:151], v[84:87], v[28:31]\n\t"
        "ds_read_b32 v65, v70 offset:1280\n\t"
        "ds_read_b128 v[108:111], v128 offset:16448\n\t"
        "v_and_b32 v50, s54, v50\n\t"
        "v_mfma_f32_16x16x32_bf16 v[32:35], a[152:155], v[84:87], v[32:35]\n\t"
        "ds_read_b32 v66, v70 offset:2560\n\t"
        "ds_read_b64_tr_b16 a[158:159], v131 offset:37632\n\t"
        "v_pk_max_u16 v60, v60, v50\n\t"
        "v_and_b32 v51, s54, v51\n\t"
        "s_waitcnt lgkmcnt(11)\n\t"
        "v_mfma_f32_16x16x32_bf16 v[0:3], a[12:15], v[88:91], v[0:3]\n\t"
        "ds_read_b64_tr_b16 a[160:161], v132 offset:36864\n\t"
        "ds_read_b64_tr_b16 a[162:163], v132 offset:37632\n\t"
        "v_pk_max_u16 v60, v60, v51\n\t"
        "v_mfma_f32_16x16x32_bf16 v[4:7], a[16:19], v[88:91], v[4:7]\n\t"
        "buffer_load_dwordx4 v142, s[24:27], s36 offen offset:2048 lds\n\t"
        "s_add_u32 m0, s37, 12288\n\t"
        "ds_read_b64_tr_b16 a[164:165], v133 offset:36864\n\t"
        "v_mfma_f32_16x16x32_bf16 v[8:11], a[20:23], v[88:91], v[8:11]\n\t"
        "ds_read_b64_tr_b16 a[166:167], v133 offset:37632\n\t"
        "s_add_u32 s36, s36, s38\n\t"
        "s_waitcnt lgkmcnt(11)\n\t"
        "v_mfma_f32_16x16x32_bf16 v[0:3], a[48:51], v[92:95], v[0:3]\n\t"
        "ds_read_b128 v[112:115], v129 offset:64\n\t"
        "ds_read_b64_tr_b16 a[168:169], v131 offset:43008\n\t"
        "v_and_b32 v61, s54, v61\n\t"
        "v_mfma_f32_16x16x32_bf16 v[4:7], a[52:55], v[92:95], v[4:7]\n\t"
        "ds_read_b64_tr_b16 a[170:171], v131 offset:43776\n\t"
        "ds_read_b64_tr_b16 a[172:173], v132 offset:43008\n\t"
        "v_pk_max_u16 v60, v60, v61\n\t"
        "v_cmp_ne_u32 s[12:13], v60, v154\n\t"
        "v_mfma_f32_16x16x32_bf16 v[8:11], a[56:59], v[92:95], v[8:11]\n\t"
        "buffer_load_dwordx4 v140, s[24:27], s36 offen offset:0 lds\n\t"
        "ds_read_b64_tr_b16 a[174:175], v132 offset:43776\n\t"
        "s_lshl_b32 s6, 256, s4\n\t"
        "v_mfma_f32_16x16x32_bf16 v[0:3], a[84:87], v[96:99], v[0:3]\n\t"
        "s_cmp_lg_u64 s[12:13], 0\n\t"
        "s_cselect_b32 s6, s6, 0\n\t"
        "ds_read_b128 v[84:87], v129 offset:4160\n\t"
        "ds_read_b64_tr_b16 a[176:177], v133 offset:43008\n\t"
        "v_mfma_f32_16x16x32_bf16 v[4:7], a[88:91], v[96:99], v[4:7]\n\t"
        "ds_read_b64_tr_b16 a[178:179], v133 offset:43776\n\t"
        "s_waitcnt lgkmcnt(10)\n\t"
        "v_perm_b32 v56, v64, v64, s44\n\t"
        "v_mfma_f32_16x16x32_bf16 v[8:11], a[92:95], v[96:99], v[8:11]\n\t"
        "v_perm_b32 v57, v64, v64, s45\n\t"
        "v_add_f32 v58, s46, v68\n\t"
        "v_mfma_f32_16x16x32_bf16 v[0:3], a[120:123], v[100:103], v[0:3]\n\t"
        "buffer_load_dwordx4 v141, s[24:27], s36 offen offset:1024 lds\n\t"
        "v_add_f32 v59, s47, v69\n\t"
        "v_mfma_f32_16x16x32_bf16 v[4:7], a[124:127], v[100:103], v[4:7]\n\t"
        "ds_read_b128 v[88:91], v129 offset:8256\n\t"
        "v_add_f32 v56, v56, v58\n\t"
        "v_add_f32 v57, v57, v59\n\t"
        "v_mfma_f32_16x16x32_bf16 v[8:11], a[128:131], v[100:103], v[8:11]\n\t"
        "v_cvt_pk_bf16_f32 v52, v56, v57\n\t"
        "v_cvt_pk_bf16_f32 v58, v58, v59\n\t"
        "v_mfma_f32_16x16x32_bf16 v[24:27], a[24:27], v[104:107], v[24:27]\n\t"
        "v_cmp_ne_u32 s[64:65], v52, v58\n\t"
        "v_perm_b32 v57, v67, v67, s45\n\t"
        "v_mfma_f32_16x16x32_bf16 v[28:31], a[28:31], v[104:107], v[28:31]\n\t"
        "buffer_load_dwordx4 v142, s[24:27], s36 offen offset:2048 lds\n\t"
        "s_add_u32 m0, s37, 24576\n\t"
        "v_mfma_f32_16x16x32_bf16 v[32:35], a[32:35], v[104:107], v[32:35]\n\t"
        "s_add_u32 s36, s36, s5\n\t"
        "v_mbcnt_lo_u32_b32 v56, s64, 1\n\t"
        "v_mfma_f32_16x16x32_bf16 v[36:39], a[0:3], v[104:107], v[36:39]\n\t"
        "ds_read_b128 v[92:95], v129 offset:12352\n\t"
        "v_mbcnt_hi_u32_b32 v56, s65, v56\n\t"
        "s_bcnt1_i32_b64 s61, s[64:65]\n\t"
        "v_mfma_f32_16x16x32_bf16 v[40:43], a[4:7], v[104:107], v[40:43]\n\t"
        "v_add_u32 v54, s61, 1\n\t"
        "v_lshl_add_u32 v56, v56, 3, s60\n\t"
        "v_mfma_f32_16x16x32_bf16 v[44:47], a[8:11], v[104:107], v[44:47]\n\t"
        "s_mov_b64 exec, s[64:65]\n\t"
        "ds_write_b64 v56, v[52:53]\n\t"
        "s_mov_b64 exec, -1\n\t"
        "v_mfma_f32_16x16x32_bf16 v[24:27], a[60:63], v[108:111], v[24:27]\n\t"
        "buffer_load_dwordx4 v140, s[24:27], s36 offen offset:0 lds\n\t"
        "v_swap_b32 v53, v80\n\t"
        "v_mfma_f32_16x16x32_bf16 v[28:31], a[64:67], v[108:111], v[28:31]\n\t"
        "v_perm_b32 v56, v67, v67, s44\n\t"
        "v_add_f32 v56, v56, v78\n\t"
        "v_mfma_f32_16x16x32_bf16 v[32:35], a[68:71], v[108:111], v[32:35]\n\t"
        "v_add_f32 v57, v57, v79\n\t"
        "v_cvt_pk_bf16_f32 v52, v56, v57\n\t"
        "v_mfma_f32_16x16x32_bf16 v[36:39], a[36:39], v[108:111], v[36:39]\n\t"
        "ds_read_b128 v[96:99], v128 offset:16512\n\t"
        "v_cvt_pk_bf16_f32 v58, v78, v79\n\t"
        "v_mfma_f32_16x16x32_bf16 v[40:43], a[40:43], v[108:111], v[40:43]\n\t"
        "buffer_load_dwordx4 v141, s[24:27], s36 offen offset:1024 lds\n\t"
        "v_cmp_ne_u32 s[64:65], v52, v58\n\t"
        "v_readlane_b32 s47, v155, 41\n\t"
        "v_mfma_f32_16x16x32_bf16 v[44:47], a[44:47], v[108:111], v[44:47]\n\t"
        "s_waitcnt vmcnt(5) lgkmcnt(3)\n\t"
        "s_barrier\n\t"
        "ds_read_b64_tr_b16 a[180:181], v131 offset:49152\n\t"
        "ds_read_b64_tr_b16 a[182:183], v131 offset:49920\n\t"
        "v_mfma_f32_16x16x32_bf16 v[0:3], a[156:159], v[112:115], v[0:3]\n\t"
        "ds_read_b64_tr_b16 a[184:185], v132 offset:49152\n\t"
        "ds_read_b64_tr_b16 a[186:187], v132 offset:49920\n\t"
        "s_and_b64 s[64:65], s[64:65], 0xffffff\n\t"
        "v_mfma_f32_16x16x32_bf16 v[4:7], a[160:163], v[112:115], v[4:7]\n\t"
        "ds_read_b64_tr_b16 a[188:189], v133 offset:49152\n\t"
        "ds_read_b64_tr_b16 a[190:191], v133 offset:49920\n\t"
        "v_mbcnt_lo_u32_b32 v56, s64, v54\n\t"
        "v_mfma_f32_16x16x32_bf16 v[8:11], a[164:167], v[112:115], v[8:11]\n\t"
        "buffer_load_dwordx4 v142, s[24:27], s36 offen offset:2048 lds\n\t"
        "s_add_u32 m0, s37, 36864\n\t"
        "v_mfma_f32_16x16x32_bf16 v[12:15], a[156:159], v[84:87], v[12:15]\n\t"
        "ds_read_b128 v[100:103], v129 offset:16384\n\t"
        "s_add_u32 s36, s36, s39\n\t"
        "ds_read_b64_tr_b16 a[192:193], v131 offset:55296\n\t"
        "v_mfma_f32_16x16x32_bf16 v[16:19], a[160:163], v[84:87], v[16:19]\n\t"
        "ds_read_b64_tr_b16 a[194:195], v131 offset:56064\n\t"
        "ds_read_b64_tr_b16 a[196:197], v132 offset:55296\n\t"
        "s_bcnt1_i32_b64 s46, s[64:65]\n\t"
        "v_mfma_f32_16x16x32_bf16 v[20:23], a[164:167], v[84:87], v[20:23]\n\t"
        "ds_read_b64_tr_b16 a[198:199], v132 offset:56064\n\t"
        "ds_read_b64_tr_b16 a[200:201], v133 offset:55296\n\t"
        "s_add_u32 s61, s61, s46\n\t"
        "v_add_u32 v54, s46, v54\n\t"
        "v_mfma_f32_16x16x32_bf16 v[0:3], a[168:171], v[88:91], v[0:3]\n\t"
        "buffer_load_dwordx4 v140, s[24:27], s36 offen offset:0 lds\n\t"
        "ds_read_b64_tr_b16 a[202:203], v133 offset:56064\n\t"
        "v_lshl_add_u32 v56, v56, 3, s60\n\t"
        "v_mfma_f32_16x16x32_bf16 v[4:7], a[172:175], v[88:91], v[4:7]\n\t"
        "s_mov_b64 exec, s[64:65]\n\t"
        "ds_write_b64 v56, v[52:53]\n\t"
        "s_mov_b64 exec, -1\n\t"
        "v_mfma_f32_16x16x32_bf16 v[8:11], a[176:179], v[88:91], v[8:11]\n\t"
        "ds_read_b64_tr_b16 a[204:205], v131 offset:0\n\t"
        "v_add_u32 v53, 2048, v80\n\t"
        "v_mfma_f32_16x16x32_bf16 v[24:27], a[156:159], v[88:91], v[24:27]\n\t"
        "ds_read_b128 v[104:107], v129 offset:128\n\t"
        "v_readlane_b32 s46, v155, 38\n\t"
        "v_mfma_f32_16x16x32_bf16 v[28:31], a[160:163], v[88:91], v[28:31]\n\t"
        "buffer_load_dwordx4 v141, s[24:27], s36 offen offset:1024 lds\n\t"
        "v_perm_b32 v56, v65, v65, s44\n\t"
        "v_mfma_f32_16x16x32_bf16 v[32:35], a[164:167], v[88:91], v[32:35]\n\t"
        "ds_read_b64_tr_b16 a[206:207], v131 offset:768\n\t"
        "v_perm_b32 v57, v65, v65, s45\n\t"
        "s_waitcnt lgkmcnt(8)\n\t"
        "v_mfma_f32_16x16x32_bf16 v[12:15], a[168:171], v[92:95], v[12:15]\n\t"
        "ds_read_b128 v[108:111], v129 offset:4224\n\t"
        "v_add_f32 v58, s46, v68\n\t"
        "v_mfma_f32_16x16x32_bf16 v[16:19], a[172:175], v[92:95], v[16:19]\n\t"
        "ds_read_b64_tr_b16 a[208:209], v132 offset:0\n\t"
        "v_add_f32 v59, s47, v69\n\t"
        "v_add_f32 v56, v56, v58\n\t"
        "v_mfma_f32_16x16x32_bf16 v[20:23], a[176:179], v[92:95], v[20:23]\n\t"
        "buffer_load_dwordx4 v142, s[24:27], s36 offen offset:2048 lds\n\t"
        "s_add_u32 m0, s37, 49152\n\t"
        "v_mfma_f32_16x16x32_bf16 v[36:39], a[156:159], v[92:95], v[36:39]\n\t"
        "ds_read_b64_tr_b16 a[210:211], v132 offset:768\n\t"
        "s_add_u32 s36, s36, s38\n\t"
        "ds_read_b64_tr_b16 a[212:213], v133 offset:0\n\t"
        "v_mfma_f32_16x16x32_bf16 v[40:43], a[160:163], v[92:95], v[40:43]\n\t"
        "ds_read_b64_tr_b16 a[214:215], v133 offset:768\n\t"
        "v_add_f32 v57, v57, v59\n\t"
        "v_mfma_f32_16x16x32_bf16 v[44:47], a[164:167], v[92:95], v[44:47]\n\t"
        "ds_read_b64_tr_b16 a[216:217], v131 offset:6144\n\t"
        "v_cvt_pk_bf16_f32 v52, v56, v57\n\t"
        "v_mfma_f32_16x16x32_bf16 v[24:27], a[96:99], v[96:99], v[24:27]\n\t"
        "ds_read_b128 v[112:115], v129 offset:8320\n\t"
        "v_cvt_pk_bf16_f32 v58, v58, v59\n\t"
        "v_mfma_f32_16x16x32_bf16 v[28:31], a[100:103], v[96:99], v[28:31]\n\t"
        "ds_read_b64_tr_b16 a[218:219], v131 offset:6912\n\t"
        "v_cmp_ne_u32 s[64:65], v52, v58\n\t"
        "v_mfma_f32_16x16x32_bf16 v[32:35], a[104:107], v[96:99], v[32:35]\n\t"
        "ds_read_b64_tr_b16 a[220:221], v132 offset:6144\n\t"
        "v_mbcnt_lo_u32_b32 v56, s64, v54\n\t"
        "v_mfma_f32_16x16x32_bf16 v[36:39], a[72:75], v[96:99], v[36:39]\n\t"
        "s_waitcnt vmcnt(6) lgkmcnt(9)\n\t"
        "s_barrier\n\t"
        "v_mfma_f32_16x16x32_bf16 v[40:43], a[76:79], v[96:99], v[40:43]\n\t"
        "buffer_load_dwordx4 v140, s[24:27], s36 offen offset:0 lds\n\t"
        "v_mbcnt_hi_u32_b32 v56, s65, v56\n\t"
        "s_bcnt1_i32_b64 s46, s[64:65]\n\t"
        "v_mfma_f32_16x16x32_bf16 v[44:47], a[80:83], v[96:99], v[44:47]\n\t"
        "ds_read_b64_tr_b16 a[222:223], v132 offset:6912\n\t"
        "s_add_u32 s61, s61, s46\n\t"
        "v_mfma_f32_16x16x32_bf16 v[24:27], a[132:135], v[100:103], v[24:27]\n\t"
        "ds_read_b64_tr_b16 a[224:225], v133 offset:6144\n\t"
        "v_add_u32 v54, s46, v54\n\t"
        "v_mfma_f32_16x16x32_bf16 v[28:31], a[136:139], v[100:103], v[28:31]\n\t"
        "ds_read_b64_tr_b16 a[226:227], v133 offset:6912\n\t"
        "v_min_u32 v56, 97, v56\n\t"
        "v_mfma_f32_16x16x32_bf16 v[32:35], a[140:143], v[100:103], v[32:35]\n\t"
        "buffer_load_dwordx4 v141, s[24:27], s36 offen offset:1024 lds\n\t"
        "v_lshl_add_u32 v56, v56, 3, s60\n\t"
        "v_mfma_f32_16x16x32_bf16 v[36:39], a[108:111], v[100:103], v[36:39]\n\t"
        "s_mov_b64 exec, s[64:65]\n\t"
        "ds_write_b64 v56, v[52:53]\n\t"
        "s_mov_b64 exec, -1\n\t"
        "v_mfma_f32_16x16x32_bf16 v[40:43], a[112:115], v[100:103], v[40:43]\n\t"
        "ds_read_b128 v[84:87], v129 offset:12416\n\t"
        "v_add_u32 v53, 2048, v53\n\t"
        "v_mfma_f32_16x16x32_bf16 v[44:47], a[116:119], v[100:103], v[44:47]\n\t"
        "ds_read_b64_tr_b16 a[228:229], v131 offset:12288\n\t"
        "v_readlane_b32 s46, v155, 39\n\t"
        "v_mfma_f32_16x16x32_bf16 v[0:3], a[192:195], v[104:107], v[0:3]\n\t"
        "buffer_load_dwordx4 v142, s[24:27], s36 offen offset:2048 lds\n\t"
        "v_readlane_b32 s40, v155, 12\n\t"
        "v_mfma_f32_16x16x32_bf16 v[4:7], a[196:199], v[104:107], v[4:7]\n\t"
        "ds_read_b64_tr_b16 a[230:231], v131 offset:13056\n\t"
        "v_readlane_b32 s43, v155, 0\n\t"
        "s_add_u32 m0, s40, s35\n\t"
        "v_mfma_f32_16x16x32_bf16 v[8:11], a[200:203], v[104:107], v[8:11]\n\t"
        "ds_read_b64_tr_b16 a[232:233], v132 offset:12288\n\t"
        "v_readlane_b32 s47, v155, 42\n\t"
        "s_waitcnt lgkmcnt(7)\n\t"
        "v_mfma_f32_16x16x32_bf16 v[0:3], a[180:183], v[108:111], v[0:3]\n\t"
        "ds_read_b64_tr_b16 a[234:235], v132 offset:13056\n\t"
        "v_perm_b32 v56, v66, v66, s44\n\t"
        "v_mfma_f32_16x16x32_bf16 v[4:7], a[184:187], v[108:111], v[4:7]\n\t"
        "buffer_load_dwordx4 v134, s[16:19], s43 offen  lds\n\t"
        "v_readlane_b32 s40, v155, 13\n\t"
        "v_mfma_f32_16x16x32_bf16 v[8:11], a[188:191], v[108:111], v[8:11]\n\t"
        "ds_read_b64_tr_b16 a[236:237], v133 offset:12288\n\t"
        "v_readlane_b32 s43, v155, 1\n\t"
        "v_mfma_f32_16x16x32_bf16 v[12:15], a[192:195], v[108:111], v[12:15]\n\t"
        "ds_read_b128 v[88:91], v129 offset:16448\n\t"
        "s_add_u32 m0, s40, s35\n\t"
        "v_mfma_f32_16x16x32_bf16 v[16:19], a[196:199], v[108:111], v[16:19]\n\t"
        "ds_read_b64_tr_b16 a[238:239], v133 offset:13056\n\t"
        "v_perm_b32 v57, v66, v66, s45\n\t"
        "v_mfma_f32_16x16x32_bf16 v[20:23], a[200:203], v[108:111], v[20:23]\n\t"
        "buffer_load_dwordx4 v135, s[16:19], s43 offen  lds\n\t"
        "v_readlane_b32 s40, v155, 14\n\t"
        "v_mfma_f32_16x16x32_bf16 v[0:3], a[204:207], v[112:115], v[0:3]\n\t"
        "ds_read_b64_tr_b16 a[240:241], v131 offset:18432\n\t"
        "v_readlane_b32 s43, v155, 2\n\t"
        "v_mfma_f32_16x16x32_bf16 v[4:7], a[208:211], v[112:115], v[4:7]\n\t"
        "ds_read_b64_tr_b16 a[242:243], v131 offset:19200\n\t"
        "s_add_u32 m0, s40, s35\n\t"
        "v_mfma_f32_16x16x32_bf16 v[8:11], a[212:215], v[112:115], v[8:11]\n\t"
        "ds_read_b64_tr_b16 a[244:245], v132 offset:18432\n\t"
        "v_add_f32 v58, s46, v68\n\t"
        "v_add_f32 v59, s47, v69\n\t"
        "v_mfma_f32_16x16x32_bf16 v[12:15], a[180:183], v[112:115], v[12:15]\n\t"
        "buffer_load_dwordx4 v134, s[16:19], s43 offen  lds\n\t"
        "v_readlane_b32 s40, v155, 15\n\t"
        "v_mfma_f32_16x16x32_bf16 v[16:19], a[184:187], v[112:115], v[16:19]\n\t"
        "ds_read_b128 v[92:95], v130 offset:0\n\t"
        "v_readlane_b32 s43, v155, 3\n\t"
        "v_mfma_f32_16x16x32_bf16 v[20:23], a[188:191], v[112:115], v[20:23]\n\t"
        "ds_read_b64_tr_b16 a[246:247], v132 offset:19200\n\t"
        "s_add_u32 m0, s40, s35\n\t"
        "v_mfma_f32_16x16x32_bf16 v[24:27], a[192:195], v[112:115], v[24:27]\n\t"
        "ds_read_b128 v[96:99], v130 offset:4096\n\t"
        "v_add_f32 v56, v56, v58\n\t"
        "v_mfma_f32_16x16x32_bf16 v[28:31], a[196:199], v[112:115], v[28:31]\n\t"
        "buffer_load_dwordx4 v135, s[16:19], s43 offen  lds\n\t"
        "v_readlane_b32 s40, v155, 16\n\t"
        "v_mfma_f32_16x16x32_bf16 v[32:35], a[200:203], v[112:115], v[32:35]\n\t"
        "ds_read_b64_tr_b16 a[248:249], v133 offset:18432\n\t"
        "v_readlane_b32 s43, v155, 4\n\t"
        "s_waitcnt lgkmcnt(7)\n\t"
        "v_mfma_f32_16x16x32_bf16 v[12:15], a[204:207], v[84:87], v[12:15]\n\t"
        "ds_read_b64_tr_b16 a[250:251], v133 offset:19200\n\t"
        "s_add_u32 m0, s40, s35\n\t"
        "v_mfma_f32_16x16x32_bf16 v[16:19], a[208:211], v[84:87], v[16:19]\n\t"
        "v_add_f32 v57, v57, v59\n\t"
        "v_cvt_pk_bf16_f32 v52, v56, v57\n\t"
        "v_mfma_f32_16x16x32_bf16 v[20:23], a[212:215], v[84:87], v[20:23]\n\t"
        "buffer_load_dwordx4 v134, s[16:19], s43 offen  lds\n\t"
        "v_readlane_b32 s40, v155, 17\n\t"
        "v_mfma_f32_16x16x32_bf16 v[24:27], a[180:183], v[84:87], v[24:27]\n\t"
        "ds_read_b128 v[100:103], v130 offset:8192\n\t"
        "v_readlane_b32 s43, v155, 5\n\t"
        "v_mfma_f32_16x16x32_bf16 v[28:31], a[184:187], v[84:87], v[28:31]\n\t"
        "s_add_u32 m0, s40, s35\n\t"
        "v_cvt_pk_bf16_f32 v58, v58, v59\n\t"
        "v_mfma_f32_16x16x32_bf16 v[32:35], a[188:191], v[84:87], v[32:35]\n\t"
        "v_cmp_ne_u32 s[64:65], v52, v58\n\t"
        "v_mfma_f32_16x16x32_bf16 v[36:39], a[192:195], v[84:87], v[36:39]\n\t"
        "buffer_load_dwordx4 v135, s[16:19], s43 offen  lds\n\t"
        "v_readlane_b32 s40, v155, 18\n\t"
        "v_readlane_b32 s43, v155, 6\n\t"
        "v_mfma_f32_16x16x32_bf16 v[40:43], a[196:199], v[84:87], v[40:43]\n\t"
        "s_add_u32 m0, s40, s35\n\t"
        "v_mbcnt_lo_u32_b32 v56, s64, v54\n\t"
        "v_mfma_f32_16x16x32_bf16 v[44:47], a[200:203], v[84:87], v[44:47]\n\t"
        "s_waitcnt vmcnt(6) lgkmcnt(1)\n\t"
        "s_barrier\n\t"
        "v_mfma_f32_16x16x32_bf16 v[24:27], a[168:171], v[88:91], v[24:27]\n\t"
        "ds_read_b64_tr_b16 a[252:253], v131 offset:24576\n\t"
        "v_mbcnt_hi_u32_b32 v56, s65, v56\n\t"
        "v_mfma_f32_16x16x32_bf16 v[28:31], a[172:175], v[88:91], v[28:31]\n\t"
        "buffer_load_dwordx4 v136, s[16:19], s43 offen  lds\n\t"
        "v_readlane_b32 s40, v155, 20\n\t"
        "v_mfma_f32_16x16x32_bf16 v[32:35], a[176:179], v[88:91], v[32:35]\n\t"
        "ds_read_b64_tr_b16 a[254:255], v131 offset:25344\n\t"
        "v_readlane_b32 s43, v155, 8\n\t"
        "v_mfma_f32_16x16x32_bf16 v[36:39], a[144:147], v[88:91], v[36:39]\n\t"
        "ds_read_b128 v[104:107], v130 offset:12288\n\t"
        "s_add_u32 m0, s40, s35\n\t"
        "v_mfma_f32_16x16x32_bf16 v[40:43], a[148:151], v[88:91], v[40:43]\n\t"
        "ds_read_b64_tr_b16 v[188:189], v132 offset:24576\n\t"
        "s_bcnt1_i32_b64 s46, s[64:65]\n\t"
        "v_mfma_f32_16x16x32_bf16 v[44:47], a[152:155], v[88:91], v[44:47]\n\t"
        "buffer_load_dwordx4 v136, s[16:19], s43 offen  lds\n\t"
        "v_readlane_b32 s40, v155, 19\n\t"
        "v_mfma_f32_16x16x32_bf16 v[0:3], a[228:231], v[92:95], v[0:3]\n\t"
        "ds_read_b64_tr_b16 v[190:191], v132 offset:25344\n\t"
        "ds_read_b64_tr_b16 v[192:193], v133 offset:24576\n\t"
        "v_readlane_b32 s43, v155, 7\n\t"
        "v_mfma_f32_16x16x32_bf16 v[4:7], a[232:235], v[92:95], v[4:7]\n\t"
        "ds_read_b64_tr_b16 v[194:195], v133 offset:25344\n\t"
        "s_add_u32 m0, s40, s35\n\t"
        "v_mfma_f32_16x16x32_bf16 v[8:11], a[236:239], v[92:95], v[8:11]\n\t"
        "ds_read_b64_tr_b16 v[196:197], v131 offset:30720\n\t"
        "s_add_u32 s61, s61, s46\n\t"
        "v_mfma_f32_16x16x32_bf16 v[0:3], a[216:219], v[96:99], v[0:3]\n\t"
        "buffer_load_dwordx4 v137, s[16:19], s43 offen  lds\n\t"
        "s_mov_b32 s3, s61\n\t"
        "v_mfma_f32_16x16x32_bf16 v[4:7], a[220:223], v[96:99], v[4:7]\n\t"
        "ds_read_b64_tr_b16 v[198:199], v131 offset:31488\n\t"
        "v_min_u32 v56, 97, v56\n\t"
        "v_mfma_f32_16x16x32_bf16 v[8:11], a[224:227], v[96:99], v[8:11]\n\t"
        "ds_read_b64_tr_b16 v[200:201], v132 offset:30720\n\t"
        "v_lshl_add_u32 v56, v56, 3, s60\n\t"
        "v_mfma_f32_16x16x32_bf16 v[12:15], a[228:231], v[96:99], v[12:15]\n\t"
        "s_mov_b64 exec, s[64:65]\n\t"
        "ds_write_b64 v56, v[52:53]\n\t"
        "s_mov_b64 exec, -1\n\t"
        "v_mfma_f32_16x16x32_bf16 v[16:19], a[232:235], v[96:99], v[16:19]\n\t"
        "s_cmp_gt_u32 s3, 64\n\t"
        "s_cselect_b32 s8, 1, 0\n\t"
        "v_mfma_f32_16x16x32_bf16 v[20:23], a[236:239], v[96:99], v[20:23]\n\t"
        "ds_read_b128 v[108:111], v129 offset:16512\n\t"
        "s_or_b32 s56, s56, s8\n\t"
        "s_waitcnt lgkmcnt(7)\n\t"
        "v_mfma_f32_16x16x32_bf16 v[0:3], a[240:243], v[100:103], v[0:3]\n\t"
        "ds_read_b64_tr_b16 v[202:203], v132 offset:31488\n\t"
        "s_min_u32 s61, s61, 80\n\t"
        "v_mfma_f32_16x16x32_bf16 v[4:7], a[244:247], v[100:103], v[4:7]\n\t"
        "ds_read_b64_tr_b16 v[204:205], v133 offset:30720\n\t"
        "v_min_u32 v78, s61, v116\n\t"
        "v_mfma_f32_16x16x32_bf16 v[8:11], a[248:251], v[100:103], v[8:11]\n\t"
        "ds_read_b64_tr_b16 v[206:207], v133 offset:31488\n\t"
        "v_lshl_add_u32 v78, v78, 3, s60\n\t"
        "v_mfma_f32_16x16x32_bf16 v[12:15], a[216:219], v[100:103], v[12:15]\n\t"
        "ds_read_b64 v[62:63], v78\n\t"
        "v_cmp_ge_u32 s[66:67], s61, v116\n\t"
        "v_mfma_f32_16x16x32_bf16 v[16:19], a[220:223], v[100:103], v[16:19]\n\t"
        "ds_read_b128 v[112:115], v130 offset:64\n\t"
        "s_mov_b32 s46, 0x07060302\n\t"
        "v_mfma_f32_16x16x32_bf16 v[20:23], a[224:227], v[100:103], v[20:23]\n\t"
        "ds_read_b64_tr_b16 v[208:209], v131 offset:36864\n\t"
        "v_mfma_f32_16x16x32_bf16 v[24:27], a[228:231], v[100:103], v[24:27]\n\t"
        "ds_read_b128 v[84:87], v130 offset:4160\n\t"
        "v_mfma_f32_16x16x32_bf16 v[28:31], a[232:235], v[100:103], v[28:31]\n\t"
        "ds_read_b64_tr_b16 v[210:211], v131 offset:37632\n\t"
        "v_mfma_f32_16x16x32_bf16 v[32:35], a[236:239], v[100:103], v[32:35]\n\t"
        "ds_read_b64_tr_b16 v[212:213], v132 offset:36864\n\t"
        "v_mfma_f32_16x16x32_bf16 v[12:15], a[240:243], v[104:107], v[12:15]\n\t"
        "ds_read_b64_tr_b16 v[214:215], v132 offset:37632\n\t"
        "v_mfma_f32_16x16x32_bf16 v[16:19], a[244:247], v[104:107], v[16:19]\n\t"
        "ds_read_b64_tr_b16 v[216:217], v133 offset:36864\n\t"
        "v_mfma_f32_16x16x32_bf16 v[20:23], a[248:251], v[104:107], v[20:23]\n\t"
        "ds_read_b64_tr_b16 v[218:219], v133 offset:37632\n\t"
        "v_mfma_f32_16x16x32_bf16 v[24:27], a[216:219], v[104:107], v[24:27]\n\t"
        "s_mov_b64 exec, s[66:67]\n\t"
        "s_waitcnt lgkmcnt(8)\n\t"
        "ds_write_b32 v63, v117\n\t"
        "s_mov_b64 exec, -1\n\t"
        "v_mfma_f32_16x16x32_bf16 v[28:31], a[220:223], v[104:107], v[28:31]\n\t"
        "ds_read_b128 v[88:91], v130 offset:8256\n\t"
        "v_and_b32 v78, 0xfffff800, v63\n\t"
        "v_lshlrev_b32 v72, 16, v62\n\t"
        "v_mfma_f32_16x16x32_bf16 v[32:35], a[224:227], v[104:107], v[32:35]\n\t"
        "ds_read_b128 v[64:67], v78 offset:1536\n\t"
        "v_and_b32 v73, 0xffff0000, v62\n\t"
        "v_mfma_f32_16x16x32_bf16 v[36:39], a[228:231], v[104:107], v[36:39]\n\t"
        "ds_read_b64_tr_b16 v[220:221], v131 offset:43008\n\t"
        "v_med3_f32 v72, v72, 0, s63\n\t"
        "v_mfma_f32_16x16x32_bf16 v[40:43], a[232:235], v[104:107], v[40:43]\n\t"
        "ds_read_b64_tr_b16 v[222:223], v131 offset:43776\n\t"
        "v_med3_f32 v73, v73, 0, s63\n\t"
        "v_mfma_f32_16x16x32_bf16 v[44:47], a[236:239], v[104:107], v[44:47]\n\t"
        "ds_read_b64_tr_b16 v[224:225], v132 offset:43008\n\t"
        "v_perm_b32 v62, v73, v72, s46\n\t"
        "v_mfma_f32_16x16x32_bf16 v[24:27], a[204:207], v[108:111], v[24:27]\n\t"
        "ds_read_b64_tr_b16 v[226:227], v132 offset:43776\n\t"
        "v_fract_f32 v74, v72\n\t"
        "v_mfma_f32_16x16x32_bf16 v[28:31], a[208:211], v[108:111], v[28:31]\n\t"
        "ds_read_b64_tr_b16 v[228:229], v133 offset:43008\n\t"
        "v_fract_f32 v75, v73\n\t"
        "v_mfma_f32_16x16x32_bf16 v[32:35], a[212:215], v[108:111], v[32:35]\n\t"
        "ds_read_b64_tr_b16 v[230:231], v133 offset:43776\n\t"
        "v_cvt_u32_f32 v76, v72\n\t"
        "v_mfma_f32_16x16x32_bf16 v[36:39], a[180:183], v[108:111], v[36:39]\n\t"
        "ds_read_b128 v[92:95], v130 offset:12352\n\t"
        "v_cvt_u32_f32 v77, v73\n\t"
        "v_cmp_neq_f32 s[64:65], 0, v74\n\t"
        "v_mfma_f32_16x16x32_bf16 v[40:43], a[184:187], v[108:111], v[40:43]\n\t"
        "ds_read_b64_tr_b16 v[232:233], v131 offset:49152\n\t"
        "v_cmp_neq_f32 s[44:45], 0, v75\n\t"
        "v_mfma_f32_16x16x32_bf16 v[44:47], a[188:191], v[108:111], v[44:47]\n\t"
        "ds_read_b64_tr_b16 v[234:235], v131 offset:49920\n\t"
        "v_sub_f32 v78, 1.0, v74\n\t"
        "s_waitcnt lgkmcnt(9)\n\t"
        "v_mfma_f32_16x16x32_bf16 v[0:3], v[196:199], v[112:115], v[0:3]\n\t"
        "ds_read_b64_tr_b16 v[236:237], v132 offset:49152\n\t"
        "v_sub_f32 v79, 1.0, v75\n\t"
        "v_mfma_f32_16x16x32_bf16 v[4:7], v[200:203], v[112:115], v[4:7]\n\t"
        "ds_read_b64_tr_b16 v[238:239], v132 offset:49920\n\t"
        "v_cvt_pk_bf16_f32 v79, v78, v79\n\t"
        "v_mfma_f32_16x16x32_bf16 v[8:11], v[204:207], v[112:115], v[8:11]\n\t"
        "ds_read_b64_tr_b16 v[240:241], v133 offset:49152\n\t"
        "v_lshlrev_b32 v78, 16, v79\n\t"
        "v_mfma_f32_16x16x32_bf16 v[0:3], a[252:255], v[84:87], v[0:3]\n\t"
        "ds_read_b64_tr_b16 v[242:243], v133 offset:49920\n\t"
        "v_and_b32 v79, 0xffff0000, v79\n\t"
        "v_mfma_f32_16x16x32_bf16 v[4:7], v[188:191], v[84:87], v[4:7]\n\t"
        "ds_read_b64_tr_b16 v[244:245], v131 offset:55296\n\t"
        "v_mul_f32 v80, v78, v79\n\t"
        "v_mfma_f32_16x16x32_bf16 v[8:11], v[192:195], v[84:87], v[8:11]\n\t"
        "ds_read_b64_tr_b16 v[246:247], v131 offset:56064\n\t"
        "v_mul_f32 v78, v78, v75\n\t"
        "v_mfma_f32_16x16x32_bf16 v[12:15], v[196:199], v[84:87], v[12:15]\n\t"
        "ds_read_b128 v[96:99], v130 offset:128\n\t"
        "v_mul_f32 v79, v74, v79\n\t"
        "v_mfma_f32_16x16x32_bf16 v[16:19], v[200:203], v[84:87], v[16:19]\n\t"
        "ds_read_b64_tr_b16 v[248:249], v132 offset:55296\n\t"
        "v_mul_f32 v81, v74, v75\n\t"
        "v_mfma_f32_16x16x32_bf16 v[20:23], v[204:207], v[84:87], v[20:23]\n\t"
        "ds_read_b64_tr_b16 v[250:251], v132 offset:56064\n\t"
        "v_cndmask_b32 v78, v79, v78, s[44:45]\n\t"
        "v_mfma_f32_16x16x32_bf16 v[0:3], v[208:211], v[88:91], v[0:3]\n\t"
        "ds_read_b128 v[100:103], v130 offset:4224\n\t"
        "v_cndmask_b32 v79, 0, v79, s[44:45]\n\t"
        "v_mfma_f32_16x16x32_bf16 v[4:7], v[212:215], v[88:91], v[4:7]\n\t"
        "ds_read_b64_tr_b16 v[252:253], v133 offset:55296\n\t"
        "v_cvt_pk_bf16_f32 v70, v80, v78\n\t"
        "v_mfma_f32_16x16x32_bf16 v[8:11], v[216:219], v[88:91], v[8:11]\n\t"
        "ds_read_b64_tr_b16 v[254:255], v133 offset:56064\n\t"
        "v_cvt_pk_bf16_f32 v71, v79, v81\n\t"
        "v_mfma_f32_16x16x32_bf16 v[12:15], a[252:255], v[88:91], v[12:15]\n\t"
        "v_pk_max_u16 v78, v62, v64\n\t"
        "v_pk_min_u16 v78, v78, v65\n\t"
        "v_mfma_f32_16x16x32_bf16 v[16:19], v[188:191], v[88:91], v[16:19]\n\t"
        "v_cmp_ne_u32 vcc, v78, v62\n\t"
        "v_sub_u32 v78, v77, v67\n\t"
        "v_mfma_f32_16x16x32_bf16 v[20:23], v[192:195], v[88:91], v[20:23]\n\t"
        "v_mad_u32_u24 v79, v77, 6, v66\n\t"
        "v_mad_u32_u24 v79, v76, s62, v79\n\t"
        "v_mfma_f32_16x16x32_bf16 v[24:27], v[196:199], v[88:91], v[24:27]\n\t"
        "ds_read_b128 v[104:107], v130 offset:8320\n\t"
        "s_and_b64 vcc, vcc, s[66:67]\n\t"
        "v_mfma_f32_16x16x32_bf16 v[28:31], v[200:203], v[88:91], v[28:31]\n\t"
        "s_or_b64 s[56:57], s[56:57], vcc\n\t"
        "v_bfe_u32 v80, v78, 2, 1\n\t"
        "v_mfma_f32_16x16x32_bf16 v[32:35], v[204:207], v[88:91], v[32:35]\n\t"
        "v_or_b32 v80, v80, v79\n\t"
        "v_add_u32 v78, 1, v78\n\t"
        "s_waitcnt lgkmcnt(5)\n\t"
        "v_mfma_f32_16x16x32_bf16 v[12:15], v[208:211], v[92:95], v[12:15]\n\t"
        "v_bfe_u32 v78, v78, 2, 1\n\t"
        "v_add3_u32 v78, v79, v78, 6\n\t"
        "v_mfma_f32_16x16x32_bf16 v[16:19], v[212:215], v[92:95], v[16:19]\n\t"
        "v_cndmask_b32 v78, v80, v78, s[44:45]\n\t"
        "v_cndmask_b32 v79, 0, v119, s[64:65]\n\t"
        "v_mfma_f32_16x16x32_bf16 v[20:23], v[216:219], v[92:95], v[20:23]\n\t"
        "v_add_u32 v81, v80, v79\n\t"
        "v_add_u32 v82, v78, v79\n\t"
        "v_mfma_f32_16x16x32_bf16 v[24:27], a[252:255], v[92:95], v[24:27]\n\t"
        "v_cndmask_b32 v78, v81, v78, s[44:45]\n\t"
        "v_lshl_or_b32 v68, v78, 16, v80\n\t"
        "v_mfma_f32_16x16x32_bf16 v[28:31], v[188:191], v[92:95], v[28:31]\n\t"
        "v_lshl_or_b32 v69, v82, 16, v81\n\t"
        "v_mfma_f32_16x16x32_bf16 v[32:35], v[192:195], v[92:95], v[32:35]\n\t"
        "ds_write_b128 v118, v[68:71]\n\t"
        "v_mfma_f32_16x16x32_bf16 v[36:39], v[196:199], v[92:95], v[36:39]\n\t"
        "s_cmp_gt_u32 s3, 16\n\t"
        "s_cselect_b32 s8, 2, 0\n\t"
        "v_mfma_f32_16x16x32_bf16 v[40:43], v[200:203], v[92:95], v[40:43]\n\t"
        "s_cmp_lg_u64 s[56:57], 0\n\t"
        "s_addc_u32 s8, s8, s6\n\t"
        "v_mfma_f32_16x16x32_bf16 v[44:47], v[204:207], v[92:95], v[44:47]\n\t"
        "ds_read_b128 v[160:163], v185 offset:0\n\t"
        "v_mov_b32 v151, s8\n\t"
        "v_mfma_f32_16x16x32_bf16 v[0:3], v[232:235], v[96:99], v[0:3]\n\t"
        "s_waitcnt vmcnt(0) lgkmcnt(2)\n\t"
        "s_barrier\n\t"
        "v_mfma_f32_16x16x32_bf16 v[4:7], v[236:239], v[96:99], v[4:7]\n\t"
        "s_mov_b64 exec, 1\n\t"
        "ds_write_b32 v153, v151\n\t"
        "s_mov_b64 exec, -1\n\t"
        "v_mfma_f32_16x16x32_bf16 v[8:11], v[240:243], v[96:99], v[8:11]\n\t"
        "ds_read_b128 v[108:111], v130 offset:12416\n\t"
        "v_xor_b32 v154, 32, v186\n\t"
        "v_mfma_f32_16x16x32_bf16 v[0:3], v[220:223], v[100:103], v[0:3]\n\t"
        "v_mfma_f32_16x16x32_bf16 v[4:7], v[224:227], v[100:103], v[4:7]\n\t"
        "v_mfma_f32_16x16x32_bf16 v[8:11], v[228:231], v[100:103], v[8:11]\n\t"
        "v_mfma_f32_16x16x32_bf16 v[12:15], v[232:235], v[100:103], v[12:15]\n\t"
        "ds_read_b128 v[112:115], v130 offset:16384\n\t"
        "v_mfma_f32_16x16x32_bf16 v[16:19], v[236:239], v[100:103], v[16:19]\n\t"
        "v_mfma_f32_16x16x32_bf16 v[20:23], v[240:243], v[100:103], v[20:23]\n\t"
        "s_waitcnt lgkmcnt(3)\n\t"
        "v_lshlrev_b32_sdwa v157, 5, v160 dst_sel:DWORD dst_unused:UNUSED_PAD src0_sel:DWORD src1_sel:WORD_0\n\t"
        "v_lshlrev_b32_sdwa v158, 5, v160 dst_sel:DWORD dst_unused:UNUSED_PAD src0_sel:DWORD src1_sel:WORD_1\n\t"
        "v_mfma_f32_16x16x32_bf16 v[0:3], v[244:247], v[104:107], v[0:3]\n\t"
        "v_or_b32 v157, v184, v157\n\t"
        "v_or_b32 v158, v184, v158\n\t"
        "v_mfma_f32_16x16x32_bf16 v[4:7], v[248:251], v[104:107], v[4:7]\n\t"
        "ds_read_b64_tr_b16 v[60:61], v157 offset:0\n\t"
        "v_xor_b32 v159, 32, v157\n\t"
        "v_mfma_f32_16x16x32_bf16 v[8:11], v[252:255], v[104:107], v[8:11]\n\t"
        "ds_read_b64_tr_b16 v[62:63], v158 offset:0\n\t"
        "v_xor_b32 v152, 32, v158\n\t"
        "v_mfma_f32_16x16x32_bf16 v[12:15], v[220:223], v[104:107], v[12:15]\n\t"
        "ds_read_b64_tr_b16 v[64:65], v159 offset:0\n\t"
        "v_perm_b32 v164, v162, v162, v168\n\t"
        "v_mfma_f32_16x16x32_bf16 v[16:19], v[224:227], v[104:107], v[16:19]\n\t"
        "ds_read_b64_tr_b16 v[66:67], v152 offset:0\n\t"
        "v_perm_b32 v165, v162, v162, v169\n\t"
        "v_mfma_f32_16x16x32_bf16 v[20:23], v[228:231], v[104:107], v[20:23]\n\t"
        "ds_read_b64_tr_b16 v[68:69], v157 offset:64\n\t"
        "v_perm_b32 v166, v162, v162, v170\n\t"
        "v_mfma_f32_16x16x32_bf16 v[24:27], v[232:235], v[104:107], v[24:27]\n\t"
        "ds_read_b64_tr_b16 v[70:71], v158 offset:64\n\t"
        "v_perm_b32 v167, v162, v162, v171\n\t"
        "v_mfma_f32_16x16x32_bf16 v[28:31], v[236:239], v[104:107], v[28:31]\n\t"
        "ds_read_b64_tr_b16 v[72:73], v159 offset:64\n\t"
        "v_cmp_ne_u32 vcc, 0, v163\n\t"
        "v_mfma_f32_16x16x32_bf16 v[32:35], v[240:243], v[104:107], v[32:35]\n\t"
        "ds_read_b128 v[84:87], v130 offset:16448\n\t"
        "s_waitcnt lgkmcnt(8)\n\t"
        "v_mfma_f32_16x16x32_bf16 v[12:15], v[244:247], v[108:111], v[12:15]\n\t"
        "ds_read_b64_tr_b16 v[74:75], v152 offset:64\n\t"
        "v_mfma_f32_16x16x32_bf16 v[16:19], v[248:251], v[108:111], v[16:19]\n\t"
        "ds_read_b64_tr_b16 v[76:77], v157 offset:128\n\t"
        "v_mfma_f32_16x16x32_bf16 v[20:23], v[252:255], v[108:111], v[20:23]\n\t"
        "ds_read_b64_tr_b16 v[78:79], v158 offset:128\n\t"
        "s_waitcnt lgkmcnt(9)\n\t"
        "v_mfma_f32_16x16x32_bf16 v[48:51], v[60:63], v[164:167], 0\n\t"
        "ds_read_b64_tr_b16 v[80:81], v159 offset:128\n\t"
        "v_mfma_f32_16x16x32_bf16 v[24:27], v[220:223], v[108:111], v[24:27]\n\t"
        "ds_read_b64_tr_b16 v[82:83], v152 offset:128\n\t"
        "s_waitcnt lgkmcnt(9)\n\t"
        "v_mfma_f32_16x16x32_bf16 v[52:55], v[64:67], v[164:167], 0\n\t"
        "ds_read_b128 v[88:91], v130 offset:16512\n\t"
        "v_mfma_f32_16x16x32_bf16 v[28:31], v[224:227], v[108:111], v[28:31]\n\t"
        "s_waitcnt lgkmcnt(8)\n\t"
        "v_mfma_f32_16x16x32_bf16 v[56:59], v[68:71], v[164:167], 0\n\t"
        "v_mfma_f32_16x16x32_bf16 v[32:35], v[228:231], v[108:111], v[32:35]\n\t"
        "v_mfma_f32_16x16x32_bf16 v[36:39], v[232:235], v[108:111], v[36:39]\n\t"
        ".LpS1%=:\n\t"
        "s_cbranch_execz .LrS%=\n\t"
        "ds_read_b128 v[92:95], v128 offset:20480\n\t"
        "s_waitcnt lgkmcnt(6)\n\t"
        "v_mfma_f32_16x16x32_bf16 v[116:119], v[72:75], v[164:167], 0\n\t"
        "v_mfma_f32_16x16x32_bf16 v[40:43], v[236:239], v[108:111], v[40:43]\n\t"
        "s_waitcnt lgkmcnt(4)\n\t"
        "v_mfma_f32_16x16x32_bf16 v[120:123], v[76:79], v[164:167], 0\n\t"
        "v_mfma_f32_16x16x32_bf16 v[44:47], v[240:243], v[108:111], v[44:47]\n\t"
        "s_waitcnt lgkmcnt(2)\n\t"
        "v_mfma_f32_16x16x32_bf16 v[124:127], v[80:83], v[164:167], 0\n\t"
        "s_cbranch_vccz .Lx56%=\n\t"
        "v_lshlrev_b32_sdwa v157, 5, v161 dst_sel:DWORD dst_unused:UNUSED_PAD src0_sel:DWORD src1_sel:WORD_0\n\t"
        "v_lshlrev_b32_sdwa v158, 5, v161 dst_sel:DWORD dst_unused:UNUSED_PAD src0_sel:DWORD src1_sel:WORD_1\n\t"
        "v_or_b32 v157, v184, v157\n\t"
        "v_or_b32 v158, v184, v158\n\t"
        "v_xor_b32 v159, 32, v157\n\t"
        "v_xor_b32 v152, 32, v158\n\t"
        "ds_read_b64_tr_b16 v[60:61], v157 offset:0\n\t"
        "ds_read_b64_tr_b16 v[62:63], v158 offset:0\n\t"
        "ds_read_b64_tr_b16 v[64:65], v159 offset:0\n\t"
        "ds_read_b64_tr_b16 v[66:67], v152 offset:0\n\t"
        "ds_read_b64_tr_b16 v[68:69], v157 offset:64\n\t"
        "ds_read_b64_tr_b16 v[70:71], v158 offset:64\n\t"
        "ds_read_b64_tr_b16 v[72:73], v159 offset:64\n\t"
        "ds_read_b64_tr_b16 v[74:75], v152 offset:64\n\t"
        "ds_read_b64_tr_b16 v[76:77], v157 offset:128\n\t"
        "ds_read_b64_tr_b16 v[78:79], v158 offset:128\n\t"
        "ds_read_b64_tr_b16 v[80:81], v159 offset:128\n\t"
        "ds_read_b64_tr_b16 v[82:83], v152 offset:128\n\t"
        "v_perm_b32 v164, v163, v163, v168\n\t"
        "v_perm_b32 v165, v163, v163, v169\n\t"
        "v_perm_b32 v166, v163, v163, v170\n\t"
        "v_perm_b32 v167, v163, v163, v171\n\t"
        "s_waitcnt lgkmcnt(10)\n\t"
        "s_nop 0\n\t"
        "v_mfma_f32_16x16x32_bf16 v[48:51], v[60:63], v[164:167], v[48:51]\n\t"
        "s_waitcnt lgkmcnt(8)\n\t"
        "v_mfma_f32_16x16x32_bf16 v[52:55], v[64:67], v[164:167], v[52:55]\n\t"
        "s_waitcnt lgkmcnt(6)\n\t"
        "v_mfma_f32_16x16x32_bf16 v[56:59], v[68:71], v[164:167], v[56:59]\n\t"
        "s_waitcnt lgkmcnt(4)\n\t"
        "v_mfma_f32_16x16x32_bf16 v[116:119], v[72:75], v[164:167], v[116:119]\n\t"
        "s_waitcnt lgkmcnt(2)\n\t"
        "v_mfma_f32_16x16x32_bf16 v[120:123], v[76:79], v[164:167], v[120:123]\n\t"
        "s_waitcnt lgkmcnt(0)\n\t"
        "v_mfma_f32_16x16x32_bf16 v[124:127], v[80:83], v[164:167], v[124:127]\n\t"
        "s_nop 4\n\t"
        ".Lx56%=:\n\t"
        "v_cvt_pk_bf16_f32 v48, v48, v49\n\t"
        "v_cvt_pk_bf16_f32 v49, v50, v51\n\t"
        "v_mfma_f32_16x16x32_bf16 v[24:27], a[240:243], v[112:115], v[24:27]\n\t"
        "ds_write_b64 v186, v[48:49] offset:0\n\t"
        "v_mfma_f32_16x16x32_bf16 v[28:31], a[244:247], v[112:115], v[28:31]\n\t"
        "ds_read_b128 v[96:99], v128 offset:20544\n\t"
        "v_cvt_pk_bf16_f32 v52, v52, v53\n\t"
        "v_cvt_pk_bf16_f32 v53, v54, v55\n\t"
        "v_mfma_f32_16x16x32_bf16 v[32:35], a[248:251], v[112:115], v[32:35]\n\t"
        "ds_write_b64 v154, v[52:53] offset:0\n\t"
        "v_mfma_f32_16x16x32_bf16 v[36:39], a[216:219], v[112:115], v[36:39]\n\t"
        "ds_read_b128 v[100:103], v128 offset:20608\n\t"
        "v_cvt_pk_bf16_f32 v56, v56, v57\n\t"
        "v_cvt_pk_bf16_f32 v57, v58, v59\n\t"
        "v_mfma_f32_16x16x32_bf16 v[40:43], a[220:223], v[112:115], v[40:43]\n\t"
        "ds_write_b64 v186, v[56:57] offset:64\n\t"
        "v_mfma_f32_16x16x32_bf16 v[44:47], a[224:227], v[112:115], v[44:47]\n\t"
        "v_cvt_pk_bf16_f32 v116, v116, v117\n\t"
        "v_cvt_pk_bf16_f32 v117, v118, v119\n\t"
        "v_mfma_f32_16x16x32_bf16 v[24:27], v[208:211], v[84:87], v[24:27]\n\t"
        "ds_write_b64 v154, v[116:117] offset:64\n\t"
        "v_mfma_f32_16x16x32_bf16 v[28:31], v[212:215], v[84:87], v[28:31]\n\t"
        "ds_read_b128 v[104:107], v129 offset:20480\n\t"
        "v_cvt_pk_bf16_f32 v120, v120, v121\n\t"
        "v_mfma_f32_16x16x32_bf16 v[32:35], v[216:219], v[84:87], v[32:35]\n\t"
        "v_cvt_pk_bf16_f32 v121, v122, v123\n\t"
        "v_cvt_pk_bf16_f32 v124, v124, v125\n\t"
        "v_cvt_pk_bf16_f32 v125, v126, v127\n\t"
        "v_mfma_f32_16x16x32_bf16 v[36:39], a[252:255], v[84:87], v[36:39]\n\t"
        "ds_write_b64 v186, v[120:121] offset:128\n\t"
        "v_mfma_f32_16x16x32_bf16 v[40:43], v[188:191], v[84:87], v[40:43]\n\t"
        "ds_write_b64 v154, v[124:125] offset:128\n\t"
        "v_mfma_f32_16x16x32_bf16 v[44:47], v[192:195], v[84:87], v[44:47]\n\t"
        "ds_read_b128 v[108:111], v129 offset:20544\n\t"
        "s_waitcnt lgkmcnt(6)\n\t"
        "v_mfma_f32_16x16x32_bf16 v[24:27], v[244:247], v[88:91], v[24:27]\n\t"
        "ds_read_b128 v[112:115], v129 offset:20608\n\t"
        "v_mfma_f32_16x16x32_bf16 v[28:31], v[248:251], v[88:91], v[28:31]\n\t"
        "v_mfma_f32_16x16x32_bf16 v[32:35], v[252:255], v[88:91], v[32:35]\n\t"
        "v_mfma_f32_16x16x32_bf16 v[36:39], v[220:223], v[88:91], v[36:39]\n\t"
        "ds_read_b128 v[84:87], v130 offset:20480\n\t"
        "v_mfma_f32_16x16x32_bf16 v[40:43], v[224:227], v[88:91], v[40:43]\n\t"
        "s_waitcnt lgkmcnt(3)\n\t"
        "s_barrier\n\t"
        "v_mfma_f32_16x16x32_bf16 v[44:47], v[228:231], v[88:91], v[44:47]\n\t"
        "ds_read_b128 v[124:127], v182 offset:0\n\t"
        "v_mfma_f32_16x16x32_bf16 v[36:39], a[24:27], v[92:95], v[36:39]\n\t"
        "ds_read_b128 v[132:135], v187 offset:0\n\t"
        "v_mfma_f32_16x16x32_bf16 v[40:43], a[28:31], v[92:95], v[40:43]\n\t"
        "ds_read_b128 v[136:139], v187 offset:16\n\t"
        "v_mfma_f32_16x16x32_bf16 v[44:47], a[32:35], v[92:95], v[44:47]\n\t"
        "ds_read_b32 v140, v187 offset:32\n\t"
        "v_mfma_f32_16x16x32_bf16 v[36:39], a[60:63], v[96:99], v[36:39]\n\t"
        "ds_read_b128 v[88:91], v130 offset:20544\n\t"
        "v_mfma_f32_16x16x32_bf16 v[40:43], a[64:67], v[96:99], v[40:43]\n\t"
        "ds_read_b128 v[92:95], v130 offset:20608\n\t"
        "v_mfma_f32_16x16x32_bf16 v[44:47], a[68:71], v[96:99], v[44:47]\n\t"
        "v_mfma_f32_16x16x32_bf16 v[36:39], a[96:99], v[100:103], v[36:39]\n\t"
        "v_mfma_f32_16x16x32_bf16 v[40:43], a[100:103], v[100:103], v[40:43]\n\t"
        "v_mfma_f32_16x16x32_bf16 v[44:47], a[104:107], v[100:103], v[44:47]\n\t"
        "s_waitcnt lgkmcnt(5)\n\t"
        "v_or3_b32 v124, v124, v125, v126\n\t"
        "v_or_b32 v124, v124, v127\n\t"
        "v_mfma_f32_16x16x32_bf16 v[36:39], a[132:135], v[104:107], v[36:39]\n\t"
        "v_readfirstlane_b32 s6, v124\n\t"
        "v_mfma_f32_16x16x32_bf16 v[40:43], a[136:139], v[104:107], v[40:43]\n\t"
        "s_nop 0\n\t"
        "s_bitcmp1_b32 s6, 1\n\t"
        "s_cbranch_scc1 .Lx58%=\n\t"
        ".Lx59%=:\n\t"
        "v_mfma_f32_16x16x32_bf16 v[44:47], a[140:143], v[104:107], v[44:47]\n\t"
        "s_waitcnt lgkmcnt(3)\n\t"
        "v_xor_b32 v132, v181, v132\n\t"
        "v_add_u32 v151, s9, v172\n\t"
        "v_mfma_f32_16x16x32_bf16 v[36:39], a[168:171], v[108:111], v[36:39]\n\t"
        "v_max_u32 v151, v151, v132\n\t"
        "v_xor_b32 v133, v181, v133\n\t"
        "v_mfma_f32_16x16x32_bf16 v[40:43], a[172:175], v[108:111], v[40:43]\n\t"
        "ds_read_b128 v[96:99], v151 offset:0\n\t"
        "v_add_u32 v183, s9, v173\n\t"
        "v_mfma_f32_16x16x32_bf16 v[44:47], a[176:179], v[108:111], v[44:47]\n\t"
        "ds_read_b128 v[100:103], v151 offset:64\n\t"
        "v_max_u32 v183, v183, v133\n\t"
        "v_mfma_f32_16x16x32_bf16 v[36:39], a[204:207], v[112:115], v[36:39]\n\t"
        "ds_read_b128 v[104:107], v151 offset:128\n\t"
        "v_xor_b32 v134, v181, v134\n\t"
        "v_mfma_f32_16x16x32_bf16 v[40:43], a[208:211], v[112:115], v[40:43]\n\t"
        "ds_read_b128 v[108:111], v183 offset:0\n\t"
        "v_add_u32 v151, s9, v174\n\t"
        "v_mfma_f32_16x16x32_bf16 v[44:47], a[212:215], v[112:115], v[44:47]\n\t"
        "v_max_u32 v151, v151, v134\n\t"
        "v_xor_b32 v135, v181, v135\n\t"
        "v_mfma_f32_16x16x32_bf16 v[36:39], a[240:243], v[84:87], v[36:39]\n\t"
        "ds_read_b128 v[112:115], v183 offset:64\n\t"
        "s_add_u32 s55, s41, 0\n\t"
        "v_mfma_f32_16x16x32_bf16 v[40:43], a[244:247], v[84:87], v[40:43]\n\t"
        "v_cvt_pk_bf16_f32 v116, v0, v1\n\t"
        "v_cvt_pk_bf16_f32 v117, v2, v3\n\t"
        "v_mfma_f32_16x16x32_bf16 v[44:47], a[248:251], v[84:87], v[44:47]\n\t"
        "v_cvt_pk_bf16_f32 v118, v4, v5\n\t"
        "v_cvt_pk_bf16_f32 v119, v6, v7\n\t"
        "s_waitcnt lgkmcnt(5)\n\t"
        "v_mfma_f32_16x16x32_bf16 v[36:39], v[208:211], v[88:91], v[36:39]\n\t"
        "ds_read_b128 v[84:87], v183 offset:128\n\t"
        "v_add_u32 v183, s9, v175\n\t"
        "v_mfma_f32_16x16x32_bf16 v[40:43], v[212:215], v[88:91], v[40:43]\n\t"
        "v_max_u32 v183, v183, v135\n\t"
        "v_xor_b32 v136, v181, v136\n\t"
        "v_mfma_f32_16x16x32_bf16 v[44:47], v[216:219], v[88:91], v[44:47]\n\t"
        "ds_read_b128 v[132:135], v187 offset:2048\n\t"
        "v_cvt_pk_bf16_f32 v120, v8, v9\n\t"
        "v_mfma_f32_16x16x32_bf16 v[36:39], v[244:247], v[92:95], v[36:39]\n\t"
        "ds_read_b128 v[88:91], v151 offset:0\n\t"
        "v_cvt_pk_bf16_f32 v121, v10, v11\n\t"
        "v_mfma_f32_16x16x32_bf16 v[40:43], v[248:251], v[92:95], v[40:43]\n\t"
        "v_permlane16_swap_b32 v116, v118\n\t"
        "v_mfma_f32_16x16x32_bf16 v[44:47], v[252:255], v[92:95], v[44:47]\n\t"
        "v_permlane16_swap_b32 v117, v119\n\t"
        "s_waitcnt lgkmcnt(4)\n\t"
        "v_mfma_f32_16x16x32_bf16 v[48:51], a[12:15], v[96:99], 0\n\t"
        "buffer_store_dwordx4 v[116:119], v144, s[28:31], s55 offen sc1\n\t"
        "v_mfma_f32_16x16x32_bf16 v[52:55], a[16:19], v[96:99], 0\n\t"
        "ds_write_b64 v146, v[120:121] offset:0\n\t"
        "v_mfma_f32_16x16x32_bf16 v[56:59], a[20:23], v[96:99], 0\n\t"
        "ds_read_b128 v[92:95], v151 offset:64\n\t"
        "s_add_u32 s55, s41, 43008\n\t"
        "v_mfma_f32_16x16x32_bf16 v[48:51], a[48:51], v[100:103], v[48:51]\n\t"
        "ds_read_b128 v[96:99], v151 offset:128\n\t"
        "v_add_u32 v151, s9, v176\n\t"
        "v_mfma_f32_16x16x32_bf16 v[52:55], a[52:55], v[100:103], v[52:55]\n\t"
        "v_max_u32 v151, v151, v136\n\t"
        "v_xor_b32 v137, v181, v137\n\t"
        "v_mfma_f32_16x16x32_bf16 v[56:59], a[56:59], v[100:103], v[56:59]\n\t"
        "v_cvt_pk_bf16_f32 v116, v12, v13\n\t"
        "v_cvt_pk_bf16_f32 v117, v14, v15\n\t"
        "v_mfma_f32_16x16x32_bf16 v[48:51], a[84:87], v[104:107], v[48:51]\n\t"
        "ds_read_b128 v[100:103], v183 offset:0\n\t"
        "v_cvt_pk_bf16_f32 v118, v16, v17\n\t"
        "v_mfma_f32_16x16x32_bf16 v[52:55], a[88:91], v[104:107], v[52:55]\n\t"
        "v_cvt_pk_bf16_f32 v119, v18, v19\n\t"
        "v_cvt_pk_bf16_f32 v120, v20, v21\n\t"
        "v_mfma_f32_16x16x32_bf16 v[56:59], a[92:95], v[104:107], v[56:59]\n\t"
        "v_cvt_pk_bf16_f32 v121, v22, v23\n\t"
        "v_mfma_f32_16x16x32_bf16 v[48:51], a[120:123], v[108:111], v[48:51]\n\t"
        "v_permlane16_swap_b32 v116, v118\n\t"
        "v_mfma_f32_16x16x32_bf16 v[52:55], a[124:127], v[108:111], v[52:55]\n\t"
        "v_permlane16_swap_b32 v117, v119\n\t"
        "v_mfma_f32_16x16x32_bf16 v[56:59], a[128:131], v[108:111], v[56:59]\n\t"
        "ds_read_b128 v[104:107], v183 offset:64\n\t"
        "s_waitcnt lgkmcnt(4)\n\t"
        "v_mfma_f32_16x16x32_bf16 v[48:51], a[156:159], v[112:115], v[48:51]\n\t"
        "ds_read_b128 v[108:111], v183 offset:128\n\t"
        "v_add_u32 v183, s9, v177\n\t"
        "v_mfma_f32_16x16x32_bf16 v[52:55], a[160:163], v[112:115], v[52:55]\n\t"
        "v_max_u32 v183, v183, v137\n\t"
        "v_xor_b32 v138, v181, v138\n\t"
        "v_mfma_f32_16x16x32_bf16 v[56:59], a[164:167], v[112:115], v[56:59]\n\t"
        "buffer_store_dwordx4 v[116:119], v144, s[28:31], s55 offen sc1\n\t"
        "v_mfma_f32_16x16x32_bf16 v[48:51], a[192:195], v[84:87], v[48:51]\n\t"
        "ds_write_b64 v146, v[120:121] offset:2048\n\t"
        "v_mfma_f32_16x16x32_bf16 v[52:55], a[196:199], v[84:87], v[52:55]\n\t"
        "ds_read_b128 v[112:115], v151 offset:0\n\t"
        "s_add_u32 s55, s41, 86016\n\t"
        "v_mfma_f32_16x16x32_bf16 v[56:59], a[200:203], v[84:87], v[56:59]\n\t"
        "v_cvt_pk_bf16_f32 v116, v24, v25\n\t"
        "v_cvt_pk_bf16_f32 v117, v26, v27\n\t"
        "v_mfma_f32_16x16x32_bf16 v[48:51], a[228:231], v[88:91], v[48:51]\n\t"
        "ds_read_b128 v[84:87], v151 offset:64\n\t"
        "v_cvt_pk_bf16_f32 v118, v28, v29\n\t"
        "v_mfma_f32_16x16x32_bf16 v[52:55], a[232:235], v[88:91], v[52:55]\n\t"
        "v_cvt_pk_bf16_f32 v119, v30, v31\n\t"
        "v_cvt_pk_bf16_f32 v120, v32, v33\n\t"
        "v_mfma_f32_16x16x32_bf16 v[56:59], a[236:239], v[88:91], v[56:59]\n\t"
        "v_cvt_pk_bf16_f32 v121, v34, v35\n\t"
        "s_waitcnt lgkmcnt(4)\n\t"
        "v_mfma_f32_16x16x32_bf16 v[48:51], v[196:199], v[92:95], v[48:51]\n\t"
        "v_permlane16_swap_b32 v116, v118\n\t"
        "v_mfma_f32_16x16x32_bf16 v[52:55], v[200:203], v[92:95], v[52:55]\n\t"
        "v_permlane16_swap_b32 v117, v119\n\t"
        "v_mfma_f32_16x16x32_bf16 v[56:59], v[204:207], v[92:95], v[56:59]\n\t"
        "ds_read_b128 v[88:91], v151 offset:128\n\t"
        "v_add_u32 v151, s9, v178\n\t"
        "v_mfma_f32_16x16x32_bf16 v[48:51], v[232:235], v[96:99], v[48:51]\n\t"
        "ds_read_b128 v[92:95], v183 offset:0\n\t"
        "v_max_u32 v151, v151, v138\n\t"
        "v_mfma_f32_16x16x32_bf16 v[52:55], v[236:239], v[96:99], v[52:55]\n\t"
        "v_xor_b32 v139, v181, v139\n\t"
        "v_mfma_f32_16x16x32_bf16 v[56:59], v[240:243], v[96:99], v[56:59]\n\t"
        "v_mfma_f32_16x16x32_bf16 v[48:51], a[0:3], v[100:103], v[48:51]\n\t"
        "ds_read_b128 v[96:99], v183 offset:64\n\t"
        "v_mfma_f32_16x16x32_bf16 v[52:55], a[4:7], v[100:103], v[52:55]\n\t"
        "buffer_store_dwordx4 v[116:119], v144, s[28:31], s55 offen sc1\n\t"
        "v_mfma_f32_16x16x32_bf16 v[56:59], a[8:11], v[100:103], v[56:59]\n\t"
        "ds_write_b64 v146, v[120:121] offset:4096\n\t"
        "v_mfma_f32_16x16x32_bf16 v[48:51], a[36:39], v[104:107], v[48:51]\n\t"
        "ds_read_b128 v[100:103], v183 offset:128\n\t"
        "v_add_u32 v183, s9, v179\n\t"
        "v_mfma_f32_16x16x32_bf16 v[52:55], a[40:43], v[104:107], v[52:55]\n\t"
        "v_max_u32 v183, v183, v139\n\t"
        "v_xor_b32 v140, v181, v140\n\t"
        "v_mfma_f32_16x16x32_bf16 v[56:59], a[44:47], v[104:107], v[56:59]\n\t"
        "ds_read_b128 v[136:139], v187 offset:2064\n\t"
        "s_add_u32 s55, s41, 129024\n\t"
        "s_waitcnt lgkmcnt(5)\n\t"
        "v_mfma_f32_16x16x32_bf16 v[48:51], a[72:75], v[108:111], v[48:51]\n\t"
        "ds_read_b128 v[104:107], v151 offset:0\n\t"
        "v_cvt_pk_bf16_f32 v116, v36, v37\n\t"
        "v_mfma_f32_16x16x32_bf16 v[52:55], a[76:79], v[108:111], v[52:55]\n\t"
        "v_cvt_pk_bf16_f32 v117, v38, v39\n\t"
        "v_cvt_pk_bf16_f32 v118, v40, v41\n\t"
        "v_mfma_f32_16x16x32_bf16 v[56:59], a[80:83], v[108:111], v[56:59]\n\t"
        "v_cvt_pk_bf16_f32 v119, v42, v43\n\t"
        "v_cvt_pk_bf16_f32 v120, v44, v45\n\t"
        "v_mfma_f32_16x16x32_bf16 v[48:51], a[108:111], v[112:115], v[48:51]\n\t"
        "ds_read_b128 v[108:111], v151 offset:64\n\t"
        "v_cvt_pk_bf16_f32 v121, v46, v47\n\t"
        "v_mfma_f32_16x16x32_bf16 v[52:55], a[112:115], v[112:115], v[52:55]\n\t"
        "v_permlane16_swap_b32 v116, v118\n\t"
        "v_mfma_f32_16x16x32_bf16 v[56:59], a[116:119], v[112:115], v[56:59]\n\t"
        "v_permlane16_swap_b32 v117, v119\n\t"
        "v_mfma_f32_16x16x32_bf16 v[48:51], a[144:147], v[84:87], v[48:51]\n\t"
        "ds_read_b128 v[112:115], v151 offset:128\n\t"
        "v_add_u32 v151, s9, v180\n\t"
        "v_mfma_f32_16x16x32_bf16 v[52:55], a[148:151], v[84:87], v[52:55]\n\t"
        "v_max_u32 v151, v151, v140\n\t"
        "v_xor_b32 v132, v181, v132\n\t"
        "v_mfma_f32_16x16x32_bf16 v[56:59], a[152:155], v[84:87], v[56:59]\n\t"
        "ds_read_b32 v140, v187 offset:2080\n\t"
        "v_mfma_f32_16x16x32_bf16 v[48:51], a[180:183], v[88:91], v[48:51]\n\t"
        "buffer_store_dwordx4 v[116:119], v144, s[28:31], s55 offen sc1\n\t"
        "v_mfma_f32_16x16x32_bf16 v[52:55], a[184:187], v[88:91], v[52:55]\n\t"
        "ds_write_b64 v146, v[120:121] offset:6144\n\t"
        "v_mfma_f32_16x16x32_bf16 v[56:59], a[188:191], v[88:91], v[56:59]\n\t"
        "ds_read_b128 v[84:87], v183 offset:0\n\t"
        "s_waitcnt lgkmcnt(5)\n\t"
        "v_mfma_f32_16x16x32_bf16 v[48:51], a[216:219], v[92:95], v[48:51]\n\t"
        "ds_read_b128 v[88:91], v183 offset:64\n\t"
        "v_mfma_f32_16x16x32_bf16 v[52:55], a[220:223], v[92:95], v[52:55]\n\t"
        "v_mfma_f32_16x16x32_bf16 v[56:59], a[224:227], v[92:95], v[56:59]\n\t"
        "v_mfma_f32_16x16x32_bf16 v[48:51], a[252:255], v[96:99], v[48:51]\n\t"
        "ds_read_b128 v[92:95], v183 offset:128\n\t"
        "v_readlane_b32 s9, v155, 47\n\t"
        "v_mfma_f32_16x16x32_bf16 v[52:55], v[188:191], v[96:99], v[52:55]\n\t"
        "s_waitcnt lgkmcnt(3)\n\t"
        "s_barrier\n\t"
        "v_mfma_f32_16x16x32_bf16 v[56:59], v[192:195], v[96:99], v[56:59]\n\t"
        "ds_read_b128 v[124:127], v147 offset:0\n\t"
        "v_add_u32 v183, s9, v172\n\t"
        "v_mfma_f32_16x16x32_bf16 v[48:51], v[220:223], v[100:103], v[48:51]\n\t"
        "ds_read_b128 v[96:99], v151 offset:0\n\t"
        "v_max_u32 v183, v183, v132\n\t"
        "v_mfma_f32_16x16x32_bf16 v[52:55], v[224:227], v[100:103], v[52:55]\n\t"
        "v_xor_b32 v133, v181, v133\n\t"
        "v_mfma_f32_16x16x32_bf16 v[56:59], v[228:231], v[100:103], v[56:59]\n\t"
        "v_mfma_f32_16x16x32_bf16 v[48:51], a[24:27], v[104:107], v[48:51]\n\t"
        "ds_read_b128 v[100:103], v151 offset:64\n\t"
        "v_mfma_f32_16x16x32_bf16 v[52:55], a[28:31], v[104:107], v[52:55]\n\t"
        "v_mfma_f32_16x16x32_bf16 v[56:59], a[32:35], v[104:107], v[56:59]\n\t"
        "v_mfma_f32_16x16x32_bf16 v[48:51], a[60:63], v[108:111], v[48:51]\n\t"
        "ds_read_b128 v[104:107], v151 offset:128\n\t"
        "v_add_u32 v151, s9, v173\n\t"
        "v_mfma_f32_16x16x32_bf16 v[52:55], a[64:67], v[108:111], v[52:55]\n\t"
        "v_max_u32 v151, v151, v133\n\t"
        "v_xor_b32 v134, v181, v134\n\t"
        "v_mfma_f32_16x16x32_bf16 v[56:59], a[68:71], v[108:111], v[56:59]\n\t"
        "s_waitcnt lgkmcnt(3)\n\t"
        "buffer_store_dwordx4 v[124:127], v145, s[28:31], s50 offen sc1\n\t"
        "v_mfma_f32_16x16x32_bf16 v[48:51], a[96:99], v[112:115], v[48:51]\n\t"
        "ds_read_b128 v[124:127], v147 offset:4096\n\t"
        "s_add_u32 s50, s50, 86016\n\t"
        "v_mfma_f32_16x16x32_bf16 v[52:55], a[100:103], v[112:115], v[52:55]\n\t"
        "ds_read_b128 v[108:111], v183 offset:0\n\t"
        "v_mfma_f32_16x16x32_bf16 v[56:59], a[104:107], v[112:115], v[56:59]\n\t"
        "v_mfma_f32_16x16x32_bf16 v[48:51], a[132:135], v[84:87], v[48:51]\n\t"
        "ds_read_b128 v[112:115], v183 offset:64\n\t"
        "v_mfma_f32_16x16x32_bf16 v[52:55], a[136:139], v[84:87], v[52:55]\n\t"
        "v_mfma_f32_16x16x32_bf16 v[56:59], a[140:143], v[84:87], v[56:59]\n\t"
        "v_mfma_f32_16x16x32_bf16 v[48:51], a[168:171], v[88:91], v[48:51]\n\t"
        "ds_read_b128 v[84:87], v183 offset:128\n\t"
        "v_add_u32 v183, s9, v174\n\t"
        "v_mfma_f32_16x16x32_bf16 v[52:55], a[172:175], v[88:91], v[52:55]\n\t"
        "v_max_u32 v183, v183, v134\n\t"
        "v_xor_b32 v135, v181, v135\n\t"
        "v_mfma_f32_16x16x32_bf16 v[56:59], a[176:179], v[88:91], v[56:59]\n\t"
        "v_mfma_f32_16x16x32_bf16 v[48:51], a[204:207], v[92:95], v[48:51]\n\t"
        "ds_read_b128 v[88:91], v151 offset:0\n\t"
        "v_mfma_f32_16x16x32_bf16 v[52:55], a[208:211], v[92:95], v[52:55]\n\t"
        "v_mfma_f32_16x16x32_bf16 v[56:59], a[212:215], v[92:95], v[56:59]\n\t"
        "s_waitcnt lgkmcnt(3)\n\t"
        "v_mfma_f32_16x16x32_bf16 v[48:51], a[240:243], v[96:99], v[48:51]\n\t"
        "ds_read_b128 v[92:95], v151 offset:64\n\t"
        "v_mfma_f32_16x16x32_bf16 v[52:55], a[244:247], v[96:99], v[52:55]\n\t"
        "buffer_store_dwordx4 v[124:127], v145, s[28:31], s50 offen sc1\n\t"
        "v_mfma_f32_16x16x32_bf16 v[56:59], a[248:251], v[96:99], v[56:59]\n\t"
        "v_mfma_f32_16x16x32_bf16 v[48:51], v[208:211], v[100:103], v[48:51]\n\t"
        "ds_read_b128 v[96:99], v151 offset:128\n\t"
        "v_add_u32 v151, s9, v175\n\t"
        "v_mfma_f32_16x16x32_bf16 v[52:55], v[212:215], v[100:103], v[52:55]\n\t"
        "v_max_u32 v151, v151, v135\n\t"
        "v_xor_b32 v136, v181, v136\n\t"
        "v_mfma_f32_16x16x32_bf16 v[56:59], v[216:219], v[100:103], v[56:59]\n\t"
        "ds_read_b128 v[132:135], v187 offset:4096\n\t"
        "v_mfma_f32_16x16x32_bf16 v[48:51], v[244:247], v[104:107], v[48:51]\n\t"
        "ds_read_b128 v[100:103], v183 offset:0\n\t"
        "v_mfma_f32_16x16x32_bf16 v[52:55], v[248:251], v[104:107], v[52:55]\n\t"
        "v_mfma_f32_16x16x32_bf16 v[56:59], v[252:255], v[104:107], v[56:59]\n\t"
        "v_mfma_f32_16x16x32_bf16 v[60:63], a[12:15], v[108:111], 0\n\t"
        "ds_read_b128 v[104:107], v183 offset:64\n\t"
        "v_mfma_f32_16x16x32_bf16 v[64:67], a[16:19], v[108:111], 0\n\t"
        "v_mfma_f32_16x16x32_bf16 v[68:71], a[20:23], v[108:111], 0\n\t"
        "s_waitcnt lgkmcnt(4)\n\t"
        "v_mfma_f32_16x16x32_bf16 v[60:63], a[48:51], v[112:115], v[60:63]\n\t"
        "ds_read_b128 v[108:111], v183 offset:128\n\t"
        "v_add_u32 v183, s9, v176\n\t"
        "v_mfma_f32_16x16x32_bf16 v[64:67], a[52:55], v[112:115], v[64:67]\n\t"
        "v_max_u32 v183, v183, v136\n\t"
        "v_xor_b32 v137, v181, v137\n\t"
        "v_mfma_f32_16x16x32_bf16 v[68:71], a[56:59], v[112:115], v[68:71]\n\t"
        "v_readlane_b32 s55, v155, 49\n\t"
        "v_cvt_pk_bf16_f32 v116, v48, v49\n\t"
        "v_mfma_f32_16x16x32_bf16 v[60:63], a[84:87], v[84:87], v[60:63]\n\t"
        "ds_read_b128 v[112:115], v151 offset:0\n\t"
        "v_cvt_pk_bf16_f32 v117, v50, v51\n\t"
        "v_mfma_f32_16x16x32_bf16 v[64:67], a[88:91], v[84:87], v[64:67]\n\t"
        "v_cvt_pk_bf16_f32 v118, v52, v53\n\t"
        "v_cvt_pk_bf16_f32 v119, v54, v55\n\t"
        "v_mfma_f32_16x16x32_bf16 v[68:71], a[92:95], v[84:87], v[68:71]\n\t"
        "v_cvt_pk_bf16_f32 v120, v56, v57\n\t"
        "v_cvt_pk_bf16_f32 v121, v58, v59\n\t"
        "v_mfma_f32_16x16x32_bf16 v[60:63], a[120:123], v[88:91], v[60:63]\n\t"
        "v_permlane16_swap_b32 v116, v118\n\t"
        "v_mfma_f32_16x16x32_bf16 v[64:67], a[124:127], v[88:91], v[64:67]\n\t"
        "v_permlane16_swap_b32 v117, v119\n\t"
        "v_mfma_f32_16x16x32_bf16 v[68:71], a[128:131], v[88:91], v[68:71]\n\t"
        "buffer_store_dwordx4 v[116:119], v148, s[28:31], s55 offen sc1\n\t"
        "v_mfma_f32_16x16x32_bf16 v[60:63], a[156:159], v[92:95], v[60:63]\n\t"
        "ds_write_b64 v146, v[120:121] offset:8192\n\t"
        "v_mfma_f32_16x16x32_bf16 v[64:67], a[160:163], v[92:95], v[64:67]\n\t"
        "ds_read_b128 v[84:87], v151 offset:64\n\t"
        "v_mfma_f32_16x16x32_bf16 v[68:71], a[164:167], v[92:95], v[68:71]\n\t"
        "ds_read_b128 v[88:91], v151 offset:128\n\t"
        "v_add_u32 v151, s9, v177\n\t"
        "s_waitcnt lgkmcnt(4)\n\t"
        "v_mfma_f32_16x16x32_bf16 v[60:63], a[192:195], v[96:99], v[60:63]\n\t"
        "ds_read_b128 v[92:95], v183 offset:0\n\t"
        "v_max_u32 v151, v151, v137\n\t"
        "v_mfma_f32_16x16x32_bf16 v[64:67], a[196:199], v[96:99], v[64:67]\n\t"
        "v_xor_b32 v138, v181, v138\n\t"
        "v_mfma_f32_16x16x32_bf16 v[68:71], a[200:203], v[96:99], v[68:71]\n\t"
        "v_mfma_f32_16x16x32_bf16 v[60:63], a[228:231], v[100:103], v[60:63]\n\t"
        "s_waitcnt lgkmcnt(3)\n\t"
        "s_barrier\n\t"
        "v_mfma_f32_16x16x32_bf16 v[64:67], a[232:235], v[100:103], v[64:67]\n\t"
        "ds_read_b128 v[124:127], v147 offset:8192\n\t"
        "v_mfma_f32_16x16x32_bf16 v[68:71], a[236:239], v[100:103], v[68:71]\n\t"
        "ds_read_b128 v[96:99], v183 offset:64\n\t"
        "v_mfma_f32_16x16x32_bf16 v[60:63], v[196:199], v[104:107], v[60:63]\n\t"
        "ds_read_b128 v[100:103], v183 offset:128\n\t"
        "v_add_u32 v183, s9, v178\n\t"
        "v_mfma_f32_16x16x32_bf16 v[64:67], v[200:203], v[104:107], v[64:67]\n\t"
        "v_max_u32 v183, v183, v138\n\t"
        "v_xor_b32 v139, v181, v139\n\t"
        "v_mfma_f32_16x16x32_bf16 v[68:71], v[204:207], v[104:107], v[68:71]\n\t"
        "v_mfma_f32_16x16x32_bf16 v[60:63], v[232:235], v[108:111], v[60:63]\n\t"
        "ds_read_b128 v[104:107], v151 offset:0\n\t"
        "v_mfma_f32_16x16x32_bf16 v[64:67], v[236:239], v[108:111], v[64:67]\n\t"
        "v_mfma_f32_16x16x32_bf16 v[68:71], v[240:243], v[108:111], v[68:71]\n\t"
        "v_mfma_f32_16x16x32_bf16 v[60:63], a[0:3], v[112:115], v[60:63]\n\t"
        "ds_read_b128 v[108:111], v151 offset:64\n\t"
        "v_mfma_f32_16x16x32_bf16 v[64:67], a[4:7], v[112:115], v[64:67]\n\t"
        "s_waitcnt lgkmcnt(4)\n\t"
        "buffer_store_dwordx4 v[124:127], v150, s[28:31], s51 offen sc1\n\t"
        "v_mfma_f32_16x16x32_bf16 v[68:71], a[8:11], v[112:115], v[68:71]\n\t"
        "v_mfma_f32_16x16x32_bf16 v[60:63], a[36:39], v[84:87], v[60:63]\n\t"
        "ds_read_b128 v[112:115], v151 offset:128\n\t"
        "v_add_u32 v151, s9, v179\n\t"
        "v_mfma_f32_16x16x32_bf16 v[64:67], a[40:43], v[84:87], v[64:67]\n\t"
        "v_max_u32 v151, v151, v139\n\t"
        "v_xor_b32 v140, v181, v140\n\t"
        "v_mfma_f32_16x16x32_bf16 v[68:71], a[44:47], v[84:87], v[68:71]\n\t"
        "ds_read_b128 v[136:139], v187 offset:4112\n\t"
        "v_mfma_f32_16x16x32_bf16 v[60:63], a[72:75], v[88:91], v[60:63]\n\t"
        "ds_read_b128 v[84:87], v183 offset:0\n\t"
        "v_mfma_f32_16x16x32_bf16 v[64:67], a[76:79], v[88:91], v[64:67]\n\t"
        "v_mfma_f32_16x16x32_bf16 v[68:71], a[80:83], v[88:91], v[68:71]\n\t"
        "v_mfma_f32_16x16x32_bf16 v[60:63], a[108:111], v[92:95], v[60:63]\n\t"
        "ds_read_b128 v[88:91], v183 offset:64\n\t"
        "v_mfma_f32_16x16x32_bf16 v[64:67], a[112:115], v[92:95], v[64:67]\n\t"
        "v_mfma_f32_16x16x32_bf16 v[68:71], a[116:119], v[92:95], v[68:71]\n\t"
        "s_waitcnt lgkmcnt(4)\n\t"
        "v_mfma_f32_16x16x32_bf16 v[60:63], a[144:147], v[96:99], v[60:63]\n\t"
        "ds_read_b128 v[92:95], v183 offset:128\n\t"
        "v_add_u32 v183, s9, v180\n\t"
        "v_mfma_f32_16x16x32_bf16 v[64:67], a[148:151], v[96:99], v[64:67]\n\t"
        "v_max_u32 v183, v183, v140\n\t"
        "v_xor_b32 v132, v181, v132\n\t"
        "v_mfma_f32_16x16x32_bf16 v[68:71], a[152:155], v[96:99], v[68:71]\n\t"
        "ds_read_b32 v140, v187 offset:4128\n\t"
        "v_mfma_f32_16x16x32_bf16 v[60:63], a[180:183], v[100:103], v[60:63]\n\t"
        "ds_read_b128 v[96:99], v151 offset:0\n\t"
        "v_mfma_f32_16x16x32_bf16 v[64:67], a[184:187], v[100:103], v[64:67]\n\t"
        "v_mfma_f32_16x16x32_bf16 v[68:71], a[188:191], v[100:103], v[68:71]\n\t"
        "v_mfma_f32_16x16x32_bf16 v[60:63], a[216:219], v[104:107], v[60:63]\n\t"
        "ds_read_b128 v[100:103], v151 offset:64\n\t"
        "v_mfma_f32_16x16x32_bf16 v[64:67], a[220:223], v[104:107], v[64:67]\n\t"
        "v_mfma_f32_16x16x32_bf16 v[68:71], a[224:227], v[104:107], v[68:71]\n\t"
        "v_mfma_f32_16x16x32_bf16 v[60:63], a[252:255], v[108:111], v[60:63]\n\t"
        "ds_read_b128 v[104:107], v151 offset:128\n\t"
        "v_readlane_b32 s9, v155, 48\n\t"
        "v_mfma_f32_16x16x32_bf16 v[64:67], v[188:191], v[108:111], v[64:67]\n\t"
        "s_nop 0\n\t"
        "v_add_u32 v151, s9, v172\n\t"
        "v_max_u32 v151, v151, v132\n\t"
        "v_mfma_f32_16x16x32_bf16 v[68:71], v[192:195], v[108:111], v[68:71]\n\t"
        "v_xor_b32 v133, v181, v133\n\t"
        "s_waitcnt lgkmcnt(4)\n\t"
        "v_mfma_f32_16x16x32_bf16 v[60:63], v[220:223], v[112:115], v[60:63]\n\t"
        "ds_read_b128 v[108:111], v183 offset:0\n\t"
        "v_mfma_f32_16x16x32_bf16 v[64:67], v[224:227], v[112:115], v[64:67]\n\t"
        "v_mfma_f32_16x16x32_bf16 v[68:71], v[228:231], v[112:115], v[68:71]\n\t"
        "v_mfma_f32_16x16x32_bf16 v[60:63], a[24:27], v[84:87], v[60:63]\n\t"
        "ds_read_b128 v[112:115], v183 offset:64\n\t"
        "v_mfma_f32_16x16x32_bf16 v[64:67], a[28:31], v[84:87], v[64:67]\n\t"
        "v_mfma_f32_16x16x32_bf16 v[68:71], a[32:35], v[84:87], v[68:71]\n\t"
        "v_mfma_f32_16x16x32_bf16 v[60:63], a[60:63], v[88:91], v[60:63]\n\t"
        "ds_read_b128 v[84:87], v183 offset:128\n\t"
        "v_add_u32 v183, s9, v173\n\t"
        "v_mfma_f32_16x16x32_bf16 v[64:67], a[64:67], v[88:91], v[64:67]\n\t"
        "v_max_u32 v183, v183, v133\n\t"
        "v_xor_b32 v134, v181, v134\n\t"
        "v_mfma_f32_16x16x32_bf16 v[68:71], a[68:71], v[88:91], v[68:71]\n\t"
        "v_mfma_f32_16x16x32_bf16 v[60:63], a[96:99], v[92:95], v[60:63]\n\t"
        "ds_read_b128 v[88:91], v151 offset:0\n\t"
        "v_mfma_f32_16x16x32_bf16 v[64:67], a[100:103], v[92:95], v[64:67]\n\t"
        "v_mfma_f32_16x16x32_bf16 v[68:71], a[104:107], v[92:95], v[68:71]\n\t"
        "s_waitcnt lgkmcnt(3)\n\t"
        "v_mfma_f32_16x16x32_bf16 v[60:63], a[132:135], v[96:99], v[60:63]\n\t"
        "ds_read_b128 v[92:95], v151 offset:64\n\t"
        "v_mfma_f32_16x16x32_bf16 v[64:67], a[136:139], v[96:99], v[64:67]\n\t"
        "v_mfma_f32_16x16x32_bf16 v[68:71], a[140:143], v[96:99], v[68:71]\n\t"
        "v_mfma_f32_16x16x32_bf16 v[60:63], a[168:171], v[100:103], v[60:63]\n\t"
        "ds_read_b128 v[96:99], v151 offset:128\n\t"
        "v_add_u32 v151, s9, v174\n\t"
        "v_mfma_f32_16x16x32_bf16 v[64:67], a[172:175], v[100:103], v[64:67]\n\t"
        "v_max_u32 v151, v151, v134\n\t"
        "v_xor_b32 v135, v181, v135\n\t"
        "v_mfma_f32_16x16x32_bf16 v[68:71], a[176:179], v[100:103], v[68:71]\n\t"
        "v_mfma_f32_16x16x32_bf16 v[60:63], a[204:207], v[104:107], v[60:63]\n\t"
        "ds_read_b128 v[100:103], v183 offset:0\n\t"
        "v_mfma_f32_16x16x32_bf16 v[64:67], a[208:211], v[104:107], v[64:67]\n\t"
        "v_mfma_f32_16x16x32_bf16 v[68:71], a[212:215], v[104:107], v[68:71]\n\t"
        "v_mfma_f32_16x16x32_bf16 v[60:63], a[240:243], v[108:111], v[60:63]\n\t"
        "ds_read_b128 v[104:107], v183 offset:64\n\t"
        "v_mfma_f32_16x16x32_bf16 v[64:67], a[244:247], v[108:111], v[64:67]\n\t"
        "v_mfma_f32_16x16x32_bf16 v[68:71], a[248:251], v[108:111], v[68:71]\n\t"
        "s_waitcnt lgkmcnt(3)\n\t"
        "v_mfma_f32_16x16x32_bf16 v[60:63], v[208:211], v[112:115], v[60:63]\n\t"
        "ds_read_b128 v[108:111], v183 offset:128\n\t"
        "v_add_u32 v183, s9, v175\n\t"
        "v_mfma_f32_16x16x32_bf16 v[64:67], v[212:215], v[112:115], v[64:67]\n\t"
        "v_max_u32 v183, v183, v135\n\t"
        "v_xor_b32 v136, v181, v136\n\t"
        "v_mfma_f32_16x16x32_bf16 v[68:71], v[216:219], v[112:115], v[68:71]\n\t"
        "v_mfma_f32_16x16x32_bf16 v[60:63], v[244:247], v[84:87], v[60:63]\n\t"
        "ds_read_b128 v[112:115], v151 offset:0\n\t"
        "v_mfma_f32_16x16x32_bf16 v[64:67], v[248:251], v[84:87], v[64:67]\n\t"
        "v_mfma_f32_16x16x32_bf16 v[68:71], v[252:255], v[84:87], v[68:71]\n\t"
        "v_mfma_f32_16x16x32_bf16 v[72:75], a[12:15], v[88:91], 0\n\t"
        "ds_read_b128 v[84:87], v151 offset:64\n\t"
        "v_mfma_f32_16x16x32_bf16 v[76:79], a[16:19], v[88:91], 0\n\t"
        "v_mfma_f32_16x16x32_bf16 v[80:83], a[20:23], v[88:91], 0\n\t"
        "v_mfma_f32_16x16x32_bf16 v[72:75], a[48:51], v[92:95], v[72:75]\n\t"
        "ds_read_b128 v[88:91], v151 offset:128\n\t"
        "v_add_u32 v151, s9, v176\n\t"
        "v_mfma_f32_16x16x32_bf16 v[76:79], a[52:55], v[92:95], v[76:79]\n\t"
        "v_max_u32 v151, v151, v136\n\t"
        "v_xor_b32 v137, v181, v137\n\t"
        "v_mfma_f32_16x16x32_bf16 v[80:83], a[56:59], v[92:95], v[80:83]\n\t"
        "v_readlane_b32 s55, v155, 50\n\t"
        "v_cvt_pk_bf16_f32 v116, v60, v61\n\t"
        "s_waitcnt lgkmcnt(3)\n\t"
        "v_mfma_f32_16x16x32_bf16 v[72:75], a[84:87], v[96:99], v[72:75]\n\t"
        "ds_read_b128 v[92:95], v183 offset:0\n\t"
        "v_cvt_pk_bf16_f32 v117, v62, v63\n\t"
        "v_mfma_f32_16x16x32_bf16 v[76:79], a[88:91], v[96:99], v[76:79]\n\t"
        "v_cvt_pk_bf16_f32 v118, v64, v65\n\t"
        "v_cvt_pk_bf16_f32 v119, v66, v67\n\t"
        "v_mfma_f32_16x16x32_bf16 v[80:83], a[92:95], v[96:99], v[80:83]\n\t"
        "v_cvt_pk_bf16_f32 v120, v68, v69\n\t"
        "v_cvt_pk_bf16_f32 v121, v70, v71\n\t"
        "v_mfma_f32_16x16x32_bf16 v[72:75], a[120:123], v[100:103], v[72:75]\n\t"
        "v_permlane16_swap_b32 v116, v118\n\t"
        "v_mfma_f32_16x16x32_bf16 v[76:79], a[124:127], v[100:103], v[76:79]\n\t"
        "v_permlane16_swap_b32 v117, v119\n\t"
        "v_mfma_f32_16x16x32_bf16 v[80:83], a[128:131], v[100:103], v[80:83]\n\t"
        "buffer_store_dwordx4 v[116:119], v148, s[28:31], s55 offen sc1\n\t"
        "v_mfma_f32_16x16x32_bf16 v[72:75], a[156:159], v[104:107], v[72:75]\n\t"
        "ds_write_b64 v146, v[120:121] offset:0\n\t"
        "v_mfma_f32_16x16x32_bf16 v[76:79], a[160:163], v[104:107], v[76:79]\n\t"
        "ds_read_b128 v[96:99], v183 offset:64\n\t"
        "v_mfma_f32_16x16x32_bf16 v[80:83], a[164:167], v[104:107], v[80:83]\n\t"
        "ds_read_b128 v[100:103], v183 offset:128\n\t"
        "v_add_u32 v183, s9, v177\n\t"
        "v_mfma_f32_16x16x32_bf16 v[72:75], a[192:195], v[108:111], v[72:75]\n\t"
        "ds_read_b128 v[104:107], v151 offset:0\n\t"
        "v_max_u32 v183, v183, v137\n\t"
        "v_mfma_f32_16x16x32_bf16 v[76:79], a[196:199], v[108:111], v[76:79]\n\t"
        "v_xor_b32 v138, v181, v138\n\t"
        "v_mfma_f32_16x16x32_bf16 v[80:83], a[200:203], v[108:111], v[80:83]\n\t"
        "s_waitcnt lgkmcnt(4)\n\t"
        "v_mfma_f32_16x16x32_bf16 v[72:75], a[228:231], v[112:115], v[72:75]\n\t"
        "ds_read_b128 v[108:111], v151 offset:64\n\t"
        "v_mfma_f32_16x16x32_bf16 v[76:79], a[232:235], v[112:115], v[76:79]\n\t"
        "v_mfma_f32_16x16x32_bf16 v[80:83], a[236:239], v[112:115], v[80:83]\n\t"
        "v_mfma_f32_16x16x32_bf16 v[72:75], v[196:199], v[84:87], v[72:75]\n\t"
        "ds_read_b128 v[112:115], v151 offset:128\n\t"
        "v_add_u32 v151, s9, v178\n\t"
        "v_mfma_f32_16x16x32_bf16 v[76:79], v[200:203], v[84:87], v[76:79]\n\t"
        "v_max_u32 v151, v151, v138\n\t"
        "v_xor_b32 v139, v181, v139\n\t"
        "v_mfma_f32_16x16x32_bf16 v[80:83], v[204:207], v[84:87], v[80:83]\n\t"
        "v_mfma_f32_16x16x32_bf16 v[72:75], v[232:235], v[88:91], v[72:75]\n\t"
        "ds_read_b128 v[84:87], v183 offset:0\n\t"
        "v_mfma_f32_16x16x32_bf16 v[76:79], v[236:239], v[88:91], v[76:79]\n\t"
        "v_mfma_f32_16x16x32_bf16 v[80:83], v[240:243], v[88:91], v[80:83]\n\t"
        "v_mfma_f32_16x16x32_bf16 v[72:75], a[0:3], v[92:95], v[72:75]\n\t"
        "ds_read_b128 v[88:91], v183 offset:64\n\t"
        "v_mfma_f32_16x16x32_bf16 v[76:79], a[4:7], v[92:95], v[76:79]\n\t"
        "v_mfma_f32_16x16x32_bf16 v[80:83], a[8:11], v[92:95], v[80:83]\n\t"
        "s_waitcnt lgkmcnt(3)\n\t"
        "v_mfma_f32_16x16x32_bf16 v[72:75], a[36:39], v[96:99], v[72:75]\n\t"
        "ds_read_b128 v[92:95], v183 offset:128\n\t"
        "v_add_u32 v183, s9, v179\n\t"
        "v_mfma_f32_16x16x32_bf16 v[76:79], a[40:43], v[96:99], v[76:79]\n\t"
        "v_max_u32 v183, v183, v139\n\t"
        "v_xor_b32 v140, v181, v140\n\t"
        "v_mfma_f32_16x16x32_bf16 v[80:83], a[44:47], v[96:99], v[80:83]\n\t"
        ".LpS2%=:\n\t"
        "s_cbranch_execz .LrS%=\n\t"
        "v_mfma_f32_16x16x32_bf16 v[72:75], a[72:75], v[100:103], v[72:75]\n\t"
        "ds_read_b128 v[96:99], v151 offset:0\n\t"
        "v_mfma_f32_16x16x32_bf16 v[76:79], a[76:79], v[100:103], v[76:79]\n\t"
        "v_mfma_f32_16x16x32_bf16 v[80:83], a[80:83], v[100:103], v[80:83]\n\t"
        "v_mfma_f32_16x16x32_bf16 v[72:75], a[108:111], v[104:107], v[72:75]\n\t"
        "ds_read_b128 v[100:103], v151 offset:64\n\t"
        "v_mfma_f32_16x16x32_bf16 v[76:79], a[112:115], v[104:107], v[76:79]\n\t"
        "v_mfma_f32_16x16x32_bf16 v[80:83], a[116:119], v[104:107], v[80:83]\n\t"
        "ds_read_b128 v[104:107], v151 offset:128\n\t"
        "v_add_u32 v151, s9, v180\n\t"
        "v_mfma_f32_16x16x32_bf16 v[72:75], a[144:147], v[108:111], v[72:75]\n\t"
        "v_max_u32 v151, v151, v140\n\t"
        "v_mfma_f32_16x16x32_bf16 v[76:79], a[148:151], v[108:111], v[76:79]\n\t"
        "v_mfma_f32_16x16x32_bf16 v[80:83], a[152:155], v[108:111], v[80:83]\n\t"
        "ds_read_b128 v[108:111], v183 offset:0\n\t"
        "s_waitcnt lgkmcnt(4)\n\t"
        "v_mfma_f32_16x16x32_bf16 v[72:75], a[180:183], v[112:115], v[72:75]\n\t"
        "v_mfma_f32_16x16x32_bf16 v[76:79], a[184:187], v[112:115], v[76:79]\n\t"
        "v_mfma_f32_16x16x32_bf16 v[80:83], a[188:191], v[112:115], v[80:83]\n\t"
        "ds_read_b128 v[112:115], v183 offset:64\n\t"
        "v_mfma_f32_16x16x32_bf16 v[72:75], a[216:219], v[84:87], v[72:75]\n\t"
        "v_mfma_f32_16x16x32_bf16 v[76:79], a[220:223], v[84:87], v[76:79]\n\t"
        "v_mfma_f32_16x16x32_bf16 v[80:83], a[224:227], v[84:87], v[80:83]\n\t"
        "ds_read_b128 v[84:87], v183 offset:128\n\t"
        "v_mfma_f32_16x16x32_bf16 v[72:75], a[252:255], v[88:91], v[72:75]\n\t"
        "v_mfma_f32_16x16x32_bf16 v[76:79], v[188:191], v[88:91], v[76:79]\n\t"
        "v_mfma_f32_16x16x32_bf16 v[80:83], v[192:195], v[88:91], v[80:83]\n\t"
        "ds_read_b128 v[88:91], v151 offset:0\n\t"
        "v_mfma_f32_16x16x32_bf16 v[72:75], v[220:223], v[92:95], v[72:75]\n\t"
        "v_mfma_f32_16x16x32_bf16 v[76:79], v[224:227], v[92:95], v[76:79]\n\t"
        "v_mfma_f32_16x16x32_bf16 v[80:83], v[228:231], v[92:95], v[80:83]\n\t"
        "ds_read_b128 v[92:95], v151 offset:64\n\t"
        "s_waitcnt lgkmcnt(4)\n\t"
        "v_mfma_f32_16x16x32_bf16 v[72:75], a[24:27], v[96:99], v[72:75]\n\t"
        "v_mfma_f32_16x16x32_bf16 v[76:79], a[28:31], v[96:99], v[76:79]\n\t"
        "v_mfma_f32_16x16x32_bf16 v[80:83], a[32:35], v[96:99], v[80:83]\n\t"
        "ds_read_b128 v[96:99], v151 offset:128\n\t"
        "v_mfma_f32_16x16x32_bf16 v[80:83], a[68:71], v[100:103], v[80:83]\n\t"
        "v_mfma_f32_16x16x32_bf16 v[80:83], a[104:107], v[104:107], v[80:83]\n\t"
        "v_mfma_f32_16x16x32_bf16 v[80:83], a[140:143], v[108:111], v[80:83]\n\t"
        "s_waitcnt lgkmcnt(3)\n\t"
        "v_mfma_f32_16x16x32_bf16 v[80:83], a[176:179], v[112:115], v[80:83]\n\t"
        "v_mfma_f32_16x16x32_bf16 v[80:83], a[212:215], v[84:87], v[80:83]\n\t"
        "s_waitcnt lgkmcnt(2)\n\t"
        "v_mfma_f32_16x16x32_bf16 v[80:83], a[248:251], v[88:91], v[80:83]\n\t"
        "s_waitcnt lgkmcnt(1)\n\t"
        "v_mfma_f32_16x16x32_bf16 v[80:83], v[216:219], v[92:95], v[80:83]\n\t"
        "s_waitcnt lgkmcnt(0)\n\t"
        "v_mfma_f32_16x16x32_bf16 v[80:83], v[252:255], v[96:99], v[80:83]\n\t"
        "v_mfma_f32_16x16x32_bf16 v[72:75], a[60:63], v[100:103], v[72:75]\n\t"
        "v_mfma_f32_16x16x32_bf16 v[76:79], a[64:67], v[100:103], v[76:79]\n\t"
        "s_nop 5\n\t"
        "v_cvt_pk_bf16_f32 v120, v80, v81\n\t"
        "v_cvt_pk_bf16_f32 v121, v82, v83\n\t"
        "v_mfma_f32_16x16x32_bf16 v[72:75], a[96:99], v[104:107], v[72:75]\n\t"
        "ds_write_b64 v146, v[120:121] offset:2048\n\t"
        "v_mfma_f32_16x16x32_bf16 v[76:79], a[100:103], v[104:107], v[76:79]\n\t"
        "v_mfma_f32_16x16x32_bf16 v[72:75], a[132:135], v[108:111], v[72:75]\n\t"
        "v_mfma_f32_16x16x32_bf16 v[76:79], a[136:139], v[108:111], v[76:79]\n\t"
        "s_waitcnt lgkmcnt(0)\n\t"
        "s_barrier\n\t"
        "v_mfma_f32_16x16x32_bf16 v[72:75], a[168:171], v[112:115], v[72:75]\n\t"
        "ds_read_b128 v[124:127], v147 offset:0\n\t"
        "v_readlane_b32 s55, v155, 51\n\t"
        "v_mfma_f32_16x16x32_bf16 v[76:79], a[172:175], v[112:115], v[76:79]\n\t"
        "v_mfma_f32_16x16x32_bf16 v[72:75], a[204:207], v[84:87], v[72:75]\n\t"
        "v_mfma_f32_16x16x32_bf16 v[76:79], a[208:211], v[84:87], v[76:79]\n\t"
        "v_mfma_f32_16x16x32_bf16 v[72:75], a[240:243], v[88:91], v[72:75]\n\t"
        "v_mfma_f32_16x16x32_bf16 v[76:79], a[244:247], v[88:91], v[76:79]\n\t"
        "v_mfma_f32_16x16x32_bf16 v[72:75], v[208:211], v[92:95], v[72:75]\n\t"
        "v_mfma_f32_16x16x32_bf16 v[76:79], v[212:215], v[92:95], v[76:79]\n\t"
        "v_mfma_f32_16x16x32_bf16 v[72:75], v[244:247], v[96:99], v[72:75]\n\t"
        "v_mfma_f32_16x16x32_bf16 v[76:79], v[248:251], v[96:99], v[76:79]\n\t"
        "s_nop 6\n\t"
        "v_cvt_pk_bf16_f32 v116, v72, v73\n\t"
        "v_cvt_pk_bf16_f32 v117, v74, v75\n\t"
        "v_cvt_pk_bf16_f32 v118, v76, v77\n\t"
        "v_cvt_pk_bf16_f32 v119, v78, v79\n\t"
        "s_nop 0\n\t"
        "v_permlane16_swap_b32 v116, v118\n\t"
        "v_permlane16_swap_b32 v117, v119\n\t"
        "buffer_store_dwordx4 v[116:119], v148, s[28:31], s55 offen  \n\t"
        "s_waitcnt lgkmcnt(0)\n\t"
        "buffer_store_dwordx4 v[124:127], v150, s[28:31], s52 offen  \n\t"
        "s_andn2_b32 s6, s6, 2\n\t"
        "s_cbranch_scc1 99f\n\t"
        "s_endpgm\n\t"
        "21:\n\t"
        "s_bitcmp1_b32 s7, 15\n\t"
        "s_cbranch_scc1 22f\n\t"
        "ds_bpermute_b32 v64, v65, v155 offset:172\n\t"
        "s_add_u32 m0, s37, -24576\n\t"
        "s_add_u32 s36, s36, 6144\n\t"
        "buffer_load_dwordx4 v140, s[24:27], s36 offen offset:0 lds\n\t"
        "ds_bpermute_b32 v78, v81, v155 offset:148\n\t"
        "ds_bpermute_b32 v79, v81, v155 offset:160\n\t"
        "v_readlane_b32 s71, v155, 26\n\t"
        "v_readlane_b32 s10, v155, 29\n\t"
        "s_nop 0\n\t"
        "v_mul_u32_u24 v73, s71, v66\n\t"
        "v_lshrrev_b32 v73, 11, v73\n\t"
        "v_lshlrev_b32 v66, 4, v66\n\t"
        "v_mad_u32_u24 v73, v73, s10, v66\n\t"
        "s_waitcnt lgkmcnt(2)\n\t"
        "v_add_u32 v64, v64, v73\n\t"
        "buffer_load_dwordx4 v141, s[24:27], s36 offen offset:1024 lds\n\t"
        "v_readlane_b32 s69, v155, 34\n\t"
        "v_readlane_b32 s70, v155, 35\n\t"
        "s_nop 0\n\t"
        "v_mad_u32_u24 v70, v52, s69, v70\n\t"
        "v_mad_u32_u24 v70, v158, s70, v70\n\t"
        "s_mul_i32 s71, s69, s68\n\t"
        "s_add_u32 s71, s71, 157728\n\t"
        "v_mad_u32_u24 v67, v82, s70, v67\n\t"
        "v_add_u32 v67, s71, v67\n\t"
        "v_readlane_b32 s11, v155, 30\n\t"
        "buffer_load_dwordx4 v142, s[24:27], s36 offen offset:2048 lds\n\t"
        "s_movk_i32 s10, 223\n\t"
        "v_mov_b32 v75, 0x7ff00000\n\t"
        "v_readlane_b32 s12, v155, 24\n\t"
        "v_readlane_b32 s13, v155, 25\n\t"
        "s_nop 0\n\t"
        "s_bfe_u32 s70, s12, 524288\n\t"
        "s_bfe_u32 s71, s13, 524288\n\t"
        "v_cmp_gt_u32 vcc, s71, v71\n\t"
        "v_add3_u32 v73, v71, s70, -2\n\t"
        "v_med3_i32 v73, v73, 0, s10\n\t"
        "s_add_u32 m0, s35, 4096\n\t"
        "s_add_u32 s43, s34, 43008\n\t"
        "buffer_load_dwordx4 v143, s[16:19], s43 offen  lds\n\t"
        "v_cndmask_b32 v74, v75, v72, vcc\n\t"
        "v_mad_u32_u24 v134, v73, s11, v74\n\t"
        "s_bfe_u32 s70, s12, 524296\n\t"
        "s_bfe_u32 s71, s13, 524296\n\t"
        "v_cmp_gt_u32 vcc, s71, v71\n\t"
        "v_add3_u32 v73, v71, s70, -2\n\t"
        "v_med3_i32 v73, v73, 0, s10\n\t"
        "v_cndmask_b32 v74, v75, v72, vcc\n\t"
        "v_mad_u32_u24 v135, v73, s11, v74\n\t"
        "s_add_u32 m0, s35, 20480\n\t"
        "s_add_u32 s43, s34, 215040\n\t"
        "buffer_load_dwordx4 v143, s[16:19], s43 offen  lds\n\t"
        "s_bfe_u32 s70, s12, 524304\n\t"
        "s_bfe_u32 s71, s13, 524304\n\t"
        "v_cmp_gt_u32 vcc, s71, v71\n\t"
        "v_add3_u32 v73, v71, s70, -2\n\t"
        "v_med3_i32 v73, v73, 0, s10\n\t"
        "v_cndmask_b32 v74, v75, v72, vcc\n\t"
        "v_mad_u32_u24 v136, v73, s11, v74\n\t"
        "s_bfe_u32 s70, s12, 524312\n\t"
        "s_bfe_u32 s71, s13, 524312\n\t"
        "v_cmp_gt_u32 vcc, s71, v71\n\t"
        "v_add3_u32 v73, v71, s70, -2\n\t"
        "v_med3_i32 v73, v73, 0, s10\n\t"
        "v_cndmask_b32 v74, v75, v72, vcc\n\t"
        "v_mad_u32_u24 v137, v73, s11, v74\n\t"
        "s_mov_b64 exec, 0\n\t"
        "s_cmp_eq_u32 s4, 0\n\t"
        "s_cbranch_scc1 .LpX0%=\n\t"
        "s_cmp_eq_u32 s4, 1\n\t"
        "s_cbranch_scc1 .LpX1%=\n\t"
        "s_cmp_eq_u32 s4, 2\n\t"
        "s_cbranch_scc1 .LpX2%=\n\t"
        "s_branch .LwX%=\n\t"
        ".LrX%=:\n\t"
        "s_mov_b64 exec, -1\n\t"
        "s_waitcnt vmcnt(9)\n\t"
        "s_barrier\n\t"
        "ds_read_b64_tr_b16 a[0:1], v138 offset:6144\n\t"
        "ds_read_b64_tr_b16 a[2:3], v138 offset:6912\n\t"
        "ds_read_b64_tr_b16 a[4:5], v139 offset:6144\n\t"
        "ds_read_b64_tr_b16 a[6:7], v139 offset:6912\n\t"
        "ds_read_b64_tr_b16 a[8:9], v149 offset:6144\n\t"
        "ds_read_b64_tr_b16 a[10:11], v149 offset:6912\n\t"
        "ds_read_b128 v[84:87], v128 offset:8192\n\t"
        "ds_read_b64_tr_b16 a[12:13], v138 offset:12288\n\t"
        "ds_read_b64_tr_b16 a[14:15], v138 offset:13056\n\t"
        "ds_read_b64_tr_b16 a[16:17], v139 offset:12288\n\t"
        "ds_read_b64_tr_b16 a[18:19], v139 offset:13056\n\t"
        "ds_read_b64_tr_b16 a[20:21], v149 offset:12288\n\t"
        "ds_read_b64_tr_b16 a[22:23], v149 offset:13056\n\t"
        "ds_read_b128 v[88:91], v128 offset:8192\n\t"
        "s_add_u32 m0, s37, -12288\n\t"
        "s_add_u32 s36, s36, s39\n\t"
        "s_waitcnt lgkmcnt(7)\n\t"
        "v_mfma_f32_16x16x32_bf16 v[12:15], a[0:3], v[84:87], 0\n\t"
        "buffer_load_dwordx4 v140, s[24:27], s36 offen offset:0 lds\n\t"
        "buffer_load_dwordx4 v141, s[24:27], s36 offen offset:1024 lds\n\t"
        "buffer_load_dwordx4 v142, s[24:27], s36 offen offset:2048 lds\n\t"
        "s_add_u32 m0, s35, 16384\n\t"
        "s_add_u32 s43, s34, 172032\n\t"
        "buffer_load_dwordx4 v143, s[16:19], s43 offen  lds\n\t"
        "s_add_u32 m0, s35, 32768\n\t"
        "s_add_u32 s43, s34, 344064\n\t"
        "s_min_u32 s43, s43, 9590784\n\t"
        "buffer_load_dwordx4 v143, s[16:19], s43 offen  lds\n\t"
        "s_waitcnt vmcnt(10)\n\t"
        "s_barrier\n\t"
        "ds_read_b128 v[92:95], v128 offset:12288\n\t"
        "ds_read_b64_tr_b16 a[24:25], v138 offset:0\n\t"
        "v_mfma_f32_16x16x32_bf16 v[16:19], a[4:7], v[84:87], 0\n\t"
        "ds_read_b64_tr_b16 a[26:27], v138 offset:768\n\t"
        "s_add_u32 m0, s35, 157696\n\t"
        "s_nop 0\n\t"
        "buffer_load_dwordx4 v64, s[20:23], 0 offen lds\n\t"
        "s_add_u32 m0, s37, 0\n\t"
        "s_add_u32 s36, s36, s38\n\t"
        "v_mfma_f32_16x16x32_bf16 v[20:23], a[8:11], v[84:87], 0\n\t"
        "buffer_load_dwordx4 v140, s[24:27], s36 offen offset:0 lds\n\t"
        "ds_read_b64_tr_b16 a[28:29], v139 offset:0\n\t"
        "v_readlane_b32 s44, v155, 32\n\t"
        "s_waitcnt lgkmcnt(4)\n\t"
        "v_mfma_f32_16x16x32_bf16 v[24:27], a[12:15], v[88:91], 0\n\t"
        "ds_read_b64_tr_b16 a[30:31], v139 offset:768\n\t"
        "ds_read_b64_tr_b16 a[32:33], v149 offset:0\n\t"
        "ds_read_b64_tr_b16 a[34:35], v149 offset:768\n\t"
        "v_readlane_b32 s45, v155, 33\n\t"
        "v_mfma_f32_16x16x32_bf16 v[28:31], a[16:19], v[88:91], 0\n\t"
        "buffer_load_dwordx4 v141, s[24:27], s36 offen offset:1024 lds\n\t"
        "ds_read_b128 v[96:99], v128 offset:8192\n\t"
        "v_readlane_b32 s62, v155, 36\n\t"
        "v_mfma_f32_16x16x32_bf16 v[32:35], a[20:23], v[88:91], 0\n\t"
        "ds_read_b128 v[100:103], v128 offset:12288\n\t"
        "s_lshl_b32 s70, s62, 5\n\t"
        "s_waitcnt lgkmcnt(8)\n\t"
        "v_mfma_f32_16x16x32_bf16 v[24:27], a[0:3], v[92:95], v[24:27]\n\t"
        "s_sub_u32 s71, s70, 16384\n\t"
        "s_mul_i32 s71, s71, s68\n\t"
        "v_mfma_f32_16x16x32_bf16 v[28:31], a[4:7], v[92:95], v[28:31]\n\t"
        "buffer_load_dwordx4 v142, s[24:27], s36 offen offset:2048 lds\n\t"
        "s_add_u32 m0, s35, 0\n\t"
        "s_add_u32 s43, s34, 0\n\t"
        "v_mfma_f32_16x16x32_bf16 v[32:35], a[8:11], v[92:95], v[32:35]\n\t"
        "s_mov_b32 exec_lo, 0\n\t"
        "s_mov_b32 exec_hi, 0xfff00000\n\t"
        "ds_write_b32 v119, v155\n\t"
        "s_mov_b64 exec, -1\n\t"
        "v_mfma_f32_16x16x32_bf16 v[36:39], a[12:15], v[92:95], 0\n\t"
        "v_add_u32 v172, s71, v128\n\t"
        "v_add_u32 v173, s71, v129\n\t"
        "v_mfma_f32_16x16x32_bf16 v[40:43], a[16:19], v[92:95], 0\n\t"
        "buffer_load_dwordx4 v143, s[16:19], s43 offen  lds\n\t"
        "s_add_u32 m0, s35, 36864\n\t"
        "s_add_u32 s43, s34, 387072\n\t"
        "s_min_u32 s43, s43, 9590784\n\t"
        "v_mfma_f32_16x16x32_bf16 v[44:47], a[20:23], v[92:95], 0\n\t"
        "buffer_load_dwordx4 v143, s[16:19], s43 offen  lds\n\t"
        "s_add_u32 m0, s37, 12288\n\t"
        "s_add_u32 s36, s36, s5\n\t"
        "buffer_load_dwordx4 v140, s[24:27], s36 offen offset:0 lds\n\t"
        "s_waitcnt vmcnt(9) lgkmcnt(3)\n\t"
        "s_barrier\n\t"
        "ds_read_b128 v[104:107], v128 offset:4096\n\t"
        "ds_read_b64_tr_b16 a[36:37], v138 offset:18432\n\t"
        "s_waitcnt lgkmcnt(3)\n\t"
        "v_mfma_f32_16x16x32_bf16 v[0:3], a[24:27], v[96:99], 0\n\t"
        "buffer_load_dwordx4 v141, s[24:27], s36 offen offset:1024 lds\n\t"
        "ds_read_b64_tr_b16 a[38:39], v138 offset:19200\n\t"
        "v_add_u32 v174, s71, v130\n\t"
        "v_mfma_f32_16x16x32_bf16 v[4:7], a[28:31], v[96:99], 0\n\t"
        "ds_read_b64_tr_b16 a[40:41], v139 offset:18432\n\t"
        "ds_read_b64_tr_b16 a[42:43], v139 offset:19200\n\t"
        "s_add_u32 s71, s71, s70\n\t"
        "v_mfma_f32_16x16x32_bf16 v[8:11], a[32:35], v[96:99], 0\n\t"
        "buffer_load_dwordx4 v142, s[24:27], s36 offen offset:2048 lds\n\t"
        "s_add_u32 m0, s37, 24576\n\t"
        "ds_read_b64_tr_b16 a[44:45], v149 offset:18432\n\t"
        "s_add_u32 s36, s36, s39\n\t"
        "v_mfma_f32_16x16x32_bf16 v[12:15], a[24:27], v[100:103], v[12:15]\n\t"
        "ds_read_b64_tr_b16 a[46:47], v149 offset:19200\n\t"
        "ds_read_b64_tr_b16 a[48:49], v138 offset:24576\n\t"
        "ds_read_b64_tr_b16 a[50:51], v138 offset:25344\n\t"
        "v_add_u32 v175, s71, v128\n\t"
        "v_mfma_f32_16x16x32_bf16 v[16:19], a[28:31], v[100:103], v[16:19]\n\t"
        "ds_read_b64_tr_b16 a[52:53], v139 offset:24576\n\t"
        "ds_read_b64_tr_b16 a[54:55], v139 offset:25344\n\t"
        "ds_read_b64_tr_b16 a[56:57], v149 offset:24576\n\t"
        "v_add_u32 v176, s71, v129\n\t"
        "v_mfma_f32_16x16x32_bf16 v[20:23], a[32:35], v[100:103], v[20:23]\n\t"
        "ds_read_b64_tr_b16 a[58:59], v149 offset:25344\n\t"
        "ds_read_b128 v[108:111], v128 offset:4160\n\t"
        "v_add_u32 v177, s71, v130\n\t"
        "s_add_u32 s71, s71, s70\n\t"
        "s_waitcnt lgkmcnt(12)\n\t"
        "v_mfma_f32_16x16x32_bf16 v[0:3], a[0:3], v[104:107], v[0:3]\n\t"
        "ds_read_b64_tr_b16 a[60:61], v138 offset:30720\n\t"
        "ds_read_b64_tr_b16 a[62:63], v138 offset:31488\n\t"
        "v_add_u32 v178, s71, v128\n\t"
        "v_mfma_f32_16x16x32_bf16 v[4:7], a[4:7], v[104:107], v[4:7]\n\t"
        "ds_read_b64_tr_b16 a[64:65], v139 offset:30720\n\t"
        "ds_read_b64_tr_b16 a[66:67], v139 offset:31488\n\t"
        "v_add_u32 v179, s71, v129\n\t"
        "v_mfma_f32_16x16x32_bf16 v[8:11], a[8:11], v[104:107], v[8:11]\n\t"
        "ds_read_b64_tr_b16 a[68:69], v149 offset:30720\n\t"
        "ds_read_b64_tr_b16 a[70:71], v149 offset:31488\n\t"
        "v_add_u32 v180, s71, v130\n\t"
        "v_mfma_f32_16x16x32_bf16 v[12:15], a[12:15], v[104:107], v[12:15]\n\t"
        "ds_read_b128 v[112:115], v128 offset:8256\n\t"
        "v_add_f32 v78, v78, v56\n\t"
        "v_mfma_f32_16x16x32_bf16 v[16:19], a[16:19], v[104:107], v[16:19]\n\t"
        "s_cmp_eq_u32 s69, 36\n\t"
        "s_cselect_b64 exec, -1, 0\n\t"
        "v_swap_b32 v68, v69\n\t"
        "v_swap_b32 v173, v175\n\t"
        "v_swap_b32 v174, v178\n\t"
        "v_swap_b32 v177, v179\n\t"
        "s_mov_b64 exec, -1\n\t"
        "v_mfma_f32_16x16x32_bf16 v[20:23], a[20:23], v[104:107], v[20:23]\n\t"
        "ds_read_b128 v[84:87], v128 offset:12352\n\t"
        "v_add3_u32 v68, v68, v52, -1\n\t"
        "v_add3_u32 v69, v69, v158, -1\n\t"
        "s_waitcnt lgkmcnt(8)\n\t"
        "v_mfma_f32_16x16x32_bf16 v[0:3], a[36:39], v[108:111], v[0:3]\n\t"
        "ds_read_b64_tr_b16 a[72:73], v138 offset:36864\n\t"
        "ds_read_b64_tr_b16 a[74:75], v138 offset:37632\n\t"
        "v_cvt_f32_i32 v68, v68\n\t"
        "v_mfma_f32_16x16x32_bf16 v[4:7], a[40:43], v[108:111], v[4:7]\n\t"
        "ds_read_b64_tr_b16 a[76:77], v139 offset:36864\n\t"
        "ds_read_b64_tr_b16 a[78:79], v139 offset:37632\n\t"
        "v_cvt_f32_i32 v69, v69\n\t"
        "v_mfma_f32_16x16x32_bf16 v[8:11], a[44:47], v[108:111], v[8:11]\n\t"
        "ds_read_b64_tr_b16 a[80:81], v149 offset:36864\n\t"
        "ds_read_b64_tr_b16 a[82:83], v149 offset:37632\n\t"
        "v_add_f32 v79, v79, v57\n\t"
        "v_mfma_f32_16x16x32_bf16 v[12:15], a[48:51], v[108:111], v[12:15]\n\t"
        "v_readlane_b32 s69, v155, 31\n\t"
        "v_mfma_f32_16x16x32_bf16 v[16:19], a[52:55], v[108:111], v[16:19]\n\t"
        "s_nop 0\n\t"
        "v_mov_b32 v154, s69\n\t"
        "v_mov_b32 v119, s62\n\t"
        "v_readlane_b32 s53, v155, 28\n\t"
        "v_mfma_f32_16x16x32_bf16 v[20:23], a[56:59], v[108:111], v[20:23]\n\t"
        "buffer_load_dwordx4 v140, s[24:27], s36 offen offset:0 lds\n\t"
        "v_readlane_b32 s9, v155, 46\n\t"
        "s_waitcnt lgkmcnt(7)\n\t"
        "v_mfma_f32_16x16x32_bf16 v[0:3], a[60:63], v[112:115], v[0:3]\n\t"
        "s_sub_u32 s10, 43200, s11\n\t"
        "s_mul_i32 s10, s10, s68\n\t"
        "v_mfma_f32_16x16x32_bf16 v[4:7], a[64:67], v[112:115], v[4:7]\n\t"
        "s_sub_u32 s11, s11, 192\n\t"
        "v_mad_u32_u24 v148, v158, s11, v144\n\t"
        "v_mfma_f32_16x16x32_bf16 v[8:11], a[68:71], v[112:115], v[8:11]\n\t"
        "v_add_u32 v148, s10, v148\n\t"
        "v_mad_u32_u24 v150, v83, s11, v145\n\t"
        "v_mfma_f32_16x16x32_bf16 v[12:15], a[36:39], v[112:115], v[12:15]\n\t"
        "s_lshl_b32 s70, s49, 1\n\t"
        "s_add_u32 s70, s70, 49\n\t"
        "v_mfma_f32_16x16x32_bf16 v[16:19], a[40:43], v[112:115], v[16:19]\n\t"
        "buffer_load_dwordx4 v141, s[24:27], s36 offen offset:1024 lds\n\t"
        "v_readlane_b32 s52, v155, s70\n\t"
        "v_mfma_f32_16x16x32_bf16 v[20:23], a[44:47], v[112:115], v[20:23]\n\t"
        "s_nop 0\n\t"
        "s_add_u32 s52, s52, s10\n\t"
        "v_mfma_f32_16x16x32_bf16 v[24:27], a[48:51], v[112:115], v[24:27]\n\t"
        "v_mfma_f32_16x16x32_bf16 v[28:31], a[52:55], v[112:115], v[28:31]\n\t"
        "v_mfma_f32_16x16x32_bf16 v[32:35], a[56:59], v[112:115], v[32:35]\n\t"
        "buffer_load_dwordx4 v142, s[24:27], s36 offen offset:2048 lds\n\t"
        "s_waitcnt lgkmcnt(0)\n\t"
        "v_mfma_f32_16x16x32_bf16 v[12:15], a[60:63], v[84:87], v[12:15]\n\t"
        "v_mfma_f32_16x16x32_bf16 v[16:19], a[64:67], v[84:87], v[16:19]\n\t"
        "v_mfma_f32_16x16x32_bf16 v[20:23], a[68:71], v[84:87], v[20:23]\n\t"
        "v_mfma_f32_16x16x32_bf16 v[24:27], a[36:39], v[84:87], v[24:27]\n\t"
        "buffer_load_dwordx4 v[48:51], v55, s[20:23], s48 offen\n\t"
        "ds_read_b128 v[88:91], v128 offset:4224\n\t"
        "v_mfma_f32_16x16x32_bf16 v[28:31], a[40:43], v[84:87], v[28:31]\n\t"
        "v_mfma_f32_16x16x32_bf16 v[32:35], a[44:47], v[84:87], v[32:35]\n\t"
        "v_mfma_f32_16x16x32_bf16 v[36:39], a[48:51], v[84:87], v[36:39]\n\t"
        "ds_read_b128 v[92:95], v128 offset:8320\n\t"
        "v_mfma_f32_16x16x32_bf16 v[40:43], a[52:55], v[84:87], v[40:43]\n\t"
        "buffer_load_dword v61, v61, s[20:23], s48 offen offset:512\n\t"
        "s_add_u32 m0, s37, 36864\n\t"
        "s_add_u32 s36, s36, s38\n\t"
        "v_mfma_f32_16x16x32_bf16 v[44:47], a[56:59], v[84:87], v[44:47]\n\t"
        "s_waitcnt lgkmcnt(1)\n\t"
        "v_mfma_f32_16x16x32_bf16 v[0:3], a[72:75], v[88:91], v[0:3]\n\t"
        "ds_read_b128 v[96:99], v128 offset:12416\n\t"
        "v_mfma_f32_16x16x32_bf16 v[4:7], a[76:79], v[88:91], v[4:7]\n\t"
        "s_waitcnt vmcnt(10)\n\t"
        "s_barrier\n\t"
        "ds_read_b128 v[100:103], v128 offset:16384\n\t"
        "ds_read_b128 v[104:107], v128 offset:16448\n\t"
        "v_mfma_f32_16x16x32_bf16 v[8:11], a[80:83], v[88:91], v[8:11]\n\t"
        "ds_read_b32 v64, v70 offset:0\n\t"
        "buffer_load_dwordx4 v140, s[24:27], s36 offen offset:0 lds\n\t"
        "v_readlane_b32 s46, v155, 37\n\t"
        "v_readlane_b32 s47, v155, 40\n\t"
        "s_waitcnt lgkmcnt(4)\n\t"
        "v_mfma_f32_16x16x32_bf16 v[12:15], a[72:75], v[92:95], v[12:15]\n\t"
        "ds_read_b32 v67, v67 offset:0\n\t"
        "ds_read_b64_tr_b16 a[84:85], v131 offset:0\n\t"
        "v_add_f32 v58, s46, v68\n\t"
        "v_mfma_f32_16x16x32_bf16 v[16:19], a[76:79], v[92:95], v[16:19]\n\t"
        "ds_read_b32 v66, v70 offset:2560\n\t"
        "ds_read_b64_tr_b16 a[86:87], v131 offset:768\n\t"
        "v_add_f32 v59, s47, v69\n\t"
        "v_readlane_b32 s47, v155, 42\n\t"
        "v_mfma_f32_16x16x32_bf16 v[20:23], a[80:83], v[92:95], v[20:23]\n\t"
        "ds_read_b64_tr_b16 a[88:89], v132 offset:0\n\t"
        "ds_read_b64_tr_b16 a[90:91], v132 offset:768\n\t"
        "s_waitcnt lgkmcnt(9)\n\t"
        "v_mfma_f32_16x16x32_bf16 v[24:27], a[72:75], v[96:99], v[24:27]\n\t"
        "buffer_load_dwordx4 v141, s[24:27], s36 offen offset:1024 lds\n\t"
        "ds_read_b64_tr_b16 a[92:93], v133 offset:0\n\t"
        "v_mfma_f32_16x16x32_bf16 v[28:31], a[76:79], v[96:99], v[28:31]\n\t"
        "ds_read_b64_tr_b16 a[94:95], v133 offset:768\n\t"
        "ds_read_b128 v[108:111], v128 offset:4224\n\t"
        "v_mfma_f32_16x16x32_bf16 v[32:35], a[80:83], v[96:99], v[32:35]\n\t"
        "ds_read_b64_tr_b16 a[96:97], v131 offset:6144\n\t"
        "ds_read_b64_tr_b16 a[98:99], v131 offset:6912\n\t"
        "s_waitcnt lgkmcnt(12)\n\t"
        "v_mfma_f32_16x16x32_bf16 v[24:27], a[24:27], v[100:103], v[24:27]\n\t"
        "ds_read_b64_tr_b16 a[100:101], v132 offset:6144\n\t"
        "ds_read_b64_tr_b16 a[102:103], v132 offset:6912\n\t"
        "v_mfma_f32_16x16x32_bf16 v[28:31], a[28:31], v[100:103], v[28:31]\n\t"
        "buffer_load_dwordx4 v142, s[24:27], s36 offen offset:2048 lds\n\t"
        "s_add_u32 m0, s37, 49152\n\t"
        "ds_read_b64_tr_b16 a[104:105], v133 offset:6144\n\t"
        "v_mfma_f32_16x16x32_bf16 v[32:35], a[32:35], v[100:103], v[32:35]\n\t"
        "ds_read_b64_tr_b16 a[106:107], v133 offset:6912\n\t"
        "s_add_u32 s36, s36, s5\n\t"
        "s_waitcnt lgkmcnt(12)\n\t"
        "v_perm_b32 v56, v64, v64, s44\n\t"
        "v_mfma_f32_16x16x32_bf16 v[36:39], a[0:3], v[100:103], v[36:39]\n\t"
        "ds_read_b128 v[112:115], v128 offset:8320\n\t"
        "v_perm_b32 v57, v64, v64, s45\n\t"
        "v_mfma_f32_16x16x32_bf16 v[40:43], a[4:7], v[100:103], v[40:43]\n\t"
        "ds_read_b128 v[84:87], v128 offset:12416\n\t"
        "v_add_f32 v56, v56, v58\n\t"
        "v_mfma_f32_16x16x32_bf16 v[44:47], a[8:11], v[100:103], v[44:47]\n\t"
        "buffer_load_dwordx4 v140, s[24:27], s36 offen offset:0 lds\n\t"
        "v_add_f32 v57, v57, v59\n\t"
        "v_cvt_pk_bf16_f32 v52, v56, v57\n\t"
        "v_mfma_f32_16x16x32_bf16 v[24:27], a[60:63], v[104:107], v[24:27]\n\t"
        "ds_read_b128 v[88:91], v128 offset:16512\n\t"
        "v_cvt_pk_bf16_f32 v58, v58, v59\n\t"
        "v_mfma_f32_16x16x32_bf16 v[28:31], a[64:67], v[104:107], v[28:31]\n\t"
        "v_cmp_ne_u32 s[64:65], v52, v58\n\t"
        "v_perm_b32 v57, v67, v67, s45\n\t"
        "v_mfma_f32_16x16x32_bf16 v[32:35], a[68:71], v[104:107], v[32:35]\n\t"
        "v_mbcnt_lo_u32_b32 v56, s64, 1\n\t"
        "v_mbcnt_hi_u32_b32 v56, s65, v56\n\t"
        "v_mfma_f32_16x16x32_bf16 v[36:39], a[36:39], v[104:107], v[36:39]\n\t"
        "buffer_load_dwordx4 v141, s[24:27], s36 offen offset:1024 lds\n\t"
        "s_bcnt1_i32_b64 s61, s[64:65]\n\t"
        "v_mfma_f32_16x16x32_bf16 v[40:43], a[40:43], v[104:107], v[40:43]\n\t"
        "v_add_u32 v54, s61, 1\n\t"
        "v_lshl_add_u32 v56, v56, 3, s60\n\t"
        "v_add_f32 v57, v57, v79\n\t"
        "v_mfma_f32_16x16x32_bf16 v[44:47], a[44:47], v[104:107], v[44:47]\n\t"
        "s_mov_b64 exec, s[64:65]\n\t"
        "ds_write_b64 v56, v[52:53]\n\t"
        "s_mov_b64 exec, -1\n\t"
        "s_waitcnt lgkmcnt(3)\n\t"
        "v_mfma_f32_16x16x32_bf16 v[12:15], a[84:87], v[108:111], v[12:15]\n\t"
        "v_swap_b32 v53, v80\n\t"
        "v_perm_b32 v56, v67, v67, s44\n\t"
        "v_mfma_f32_16x16x32_bf16 v[16:19], a[88:91], v[108:111], v[16:19]\n\t"
        "buffer_load_dwordx4 v142, s[24:27], s36 offen offset:2048 lds\n\t"
        "v_readlane_b32 s40, v155, 12\n\t"
        "v_mfma_f32_16x16x32_bf16 v[20:23], a[92:95], v[108:111], v[20:23]\n\t"
        "v_readlane_b32 s43, v155, 0\n\t"
        "s_add_u32 m0, s40, s35\n\t"
        "v_mfma_f32_16x16x32_bf16 v[0:3], a[96:99], v[112:115], v[0:3]\n\t"
        "v_add_f32 v56, v56, v78\n\t"
        "v_cvt_pk_bf16_f32 v52, v56, v57\n\t"
        "v_mfma_f32_16x16x32_bf16 v[4:7], a[100:103], v[112:115], v[4:7]\n\t"
        "v_cvt_pk_bf16_f32 v58, v78, v79\n\t"
        "v_cmp_ne_u32 s[64:65], v52, v58\n\t"
        "v_perm_b32 v57, v66, v66, s45\n\t"
        "v_mfma_f32_16x16x32_bf16 v[8:11], a[104:107], v[112:115], v[8:11]\n\t"
        "buffer_load_dwordx4 v134, s[16:19], s43 offen  lds\n\t"
        "v_readlane_b32 s40, v155, 13\n\t"
        "v_mfma_f32_16x16x32_bf16 v[24:27], a[84:87], v[112:115], v[24:27]\n\t"
        "v_readlane_b32 s43, v155, 1\n\t"
        "s_add_u32 m0, s40, s35\n\t"
        "v_mfma_f32_16x16x32_bf16 v[28:31], a[88:91], v[112:115], v[28:31]\n\t"
        "s_waitcnt vmcnt(9)\n\t"
        "s_barrier\n\t"
        "v_mfma_f32_16x16x32_bf16 v[32:35], a[92:95], v[112:115], v[32:35]\n\t"
        "ds_read_b128 v[92:95], v128 offset:0\n\t"
        "s_and_b64 s[64:65], s[64:65], 0xffffff\n\t"
        "s_waitcnt lgkmcnt(2)\n\t"
        "v_mfma_f32_16x16x32_bf16 v[12:15], a[96:99], v[84:87], v[12:15]\n\t"
        "buffer_load_dwordx4 v135, s[16:19], s43 offen  lds\n\t"
        "ds_read_b128 v[96:99], v128 offset:64\n\t"
        "v_readlane_b32 s40, v155, 14\n\t"
        "v_mfma_f32_16x16x32_bf16 v[16:19], a[100:103], v[84:87], v[16:19]\n\t"
        "ds_read_b128 v[100:103], v128 offset:128\n\t"
        "ds_read_b128 v[104:107], v128 offset:20480\n\t"
        "v_readlane_b32 s43, v155, 2\n\t"
        "v_mfma_f32_16x16x32_bf16 v[20:23], a[104:107], v[84:87], v[20:23]\n\t"
        "ds_read_b64_tr_b16 a[108:109], v131 offset:12288\n\t"
        "s_add_u32 m0, s40, s35\n\t"
        "ds_read_b64_tr_b16 a[110:111], v131 offset:13056\n\t"
        "v_mfma_f32_16x16x32_bf16 v[36:39], a[84:87], v[84:87], v[36:39]\n\t"
        "ds_read_b128 v[108:111], v128 offset:20544\n\t"
        "ds_read_b64_tr_b16 a[112:113], v132 offset:12288\n\t"
        "v_mbcnt_lo_u32_b32 v56, s64, v54\n\t"
        "s_bcnt1_i32_b64 s46, s[64:65]\n\t"
        "v_mfma_f32_16x16x32_bf16 v[40:43], a[88:91], v[84:87], v[40:43]\n\t"
        "buffer_load_dwordx4 v134, s[16:19], s43 offen  lds\n\t"
        "ds_read_b64_tr_b16 a[114:115], v132 offset:13056\n\t"
        "s_bitcmp1_b32 s42, 10\n\t"
        "s_cbranch_scc0 .Lx60%=\n\t"
        "v_readlane_b32 s40, v155, 20\n\t"
        "v_readlane_b32 s43, v155, 8\n\t"
        "s_nop 0\n\t"
        "s_add_u32 m0, s40, s35\n\t"
        "s_nop 2\n\t"
        "buffer_load_dwordx4 v136, s[16:19], s43 offen  lds\n\t"
        ".Lx60%=:\n\t"
        "v_mfma_f32_16x16x32_bf16 v[44:47], a[92:95], v[84:87], v[44:47]\n\t"
        "ds_read_b64_tr_b16 a[116:117], v133 offset:12288\n\t"
        "ds_read_b64_tr_b16 a[118:119], v133 offset:13056\n\t"
        "v_readlane_b32 s40, v155, 15\n\t"
        "v_mfma_f32_16x16x32_bf16 v[24:27], a[96:99], v[88:91], v[24:27]\n\t"
        "ds_read_b128 v[112:115], v128 offset:20608\n\t"
        "v_readlane_b32 s43, v155, 3\n\t"
        "v_mfma_f32_16x16x32_bf16 v[28:31], a[100:103], v[88:91], v[28:31]\n\t"
        "ds_read_b64_tr_b16 a[120:121], v131 offset:18432\n\t"
        "s_add_u32 m0, s40, s35\n\t"
        "v_mfma_f32_16x16x32_bf16 v[32:35], a[104:107], v[88:91], v[32:35]\n\t"
        "ds_read_b64_tr_b16 a[122:123], v131 offset:19200\n\t"
        "s_add_u32 s61, s61, s46\n\t"
        "v_mfma_f32_16x16x32_bf16 v[36:39], a[72:75], v[88:91], v[36:39]\n\t"
        "ds_read_b64_tr_b16 a[124:125], v132 offset:18432\n\t"
        "v_add_u32 v54, s46, v54\n\t"
        "v_mfma_f32_16x16x32_bf16 v[40:43], a[76:79], v[88:91], v[40:43]\n\t"
        "ds_read_b64_tr_b16 a[126:127], v132 offset:19200\n\t"
        "s_mov_b32 s3, s61\n\t"
        "v_mfma_f32_16x16x32_bf16 v[44:47], a[80:83], v[88:91], v[44:47]\n\t"
        "ds_read_b64_tr_b16 a[128:129], v133 offset:18432\n\t"
        "v_lshl_add_u32 v56, v56, 3, s60\n\t"
        "v_readlane_b32 s46, v155, 39\n\t"
        "s_waitcnt lgkmcnt(9)\n\t"
        "v_mfma_f32_16x16x32_bf16 v[0:3], a[12:15], v[92:95], v[0:3]\n\t"
        "s_mov_b64 exec, s[64:65]\n\t"
        "ds_write_b64 v56, v[52:53]\n\t"
        "s_mov_b64 exec, -1\n\t"
        "ds_read_b64_tr_b16 a[130:131], v133 offset:19200\n\t"
        "v_mfma_f32_16x16x32_bf16 v[4:7], a[16:19], v[92:95], v[4:7]\n\t"
        "ds_read_b128 v[84:87], v129 offset:0\n\t"
        "v_add_u32 v53, 4096, v80\n\t"
        "v_mfma_f32_16x16x32_bf16 v[8:11], a[20:23], v[92:95], v[8:11]\n\t"
        "ds_read_b128 v[88:91], v129 offset:4096\n\t"
        "v_perm_b32 v56, v66, v66, s44\n\t"
        "v_mfma_f32_16x16x32_bf16 v[0:3], a[48:51], v[96:99], v[0:3]\n\t"
        "ds_read_b64_tr_b16 a[132:133], v131 offset:24576\n\t"
        "v_add_f32 v58, s46, v68\n\t"
        "v_mfma_f32_16x16x32_bf16 v[4:7], a[52:55], v[96:99], v[4:7]\n\t"
        "buffer_load_dwordx4 v135, s[16:19], s43 offen  lds\n\t"
        "s_add_u32 m0, s37, 0\n\t"
        "v_mfma_f32_16x16x32_bf16 v[8:11], a[56:59], v[96:99], v[8:11]\n\t"
        "ds_read_b64_tr_b16 a[134:135], v131 offset:25344\n\t"
        "s_add_u32 s36, s36, s39\n\t"
        "v_mfma_f32_16x16x32_bf16 v[0:3], a[84:87], v[100:103], v[0:3]\n\t"
        "ds_read_b64_tr_b16 a[136:137], v132 offset:24576\n\t"
        "v_add_f32 v59, s47, v69\n\t"
        "v_mfma_f32_16x16x32_bf16 v[4:7], a[88:91], v[100:103], v[4:7]\n\t"
        "ds_read_b64_tr_b16 a[138:139], v132 offset:25344\n\t"
        "v_add_f32 v56, v56, v58\n\t"
        "v_add_f32 v57, v57, v59\n\t"
        "v_mfma_f32_16x16x32_bf16 v[8:11], a[92:95], v[100:103], v[8:11]\n\t"
        "ds_read_b64_tr_b16 a[140:141], v133 offset:24576\n\t"
        "v_max_f32 v56, 0, v56\n\t"
        "v_mfma_f32_16x16x32_bf16 v[36:39], a[24:27], v[104:107], v[36:39]\n\t"
        "buffer_load_dwordx4 v140, s[24:27], s36 offen offset:0 lds\n\t"
        "v_max_f32 v57, 0, v57\n\t"
        "v_mfma_f32_16x16x32_bf16 v[40:43], a[28:31], v[104:107], v[40:43]\n\t"
        "ds_read_b64_tr_b16 a[142:143], v133 offset:25344\n\t"
        "v_max_f32 v58, 0, v58\n\t"
        "v_mfma_f32_16x16x32_bf16 v[44:47], a[32:35], v[104:107], v[44:47]\n\t"
        "ds_read_b128 v[92:95], v129 offset:8192\n\t"
        "v_max_f32 v59, 0, v59\n\t"
        "v_mfma_f32_16x16x32_bf16 v[36:39], a[60:63], v[108:111], v[36:39]\n\t"
        "ds_read_b64_tr_b16 a[144:145], v131 offset:30720\n\t"
        "v_cvt_pk_bf16_f32 v52, v56, v57\n\t"
        "v_mfma_f32_16x16x32_bf16 v[40:43], a[64:67], v[108:111], v[40:43]\n\t"
        "buffer_load_dwordx4 v141, s[24:27], s36 offen offset:1024 lds\n\t"
        "v_cvt_pk_bf16_f32 v58, v58, v59\n\t"
        "v_mfma_f32_16x16x32_bf16 v[44:47], a[68:71], v[108:111], v[44:47]\n\t"
        "ds_read_b64_tr_b16 a[146:147], v131 offset:31488\n\t"
        "v_cmp_ne_u32 s[64:65], v52, v58\n\t"
        "s_waitcnt lgkmcnt(7)\n\t"
        "v_mfma_f32_16x16x32_bf16 v[36:39], a[96:99], v[112:115], v[36:39]\n\t"
        "ds_read_b128 v[96:99], v129 offset:12288\n\t"
        "v_mbcnt_lo_u32_b32 v56, s64, v54\n\t"
        "v_mfma_f32_16x16x32_bf16 v[40:43], a[100:103], v[112:115], v[40:43]\n\t"
        "ds_read_b64_tr_b16 a[148:149], v132 offset:30720\n\t"
        "v_mbcnt_hi_u32_b32 v56, s65, v56\n\t"
        "v_mfma_f32_16x16x32_bf16 v[44:47], a[104:107], v[112:115], v[44:47]\n\t"
        "buffer_load_dwordx4 v142, s[24:27], s36 offen offset:2048 lds\n\t"
        ".LpX0%=:\n\t"
        "s_cbranch_execz .LrX%=\n\t"
        "s_add_u32 m0, s37, 12288\n\t"
        "v_mfma_f32_16x16x32_bf16 v[0:3], a[120:123], v[84:87], v[0:3]\n\t"
        "ds_read_b64_tr_b16 a[150:151], v132 offset:31488\n\t"
        "s_add_u32 s36, s36, s38\n\t"
        "s_bcnt1_i32_b64 s46, s[64:65]\n\t"
        "v_mfma_f32_16x16x32_bf16 v[4:7], a[124:127], v[84:87], v[4:7]\n\t"
        "ds_read_b64_tr_b16 a[152:153], v133 offset:30720\n\t"
        "s_add_u32 s61, s61, s46\n\t"
        "v_mfma_f32_16x16x32_bf16 v[8:11], a[128:131], v[84:87], v[8:11]\n\t"
        "ds_read_b64_tr_b16 a[154:155], v133 offset:31488\n\t"
        "v_min_u32 v56, 97, v56\n\t"
        "v_mfma_f32_16x16x32_bf16 v[0:3], a[108:111], v[88:91], v[0:3]\n\t"
        "v_lshl_add_u32 v56, v56, 3, s60\n\t"
        "v_mfma_f32_16x16x32_bf16 v[4:7], a[112:115], v[88:91], v[4:7]\n\t"
        "s_mov_b64 exec, s[64:65]\n\t"
        "ds_write_b64 v56, v[52:53]\n\t"
        "s_mov_b64 exec, -1\n\t"
        "v_mfma_f32_16x16x32_bf16 v[8:11], a[116:119], v[88:91], v[8:11]\n\t"
        "s_cmp_gt_u32 s61, s53\n\t"
        "s_cselect_b32 s8, 1, 0\n\t"
        "v_mfma_f32_16x16x32_bf16 v[12:15], a[120:123], v[88:91], v[12:15]\n\t"
        "ds_read_b128 v[100:103], v129 offset:16384\n\t"
        "s_waitcnt vmcnt(14)\n\t"
        "v_and_b32 v48, s54, v48\n\t"
        "v_mfma_f32_16x16x32_bf16 v[16:19], a[124:127], v[88:91], v[16:19]\n\t"
        "v_pk_max_u16 v60, v154, v48\n\t"
        "v_and_b32 v49, s54, v49\n\t"
        "v_mfma_f32_16x16x32_bf16 v[20:23], a[128:131], v[88:91], v[20:23]\n\t"
        "v_pk_max_u16 v60, v60, v49\n\t"
        "v_and_b32 v50, s54, v50\n\t"
        "s_waitcnt lgkmcnt(5)\n\t"
        "v_mfma_f32_16x16x32_bf16 v[0:3], a[132:135], v[92:95], v[0:3]\n\t"
        "v_pk_max_u16 v60, v60, v50\n\t"
        "v_and_b32 v51, s54, v51\n\t"
        "v_mfma_f32_16x16x32_bf16 v[4:7], a[136:139], v[92:95], v[4:7]\n\t"
        "v_pk_max_u16 v60, v60, v51\n\t"
        "s_waitcnt vmcnt(13)\n\t"
        "v_and_b32 v61, s54, v61\n\t"
        "v_mfma_f32_16x16x32_bf16 v[8:11], a[140:143], v[92:95], v[8:11]\n\t"
        "v_pk_max_u16 v60, v60, v61\n\t"
        "v_cmp_ne_u32 s[12:13], v60, v154\n\t"
        "v_mfma_f32_16x16x32_bf16 v[12:15], a[108:111], v[92:95], v[12:15]\n\t"
        "ds_read_b128 v[104:107], v129 offset:20480\n\t"
        "s_lshl_b32 s6, 256, s4\n\t"
        "v_mfma_f32_16x16x32_bf16 v[16:19], a[112:115], v[92:95], v[16:19]\n\t"
        "s_cmp_lg_u64 s[12:13], 0\n\t"
        "s_cselect_b32 s6, s6, 0\n\t"
        "s_or_b32 s56, s56, s8\n\t"
        "v_mfma_f32_16x16x32_bf16 v[20:23], a[116:119], v[92:95], v[20:23]\n\t"
        "s_waitcnt vmcnt(7) lgkmcnt(3)\n\t"
        "s_barrier\n\t"
        "v_mfma_f32_16x16x32_bf16 v[24:27], a[120:123], v[92:95], v[24:27]\n\t"
        "s_cmp_gt_u32 s3, 64\n\t"
        "s_cselect_b32 s8, 1, 0\n\t"
        "v_mfma_f32_16x16x32_bf16 v[28:31], a[124:127], v[92:95], v[28:31]\n\t"
        "buffer_load_dwordx4 v140, s[24:27], s36 offen offset:0 lds\n\t"
        "ds_read_b64_tr_b16 a[156:157], v131 offset:36864\n\t"
        "s_or_b32 s56, s56, s8\n\t"
        "v_mfma_f32_16x16x32_bf16 v[32:35], a[128:131], v[92:95], v[32:35]\n\t"
        "ds_read_b64_tr_b16 a[158:159], v131 offset:37632\n\t"
        "s_min_u32 s61, s61, 80\n\t"
        "v_mfma_f32_16x16x32_bf16 v[12:15], a[132:135], v[96:99], v[12:15]\n\t"
        "ds_read_b64_tr_b16 a[160:161], v132 offset:36864\n\t"
        "ds_read_b64_tr_b16 a[162:163], v132 offset:37632\n\t"
        "v_min_u32 v78, s61, v116\n\t"
        "v_mfma_f32_16x16x32_bf16 v[16:19], a[136:139], v[96:99], v[16:19]\n\t"
        "ds_read_b64_tr_b16 a[164:165], v133 offset:36864\n\t"
        "ds_read_b64_tr_b16 a[166:167], v133 offset:37632\n\t"
        "v_lshl_add_u32 v78, v78, 3, s60\n\t"
        "v_mfma_f32_16x16x32_bf16 v[20:23], a[140:143], v[96:99], v[20:23]\n\t"
        "buffer_load_dwordx4 v141, s[24:27], s36 offen offset:1024 lds\n\t"
        "v_cmp_ge_u32 s[66:67], s61, v116\n\t"
        "v_mfma_f32_16x16x32_bf16 v[24:27], a[108:111], v[96:99], v[24:27]\n\t"
        "ds_read_b128 v[108:111], v129 offset:64\n\t"
        "s_mov_b32 s46, 0x07060302\n\t"
        "v_mfma_f32_16x16x32_bf16 v[28:31], a[112:115], v[96:99], v[28:31]\n\t"
        "ds_read_b64 v[62:63], v78\n\t"
        "v_mfma_f32_16x16x32_bf16 v[32:35], a[116:119], v[96:99], v[32:35]\n\t"
        "ds_read_b128 v[112:115], v129 offset:4160\n\t"
        "v_mfma_f32_16x16x32_bf16 v[36:39], a[120:123], v[96:99], v[36:39]\n\t"
        "buffer_load_dwordx4 v142, s[24:27], s36 offen offset:2048 lds\n\t"
        "v_readlane_b32 s40, v155, 16\n\t"
        "v_mfma_f32_16x16x32_bf16 v[40:43], a[124:127], v[96:99], v[40:43]\n\t"
        "ds_read_b64_tr_b16 a[168:169], v131 offset:43008\n\t"
        "v_readlane_b32 s43, v155, 4\n\t"
        "v_mfma_f32_16x16x32_bf16 v[44:47], a[128:131], v[96:99], v[44:47]\n\t"
        "ds_read_b64_tr_b16 a[170:171], v131 offset:43776\n\t"
        "s_add_u32 m0, s40, s35\n\t"
        "s_waitcnt lgkmcnt(9)\n\t"
        "v_mfma_f32_16x16x32_bf16 v[24:27], a[132:135], v[100:103], v[24:27]\n\t"
        "ds_read_b64_tr_b16 a[172:173], v132 offset:43008\n\t"
        "v_mfma_f32_16x16x32_bf16 v[28:31], a[136:139], v[100:103], v[28:31]\n\t"
        "buffer_load_dwordx4 v134, s[16:19], s43 offen  lds\n\t"
        "s_bitcmp1_b32 s42, 11\n\t"
        "s_cbranch_scc0 .Lx62%=\n\t"
        "v_readlane_b32 s40, v155, 21\n\t"
        "v_readlane_b32 s43, v155, 9\n\t"
        "s_nop 0\n\t"
        "s_add_u32 m0, s40, s35\n\t"
        "s_nop 2\n\t"
        "buffer_load_dwordx4 v137, s[16:19], s43 offen  lds\n\t"
        "v_readlane_b32 s40, v155, 22\n\t"
        "v_readlane_b32 s43, v155, 10\n\t"
        "s_nop 0\n\t"
        "s_add_u32 m0, s40, s35\n\t"
        "s_nop 2\n\t"
        "buffer_load_dwordx4 v136, s[16:19], s43 offen  lds\n\t"
        ".Lx62%=:\n\t"
        "v_mfma_f32_16x16x32_bf16 v[32:35], a[140:143], v[100:103], v[32:35]\n\t"
        "ds_read_b64_tr_b16 a[174:175], v132 offset:43776\n\t"
        "v_readlane_b32 s40, v155, 17\n\t"
        "v_mfma_f32_16x16x32_bf16 v[36:39], a[108:111], v[100:103], v[36:39]\n\t"
        "ds_read_b64_tr_b16 a[176:177], v133 offset:43008\n\t"
        "ds_read_b64_tr_b16 a[178:179], v133 offset:43776\n\t"
        "v_readlane_b32 s43, v155, 5\n\t"
        "v_mfma_f32_16x16x32_bf16 v[40:43], a[112:115], v[100:103], v[40:43]\n\t"
        "ds_read_b128 v[84:87], v129 offset:8256\n\t"
        "s_add_u32 m0, s40, s35\n\t"
        "v_mfma_f32_16x16x32_bf16 v[44:47], a[116:119], v[100:103], v[44:47]\n\t"
        "s_mov_b64 exec, s[66:67]\n\t"
        "s_waitcnt lgkmcnt(7)\n\t"
        "ds_write_b32 v63, v117\n\t"
        "s_mov_b64 exec, -1\n\t"
        "v_mfma_f32_16x16x32_bf16 v[36:39], a[132:135], v[104:107], v[36:39]\n\t"
        "buffer_load_dwordx4 v135, s[16:19], s43 offen  lds\n\t"
        "v_readlane_b32 s40, v155, 18\n\t"
        "v_mfma_f32_16x16x32_bf16 v[40:43], a[136:139], v[104:107], v[40:43]\n\t"
        "ds_read_b128 v[88:91], v129 offset:12352\n\t"
        "v_readlane_b32 s43, v155, 6\n\t"
        "v_mfma_f32_16x16x32_bf16 v[44:47], a[140:143], v[104:107], v[44:47]\n\t"
        "ds_read_b64_tr_b16 a[180:181], v131 offset:49152\n\t"
        "s_add_u32 m0, s40, s35\n\t"
        "v_mfma_f32_16x16x32_bf16 v[0:3], a[156:159], v[108:111], v[0:3]\n\t"
        "ds_read_b64_tr_b16 a[182:183], v131 offset:49920\n\t"
        "v_and_b32 v78, 0xfffff800, v63\n\t"
        "v_mfma_f32_16x16x32_bf16 v[4:7], a[160:163], v[108:111], v[4:7]\n\t"
        "ds_read_b128 v[64:67], v78 offset:1536\n\t"
        "v_lshlrev_b32 v72, 16, v62\n\t"
        "v_mfma_f32_16x16x32_bf16 v[8:11], a[164:167], v[108:111], v[8:11]\n\t"
        "buffer_load_dwordx4 v136, s[16:19], s43 offen  lds\n\t"
        "s_bitcmp1_b32 s42, 11\n\t"
        "s_cbranch_scc0 .Lx64%=\n\t"
        "v_readlane_b32 s40, v155, 23\n\t"
        "v_readlane_b32 s43, v155, 11\n\t"
        "s_nop 0\n\t"
        "s_add_u32 m0, s40, s35\n\t"
        "s_nop 2\n\t"
        "buffer_load_dwordx4 v137, s[16:19], s43 offen  lds\n\t"
        ".Lx64%=:\n\t"
        "v_mfma_f32_16x16x32_bf16 v[0:3], a[144:147], v[112:115], v[0:3]\n\t"
        "ds_read_b64_tr_b16 a[184:185], v132 offset:49152\n\t"
        "v_readlane_b32 s40, v155, 19\n\t"
        "v_mfma_f32_16x16x32_bf16 v[4:7], a[148:151], v[112:115], v[4:7]\n\t"
        "ds_read_b64_tr_b16 a[186:187], v132 offset:49920\n\t"
        "v_readlane_b32 s43, v155, 7\n\t"
        "v_mfma_f32_16x16x32_bf16 v[8:11], a[152:155], v[112:115], v[8:11]\n\t"
        "ds_read_b64_tr_b16 a[188:189], v133 offset:49152\n\t"
        "s_add_u32 m0, s40, s35\n\t"
        "v_mfma_f32_16x16x32_bf16 v[12:15], a[156:159], v[112:115], v[12:15]\n\t"
        "s_nop 0\n\t"
        "buffer_load_dwordx4 v137, s[16:19], s43 offen  lds\n\t"
        "s_add_u32 m0, s37, 24576\n\t"
        "ds_read_b128 v[92:95], v129 offset:16448\n\t"
        "v_mfma_f32_16x16x32_bf16 v[16:19], a[160:163], v[112:115], v[16:19]\n\t"
        "ds_read_b64_tr_b16 a[190:191], v133 offset:49920\n\t"
        "s_add_u32 s36, s36, s5\n\t"
        "v_mfma_f32_16x16x32_bf16 v[20:23], a[164:167], v[112:115], v[20:23]\n\t"
        "ds_read_b64_tr_b16 a[192:193], v131 offset:55296\n\t"
        "v_and_b32 v73, 0xffff0000, v62\n\t"
        "v_med3_f32 v72, v72, 0, s63\n\t"
        "s_waitcnt lgkmcnt(7)\n\t"
        "v_mfma_f32_16x16x32_bf16 v[0:3], a[168:171], v[84:87], v[0:3]\n\t"
        "ds_read_b64_tr_b16 a[194:195], v131 offset:56064\n\t"
        "v_med3_f32 v73, v73, 0, s63\n\t"
        "v_mfma_f32_16x16x32_bf16 v[4:7], a[172:175], v[84:87], v[4:7]\n\t"
        "buffer_load_dwordx4 v140, s[24:27], s36 offen offset:0 lds\n\t"
        "v_perm_b32 v62, v73, v72, s46\n\t"
        "v_mfma_f32_16x16x32_bf16 v[8:11], a[176:179], v[84:87], v[8:11]\n\t"
        "ds_read_b64_tr_b16 a[196:197], v132 offset:55296\n\t"
        "v_fract_f32 v74, v72\n\t"
        "v_mfma_f32_16x16x32_bf16 v[12:15], a[144:147], v[84:87], v[12:15]\n\t"
        "ds_read_b128 v[96:99], v129 offset:20544\n\t"
        "v_fract_f32 v75, v73\n\t"
        "v_mfma_f32_16x16x32_bf16 v[16:19], a[148:151], v[84:87], v[16:19]\n\t"
        "ds_read_b64_tr_b16 a[198:199], v132 offset:56064\n\t"
        "v_cvt_u32_f32 v76, v72\n\t"
        "v_mfma_f32_16x16x32_bf16 v[20:23], a[152:155], v[84:87], v[20:23]\n\t"
        "buffer_load_dwordx4 v141, s[24:27], s36 offen offset:1024 lds\n\t"
        "v_cvt_u32_f32 v77, v73\n\t"
        "v_mfma_f32_16x16x32_bf16 v[24:27], a[156:159], v[84:87], v[24:27]\n\t"
        "ds_read_b64_tr_b16 a[200:201], v133 offset:55296\n\t"
        "v_cmp_neq_f32 s[64:65], 0, v74\n\t"
        "v_mfma_f32_16x16x32_bf16 v[28:31], a[160:163], v[84:87], v[28:31]\n\t"
        "ds_read_b64_tr_b16 a[202:203], v133 offset:56064\n\t"
        "v_cmp_neq_f32 s[44:45], 0, v75\n\t"
        "v_mfma_f32_16x16x32_bf16 v[32:35], a[164:167], v[84:87], v[32:35]\n\t"
        "ds_read_b128 v[100:103], v129 offset:128\n\t"
        "v_sub_f32 v78, 1.0, v74\n\t"
        "v_mfma_f32_16x16x32_bf16 v[12:15], a[168:171], v[88:91], v[12:15]\n\t"
        "buffer_load_dwordx4 v142, s[24:27], s36 offen offset:2048 lds\n\t"
        "s_add_u32 m0, s37, 36864\n\t"
        "v_mfma_f32_16x16x32_bf16 v[16:19], a[172:175], v[88:91], v[16:19]\n\t"
        "ds_read_b128 v[104:107], v129 offset:4224\n\t"
        "s_add_u32 s36, s36, s39\n\t"
        "v_mfma_f32_16x16x32_bf16 v[20:23], a[176:179], v[88:91], v[20:23]\n\t"
        "v_sub_f32 v79, 1.0, v75\n\t"
        "v_cvt_pk_bf16_f32 v79, v78, v79\n\t"
        "v_mfma_f32_16x16x32_bf16 v[24:27], a[144:147], v[88:91], v[24:27]\n\t"
        "s_waitcnt vmcnt(10)\n\t"
        "s_barrier\n\t"
        "v_mfma_f32_16x16x32_bf16 v[28:31], a[148:151], v[88:91], v[28:31]\n\t"
        "buffer_load_dwordx4 v140, s[24:27], s36 offen offset:0 lds\n\t"
        "v_lshlrev_b32 v78, 16, v79\n\t"
        "v_mfma_f32_16x16x32_bf16 v[32:35], a[152:155], v[88:91], v[32:35]\n\t"
        "ds_read_b64_tr_b16 a[204:205], v131 offset:0\n\t"
        "v_and_b32 v79, 0xffff0000, v79\n\t"
        "v_mfma_f32_16x16x32_bf16 v[36:39], a[156:159], v[88:91], v[36:39]\n\t"
        "ds_read_b64_tr_b16 a[206:207], v131 offset:768\n\t"
        "v_mul_f32 v80, v78, v79\n\t"
        "v_mfma_f32_16x16x32_bf16 v[40:43], a[160:163], v[88:91], v[40:43]\n\t"
        "ds_read_b64_tr_b16 a[208:209], v132 offset:0\n\t"
        "v_mul_f32 v78, v78, v75\n\t"
        "v_mfma_f32_16x16x32_bf16 v[44:47], a[164:167], v[88:91], v[44:47]\n\t"
        "buffer_load_dwordx4 v141, s[24:27], s36 offen offset:1024 lds\n\t"
        "v_mul_f32 v79, v74, v79\n\t"
        "s_waitcnt lgkmcnt(5)\n\t"
        "v_mfma_f32_16x16x32_bf16 v[24:27], a[168:171], v[92:95], v[24:27]\n\t"
        "ds_read_b64_tr_b16 a[210:211], v132 offset:768\n\t"
        "v_mul_f32 v81, v74, v75\n\t"
        "v_mfma_f32_16x16x32_bf16 v[28:31], a[172:175], v[92:95], v[28:31]\n\t"
        "ds_read_b64_tr_b16 a[212:213], v133 offset:0\n\t"
        "v_cndmask_b32 v78, v79, v78, s[44:45]\n\t"
        "v_mfma_f32_16x16x32_bf16 v[32:35], a[176:179], v[92:95], v[32:35]\n\t"
        "ds_read_b64_tr_b16 a[214:215], v133 offset:768\n\t"
        "v_cndmask_b32 v79, 0, v79, s[44:45]\n\t"
        "v_mfma_f32_16x16x32_bf16 v[36:39], a[144:147], v[92:95], v[36:39]\n\t"
        "buffer_load_dwordx4 v142, s[24:27], s36 offen offset:2048 lds\n\t"
        "s_add_u32 m0, s37, 49152\n\t"
        "ds_read_b128 v[108:111], v129 offset:8320\n\t"
        "v_mfma_f32_16x16x32_bf16 v[40:43], a[148:151], v[92:95], v[40:43]\n\t"
        "ds_read_b64_tr_b16 a[216:217], v131 offset:6144\n\t"
        "s_add_u32 s36, s36, s38\n\t"
        "v_mfma_f32_16x16x32_bf16 v[44:47], a[152:155], v[92:95], v[44:47]\n\t"
        "ds_read_b64_tr_b16 a[218:219], v131 offset:6912\n\t"
        "v_cvt_pk_bf16_f32 v70, v80, v78\n\t"
        "v_mfma_f32_16x16x32_bf16 v[36:39], a[168:171], v[96:99], v[36:39]\n\t"
        "s_waitcnt vmcnt(10)\n\t"
        "s_barrier\n\t"
        "v_mfma_f32_16x16x32_bf16 v[40:43], a[172:175], v[96:99], v[40:43]\n\t"
        "buffer_load_dwordx4 v140, s[24:27], s36 offen offset:0 lds\n\t"
        "v_cvt_pk_bf16_f32 v71, v79, v81\n\t"
        "v_mfma_f32_16x16x32_bf16 v[44:47], a[176:179], v[96:99], v[44:47]\n\t"
        "ds_read_b128 v[112:115], v129 offset:12416\n\t"
        "v_pk_max_u16 v78, v62, v64\n\t"
        "s_waitcnt lgkmcnt(7)\n\t"
        "v_mfma_f32_16x16x32_bf16 v[0:3], a[192:195], v[100:103], v[0:3]\n\t"
        "ds_read_b64_tr_b16 a[220:221], v132 offset:6144\n\t"
        "v_pk_min_u16 v78, v78, v65\n\t"
        "v_mfma_f32_16x16x32_bf16 v[4:7], a[196:199], v[100:103], v[4:7]\n\t"
        "ds_read_b64_tr_b16 a[222:223], v132 offset:6912\n\t"
        "v_cmp_ne_u32 vcc, v78, v62\n\t"
        "v_mfma_f32_16x16x32_bf16 v[8:11], a[200:203], v[100:103], v[8:11]\n\t"
        "ds_read_b64_tr_b16 a[224:225], v133 offset:6144\n\t"
        "v_sub_u32 v78, v77, v67\n\t"
        "v_mfma_f32_16x16x32_bf16 v[0:3], a[180:183], v[104:107], v[0:3]\n\t"
        "ds_read_b64_tr_b16 a[226:227], v133 offset:6912\n\t"
        "v_mad_u32_u24 v79, v77, 6, v66\n\t"
        "v_mfma_f32_16x16x32_bf16 v[4:7], a[184:187], v[104:107], v[4:7]\n\t"
        "ds_read_b64_tr_b16 a[228:229], v131 offset:12288\n\t"
        "v_mad_u32_u24 v79, v76, s62, v79\n\t"
        "v_mfma_f32_16x16x32_bf16 v[8:11], a[188:191], v[104:107], v[8:11]\n\t"
        "ds_read_b64_tr_b16 a[230:231], v131 offset:13056\n\t"
        "s_and_b64 vcc, vcc, s[66:67]\n\t"
        "v_mfma_f32_16x16x32_bf16 v[12:15], a[192:195], v[104:107], v[12:15]\n\t"
        "ds_read_b128 v[84:87], v129 offset:16512\n\t"
        "s_or_b64 s[56:57], s[56:57], vcc\n\t"
        "v_mfma_f32_16x16x32_bf16 v[16:19], a[196:199], v[104:107], v[16:19]\n\t"
        "ds_read_b64_tr_b16 a[232:233], v132 offset:12288\n\t"
        "v_bfe_u32 v80, v78, 2, 1\n\t"
        "v_mfma_f32_16x16x32_bf16 v[20:23], a[200:203], v[104:107], v[20:23]\n\t"
        "ds_read_b64_tr_b16 a[234:235], v132 offset:13056\n\t"
        "v_or_b32 v80, v80, v79\n\t"
        "s_waitcnt lgkmcnt(10)\n\t"
        "v_mfma_f32_16x16x32_bf16 v[0:3], a[204:207], v[108:111], v[0:3]\n\t"
        "ds_read_b64_tr_b16 a[236:237], v133 offset:12288\n\t"
        "v_add_u32 v78, 1, v78\n\t"
        "v_mfma_f32_16x16x32_bf16 v[4:7], a[208:211], v[108:111], v[4:7]\n\t"
        "ds_read_b64_tr_b16 a[238:239], v133 offset:13056\n\t"
        "v_bfe_u32 v78, v78, 2, 1\n\t"
        "v_mfma_f32_16x16x32_bf16 v[8:11], a[212:215], v[108:111], v[8:11]\n\t"
        "ds_read_b64_tr_b16 a[240:241], v131 offset:18432\n\t"
        "v_add3_u32 v78, v79, v78, 6\n\t"
        "v_mfma_f32_16x16x32_bf16 v[12:15], a[180:183], v[108:111], v[12:15]\n\t"
        "ds_read_b128 v[88:91], v130 offset:0\n\t"
        "v_cndmask_b32 v78, v80, v78, s[44:45]\n\t"
        "v_mfma_f32_16x16x32_bf16 v[16:19], a[184:187], v[108:111], v[16:19]\n\t"
        "ds_read_b64_tr_b16 a[242:243], v131 offset:19200\n\t"
        "v_cndmask_b32 v79, 0, v119, s[64:65]\n\t"
        "v_mfma_f32_16x16x32_bf16 v[20:23], a[188:191], v[108:111], v[20:23]\n\t"
        "ds_read_b64_tr_b16 a[244:245], v132 offset:18432\n\t"
        "v_add_u32 v81, v80, v79\n\t"
        "v_mfma_f32_16x16x32_bf16 v[24:27], a[192:195], v[108:111], v[24:27]\n\t"
        "ds_read_b128 v[92:95], v130 offset:4096\n\t"
        "v_add_u32 v82, v78, v79\n\t"
        "v_mfma_f32_16x16x32_bf16 v[28:31], a[196:199], v[108:111], v[28:31]\n\t"
        "ds_read_b64_tr_b16 a[246:247], v132 offset:19200\n\t"
        "ds_read_b64_tr_b16 a[248:249], v133 offset:18432\n\t"
        "v_cndmask_b32 v78, v81, v78, s[44:45]\n\t"
        "v_mfma_f32_16x16x32_bf16 v[32:35], a[200:203], v[108:111], v[32:35]\n\t"
        "buffer_load_dwordx4 v141, s[24:27], s36 offen offset:1024 lds\n\t"
        "v_lshl_or_b32 v68, v78, 16, v80\n\t"
        "s_waitcnt lgkmcnt(9)\n\t"
        "v_mfma_f32_16x16x32_bf16 v[12:15], a[204:207], v[112:115], v[12:15]\n\t"
        "ds_read_b64_tr_b16 a[250:251], v133 offset:19200\n\t"
        "v_lshl_or_b32 v69, v82, 16, v81\n\t"
        "v_mfma_f32_16x16x32_bf16 v[16:19], a[208:211], v[112:115], v[16:19]\n\t"
        "ds_write_b128 v118, v[68:71]\n\t"
        "v_mfma_f32_16x16x32_bf16 v[20:23], a[212:215], v[112:115], v[20:23]\n\t"
        "s_cmp_gt_u32 s3, 32\n\t"
        "s_cselect_b32 s8, 2, 0\n\t"
        "v_mfma_f32_16x16x32_bf16 v[24:27], a[180:183], v[112:115], v[24:27]\n\t"
        "s_cmp_lg_u64 s[56:57], 0\n\t"
        "s_addc_u32 s8, s8, s6\n\t"
        "v_mfma_f32_16x16x32_bf16 v[28:31], a[184:187], v[112:115], v[28:31]\n\t"
        "ds_read_b128 v[160:163], v185 offset:0\n\t"
        "v_mov_b32 v151, s8\n\t"
        "v_mfma_f32_16x16x32_bf16 v[32:35], a[188:191], v[112:115], v[32:35]\n\t"
        "buffer_load_dwordx4 v142, s[24:27], s36 offen offset:2048 lds\n\t"
        "s_waitcnt vmcnt(3)\n\t"
        "s_barrier\n\t"
        "v_mfma_f32_16x16x32_bf16 v[36:39], a[192:195], v[112:115], v[36:39]\n\t"
        "s_mov_b64 exec, 1\n\t"
        "ds_write_b32 v153, v151\n\t"
        "s_mov_b64 exec, -1\n\t"
        "v_mfma_f32_16x16x32_bf16 v[40:43], a[196:199], v[112:115], v[40:43]\n\t"
        "ds_read_b128 v[96:99], v130 offset:8192\n\t"
        "v_xor_b32 v154, 32, v186\n\t"
        "v_mfma_f32_16x16x32_bf16 v[44:47], a[200:203], v[112:115], v[44:47]\n\t"
        "ds_read_b64_tr_b16 a[252:253], v131 offset:24576\n\t"
        "v_mfma_f32_16x16x32_bf16 v[24:27], a[204:207], v[84:87], v[24:27]\n\t"
        "ds_read_b64_tr_b16 a[254:255], v131 offset:25344\n\t"
        "v_mfma_f32_16x16x32_bf16 v[28:31], a[208:211], v[84:87], v[28:31]\n\t"
        "ds_read_b64_tr_b16 v[188:189], v132 offset:24576\n\t"
        "v_mfma_f32_16x16x32_bf16 v[32:35], a[212:215], v[84:87], v[32:35]\n\t"
        "ds_read_b64_tr_b16 v[190:191], v132 offset:25344\n\t"
        "v_mfma_f32_16x16x32_bf16 v[36:39], a[180:183], v[84:87], v[36:39]\n\t"
        "ds_read_b128 v[100:103], v130 offset:12288\n\t"
        "v_mfma_f32_16x16x32_bf16 v[40:43], a[184:187], v[84:87], v[40:43]\n\t"
        "ds_read_b64_tr_b16 v[192:193], v133 offset:24576\n\t"
        "s_waitcnt lgkmcnt(8)\n\t"
        "v_lshlrev_b32_sdwa v157, 5, v160 dst_sel:DWORD dst_unused:UNUSED_PAD src0_sel:DWORD src1_sel:WORD_0\n\t"
        "v_mfma_f32_16x16x32_bf16 v[44:47], a[188:191], v[84:87], v[44:47]\n\t"
        "ds_read_b64_tr_b16 v[194:195], v133 offset:25344\n\t"
        "v_lshlrev_b32_sdwa v158, 5, v160 dst_sel:DWORD dst_unused:UNUSED_PAD src0_sel:DWORD src1_sel:WORD_1\n\t"
        "v_mfma_f32_16x16x32_bf16 v[0:3], a[228:231], v[88:91], v[0:3]\n\t"
        "ds_read_b64_tr_b16 v[196:197], v131 offset:30720\n\t"
        "v_or_b32 v157, v184, v157\n\t"
        "v_mfma_f32_16x16x32_bf16 v[4:7], a[232:235], v[88:91], v[4:7]\n\t"
        "ds_read_b64_tr_b16 v[60:61], v157 offset:0\n\t"
        "v_or_b32 v158, v184, v158\n\t"
        "v_mfma_f32_16x16x32_bf16 v[8:11], a[236:239], v[88:91], v[8:11]\n\t"
        "ds_read_b64_tr_b16 v[62:63], v158 offset:0\n\t"
        "v_xor_b32 v159, 32, v157\n\t"
        "v_mfma_f32_16x16x32_bf16 v[0:3], a[216:219], v[92:95], v[0:3]\n\t"
        "ds_read_b64_tr_b16 v[64:65], v159 offset:0\n\t"
        "v_xor_b32 v152, 32, v158\n\t"
        "v_mfma_f32_16x16x32_bf16 v[4:7], a[220:223], v[92:95], v[4:7]\n\t"
        "ds_read_b64_tr_b16 v[66:67], v152 offset:0\n\t"
        "v_perm_b32 v164, v162, v162, v168\n\t"
        "v_mfma_f32_16x16x32_bf16 v[8:11], a[224:227], v[92:95], v[8:11]\n\t"
        "ds_read_b64_tr_b16 v[68:69], v157 offset:64\n\t"
        "v_perm_b32 v165, v162, v162, v169\n\t"
        "v_mfma_f32_16x16x32_bf16 v[12:15], a[228:231], v[92:95], v[12:15]\n\t"
        "ds_read_b64_tr_b16 v[70:71], v158 offset:64\n\t"
        "v_perm_b32 v166, v162, v162, v170\n\t"
        "v_mfma_f32_16x16x32_bf16 v[16:19], a[232:235], v[92:95], v[16:19]\n\t"
        "ds_read_b64_tr_b16 v[72:73], v159 offset:64\n\t"
        "v_perm_b32 v167, v162, v162, v171\n\t"
        "v_mfma_f32_16x16x32_bf16 v[20:23], a[236:239], v[92:95], v[20:23]\n\t"
        "ds_read_b64_tr_b16 v[74:75], v152 offset:64\n\t"
        "s_waitcnt lgkmcnt(10)\n\t"
        "v_mfma_f32_16x16x32_bf16 v[0:3], a[240:243], v[96:99], v[0:3]\n\t"
        "ds_read_b64_tr_b16 v[76:77], v157 offset:128\n\t"
        "ds_read_b128 v[104:107], v130 offset:16384\n\t"
        "v_lshlrev_b32_sdwa v157, 5, v161 dst_sel:DWORD dst_unused:UNUSED_PAD src0_sel:DWORD src1_sel:WORD_0\n\t"
        "v_mfma_f32_16x16x32_bf16 v[4:7], a[244:247], v[96:99], v[4:7]\n\t"
        "ds_read_b64_tr_b16 v[78:79], v158 offset:128\n\t"
        "v_lshlrev_b32_sdwa v158, 5, v161 dst_sel:DWORD dst_unused:UNUSED_PAD src0_sel:DWORD src1_sel:WORD_1\n\t"
        "v_mfma_f32_16x16x32_bf16 v[8:11], a[248:251], v[96:99], v[8:11]\n\t"
        "ds_read_b64_tr_b16 v[80:81], v159 offset:128\n\t"
        "v_or_b32 v157, v184, v157\n\t"
        "s_waitcnt lgkmcnt(10)\n\t"
        "v_mfma_f32_16x16x32_bf16 v[48:51], v[60:63], v[164:167], 0\n\t"
        "ds_read_b64_tr_b16 v[82:83], v152 offset:128\n\t"
        "v_or_b32 v158, v184, v158\n\t"
        "v_mfma_f32_16x16x32_bf16 v[12:15], a[216:219], v[96:99], v[12:15]\n\t"
        "ds_read_b64_tr_b16 v[60:61], v157 offset:0\n\t"
        "v_xor_b32 v159, 32, v157\n\t"
        "s_waitcnt lgkmcnt(10)\n\t"
        "v_mfma_f32_16x16x32_bf16 v[52:55], v[64:67], v[164:167], 0\n\t"
        "ds_read_b64_tr_b16 v[62:63], v158 offset:0\n\t"
        "v_xor_b32 v152, 32, v158\n\t"
        "v_mfma_f32_16x16x32_bf16 v[16:19], a[220:223], v[96:99], v[16:19]\n\t"
        "ds_read_b64_tr_b16 v[64:65], v159 offset:0\n\t"
        "s_waitcnt lgkmcnt(10)\n\t"
        "v_mfma_f32_16x16x32_bf16 v[56:59], v[68:71], v[164:167], 0\n\t"
        "ds_read_b64_tr_b16 v[66:67], v152 offset:0\n\t"
        "v_mfma_f32_16x16x32_bf16 v[20:23], a[224:227], v[96:99], v[20:23]\n\t"
        "ds_read_b64_tr_b16 v[68:69], v157 offset:64\n\t"
        "s_waitcnt lgkmcnt(10)\n\t"
        "v_mfma_f32_16x16x32_bf16 v[116:119], v[72:75], v[164:167], 0\n\t"
        "s_waitcnt vmcnt(0)\n\t"
        "s_barrier\n\t"
        "v_mfma_f32_16x16x32_bf16 v[24:27], a[228:231], v[96:99], v[24:27]\n\t"
        "ds_read_b64_tr_b16 v[70:71], v158 offset:64\n\t"
        "s_waitcnt lgkmcnt(8)\n\t"
        "v_mfma_f32_16x16x32_bf16 v[120:123], v[76:79], v[164:167], 0\n\t"
        "ds_read_b64_tr_b16 v[72:73], v159 offset:64\n\t"
        "v_mfma_f32_16x16x32_bf16 v[28:31], a[232:235], v[96:99], v[28:31]\n\t"
        "ds_read_b64_tr_b16 v[74:75], v152 offset:64\n\t"
        "s_waitcnt lgkmcnt(8)\n\t"
        "v_mfma_f32_16x16x32_bf16 v[124:127], v[80:83], v[164:167], 0\n\t"
        "ds_read_b64_tr_b16 v[76:77], v157 offset:128\n\t"
        "v_perm_b32 v164, v163, v163, v168\n\t"
        "v_mfma_f32_16x16x32_bf16 v[32:35], a[236:239], v[96:99], v[32:35]\n\t"
        "ds_read_b128 v[108:111], v129 offset:20608\n\t"
        "v_perm_b32 v165, v163, v163, v169\n\t"
        "v_mfma_f32_16x16x32_bf16 v[12:15], a[240:243], v[100:103], v[12:15]\n\t"
        "ds_read_b64_tr_b16 v[78:79], v158 offset:128\n\t"
        "v_perm_b32 v166, v163, v163, v170\n\t"
        "v_mfma_f32_16x16x32_bf16 v[16:19], a[244:247], v[100:103], v[16:19]\n\t"
        "ds_read_b64_tr_b16 v[80:81], v159 offset:128\n\t"
        "v_perm_b32 v167, v163, v163, v171\n\t"
        "v_mfma_f32_16x16x32_bf16 v[20:23], a[248:251], v[100:103], v[20:23]\n\t"
        "ds_read_b128 v[112:115], v130 offset:20480\n\t"
        "s_waitcnt lgkmcnt(9)\n\t"
        "v_mfma_f32_16x16x32_bf16 v[48:51], v[60:63], v[164:167], v[48:51]\n\t"
        "ds_read_b64_tr_b16 v[82:83], v152 offset:128\n\t"
        "v_mfma_f32_16x16x32_bf16 v[24:27], a[216:219], v[100:103], v[24:27]\n\t"
        "ds_read_b128 v[160:163], v185 offset:256\n\t"
        "ds_read_b64_tr_b16 v[198:199], v131 offset:31488\n\t"
        "v_mfma_f32_16x16x32_bf16 v[52:55], v[64:67], v[164:167], v[52:55]\n\t"
        "ds_read_b64_tr_b16 v[200:201], v132 offset:30720\n\t"
        "ds_read_b64_tr_b16 v[202:203], v132 offset:31488\n\t"
        "v_mfma_f32_16x16x32_bf16 v[28:31], a[220:223], v[100:103], v[28:31]\n\t"
        "ds_read_b64_tr_b16 v[204:205], v133 offset:30720\n\t"
        "ds_read_b64_tr_b16 v[206:207], v133 offset:31488\n\t"
        "s_waitcnt lgkmcnt(12)\n\t"
        "v_mfma_f32_16x16x32_bf16 v[56:59], v[68:71], v[164:167], v[56:59]\n\t"
        "ds_read_b128 v[84:87], v130 offset:64\n\t"
        "v_cvt_pk_bf16_f32 v48, v48, v49\n\t"
        "v_mfma_f32_16x16x32_bf16 v[32:35], a[224:227], v[100:103], v[32:35]\n\t"
        "ds_read_b128 v[88:91], v130 offset:4160\n\t"
        "v_cvt_pk_bf16_f32 v49, v50, v51\n\t"
        "v_mfma_f32_16x16x32_bf16 v[116:119], v[72:75], v[164:167], v[116:119]\n\t"
        "ds_write_b64 v186, v[48:49] offset:0\n\t"
        "v_mfma_f32_16x16x32_bf16 v[36:39], a[228:231], v[100:103], v[36:39]\n\t"
        "ds_read_b64_tr_b16 v[208:209], v131 offset:36864\n\t"
        "v_cvt_pk_bf16_f32 v52, v52, v53\n\t"
        "s_waitcnt lgkmcnt(12)\n\t"
        "v_mfma_f32_16x16x32_bf16 v[120:123], v[76:79], v[164:167], v[120:123]\n\t"
        "ds_read_b64_tr_b16 v[210:211], v131 offset:37632\n\t"
        "v_cvt_pk_bf16_f32 v53, v54, v55\n\t"
        "v_mfma_f32_16x16x32_bf16 v[40:43], a[232:235], v[100:103], v[40:43]\n\t"
        "ds_write_b64 v154, v[52:53] offset:0\n\t"
        "s_waitcnt lgkmcnt(11)\n\t"
        "v_mfma_f32_16x16x32_bf16 v[124:127], v[80:83], v[164:167], v[124:127]\n\t"
        "ds_read_b64_tr_b16 v[212:213], v132 offset:36864\n\t"
        "v_cvt_pk_bf16_f32 v56, v56, v57\n\t"
        "v_mfma_f32_16x16x32_bf16 v[44:47], a[236:239], v[100:103], v[44:47]\n\t"
        "ds_read_b64_tr_b16 v[214:215], v132 offset:37632\n\t"
        "v_cvt_pk_bf16_f32 v57, v58, v59\n\t"
        "v_mfma_f32_16x16x32_bf16 v[24:27], a[240:243], v[104:107], v[24:27]\n\t"
        "ds_write_b64 v186, v[56:57] offset:64\n\t"
        "v_mfma_f32_16x16x32_bf16 v[28:31], a[244:247], v[104:107], v[28:31]\n\t"
        "ds_read_b64_tr_b16 v[216:217], v133 offset:36864\n\t"
        "v_cvt_pk_bf16_f32 v116, v116, v117\n\t"
        "v_mfma_f32_16x16x32_bf16 v[32:35], a[248:251], v[104:107], v[32:35]\n\t"
        "ds_read_b64_tr_b16 v[218:219], v133 offset:37632\n\t"
        "v_cvt_pk_bf16_f32 v117, v118, v119\n\t"
        "v_mfma_f32_16x16x32_bf16 v[36:39], a[216:219], v[104:107], v[36:39]\n\t"
        "ds_write_b64 v154, v[116:117] offset:64\n\t"
        "v_mfma_f32_16x16x32_bf16 v[40:43], a[220:223], v[104:107], v[40:43]\n\t"
        "ds_read_b128 v[92:95], v130 offset:8256\n\t"
        "v_cvt_pk_bf16_f32 v120, v120, v121\n\t"
        "v_mfma_f32_16x16x32_bf16 v[44:47], a[224:227], v[104:107], v[44:47]\n\t"
        "ds_read_b64_tr_b16 v[220:221], v131 offset:43008\n\t"
        "v_cvt_pk_bf16_f32 v121, v122, v123\n\t"
        "v_mfma_f32_16x16x32_bf16 v[36:39], a[204:207], v[108:111], v[36:39]\n\t"
        "ds_write_b64 v186, v[120:121] offset:128\n\t"
        "v_mfma_f32_16x16x32_bf16 v[40:43], a[208:211], v[108:111], v[40:43]\n\t"
        "ds_read_b64_tr_b16 v[222:223], v131 offset:43776\n\t"
        "v_cvt_pk_bf16_f32 v124, v124, v125\n\t"
        "v_mfma_f32_16x16x32_bf16 v[44:47], a[212:215], v[108:111], v[44:47]\n\t"
        "ds_read_b64_tr_b16 v[224:225], v132 offset:43008\n\t"
        "v_cvt_pk_bf16_f32 v125, v126, v127\n\t"
        "v_mfma_f32_16x16x32_bf16 v[36:39], a[240:243], v[112:115], v[36:39]\n\t"
        "ds_write_b64 v154, v[124:125] offset:128\n\t"
        "v_mfma_f32_16x16x32_bf16 v[40:43], a[244:247], v[112:115], v[40:43]\n\t"
        "ds_read_b128 v[96:99], v130 offset:12352\n\t"
        "v_lshlrev_b32_sdwa v157, 5, v160 dst_sel:DWORD dst_unused:UNUSED_PAD src0_sel:DWORD src1_sel:WORD_0\n\t"
        "v_mfma_f32_16x16x32_bf16 v[44:47], a[248:251], v[112:115], v[44:47]\n\t"
        "ds_read_b64_tr_b16 v[226:227], v132 offset:43776\n\t"
        "v_lshlrev_b32_sdwa v158, 5, v160 dst_sel:DWORD dst_unused:UNUSED_PAD src0_sel:DWORD src1_sel:WORD_1\n\t"
        "s_waitcnt lgkmcnt(9)\n\t"
        "v_mfma_f32_16x16x32_bf16 v[0:3], v[196:199], v[84:87], v[0:3]\n\t"
        "ds_read_b64_tr_b16 v[228:229], v133 offset:43008\n\t"
        "v_or_b32 v157, v184, v157\n\t"
        "v_mfma_f32_16x16x32_bf16 v[4:7], v[200:203], v[84:87], v[4:7]\n\t"
        "ds_read_b64_tr_b16 v[60:61], v157 offset:0\n\t"
        "v_or_b32 v158, v184, v158\n\t"
        "v_mfma_f32_16x16x32_bf16 v[8:11], v[204:207], v[84:87], v[8:11]\n\t"
        "ds_read_b64_tr_b16 v[62:63], v158 offset:0\n\t"
        "v_xor_b32 v159, 32, v157\n\t"
        "v_mfma_f32_16x16x32_bf16 v[0:3], a[252:255], v[88:91], v[0:3]\n\t"
        "ds_read_b64_tr_b16 v[64:65], v159 offset:0\n\t"
        "v_xor_b32 v152, 32, v158\n\t"
        "v_mfma_f32_16x16x32_bf16 v[4:7], v[188:191], v[88:91], v[4:7]\n\t"
        "ds_read_b64_tr_b16 v[66:67], v152 offset:0\n\t"
        "v_perm_b32 v164, v162, v162, v168\n\t"
        "v_mfma_f32_16x16x32_bf16 v[8:11], v[192:195], v[88:91], v[8:11]\n\t"
        "ds_read_b64_tr_b16 v[68:69], v157 offset:64\n\t"
        "v_perm_b32 v165, v162, v162, v169\n\t"
        "v_mfma_f32_16x16x32_bf16 v[12:15], v[196:199], v[88:91], v[12:15]\n\t"
        "ds_read_b64_tr_b16 v[70:71], v158 offset:64\n\t"
        "ds_read_b128 v[100:103], v130 offset:16448\n\t"
        "v_perm_b32 v166, v162, v162, v170\n\t"
        "v_mfma_f32_16x16x32_bf16 v[16:19], v[200:203], v[88:91], v[16:19]\n\t"
        "ds_read_b64_tr_b16 v[72:73], v159 offset:64\n\t"
        "ds_read_b64_tr_b16 v[230:231], v133 offset:43776\n\t"
        "v_perm_b32 v167, v162, v162, v171\n\t"
        "v_mfma_f32_16x16x32_bf16 v[20:23], v[204:207], v[88:91], v[20:23]\n\t"
        "ds_read_b64_tr_b16 v[74:75], v152 offset:64\n\t"
        "s_waitcnt lgkmcnt(11)\n\t"
        "v_mfma_f32_16x16x32_bf16 v[0:3], v[208:211], v[92:95], v[0:3]\n\t"
        "ds_read_b64_tr_b16 v[76:77], v157 offset:128\n\t"
        "v_lshlrev_b32_sdwa v157, 5, v161 dst_sel:DWORD dst_unused:UNUSED_PAD src0_sel:DWORD src1_sel:WORD_0\n\t"
        "v_mfma_f32_16x16x32_bf16 v[4:7], v[212:215], v[92:95], v[4:7]\n\t"
        "ds_read_b64_tr_b16 v[78:79], v158 offset:128\n\t"
        "v_lshlrev_b32_sdwa v158, 5, v161 dst_sel:DWORD dst_unused:UNUSED_PAD src0_sel:DWORD src1_sel:WORD_1\n\t"
        "v_mfma_f32_16x16x32_bf16 v[8:11], v[216:219], v[92:95], v[8:11]\n\t"
        "ds_read_b64_tr_b16 v[80:81], v159 offset:128\n\t"
        "v_or_b32 v157, v184, v157\n\t"
        "s_waitcnt lgkmcnt(11)\n\t"
        "v_mfma_f32_16x16x32_bf16 v[48:51], v[60:63], v[164:167], 0\n\t"
        "ds_read_b64_tr_b16 v[82:83], v152 offset:128\n\t"
        "v_or_b32 v158, v184, v158\n\t"
        "v_mfma_f32_16x16x32_bf16 v[12:15], a[252:255], v[92:95], v[12:15]\n\t"
        "ds_read_b64_tr_b16 v[60:61], v157 offset:0\n\t"
        "v_xor_b32 v159, 32, v157\n\t"
        "s_waitcnt lgkmcnt(11)\n\t"
        "v_mfma_f32_16x16x32_bf16 v[52:55], v[64:67], v[164:167], 0\n\t"
        "ds_read_b64_tr_b16 v[62:63], v158 offset:0\n\t"
        "v_xor_b32 v152, 32, v158\n\t"
        "v_mfma_f32_16x16x32_bf16 v[16:19], v[188:191], v[92:95], v[16:19]\n\t"
        "ds_read_b64_tr_b16 v[64:65], v159 offset:0\n\t"
        "s_waitcnt lgkmcnt(10)\n\t"
        "v_mfma_f32_16x16x32_bf16 v[56:59], v[68:71], v[164:167], 0\n\t"
        "ds_read_b64_tr_b16 v[66:67], v152 offset:0\n\t"
        "v_mfma_f32_16x16x32_bf16 v[20:23], v[192:195], v[92:95], v[20:23]\n\t"
        "ds_read_b64_tr_b16 v[68:69], v157 offset:64\n\t"
        "s_waitcnt lgkmcnt(9)\n\t"
        "v_mfma_f32_16x16x32_bf16 v[116:119], v[72:75], v[164:167], 0\n\t"
        "ds_read_b64_tr_b16 v[70:71], v158 offset:64\n\t"
        "v_mfma_f32_16x16x32_bf16 v[24:27], v[196:199], v[92:95], v[24:27]\n\t"
        "ds_read_b64_tr_b16 v[72:73], v159 offset:64\n\t"
        "s_waitcnt lgkmcnt(9)\n\t"
        "v_mfma_f32_16x16x32_bf16 v[120:123], v[76:79], v[164:167], 0\n\t"
        "ds_read_b64_tr_b16 v[74:75], v152 offset:64\n\t"
        "v_mfma_f32_16x16x32_bf16 v[28:31], v[200:203], v[92:95], v[28:31]\n\t"
        "ds_read_b64_tr_b16 v[76:77], v157 offset:128\n\t"
        "s_waitcnt lgkmcnt(9)\n\t"
        "v_mfma_f32_16x16x32_bf16 v[124:127], v[80:83], v[164:167], 0\n\t"
        "ds_read_b64_tr_b16 v[78:79], v158 offset:128\n\t"
        "v_perm_b32 v164, v163, v163, v168\n\t"
        "v_mfma_f32_16x16x32_bf16 v[32:35], v[204:207], v[92:95], v[32:35]\n\t"
        "ds_read_b128 v[104:107], v130 offset:20544\n\t"
        ".LpX1%=:\n\t"
        "s_cbranch_execz .LrX%=\n\t"
        "v_perm_b32 v165, v163, v163, v169\n\t"
        "v_mfma_f32_16x16x32_bf16 v[12:15], v[208:211], v[96:99], v[12:15]\n\t"
        "ds_read_b64_tr_b16 v[80:81], v159 offset:128\n\t"
        "ds_read_b64_tr_b16 v[232:233], v131 offset:49152\n\t"
        "v_perm_b32 v166, v163, v163, v170\n\t"
        "v_mfma_f32_16x16x32_bf16 v[16:19], v[212:215], v[96:99], v[16:19]\n\t"
        "ds_read_b64_tr_b16 v[82:83], v152 offset:128\n\t"
        "ds_read_b64_tr_b16 v[234:235], v131 offset:49920\n\t"
        "v_perm_b32 v167, v163, v163, v171\n\t"
        "v_mfma_f32_16x16x32_bf16 v[20:23], v[216:219], v[96:99], v[20:23]\n\t"
        "ds_read_b64_tr_b16 v[236:237], v132 offset:49152\n\t"
        "ds_read_b64_tr_b16 v[238:239], v132 offset:49920\n\t"
        "s_waitcnt lgkmcnt(11)\n\t"
        "v_mfma_f32_16x16x32_bf16 v[48:51], v[60:63], v[164:167], v[48:51]\n\t"
        "ds_read_b64_tr_b16 v[240:241], v133 offset:49152\n\t"
        "ds_read_b64_tr_b16 v[242:243], v133 offset:49920\n\t"
        "v_mfma_f32_16x16x32_bf16 v[24:27], a[252:255], v[96:99], v[24:27]\n\t"
        "ds_read_b128 v[108:111], v130 offset:128\n\t"
        "v_mfma_f32_16x16x32_bf16 v[52:55], v[64:67], v[164:167], v[52:55]\n\t"
        "ds_read_b128 v[112:115], v130 offset:4224\n\t"
        "v_mfma_f32_16x16x32_bf16 v[28:31], v[188:191], v[96:99], v[28:31]\n\t"
        "ds_read_b64_tr_b16 v[244:245], v131 offset:55296\n\t"
        "v_mfma_f32_16x16x32_bf16 v[56:59], v[68:71], v[164:167], v[56:59]\n\t"
        "ds_read_b64_tr_b16 v[246:247], v131 offset:56064\n\t"
        "v_cvt_pk_bf16_f32 v48, v48, v49\n\t"
        "v_mfma_f32_16x16x32_bf16 v[32:35], v[192:195], v[96:99], v[32:35]\n\t"
        "ds_read_b64_tr_b16 v[248:249], v132 offset:55296\n\t"
        "v_cvt_pk_bf16_f32 v49, v50, v51\n\t"
        "s_waitcnt lgkmcnt(12)\n\t"
        "v_mfma_f32_16x16x32_bf16 v[116:119], v[72:75], v[164:167], v[116:119]\n\t"
        "ds_write_b64 v186, v[48:49] offset:12288\n\t"
        "v_mfma_f32_16x16x32_bf16 v[36:39], v[196:199], v[96:99], v[36:39]\n\t"
        "ds_read_b64_tr_b16 v[250:251], v132 offset:56064\n\t"
        "v_cvt_pk_bf16_f32 v52, v52, v53\n\t"
        "v_mfma_f32_16x16x32_bf16 v[120:123], v[76:79], v[164:167], v[120:123]\n\t"
        "ds_read_b64_tr_b16 v[252:253], v133 offset:55296\n\t"
        "v_cvt_pk_bf16_f32 v53, v54, v55\n\t"
        "v_mfma_f32_16x16x32_bf16 v[40:43], v[200:203], v[96:99], v[40:43]\n\t"
        "ds_write_b64 v154, v[52:53] offset:12288\n\t"
        "s_waitcnt lgkmcnt(9)\n\t"
        "v_mfma_f32_16x16x32_bf16 v[124:127], v[80:83], v[164:167], v[124:127]\n\t"
        "ds_read_b64_tr_b16 v[254:255], v133 offset:56064\n\t"
        "v_cvt_pk_bf16_f32 v56, v56, v57\n\t"
        "v_mfma_f32_16x16x32_bf16 v[44:47], v[204:207], v[96:99], v[44:47]\n\t"
        "ds_read_b128 v[84:87], v130 offset:8320\n\t"
        "v_cvt_pk_bf16_f32 v57, v58, v59\n\t"
        "v_mfma_f32_16x16x32_bf16 v[24:27], v[208:211], v[100:103], v[24:27]\n\t"
        "ds_write_b64 v186, v[56:57] offset:12352\n\t"
        "v_mfma_f32_16x16x32_bf16 v[28:31], v[212:215], v[100:103], v[28:31]\n\t"
        "v_cvt_pk_bf16_f32 v116, v116, v117\n\t"
        "v_cvt_pk_bf16_f32 v117, v118, v119\n\t"
        "v_mfma_f32_16x16x32_bf16 v[32:35], v[216:219], v[100:103], v[32:35]\n\t"
        "ds_write_b64 v154, v[116:117] offset:12352\n\t"
        "v_mfma_f32_16x16x32_bf16 v[36:39], a[252:255], v[100:103], v[36:39]\n\t"
        "v_cvt_pk_bf16_f32 v120, v120, v121\n\t"
        "v_cvt_pk_bf16_f32 v121, v122, v123\n\t"
        "v_mfma_f32_16x16x32_bf16 v[40:43], v[188:191], v[100:103], v[40:43]\n\t"
        "ds_write_b64 v186, v[120:121] offset:12416\n\t"
        "v_mfma_f32_16x16x32_bf16 v[44:47], v[192:195], v[100:103], v[44:47]\n\t"
        "v_cvt_pk_bf16_f32 v124, v124, v125\n\t"
        "v_cvt_pk_bf16_f32 v125, v126, v127\n\t"
        "v_mfma_f32_16x16x32_bf16 v[36:39], v[208:211], v[104:107], v[36:39]\n\t"
        "ds_write_b64 v154, v[124:125] offset:12416\n\t"
        "v_mfma_f32_16x16x32_bf16 v[40:43], v[212:215], v[104:107], v[40:43]\n\t"
        "ds_read_b128 v[88:91], v130 offset:12416\n\t"
        "v_mfma_f32_16x16x32_bf16 v[44:47], v[216:219], v[104:107], v[44:47]\n\t"
        "s_waitcnt lgkmcnt(5)\n\t"
        "v_mfma_f32_16x16x32_bf16 v[0:3], v[232:235], v[108:111], v[0:3]\n\t"
        "v_mfma_f32_16x16x32_bf16 v[4:7], v[236:239], v[108:111], v[4:7]\n\t"
        "v_mfma_f32_16x16x32_bf16 v[8:11], v[240:243], v[108:111], v[8:11]\n\t"
        "v_mfma_f32_16x16x32_bf16 v[0:3], v[220:223], v[112:115], v[0:3]\n\t"
        "s_waitcnt lgkmcnt(1)\n\t"
        "s_barrier\n\t"
        "v_mfma_f32_16x16x32_bf16 v[4:7], v[224:227], v[112:115], v[4:7]\n\t"
        "ds_read_b128 v[124:127], v182 offset:0\n\t"
        "v_mfma_f32_16x16x32_bf16 v[8:11], v[228:231], v[112:115], v[8:11]\n\t"
        "ds_read_b128 v[132:135], v187 offset:0\n\t"
        "v_mfma_f32_16x16x32_bf16 v[12:15], v[232:235], v[112:115], v[12:15]\n\t"
        "ds_read_b128 v[136:139], v187 offset:16\n\t"
        "v_mfma_f32_16x16x32_bf16 v[16:19], v[236:239], v[112:115], v[16:19]\n\t"
        "ds_read_b32 v140, v187 offset:32\n\t"
        "v_mfma_f32_16x16x32_bf16 v[20:23], v[240:243], v[112:115], v[20:23]\n\t"
        "ds_read_b128 v[92:95], v130 offset:16512\n\t"
        "v_mfma_f32_16x16x32_bf16 v[0:3], v[244:247], v[84:87], v[0:3]\n\t"
        "v_mfma_f32_16x16x32_bf16 v[4:7], v[248:251], v[84:87], v[4:7]\n\t"
        "v_mfma_f32_16x16x32_bf16 v[8:11], v[252:255], v[84:87], v[8:11]\n\t"
        "v_mfma_f32_16x16x32_bf16 v[12:15], v[220:223], v[84:87], v[12:15]\n\t"
        "ds_read_b128 v[96:99], v130 offset:20608\n\t"
        "v_mfma_f32_16x16x32_bf16 v[16:19], v[224:227], v[84:87], v[16:19]\n\t"
        "s_waitcnt lgkmcnt(5)\n\t"
        "v_or3_b32 v124, v124, v125, v126\n\t"
        "v_or_b32 v124, v124, v127\n\t"
        "v_mfma_f32_16x16x32_bf16 v[20:23], v[228:231], v[84:87], v[20:23]\n\t"
        "v_readfirstlane_b32 s6, v124\n\t"
        "v_mfma_f32_16x16x32_bf16 v[24:27], v[232:235], v[84:87], v[24:27]\n\t"
        "s_nop 0\n\t"
        "s_bitcmp1_b32 s6, 1\n\t"
        "s_cbranch_scc1 .Lx66%=\n\t"
        ".Lx67%=:\n\t"
        "v_mfma_f32_16x16x32_bf16 v[28:31], v[236:239], v[84:87], v[28:31]\n\t"
        "ds_read_b128 v[160:163], v185 offset:512\n\t"
        "s_waitcnt lgkmcnt(4)\n\t"
        "v_xor_b32 v132, v181, v132\n\t"
        "v_add_u32 v151, s9, v172\n\t"
        "v_mfma_f32_16x16x32_bf16 v[32:35], v[240:243], v[84:87], v[32:35]\n\t"
        "v_max_u32 v151, v151, v132\n\t"
        "v_xor_b32 v133, v181, v133\n\t"
        "v_add_u32 v183, s9, v173\n\t"
        "v_mfma_f32_16x16x32_bf16 v[12:15], v[244:247], v[88:91], v[12:15]\n\t"
        "ds_read_b128 v[100:103], v151 offset:0\n\t"
        "v_max_u32 v183, v183, v133\n\t"
        "v_xor_b32 v134, v181, v134\n\t"
        "v_mfma_f32_16x16x32_bf16 v[16:19], v[248:251], v[88:91], v[16:19]\n\t"
        "ds_read_b128 v[104:107], v151 offset:64\n\t"
        "s_add_u32 s55, s41, 0\n\t"
        "v_cvt_pk_bf16_f32 v116, v0, v1\n\t"
        "v_mfma_f32_16x16x32_bf16 v[20:23], v[252:255], v[88:91], v[20:23]\n\t"
        "v_cvt_pk_bf16_f32 v117, v2, v3\n\t"
        "v_cvt_pk_bf16_f32 v118, v4, v5\n\t"
        "v_cvt_pk_bf16_f32 v119, v6, v7\n\t"
        "v_mfma_f32_16x16x32_bf16 v[24:27], v[220:223], v[88:91], v[24:27]\n\t"
        "ds_read_b128 v[108:111], v151 offset:128\n\t"
        "v_add_u32 v151, s9, v174\n\t"
        "v_max_u32 v151, v151, v134\n\t"
        "v_mfma_f32_16x16x32_bf16 v[28:31], v[224:227], v[88:91], v[28:31]\n\t"
        "v_xor_b32 v135, v181, v135\n\t"
        "v_cvt_pk_bf16_f32 v120, v8, v9\n\t"
        "v_cvt_pk_bf16_f32 v121, v10, v11\n\t"
        "v_mfma_f32_16x16x32_bf16 v[32:35], v[228:231], v[88:91], v[32:35]\n\t"
        "v_permlane16_swap_b32 v116, v118\n\t"
        "v_mfma_f32_16x16x32_bf16 v[36:39], v[232:235], v[88:91], v[36:39]\n\t"
        "v_permlane16_swap_b32 v117, v119\n\t"
        "v_mfma_f32_16x16x32_bf16 v[40:43], v[236:239], v[88:91], v[40:43]\n\t"
        "buffer_store_dwordx4 v[116:119], v144, s[28:31], s55 offen sc1\n\t"
        "v_mfma_f32_16x16x32_bf16 v[44:47], v[240:243], v[88:91], v[44:47]\n\t"
        "ds_write_b64 v146, v[120:121] offset:0\n\t"
        "s_add_u32 s55, s41, 43008\n\t"
        "v_cvt_pk_bf16_f32 v116, v12, v13\n\t"
        "s_waitcnt lgkmcnt(2)\n\t"
        "v_mfma_f32_16x16x32_bf16 v[24:27], v[244:247], v[92:95], v[24:27]\n\t"
        "ds_read_b128 v[112:115], v183 offset:0\n\t"
        "v_cvt_pk_bf16_f32 v117, v14, v15\n\t"
        "v_cvt_pk_bf16_f32 v118, v16, v17\n\t"
        "v_mfma_f32_16x16x32_bf16 v[28:31], v[248:251], v[92:95], v[28:31]\n\t"
        "ds_read_b128 v[84:87], v183 offset:64\n\t"
        "v_cvt_pk_bf16_f32 v119, v18, v19\n\t"
        "v_cvt_pk_bf16_f32 v120, v20, v21\n\t"
        "v_mfma_f32_16x16x32_bf16 v[32:35], v[252:255], v[92:95], v[32:35]\n\t"
        "v_cvt_pk_bf16_f32 v121, v22, v23\n\t"
        "v_permlane16_swap_b32 v116, v118\n\t"
        "v_mfma_f32_16x16x32_bf16 v[36:39], v[220:223], v[92:95], v[36:39]\n\t"
        "v_permlane16_swap_b32 v117, v119\n\t"
        "v_lshlrev_b32_sdwa v157, 5, v160 dst_sel:DWORD dst_unused:UNUSED_PAD src0_sel:DWORD src1_sel:WORD_0\n\t"
        "v_mfma_f32_16x16x32_bf16 v[40:43], v[224:227], v[92:95], v[40:43]\n\t"
        "ds_read_b128 v[88:91], v183 offset:128\n\t"
        "v_add_u32 v183, s9, v175\n\t"
        "v_max_u32 v183, v183, v135\n\t"
        "v_mfma_f32_16x16x32_bf16 v[44:47], v[228:231], v[92:95], v[44:47]\n\t"
        "v_xor_b32 v136, v181, v136\n\t"
        "v_lshlrev_b32_sdwa v158, 5, v160 dst_sel:DWORD dst_unused:UNUSED_PAD src0_sel:DWORD src1_sel:WORD_1\n\t"
        "v_or_b32 v157, v184, v157\n\t"
        "v_mfma_f32_16x16x32_bf16 v[36:39], v[244:247], v[96:99], v[36:39]\n\t"
        "ds_read_b64_tr_b16 v[60:61], v157 offset:0\n\t"
        "ds_read_b64_tr_b16 v[68:69], v157 offset:64\n\t"
        "v_or_b32 v158, v184, v158\n\t"
        "v_mfma_f32_16x16x32_bf16 v[40:43], v[248:251], v[96:99], v[40:43]\n\t"
        "ds_read_b64_tr_b16 v[62:63], v158 offset:0\n\t"
        "ds_read_b64_tr_b16 v[70:71], v158 offset:64\n\t"
        "v_xor_b32 v159, 32, v157\n\t"
        "v_mfma_f32_16x16x32_bf16 v[44:47], v[252:255], v[96:99], v[44:47]\n\t"
        "ds_read_b64_tr_b16 v[64:65], v159 offset:0\n\t"
        "ds_read_b64_tr_b16 v[72:73], v159 offset:64\n\t"
        "v_xor_b32 v152, 32, v158\n\t"
        "v_mfma_f32_16x16x32_bf16 v[48:51], a[12:15], v[100:103], 0\n\t"
        "ds_read_b64_tr_b16 v[66:67], v152 offset:0\n\t"
        "ds_read_b64_tr_b16 v[74:75], v152 offset:64\n\t"
        "v_perm_b32 v164, v162, v162, v168\n\t"
        "v_mfma_f32_16x16x32_bf16 v[52:55], a[16:19], v[100:103], 0\n\t"
        "ds_read_b64_tr_b16 v[76:77], v157 offset:128\n\t"
        "ds_read_b64_tr_b16 v[78:79], v158 offset:128\n\t"
        "v_perm_b32 v165, v162, v162, v169\n\t"
        "v_mfma_f32_16x16x32_bf16 v[56:59], a[20:23], v[100:103], 0\n\t"
        "ds_read_b128 v[92:95], v151 offset:0\n\t"
        "ds_read_b64_tr_b16 v[80:81], v159 offset:128\n\t"
        "v_perm_b32 v166, v162, v162, v170\n\t"
        "v_mfma_f32_16x16x32_bf16 v[48:51], a[48:51], v[104:107], v[48:51]\n\t"
        "buffer_store_dwordx4 v[116:119], v144, s[28:31], s55 offen sc1\n\t"
        "v_mfma_f32_16x16x32_bf16 v[52:55], a[52:55], v[104:107], v[52:55]\n\t"
        "ds_write_b64 v146, v[120:121] offset:2048\n\t"
        "s_add_u32 s55, s41, 86016\n\t"
        "v_mfma_f32_16x16x32_bf16 v[56:59], a[56:59], v[104:107], v[56:59]\n\t"
        "ds_read_b128 v[96:99], v151 offset:64\n\t"
        "ds_read_b64_tr_b16 v[82:83], v152 offset:128\n\t"
        "v_cvt_pk_bf16_f32 v116, v24, v25\n\t"
        "s_waitcnt lgkmcnt(9)\n\t"
        "v_mfma_f32_16x16x32_bf16 v[48:51], a[84:87], v[108:111], v[48:51]\n\t"
        "ds_read_b128 v[100:103], v151 offset:128\n\t"
        "v_add_u32 v151, s9, v176\n\t"
        "v_max_u32 v151, v151, v136\n\t"
        "v_mfma_f32_16x16x32_bf16 v[52:55], a[88:91], v[108:111], v[52:55]\n\t"
        "ds_read_b128 v[104:107], v183 offset:0\n\t"
        "v_xor_b32 v137, v181, v137\n\t"
        "v_cvt_pk_bf16_f32 v117, v26, v27\n\t"
        "v_mfma_f32_16x16x32_bf16 v[56:59], a[92:95], v[108:111], v[56:59]\n\t"
        "v_cvt_pk_bf16_f32 v118, v28, v29\n\t"
        "v_cvt_pk_bf16_f32 v119, v30, v31\n\t"
        "v_cvt_pk_bf16_f32 v120, v32, v33\n\t"
        "v_mfma_f32_16x16x32_bf16 v[48:51], a[120:123], v[112:115], v[48:51]\n\t"
        "ds_read_b128 v[108:111], v183 offset:64\n\t"
        "v_cvt_pk_bf16_f32 v121, v34, v35\n\t"
        "v_perm_b32 v167, v162, v162, v171\n\t"
        "v_mfma_f32_16x16x32_bf16 v[52:55], a[124:127], v[112:115], v[52:55]\n\t"
        "v_permlane16_swap_b32 v116, v118\n\t"
        "v_lshlrev_b32_sdwa v157, 5, v161 dst_sel:DWORD dst_unused:UNUSED_PAD src0_sel:DWORD src1_sel:WORD_0\n\t"
        "v_mfma_f32_16x16x32_bf16 v[0:3], v[60:63], v[164:167], 0\n\t"
        "v_permlane16_swap_b32 v117, v119\n\t"
        "v_lshlrev_b32_sdwa v158, 5, v161 dst_sel:DWORD dst_unused:UNUSED_PAD src0_sel:DWORD src1_sel:WORD_1\n\t"
        "v_mfma_f32_16x16x32_bf16 v[56:59], a[128:131], v[112:115], v[56:59]\n\t"
        "v_or_b32 v157, v184, v157\n\t"
        "v_or_b32 v158, v184, v158\n\t"
        "v_xor_b32 v159, 32, v157\n\t"
        "s_waitcnt lgkmcnt(4)\n\t"
        "v_mfma_f32_16x16x32_bf16 v[4:7], v[64:67], v[164:167], 0\n\t"
        "ds_read_b64_tr_b16 v[60:61], v157 offset:0\n\t"
        "ds_read_b64_tr_b16 v[62:63], v158 offset:0\n\t"
        "v_xor_b32 v152, 32, v158\n\t"
        "v_mfma_f32_16x16x32_bf16 v[48:51], a[156:159], v[84:87], v[48:51]\n\t"
        "ds_read_b64_tr_b16 v[64:65], v159 offset:0\n\t"
        "ds_read_b64_tr_b16 v[66:67], v152 offset:0\n\t"
        "v_mfma_f32_16x16x32_bf16 v[8:11], v[68:71], v[164:167], 0\n\t"
        "ds_read_b64_tr_b16 v[68:69], v157 offset:64\n\t"
        "ds_read_b64_tr_b16 v[70:71], v158 offset:64\n\t"
        "v_mfma_f32_16x16x32_bf16 v[52:55], a[160:163], v[84:87], v[52:55]\n\t"
        "ds_read_b128 v[112:115], v183 offset:128\n\t"
        "v_add_u32 v183, s9, v177\n\t"
        "v_max_u32 v183, v183, v137\n\t"
        "v_mfma_f32_16x16x32_bf16 v[12:15], v[72:75], v[164:167], 0\n\t"
        "buffer_store_dwordx4 v[116:119], v144, s[28:31], s55 offen sc1\n\t"
        "v_mfma_f32_16x16x32_bf16 v[56:59], a[164:167], v[84:87], v[56:59]\n\t"
        "ds_write_b64 v146, v[120:121] offset:4096\n\t"
        "v_xor_b32 v138, v181, v138\n\t"
        "v_mfma_f32_16x16x32_bf16 v[16:19], v[76:79], v[164:167], 0\n\t"
        "ds_read_b64_tr_b16 v[72:73], v159 offset:64\n\t"
        "ds_read_b64_tr_b16 v[74:75], v152 offset:64\n\t"
        "s_add_u32 s55, s41, 129024\n\t"
        "v_mfma_f32_16x16x32_bf16 v[48:51], a[192:195], v[88:91], v[48:51]\n\t"
        "ds_read_b64_tr_b16 v[76:77], v157 offset:128\n\t"
        "ds_read_b64_tr_b16 v[78:79], v158 offset:128\n\t"
        "v_cvt_pk_bf16_f32 v116, v36, v37\n\t"
        "s_waitcnt lgkmcnt(10)\n\t"
        "v_mfma_f32_16x16x32_bf16 v[20:23], v[80:83], v[164:167], 0\n\t"
        "ds_read_b64_tr_b16 v[80:81], v159 offset:128\n\t"
        "ds_read_b64_tr_b16 v[82:83], v152 offset:128\n\t"
        "v_cvt_pk_bf16_f32 v117, v38, v39\n\t"
        "v_mfma_f32_16x16x32_bf16 v[52:55], a[196:199], v[88:91], v[52:55]\n\t"
        "ds_read_b128 v[84:87], v151 offset:0\n\t"
        "v_cvt_pk_bf16_f32 v118, v40, v41\n\t"
        "v_cvt_pk_bf16_f32 v119, v42, v43\n\t"
        "v_mfma_f32_16x16x32_bf16 v[56:59], a[200:203], v[88:91], v[56:59]\n\t"
        "v_cvt_pk_bf16_f32 v120, v44, v45\n\t"
        "v_cvt_pk_bf16_f32 v121, v46, v47\n\t"
        "v_perm_b32 v164, v163, v163, v168\n\t"
        "v_mfma_f32_16x16x32_bf16 v[48:51], a[228:231], v[92:95], v[48:51]\n\t"
        "v_permlane16_swap_b32 v116, v118\n\t"
        "v_perm_b32 v165, v163, v163, v169\n\t"
        "v_mfma_f32_16x16x32_bf16 v[52:55], a[232:235], v[92:95], v[52:55]\n\t"
        "v_permlane16_swap_b32 v117, v119\n\t"
        "v_perm_b32 v166, v163, v163, v170\n\t"
        "v_mfma_f32_16x16x32_bf16 v[56:59], a[236:239], v[92:95], v[56:59]\n\t"
        "ds_read_b128 v[88:91], v151 offset:64\n\t"
        "v_perm_b32 v167, v163, v163, v171\n\t"
        "v_mfma_f32_16x16x32_bf16 v[48:51], v[196:199], v[96:99], v[48:51]\n\t"
        "ds_read_b128 v[160:163], v185 offset:768\n\t"
        "v_mfma_f32_16x16x32_bf16 v[0:3], v[60:63], v[164:167], v[0:3]\n\t"
        "ds_read_b128 v[92:95], v151 offset:128\n\t"
        "v_add_u32 v151, s9, v178\n\t"
        "v_max_u32 v151, v151, v138\n\t"
        "v_xor_b32 v139, v181, v139\n\t"
        "v_mfma_f32_16x16x32_bf16 v[52:55], v[200:203], v[96:99], v[52:55]\n\t"
        "s_waitcnt lgkmcnt(5)\n\t"
        "v_mfma_f32_16x16x32_bf16 v[4:7], v[64:67], v[164:167], v[4:7]\n\t"
        "v_mfma_f32_16x16x32_bf16 v[56:59], v[204:207], v[96:99], v[56:59]\n\t"
        "buffer_store_dwordx4 v[116:119], v144, s[28:31], s55 offen sc1\n\t"
        "v_mfma_f32_16x16x32_bf16 v[8:11], v[68:71], v[164:167], v[8:11]\n\t"
        "ds_write_b64 v146, v[120:121] offset:6144\n\t"
        "v_cvt_pk_bf16_f32 v0, v0, v1\n\t"
        "v_cvt_pk_bf16_f32 v1, v2, v3\n\t"
        "v_mfma_f32_16x16x32_bf16 v[48:51], v[232:235], v[100:103], v[48:51]\n\t"
        "ds_write_b64 v186, v[0:1] offset:24576\n\t"
        "v_mfma_f32_16x16x32_bf16 v[12:15], v[72:75], v[164:167], v[12:15]\n\t"
        "ds_read_b128 v[96:99], v183 offset:0\n\t"
        "v_cvt_pk_bf16_f32 v4, v4, v5\n\t"
        "v_cvt_pk_bf16_f32 v5, v6, v7\n\t"
        "v_mfma_f32_16x16x32_bf16 v[52:55], v[236:239], v[100:103], v[52:55]\n\t"
        "ds_write_b64 v154, v[4:5] offset:24576\n\t"
        "v_mfma_f32_16x16x32_bf16 v[16:19], v[76:79], v[164:167], v[16:19]\n\t"
        "v_cvt_pk_bf16_f32 v8, v8, v9\n\t"
        "v_cvt_pk_bf16_f32 v9, v10, v11\n\t"
        "s_waitcnt lgkmcnt(4)\n\t"
        "v_lshlrev_b32_sdwa v157, 5, v160 dst_sel:DWORD dst_unused:UNUSED_PAD src0_sel:DWORD src1_sel:WORD_0\n\t"
        "v_lshlrev_b32_sdwa v158, 5, v160 dst_sel:DWORD dst_unused:UNUSED_PAD src0_sel:DWORD src1_sel:WORD_1\n\t"
        "v_or_b32 v157, v184, v157\n\t"
        "v_mfma_f32_16x16x32_bf16 v[56:59], v[240:243], v[100:103], v[56:59]\n\t"
        "ds_write_b64 v186, v[8:9] offset:24640\n\t"
        "v_mfma_f32_16x16x32_bf16 v[20:23], v[80:83], v[164:167], v[20:23]\n\t"
        "ds_read_b64_tr_b16 v[60:61], v157 offset:0\n\t"
        "ds_read_b64_tr_b16 v[68:69], v157 offset:64\n\t"
        "v_cvt_pk_bf16_f32 v12, v12, v13\n\t"
        "v_cvt_pk_bf16_f32 v13, v14, v15\n\t"
        "v_mfma_f32_16x16x32_bf16 v[48:51], a[0:3], v[104:107], v[48:51]\n\t"
        "ds_write_b64 v154, v[12:13] offset:24640\n\t"
        "v_mfma_f32_16x16x32_bf16 v[52:55], a[4:7], v[104:107], v[52:55]\n\t"
        "ds_read_b64_tr_b16 v[76:77], v157 offset:128\n\t"
        "v_cvt_pk_bf16_f32 v16, v16, v17\n\t"
        "v_cvt_pk_bf16_f32 v17, v18, v19\n\t"
        "v_or_b32 v158, v184, v158\n\t"
        "v_mfma_f32_16x16x32_bf16 v[56:59], a[8:11], v[104:107], v[56:59]\n\t"
        "ds_write_b64 v186, v[16:17] offset:24704\n\t"
        "v_mfma_f32_16x16x32_bf16 v[48:51], a[36:39], v[108:111], v[48:51]\n\t"
        "ds_read_b64_tr_b16 v[62:63], v158 offset:0\n\t"
        "ds_read_b64_tr_b16 v[70:71], v158 offset:64\n\t"
        "v_cvt_pk_bf16_f32 v20, v20, v21\n\t"
        "v_cvt_pk_bf16_f32 v21, v22, v23\n\t"
        "v_mfma_f32_16x16x32_bf16 v[52:55], a[40:43], v[108:111], v[52:55]\n\t"
        "ds_write_b64 v154, v[20:21] offset:24704\n\t"
        "v_mfma_f32_16x16x32_bf16 v[56:59], a[44:47], v[108:111], v[56:59]\n\t"
        "ds_read_b128 v[100:103], v183 offset:64\n\t"
        "ds_read_b64_tr_b16 v[78:79], v158 offset:128\n\t"
        "v_xor_b32 v159, 32, v157\n\t"
        "v_xor_b32 v152, 32, v158\n\t"
        "v_mfma_f32_16x16x32_bf16 v[48:51], a[72:75], v[112:115], v[48:51]\n\t"
        "ds_read_b64_tr_b16 v[64:65], v159 offset:0\n\t"
        "ds_read_b64_tr_b16 v[66:67], v152 offset:0\n\t"
        "v_perm_b32 v164, v162, v162, v168\n\t"
        "v_mfma_f32_16x16x32_bf16 v[52:55], a[76:79], v[112:115], v[52:55]\n\t"
        "ds_read_b64_tr_b16 v[72:73], v159 offset:64\n\t"
        "ds_read_b64_tr_b16 v[74:75], v152 offset:64\n\t"
        "v_perm_b32 v165, v162, v162, v169\n\t"
        "v_mfma_f32_16x16x32_bf16 v[56:59], a[80:83], v[112:115], v[56:59]\n\t"
        "ds_read_b128 v[104:107], v183 offset:128\n\t"
        "ds_read_b64_tr_b16 v[80:81], v159 offset:128\n\t"
        "v_add_u32 v183, s9, v179\n\t"
        "v_mfma_f32_16x16x32_bf16 v[48:51], a[108:111], v[84:87], v[48:51]\n\t"
        "ds_read_b64_tr_b16 v[82:83], v152 offset:128\n\t"
        "v_max_u32 v183, v183, v139\n\t"
        "v_perm_b32 v166, v162, v162, v170\n\t"
        "v_mfma_f32_16x16x32_bf16 v[52:55], a[112:115], v[84:87], v[52:55]\n\t"
        "ds_read_b128 v[108:111], v151 offset:0\n\t"
        "v_perm_b32 v167, v162, v162, v171\n\t"
        "v_lshlrev_b32_sdwa v157, 5, v161 dst_sel:DWORD dst_unused:UNUSED_PAD src0_sel:DWORD src1_sel:WORD_0\n\t"
        "v_mfma_f32_16x16x32_bf16 v[56:59], a[116:119], v[84:87], v[56:59]\n\t"
        "ds_read_b128 v[112:115], v151 offset:64\n\t"
        "v_lshlrev_b32_sdwa v158, 5, v161 dst_sel:DWORD dst_unused:UNUSED_PAD src0_sel:DWORD src1_sel:WORD_1\n\t"
        "v_or_b32 v157, v184, v157\n\t"
        "v_mfma_f32_16x16x32_bf16 v[48:51], a[144:147], v[88:91], v[48:51]\n\t"
        "ds_read_b128 v[84:87], v151 offset:128\n\t"
        "v_or_b32 v158, v184, v158\n\t"
        "v_xor_b32 v159, 32, v157\n\t"
        "s_waitcnt lgkmcnt(10)\n\t"
        "v_mfma_f32_16x16x32_bf16 v[0:3], v[60:63], v[164:167], 0\n\t"
        "ds_read_b64_tr_b16 v[60:61], v157 offset:0\n\t"
        "ds_read_b64_tr_b16 v[62:63], v158 offset:0\n\t"
        "v_xor_b32 v152, 32, v158\n\t"
        "v_mfma_f32_16x16x32_bf16 v[52:55], a[148:151], v[88:91], v[52:55]\n\t"
        "v_mfma_f32_16x16x32_bf16 v[8:11], v[68:71], v[164:167], 0\n\t"
        "ds_read_b64_tr_b16 v[68:69], v157 offset:64\n\t"
        "ds_read_b64_tr_b16 v[70:71], v158 offset:64\n\t"
        "v_mfma_f32_16x16x32_bf16 v[56:59], a[152:155], v[88:91], v[56:59]\n\t"
        "s_waitcnt lgkmcnt(8)\n\t"
        "v_mfma_f32_16x16x32_bf16 v[4:7], v[64:67], v[164:167], 0\n\t"
        "ds_read_b64_tr_b16 v[64:65], v159 offset:0\n\t"
        "ds_read_b64_tr_b16 v[66:67], v152 offset:0\n\t"
        "v_mfma_f32_16x16x32_bf16 v[48:51], a[180:183], v[92:95], v[48:51]\n\t"
        "ds_read_b128 v[88:91], v183 offset:0\n\t"
        "v_mfma_f32_16x16x32_bf16 v[12:15], v[72:75], v[164:167], 0\n\t"
        "ds_read_b64_tr_b16 v[72:73], v159 offset:64\n\t"
        "ds_read_b64_tr_b16 v[74:75], v152 offset:64\n\t"
        "v_mfma_f32_16x16x32_bf16 v[52:55], a[184:187], v[92:95], v[52:55]\n\t"
        "v_mfma_f32_16x16x32_bf16 v[16:19], v[76:79], v[164:167], 0\n\t"
        "ds_read_b64_tr_b16 v[76:77], v157 offset:128\n\t"
        "ds_read_b64_tr_b16 v[78:79], v158 offset:128\n\t"
        "v_mfma_f32_16x16x32_bf16 v[56:59], a[188:191], v[92:95], v[56:59]\n\t"
        "s_waitcnt lgkmcnt(9)\n\t"
        "v_mfma_f32_16x16x32_bf16 v[20:23], v[80:83], v[164:167], 0\n\t"
        "ds_read_b64_tr_b16 v[80:81], v159 offset:128\n\t"
        "ds_read_b64_tr_b16 v[82:83], v152 offset:128\n\t"
        "v_perm_b32 v164, v163, v163, v168\n\t"
        "v_perm_b32 v165, v163, v163, v169\n\t"
        "v_perm_b32 v166, v163, v163, v170\n\t"
        "v_mfma_f32_16x16x32_bf16 v[48:51], a[216:219], v[96:99], v[48:51]\n\t"
        "ds_read_b128 v[92:95], v183 offset:64\n\t"
        "v_perm_b32 v167, v163, v163, v171\n\t"
        "v_mfma_f32_16x16x32_bf16 v[52:55], a[220:223], v[96:99], v[52:55]\n\t"
        "s_nop 0\n\t"
        "v_mfma_f32_16x16x32_bf16 v[0:3], v[60:63], v[164:167], v[0:3]\n\t"
        "v_mfma_f32_16x16x32_bf16 v[56:59], a[224:227], v[96:99], v[56:59]\n\t"
        "s_waitcnt lgkmcnt(7)\n\t"
        "v_mfma_f32_16x16x32_bf16 v[4:7], v[64:67], v[164:167], v[4:7]\n\t"
        "v_mfma_f32_16x16x32_bf16 v[48:51], a[252:255], v[100:103], v[48:51]\n\t"
        "ds_read_b128 v[96:99], v183 offset:128\n\t"
        "v_mfma_f32_16x16x32_bf16 v[8:11], v[68:71], v[164:167], v[8:11]\n\t"
        "s_nop 1\n\t"
        "v_cvt_pk_bf16_f32 v0, v0, v1\n\t"
        "v_cvt_pk_bf16_f32 v1, v2, v3\n\t"
        "v_mfma_f32_16x16x32_bf16 v[52:55], v[188:191], v[100:103], v[52:55]\n\t"
        "ds_write_b64 v186, v[0:1] offset:36864\n\t"
        "s_waitcnt lgkmcnt(4)\n\t"
        "v_mfma_f32_16x16x32_bf16 v[12:15], v[72:75], v[164:167], v[12:15]\n\t"
        "v_cvt_pk_bf16_f32 v4, v4, v5\n\t"
        "v_cvt_pk_bf16_f32 v5, v6, v7\n\t"
        "v_mfma_f32_16x16x32_bf16 v[56:59], v[192:195], v[100:103], v[56:59]\n\t"
        "ds_write_b64 v154, v[4:5] offset:36864\n\t"
        "v_mfma_f32_16x16x32_bf16 v[16:19], v[76:79], v[164:167], v[16:19]\n\t"
        "v_cvt_pk_bf16_f32 v8, v8, v9\n\t"
        "v_cvt_pk_bf16_f32 v9, v10, v11\n\t"
        "v_mfma_f32_16x16x32_bf16 v[48:51], v[220:223], v[104:107], v[48:51]\n\t"
        "ds_write_b64 v186, v[8:9] offset:36928\n\t"
        "s_waitcnt lgkmcnt(4)\n\t"
        "v_mfma_f32_16x16x32_bf16 v[20:23], v[80:83], v[164:167], v[20:23]\n\t"
        "v_cvt_pk_bf16_f32 v12, v12, v13\n\t"
        "v_cvt_pk_bf16_f32 v13, v14, v15\n\t"
        "v_mfma_f32_16x16x32_bf16 v[52:55], v[224:227], v[104:107], v[52:55]\n\t"
        "ds_write_b64 v154, v[12:13] offset:36928\n\t"
        "v_mfma_f32_16x16x32_bf16 v[56:59], v[228:231], v[104:107], v[56:59]\n\t"
        "v_cvt_pk_bf16_f32 v16, v16, v17\n\t"
        "v_cvt_pk_bf16_f32 v17, v18, v19\n\t"
        "v_mfma_f32_16x16x32_bf16 v[48:51], a[24:27], v[108:111], v[48:51]\n\t"
        "ds_write_b64 v186, v[16:17] offset:36992\n\t"
        "v_mfma_f32_16x16x32_bf16 v[52:55], a[28:31], v[108:111], v[52:55]\n\t"
        "v_cvt_pk_bf16_f32 v20, v20, v21\n\t"
        "v_cvt_pk_bf16_f32 v21, v22, v23\n\t"
        "v_mfma_f32_16x16x32_bf16 v[56:59], a[32:35], v[108:111], v[56:59]\n\t"
        "ds_write_b64 v154, v[20:21] offset:36992\n\t"
        "s_cmp_gt_u32 s61, 64\n\t"
        "s_cbranch_scc1 .Lx68%=\n\t"
        ".Lx69%=:\n\t"
        "v_mfma_f32_16x16x32_bf16 v[48:51], a[60:63], v[112:115], v[48:51]\n\t"
        "v_mfma_f32_16x16x32_bf16 v[52:55], a[64:67], v[112:115], v[52:55]\n\t"
        "v_mfma_f32_16x16x32_bf16 v[56:59], a[68:71], v[112:115], v[56:59]\n\t"
        "v_mfma_f32_16x16x32_bf16 v[48:51], a[96:99], v[84:87], v[48:51]\n\t"
        "v_mfma_f32_16x16x32_bf16 v[52:55], a[100:103], v[84:87], v[52:55]\n\t"
        "v_mfma_f32_16x16x32_bf16 v[56:59], a[104:107], v[84:87], v[56:59]\n\t"
        "s_waitcnt lgkmcnt(0)\n\t"
        "s_barrier\n\t"
        "v_xor_b32 v140, v181, v140\n\t"
        "v_mfma_f32_16x16x32_bf16 v[48:51], a[132:135], v[88:91], v[48:51]\n\t"
        "ds_read_b128 v[132:135], v187 offset:4096\n\t"
        "v_mfma_f32_16x16x32_bf16 v[52:55], a[136:139], v[88:91], v[52:55]\n\t"
        "ds_read_b128 v[136:139], v187 offset:4112\n\t"
        "v_add_u32 v151, s9, v180\n\t"
        "v_max_u32 v151, v151, v140\n\t"
        "s_waitcnt lgkmcnt(1)\n\t"
        "v_xor_b32 v132, v181, v132\n\t"
        "v_mfma_f32_16x16x32_bf16 v[56:59], a[140:143], v[88:91], v[56:59]\n\t"
        "ds_read_b128 v[100:103], v151 offset:0\n\t"
        "v_mfma_f32_16x16x32_bf16 v[48:51], a[168:171], v[92:95], v[48:51]\n\t"
        "ds_read_b128 v[104:107], v151 offset:64\n\t"
        "v_mfma_f32_16x16x32_bf16 v[52:55], a[172:175], v[92:95], v[52:55]\n\t"
        "ds_read_b128 v[108:111], v151 offset:128\n\t"
        "v_mfma_f32_16x16x32_bf16 v[56:59], a[176:179], v[92:95], v[56:59]\n\t"
        "ds_read_b32 v140, v187 offset:4128\n\t"
        "v_readlane_b32 s9, v155, 48\n\t"
        "v_mfma_f32_16x16x32_bf16 v[48:51], a[204:207], v[96:99], v[48:51]\n\t"
        "ds_read_b128 v[124:127], v182 offset:16\n\t"
        "v_add_u32 v183, s9, v172\n\t"
        "v_max_u32 v183, v183, v132\n\t"
        "v_xor_b32 v133, v181, v133\n\t"
        "v_mfma_f32_16x16x32_bf16 v[52:55], a[208:211], v[96:99], v[52:55]\n\t"
        "ds_read_b128 v[112:115], v183 offset:0\n\t"
        "v_add_u32 v151, s9, v173\n\t"
        "v_mfma_f32_16x16x32_bf16 v[56:59], a[212:215], v[96:99], v[56:59]\n\t"
        "ds_read_b128 v[84:87], v183 offset:64\n\t"
        "v_max_u32 v151, v151, v133\n\t"
        "s_waitcnt lgkmcnt(6)\n\t"
        "v_mfma_f32_16x16x32_bf16 v[48:51], a[240:243], v[100:103], v[48:51]\n\t"
        "ds_read_b128 v[88:91], v183 offset:128\n\t"
        "v_xor_b32 v134, v181, v134\n\t"
        "v_mfma_f32_16x16x32_bf16 v[52:55], a[244:247], v[100:103], v[52:55]\n\t"
        "ds_read_b128 v[92:95], v151 offset:0\n\t"
        "v_add_u32 v183, s9, v174\n\t"
        "v_mfma_f32_16x16x32_bf16 v[56:59], a[248:251], v[100:103], v[56:59]\n\t"
        "ds_read_b128 v[96:99], v151 offset:64\n\t"
        "v_max_u32 v183, v183, v134\n\t"
        "s_waitcnt lgkmcnt(8)\n\t"
        "v_mfma_f32_16x16x32_bf16 v[48:51], v[208:211], v[104:107], v[48:51]\n\t"
        "ds_read_b128 v[100:103], v151 offset:128\n\t"
        "v_xor_b32 v135, v181, v135\n\t"
        "v_mfma_f32_16x16x32_bf16 v[52:55], v[212:215], v[104:107], v[52:55]\n\t"
        "v_add_u32 v151, s9, v175\n\t"
        "v_max_u32 v151, v151, v135\n\t"
        "v_mfma_f32_16x16x32_bf16 v[56:59], v[216:219], v[104:107], v[56:59]\n\t"
        "v_xor_b32 v136, v181, v136\n\t"
        "s_waitcnt lgkmcnt(7)\n\t"
        "v_mfma_f32_16x16x32_bf16 v[48:51], v[244:247], v[108:111], v[48:51]\n\t"
        "ds_read_b128 v[104:107], v183 offset:0\n\t"
        "s_waitcnt lgkmcnt(7)\n\t"
        "v_or3_b32 v124, v124, v125, v126\n\t"
        "v_mfma_f32_16x16x32_bf16 v[52:55], v[248:251], v[108:111], v[52:55]\n\t"
        "v_or_b32 v124, v124, v127\n\t"
        "v_mfma_f32_16x16x32_bf16 v[56:59], v[252:255], v[108:111], v[56:59]\n\t"
        "v_readfirstlane_b32 s8, v124\n\t"
        "s_waitcnt lgkmcnt(6)\n\t"
        "v_mfma_f32_16x16x32_bf16 v[72:75], a[12:15], v[112:115], 0\n\t"
        "ds_read_b128 v[124:127], v147 offset:0\n\t"
        "s_or_b32 s6, s6, s8\n\t"
        "v_mfma_f32_16x16x32_bf16 v[76:79], a[16:19], v[112:115], 0\n\t"
        "ds_read_b128 v[108:111], v183 offset:64\n\t"
        "v_mfma_f32_16x16x32_bf16 v[80:83], a[20:23], v[112:115], 0\n\t"
        "s_waitcnt lgkmcnt(5)\n\t"
        "v_mfma_f32_16x16x32_bf16 v[72:75], a[48:51], v[84:87], v[72:75]\n\t"
        "ds_read_b128 v[112:115], v183 offset:128\n\t"
        "v_add_u32 v183, s9, v176\n\t"
        "v_mfma_f32_16x16x32_bf16 v[76:79], a[52:55], v[84:87], v[76:79]\n\t"
        "v_max_u32 v183, v183, v136\n\t"
        "v_xor_b32 v137, v181, v137\n\t"
        "v_mfma_f32_16x16x32_bf16 v[80:83], a[56:59], v[84:87], v[80:83]\n\t"
        "v_mfma_f32_16x16x32_bf16 v[72:75], a[84:87], v[88:91], v[72:75]\n\t"
        "ds_read_b128 v[84:87], v151 offset:0\n\t"
        "v_mfma_f32_16x16x32_bf16 v[76:79], a[88:91], v[88:91], v[76:79]\n\t"
        "v_mfma_f32_16x16x32_bf16 v[80:83], a[92:95], v[88:91], v[80:83]\n\t"
        "v_mfma_f32_16x16x32_bf16 v[72:75], a[120:123], v[92:95], v[72:75]\n\t"
        "s_waitcnt lgkmcnt(3)\n\t"
        "buffer_store_dwordx4 v[124:127], v145, s[28:31], s50 offen sc1\n\t"
        "v_mfma_f32_16x16x32_bf16 v[76:79], a[124:127], v[92:95], v[76:79]\n\t"
        "ds_read_b128 v[124:127], v147 offset:4096\n\t"
        "s_add_u32 s50, s50, 86016\n\t"
        "v_mfma_f32_16x16x32_bf16 v[80:83], a[128:131], v[92:95], v[80:83]\n\t"
        "ds_read_b128 v[88:91], v151 offset:64\n\t"
        "v_mfma_f32_16x16x32_bf16 v[72:75], a[156:159], v[96:99], v[72:75]\n\t"
        "ds_read_b128 v[92:95], v151 offset:128\n\t"
        "v_add_u32 v151, s9, v177\n\t"
        "v_mfma_f32_16x16x32_bf16 v[76:79], a[160:163], v[96:99], v[76:79]\n\t"
        "v_max_u32 v151, v151, v137\n\t"
        "v_xor_b32 v138, v181, v138\n\t"
        "v_mfma_f32_16x16x32_bf16 v[80:83], a[164:167], v[96:99], v[80:83]\n\t"
        "v_mfma_f32_16x16x32_bf16 v[72:75], a[192:195], v[100:103], v[72:75]\n\t"
        "ds_read_b128 v[96:99], v183 offset:0\n\t"
        "v_mfma_f32_16x16x32_bf16 v[76:79], a[196:199], v[100:103], v[76:79]\n\t"
        "v_mfma_f32_16x16x32_bf16 v[80:83], a[200:203], v[100:103], v[80:83]\n\t"
        "v_mfma_f32_16x16x32_bf16 v[72:75], a[228:231], v[104:107], v[72:75]\n\t"
        "ds_read_b128 v[100:103], v183 offset:64\n\t"
        "v_mfma_f32_16x16x32_bf16 v[76:79], a[232:235], v[104:107], v[76:79]\n\t"
        "v_mfma_f32_16x16x32_bf16 v[80:83], a[236:239], v[104:107], v[80:83]\n\t"
        "s_waitcnt lgkmcnt(4)\n\t"
        "v_mfma_f32_16x16x32_bf16 v[72:75], v[196:199], v[108:111], v[72:75]\n\t"
        "ds_read_b128 v[104:107], v183 offset:128\n\t"
        "v_add_u32 v183, s9, v178\n\t"
        "v_mfma_f32_16x16x32_bf16 v[76:79], v[200:203], v[108:111], v[76:79]\n\t"
        "v_max_u32 v183, v183, v138\n\t"
        "v_xor_b32 v139, v181, v139\n\t"
        "v_mfma_f32_16x16x32_bf16 v[80:83], v[204:207], v[108:111], v[80:83]\n\t"
        "buffer_store_dwordx4 v[124:127], v145, s[28:31], s50 offen sc1\n\t"
        "v_mfma_f32_16x16x32_bf16 v[72:75], v[232:235], v[112:115], v[72:75]\n\t"
        "ds_read_b128 v[108:111], v151 offset:0\n\t"
        "v_readlane_b32 s55, v155, 49\n\t"
        "v_mfma_f32_16x16x32_bf16 v[76:79], v[236:239], v[112:115], v[76:79]\n\t"
        "v_cvt_pk_bf16_f32 v116, v48, v49\n\t"
        "v_cvt_pk_bf16_f32 v117, v50, v51\n\t"
        "v_mfma_f32_16x16x32_bf16 v[80:83], v[240:243], v[112:115], v[80:83]\n\t"
        "v_cvt_pk_bf16_f32 v118, v52, v53\n\t"
        "v_cvt_pk_bf16_f32 v119, v54, v55\n\t"
        "v_mfma_f32_16x16x32_bf16 v[72:75], a[0:3], v[84:87], v[72:75]\n\t"
        "ds_read_b128 v[112:115], v151 offset:64\n\t"
        "v_cvt_pk_bf16_f32 v120, v56, v57\n\t"
        "v_mfma_f32_16x16x32_bf16 v[76:79], a[4:7], v[84:87], v[76:79]\n\t"
        "v_cvt_pk_bf16_f32 v121, v58, v59\n\t"
        "v_mfma_f32_16x16x32_bf16 v[80:83], a[8:11], v[84:87], v[80:83]\n\t"
        "v_permlane16_swap_b32 v116, v118\n\t"
        "s_waitcnt lgkmcnt(3)\n\t"
        "v_mfma_f32_16x16x32_bf16 v[72:75], a[36:39], v[88:91], v[72:75]\n\t"
        "v_permlane16_swap_b32 v117, v119\n\t"
        "v_mfma_f32_16x16x32_bf16 v[76:79], a[40:43], v[88:91], v[76:79]\n\t"
        "ds_read_b128 v[84:87], v151 offset:128\n\t"
        ".LpX2%=:\n\t"
        "s_cbranch_execz .LrX%=\n\t"
        "v_add_u32 v151, s9, v179\n\t"
        "v_mfma_f32_16x16x32_bf16 v[80:83], a[44:47], v[88:91], v[80:83]\n\t"
        "v_max_u32 v151, v151, v139\n\t"
        "v_xor_b32 v140, v181, v140\n\t"
        "v_mfma_f32_16x16x32_bf16 v[72:75], a[72:75], v[92:95], v[72:75]\n\t"
        "ds_read_b128 v[88:91], v183 offset:0\n\t"
        "v_mfma_f32_16x16x32_bf16 v[76:79], a[76:79], v[92:95], v[76:79]\n\t"
        "v_mfma_f32_16x16x32_bf16 v[80:83], a[80:83], v[92:95], v[80:83]\n\t"
        "v_mfma_f32_16x16x32_bf16 v[72:75], a[108:111], v[96:99], v[72:75]\n\t"
        "ds_read_b128 v[92:95], v183 offset:64\n\t"
        "v_mfma_f32_16x16x32_bf16 v[76:79], a[112:115], v[96:99], v[76:79]\n\t"
        "buffer_store_dwordx4 v[116:119], v148, s[28:31], s55 offen sc1\n\t"
        "v_mfma_f32_16x16x32_bf16 v[80:83], a[116:119], v[96:99], v[80:83]\n\t"
        "ds_write_b64 v146, v[120:121] offset:8192\n\t"
        "v_mfma_f32_16x16x32_bf16 v[72:75], a[144:147], v[100:103], v[72:75]\n\t"
        "ds_read_b128 v[96:99], v183 offset:128\n\t"
        "v_add_u32 v183, s9, v180\n\t"
        "v_mfma_f32_16x16x32_bf16 v[76:79], a[148:151], v[100:103], v[76:79]\n\t"
        "v_max_u32 v183, v183, v140\n\t"
        "v_mfma_f32_16x16x32_bf16 v[80:83], a[152:155], v[100:103], v[80:83]\n\t"
        "ds_read_b128 v[100:103], v151 offset:0\n\t"
        "s_waitcnt lgkmcnt(5)\n\t"
        "v_mfma_f32_16x16x32_bf16 v[72:75], a[180:183], v[104:107], v[72:75]\n\t"
        "v_mfma_f32_16x16x32_bf16 v[76:79], a[184:187], v[104:107], v[76:79]\n\t"
        "v_mfma_f32_16x16x32_bf16 v[80:83], a[188:191], v[104:107], v[80:83]\n\t"
        "ds_read_b128 v[104:107], v151 offset:64\n\t"
        "v_mfma_f32_16x16x32_bf16 v[72:75], a[216:219], v[108:111], v[72:75]\n\t"
        "v_mfma_f32_16x16x32_bf16 v[76:79], a[220:223], v[108:111], v[76:79]\n\t"
        "v_mfma_f32_16x16x32_bf16 v[80:83], a[224:227], v[108:111], v[80:83]\n\t"
        "ds_read_b128 v[108:111], v151 offset:128\n\t"
        "v_mfma_f32_16x16x32_bf16 v[72:75], a[252:255], v[112:115], v[72:75]\n\t"
        "v_mfma_f32_16x16x32_bf16 v[76:79], v[188:191], v[112:115], v[76:79]\n\t"
        "v_mfma_f32_16x16x32_bf16 v[80:83], v[192:195], v[112:115], v[80:83]\n\t"
        "ds_read_b128 v[112:115], v183 offset:0\n\t"
        "v_mfma_f32_16x16x32_bf16 v[72:75], v[220:223], v[84:87], v[72:75]\n\t"
        "v_mfma_f32_16x16x32_bf16 v[76:79], v[224:227], v[84:87], v[76:79]\n\t"
        "v_mfma_f32_16x16x32_bf16 v[80:83], v[228:231], v[84:87], v[80:83]\n\t"
        "ds_read_b128 v[84:87], v183 offset:64\n\t"
        "s_waitcnt lgkmcnt(4)\n\t"
        "v_mfma_f32_16x16x32_bf16 v[72:75], a[24:27], v[88:91], v[72:75]\n\t"
        "v_mfma_f32_16x16x32_bf16 v[76:79], a[28:31], v[88:91], v[76:79]\n\t"
        "v_mfma_f32_16x16x32_bf16 v[80:83], a[32:35], v[88:91], v[80:83]\n\t"
        "ds_read_b128 v[88:91], v183 offset:128\n\t"
        "v_mfma_f32_16x16x32_bf16 v[80:83], a[68:71], v[92:95], v[80:83]\n\t"
        "v_mfma_f32_16x16x32_bf16 v[80:83], a[104:107], v[96:99], v[80:83]\n\t"
        "v_mfma_f32_16x16x32_bf16 v[80:83], a[140:143], v[100:103], v[80:83]\n\t"
        "s_waitcnt lgkmcnt(3)\n\t"
        "v_mfma_f32_16x16x32_bf16 v[80:83], a[176:179], v[104:107], v[80:83]\n\t"
        "v_mfma_f32_16x16x32_bf16 v[80:83], a[212:215], v[108:111], v[80:83]\n\t"
        "s_waitcnt lgkmcnt(2)\n\t"
        "v_mfma_f32_16x16x32_bf16 v[80:83], a[248:251], v[112:115], v[80:83]\n\t"
        "s_waitcnt lgkmcnt(1)\n\t"
        "v_mfma_f32_16x16x32_bf16 v[80:83], v[216:219], v[84:87], v[80:83]\n\t"
        "s_waitcnt lgkmcnt(0)\n\t"
        "v_mfma_f32_16x16x32_bf16 v[80:83], v[252:255], v[88:91], v[80:83]\n\t"
        "v_mfma_f32_16x16x32_bf16 v[72:75], a[60:63], v[92:95], v[72:75]\n\t"
        "v_mfma_f32_16x16x32_bf16 v[76:79], a[64:67], v[92:95], v[76:79]\n\t"
        "s_nop 5\n\t"
        "v_cvt_pk_bf16_f32 v120, v80, v81\n\t"
        "v_cvt_pk_bf16_f32 v121, v82, v83\n\t"
        "v_mfma_f32_16x16x32_bf16 v[72:75], a[96:99], v[96:99], v[72:75]\n\t"
        "ds_write_b64 v146, v[120:121] offset:10240\n\t"
        "v_mfma_f32_16x16x32_bf16 v[76:79], a[100:103], v[96:99], v[76:79]\n\t"
        "v_mfma_f32_16x16x32_bf16 v[72:75], a[132:135], v[100:103], v[72:75]\n\t"
        "v_mfma_f32_16x16x32_bf16 v[76:79], a[136:139], v[100:103], v[76:79]\n\t"
        "s_waitcnt lgkmcnt(0)\n\t"
        "s_barrier\n\t"
        "v_mfma_f32_16x16x32_bf16 v[72:75], a[168:171], v[104:107], v[72:75]\n\t"
        "ds_read_b128 v[124:127], v147 offset:8192\n\t"
        "v_readlane_b32 s55, v155, 51\n\t"
        "v_mfma_f32_16x16x32_bf16 v[76:79], a[172:175], v[104:107], v[76:79]\n\t"
        "v_mfma_f32_16x16x32_bf16 v[72:75], a[204:207], v[108:111], v[72:75]\n\t"
        "v_mfma_f32_16x16x32_bf16 v[76:79], a[208:211], v[108:111], v[76:79]\n\t"
        "v_mfma_f32_16x16x32_bf16 v[72:75], a[240:243], v[112:115], v[72:75]\n\t"
        "v_mfma_f32_16x16x32_bf16 v[76:79], a[244:247], v[112:115], v[76:79]\n\t"
        "v_mfma_f32_16x16x32_bf16 v[72:75], v[208:211], v[84:87], v[72:75]\n\t"
        "v_mfma_f32_16x16x32_bf16 v[76:79], v[212:215], v[84:87], v[76:79]\n\t"
        "v_mfma_f32_16x16x32_bf16 v[72:75], v[244:247], v[88:91], v[72:75]\n\t"
        "v_mfma_f32_16x16x32_bf16 v[76:79], v[248:251], v[88:91], v[76:79]\n\t"
        "s_nop 6\n\t"
        "v_cvt_pk_bf16_f32 v116, v72, v73\n\t"
        "v_cvt_pk_bf16_f32 v117, v74, v75\n\t"
        "v_cvt_pk_bf16_f32 v118, v76, v77\n\t"
        "v_cvt_pk_bf16_f32 v119, v78, v79\n\t"
        "s_nop 0\n\t"
        "v_permlane16_swap_b32 v116, v118\n\t"
        "v_permlane16_swap_b32 v117, v119\n\t"
        "buffer_store_dwordx4 v[116:119], v148, s[28:31], s55 offen  \n\t"
        "s_waitcnt lgkmcnt(0)\n\t"
        "buffer_store_dwordx4 v[124:127], v150, s[28:31], s52 offen  \n\t"
        "s_andn2_b32 s6, s6, 2\n\t"
        "s_cbranch_scc1 99f\n\t"
        "s_endpgm\n\t"
        "22:\n\t"
        "ds_bpermute_b32 v64, v65, v155 offset:172\n\t"
        "s_add_u32 m0, s37, -24576\n\t"
        "s_add_u32 s36, s36, 6144\n\t"
        "buffer_load_dwordx4 v140, s[24:27], s36 offen offset:0 lds\n\t"
        "ds_bpermute_b32 v78, v81, v155 offset:148\n\t"
        "ds_bpermute_b32 v79, v81, v155 offset:160\n\t"
        "v_readlane_b32 s71, v155, 26\n\t"
        "v_readlane_b32 s10, v155, 29\n\t"
        "s_nop 0\n\t"
        "v_mul_u32_u24 v73, s71, v66\n\t"
        "v_lshrrev_b32 v73, 11, v73\n\t"
        "v_lshlrev_b32 v66, 4, v66\n\t"
        "v_mad_u32_u24 v73, v73, s10, v66\n\t"
        "s_waitcnt lgkmcnt(2)\n\t"
        "v_add_u32 v64, v64, v73\n\t"
        "buffer_load_dwordx4 v141, s[24:27], s36 offen offset:1024 lds\n\t"
        "v_readlane_b32 s69, v155, 34\n\t"
        "v_readlane_b32 s70, v155, 35\n\t"
        "s_nop 0\n\t"
        "v_mad_u32_u24 v70, v52, s69, v70\n\t"
        "v_mad_u32_u24 v70, v158, s70, v70\n\t"
        "s_mul_i32 s71, s69, s68\n\t"
        "s_add_u32 s71, s71, 157728\n\t"
        "v_mad_u32_u24 v67, v82, s70, v67\n\t"
        "v_add_u32 v67, s71, v67\n\t"
        "v_readlane_b32 s11, v155, 30\n\t"
        "buffer_load_dwordx4 v142, s[24:27], s36 offen offset:2048 lds\n\t"
        "s_movk_i32 s10, 223\n\t"
        "v_mov_b32 v75, 0x7ff00000\n\t"
        "v_readlane_b32 s12, v155, 24\n\t"
        "v_readlane_b32 s13, v155, 25\n\t"
        "s_nop 0\n\t"
        "s_bfe_u32 s70, s12, 524288\n\t"
        "s_bfe_u32 s71, s13, 524288\n\t"
        "v_cmp_gt_u32 vcc, s71, v71\n\t"
        "v_add3_u32 v73, v71, s70, -2\n\t"
        "v_med3_i32 v73, v73, 0, s10\n\t"
        "s_add_u32 m0, s35, 4096\n\t"
        "s_add_u32 s43, s34, 43008\n\t"
        "buffer_load_dwordx4 v143, s[16:19], s43 offen  lds\n\t"
        "v_cndmask_b32 v74, v75, v72, vcc\n\t"
        "v_mad_u32_u24 v134, v73, s11, v74\n\t"
        "s_bfe_u32 s70, s12, 524296\n\t"
        "s_bfe_u32 s71, s13, 524296\n\t"
        "v_cmp_gt_u32 vcc, s71, v71\n\t"
        "v_add3_u32 v73, v71, s70, -2\n\t"
        "v_med3_i32 v73, v73, 0, s10\n\t"
        "v_cndmask_b32 v74, v75, v72, vcc\n\t"
        "v_mad_u32_u24 v135, v73, s11, v74\n\t"
        "s_add_u32 m0, s35, 20480\n\t"
        "s_add_u32 s43, s34, 215040\n\t"
        "buffer_load_dwordx4 v143, s[16:19], s43 offen  lds\n\t"
        "s_bfe_u32 s70, s12, 524304\n\t"
        "s_bfe_u32 s71, s13, 524304\n\t"
        "v_cmp_gt_u32 vcc, s71, v71\n\t"
        "v_add3_u32 v73, v71, s70, -2\n\t"
        "v_med3_i32 v73, v73, 0, s10\n\t"
        "v_cndmask_b32 v74, v75, v72, vcc\n\t"
        "v_mad_u32_u24 v136, v73, s11, v74\n\t"
        "s_bfe_u32 s70, s12, 524312\n\t"
        "s_bfe_u32 s71, s13, 524312\n\t"
        "v_cmp_gt_u32 vcc, s71, v71\n\t"
        "v_add3_u32 v73, v71, s70, -2\n\t"
        "v_med3_i32 v73, v73, 0, s10\n\t"
        "v_cndmask_b32 v74, v75, v72, vcc\n\t"
        "v_mad_u32_u24 v137, v73, s11, v74\n\t"
        "s_mov_b64 exec, 0\n\t"
        "s_cmp_eq_u32 s4, 0\n\t"
        "s_cbranch_scc1 .LpY0%=\n\t"
        "s_cmp_eq_u32 s4, 1\n\t"
        "s_cbranch_scc1 .LpY1%=\n\t"
        "s_cmp_eq_u32 s4, 2\n\t"
        "s_cbranch_scc1 .LpY2%=\n\t"
        "s_nop 0\n\t"
        ".LrY%=:\n\t"
        "s_mov_b64 exec, -1\n\t"
        "s_waitcnt vmcnt(9)\n\t"
        "s_barrier\n\t"
        "ds_read_b64_tr_b16 a[0:1], v138 offset:6144\n\t"
        "ds_read_b64_tr_b16 a[2:3], v138 offset:6912\n\t"
        "ds_read_b64_tr_b16 a[4:5], v139 offset:6144\n\t"
        "ds_read_b64_tr_b16 a[6:7], v139 offset:6912\n\t"
        "ds_read_b64_tr_b16 a[8:9], v149 offset:6144\n\t"
        "ds_read_b64_tr_b16 a[10:11], v149 offset:6912\n\t"
        "ds_read_b128 v[84:87], v128 offset:8192\n\t"
        "ds_read_b64_tr_b16 a[12:13], v138 offset:12288\n\t"
        "ds_read_b64_tr_b16 a[14:15], v138 offset:13056\n\t"
        "ds_read_b64_tr_b16 a[16:17], v139 offset:12288\n\t"
        "ds_read_b64_tr_b16 a[18:19], v139 offset:13056\n\t"
        "ds_read_b64_tr_b16 a[20:21], v149 offset:12288\n\t"
        "ds_read_b64_tr_b16 a[22:23], v149 offset:13056\n\t"
        "ds_read_b128 v[88:91], v128 offset:8192\n\t"
        "s_add_u32 m0, s37, -12288\n\t"
        "s_add_u32 s36, s36, s39\n\t"
        "s_waitcnt lgkmcnt(7)\n\t"
        "v_mfma_f32_16x16x32_bf16 v[12:15], a[0:3], v[84:87], 0\n\t"
        "buffer_load_dwordx4 v140, s[24:27], s36 offen offset:0 lds\n\t"
        "buffer_load_dwordx4 v141, s[24:27], s36 offen offset:1024 lds\n\t"
        "buffer_load_dwordx4 v142, s[24:27], s36 offen offset:2048 lds\n\t"
        "s_waitcnt vmcnt(8)\n\t"
        "s_barrier\n\t"
        "ds_read_b128 v[92:95], v128 offset:12288\n\t"
        "ds_read_b64_tr_b16 a[24:25], v138 offset:0\n\t"
        "v_mfma_f32_16x16x32_bf16 v[16:19], a[4:7], v[84:87], 0\n\t"
        "ds_read_b64_tr_b16 a[26:27], v138 offset:768\n\t"
        "s_add_u32 m0, s35, 157696\n\t"
        "s_nop 0\n\t"
        "buffer_load_dwordx4 v64, s[20:23], 0 offen lds\n\t"
        "s_add_u32 m0, s35, 16384\n\t"
        "s_add_u32 s43, s34, 172032\n\t"
        "v_mfma_f32_16x16x32_bf16 v[20:23], a[8:11], v[84:87], 0\n\t"
        "buffer_load_dwordx4 v143, s[16:19], s43 offen  lds\n\t"
        "s_add_u32 m0, s35, 32768\n\t"
        "ds_read_b64_tr_b16 a[28:29], v139 offset:0\n\t"
        "s_add_u32 s43, s34, 344064\n\t"
        "s_min_u32 s43, s43, 9590784\n\t"
        "s_waitcnt lgkmcnt(4)\n\t"
        "v_mfma_f32_16x16x32_bf16 v[24:27], a[12:15], v[88:91], 0\n\t"
        "buffer_load_dwordx4 v143, s[16:19], s43 offen  lds\n\t"
        "s_add_u32 m0, s37, 0\n\t"
        "ds_read_b64_tr_b16 a[30:31], v139 offset:768\n\t"
        "s_add_u32 s36, s36, s38\n\t"
        "ds_read_b64_tr_b16 a[32:33], v149 offset:0\n\t"
        "v_mfma_f32_16x16x32_bf16 v[28:31], a[16:19], v[88:91], 0\n\t"
        "buffer_load_dwordx4 v140, s[24:27], s36 offen offset:0 lds\n\t"
        "ds_read_b64_tr_b16 a[34:35], v149 offset:768\n\t"
        "ds_read_b128 v[96:99], v128 offset:8192\n\t"
        "v_readlane_b32 s44, v155, 32\n\t"
        "v_mfma_f32_16x16x32_bf16 v[32:35], a[20:23], v[88:91], 0\n\t"
        "buffer_load_dwordx4 v141, s[24:27], s36 offen offset:1024 lds\n\t"
        "v_readlane_b32 s45, v155, 33\n\t"
        "s_waitcnt lgkmcnt(7)\n\t"
        "v_mfma_f32_16x16x32_bf16 v[24:27], a[0:3], v[92:95], v[24:27]\n\t"
        "ds_read_b128 v[100:103], v128 offset:12288\n\t"
        "v_readlane_b32 s62, v155, 36\n\t"
        "v_mfma_f32_16x16x32_bf16 v[28:31], a[4:7], v[92:95], v[28:31]\n\t"
        "s_nop 0\n\t"
        "s_lshl_b32 s70, s62, 5\n\t"
        "s_sub_u32 s71, s70, 16384\n\t"
        "v_mfma_f32_16x16x32_bf16 v[32:35], a[8:11], v[92:95], v[32:35]\n\t"
        "buffer_load_dwordx4 v142, s[24:27], s36 offen offset:2048 lds\n\t"
        "s_add_u32 m0, s35, 0\n\t"
        "s_add_u32 s43, s34, 0\n\t"
        "v_mfma_f32_16x16x32_bf16 v[36:39], a[12:15], v[92:95], 0\n\t"
        "s_mov_b32 exec_lo, 0\n\t"
        "s_mov_b32 exec_hi, 0xfff00000\n\t"
        "ds_write_b32 v119, v155\n\t"
        "s_mov_b64 exec, -1\n\t"
        "v_mfma_f32_16x16x32_bf16 v[40:43], a[16:19], v[92:95], 0\n\t"
        "s_mul_i32 s71, s71, s68\n\t"
        "v_add_u32 v172, s71, v128\n\t"
        "v_mfma_f32_16x16x32_bf16 v[44:47], a[20:23], v[92:95], 0\n\t"
        "buffer_load_dwordx4 v143, s[16:19], s43 offen  lds\n\t"
        "s_add_u32 m0, s35, 36864\n\t"
        "s_add_u32 s43, s34, 387072\n\t"
        "s_min_u32 s43, s43, 9590784\n\t"
        "buffer_load_dwordx4 v143, s[16:19], s43 offen  lds\n\t"
        "s_waitcnt vmcnt(8)\n\t"
        "s_barrier\n\t"
        "ds_read_b128 v[104:107], v128 offset:4096\n\t"
        "s_add_u32 m0, s37, 12288\n\t"
        "ds_read_b64_tr_b16 a[36:37], v138 offset:18432\n\t"
        "s_add_u32 s36, s36, s5\n\t"
        "s_waitcnt lgkmcnt(4)\n\t"
        "v_mfma_f32_16x16x32_bf16 v[0:3], a[24:27], v[96:99], 0\n\t"
        "buffer_load_dwordx4 v140, s[24:27], s36 offen offset:0 lds\n\t"
        "ds_read_b64_tr_b16 a[38:39], v138 offset:19200\n\t"
        "v_add_u32 v173, s71, v129\n\t"
        "v_mfma_f32_16x16x32_bf16 v[4:7], a[28:31], v[96:99], 0\n\t"
        "buffer_load_dwordx4 v141, s[24:27], s36 offen offset:1024 lds\n\t"
        "ds_read_b64_tr_b16 a[40:41], v139 offset:18432\n\t"
        "v_add_u32 v174, s71, v130\n\t"
        "v_mfma_f32_16x16x32_bf16 v[8:11], a[32:35], v[96:99], 0\n\t"
        "ds_read_b64_tr_b16 a[42:43], v139 offset:19200\n\t"
        "ds_read_b64_tr_b16 a[44:45], v149 offset:18432\n\t"
        "s_add_u32 s71, s71, s70\n\t"
        "v_add_u32 v175, s71, v128\n\t"
        "s_waitcnt lgkmcnt(7)\n\t"
        "v_mfma_f32_16x16x32_bf16 v[12:15], a[24:27], v[100:103], v[12:15]\n\t"
        "buffer_load_dwordx4 v142, s[24:27], s36 offen offset:2048 lds\n\t"
        "s_add_u32 m0, s37, 24576\n\t"
        "ds_read_b64_tr_b16 a[46:47], v149 offset:19200\n\t"
        "s_add_u32 s36, s36, s39\n\t"
        "ds_read_b64_tr_b16 a[48:49], v138 offset:24576\n\t"
        "v_mfma_f32_16x16x32_bf16 v[16:19], a[28:31], v[100:103], v[16:19]\n\t"
        "ds_read_b64_tr_b16 a[50:51], v138 offset:25344\n\t"
        "ds_read_b64_tr_b16 a[52:53], v139 offset:24576\n\t"
        "ds_read_b64_tr_b16 a[54:55], v139 offset:25344\n\t"
        "v_add_u32 v176, s71, v129\n\t"
        "v_add_u32 v177, s71, v130\n\t"
        "v_mfma_f32_16x16x32_bf16 v[20:23], a[32:35], v[100:103], v[20:23]\n\t"
        "ds_read_b64_tr_b16 a[56:57], v149 offset:24576\n\t"
        "ds_read_b64_tr_b16 a[58:59], v149 offset:25344\n\t"
        "ds_read_b128 v[108:111], v128 offset:4160\n\t"
        "s_add_u32 s71, s71, s70\n\t"
        "s_waitcnt lgkmcnt(11)\n\t"
        "v_mfma_f32_16x16x32_bf16 v[0:3], a[0:3], v[104:107], v[0:3]\n\t"
        "ds_read_b64_tr_b16 a[60:61], v138 offset:30720\n\t"
        "ds_read_b64_tr_b16 a[62:63], v138 offset:31488\n\t"
        "v_add_u32 v178, s71, v128\n\t"
        "v_add_u32 v179, s71, v129\n\t"
        "v_mfma_f32_16x16x32_bf16 v[4:7], a[4:7], v[104:107], v[4:7]\n\t"
        "ds_read_b64_tr_b16 a[64:65], v139 offset:30720\n\t"
        "ds_read_b64_tr_b16 a[66:67], v139 offset:31488\n\t"
        "v_add_u32 v180, s71, v130\n\t"
        "v_mfma_f32_16x16x32_bf16 v[8:11], a[8:11], v[104:107], v[8:11]\n\t"
        "ds_read_b64_tr_b16 a[68:69], v149 offset:30720\n\t"
        "ds_read_b64_tr_b16 a[70:71], v149 offset:31488\n\t"
        "v_add_f32 v78, v78, v56\n\t"
        "v_add_f32 v79, v79, v57\n\t"
        "v_mfma_f32_16x16x32_bf16 v[12:15], a[12:15], v[104:107], v[12:15]\n\t"
        "ds_read_b128 v[112:115], v128 offset:8256\n\t"
        "v_mfma_f32_16x16x32_bf16 v[16:19], a[16:19], v[104:107], v[16:19]\n\t"
        "s_cmp_eq_u32 s69, 36\n\t"
        "s_cselect_b64 exec, -1, 0\n\t"
        "v_swap_b32 v68, v69\n\t"
        "v_swap_b32 v173, v175\n\t"
        "v_swap_b32 v174, v178\n\t"
        "v_swap_b32 v177, v179\n\t"
        "s_mov_b64 exec, -1\n\t"
        "v_mfma_f32_16x16x32_bf16 v[20:23], a[20:23], v[104:107], v[20:23]\n\t"
        "ds_read_b128 v[84:87], v128 offset:12352\n\t"
        "v_add3_u32 v68, v68, v52, -1\n\t"
        "v_add3_u32 v69, v69, v158, -1\n\t"
        "s_waitcnt lgkmcnt(8)\n\t"
        "v_mfma_f32_16x16x32_bf16 v[0:3], a[36:39], v[108:111], v[0:3]\n\t"
        "ds_read_b64_tr_b16 a[72:73], v138 offset:36864\n\t"
        "ds_read_b64_tr_b16 a[74:75], v138 offset:37632\n\t"
        "v_cvt_f32_i32 v68, v68\n\t"
        "v_cvt_f32_i32 v69, v69\n\t"
        "v_mfma_f32_16x16x32_bf16 v[4:7], a[40:43], v[108:111], v[4:7]\n\t"
        "ds_read_b64_tr_b16 a[76:77], v139 offset:36864\n\t"
        "ds_read_b64_tr_b16 a[78:79], v139 offset:37632\n\t"
        "v_readlane_b32 s69, v155, 31\n\t"
        "v_mfma_f32_16x16x32_bf16 v[8:11], a[44:47], v[108:111], v[8:11]\n\t"
        "ds_read_b64_tr_b16 a[80:81], v149 offset:36864\n\t"
        "ds_read_b64_tr_b16 a[82:83], v149 offset:37632\n\t"
        "v_mov_b32 v154, s69\n\t"
        "v_mov_b32 v119, s62\n\t"
        "v_mfma_f32_16x16x32_bf16 v[12:15], a[48:51], v[108:111], v[12:15]\n\t"
        "v_readlane_b32 s53, v155, 28\n\t"
        "v_readlane_b32 s9, v155, 46\n\t"
        "v_mfma_f32_16x16x32_bf16 v[16:19], a[52:55], v[108:111], v[16:19]\n\t"
        "buffer_load_dwordx4 v140, s[24:27], s36 offen offset:0 lds\n\t"
        "s_sub_u32 s10, 43200, s11\n\t"
        "s_mul_i32 s10, s10, s68\n\t"
        "v_mfma_f32_16x16x32_bf16 v[20:23], a[56:59], v[108:111], v[20:23]\n\t"
        "s_sub_u32 s11, s11, 192\n\t"
        "v_mad_u32_u24 v148, v158, s11, v144\n\t"
        "s_waitcnt lgkmcnt(7)\n\t"
        "v_mfma_f32_16x16x32_bf16 v[0:3], a[60:63], v[112:115], v[0:3]\n\t"
        "v_add_u32 v148, s10, v148\n\t"
        "v_mad_u32_u24 v150, v83, s11, v145\n\t"
        "s_lshl_b32 s70, s49, 1\n\t"
        "v_mfma_f32_16x16x32_bf16 v[4:7], a[64:67], v[112:115], v[4:7]\n\t"
        "s_add_u32 s70, s70, 49\n\t"
        "v_readlane_b32 s52, v155, s70\n\t"
        "v_mfma_f32_16x16x32_bf16 v[8:11], a[68:71], v[112:115], v[8:11]\n\t"
        "buffer_load_dwordx4 v141, s[24:27], s36 offen offset:1024 lds\n\t"
        "s_add_u32 s52, s52, s10\n\t"
        "v_mfma_f32_16x16x32_bf16 v[12:15], a[36:39], v[112:115], v[12:15]\n\t"
        "v_mfma_f32_16x16x32_bf16 v[16:19], a[40:43], v[112:115], v[16:19]\n\t"
        "v_mfma_f32_16x16x32_bf16 v[20:23], a[44:47], v[112:115], v[20:23]\n\t"
        "v_mfma_f32_16x16x32_bf16 v[24:27], a[48:51], v[112:115], v[24:27]\n\t"
        "buffer_load_dwordx4 v142, s[24:27], s36 offen offset:2048 lds\n\t"
        "v_mfma_f32_16x16x32_bf16 v[28:31], a[52:55], v[112:115], v[28:31]\n\t"
        "v_mfma_f32_16x16x32_bf16 v[32:35], a[56:59], v[112:115], v[32:35]\n\t"
        "s_waitcnt lgkmcnt(0)\n\t"
        "v_mfma_f32_16x16x32_bf16 v[12:15], a[60:63], v[84:87], v[12:15]\n\t"
        "v_mfma_f32_16x16x32_bf16 v[16:19], a[64:67], v[84:87], v[16:19]\n\t"
        "buffer_load_dwordx4 v[48:51], v55, s[20:23], s48 offen\n\t"
        "v_mfma_f32_16x16x32_bf16 v[20:23], a[68:71], v[84:87], v[20:23]\n\t"
        "v_mfma_f32_16x16x32_bf16 v[24:27], a[36:39], v[84:87], v[24:27]\n\t"
        "ds_read_b128 v[88:91], v128 offset:4224\n\t"
        "v_mfma_f32_16x16x32_bf16 v[28:31], a[40:43], v[84:87], v[28:31]\n\t"
        "v_mfma_f32_16x16x32_bf16 v[32:35], a[44:47], v[84:87], v[32:35]\n\t"
        "buffer_load_dword v61, v61, s[20:23], s48 offen offset:512\n\t"
        "v_readlane_b32 s40, v155, 12\n\t"
        "v_readlane_b32 s43, v155, 0\n\t"
        "v_mfma_f32_16x16x32_bf16 v[36:39], a[48:51], v[84:87], v[36:39]\n\t"
        "ds_read_b128 v[92:95], v128 offset:8320\n\t"
        "s_add_u32 m0, s40, s35\n\t"
        "v_mfma_f32_16x16x32_bf16 v[40:43], a[52:55], v[84:87], v[40:43]\n\t"
        "v_mfma_f32_16x16x32_bf16 v[44:47], a[56:59], v[84:87], v[44:47]\n\t"
        "s_waitcnt lgkmcnt(1)\n\t"
        "v_mfma_f32_16x16x32_bf16 v[0:3], a[72:75], v[88:91], v[0:3]\n\t"
        "s_waitcnt vmcnt(10)\n\t"
        "s_barrier\n\t"
        "buffer_load_dwordx4 v134, s[16:19], s43 offen  lds\n\t"
        "ds_read_b128 v[96:99], v128 offset:16384\n\t"
        "v_mfma_f32_16x16x32_bf16 v[4:7], a[76:79], v[88:91], v[4:7]\n\t"
        "ds_read_b128 v[100:103], v128 offset:16448\n\t"
        "ds_read_b64_tr_b16 a[84:85], v131 offset:0\n\t"
        "v_readlane_b32 s40, v155, 13\n\t"
        "v_readlane_b32 s43, v155, 1\n\t"
        "v_mfma_f32_16x16x32_bf16 v[8:11], a[80:83], v[88:91], v[8:11]\n\t"
        "ds_read_b64_tr_b16 a[86:87], v131 offset:768\n\t"
        "s_add_u32 m0, s40, s35\n\t"
        "ds_read_b64_tr_b16 a[88:89], v132 offset:0\n\t"
        "s_waitcnt lgkmcnt(5)\n\t"
        "v_mfma_f32_16x16x32_bf16 v[12:15], a[72:75], v[92:95], v[12:15]\n\t"
        "ds_read_b64_tr_b16 a[90:91], v132 offset:768\n\t"
        "ds_read_b64_tr_b16 a[92:93], v133 offset:0\n\t"
        "v_readlane_b32 s46, v155, 37\n\t"
        "v_readlane_b32 s47, v155, 40\n\t"
        "v_mfma_f32_16x16x32_bf16 v[16:19], a[76:79], v[92:95], v[16:19]\n\t"
        "buffer_load_dwordx4 v135, s[16:19], s43 offen  lds\n\t"
        "ds_read_b64_tr_b16 a[94:95], v133 offset:768\n\t"
        "v_readlane_b32 s40, v155, 14\n\t"
        "v_readlane_b32 s43, v155, 2\n\t"
        "v_mfma_f32_16x16x32_bf16 v[20:23], a[80:83], v[92:95], v[20:23]\n\t"
        "ds_read_b128 v[104:107], v128 offset:4224\n\t"
        "s_add_u32 m0, s40, s35\n\t"
        "ds_read_b64_tr_b16 a[96:97], v131 offset:6144\n\t"
        "s_waitcnt lgkmcnt(9)\n\t"
        "v_mfma_f32_16x16x32_bf16 v[24:27], a[24:27], v[96:99], v[24:27]\n\t"
        "ds_read_b64_tr_b16 a[98:99], v131 offset:6912\n\t"
        "ds_read_b64_tr_b16 a[100:101], v132 offset:6144\n\t"
        "v_add_f32 v58, s46, v68\n\t"
        "v_add_f32 v59, s47, v69\n\t"
        "v_mfma_f32_16x16x32_bf16 v[28:31], a[28:31], v[96:99], v[28:31]\n\t"
        "ds_read_b64_tr_b16 a[102:103], v132 offset:6912\n\t"
        "ds_read_b64_tr_b16 a[104:105], v133 offset:6144\n\t"
        "v_readlane_b32 s47, v155, 42\n\t"
        "v_mfma_f32_16x16x32_bf16 v[32:35], a[32:35], v[96:99], v[32:35]\n\t"
        "buffer_load_dwordx4 v134, s[16:19], s43 offen  lds\n\t"
        "ds_read_b64_tr_b16 a[106:107], v133 offset:6912\n\t"
        "v_readlane_b32 s40, v155, 15\n\t"
        "v_readlane_b32 s43, v155, 3\n\t"
        "v_mfma_f32_16x16x32_bf16 v[36:39], a[0:3], v[96:99], v[36:39]\n\t"
        "ds_read_b128 v[108:111], v128 offset:8320\n\t"
        "s_add_u32 m0, s40, s35\n\t"
        "v_mfma_f32_16x16x32_bf16 v[40:43], a[4:7], v[96:99], v[40:43]\n\t"
        "ds_read_b32 v64, v70 offset:0\n\t"
        "v_mfma_f32_16x16x32_bf16 v[44:47], a[8:11], v[96:99], v[44:47]\n\t"
        "ds_read_b128 v[112:115], v128 offset:12416\n\t"
        "s_waitcnt lgkmcnt(10)\n\t"
        "v_mfma_f32_16x16x32_bf16 v[24:27], a[60:63], v[100:103], v[24:27]\n\t"
        "ds_read_b32 v67, v67 offset:0\n\t"
        "v_mfma_f32_16x16x32_bf16 v[28:31], a[64:67], v[100:103], v[28:31]\n\t"
        "ds_read_b32 v66, v70 offset:2560\n\t"
        "v_mfma_f32_16x16x32_bf16 v[32:35], a[68:71], v[100:103], v[32:35]\n\t"
        "buffer_load_dwordx4 v135, s[16:19], s43 offen  lds\n\t"
        "s_add_u32 m0, s37, 36864\n\t"
        "s_add_u32 s36, s36, s38\n\t"
        "v_mfma_f32_16x16x32_bf16 v[36:39], a[36:39], v[100:103], v[36:39]\n\t"
        "ds_read_b128 v[84:87], v128 offset:16512\n\t"
        "v_mfma_f32_16x16x32_bf16 v[40:43], a[40:43], v[100:103], v[40:43]\n\t"
        "v_mfma_f32_16x16x32_bf16 v[44:47], a[44:47], v[100:103], v[44:47]\n\t"
        "s_waitcnt lgkmcnt(7)\n\t"
        "v_mfma_f32_16x16x32_bf16 v[12:15], a[84:87], v[104:107], v[12:15]\n\t"
        "buffer_load_dwordx4 v140, s[24:27], s36 offen offset:0 lds\n\t"
        "v_mfma_f32_16x16x32_bf16 v[16:19], a[88:91], v[104:107], v[16:19]\n\t"
        "s_waitcnt lgkmcnt(4)\n\t"
        "v_perm_b32 v56, v64, v64, s44\n\t"
        "v_perm_b32 v57, v64, v64, s45\n\t"
        "v_add_f32 v56, v56, v58\n\t"
        "v_mfma_f32_16x16x32_bf16 v[20:23], a[92:95], v[104:107], v[20:23]\n\t"
        "v_add_f32 v57, v57, v59\n\t"
        "v_cvt_pk_bf16_f32 v52, v56, v57\n\t"
        "v_cvt_pk_bf16_f32 v58, v58, v59\n\t"
        "v_mfma_f32_16x16x32_bf16 v[0:3], a[96:99], v[108:111], v[0:3]\n\t"
        "v_cmp_ne_u32 s[64:65], v52, v58\n\t"
        "s_waitcnt lgkmcnt(2)\n\t"
        "v_perm_b32 v57, v67, v67, s45\n\t"
        "v_add_f32 v57, v57, v79\n\t"
        "v_mfma_f32_16x16x32_bf16 v[4:7], a[100:103], v[108:111], v[4:7]\n\t"
        "buffer_load_dwordx4 v141, s[24:27], s36 offen offset:1024 lds\n\t"
        "v_mbcnt_lo_u32_b32 v56, s64, 1\n\t"
        "v_mfma_f32_16x16x32_bf16 v[8:11], a[104:107], v[108:111], v[8:11]\n\t"
        "v_mbcnt_hi_u32_b32 v56, s65, v56\n\t"
        "s_bcnt1_i32_b64 s61, s[64:65]\n\t"
        "v_mfma_f32_16x16x32_bf16 v[24:27], a[84:87], v[108:111], v[24:27]\n\t"
        "v_add_u32 v54, s61, 1\n\t"
        "v_lshl_add_u32 v56, v56, 3, s60\n\t"
        "v_cvt_pk_bf16_f32 v58, v78, v79\n\t"
        "v_mfma_f32_16x16x32_bf16 v[28:31], a[88:91], v[108:111], v[28:31]\n\t"
        "s_mov_b64 exec, s[64:65]\n\t"
        "ds_write_b64 v56, v[52:53]\n\t"
        "s_mov_b64 exec, -1\n\t"
        "v_mfma_f32_16x16x32_bf16 v[32:35], a[92:95], v[108:111], v[32:35]\n\t"
        "s_waitcnt vmcnt(8) lgkmcnt(1)\n\t"
        "s_barrier\n\t"
        "buffer_load_dwordx4 v142, s[24:27], s36 offen offset:2048 lds\n\t"
        "s_add_u32 m0, s37, 49152\n\t"
        "ds_read_b128 v[88:91], v128 offset:0\n\t"
        "v_mfma_f32_16x16x32_bf16 v[12:15], a[96:99], v[112:115], v[12:15]\n\t"
        "ds_read_b128 v[92:95], v128 offset:64\n\t"
        "s_add_u32 s36, s36, s5\n\t"
        "ds_read_b128 v[96:99], v128 offset:128\n\t"
        "v_mfma_f32_16x16x32_bf16 v[16:19], a[100:103], v[112:115], v[16:19]\n\t"
        "ds_read_b64_tr_b16 a[108:109], v131 offset:12288\n\t"
        "ds_read_b64_tr_b16 a[110:111], v131 offset:13056\n\t"
        "v_swap_b32 v53, v80\n\t"
        "v_perm_b32 v56, v67, v67, s44\n\t"
        "v_mfma_f32_16x16x32_bf16 v[20:23], a[104:107], v[112:115], v[20:23]\n\t"
        "ds_read_b64_tr_b16 a[112:113], v132 offset:12288\n\t"
        "ds_read_b64_tr_b16 a[114:115], v132 offset:13056\n\t"
        "v_add_f32 v56, v56, v78\n\t"
        "v_cvt_pk_bf16_f32 v52, v56, v57\n\t"
        "v_mfma_f32_16x16x32_bf16 v[24:27], a[72:75], v[112:115], v[24:27]\n\t"
        "buffer_load_dwordx4 v140, s[24:27], s36 offen offset:0 lds\n\t"
        "ds_read_b128 v[100:103], v128 offset:20480\n\t"
        "v_cmp_ne_u32 s[64:65], v52, v58\n\t"
        "v_mfma_f32_16x16x32_bf16 v[28:31], a[76:79], v[112:115], v[28:31]\n\t"
        "ds_read_b64_tr_b16 a[116:117], v133 offset:12288\n\t"
        "ds_read_b64_tr_b16 a[118:119], v133 offset:13056\n\t"
        "s_and_b64 s[64:65], s[64:65], 0xffffff\n\t"
        "v_mbcnt_lo_u32_b32 v56, s64, v54\n\t"
        "v_mfma_f32_16x16x32_bf16 v[32:35], a[80:83], v[112:115], v[32:35]\n\t"
        "ds_read_b64_tr_b16 a[120:121], v131 offset:18432\n\t"
        "ds_read_b64_tr_b16 a[122:123], v131 offset:19200\n\t"
        "s_bcnt1_i32_b64 s46, s[64:65]\n\t"
        "v_mfma_f32_16x16x32_bf16 v[36:39], a[84:87], v[112:115], v[36:39]\n\t"
        "ds_read_b128 v[104:107], v128 offset:20544\n\t"
        "ds_read_b64_tr_b16 a[124:125], v132 offset:18432\n\t"
        "s_add_u32 s61, s61, s46\n\t"
        "v_add_u32 v54, s46, v54\n\t"
        "v_mfma_f32_16x16x32_bf16 v[40:43], a[88:91], v[112:115], v[40:43]\n\t"
        "buffer_load_dwordx4 v141, s[24:27], s36 offen offset:1024 lds\n\t"
        "ds_read_b64_tr_b16 a[126:127], v132 offset:19200\n\t"
        "s_mov_b32 s3, s61\n\t"
        "v_mfma_f32_16x16x32_bf16 v[44:47], a[92:95], v[112:115], v[44:47]\n\t"
        "ds_read_b64_tr_b16 a[128:129], v133 offset:18432\n\t"
        "ds_read_b64_tr_b16 a[130:131], v133 offset:19200\n\t"
        "v_lshl_add_u32 v56, v56, 3, s60\n\t"
        "v_readlane_b32 s46, v155, 39\n\t"
        "v_mfma_f32_16x16x32_bf16 v[24:27], a[96:99], v[84:87], v[24:27]\n\t"
        "s_mov_b64 exec, s[64:65]\n\t"
        "ds_write_b64 v56, v[52:53]\n\t"
        "s_mov_b64 exec, -1\n\t"
        "ds_read_b128 v[108:111], v129 offset:0\n\t"
        "v_mfma_f32_16x16x32_bf16 v[28:31], a[100:103], v[84:87], v[28:31]\n\t"
        "ds_read_b64_tr_b16 a[132:133], v131 offset:24576\n\t"
        "ds_read_b64_tr_b16 a[134:135], v131 offset:25344\n\t"
        "v_add_u32 v53, 4096, v80\n\t"
        "v_perm_b32 v56, v66, v66, s44\n\t"
        "v_mfma_f32_16x16x32_bf16 v[32:35], a[104:107], v[84:87], v[32:35]\n\t"
        "buffer_load_dwordx4 v142, s[24:27], s36 offen offset:2048 lds\n\t"
        "ds_read_b64_tr_b16 a[136:137], v132 offset:24576\n\t"
        "v_readlane_b32 s40, v155, 16\n\t"
        "v_mfma_f32_16x16x32_bf16 v[36:39], a[72:75], v[84:87], v[36:39]\n\t"
        "ds_read_b128 v[112:115], v129 offset:4096\n\t"
        "ds_read_b64_tr_b16 a[138:139], v132 offset:25344\n\t"
        "v_readlane_b32 s43, v155, 4\n\t"
        "s_add_u32 m0, s40, s35\n\t"
        "v_mfma_f32_16x16x32_bf16 v[40:43], a[76:79], v[84:87], v[40:43]\n\t"
        "ds_read_b64_tr_b16 a[140:141], v133 offset:24576\n\t"
        "ds_read_b64_tr_b16 a[142:143], v133 offset:25344\n\t"
        "v_perm_b32 v57, v66, v66, s45\n\t"
        "v_mfma_f32_16x16x32_bf16 v[44:47], a[80:83], v[84:87], v[44:47]\n\t"
        "ds_read_b64_tr_b16 a[144:145], v131 offset:30720\n\t"
        "ds_read_b64_tr_b16 a[146:147], v131 offset:31488\n\t"
        "v_add_f32 v58, s46, v68\n\t"
        "s_waitcnt lgkmcnt(11)\n\t"
        "v_mfma_f32_16x16x32_bf16 v[0:3], a[12:15], v[88:91], v[0:3]\n\t"
        "buffer_load_dwordx4 v134, s[16:19], s43 offen  lds\n\t"
        "ds_read_b64_tr_b16 a[148:149], v132 offset:30720\n\t"
        "v_readlane_b32 s40, v155, 17\n\t"
        "v_readlane_b32 s43, v155, 5\n\t"
        "v_mfma_f32_16x16x32_bf16 v[4:7], a[16:19], v[88:91], v[4:7]\n\t"
        "ds_read_b64_tr_b16 a[150:151], v132 offset:31488\n\t"
        "s_add_u32 m0, s40, s35\n\t"
        "ds_read_b64_tr_b16 a[152:153], v133 offset:30720\n\t"
        "v_mfma_f32_16x16x32_bf16 v[8:11], a[20:23], v[88:91], v[8:11]\n\t"
        "ds_read_b64_tr_b16 a[154:155], v133 offset:31488\n\t"
        "v_add_f32 v59, s47, v69\n\t"
        "v_add_f32 v56, v56, v58\n\t"
        "v_mfma_f32_16x16x32_bf16 v[0:3], a[48:51], v[92:95], v[0:3]\n\t"
        "ds_read_b128 v[84:87], v129 offset:8192\n\t"
        "v_add_f32 v57, v57, v59\n\t"
        "v_mfma_f32_16x16x32_bf16 v[4:7], a[52:55], v[92:95], v[4:7]\n\t"
        "buffer_load_dwordx4 v135, s[16:19], s43 offen  lds\n\t"
        "v_readlane_b32 s40, v155, 18\n\t"
        "v_readlane_b32 s43, v155, 6\n\t"
        "v_mfma_f32_16x16x32_bf16 v[8:11], a[56:59], v[92:95], v[8:11]\n\t"
        "s_add_u32 m0, s40, s35\n\t"
        "v_max_f32 v56, 0, v56\n\t"
        "v_mfma_f32_16x16x32_bf16 v[0:3], a[84:87], v[96:99], v[0:3]\n\t"
        "v_max_f32 v57, 0, v57\n\t"
        "v_max_f32 v58, 0, v58\n\t"
        "v_mfma_f32_16x16x32_bf16 v[4:7], a[88:91], v[96:99], v[4:7]\n\t"
        "v_max_f32 v59, 0, v59\n\t"
        "v_cvt_pk_bf16_f32 v52, v56, v57\n\t"
        "v_cvt_pk_bf16_f32 v58, v58, v59\n\t"
        "v_mfma_f32_16x16x32_bf16 v[8:11], a[92:95], v[96:99], v[8:11]\n\t"
        "buffer_load_dwordx4 v136, s[16:19], s43 offen  lds\n\t"
        "v_readlane_b32 s40, v155, 19\n\t"
        "v_mfma_f32_16x16x32_bf16 v[36:39], a[24:27], v[100:103], v[36:39]\n\t"
        "v_readlane_b32 s43, v155, 7\n\t"
        "s_add_u32 m0, s40, s35\n\t"
        "v_cmp_ne_u32 s[64:65], v52, v58\n\t"
        "v_mfma_f32_16x16x32_bf16 v[40:43], a[28:31], v[100:103], v[40:43]\n\t"
        "s_nop 0\n\t"
        "v_mbcnt_lo_u32_b32 v56, s64, v54\n\t"
        "v_mbcnt_hi_u32_b32 v56, s65, v56\n\t"
        "v_mfma_f32_16x16x32_bf16 v[44:47], a[32:35], v[100:103], v[44:47]\n\t"
        ".LpY0%=:\n\t"
        "s_cbranch_execz .LrY%=\n\t"
        "s_bcnt1_i32_b64 s46, s[64:65]\n\t"
        "s_add_u32 s61, s61, s46\n\t"
        "v_mfma_f32_16x16x32_bf16 v[36:39], a[60:63], v[104:107], v[36:39]\n\t"
        "buffer_load_dwordx4 v137, s[16:19], s43 offen  lds\n\t"
        "s_add_u32 m0, s37, 0\n\t"
        "ds_read_b128 v[88:91], v129 offset:12288\n\t"
        "v_mfma_f32_16x16x32_bf16 v[40:43], a[64:67], v[104:107], v[40:43]\n\t"
        "s_add_u32 s36, s36, s39\n\t"
        "s_waitcnt vmcnt(15)\n\t"
        "v_and_b32 v48, s54, v48\n\t"
        "v_pk_max_u16 v60, v154, v48\n\t"
        "v_mfma_f32_16x16x32_bf16 v[44:47], a[68:71], v[104:107], v[44:47]\n\t"
        "v_and_b32 v49, s54, v49\n\t"
        "v_pk_max_u16 v60, v60, v49\n\t"
        "v_and_b32 v50, s54, v50\n\t"
        "s_waitcnt lgkmcnt(1)\n\t"
        "v_mfma_f32_16x16x32_bf16 v[0:3], a[120:123], v[108:111], v[0:3]\n\t"
        "v_pk_max_u16 v60, v60, v50\n\t"
        "v_and_b32 v51, s54, v51\n\t"
        "v_mfma_f32_16x16x32_bf16 v[4:7], a[124:127], v[108:111], v[4:7]\n\t"
        "buffer_load_dwordx4 v140, s[24:27], s36 offen offset:0 lds\n\t"
        "v_pk_max_u16 v60, v60, v51\n\t"
        "v_mfma_f32_16x16x32_bf16 v[8:11], a[128:131], v[108:111], v[8:11]\n\t"
        "s_waitcnt vmcnt(15)\n\t"
        "v_and_b32 v61, s54, v61\n\t"
        "v_pk_max_u16 v60, v60, v61\n\t"
        "v_cmp_ne_u32 s[12:13], v60, v154\n\t"
        "v_mfma_f32_16x16x32_bf16 v[0:3], a[108:111], v[112:115], v[0:3]\n\t"
        "s_lshl_b32 s6, 256, s4\n\t"
        "v_min_u32 v56, 97, v56\n\t"
        "v_mfma_f32_16x16x32_bf16 v[4:7], a[112:115], v[112:115], v[4:7]\n\t"
        "s_cmp_lg_u64 s[12:13], 0\n\t"
        "s_cselect_b32 s6, s6, 0\n\t"
        "v_mfma_f32_16x16x32_bf16 v[8:11], a[116:119], v[112:115], v[8:11]\n\t"
        "buffer_load_dwordx4 v141, s[24:27], s36 offen offset:1024 lds\n\t"
        "v_lshl_add_u32 v56, v56, 3, s60\n\t"
        "v_mfma_f32_16x16x32_bf16 v[12:15], a[120:123], v[112:115], v[12:15]\n\t"
        "s_mov_b64 exec, s[64:65]\n\t"
        "ds_write_b64 v56, v[52:53]\n\t"
        "s_mov_b64 exec, -1\n\t"
        "v_mfma_f32_16x16x32_bf16 v[16:19], a[124:127], v[112:115], v[16:19]\n\t"
        "s_cmp_gt_u32 s61, s53\n\t"
        "s_cselect_b32 s8, 1, 0\n\t"
        "s_or_b32 s56, s56, s8\n\t"
        "v_mfma_f32_16x16x32_bf16 v[20:23], a[128:131], v[112:115], v[20:23]\n\t"
        "s_cmp_gt_u32 s3, 64\n\t"
        "s_cselect_b32 s8, 1, 0\n\t"
        "s_or_b32 s56, s56, s8\n\t"
        "v_mfma_f32_16x16x32_bf16 v[0:3], a[132:135], v[84:87], v[0:3]\n\t"
        "s_waitcnt vmcnt(9)\n\t"
        "s_barrier\n\t"
        "buffer_load_dwordx4 v142, s[24:27], s36 offen offset:2048 lds\n\t"
        "s_add_u32 m0, s37, 12288\n\t"
        "ds_read_b128 v[92:95], v129 offset:16384\n\t"
        "v_mfma_f32_16x16x32_bf16 v[4:7], a[136:139], v[84:87], v[4:7]\n\t"
        "ds_read_b64_tr_b16 a[156:157], v131 offset:36864\n\t"
        "s_add_u32 s36, s36, s38\n\t"
        "ds_read_b64_tr_b16 a[158:159], v131 offset:37632\n\t"
        "v_mfma_f32_16x16x32_bf16 v[8:11], a[140:143], v[84:87], v[8:11]\n\t"
        "ds_read_b64_tr_b16 a[160:161], v132 offset:36864\n\t"
        "ds_read_b64_tr_b16 a[162:163], v132 offset:37632\n\t"
        "s_min_u32 s61, s61, 80\n\t"
        "v_min_u32 v78, s61, v116\n\t"
        "v_mfma_f32_16x16x32_bf16 v[12:15], a[108:111], v[84:87], v[12:15]\n\t"
        "ds_read_b128 v[96:99], v128 offset:20608\n\t"
        "ds_read_b64_tr_b16 a[164:165], v133 offset:36864\n\t"
        "v_lshl_add_u32 v78, v78, 3, s60\n\t"
        "v_mfma_f32_16x16x32_bf16 v[16:19], a[112:115], v[84:87], v[16:19]\n\t"
        "ds_read_b64 v[62:63], v78\n\t"
        "buffer_load_dwordx4 v140, s[24:27], s36 offen offset:0 lds\n\t"
        "v_cmp_ge_u32 s[66:67], s61, v116\n\t"
        "v_mfma_f32_16x16x32_bf16 v[20:23], a[116:119], v[84:87], v[20:23]\n\t"
        "ds_read_b64_tr_b16 a[166:167], v133 offset:37632\n\t"
        "ds_read_b64_tr_b16 a[168:169], v131 offset:43008\n\t"
        "s_mov_b32 s46, 0x07060302\n\t"
        "v_mfma_f32_16x16x32_bf16 v[24:27], a[120:123], v[84:87], v[24:27]\n\t"
        "ds_read_b128 v[100:103], v129 offset:64\n\t"
        "ds_read_b64_tr_b16 a[170:171], v131 offset:43776\n\t"
        "v_mfma_f32_16x16x32_bf16 v[28:31], a[124:127], v[84:87], v[28:31]\n\t"
        "ds_read_b64_tr_b16 a[172:173], v132 offset:43008\n\t"
        "ds_read_b64_tr_b16 a[174:175], v132 offset:43776\n\t"
        "v_mfma_f32_16x16x32_bf16 v[32:35], a[128:131], v[84:87], v[32:35]\n\t"
        "s_waitcnt vmcnt(8)\n\t"
        "s_barrier\n\t"
        "buffer_load_dwordx4 v141, s[24:27], s36 offen offset:1024 lds\n\t"
        "ds_read_b64_tr_b16 a[176:177], v133 offset:43008\n\t"
        "s_waitcnt lgkmcnt(11)\n\t"
        "v_mfma_f32_16x16x32_bf16 v[12:15], a[132:135], v[88:91], v[12:15]\n\t"
        "ds_read_b128 v[104:107], v129 offset:4160\n\t"
        "ds_read_b64_tr_b16 a[178:179], v133 offset:43776\n\t"
        "v_mfma_f32_16x16x32_bf16 v[16:19], a[136:139], v[88:91], v[16:19]\n\t"
        "ds_read_b64_tr_b16 a[180:181], v131 offset:49152\n\t"
        "ds_read_b64_tr_b16 a[182:183], v131 offset:49920\n\t"
        "v_mfma_f32_16x16x32_bf16 v[20:23], a[140:143], v[88:91], v[20:23]\n\t"
        "ds_read_b64_tr_b16 a[184:185], v132 offset:49152\n\t"
        "ds_read_b64_tr_b16 a[186:187], v132 offset:49920\n\t"
        "v_mfma_f32_16x16x32_bf16 v[24:27], a[108:111], v[88:91], v[24:27]\n\t"
        "buffer_load_dwordx4 v142, s[24:27], s36 offen offset:2048 lds\n\t"
        "s_add_u32 m0, s37, 24576\n\t"
        "ds_read_b64_tr_b16 a[188:189], v133 offset:49152\n\t"
        "v_mfma_f32_16x16x32_bf16 v[28:31], a[112:115], v[88:91], v[28:31]\n\t"
        "s_mov_b64 exec, s[66:67]\n\t"
        "s_waitcnt lgkmcnt(12)\n\t"
        "ds_write_b32 v63, v117\n\t"
        "s_mov_b64 exec, -1\n\t"
        "s_add_u32 s36, s36, s5\n\t"
        "ds_read_b64_tr_b16 a[190:191], v133 offset:49920\n\t"
        "v_mfma_f32_16x16x32_bf16 v[32:35], a[116:119], v[88:91], v[32:35]\n\t"
        "ds_read_b64_tr_b16 a[192:193], v131 offset:55296\n\t"
        "ds_read_b64_tr_b16 a[194:195], v131 offset:56064\n\t"
        "v_and_b32 v78, 0xfffff800, v63\n\t"
        "v_lshlrev_b32 v72, 16, v62\n\t"
        "v_mfma_f32_16x16x32_bf16 v[36:39], a[120:123], v[88:91], v[36:39]\n\t"
        "ds_read_b128 v[64:67], v78 offset:1536\n\t"
        "ds_read_b128 v[108:111], v129 offset:8256\n\t"
        "v_and_b32 v73, 0xffff0000, v62\n\t"
        "v_med3_f32 v72, v72, 0, s63\n\t"
        "v_mfma_f32_16x16x32_bf16 v[40:43], a[124:127], v[88:91], v[40:43]\n\t"
        "buffer_load_dwordx4 v140, s[24:27], s36 offen offset:0 lds\n\t"
        "ds_read_b64_tr_b16 a[196:197], v132 offset:55296\n\t"
        "v_med3_f32 v73, v73, 0, s63\n\t"
        "v_mfma_f32_16x16x32_bf16 v[44:47], a[128:131], v[88:91], v[44:47]\n\t"
        "ds_read_b64_tr_b16 a[198:199], v132 offset:56064\n\t"
        "ds_read_b64_tr_b16 a[200:201], v133 offset:55296\n\t"
        "v_perm_b32 v62, v73, v72, s46\n\t"
        "v_mfma_f32_16x16x32_bf16 v[24:27], a[132:135], v[92:95], v[24:27]\n\t"
        "ds_read_b64_tr_b16 a[202:203], v133 offset:56064\n\t"
        "v_fract_f32 v74, v72\n\t"
        "v_fract_f32 v75, v73\n\t"
        "v_mfma_f32_16x16x32_bf16 v[28:31], a[136:139], v[92:95], v[28:31]\n\t"
        "v_cvt_u32_f32 v76, v72\n\t"
        "v_cvt_u32_f32 v77, v73\n\t"
        "v_mfma_f32_16x16x32_bf16 v[32:35], a[140:143], v[92:95], v[32:35]\n\t"
        "v_cmp_neq_f32 s[64:65], 0, v74\n\t"
        "v_cmp_neq_f32 s[44:45], 0, v75\n\t"
        "v_mfma_f32_16x16x32_bf16 v[36:39], a[108:111], v[92:95], v[36:39]\n\t"
        "v_sub_f32 v78, 1.0, v74\n\t"
        "v_sub_f32 v79, 1.0, v75\n\t"
        "v_cvt_pk_bf16_f32 v79, v78, v79\n\t"
        "v_mfma_f32_16x16x32_bf16 v[40:43], a[112:115], v[92:95], v[40:43]\n\t"
        "v_lshlrev_b32 v78, 16, v79\n\t"
        "v_and_b32 v79, 0xffff0000, v79\n\t"
        "v_mfma_f32_16x16x32_bf16 v[44:47], a[116:119], v[92:95], v[44:47]\n\t"
        "buffer_load_dwordx4 v141, s[24:27], s36 offen offset:1024 lds\n\t"
        "v_mul_f32 v80, v78, v79\n\t"
        "v_mfma_f32_16x16x32_bf16 v[36:39], a[96:99], v[96:99], v[36:39]\n\t"
        "ds_read_b128 v[112:115], v129 offset:128\n\t"
        "v_mul_f32 v78, v78, v75\n\t"
        "v_mul_f32 v79, v74, v79\n\t"
        "v_mfma_f32_16x16x32_bf16 v[40:43], a[100:103], v[96:99], v[40:43]\n\t"
        "v_mul_f32 v81, v74, v75\n\t"
        "v_cndmask_b32 v78, v79, v78, s[44:45]\n\t"
        "v_mfma_f32_16x16x32_bf16 v[44:47], a[104:107], v[96:99], v[44:47]\n\t"
        "v_cndmask_b32 v79, 0, v79, s[44:45]\n\t"
        "v_cvt_pk_bf16_f32 v70, v80, v78\n\t"
        "s_waitcnt lgkmcnt(2)\n\t"
        "v_mfma_f32_16x16x32_bf16 v[0:3], a[156:159], v[100:103], v[0:3]\n\t"
        "buffer_load_dwordx4 v142, s[24:27], s36 offen offset:2048 lds\n\t"
        "s_add_u32 m0, s37, 36864\n\t"
        "ds_read_b128 v[84:87], v129 offset:4224\n\t"
        "v_mfma_f32_16x16x32_bf16 v[4:7], a[160:163], v[100:103], v[4:7]\n\t"
        "s_add_u32 s36, s36, s39\n\t"
        "v_cvt_pk_bf16_f32 v71, v79, v81\n\t"
        "v_pk_max_u16 v78, v62, v64\n\t"
        "v_mfma_f32_16x16x32_bf16 v[8:11], a[164:167], v[100:103], v[8:11]\n\t"
        "v_pk_min_u16 v78, v78, v65\n\t"
        "v_cmp_ne_u32 vcc, v78, v62\n\t"
        "v_mfma_f32_16x16x32_bf16 v[0:3], a[144:147], v[104:107], v[0:3]\n\t"
        "v_sub_u32 v78, v77, v67\n\t"
        "v_mad_u32_u24 v79, v77, 6, v66\n\t"
        "v_mfma_f32_16x16x32_bf16 v[4:7], a[148:151], v[104:107], v[4:7]\n\t"
        "v_mad_u32_u24 v79, v76, s62, v79\n\t"
        "s_and_b64 vcc, vcc, s[66:67]\n\t"
        "v_mfma_f32_16x16x32_bf16 v[8:11], a[152:155], v[104:107], v[8:11]\n\t"
        "s_waitcnt vmcnt(6) lgkmcnt(2)\n\t"
        "s_barrier\n\t"
        "buffer_load_dwordx4 v140, s[24:27], s36 offen offset:0 lds\n\t"
        "ds_read_b64_tr_b16 a[204:205], v131 offset:0\n\t"
        "v_mfma_f32_16x16x32_bf16 v[12:15], a[156:159], v[104:107], v[12:15]\n\t"
        "ds_read_b128 v[88:91], v129 offset:12352\n\t"
        "ds_read_b64_tr_b16 a[206:207], v131 offset:768\n\t"
        "s_or_b64 s[56:57], s[56:57], vcc\n\t"
        "v_bfe_u32 v80, v78, 2, 1\n\t"
        "v_mfma_f32_16x16x32_bf16 v[16:19], a[160:163], v[104:107], v[16:19]\n\t"
        "ds_read_b64_tr_b16 a[208:209], v132 offset:0\n\t"
        "ds_read_b64_tr_b16 a[210:211], v132 offset:768\n\t"
        "v_or_b32 v80, v80, v79\n\t"
        "v_mfma_f32_16x16x32_bf16 v[20:23], a[164:167], v[104:107], v[20:23]\n\t"
        "ds_read_b64_tr_b16 a[212:213], v133 offset:0\n\t"
        "ds_read_b64_tr_b16 a[214:215], v133 offset:768\n\t"
        "v_add_u32 v78, 1, v78\n\t"
        "v_mfma_f32_16x16x32_bf16 v[0:3], a[168:171], v[108:111], v[0:3]\n\t"
        "buffer_load_dwordx4 v141, s[24:27], s36 offen offset:1024 lds\n\t"
        "ds_read_b64_tr_b16 a[216:217], v131 offset:6144\n\t"
        "v_bfe_u32 v78, v78, 2, 1\n\t"
        "v_mfma_f32_16x16x32_bf16 v[4:7], a[172:175], v[108:111], v[4:7]\n\t"
        "ds_read_b64_tr_b16 a[218:219], v131 offset:6912\n\t"
        "ds_read_b64_tr_b16 a[220:221], v132 offset:6144\n\t"
        "v_add3_u32 v78, v79, v78, 6\n\t"
        "v_cndmask_b32 v78, v80, v78, s[44:45]\n\t"
        "v_mfma_f32_16x16x32_bf16 v[8:11], a[176:179], v[108:111], v[8:11]\n\t"
        "ds_read_b64_tr_b16 a[222:223], v132 offset:6912\n\t"
        "ds_read_b64_tr_b16 a[224:225], v133 offset:6144\n\t"
        "v_cndmask_b32 v79, 0, v119, s[64:65]\n\t"
        "v_mfma_f32_16x16x32_bf16 v[12:15], a[144:147], v[108:111], v[12:15]\n\t"
        "ds_read_b64_tr_b16 a[226:227], v133 offset:6912\n\t"
        "v_add_u32 v81, v80, v79\n\t"
        "v_mfma_f32_16x16x32_bf16 v[16:19], a[148:151], v[108:111], v[16:19]\n\t"
        "buffer_load_dwordx4 v142, s[24:27], s36 offen offset:2048 lds\n\t"
        "s_add_u32 m0, s37, 49152\n\t"
        "v_mfma_f32_16x16x32_bf16 v[20:23], a[152:155], v[108:111], v[20:23]\n\t"
        "s_waitcnt vmcnt(6)\n\t"
        "s_barrier\n\t"
        "ds_read_b64_tr_b16 a[228:229], v131 offset:12288\n\t"
        "s_add_u32 s36, s36, s38\n\t"
        "ds_read_b64_tr_b16 a[230:231], v131 offset:13056\n\t"
        "v_mfma_f32_16x16x32_bf16 v[24:27], a[156:159], v[108:111], v[24:27]\n\t"
        "ds_read_b128 v[92:95], v129 offset:8320\n\t"
        "ds_read_b64_tr_b16 a[232:233], v132 offset:12288\n\t"
        "v_add_u32 v82, v78, v79\n\t"
        "v_cndmask_b32 v78, v81, v78, s[44:45]\n\t"
        "v_mfma_f32_16x16x32_bf16 v[28:31], a[160:163], v[108:111], v[28:31]\n\t"
        "ds_read_b64_tr_b16 a[234:235], v132 offset:13056\n\t"
        "ds_read_b64_tr_b16 a[236:237], v133 offset:12288\n\t"
        "v_lshl_or_b32 v68, v78, 16, v80\n\t"
        "v_mfma_f32_16x16x32_bf16 v[32:35], a[164:167], v[108:111], v[32:35]\n\t"
        "buffer_load_dwordx4 v140, s[24:27], s36 offen offset:0 lds\n\t"
        "ds_read_b64_tr_b16 a[238:239], v133 offset:13056\n\t"
        "v_lshl_or_b32 v69, v82, 16, v81\n\t"
        "s_waitcnt lgkmcnt(10)\n\t"
        "v_mfma_f32_16x16x32_bf16 v[0:3], a[192:195], v[112:115], v[0:3]\n\t"
        "ds_write_b128 v118, v[68:71]\n\t"
        "v_mfma_f32_16x16x32_bf16 v[4:7], a[196:199], v[112:115], v[4:7]\n\t"
        "ds_read_b128 v[160:163], v185 offset:0\n\t"
        "v_xor_b32 v154, 32, v186\n\t"
        "v_mfma_f32_16x16x32_bf16 v[8:11], a[200:203], v[112:115], v[8:11]\n\t"
        "ds_read_b64_tr_b16 a[240:241], v131 offset:18432\n\t"
        "v_mfma_f32_16x16x32_bf16 v[0:3], a[180:183], v[84:87], v[0:3]\n\t"
        "buffer_load_dwordx4 v141, s[24:27], s36 offen offset:1024 lds\n\t"
        "v_mfma_f32_16x16x32_bf16 v[4:7], a[184:187], v[84:87], v[4:7]\n\t"
        "ds_read_b64_tr_b16 a[242:243], v131 offset:19200\n\t"
        "v_mfma_f32_16x16x32_bf16 v[8:11], a[188:191], v[84:87], v[8:11]\n\t"
        "ds_read_b64_tr_b16 a[244:245], v132 offset:18432\n\t"
        "v_mfma_f32_16x16x32_bf16 v[12:15], a[192:195], v[84:87], v[12:15]\n\t"
        "ds_read_b128 v[96:99], v130 offset:0\n\t"
        "ds_read_b64_tr_b16 a[246:247], v132 offset:19200\n\t"
        "v_mfma_f32_16x16x32_bf16 v[16:19], a[196:199], v[84:87], v[16:19]\n\t"
        "buffer_load_dwordx4 v142, s[24:27], s36 offen offset:2048 lds\n\t"
        "ds_read_b64_tr_b16 a[248:249], v133 offset:18432\n\t"
        "v_mfma_f32_16x16x32_bf16 v[20:23], a[200:203], v[84:87], v[20:23]\n\t"
        "ds_read_b64_tr_b16 a[250:251], v133 offset:19200\n\t"
        "v_mfma_f32_16x16x32_bf16 v[12:15], a[168:171], v[88:91], v[12:15]\n\t"
        "ds_read_b128 v[100:103], v130 offset:4096\n\t"
        "v_mfma_f32_16x16x32_bf16 v[16:19], a[172:175], v[88:91], v[16:19]\n\t"
        "s_waitcnt lgkmcnt(8)\n\t"
        "v_lshlrev_b32_sdwa v157, 5, v160 dst_sel:DWORD dst_unused:UNUSED_PAD src0_sel:DWORD src1_sel:WORD_0\n\t"
        "v_lshlrev_b32_sdwa v158, 5, v160 dst_sel:DWORD dst_unused:UNUSED_PAD src0_sel:DWORD src1_sel:WORD_1\n\t"
        "v_or_b32 v157, v184, v157\n\t"
        "v_mfma_f32_16x16x32_bf16 v[20:23], a[176:179], v[88:91], v[20:23]\n\t"
        "ds_read_b64_tr_b16 v[60:61], v157 offset:0\n\t"
        "v_or_b32 v158, v184, v158\n\t"
        "v_xor_b32 v159, 32, v157\n\t"
        "v_mfma_f32_16x16x32_bf16 v[24:27], a[144:147], v[88:91], v[24:27]\n\t"
        "ds_read_b64_tr_b16 v[62:63], v158 offset:0\n\t"
        "v_xor_b32 v152, 32, v158\n\t"
        "v_mfma_f32_16x16x32_bf16 v[28:31], a[148:151], v[88:91], v[28:31]\n\t"
        "ds_read_b64_tr_b16 v[64:65], v159 offset:0\n\t"
        "v_perm_b32 v164, v162, v162, v168\n\t"
        "v_mfma_f32_16x16x32_bf16 v[32:35], a[152:155], v[88:91], v[32:35]\n\t"
        "ds_read_b64_tr_b16 v[66:67], v152 offset:0\n\t"
        "v_perm_b32 v165, v162, v162, v169\n\t"
        "v_mfma_f32_16x16x32_bf16 v[36:39], a[156:159], v[88:91], v[36:39]\n\t"
        "ds_read_b64_tr_b16 v[68:69], v157 offset:64\n\t"
        "v_perm_b32 v166, v162, v162, v170\n\t"
        "v_perm_b32 v167, v162, v162, v171\n\t"
        "v_mfma_f32_16x16x32_bf16 v[40:43], a[160:163], v[88:91], v[40:43]\n\t"
        "ds_read_b64_tr_b16 v[70:71], v158 offset:64\n\t"
        "v_mfma_f32_16x16x32_bf16 v[44:47], a[164:167], v[88:91], v[44:47]\n\t"
        "s_waitcnt vmcnt(3) lgkmcnt(7)\n\t"
        "s_barrier\n\t"
        "v_mfma_f32_16x16x32_bf16 v[0:3], a[204:207], v[92:95], v[0:3]\n\t"
        "ds_read_b64_tr_b16 v[72:73], v159 offset:64\n\t"
        "ds_read_b128 v[104:107], v130 offset:8192\n\t"
        "v_mfma_f32_16x16x32_bf16 v[4:7], a[208:211], v[92:95], v[4:7]\n\t"
        "ds_read_b64_tr_b16 v[74:75], v152 offset:64\n\t"
        "ds_read_b64_tr_b16 a[252:253], v131 offset:24576\n\t"
        "v_mfma_f32_16x16x32_bf16 v[8:11], a[212:215], v[92:95], v[8:11]\n\t"
        "ds_read_b64_tr_b16 v[76:77], v157 offset:128\n\t"
        "ds_read_b64_tr_b16 a[254:255], v131 offset:25344\n\t"
        "v_lshlrev_b32_sdwa v157, 5, v161 dst_sel:DWORD dst_unused:UNUSED_PAD src0_sel:DWORD src1_sel:WORD_0\n\t"
        "v_or_b32 v157, v184, v157\n\t"
        "v_mfma_f32_16x16x32_bf16 v[12:15], a[180:183], v[92:95], v[12:15]\n\t"
        "ds_read_b64_tr_b16 v[78:79], v158 offset:128\n\t"
        "ds_read_b64_tr_b16 v[188:189], v132 offset:24576\n\t"
        "v_lshlrev_b32_sdwa v158, 5, v161 dst_sel:DWORD dst_unused:UNUSED_PAD src0_sel:DWORD src1_sel:WORD_1\n\t"
        "s_waitcnt lgkmcnt(11)\n\t"
        "v_mfma_f32_16x16x32_bf16 v[48:51], v[60:63], v[164:167], 0\n\t"
        "ds_read_b64_tr_b16 v[80:81], v159 offset:128\n\t"
        "ds_read_b64_tr_b16 v[190:191], v132 offset:25344\n\t"
        "v_or_b32 v158, v184, v158\n\t"
        "v_mfma_f32_16x16x32_bf16 v[16:19], a[184:187], v[92:95], v[16:19]\n\t"
        "ds_read_b64_tr_b16 v[82:83], v152 offset:128\n\t"
        "ds_read_b64_tr_b16 v[192:193], v133 offset:24576\n\t"
        "v_xor_b32 v159, 32, v157\n\t"
        "s_waitcnt lgkmcnt(12)\n\t"
        "v_mfma_f32_16x16x32_bf16 v[52:55], v[64:67], v[164:167], 0\n\t"
        "ds_read_b64_tr_b16 v[60:61], v157 offset:0\n\t"
        "ds_read_b64_tr_b16 v[194:195], v133 offset:25344\n\t"
        "v_xor_b32 v152, 32, v158\n\t"
        "v_mfma_f32_16x16x32_bf16 v[20:23], a[188:191], v[92:95], v[20:23]\n\t"
        "ds_read_b64_tr_b16 v[62:63], v158 offset:0\n\t"
        "v_mfma_f32_16x16x32_bf16 v[56:59], v[68:71], v[164:167], 0\n\t"
        "ds_read_b64_tr_b16 v[64:65], v159 offset:0\n\t"
        "v_mfma_f32_16x16x32_bf16 v[24:27], a[192:195], v[92:95], v[24:27]\n\t"
        "ds_read_b64_tr_b16 v[66:67], v152 offset:0\n\t"
        "v_mfma_f32_16x16x32_bf16 v[28:31], a[196:199], v[92:95], v[28:31]\n\t"
        "ds_read_b64_tr_b16 v[68:69], v157 offset:64\n\t"
        "s_waitcnt lgkmcnt(12)\n\t"
        "v_mfma_f32_16x16x32_bf16 v[116:119], v[72:75], v[164:167], 0\n\t"
        "ds_read_b64_tr_b16 v[70:71], v158 offset:64\n\t"
        "v_mfma_f32_16x16x32_bf16 v[32:35], a[200:203], v[92:95], v[32:35]\n\t"
        "ds_read_b64_tr_b16 v[72:73], v159 offset:64\n\t"
        "s_waitcnt lgkmcnt(11)\n\t"
        "v_mfma_f32_16x16x32_bf16 v[120:123], v[76:79], v[164:167], 0\n\t"
        "ds_read_b64_tr_b16 v[74:75], v152 offset:64\n\t"
        "v_mfma_f32_16x16x32_bf16 v[0:3], a[228:231], v[96:99], v[0:3]\n\t"
        "ds_read_b64_tr_b16 v[76:77], v157 offset:128\n\t"
        "s_waitcnt lgkmcnt(10)\n\t"
        "v_mfma_f32_16x16x32_bf16 v[124:127], v[80:83], v[164:167], 0\n\t"
        "ds_read_b64_tr_b16 v[78:79], v158 offset:128\n\t"
        "v_perm_b32 v164, v163, v163, v168\n\t"
        "v_perm_b32 v165, v163, v163, v169\n\t"
        "v_mfma_f32_16x16x32_bf16 v[4:7], a[232:235], v[96:99], v[4:7]\n\t"
        "ds_read_b64_tr_b16 v[80:81], v159 offset:128\n\t"
        "v_perm_b32 v166, v163, v163, v170\n\t"
        "v_perm_b32 v167, v163, v163, v171\n\t"
        "v_mfma_f32_16x16x32_bf16 v[8:11], a[236:239], v[96:99], v[8:11]\n\t"
        "ds_read_b128 v[108:111], v129 offset:12416\n\t"
        "s_waitcnt lgkmcnt(9)\n\t"
        "v_mfma_f32_16x16x32_bf16 v[48:51], v[60:63], v[164:167], v[48:51]\n\t"
        "ds_read_b64_tr_b16 v[82:83], v152 offset:128\n\t"
        "v_mfma_f32_16x16x32_bf16 v[0:3], a[216:219], v[100:103], v[0:3]\n\t"
        "ds_read_b128 v[160:163], v185 offset:256\n\t"
        "ds_read_b64_tr_b16 v[196:197], v131 offset:30720\n\t"
        "s_waitcnt lgkmcnt(10)\n\t"
        "v_mfma_f32_16x16x32_bf16 v[52:55], v[64:67], v[164:167], v[52:55]\n\t"
        "ds_read_b64_tr_b16 v[198:199], v131 offset:31488\n\t"
        "ds_read_b64_tr_b16 v[200:201], v132 offset:30720\n\t"
        "v_mfma_f32_16x16x32_bf16 v[4:7], a[220:223], v[100:103], v[4:7]\n\t"
        "ds_read_b64_tr_b16 v[202:203], v132 offset:31488\n\t"
        "ds_read_b64_tr_b16 v[204:205], v133 offset:30720\n\t"
        "s_waitcnt lgkmcnt(11)\n\t"
        "v_mfma_f32_16x16x32_bf16 v[56:59], v[68:71], v[164:167], v[56:59]\n\t"
        "ds_read_b64_tr_b16 v[206:207], v133 offset:31488\n\t"
        "v_cvt_pk_bf16_f32 v48, v48, v49\n\t"
        "v_cvt_pk_bf16_f32 v49, v50, v51\n\t"
        "v_mfma_f32_16x16x32_bf16 v[8:11], a[224:227], v[100:103], v[8:11]\n\t"
        "ds_write_b64 v186, v[48:49] offset:0\n\t"
        "v_mfma_f32_16x16x32_bf16 v[116:119], v[72:75], v[164:167], v[116:119]\n\t"
        "ds_read_b64_tr_b16 v[208:209], v131 offset:36864\n\t"
        "v_cvt_pk_bf16_f32 v52, v52, v53\n\t"
        "v_cvt_pk_bf16_f32 v53, v54, v55\n\t"
        "v_mfma_f32_16x16x32_bf16 v[12:15], a[228:231], v[100:103], v[12:15]\n\t"
        "s_waitcnt vmcnt(0)\n\t"
        "s_barrier\n\t"
        "s_waitcnt lgkmcnt(10)\n\t"
        "v_mfma_f32_16x16x32_bf16 v[120:123], v[76:79], v[164:167], v[120:123]\n\t"
        "ds_write_b64 v154, v[52:53] offset:0\n\t"
        "v_mfma_f32_16x16x32_bf16 v[16:19], a[232:235], v[100:103], v[16:19]\n\t"
        "ds_read_b128 v[112:115], v130 offset:64\n\t"
        "v_cvt_pk_bf16_f32 v56, v56, v57\n\t"
        "v_cvt_pk_bf16_f32 v57, v58, v59\n\t"
        "s_waitcnt lgkmcnt(9)\n\t"
        "v_mfma_f32_16x16x32_bf16 v[124:127], v[80:83], v[164:167], v[124:127]\n\t"
        "ds_write_b64 v186, v[56:57] offset:64\n\t"
        "v_mfma_f32_16x16x32_bf16 v[20:23], a[236:239], v[100:103], v[20:23]\n\t"
        "ds_read_b64_tr_b16 v[210:211], v131 offset:37632\n\t"
        "v_cvt_pk_bf16_f32 v116, v116, v117\n\t"
        "v_mfma_f32_16x16x32_bf16 v[0:3], a[240:243], v[104:107], v[0:3]\n\t"
        "ds_read_b128 v[84:87], v130 offset:4160\n\t"
        "v_cvt_pk_bf16_f32 v117, v118, v119\n\t"
        "v_mfma_f32_16x16x32_bf16 v[4:7], a[244:247], v[104:107], v[4:7]\n\t"
        "ds_write_b64 v154, v[116:117] offset:64\n\t"
        "v_mfma_f32_16x16x32_bf16 v[8:11], a[248:251], v[104:107], v[8:11]\n\t"
        "ds_read_b64_tr_b16 v[212:213], v132 offset:36864\n\t"
        "v_cvt_pk_bf16_f32 v120, v120, v121\n\t"
        "v_cvt_pk_bf16_f32 v121, v122, v123\n\t"
        "v_mfma_f32_16x16x32_bf16 v[12:15], a[216:219], v[104:107], v[12:15]\n\t"
        "ds_write_b64 v186, v[120:121] offset:128\n\t"
        "v_mfma_f32_16x16x32_bf16 v[16:19], a[220:223], v[104:107], v[16:19]\n\t"
        "ds_read_b64_tr_b16 v[214:215], v132 offset:37632\n\t"
        "v_cvt_pk_bf16_f32 v124, v124, v125\n\t"
        "v_cvt_pk_bf16_f32 v125, v126, v127\n\t"
        "v_mfma_f32_16x16x32_bf16 v[20:23], a[224:227], v[104:107], v[20:23]\n\t"
        "ds_write_b64 v154, v[124:125] offset:128\n\t"
        "v_mfma_f32_16x16x32_bf16 v[24:27], a[228:231], v[104:107], v[24:27]\n\t"
        "ds_read_b128 v[88:91], v130 offset:12288\n\t"
        "ds_read_b64_tr_b16 v[216:217], v133 offset:36864\n\t"
        "v_lshlrev_b32_sdwa v157, 5, v160 dst_sel:DWORD dst_unused:UNUSED_PAD src0_sel:DWORD src1_sel:WORD_0\n\t"
        "v_mfma_f32_16x16x32_bf16 v[28:31], a[232:235], v[104:107], v[28:31]\n\t"
        "ds_read_b64_tr_b16 v[218:219], v133 offset:37632\n\t"
        "v_lshlrev_b32_sdwa v158, 5, v160 dst_sel:DWORD dst_unused:UNUSED_PAD src0_sel:DWORD src1_sel:WORD_1\n\t"
        "v_mfma_f32_16x16x32_bf16 v[32:35], a[236:239], v[104:107], v[32:35]\n\t"
        "ds_read_b64_tr_b16 v[220:221], v131 offset:43008\n\t"
        "v_or_b32 v157, v184, v157\n\t"
        "v_mfma_f32_16x16x32_bf16 v[12:15], a[204:207], v[108:111], v[12:15]\n\t"
        "ds_read_b64_tr_b16 v[60:61], v157 offset:0\n\t"
        "v_or_b32 v158, v184, v158\n\t"
        "v_xor_b32 v159, 32, v157\n\t"
        "v_mfma_f32_16x16x32_bf16 v[16:19], a[208:211], v[108:111], v[16:19]\n\t"
        "ds_read_b64_tr_b16 v[62:63], v158 offset:0\n\t"
        "v_xor_b32 v152, 32, v158\n\t"
        "v_mfma_f32_16x16x32_bf16 v[20:23], a[212:215], v[108:111], v[20:23]\n\t"
        "ds_read_b64_tr_b16 v[64:65], v159 offset:0\n\t"
        "v_perm_b32 v164, v162, v162, v168\n\t"
        "v_mfma_f32_16x16x32_bf16 v[24:27], a[180:183], v[108:111], v[24:27]\n\t"
        "ds_read_b64_tr_b16 v[66:67], v152 offset:0\n\t"
        "v_perm_b32 v165, v162, v162, v169\n\t"
        "v_mfma_f32_16x16x32_bf16 v[28:31], a[184:187], v[108:111], v[28:31]\n\t"
        "ds_read_b64_tr_b16 v[68:69], v157 offset:64\n\t"
        "v_perm_b32 v166, v162, v162, v170\n\t"
        "v_perm_b32 v167, v162, v162, v171\n\t"
        "v_mfma_f32_16x16x32_bf16 v[32:35], a[188:191], v[108:111], v[32:35]\n\t"
        "ds_read_b64_tr_b16 v[70:71], v158 offset:64\n\t"
        "v_mfma_f32_16x16x32_bf16 v[36:39], a[192:195], v[108:111], v[36:39]\n\t"
        "ds_read_b64_tr_b16 v[72:73], v159 offset:64\n\t"
        "v_mfma_f32_16x16x32_bf16 v[40:43], a[196:199], v[108:111], v[40:43]\n\t"
        "ds_read_b64_tr_b16 v[74:75], v152 offset:64\n\t"
        "v_mfma_f32_16x16x32_bf16 v[44:47], a[200:203], v[108:111], v[44:47]\n\t"
        "ds_read_b64_tr_b16 v[76:77], v157 offset:128\n\t"
        "v_lshlrev_b32_sdwa v157, 5, v161 dst_sel:DWORD dst_unused:UNUSED_PAD src0_sel:DWORD src1_sel:WORD_0\n\t"
        "v_or_b32 v157, v184, v157\n\t"
        "s_waitcnt lgkmcnt(9)\n\t"
        "v_mfma_f32_16x16x32_bf16 v[0:3], v[196:199], v[112:115], v[0:3]\n\t"
        "ds_read_b64_tr_b16 v[78:79], v158 offset:128\n\t"
        "ds_read_b128 v[92:95], v130 offset:8256\n\t"
        "v_lshlrev_b32_sdwa v158, 5, v161 dst_sel:DWORD dst_unused:UNUSED_PAD src0_sel:DWORD src1_sel:WORD_1\n\t"
        "v_mfma_f32_16x16x32_bf16 v[4:7], v[200:203], v[112:115], v[4:7]\n\t"
        "ds_read_b64_tr_b16 v[80:81], v159 offset:128\n\t"
        "ds_read_b64_tr_b16 v[222:223], v131 offset:43776\n\t"
        "v_or_b32 v158, v184, v158\n\t"
        "s_waitcnt lgkmcnt(10)\n\t"
        "v_mfma_f32_16x16x32_bf16 v[48:51], v[60:63], v[164:167], 0\n\t"
        "ds_read_b64_tr_b16 v[82:83], v152 offset:128\n\t"
        "ds_read_b64_tr_b16 v[224:225], v132 offset:43008\n\t"
        "v_xor_b32 v159, 32, v157\n\t"
        "v_mfma_f32_16x16x32_bf16 v[8:11], v[204:207], v[112:115], v[8:11]\n\t"
        "ds_read_b64_tr_b16 v[60:61], v157 offset:0\n\t"
        "ds_read_b64_tr_b16 v[226:227], v132 offset:43776\n\t"
        "v_xor_b32 v152, 32, v158\n\t"
        "s_waitcnt lgkmcnt(11)\n\t"
        "v_mfma_f32_16x16x32_bf16 v[52:55], v[64:67], v[164:167], 0\n\t"
        "ds_read_b64_tr_b16 v[62:63], v158 offset:0\n\t"
        "ds_read_b64_tr_b16 v[228:229], v133 offset:43008\n\t"
        "v_mfma_f32_16x16x32_bf16 v[0:3], a[252:255], v[84:87], v[0:3]\n\t"
        "ds_read_b64_tr_b16 v[64:65], v159 offset:0\n\t"
        "ds_read_b64_tr_b16 v[230:231], v133 offset:43776\n\t"
        "v_mfma_f32_16x16x32_bf16 v[56:59], v[68:71], v[164:167], 0\n\t"
        "ds_read_b64_tr_b16 v[66:67], v152 offset:0\n\t"
        "v_mfma_f32_16x16x32_bf16 v[4:7], v[188:191], v[84:87], v[4:7]\n\t"
        "ds_read_b64_tr_b16 v[68:69], v157 offset:64\n\t"
        "s_waitcnt lgkmcnt(13)\n\t"
        "v_mfma_f32_16x16x32_bf16 v[116:119], v[72:75], v[164:167], 0\n\t"
        "ds_read_b64_tr_b16 v[70:71], v158 offset:64\n\t"
        "v_mfma_f32_16x16x32_bf16 v[8:11], v[192:195], v[84:87], v[8:11]\n\t"
        "ds_read_b64_tr_b16 v[72:73], v159 offset:64\n\t"
        "v_mfma_f32_16x16x32_bf16 v[120:123], v[76:79], v[164:167], 0\n\t"
        "ds_read_b64_tr_b16 v[74:75], v152 offset:64\n\t"
        "v_mfma_f32_16x16x32_bf16 v[12:15], v[196:199], v[84:87], v[12:15]\n\t"
        "ds_read_b64_tr_b16 v[76:77], v157 offset:128\n\t"
        "s_waitcnt lgkmcnt(12)\n\t"
        "v_mfma_f32_16x16x32_bf16 v[124:127], v[80:83], v[164:167], 0\n\t"
        "ds_read_b64_tr_b16 v[78:79], v158 offset:128\n\t"
        "v_perm_b32 v164, v163, v163, v168\n\t"
        "v_perm_b32 v165, v163, v163, v169\n\t"
        "v_mfma_f32_16x16x32_bf16 v[16:19], v[200:203], v[84:87], v[16:19]\n\t"
        "s_waitcnt lgkmcnt(7)\n\t"
        "s_barrier\n\t"
        "v_perm_b32 v166, v163, v163, v170\n\t"
        "v_mfma_f32_16x16x32_bf16 v[20:23], v[204:207], v[84:87], v[20:23]\n\t"
        "ds_read_b64_tr_b16 v[80:81], v159 offset:128\n\t"
        "v_perm_b32 v167, v163, v163, v171\n\t"
        "v_mfma_f32_16x16x32_bf16 v[12:15], a[240:243], v[88:91], v[12:15]\n\t"
        "ds_read_b64_tr_b16 v[82:83], v152 offset:128\n\t"
        "v_mfma_f32_16x16x32_bf16 v[48:51], v[60:63], v[164:167], v[48:51]\n\t"
        "ds_read_b128 v[160:163], v185 offset:512\n\t"
        "v_mfma_f32_16x16x32_bf16 v[16:19], a[244:247], v[88:91], v[16:19]\n\t"
        "ds_read_b128 v[96:99], v130 offset:12352\n\t"
        "s_waitcnt lgkmcnt(8)\n\t"
        "v_mfma_f32_16x16x32_bf16 v[52:55], v[64:67], v[164:167], v[52:55]\n\t"
        "ds_read_b64_tr_b16 v[232:233], v131 offset:49152\n\t"
        "v_mfma_f32_16x16x32_bf16 v[20:23], a[248:251], v[88:91], v[20:23]\n\t"
        "ds_read_b64_tr_b16 v[234:235], v131 offset:49920\n\t"
        "v_mfma_f32_16x16x32_bf16 v[56:59], v[68:71], v[164:167], v[56:59]\n\t"
        "ds_read_b64_tr_b16 v[236:237], v132 offset:49152\n\t"
        "v_cvt_pk_bf16_f32 v48, v48, v49\n\t"
        "v_cvt_pk_bf16_f32 v49, v50, v51\n\t"
        "v_mfma_f32_16x16x32_bf16 v[24:27], a[216:219], v[88:91], v[24:27]\n\t"
        "ds_write_b64 v186, v[48:49] offset:12288\n\t"
        "ds_read_b64_tr_b16 v[238:239], v132 offset:49920\n\t"
        "s_waitcnt lgkmcnt(9)\n\t"
        "v_mfma_f32_16x16x32_bf16 v[116:119], v[72:75], v[164:167], v[116:119]\n\t"
        "ds_read_b64_tr_b16 v[240:241], v133 offset:49152\n\t"
        "ds_read_b64_tr_b16 v[242:243], v133 offset:49920\n\t"
        "v_cvt_pk_bf16_f32 v52, v52, v53\n\t"
        "v_cvt_pk_bf16_f32 v53, v54, v55\n\t"
        "v_mfma_f32_16x16x32_bf16 v[28:31], a[220:223], v[88:91], v[28:31]\n\t"
        "ds_write_b64 v154, v[52:53] offset:12288\n\t"
        "v_mfma_f32_16x16x32_bf16 v[120:123], v[76:79], v[164:167], v[120:123]\n\t"
        "ds_read_b64_tr_b16 v[244:245], v131 offset:55296\n\t"
        "v_cvt_pk_bf16_f32 v56, v56, v57\n\t"
        "v_cvt_pk_bf16_f32 v57, v58, v59\n\t"
        "v_mfma_f32_16x16x32_bf16 v[32:35], a[224:227], v[88:91], v[32:35]\n\t"
        "ds_write_b64 v186, v[56:57] offset:12352\n\t"
        "s_waitcnt lgkmcnt(9)\n\t"
        "v_mfma_f32_16x16x32_bf16 v[124:127], v[80:83], v[164:167], v[124:127]\n\t"
        "ds_read_b64_tr_b16 v[246:247], v131 offset:56064\n\t"
        "v_cvt_pk_bf16_f32 v116, v116, v117\n\t"
        "v_cvt_pk_bf16_f32 v117, v118, v119\n\t"
        "v_mfma_f32_16x16x32_bf16 v[36:39], a[228:231], v[88:91], v[36:39]\n\t"
        "ds_write_b64 v154, v[116:117] offset:12352\n\t"
        "v_mfma_f32_16x16x32_bf16 v[40:43], a[232:235], v[88:91], v[40:43]\n\t"
        "ds_read_b128 v[100:103], v130 offset:128\n\t"
        "v_cvt_pk_bf16_f32 v120, v120, v121\n\t"
        "v_mfma_f32_16x16x32_bf16 v[44:47], a[236:239], v[88:91], v[44:47]\n\t"
        "ds_read_b64_tr_b16 v[248:249], v132 offset:55296\n\t"
        "v_cvt_pk_bf16_f32 v121, v122, v123\n\t"
        "v_mfma_f32_16x16x32_bf16 v[0:3], v[208:211], v[92:95], v[0:3]\n\t"
        "ds_write_b64 v186, v[120:121] offset:12416\n\t"
        "v_mfma_f32_16x16x32_bf16 v[4:7], v[212:215], v[92:95], v[4:7]\n\t"
        "ds_read_b128 v[104:107], v130 offset:4224\n\t"
        "v_cvt_pk_bf16_f32 v124, v124, v125\n\t"
        "v_cvt_pk_bf16_f32 v125, v126, v127\n\t"
        "v_mfma_f32_16x16x32_bf16 v[8:11], v[216:219], v[92:95], v[8:11]\n\t"
        "ds_write_b64 v154, v[124:125] offset:12416\n\t"
        "v_mfma_f32_16x16x32_bf16 v[12:15], a[252:255], v[92:95], v[12:15]\n\t"
        "ds_read_b64_tr_b16 v[250:251], v132 offset:56064\n\t"
        "v_lshlrev_b32_sdwa v157, 5, v160 dst_sel:DWORD dst_unused:UNUSED_PAD src0_sel:DWORD src1_sel:WORD_0\n\t"
        "v_lshlrev_b32_sdwa v158, 5, v160 dst_sel:DWORD dst_unused:UNUSED_PAD src0_sel:DWORD src1_sel:WORD_1\n\t"
        "v_mfma_f32_16x16x32_bf16 v[16:19], v[188:191], v[92:95], v[16:19]\n\t"
        "ds_read_b64_tr_b16 v[252:253], v133 offset:55296\n\t"
        "v_or_b32 v157, v184, v157\n\t"
        "v_mfma_f32_16x16x32_bf16 v[20:23], v[192:195], v[92:95], v[20:23]\n\t"
        "ds_read_b64_tr_b16 v[60:61], v157 offset:0\n\t"
        "v_or_b32 v158, v184, v158\n\t"
        "v_mfma_f32_16x16x32_bf16 v[24:27], v[196:199], v[92:95], v[24:27]\n\t"
        "ds_read_b64_tr_b16 v[62:63], v158 offset:0\n\t"
        "ds_read_b128 v[108:111], v129 offset:16448\n\t"
        "v_xor_b32 v159, 32, v157\n\t"
        "v_xor_b32 v152, 32, v158\n\t"
        "v_mfma_f32_16x16x32_bf16 v[28:31], v[200:203], v[92:95], v[28:31]\n\t"
        "ds_read_b64_tr_b16 v[64:65], v159 offset:0\n\t"
        "ds_read_b64_tr_b16 v[254:255], v133 offset:56064\n\t"
        "v_perm_b32 v164, v162, v162, v168\n\t"
        "v_mfma_f32_16x16x32_bf16 v[32:35], v[204:207], v[92:95], v[32:35]\n\t"
        "ds_read_b64_tr_b16 v[66:67], v152 offset:0\n\t"
        "v_perm_b32 v165, v162, v162, v169\n\t"
        "v_mfma_f32_16x16x32_bf16 v[12:15], v[208:211], v[96:99], v[12:15]\n\t"
        "ds_read_b64_tr_b16 v[68:69], v157 offset:64\n\t"
        "v_perm_b32 v166, v162, v162, v170\n\t"
        "v_mfma_f32_16x16x32_bf16 v[16:19], v[212:215], v[96:99], v[16:19]\n\t"
        "ds_read_b64_tr_b16 v[70:71], v158 offset:64\n\t"
        "v_perm_b32 v167, v162, v162, v171\n\t"
        "v_mfma_f32_16x16x32_bf16 v[20:23], v[216:219], v[96:99], v[20:23]\n\t"
        "ds_read_b64_tr_b16 v[72:73], v159 offset:64\n\t"
        ".LpY1%=:\n\t"
        "s_cbranch_execz .LrY%=\n\t"
        "v_mfma_f32_16x16x32_bf16 v[24:27], a[252:255], v[96:99], v[24:27]\n\t"
        "ds_read_b64_tr_b16 v[74:75], v152 offset:64\n\t"
        "v_mfma_f32_16x16x32_bf16 v[28:31], v[188:191], v[96:99], v[28:31]\n\t"
        "ds_read_b64_tr_b16 v[76:77], v157 offset:128\n\t"
        "v_lshlrev_b32_sdwa v157, 5, v161 dst_sel:DWORD dst_unused:UNUSED_PAD src0_sel:DWORD src1_sel:WORD_0\n\t"
        "v_or_b32 v157, v184, v157\n\t"
        "v_mfma_f32_16x16x32_bf16 v[32:35], v[192:195], v[96:99], v[32:35]\n\t"
        "ds_read_b64_tr_b16 v[78:79], v158 offset:128\n\t"
        "v_lshlrev_b32_sdwa v158, 5, v161 dst_sel:DWORD dst_unused:UNUSED_PAD src0_sel:DWORD src1_sel:WORD_1\n\t"
        "v_or_b32 v158, v184, v158\n\t"
        "v_mfma_f32_16x16x32_bf16 v[36:39], v[196:199], v[96:99], v[36:39]\n\t"
        "ds_read_b64_tr_b16 v[80:81], v159 offset:128\n\t"
        "v_xor_b32 v159, 32, v157\n\t"
        "s_waitcnt lgkmcnt(10)\n\t"
        "v_mfma_f32_16x16x32_bf16 v[48:51], v[60:63], v[164:167], 0\n\t"
        "ds_read_b64_tr_b16 v[82:83], v152 offset:128\n\t"
        "v_xor_b32 v152, 32, v158\n\t"
        "v_mfma_f32_16x16x32_bf16 v[40:43], v[200:203], v[96:99], v[40:43]\n\t"
        "ds_read_b64_tr_b16 v[60:61], v157 offset:0\n\t"
        "s_waitcnt lgkmcnt(9)\n\t"
        "v_mfma_f32_16x16x32_bf16 v[52:55], v[64:67], v[164:167], 0\n\t"
        "ds_read_b64_tr_b16 v[62:63], v158 offset:0\n\t"
        "v_mfma_f32_16x16x32_bf16 v[44:47], v[204:207], v[96:99], v[44:47]\n\t"
        "ds_read_b128 v[112:115], v130 offset:8320\n\t"
        "s_waitcnt lgkmcnt(9)\n\t"
        "v_mfma_f32_16x16x32_bf16 v[56:59], v[68:71], v[164:167], 0\n\t"
        "ds_read_b64_tr_b16 v[64:65], v159 offset:0\n\t"
        "v_mfma_f32_16x16x32_bf16 v[0:3], v[232:235], v[100:103], v[0:3]\n\t"
        "ds_read_b64_tr_b16 v[66:67], v152 offset:0\n\t"
        "s_waitcnt lgkmcnt(9)\n\t"
        "v_mfma_f32_16x16x32_bf16 v[116:119], v[72:75], v[164:167], 0\n\t"
        "ds_read_b64_tr_b16 v[68:69], v157 offset:64\n\t"
        "v_mfma_f32_16x16x32_bf16 v[4:7], v[236:239], v[100:103], v[4:7]\n\t"
        "ds_read_b64_tr_b16 v[70:71], v158 offset:64\n\t"
        "s_waitcnt lgkmcnt(9)\n\t"
        "v_mfma_f32_16x16x32_bf16 v[120:123], v[76:79], v[164:167], 0\n\t"
        "ds_read_b64_tr_b16 v[72:73], v159 offset:64\n\t"
        "v_mfma_f32_16x16x32_bf16 v[8:11], v[240:243], v[100:103], v[8:11]\n\t"
        "ds_read_b64_tr_b16 v[74:75], v152 offset:64\n\t"
        "s_waitcnt lgkmcnt(9)\n\t"
        "v_mfma_f32_16x16x32_bf16 v[124:127], v[80:83], v[164:167], 0\n\t"
        "ds_read_b64_tr_b16 v[76:77], v157 offset:128\n\t"
        "v_perm_b32 v164, v163, v163, v168\n\t"
        "v_perm_b32 v165, v163, v163, v169\n\t"
        "v_mfma_f32_16x16x32_bf16 v[0:3], v[220:223], v[104:107], v[0:3]\n\t"
        "ds_read_b64_tr_b16 v[78:79], v158 offset:128\n\t"
        "v_perm_b32 v166, v163, v163, v170\n\t"
        "v_perm_b32 v167, v163, v163, v171\n\t"
        "v_mfma_f32_16x16x32_bf16 v[4:7], v[224:227], v[104:107], v[4:7]\n\t"
        "ds_read_b64_tr_b16 v[80:81], v159 offset:128\n\t"
        "s_waitcnt lgkmcnt(9)\n\t"
        "v_mfma_f32_16x16x32_bf16 v[48:51], v[60:63], v[164:167], v[48:51]\n\t"
        "ds_read_b64_tr_b16 v[82:83], v152 offset:128\n\t"
        "v_mfma_f32_16x16x32_bf16 v[8:11], v[228:231], v[104:107], v[8:11]\n\t"
        "ds_read_b128 v[160:163], v185 offset:768\n\t"
        "s_waitcnt lgkmcnt(9)\n\t"
        "v_mfma_f32_16x16x32_bf16 v[52:55], v[64:67], v[164:167], v[52:55]\n\t"
        "ds_read_b128 v[84:87], v130 offset:12416\n\t"
        "v_mfma_f32_16x16x32_bf16 v[12:15], v[232:235], v[104:107], v[12:15]\n\t"
        "s_waitcnt lgkmcnt(8)\n\t"
        "v_mfma_f32_16x16x32_bf16 v[56:59], v[68:71], v[164:167], v[56:59]\n\t"
        "v_cvt_pk_bf16_f32 v48, v48, v49\n\t"
        "v_cvt_pk_bf16_f32 v49, v50, v51\n\t"
        "v_mfma_f32_16x16x32_bf16 v[16:19], v[236:239], v[104:107], v[16:19]\n\t"
        "ds_write_b64 v186, v[48:49] offset:24576\n\t"
        "s_waitcnt lgkmcnt(7)\n\t"
        "v_mfma_f32_16x16x32_bf16 v[116:119], v[72:75], v[164:167], v[116:119]\n\t"
        "v_cvt_pk_bf16_f32 v52, v52, v53\n\t"
        "v_cvt_pk_bf16_f32 v53, v54, v55\n\t"
        "v_mfma_f32_16x16x32_bf16 v[20:23], v[240:243], v[104:107], v[20:23]\n\t"
        "ds_write_b64 v154, v[52:53] offset:24576\n\t"
        "s_waitcnt lgkmcnt(6)\n\t"
        "v_mfma_f32_16x16x32_bf16 v[120:123], v[76:79], v[164:167], v[120:123]\n\t"
        "v_cvt_pk_bf16_f32 v56, v56, v57\n\t"
        "v_cvt_pk_bf16_f32 v57, v58, v59\n\t"
        "v_mfma_f32_16x16x32_bf16 v[24:27], a[168:171], v[108:111], v[24:27]\n\t"
        "ds_write_b64 v186, v[56:57] offset:24640\n\t"
        "s_waitcnt lgkmcnt(5)\n\t"
        "v_mfma_f32_16x16x32_bf16 v[124:127], v[80:83], v[164:167], v[124:127]\n\t"
        "v_cvt_pk_bf16_f32 v116, v116, v117\n\t"
        "v_cvt_pk_bf16_f32 v117, v118, v119\n\t"
        "s_waitcnt lgkmcnt(4)\n\t"
        "v_lshlrev_b32_sdwa v157, 5, v160 dst_sel:DWORD dst_unused:UNUSED_PAD src0_sel:DWORD src1_sel:WORD_0\n\t"
        "v_mfma_f32_16x16x32_bf16 v[28:31], a[172:175], v[108:111], v[28:31]\n\t"
        "ds_write_b64 v154, v[116:117] offset:24640\n\t"
        "v_mfma_f32_16x16x32_bf16 v[32:35], a[176:179], v[108:111], v[32:35]\n\t"
        "v_cvt_pk_bf16_f32 v120, v120, v121\n\t"
        "v_cvt_pk_bf16_f32 v121, v122, v123\n\t"
        "v_lshlrev_b32_sdwa v158, 5, v160 dst_sel:DWORD dst_unused:UNUSED_PAD src0_sel:DWORD src1_sel:WORD_1\n\t"
        "v_mfma_f32_16x16x32_bf16 v[36:39], a[144:147], v[108:111], v[36:39]\n\t"
        "ds_write_b64 v186, v[120:121] offset:24704\n\t"
        "v_mfma_f32_16x16x32_bf16 v[40:43], a[148:151], v[108:111], v[40:43]\n\t"
        "ds_read_b128 v[88:91], v129 offset:16512\n\t"
        "v_cvt_pk_bf16_f32 v124, v124, v125\n\t"
        "v_cvt_pk_bf16_f32 v125, v126, v127\n\t"
        "v_mfma_f32_16x16x32_bf16 v[44:47], a[152:155], v[108:111], v[44:47]\n\t"
        "ds_write_b64 v154, v[124:125] offset:24704\n\t"
        "v_mfma_f32_16x16x32_bf16 v[0:3], v[244:247], v[112:115], v[0:3]\n\t"
        "v_or_b32 v157, v184, v157\n\t"
        "v_or_b32 v158, v184, v158\n\t"
        "v_xor_b32 v159, 32, v157\n\t"
        "v_mfma_f32_16x16x32_bf16 v[4:7], v[248:251], v[112:115], v[4:7]\n\t"
        "ds_read_b64_tr_b16 v[60:61], v157 offset:0\n\t"
        "v_xor_b32 v152, 32, v158\n\t"
        "v_mfma_f32_16x16x32_bf16 v[8:11], v[252:255], v[112:115], v[8:11]\n\t"
        "ds_read_b64_tr_b16 v[62:63], v158 offset:0\n\t"
        "v_perm_b32 v164, v162, v162, v168\n\t"
        "v_mfma_f32_16x16x32_bf16 v[12:15], v[220:223], v[112:115], v[12:15]\n\t"
        "ds_read_b64_tr_b16 v[64:65], v159 offset:0\n\t"
        "v_perm_b32 v165, v162, v162, v169\n\t"
        "v_mfma_f32_16x16x32_bf16 v[16:19], v[224:227], v[112:115], v[16:19]\n\t"
        "ds_read_b64_tr_b16 v[66:67], v152 offset:0\n\t"
        "v_perm_b32 v166, v162, v162, v170\n\t"
        "v_perm_b32 v167, v162, v162, v171\n\t"
        "v_mfma_f32_16x16x32_bf16 v[20:23], v[228:231], v[112:115], v[20:23]\n\t"
        "ds_read_b64_tr_b16 v[68:69], v157 offset:64\n\t"
        "v_mfma_f32_16x16x32_bf16 v[24:27], v[232:235], v[112:115], v[24:27]\n\t"
        "ds_read_b64_tr_b16 v[70:71], v158 offset:64\n\t"
        "ds_read_b128 v[92:95], v130 offset:16384\n\t"
        "v_mfma_f32_16x16x32_bf16 v[28:31], v[236:239], v[112:115], v[28:31]\n\t"
        "ds_read_b64_tr_b16 v[72:73], v159 offset:64\n\t"
        "v_mfma_f32_16x16x32_bf16 v[32:35], v[240:243], v[112:115], v[32:35]\n\t"
        "ds_read_b64_tr_b16 v[74:75], v152 offset:64\n\t"
        "s_waitcnt lgkmcnt(9)\n\t"
        "v_mfma_f32_16x16x32_bf16 v[12:15], v[244:247], v[84:87], v[12:15]\n\t"
        "ds_read_b64_tr_b16 v[76:77], v157 offset:128\n\t"
        "v_lshlrev_b32_sdwa v157, 5, v161 dst_sel:DWORD dst_unused:UNUSED_PAD src0_sel:DWORD src1_sel:WORD_0\n\t"
        "v_or_b32 v157, v184, v157\n\t"
        "v_mfma_f32_16x16x32_bf16 v[16:19], v[248:251], v[84:87], v[16:19]\n\t"
        "ds_read_b64_tr_b16 v[78:79], v158 offset:128\n\t"
        "v_lshlrev_b32_sdwa v158, 5, v161 dst_sel:DWORD dst_unused:UNUSED_PAD src0_sel:DWORD src1_sel:WORD_1\n\t"
        "v_mfma_f32_16x16x32_bf16 v[20:23], v[252:255], v[84:87], v[20:23]\n\t"
        "ds_read_b64_tr_b16 v[80:81], v159 offset:128\n\t"
        "v_or_b32 v158, v184, v158\n\t"
        "v_xor_b32 v159, 32, v157\n\t"
        "s_waitcnt lgkmcnt(10)\n\t"
        "v_mfma_f32_16x16x32_bf16 v[48:51], v[60:63], v[164:167], 0\n\t"
        "ds_read_b64_tr_b16 v[82:83], v152 offset:128\n\t"
        "v_xor_b32 v152, 32, v158\n\t"
        "v_mfma_f32_16x16x32_bf16 v[24:27], v[220:223], v[84:87], v[24:27]\n\t"
        "ds_read_b64_tr_b16 v[60:61], v157 offset:0\n\t"
        "s_waitcnt lgkmcnt(10)\n\t"
        "v_mfma_f32_16x16x32_bf16 v[52:55], v[64:67], v[164:167], 0\n\t"
        "ds_read_b64_tr_b16 v[62:63], v158 offset:0\n\t"
        "v_mfma_f32_16x16x32_bf16 v[28:31], v[224:227], v[84:87], v[28:31]\n\t"
        "ds_read_b64_tr_b16 v[64:65], v159 offset:0\n\t"
        "ds_read_b128 v[96:99], v130 offset:16448\n\t"
        "s_waitcnt lgkmcnt(10)\n\t"
        "v_mfma_f32_16x16x32_bf16 v[56:59], v[68:71], v[164:167], 0\n\t"
        "ds_read_b64_tr_b16 v[66:67], v152 offset:0\n\t"
        "v_mfma_f32_16x16x32_bf16 v[32:35], v[228:231], v[84:87], v[32:35]\n\t"
        "ds_read_b64_tr_b16 v[68:69], v157 offset:64\n\t"
        "s_waitcnt lgkmcnt(10)\n\t"
        "v_mfma_f32_16x16x32_bf16 v[116:119], v[72:75], v[164:167], 0\n\t"
        "ds_read_b64_tr_b16 v[70:71], v158 offset:64\n\t"
        "v_mfma_f32_16x16x32_bf16 v[36:39], v[232:235], v[84:87], v[36:39]\n\t"
        "ds_read_b64_tr_b16 v[72:73], v159 offset:64\n\t"
        "s_waitcnt lgkmcnt(10)\n\t"
        "v_mfma_f32_16x16x32_bf16 v[120:123], v[76:79], v[164:167], 0\n\t"
        "ds_read_b64_tr_b16 v[74:75], v152 offset:64\n\t"
        "v_mfma_f32_16x16x32_bf16 v[40:43], v[236:239], v[84:87], v[40:43]\n\t"
        "ds_read_b64_tr_b16 v[76:77], v157 offset:128\n\t"
        "s_waitcnt lgkmcnt(10)\n\t"
        "v_mfma_f32_16x16x32_bf16 v[124:127], v[80:83], v[164:167], 0\n\t"
        "ds_read_b64_tr_b16 v[78:79], v158 offset:128\n\t"
        "v_perm_b32 v164, v163, v163, v168\n\t"
        "v_perm_b32 v165, v163, v163, v169\n\t"
        "v_mfma_f32_16x16x32_bf16 v[44:47], v[240:243], v[84:87], v[44:47]\n\t"
        "ds_read_b64_tr_b16 v[80:81], v159 offset:128\n\t"
        "v_perm_b32 v166, v163, v163, v170\n\t"
        "v_perm_b32 v167, v163, v163, v171\n\t"
        "v_mfma_f32_16x16x32_bf16 v[24:27], a[204:207], v[88:91], v[24:27]\n\t"
        "ds_read_b64_tr_b16 v[82:83], v152 offset:128\n\t"
        "s_waitcnt lgkmcnt(10)\n\t"
        "v_mfma_f32_16x16x32_bf16 v[48:51], v[60:63], v[164:167], v[48:51]\n\t"
        "ds_read_b128 v[100:103], v130 offset:16512\n\t"
        "v_mfma_f32_16x16x32_bf16 v[28:31], a[208:211], v[88:91], v[28:31]\n\t"
        "s_waitcnt lgkmcnt(9)\n\t"
        "v_mfma_f32_16x16x32_bf16 v[52:55], v[64:67], v[164:167], v[52:55]\n\t"
        "v_mfma_f32_16x16x32_bf16 v[32:35], a[212:215], v[88:91], v[32:35]\n\t"
        "s_waitcnt lgkmcnt(7)\n\t"
        "v_mfma_f32_16x16x32_bf16 v[56:59], v[68:71], v[164:167], v[56:59]\n\t"
        "s_nop 0\n\t"
        "v_cvt_pk_bf16_f32 v48, v48, v49\n\t"
        "v_cvt_pk_bf16_f32 v49, v50, v51\n\t"
        "v_mfma_f32_16x16x32_bf16 v[36:39], a[180:183], v[88:91], v[36:39]\n\t"
        "ds_write_b64 v186, v[48:49] offset:36864\n\t"
        "s_waitcnt lgkmcnt(5)\n\t"
        "v_mfma_f32_16x16x32_bf16 v[116:119], v[72:75], v[164:167], v[116:119]\n\t"
        "ds_read_b128 v[104:107], v129 offset:20480\n\t"
        "v_cvt_pk_bf16_f32 v52, v52, v53\n\t"
        "v_cvt_pk_bf16_f32 v53, v54, v55\n\t"
        "v_mfma_f32_16x16x32_bf16 v[40:43], a[184:187], v[88:91], v[40:43]\n\t"
        "ds_write_b64 v154, v[52:53] offset:36864\n\t"
        "s_waitcnt lgkmcnt(5)\n\t"
        "v_mfma_f32_16x16x32_bf16 v[120:123], v[76:79], v[164:167], v[120:123]\n\t"
        "v_cvt_pk_bf16_f32 v56, v56, v57\n\t"
        "v_cvt_pk_bf16_f32 v57, v58, v59\n\t"
        "v_mfma_f32_16x16x32_bf16 v[44:47], a[188:191], v[88:91], v[44:47]\n\t"
        "ds_write_b64 v186, v[56:57] offset:36928\n\t"
        "s_waitcnt lgkmcnt(4)\n\t"
        "v_mfma_f32_16x16x32_bf16 v[124:127], v[80:83], v[164:167], v[124:127]\n\t"
        "v_cvt_pk_bf16_f32 v116, v116, v117\n\t"
        "v_cvt_pk_bf16_f32 v117, v118, v119\n\t"
        "v_mfma_f32_16x16x32_bf16 v[24:27], a[240:243], v[92:95], v[24:27]\n\t"
        "ds_write_b64 v154, v[116:117] offset:36928\n\t"
        "v_mfma_f32_16x16x32_bf16 v[28:31], a[244:247], v[92:95], v[28:31]\n\t"
        "ds_read_b128 v[108:111], v129 offset:20544\n\t"
        "v_cvt_pk_bf16_f32 v120, v120, v121\n\t"
        "v_cvt_pk_bf16_f32 v121, v122, v123\n\t"
        "v_mfma_f32_16x16x32_bf16 v[32:35], a[248:251], v[92:95], v[32:35]\n\t"
        "ds_write_b64 v186, v[120:121] offset:36992\n\t"
        "v_mfma_f32_16x16x32_bf16 v[36:39], a[216:219], v[92:95], v[36:39]\n\t"
        "ds_read_b128 v[112:115], v129 offset:20608\n\t"
        "v_cvt_pk_bf16_f32 v124, v124, v125\n\t"
        "v_cvt_pk_bf16_f32 v125, v126, v127\n\t"
        "v_mfma_f32_16x16x32_bf16 v[40:43], a[220:223], v[92:95], v[40:43]\n\t"
        "ds_write_b64 v154, v[124:125] offset:36992\n\t"
        "v_mfma_f32_16x16x32_bf16 v[44:47], a[224:227], v[92:95], v[44:47]\n\t"
        "s_cmp_gt_u32 s61, 64\n\t"
        "s_cbranch_scc0 .Lx70%=\n\t"
        "v_bfe_u32 v52, v185, 4, 4\n\t"
        "v_add_u32 v48, 65, v52\n\t"
        "s_sub_u32 s8, s37, 12288\n\t"
        "v_mov_b32 v51, s62\n\t"
        "v_lshlrev_b32 v53, 3, v52\n\t"
        "v_mul_u32_u24 v52, 192, v52\n\t"
        "v_and_or_b32 v52, v53, 48, v52\n\t"
        "v_add_u32 v49, s8, v52\n\t"
        "v_subrev_u32 v186, 12288, v186\n\t"
        "v_xor_b32 v154, 32, v186\n\t"
        "v_min_u32 v76, s61, v48\n\t"
        "v_lshl_add_u32 v76, v76, 3, s60\n\t"
        "ds_read_b64 v[60:61], v76\n\t"
        "v_cmp_ge_u32 s[66:67], s61, v48\n\t"
        "s_mov_b32 s46, 0x07060302\n\t"
        "s_waitcnt lgkmcnt(0)\n\t"
        "v_and_b32 v76, 0xfffff800, v61\n\t"
        "ds_read_b128 v[62:65], v76 offset:1536\n\t"
        "v_lshlrev_b32 v70, 16, v60\n\t"
        "v_and_b32 v71, 0xffff0000, v60\n\t"
        "v_med3_f32 v70, v70, 0, s63\n\t"
        "v_med3_f32 v71, v71, 0, s63\n\t"
        "s_mov_b64 exec, s[66:67]\n\t"
        "ds_write_b32 v61, v49\n\t"
        "s_mov_b64 exec, -1\n\t"
        "v_perm_b32 v60, v71, v70, s46\n\t"
        "v_fract_f32 v72, v70\n\t"
        "v_fract_f32 v73, v71\n\t"
        "v_cvt_u32_f32 v74, v70\n\t"
        "v_cvt_u32_f32 v75, v71\n\t"
        "v_cmp_neq_f32 s[64:65], 0, v72\n\t"
        "v_cmp_neq_f32 s[44:45], 0, v73\n\t"
        "v_sub_f32 v76, 1.0, v72\n\t"
        "v_sub_f32 v77, 1.0, v73\n\t"
        "v_cvt_pk_bf16_f32 v77, v76, v77\n\t"
        "v_lshlrev_b32 v76, 16, v77\n\t"
        "v_and_b32 v77, 0xffff0000, v77\n\t"
        "v_mul_f32 v78, v76, v77\n\t"
        "v_mul_f32 v76, v76, v73\n\t"
        "v_mul_f32 v77, v72, v77\n\t"
        "v_mul_f32 v79, v72, v73\n\t"
        "v_cndmask_b32 v76, v77, v76, s[44:45]\n\t"
        "v_cndmask_b32 v77, 0, v77, s[44:45]\n\t"
        "v_cvt_pk_bf16_f32 v162, v78, v76\n\t"
        "v_cvt_pk_bf16_f32 v163, v77, v79\n\t"
        "s_waitcnt lgkmcnt(1)\n\t"
        "v_pk_max_u16 v76, v60, v62\n\t"
        "v_pk_min_u16 v76, v76, v63\n\t"
        "v_cmp_ne_u32 vcc, v76, v60\n\t"
        "v_sub_u32 v76, v75, v65\n\t"
        "v_mad_u32_u24 v77, v75, 6, v64\n\t"
        "v_mad_u32_u24 v77, v74, s62, v77\n\t"
        "s_and_b64 vcc, vcc, s[66:67]\n\t"
        "s_or_b64 s[56:57], s[56:57], vcc\n\t"
        "v_bfe_u32 v78, v76, 2, 1\n\t"
        "v_or_b32 v78, v78, v77\n\t"
        "v_add_u32 v76, 1, v76\n\t"
        "v_bfe_u32 v76, v76, 2, 1\n\t"
        "v_add3_u32 v76, v77, v76, 6\n\t"
        "v_cndmask_b32 v76, v78, v76, s[44:45]\n\t"
        "v_cndmask_b32 v77, 0, v51, s[64:65]\n\t"
        "v_add_u32 v79, v78, v77\n\t"
        "v_add_u32 v80, v76, v77\n\t"
        "v_cndmask_b32 v76, v79, v76, s[44:45]\n\t"
        "v_lshl_or_b32 v160, v76, 16, v78\n\t"
        "v_lshl_or_b32 v161, v80, 16, v79\n\t"
        "v_lshlrev_b32_sdwa v157, 5, v160 dst_sel:DWORD dst_unused:UNUSED_PAD src0_sel:DWORD src1_sel:WORD_0\n\t"
        "v_lshlrev_b32_sdwa v158, 5, v160 dst_sel:DWORD dst_unused:UNUSED_PAD src0_sel:DWORD src1_sel:WORD_1\n\t"
        "v_or_b32 v157, v184, v157\n\t"
        "v_or_b32 v158, v184, v158\n\t"
        "v_xor_b32 v159, 32, v157\n\t"
        "v_xor_b32 v152, 32, v158\n\t"
        "ds_read_b64_tr_b16 v[60:61], v157 offset:0\n\t"
        "ds_read_b64_tr_b16 v[62:63], v158 offset:0\n\t"
        "ds_read_b64_tr_b16 v[64:65], v159 offset:0\n\t"
        "ds_read_b64_tr_b16 v[66:67], v152 offset:0\n\t"
        "ds_read_b64_tr_b16 v[68:69], v157 offset:64\n\t"
        "ds_read_b64_tr_b16 v[70:71], v158 offset:64\n\t"
        "ds_read_b64_tr_b16 v[72:73], v159 offset:64\n\t"
        "ds_read_b64_tr_b16 v[74:75], v152 offset:64\n\t"
        "ds_read_b64_tr_b16 v[76:77], v157 offset:128\n\t"
        "ds_read_b64_tr_b16 v[78:79], v158 offset:128\n\t"
        "ds_read_b64_tr_b16 v[80:81], v159 offset:128\n\t"
        "ds_read_b64_tr_b16 v[82:83], v152 offset:128\n\t"
        "v_perm_b32 v164, v162, v162, v168\n\t"
        "v_perm_b32 v165, v162, v162, v169\n\t"
        "v_perm_b32 v166, v162, v162, v170\n\t"
        "v_perm_b32 v167, v162, v162, v171\n\t"
        "s_waitcnt lgkmcnt(10)\n\t"
        "s_nop 0\n\t"
        "v_mfma_f32_16x16x32_bf16 v[48:51], v[60:63], v[164:167], 0\n\t"
        "s_waitcnt lgkmcnt(8)\n\t"
        "v_mfma_f32_16x16x32_bf16 v[52:55], v[64:67], v[164:167], 0\n\t"
        "s_waitcnt lgkmcnt(6)\n\t"
        "v_mfma_f32_16x16x32_bf16 v[56:59], v[68:71], v[164:167], 0\n\t"
        "s_waitcnt lgkmcnt(4)\n\t"
        "v_mfma_f32_16x16x32_bf16 v[116:119], v[72:75], v[164:167], 0\n\t"
        "s_waitcnt lgkmcnt(2)\n\t"
        "v_mfma_f32_16x16x32_bf16 v[120:123], v[76:79], v[164:167], 0\n\t"
        "s_waitcnt lgkmcnt(0)\n\t"
        "v_mfma_f32_16x16x32_bf16 v[124:127], v[80:83], v[164:167], 0\n\t"
        "v_lshlrev_b32_sdwa v157, 5, v161 dst_sel:DWORD dst_unused:UNUSED_PAD src0_sel:DWORD src1_sel:WORD_0\n\t"
        "v_lshlrev_b32_sdwa v158, 5, v161 dst_sel:DWORD dst_unused:UNUSED_PAD src0_sel:DWORD src1_sel:WORD_1\n\t"
        "v_or_b32 v157, v184, v157\n\t"
        "v_or_b32 v158, v184, v158\n\t"
        "v_xor_b32 v159, 32, v157\n\t"
        "v_xor_b32 v152, 32, v158\n\t"
        "ds_read_b64_tr_b16 v[60:61], v157 offset:0\n\t"
        "ds_read_b64_tr_b16 v[62:63], v158 offset:0\n\t"
        "ds_read_b64_tr_b16 v[64:65], v159 offset:0\n\t"
        "ds_read_b64_tr_b16 v[66:67], v152 offset:0\n\t"
        "ds_read_b64_tr_b16 v[68:69], v157 offset:64\n\t"
        "ds_read_b64_tr_b16 v[70:71], v158 offset:64\n\t"
        "ds_read_b64_tr_b16 v[72:73], v159 offset:64\n\t"
        "ds_read_b64_tr_b16 v[74:75], v152 offset:64\n\t"
        "ds_read_b64_tr_b16 v[76:77], v157 offset:128\n\t"
        "ds_read_b64_tr_b16 v[78:79], v158 offset:128\n\t"
        "ds_read_b64_tr_b16 v[80:81], v159 offset:128\n\t"
        "ds_read_b64_tr_b16 v[82:83], v152 offset:128\n\t"
        "v_perm_b32 v164, v163, v163, v168\n\t"
        "v_perm_b32 v165, v163, v163, v169\n\t"
        "v_perm_b32 v166, v163, v163, v170\n\t"
        "v_perm_b32 v167, v163, v163, v171\n\t"
        "s_waitcnt lgkmcnt(10)\n\t"
        "s_nop 0\n\t"
        "v_mfma_f32_16x16x32_bf16 v[48:51], v[60:63], v[164:167], v[48:51]\n\t"
        "s_waitcnt lgkmcnt(8)\n\t"
        "v_mfma_f32_16x16x32_bf16 v[52:55], v[64:67], v[164:167], v[52:55]\n\t"
        "s_waitcnt lgkmcnt(6)\n\t"
        "v_mfma_f32_16x16x32_bf16 v[56:59], v[68:71], v[164:167], v[56:59]\n\t"
        "s_waitcnt lgkmcnt(4)\n\t"
        "v_mfma_f32_16x16x32_bf16 v[116:119], v[72:75], v[164:167], v[116:119]\n\t"
        "s_waitcnt lgkmcnt(2)\n\t"
        "v_mfma_f32_16x16x32_bf16 v[120:123], v[76:79], v[164:167], v[120:123]\n\t"
        "s_waitcnt lgkmcnt(0)\n\t"
        "v_mfma_f32_16x16x32_bf16 v[124:127], v[80:83], v[164:167], v[124:127]\n\t"
        "v_cvt_pk_bf16_f32 v48, v48, v49\n\t"
        "v_cvt_pk_bf16_f32 v49, v50, v51\n\t"
        "v_cvt_pk_bf16_f32 v52, v52, v53\n\t"
        "v_cvt_pk_bf16_f32 v53, v54, v55\n\t"
        "v_cvt_pk_bf16_f32 v56, v56, v57\n\t"
        "v_cvt_pk_bf16_f32 v57, v58, v59\n\t"
        "v_cvt_pk_bf16_f32 v116, v116, v117\n\t"
        "v_cvt_pk_bf16_f32 v117, v118, v119\n\t"
        "v_cvt_pk_bf16_f32 v120, v120, v121\n\t"
        "v_cvt_pk_bf16_f32 v121, v122, v123\n\t"
        "v_cvt_pk_bf16_f32 v124, v124, v125\n\t"
        "v_cvt_pk_bf16_f32 v125, v126, v127\n\t"
        "ds_write_b64 v186, v[48:49] offset:0\n\t"
        "ds_write_b64 v154, v[52:53] offset:0\n\t"
        "ds_write_b64 v186, v[56:57] offset:64\n\t"
        "ds_write_b64 v154, v[116:117] offset:64\n\t"
        "ds_write_b64 v186, v[120:121] offset:128\n\t"
        "ds_write_b64 v154, v[124:125] offset:128\n\t"
        "v_add_u32 v186, 12288, v186\n\t"
        "v_xor_b32 v154, 32, v186\n\t"
        ".Lx70%=:\n\t"
        "v_mfma_f32_16x16x32_bf16 v[24:27], v[208:211], v[96:99], v[24:27]\n\t"
        "s_cmp_gt_u32 s3, 64\n\t"
        "s_cselect_b32 s8, 2, 0\n\t"
        "v_mfma_f32_16x16x32_bf16 v[28:31], v[212:215], v[96:99], v[28:31]\n\t"
        "s_cmp_lg_u64 s[56:57], 0\n\t"
        "s_addc_u32 s8, s8, s6\n\t"
        "v_mov_b32 v151, s8\n\t"
        "v_mfma_f32_16x16x32_bf16 v[32:35], v[216:219], v[96:99], v[32:35]\n\t"
        "s_mov_b64 exec, 1\n\t"
        "ds_write_b32 v153, v151\n\t"
        "s_mov_b64 exec, -1\n\t"
        "v_mfma_f32_16x16x32_bf16 v[36:39], a[252:255], v[96:99], v[36:39]\n\t"
        "ds_read_b128 v[84:87], v130 offset:20480\n\t"
        "ds_read_b128 v[88:91], v130 offset:20544\n\t"
        "v_mfma_f32_16x16x32_bf16 v[40:43], v[188:191], v[96:99], v[40:43]\n\t"
        "v_mfma_f32_16x16x32_bf16 v[44:47], v[192:195], v[96:99], v[44:47]\n\t"
        "v_mfma_f32_16x16x32_bf16 v[24:27], v[244:247], v[100:103], v[24:27]\n\t"
        "ds_read_b128 v[92:95], v130 offset:20608\n\t"
        "v_mfma_f32_16x16x32_bf16 v[28:31], v[248:251], v[100:103], v[28:31]\n\t"
        "v_mfma_f32_16x16x32_bf16 v[32:35], v[252:255], v[100:103], v[32:35]\n\t"
        "s_waitcnt lgkmcnt(3)\n\t"
        "s_barrier\n\t"
        "v_mfma_f32_16x16x32_bf16 v[36:39], v[220:223], v[100:103], v[36:39]\n\t"
        "ds_read_b128 v[124:127], v182 offset:0\n\t"
        "v_mfma_f32_16x16x32_bf16 v[40:43], v[224:227], v[100:103], v[40:43]\n\t"
        "ds_read_b128 v[132:135], v187 offset:4096\n\t"
        "v_mfma_f32_16x16x32_bf16 v[44:47], v[228:231], v[100:103], v[44:47]\n\t"
        "ds_read_b128 v[136:139], v187 offset:4112\n\t"
        "v_mfma_f32_16x16x32_bf16 v[36:39], a[132:135], v[104:107], v[36:39]\n\t"
        "ds_read_b32 v140, v187 offset:4128\n\t"
        "v_mfma_f32_16x16x32_bf16 v[40:43], a[136:139], v[104:107], v[40:43]\n\t"
        "v_mfma_f32_16x16x32_bf16 v[44:47], a[140:143], v[104:107], v[44:47]\n\t"
        "v_mfma_f32_16x16x32_bf16 v[36:39], a[168:171], v[108:111], v[36:39]\n\t"
        "v_mfma_f32_16x16x32_bf16 v[40:43], a[172:175], v[108:111], v[40:43]\n\t"
        "v_mfma_f32_16x16x32_bf16 v[44:47], a[176:179], v[108:111], v[44:47]\n\t"
        "v_mfma_f32_16x16x32_bf16 v[36:39], a[204:207], v[112:115], v[36:39]\n\t"
        "s_waitcnt lgkmcnt(3)\n\t"
        "v_or3_b32 v124, v124, v125, v126\n\t"
        "v_or_b32 v124, v124, v127\n\t"
        "v_mfma_f32_16x16x32_bf16 v[40:43], a[208:211], v[112:115], v[40:43]\n\t"
        "v_readfirstlane_b32 s6, v124\n\t"
        "v_mfma_f32_16x16x32_bf16 v[44:47], a[212:215], v[112:115], v[44:47]\n\t"
        "s_nop 0\n\t"
        "s_bitcmp1_b32 s6, 1\n\t"
        "s_cbranch_scc1 .Lx72%=\n\t"
        ".Lx73%=:\n\t"
        "s_waitcnt lgkmcnt(2)\n\t"
        "v_xor_b32 v132, v181, v132\n\t"
        "v_readlane_b32 s9, v155, 48\n\t"
        "v_mfma_f32_16x16x32_bf16 v[36:39], a[240:243], v[84:87], v[36:39]\n\t"
        "s_nop 0\n\t"
        "v_add_u32 v151, s9, v172\n\t"
        "v_max_u32 v151, v151, v132\n\t"
        "v_xor_b32 v133, v181, v133\n\t"
        "v_add_u32 v183, s9, v173\n\t"
        "v_mfma_f32_16x16x32_bf16 v[40:43], a[244:247], v[84:87], v[40:43]\n\t"
        "ds_read_b128 v[96:99], v151 offset:0\n\t"
        "v_max_u32 v183, v183, v133\n\t"
        "v_xor_b32 v134, v181, v134\n\t"
        "v_mfma_f32_16x16x32_bf16 v[44:47], a[248:251], v[84:87], v[44:47]\n\t"
        "ds_read_b128 v[100:103], v151 offset:64\n\t"
        "s_add_u32 s55, s41, 0\n\t"
        "v_mfma_f32_16x16x32_bf16 v[36:39], v[208:211], v[88:91], v[36:39]\n\t"
        "ds_read_b128 v[104:107], v151 offset:128\n\t"
        "v_add_u32 v151, s9, v174\n\t"
        "v_mfma_f32_16x16x32_bf16 v[40:43], v[212:215], v[88:91], v[40:43]\n\t"
        "ds_read_b128 v[108:111], v183 offset:0\n\t"
        "v_max_u32 v151, v151, v134\n\t"
        "v_xor_b32 v135, v181, v135\n\t"
        "v_mfma_f32_16x16x32_bf16 v[44:47], v[216:219], v[88:91], v[44:47]\n\t"
        "ds_read_b128 v[112:115], v183 offset:64\n\t"
        "v_cvt_pk_bf16_f32 v116, v0, v1\n\t"
        "v_mfma_f32_16x16x32_bf16 v[36:39], v[244:247], v[92:95], v[36:39]\n\t"
        "ds_read_b128 v[84:87], v183 offset:128\n\t"
        "v_add_u32 v183, s9, v175\n\t"
        "v_mfma_f32_16x16x32_bf16 v[40:43], v[248:251], v[92:95], v[40:43]\n\t"
        "ds_read_b128 v[88:91], v151 offset:0\n\t"
        "v_max_u32 v183, v183, v135\n\t"
        "v_mfma_f32_16x16x32_bf16 v[44:47], v[252:255], v[92:95], v[44:47]\n\t"
        "s_waitcnt lgkmcnt(7)\n\t"
        "v_xor_b32 v136, v181, v136\n\t"
        "v_cvt_pk_bf16_f32 v117, v2, v3\n\t"
        "s_waitcnt lgkmcnt(6)\n\t"
        "v_mfma_f32_16x16x32_bf16 v[72:75], a[12:15], v[96:99], 0\n\t"
        "ds_read_b128 v[92:95], v151 offset:64\n\t"
        "v_cvt_pk_bf16_f32 v118, v4, v5\n\t"
        "v_cvt_pk_bf16_f32 v119, v6, v7\n\t"
        "v_mfma_f32_16x16x32_bf16 v[76:79], a[16:19], v[96:99], 0\n\t"
        "v_cvt_pk_bf16_f32 v120, v8, v9\n\t"
        "v_cvt_pk_bf16_f32 v121, v10, v11\n\t"
        "v_mfma_f32_16x16x32_bf16 v[80:83], a[20:23], v[96:99], 0\n\t"
        "v_permlane16_swap_b32 v116, v118\n\t"
        "s_waitcnt lgkmcnt(6)\n\t"
        "v_mfma_f32_16x16x32_bf16 v[72:75], a[48:51], v[100:103], v[72:75]\n\t"
        "v_permlane16_swap_b32 v117, v119\n\t"
        "v_mfma_f32_16x16x32_bf16 v[76:79], a[52:55], v[100:103], v[76:79]\n\t"
        "buffer_store_dwordx4 v[116:119], v144, s[28:31], s55 offen sc1\n\t"
        "v_mfma_f32_16x16x32_bf16 v[80:83], a[56:59], v[100:103], v[80:83]\n\t"
        "ds_write_b64 v146, v[120:121] offset:0\n\t"
        "s_waitcnt lgkmcnt(3)\n\t"
        "v_mfma_f32_16x16x32_bf16 v[72:75], a[84:87], v[104:107], v[72:75]\n\t"
        "ds_read_b128 v[96:99], v151 offset:128\n\t"
        "v_add_u32 v151, s9, v176\n\t"
        "v_max_u32 v151, v151, v136\n\t"
        "v_mfma_f32_16x16x32_bf16 v[76:79], a[88:91], v[104:107], v[76:79]\n\t"
        "ds_read_b128 v[100:103], v183 offset:0\n\t"
        "v_xor_b32 v137, v181, v137\n\t"
        "v_mfma_f32_16x16x32_bf16 v[80:83], a[92:95], v[104:107], v[80:83]\n\t"
        "s_add_u32 s55, s41, 43008\n\t"
        "v_cvt_pk_bf16_f32 v116, v12, v13\n\t"
        "v_mfma_f32_16x16x32_bf16 v[72:75], a[120:123], v[108:111], v[72:75]\n\t"
        "ds_read_b128 v[104:107], v183 offset:64\n\t"
        "v_cvt_pk_bf16_f32 v117, v14, v15\n\t"
        "v_mfma_f32_16x16x32_bf16 v[76:79], a[124:127], v[108:111], v[76:79]\n\t"
        "v_cvt_pk_bf16_f32 v118, v16, v17\n\t"
        "v_cvt_pk_bf16_f32 v119, v18, v19\n\t"
        "v_cvt_pk_bf16_f32 v120, v20, v21\n\t"
        "v_mfma_f32_16x16x32_bf16 v[80:83], a[128:131], v[108:111], v[80:83]\n\t"
        "v_cvt_pk_bf16_f32 v121, v22, v23\n\t"
        "v_mfma_f32_16x16x32_bf16 v[72:75], a[156:159], v[112:115], v[72:75]\n\t"
        "v_permlane16_swap_b32 v116, v118\n\t"
        "v_mfma_f32_16x16x32_bf16 v[76:79], a[160:163], v[112:115], v[76:79]\n\t"
        "v_permlane16_swap_b32 v117, v119\n\t"
        "v_mfma_f32_16x16x32_bf16 v[80:83], a[164:167], v[112:115], v[80:83]\n\t"
        "buffer_store_dwordx4 v[116:119], v144, s[28:31], s55 offen sc1\n\t"
        "v_mfma_f32_16x16x32_bf16 v[72:75], a[192:195], v[84:87], v[72:75]\n\t"
        "ds_write_b64 v146, v[120:121] offset:2048\n\t"
        "v_mfma_f32_16x16x32_bf16 v[76:79], a[196:199], v[84:87], v[76:79]\n\t"
        "ds_read_b128 v[108:111], v183 offset:128\n\t"
        "v_add_u32 v183, s9, v177\n\t"
        "v_max_u32 v183, v183, v137\n\t"
        "v_mfma_f32_16x16x32_bf16 v[80:83], a[200:203], v[84:87], v[80:83]\n\t"
        "ds_read_b128 v[112:115], v151 offset:0\n\t"
        "v_xor_b32 v138, v181, v138\n\t"
        "s_waitcnt lgkmcnt(3)\n\t"
        "v_mfma_f32_16x16x32_bf16 v[72:75], a[228:231], v[88:91], v[72:75]\n\t"
        "ds_read_b128 v[84:87], v151 offset:64\n\t"
        "s_add_u32 s55, s41, 86016\n\t"
        "v_mfma_f32_16x16x32_bf16 v[76:79], a[232:235], v[88:91], v[76:79]\n\t"
        "v_cvt_pk_bf16_f32 v116, v24, v25\n\t"
        "v_cvt_pk_bf16_f32 v117, v26, v27\n\t"
        "v_mfma_f32_16x16x32_bf16 v[80:83], a[236:239], v[88:91], v[80:83]\n\t"
        "v_cvt_pk_bf16_f32 v118, v28, v29\n\t"
        "v_cvt_pk_bf16_f32 v119, v30, v31\n\t"
        "v_cvt_pk_bf16_f32 v120, v32, v33\n\t"
        "v_mfma_f32_16x16x32_bf16 v[72:75], v[196:199], v[92:95], v[72:75]\n\t"
        "ds_read_b128 v[88:91], v151 offset:128\n\t"
        "v_add_u32 v151, s9, v178\n\t"
        "v_mfma_f32_16x16x32_bf16 v[76:79], v[200:203], v[92:95], v[76:79]\n\t"
        "v_max_u32 v151, v151, v138\n\t"
        "v_xor_b32 v139, v181, v139\n\t"
        "v_mfma_f32_16x16x32_bf16 v[80:83], v[204:207], v[92:95], v[80:83]\n\t"
        "v_cvt_pk_bf16_f32 v121, v34, v35\n\t"
        "v_mfma_f32_16x16x32_bf16 v[72:75], v[232:235], v[96:99], v[72:75]\n\t"
        "v_permlane16_swap_b32 v116, v118\n\t"
        "v_mfma_f32_16x16x32_bf16 v[76:79], v[236:239], v[96:99], v[76:79]\n\t"
        "v_permlane16_swap_b32 v117, v119\n\t"
        "v_mfma_f32_16x16x32_bf16 v[80:83], v[240:243], v[96:99], v[80:83]\n\t"
        "buffer_store_dwordx4 v[116:119], v144, s[28:31], s55 offen sc1\n\t"
        "v_mfma_f32_16x16x32_bf16 v[72:75], a[0:3], v[100:103], v[72:75]\n\t"
        "ds_write_b64 v146, v[120:121] offset:4096\n\t"
        "v_mfma_f32_16x16x32_bf16 v[76:79], a[4:7], v[100:103], v[76:79]\n\t"
        "ds_read_b128 v[92:95], v183 offset:0\n\t"
        "s_add_u32 s55, s41, 129024\n\t"
        "v_cvt_pk_bf16_f32 v116, v36, v37\n\t"
        "v_mfma_f32_16x16x32_bf16 v[80:83], a[8:11], v[100:103], v[80:83]\n\t"
        "ds_read_b128 v[96:99], v183 offset:64\n\t"
        "v_cvt_pk_bf16_f32 v117, v38, v39\n\t"
        "v_mfma_f32_16x16x32_bf16 v[72:75], a[36:39], v[104:107], v[72:75]\n\t"
        "ds_read_b128 v[100:103], v183 offset:128\n\t"
        "v_add_u32 v183, s9, v179\n\t"
        "v_mfma_f32_16x16x32_bf16 v[76:79], a[40:43], v[104:107], v[76:79]\n\t"
        "v_max_u32 v183, v183, v139\n\t"
        "v_xor_b32 v140, v181, v140\n\t"
        "v_cvt_pk_bf16_f32 v118, v40, v41\n\t"
        "v_mfma_f32_16x16x32_bf16 v[80:83], a[44:47], v[104:107], v[80:83]\n\t"
        "v_cvt_pk_bf16_f32 v119, v42, v43\n\t"
        "v_cvt_pk_bf16_f32 v120, v44, v45\n\t"
        "s_waitcnt lgkmcnt(4)\n\t"
        "v_mfma_f32_16x16x32_bf16 v[72:75], a[72:75], v[108:111], v[72:75]\n\t"
        "ds_read_b128 v[104:107], v151 offset:0\n\t"
        "v_cvt_pk_bf16_f32 v121, v46, v47\n\t"
        "v_mfma_f32_16x16x32_bf16 v[76:79], a[76:79], v[108:111], v[76:79]\n\t"
        "v_permlane16_swap_b32 v116, v118\n\t"
        "v_mfma_f32_16x16x32_bf16 v[80:83], a[80:83], v[108:111], v[80:83]\n\t"
        "v_permlane16_swap_b32 v117, v119\n\t"
        "v_mfma_f32_16x16x32_bf16 v[72:75], a[108:111], v[112:115], v[72:75]\n\t"
        "buffer_store_dwordx4 v[116:119], v144, s[28:31], s55 offen sc1\n\t"
        "v_mfma_f32_16x16x32_bf16 v[76:79], a[112:115], v[112:115], v[76:79]\n\t"
        "ds_write_b64 v146, v[120:121] offset:6144\n\t"
        "v_mfma_f32_16x16x32_bf16 v[80:83], a[116:119], v[112:115], v[80:83]\n\t"
        "ds_read_b128 v[108:111], v151 offset:64\n\t"
        "v_mfma_f32_16x16x32_bf16 v[72:75], a[144:147], v[84:87], v[72:75]\n\t"
        "ds_read_b128 v[112:115], v151 offset:128\n\t"
        "v_add_u32 v151, s9, v180\n\t"
        "v_max_u32 v151, v151, v140\n\t"
        "v_mfma_f32_16x16x32_bf16 v[76:79], a[148:151], v[84:87], v[76:79]\n\t"
        "v_mfma_f32_16x16x32_bf16 v[80:83], a[152:155], v[84:87], v[80:83]\n\t"
        "ds_read_b128 v[84:87], v183 offset:0\n\t"
        "v_mfma_f32_16x16x32_bf16 v[72:75], a[180:183], v[88:91], v[72:75]\n\t"
        "v_mfma_f32_16x16x32_bf16 v[76:79], a[184:187], v[88:91], v[76:79]\n\t"
        "s_waitcnt lgkmcnt(3)\n\t"
        "s_barrier\n\t"
        "v_mfma_f32_16x16x32_bf16 v[80:83], a[188:191], v[88:91], v[80:83]\n\t"
        "ds_read_b128 v[88:91], v183 offset:64\n\t"
        "v_mfma_f32_16x16x32_bf16 v[72:75], a[216:219], v[92:95], v[72:75]\n\t"
        "ds_read_b128 v[124:127], v147 offset:0\n\t"
        "v_mfma_f32_16x16x32_bf16 v[76:79], a[220:223], v[92:95], v[76:79]\n\t"
        "v_mfma_f32_16x16x32_bf16 v[80:83], a[224:227], v[92:95], v[80:83]\n\t"
        "ds_read_b128 v[92:95], v183 offset:128\n\t"
        "v_mfma_f32_16x16x32_bf16 v[72:75], a[252:255], v[96:99], v[72:75]\n\t"
        "v_mfma_f32_16x16x32_bf16 v[76:79], v[188:191], v[96:99], v[76:79]\n\t"
        ".LpY2%=:\n\t"
        "s_cbranch_execz .LrY%=\n\t"
        "v_mfma_f32_16x16x32_bf16 v[80:83], v[192:195], v[96:99], v[80:83]\n\t"
        "ds_read_b128 v[96:99], v151 offset:0\n\t"
        "v_mfma_f32_16x16x32_bf16 v[72:75], v[220:223], v[100:103], v[72:75]\n\t"
        "v_mfma_f32_16x16x32_bf16 v[76:79], v[224:227], v[100:103], v[76:79]\n\t"
        "v_mfma_f32_16x16x32_bf16 v[80:83], v[228:231], v[100:103], v[80:83]\n\t"
        "ds_read_b128 v[100:103], v151 offset:64\n\t"
        "v_mfma_f32_16x16x32_bf16 v[72:75], a[24:27], v[104:107], v[72:75]\n\t"
        "s_waitcnt lgkmcnt(3)\n\t"
        "buffer_store_dwordx4 v[124:127], v145, s[28:31], s50 offen sc1\n\t"
        "v_mfma_f32_16x16x32_bf16 v[76:79], a[28:31], v[104:107], v[76:79]\n\t"
        "ds_read_b128 v[124:127], v147 offset:4096\n\t"
        "s_add_u32 s50, s50, 86016\n\t"
        "v_mfma_f32_16x16x32_bf16 v[80:83], a[32:35], v[104:107], v[80:83]\n\t"
        "ds_read_b128 v[104:107], v151 offset:128\n\t"
        "v_mfma_f32_16x16x32_bf16 v[80:83], a[68:71], v[108:111], v[80:83]\n\t"
        "v_mfma_f32_16x16x32_bf16 v[80:83], a[104:107], v[112:115], v[80:83]\n\t"
        "v_mfma_f32_16x16x32_bf16 v[80:83], a[140:143], v[84:87], v[80:83]\n\t"
        "v_mfma_f32_16x16x32_bf16 v[80:83], a[176:179], v[88:91], v[80:83]\n\t"
        "s_waitcnt lgkmcnt(3)\n\t"
        "v_mfma_f32_16x16x32_bf16 v[80:83], a[212:215], v[92:95], v[80:83]\n\t"
        "v_mfma_f32_16x16x32_bf16 v[80:83], a[248:251], v[96:99], v[80:83]\n\t"
        "s_waitcnt lgkmcnt(2)\n\t"
        "v_mfma_f32_16x16x32_bf16 v[80:83], v[216:219], v[100:103], v[80:83]\n\t"
        "s_waitcnt lgkmcnt(0)\n\t"
        "v_mfma_f32_16x16x32_bf16 v[80:83], v[252:255], v[104:107], v[80:83]\n\t"
        "buffer_store_dwordx4 v[124:127], v145, s[28:31], s50 offen sc1\n\t"
        "v_mfma_f32_16x16x32_bf16 v[72:75], a[60:63], v[108:111], v[72:75]\n\t"
        "v_mfma_f32_16x16x32_bf16 v[76:79], a[64:67], v[108:111], v[76:79]\n\t"
        "s_nop 4\n\t"
        "v_cvt_pk_bf16_f32 v120, v80, v81\n\t"
        "v_cvt_pk_bf16_f32 v121, v82, v83\n\t"
        "v_mfma_f32_16x16x32_bf16 v[72:75], a[96:99], v[112:115], v[72:75]\n\t"
        "ds_write_b64 v146, v[120:121] offset:10240\n\t"
        "v_mfma_f32_16x16x32_bf16 v[76:79], a[100:103], v[112:115], v[76:79]\n\t"
        "v_mfma_f32_16x16x32_bf16 v[72:75], a[132:135], v[84:87], v[72:75]\n\t"
        "v_mfma_f32_16x16x32_bf16 v[76:79], a[136:139], v[84:87], v[76:79]\n\t"
        "s_waitcnt lgkmcnt(0)\n\t"
        "s_barrier\n\t"
        "v_readlane_b32 s55, v155, 51\n\t"
        "v_mfma_f32_16x16x32_bf16 v[72:75], a[168:171], v[88:91], v[72:75]\n\t"
        "ds_read_b128 v[124:127], v147 offset:8192\n\t"
        "v_mfma_f32_16x16x32_bf16 v[76:79], a[172:175], v[88:91], v[76:79]\n\t"
        "v_mfma_f32_16x16x32_bf16 v[72:75], a[204:207], v[92:95], v[72:75]\n\t"
        "v_mfma_f32_16x16x32_bf16 v[76:79], a[208:211], v[92:95], v[76:79]\n\t"
        "v_mfma_f32_16x16x32_bf16 v[72:75], a[240:243], v[96:99], v[72:75]\n\t"
        "v_mfma_f32_16x16x32_bf16 v[76:79], a[244:247], v[96:99], v[76:79]\n\t"
        "v_mfma_f32_16x16x32_bf16 v[72:75], v[208:211], v[100:103], v[72:75]\n\t"
        "v_mfma_f32_16x16x32_bf16 v[76:79], v[212:215], v[100:103], v[76:79]\n\t"
        "v_mfma_f32_16x16x32_bf16 v[72:75], v[244:247], v[104:107], v[72:75]\n\t"
        "v_mfma_f32_16x16x32_bf16 v[76:79], v[248:251], v[104:107], v[76:79]\n\t"
        "s_nop 6\n\t"
        "v_cvt_pk_bf16_f32 v116, v72, v73\n\t"
        "v_cvt_pk_bf16_f32 v117, v74, v75\n\t"
        "v_cvt_pk_bf16_f32 v118, v76, v77\n\t"
        "v_cvt_pk_bf16_f32 v119, v78, v79\n\t"
        "s_nop 0\n\t"
        "v_permlane16_swap_b32 v116, v118\n\t"
        "v_permlane16_swap_b32 v117, v119\n\t"
        "buffer_store_dwordx4 v[116:119], v148, s[28:31], s55 offen  \n\t"
        "s_waitcnt lgkmcnt(0)\n\t"
        "buffer_store_dwordx4 v[124:127], v150, s[28:31], s52 offen  \n\t"
        "s_andn2_b32 s6, s6, 2\n\t"
        "s_cbranch_scc1 99f\n\t"
        "s_endpgm\n\t"
        ".Lx50%=:\n\t"
        "v_mov_b32 v151, v185\n\t"
        "v_mov_b32 v183, v186\n\t"
        "s_min_u32 s8, s3, 64\n\t"
        "s_waitcnt lgkmcnt(0)\n\t"
        "30:\n\t"
        "ds_read_b128 v[160:163], v185\n\t"
        "s_waitcnt lgkmcnt(0)\n\t"
        "v_lshlrev_b32_sdwa v157, 5, v160 dst_sel:DWORD dst_unused:UNUSED_PAD src0_sel:DWORD src1_sel:WORD_0\n\t"
        "v_lshlrev_b32_sdwa v158, 5, v160 dst_sel:DWORD dst_unused:UNUSED_PAD src0_sel:DWORD src1_sel:WORD_1\n\t"
        "v_or_b32 v157, v184, v157\n\t"
        "v_or_b32 v158, v184, v158\n\t"
        "v_xor_b32 v159, 32, v157\n\t"
        "v_xor_b32 v152, 32, v158\n\t"
        "ds_read_b64_tr_b16 v[60:61], v157 offset:0\n\t"
        "ds_read_b64_tr_b16 v[62:63], v158 offset:0\n\t"
        "ds_read_b64_tr_b16 v[64:65], v159 offset:0\n\t"
        "ds_read_b64_tr_b16 v[66:67], v152 offset:0\n\t"
        "ds_read_b64_tr_b16 v[68:69], v157 offset:64\n\t"
        "ds_read_b64_tr_b16 v[70:71], v158 offset:64\n\t"
        "ds_read_b64_tr_b16 v[72:73], v159 offset:64\n\t"
        "ds_read_b64_tr_b16 v[74:75], v152 offset:64\n\t"
        "ds_read_b64_tr_b16 v[76:77], v157 offset:128\n\t"
        "ds_read_b64_tr_b16 v[78:79], v158 offset:128\n\t"
        "ds_read_b64_tr_b16 v[80:81], v159 offset:128\n\t"
        "ds_read_b64_tr_b16 v[82:83], v152 offset:128\n\t"
        "v_perm_b32 v164, v162, v162, v168\n\t"
        "v_perm_b32 v165, v162, v162, v169\n\t"
        "v_perm_b32 v166, v162, v162, v170\n\t"
        "v_perm_b32 v167, v162, v162, v171\n\t"
        "v_cmp_ne_u32 vcc, 0, v163\n\t"
        "s_waitcnt lgkmcnt(10)\n\t"
        "v_mfma_f32_16x16x32_bf16 v[48:51], v[60:63], v[164:167], 0\n\t"
        "s_waitcnt lgkmcnt(8)\n\t"
        "v_mfma_f32_16x16x32_bf16 v[52:55], v[64:67], v[164:167], 0\n\t"
        "s_waitcnt lgkmcnt(6)\n\t"
        "v_mfma_f32_16x16x32_bf16 v[56:59], v[68:71], v[164:167], 0\n\t"
        "s_waitcnt lgkmcnt(4)\n\t"
        "v_mfma_f32_16x16x32_bf16 v[116:119], v[72:75], v[164:167], 0\n\t"
        "s_waitcnt lgkmcnt(2)\n\t"
        "v_mfma_f32_16x16x32_bf16 v[120:123], v[76:79], v[164:167], 0\n\t"
        "s_waitcnt lgkmcnt(0)\n\t"
        "v_mfma_f32_16x16x32_bf16 v[124:127], v[80:83], v[164:167], 0\n\t"
        "s_cbranch_vccz 31f\n\t"
        "v_lshlrev_b32_sdwa v157, 5, v161 dst_sel:DWORD dst_unused:UNUSED_PAD src0_sel:DWORD src1_sel:WORD_0\n\t"
        "v_lshlrev_b32_sdwa v158, 5, v161 dst_sel:DWORD dst_unused:UNUSED_PAD src0_sel:DWORD src1_sel:WORD_1\n\t"
        "v_or_b32 v157, v184, v157\n\t"
        "v_or_b32 v158, v184, v158\n\t"
        "v_xor_b32 v159, 32, v157\n\t"
        "v_xor_b32 v152, 32, v158\n\t"
        "ds_read_b64_tr_b16 v[60:61], v157 offset:0\n\t"
        "ds_read_b64_tr_b16 v[62:63], v158 offset:0\n\t"
        "ds_read_b64_tr_b16 v[64:65], v159 offset:0\n\t"
        "ds_read_b64_tr_b16 v[66:67], v152 offset:0\n\t"
        "ds_read_b64_tr_b16 v[68:69], v157 offset:64\n\t"
        "ds_read_b64_tr_b16 v[70:71], v158 offset:64\n\t"
        "ds_read_b64_tr_b16 v[72:73], v159 offset:64\n\t"
        "ds_read_b64_tr_b16 v[74:75], v152 offset:64\n\t"
        "ds_read_b64_tr_b16 v[76:77], v157 offset:128\n\t"
        "ds_read_b64_tr_b16 v[78:79], v158 offset:128\n\t"
        "ds_read_b64_tr_b16 v[80:81], v159 offset:128\n\t"
        "ds_read_b64_tr_b16 v[82:83], v152 offset:128\n\t"
        "v_perm_b32 v164, v163, v163, v168\n\t"
        "v_perm_b32 v165, v163, v163, v169\n\t"
        "v_perm_b32 v166, v163, v163, v170\n\t"
        "v_perm_b32 v167, v163, v163, v171\n\t"
        "s_waitcnt lgkmcnt(10)\n\t"
        "s_nop 0\n\t"
        "v_mfma_f32_16x16x32_bf16 v[48:51], v[60:63], v[164:167], v[48:51]\n\t"
        "s_waitcnt lgkmcnt(8)\n\t"
        "v_mfma_f32_16x16x32_bf16 v[52:55], v[64:67], v[164:167], v[52:55]\n\t"
        "s_waitcnt lgkmcnt(6)\n\t"
        "v_mfma_f32_16x16x32_bf16 v[56:59], v[68:71], v[164:167], v[56:59]\n\t"
        "s_waitcnt lgkmcnt(4)\n\t"
        "v_mfma_f32_16x16x32_bf16 v[116:119], v[72:75], v[164:167], v[116:119]\n\t"
        "s_waitcnt lgkmcnt(2)\n\t"
        "v_mfma_f32_16x16x32_bf16 v[120:123], v[76:79], v[164:167], v[120:123]\n\t"
        "s_waitcnt lgkmcnt(0)\n\t"
        "v_mfma_f32_16x16x32_bf16 v[124:127], v[80:83], v[164:167], v[124:127]\n\t"
        "31:\n\t"
        "v_cvt_pk_bf16_f32 v48, v48, v49\n\t"
        "v_cvt_pk_bf16_f32 v49, v50, v51\n\t"
        "v_cvt_pk_bf16_f32 v52, v52, v53\n\t"
        "v_cvt_pk_bf16_f32 v53, v54, v55\n\t"
        "v_cvt_pk_bf16_f32 v56, v56, v57\n\t"
        "v_cvt_pk_bf16_f32 v57, v58, v59\n\t"
        "v_cvt_pk_bf16_f32 v116, v116, v117\n\t"
        "v_cvt_pk_bf16_f32 v117, v118, v119\n\t"
        "v_cvt_pk_bf16_f32 v120, v120, v121\n\t"
        "v_cvt_pk_bf16_f32 v121, v122, v123\n\t"
        "v_cvt_pk_bf16_f32 v124, v124, v125\n\t"
        "v_cvt_pk_bf16_f32 v125, v126, v127\n\t"
        "ds_write_b64 v186, v[48:49] offset:0\n\t"
        "ds_write_b64 v154, v[52:53] offset:0\n\t"
        "ds_write_b64 v186, v[56:57] offset:64\n\t"
        "ds_write_b64 v154, v[116:117] offset:64\n\t"
        "ds_write_b64 v186, v[120:121] offset:128\n\t"
        "ds_write_b64 v154, v[124:125] offset:128\n\t"
        "v_add_u32 v185, 256, v185\n\t"
        "v_add_u32 v186, 12288, v186\n\t"
        "v_xor_b32 v154, 32, v186\n\t"
        "s_sub_i32 s8, s8, 16\n\t"
        "s_cmp_gt_i32 s8, 0\n\t"
        "s_cbranch_scc1 30b\n\t"
        "v_mov_b32 v185, v151\n\t"
        "v_mov_b32 v186, v183\n\t"
        "v_xor_b32 v154, 32, v186\n\t"
        "s_waitcnt lgkmcnt(0)\n\t"
        "s_barrier\n\t"
        "s_branch .Lx51%=\n\t"
        ".LwM%=:\n\t"
        "s_branch .LrM%=\n\t"
        ".Lx54%=:\n\t"
        "v_add_u32 v185, 768, v185\n\t"
        "v_add_u32 v186, 36864, v186\n\t"
        "v_xor_b32 v154, 32, v186\n\t"
        "s_min_u32 s47, s61, 64\n\t"
        "s_sub_u32 s47, s47, 48\n\t"
        "s_waitcnt lgkmcnt(0)\n\t"
        "40:\n\t"
        "ds_read_b128 v[160:163], v185\n\t"
        "s_waitcnt lgkmcnt(0)\n\t"
        "v_lshlrev_b32_sdwa v157, 5, v160 dst_sel:DWORD dst_unused:UNUSED_PAD src0_sel:DWORD src1_sel:WORD_0\n\t"
        "v_lshlrev_b32_sdwa v158, 5, v160 dst_sel:DWORD dst_unused:UNUSED_PAD src0_sel:DWORD src1_sel:WORD_1\n\t"
        "v_or_b32 v157, v184, v157\n\t"
        "v_or_b32 v158, v184, v158\n\t"
        "v_xor_b32 v159, 32, v157\n\t"
        "v_xor_b32 v152, 32, v158\n\t"
        "ds_read_b64_tr_b16 v[60:61], v157 offset:0\n\t"
        "ds_read_b64_tr_b16 v[62:63], v158 offset:0\n\t"
        "ds_read_b64_tr_b16 v[64:65], v159 offset:0\n\t"
        "ds_read_b64_tr_b16 v[66:67], v152 offset:0\n\t"
        "ds_read_b64_tr_b16 v[68:69], v157 offset:64\n\t"
        "ds_read_b64_tr_b16 v[70:71], v158 offset:64\n\t"
        "ds_read_b64_tr_b16 v[72:73], v159 offset:64\n\t"
        "ds_read_b64_tr_b16 v[74:75], v152 offset:64\n\t"
        "ds_read_b64_tr_b16 v[76:77], v157 offset:128\n\t"
        "ds_read_b64_tr_b16 v[78:79], v158 offset:128\n\t"
        "ds_read_b64_tr_b16 v[80:81], v159 offset:128\n\t"
        "ds_read_b64_tr_b16 v[82:83], v152 offset:128\n\t"
        "v_perm_b32 v164, v162, v162, v168\n\t"
        "v_perm_b32 v165, v162, v162, v169\n\t"
        "v_perm_b32 v166, v162, v162, v170\n\t"
        "v_perm_b32 v167, v162, v162, v171\n\t"
        "v_cmp_ne_u32 vcc, 0, v163\n\t"
        "s_waitcnt lgkmcnt(10)\n\t"
        "v_mfma_f32_16x16x32_bf16 v[0:3], v[60:63], v[164:167], 0\n\t"
        "s_waitcnt lgkmcnt(8)\n\t"
        "v_mfma_f32_16x16x32_bf16 v[4:7], v[64:67], v[164:167], 0\n\t"
        "s_waitcnt lgkmcnt(6)\n\t"
        "v_mfma_f32_16x16x32_bf16 v[8:11], v[68:71], v[164:167], 0\n\t"
        "s_waitcnt lgkmcnt(4)\n\t"
        "v_mfma_f32_16x16x32_bf16 v[12:15], v[72:75], v[164:167], 0\n\t"
        "s_waitcnt lgkmcnt(2)\n\t"
        "v_mfma_f32_16x16x32_bf16 v[16:19], v[76:79], v[164:167], 0\n\t"
        "s_waitcnt lgkmcnt(0)\n\t"
        "v_mfma_f32_16x16x32_bf16 v[20:23], v[80:83], v[164:167], 0\n\t"
        "s_cbranch_vccz 41f\n\t"
        "v_lshlrev_b32_sdwa v157, 5, v161 dst_sel:DWORD dst_unused:UNUSED_PAD src0_sel:DWORD src1_sel:WORD_0\n\t"
        "v_lshlrev_b32_sdwa v158, 5, v161 dst_sel:DWORD dst_unused:UNUSED_PAD src0_sel:DWORD src1_sel:WORD_1\n\t"
        "v_or_b32 v157, v184, v157\n\t"
        "v_or_b32 v158, v184, v158\n\t"
        "v_xor_b32 v159, 32, v157\n\t"
        "v_xor_b32 v152, 32, v158\n\t"
        "ds_read_b64_tr_b16 v[60:61], v157 offset:0\n\t"
        "ds_read_b64_tr_b16 v[62:63], v158 offset:0\n\t"
        "ds_read_b64_tr_b16 v[64:65], v159 offset:0\n\t"
        "ds_read_b64_tr_b16 v[66:67], v152 offset:0\n\t"
        "ds_read_b64_tr_b16 v[68:69], v157 offset:64\n\t"
        "ds_read_b64_tr_b16 v[70:71], v158 offset:64\n\t"
        "ds_read_b64_tr_b16 v[72:73], v159 offset:64\n\t"
        "ds_read_b64_tr_b16 v[74:75], v152 offset:64\n\t"
        "ds_read_b64_tr_b16 v[76:77], v157 offset:128\n\t"
        "ds_read_b64_tr_b16 v[78:79], v158 offset:128\n\t"
        "ds_read_b64_tr_b16 v[80:81], v159 offset:128\n\t"
        "ds_read_b64_tr_b16 v[82:83], v152 offset:128\n\t"
        "v_perm_b32 v164, v163, v163, v168\n\t"
        "v_perm_b32 v165, v163, v163, v169\n\t"
        "v_perm_b32 v166, v163, v163, v170\n\t"
        "v_perm_b32 v167, v163, v163, v171\n\t"
        "s_waitcnt lgkmcnt(10)\n\t"
        "s_nop 0\n\t"
        "v_mfma_f32_16x16x32_bf16 v[0:3], v[60:63], v[164:167], v[0:3]\n\t"
        "s_waitcnt lgkmcnt(8)\n\t"
        "v_mfma_f32_16x16x32_bf16 v[4:7], v[64:67], v[164:167], v[4:7]\n\t"
        "s_waitcnt lgkmcnt(6)\n\t"
        "v_mfma_f32_16x16x32_bf16 v[8:11], v[68:71], v[164:167], v[8:11]\n\t"
        "s_waitcnt lgkmcnt(4)\n\t"
        "v_mfma_f32_16x16x32_bf16 v[12:15], v[72:75], v[164:167], v[12:15]\n\t"
        "s_waitcnt lgkmcnt(2)\n\t"
        "v_mfma_f32_16x16x32_bf16 v[16:19], v[76:79], v[164:167], v[16:19]\n\t"
        "s_waitcnt lgkmcnt(0)\n\t"
        "v_mfma_f32_16x16x32_bf16 v[20:23], v[80:83], v[164:167], v[20:23]\n\t"
        "41:\n\t"
        "v_cvt_pk_bf16_f32 v0, v0, v1\n\t"
        "v_cvt_pk_bf16_f32 v1, v2, v3\n\t"
        "v_cvt_pk_bf16_f32 v4, v4, v5\n\t"
        "v_cvt_pk_bf16_f32 v5, v6, v7\n\t"
        "v_cvt_pk_bf16_f32 v8, v8, v9\n\t"
        "v_cvt_pk_bf16_f32 v9, v10, v11\n\t"
        "v_cvt_pk_bf16_f32 v12, v12, v13\n\t"
        "v_cvt_pk_bf16_f32 v13, v14, v15\n\t"
        "v_cvt_pk_bf16_f32 v16, v16, v17\n\t"
        "v_cvt_pk_bf16_f32 v17, v18, v19\n\t"
        "v_cvt_pk_bf16_f32 v20, v20, v21\n\t"
        "v_cvt_pk_bf16_f32 v21, v22, v23\n\t"
        "ds_write_b64 v186, v[0:1] offset:0\n\t"
        "ds_write_b64 v154, v[4:5] offset:0\n\t"
        "ds_write_b64 v186, v[8:9] offset:64\n\t"
        "ds_write_b64 v154, v[12:13] offset:64\n\t"
        "ds_write_b64 v186, v[16:17] offset:128\n\t"
        "ds_write_b64 v154, v[20:21] offset:128\n\t"
        "v_add_u32 v185, 256, v185\n\t"
        "v_add_u32 v186, 12288, v186\n\t"
        "v_xor_b32 v154, 32, v186\n\t"
        "s_sub_i32 s47, s47, 16\n\t"
        "s_cmp_gt_i32 s47, 0\n\t"
        "s_cbranch_scc1 40b\n\t"
        "s_cmp_le_u32 s61, 64\n\t"
        "s_cbranch_scc1 52f\n\t"
        "v_bfe_u32 v64, v185, 4, 4\n\t"
        "v_add_u32 v60, 65, v64\n\t"
        "s_sub_u32 s8, s37, 12288\n\t"
        "v_mov_b32 v63, s62\n\t"
        "v_lshlrev_b32 v65, 3, v64\n\t"
        "v_mul_u32_u24 v64, 192, v64\n\t"
        "v_and_or_b32 v64, v65, 48, v64\n\t"
        "v_add_u32 v61, s8, v64\n\t"
        "v_subrev_u32 v186, 61440, v186\n\t"
        "v_xor_b32 v154, 32, v186\n\t"
        "v_min_u32 v16, s61, v60\n\t"
        "v_lshl_add_u32 v16, v16, 3, s60\n\t"
        "ds_read_b64 v[0:1], v16\n\t"
        "v_cmp_ge_u32 s[66:67], s61, v60\n\t"
        "s_mov_b32 s46, 0x07060302\n\t"
        "s_waitcnt lgkmcnt(0)\n\t"
        "v_and_b32 v16, 0xfffff800, v1\n\t"
        "ds_read_b128 v[2:5], v16 offset:1536\n\t"
        "v_lshlrev_b32 v10, 16, v0\n\t"
        "v_and_b32 v11, 0xffff0000, v0\n\t"
        "v_med3_f32 v10, v10, 0, s63\n\t"
        "v_med3_f32 v11, v11, 0, s63\n\t"
        "s_mov_b64 exec, s[66:67]\n\t"
        "ds_write_b32 v1, v61\n\t"
        "s_mov_b64 exec, -1\n\t"
        "v_perm_b32 v0, v11, v10, s46\n\t"
        "v_fract_f32 v12, v10\n\t"
        "v_fract_f32 v13, v11\n\t"
        "v_cvt_u32_f32 v14, v10\n\t"
        "v_cvt_u32_f32 v15, v11\n\t"
        "v_cmp_neq_f32 s[64:65], 0, v12\n\t"
        "v_cmp_neq_f32 s[44:45], 0, v13\n\t"
        "v_sub_f32 v16, 1.0, v12\n\t"
        "v_sub_f32 v17, 1.0, v13\n\t"
        "v_cvt_pk_bf16_f32 v17, v16, v17\n\t"
        "v_lshlrev_b32 v16, 16, v17\n\t"
        "v_and_b32 v17, 0xffff0000, v17\n\t"
        "v_mul_f32 v18, v16, v17\n\t"
        "v_mul_f32 v16, v16, v13\n\t"
        "v_mul_f32 v17, v12, v17\n\t"
        "v_mul_f32 v19, v12, v13\n\t"
        "v_cndmask_b32 v16, v17, v16, s[44:45]\n\t"
        "v_cndmask_b32 v17, 0, v17, s[44:45]\n\t"
        "v_cvt_pk_bf16_f32 v162, v18, v16\n\t"
        "v_cvt_pk_bf16_f32 v163, v17, v19\n\t"
        "s_waitcnt lgkmcnt(1)\n\t"
        "v_pk_max_u16 v16, v0, v2\n\t"
        "v_pk_min_u16 v16, v16, v3\n\t"
        "v_cmp_ne_u32 vcc, v16, v0\n\t"
        "v_sub_u32 v16, v15, v5\n\t"
        "v_mad_u32_u24 v17, v15, 6, v4\n\t"
        "v_mad_u32_u24 v17, v14, s62, v17\n\t"
        "s_and_b64 vcc, vcc, s[66:67]\n\t"
        "s_or_b64 s[56:57], s[56:57], vcc\n\t"
        "v_bfe_u32 v18, v16, 2, 1\n\t"
        "v_or_b32 v18, v18, v17\n\t"
        "v_add_u32 v16, 1, v16\n\t"
        "v_bfe_u32 v16, v16, 2, 1\n\t"
        "v_add3_u32 v16, v17, v16, 6\n\t"
        "v_cndmask_b32 v16, v18, v16, s[44:45]\n\t"
        "v_cndmask_b32 v17, 0, v63, s[64:65]\n\t"
        "v_add_u32 v19, v18, v17\n\t"
        "v_add_u32 v20, v16, v17\n\t"
        "v_cndmask_b32 v16, v19, v16, s[44:45]\n\t"
        "v_lshl_or_b32 v160, v16, 16, v18\n\t"
        "v_lshl_or_b32 v161, v20, 16, v19\n\t"
        "v_lshlrev_b32_sdwa v157, 5, v160 dst_sel:DWORD dst_unused:UNUSED_PAD src0_sel:DWORD src1_sel:WORD_0\n\t"
        "v_lshlrev_b32_sdwa v158, 5, v160 dst_sel:DWORD dst_unused:UNUSED_PAD src0_sel:DWORD src1_sel:WORD_1\n\t"
        "v_or_b32 v157, v184, v157\n\t"
        "v_or_b32 v158, v184, v158\n\t"
        "v_xor_b32 v159, 32, v157\n\t"
        "v_xor_b32 v152, 32, v158\n\t"
        "ds_read_b64_tr_b16 v[60:61], v157 offset:0\n\t"
        "ds_read_b64_tr_b16 v[62:63], v158 offset:0\n\t"
        "ds_read_b64_tr_b16 v[64:65], v159 offset:0\n\t"
        "ds_read_b64_tr_b16 v[66:67], v152 offset:0\n\t"
        "ds_read_b64_tr_b16 v[68:69], v157 offset:64\n\t"
        "ds_read_b64_tr_b16 v[70:71], v158 offset:64\n\t"
        "ds_read_b64_tr_b16 v[72:73], v159 offset:64\n\t"
        "ds_read_b64_tr_b16 v[74:75], v152 offset:64\n\t"
        "ds_read_b64_tr_b16 v[76:77], v157 offset:128\n\t"
        "ds_read_b64_tr_b16 v[78:79], v158 offset:128\n\t"
        "ds_read_b64_tr_b16 v[80:81], v159 offset:128\n\t"
        "ds_read_b64_tr_b16 v[82:83], v152 offset:128\n\t"
        "v_lshlrev_b32_sdwa v157, 5, v161 dst_sel:DWORD dst_unused:UNUSED_PAD src0_sel:DWORD src1_sel:WORD_0\n\t"
        "v_lshlrev_b32_sdwa v158, 5, v161 dst_sel:DWORD dst_unused:UNUSED_PAD src0_sel:DWORD src1_sel:WORD_1\n\t"
        "v_or_b32 v157, v184, v157\n\t"
        "v_or_b32 v158, v184, v158\n\t"
        "v_xor_b32 v159, 32, v157\n\t"
        "v_xor_b32 v152, 32, v158\n\t"
        "ds_read_b64_tr_b16 v[24:25], v157 offset:0\n\t"
        "ds_read_b64_tr_b16 v[26:27], v158 offset:0\n\t"
        "ds_read_b64_tr_b16 v[28:29], v159 offset:0\n\t"
        "ds_read_b64_tr_b16 v[30:31], v152 offset:0\n\t"
        "ds_read_b64_tr_b16 v[32:33], v157 offset:64\n\t"
        "ds_read_b64_tr_b16 v[34:35], v158 offset:64\n\t"
        "ds_read_b64_tr_b16 v[36:37], v159 offset:64\n\t"
        "ds_read_b64_tr_b16 v[38:39], v152 offset:64\n\t"
        "ds_read_b64_tr_b16 v[40:41], v157 offset:128\n\t"
        "ds_read_b64_tr_b16 v[42:43], v158 offset:128\n\t"
        "ds_read_b64_tr_b16 v[44:45], v159 offset:128\n\t"
        "ds_read_b64_tr_b16 v[46:47], v152 offset:128\n\t"
        "s_cmp_lg_u64 s[56:57], 0\n\t"
        "s_cselect_b32 s8, 1, 0\n\t"
        "v_mov_b32 v157, s8\n\t"
        "v_perm_b32 v164, v162, v162, v168\n\t"
        "v_perm_b32 v165, v162, v162, v169\n\t"
        "v_perm_b32 v166, v162, v162, v170\n\t"
        "v_perm_b32 v167, v162, v162, v171\n\t"
        "s_waitcnt lgkmcnt(12)\n\t"
        "s_nop 0\n\t"
        "v_mfma_f32_16x16x32_bf16 v[0:3], v[60:63], v[164:167], 0\n\t"
        "v_mfma_f32_16x16x32_bf16 v[4:7], v[64:67], v[164:167], 0\n\t"
        "v_mfma_f32_16x16x32_bf16 v[8:11], v[68:71], v[164:167], 0\n\t"
        "v_mfma_f32_16x16x32_bf16 v[12:15], v[72:75], v[164:167], 0\n\t"
        "v_mfma_f32_16x16x32_bf16 v[16:19], v[76:79], v[164:167], 0\n\t"
        "v_mfma_f32_16x16x32_bf16 v[20:23], v[80:83], v[164:167], 0\n\t"
        "v_perm_b32 v164, v163, v163, v168\n\t"
        "v_perm_b32 v165, v163, v163, v169\n\t"
        "v_perm_b32 v166, v163, v163, v170\n\t"
        "v_perm_b32 v167, v163, v163, v171\n\t"
        "s_waitcnt lgkmcnt(3)\n\t"
        "s_nop 0\n\t"
        "v_mfma_f32_16x16x32_bf16 v[0:3], v[24:27], v[164:167], v[0:3]\n\t"
        "v_mfma_f32_16x16x32_bf16 v[4:7], v[28:31], v[164:167], v[4:7]\n\t"
        "v_mfma_f32_16x16x32_bf16 v[8:11], v[32:35], v[164:167], v[8:11]\n\t"
        "v_mfma_f32_16x16x32_bf16 v[12:15], v[36:39], v[164:167], v[12:15]\n\t"
        "s_waitcnt lgkmcnt(0)\n\t"
        "v_mfma_f32_16x16x32_bf16 v[16:19], v[40:43], v[164:167], v[16:19]\n\t"
        "v_mfma_f32_16x16x32_bf16 v[20:23], v[44:47], v[164:167], v[20:23]\n\t"
        "ds_write_b32 v153, v157 offset:16\n\t"
        "s_nop 0\n\t"
        "v_cvt_pk_bf16_f32 v0, v0, v1\n\t"
        "v_cvt_pk_bf16_f32 v1, v2, v3\n\t"
        "v_cvt_pk_bf16_f32 v4, v4, v5\n\t"
        "v_cvt_pk_bf16_f32 v5, v6, v7\n\t"
        "v_cvt_pk_bf16_f32 v8, v8, v9\n\t"
        "v_cvt_pk_bf16_f32 v9, v10, v11\n\t"
        "v_cvt_pk_bf16_f32 v12, v12, v13\n\t"
        "v_cvt_pk_bf16_f32 v13, v14, v15\n\t"
        "v_cvt_pk_bf16_f32 v16, v16, v17\n\t"
        "v_cvt_pk_bf16_f32 v17, v18, v19\n\t"
        "v_cvt_pk_bf16_f32 v20, v20, v21\n\t"
        "v_cvt_pk_bf16_f32 v21, v22, v23\n\t"
        "ds_write_b64 v186, v[0:1] offset:0\n\t"
        "ds_write_b64 v154, v[4:5] offset:0\n\t"
        "ds_write_b64 v186, v[8:9] offset:64\n\t"
        "ds_write_b64 v154, v[12:13] offset:64\n\t"
        "ds_write_b64 v186, v[16:17] offset:128\n\t"
        "ds_write_b64 v154, v[20:21] offset:128\n\t"
        "52:\n\t"
        "s_branch .Lx55%=\n\t"
        ".Lx58%=:\n\t"
        "v_mov_b32 v151, v185\n\t"
        "v_mov_b32 v183, v186\n\t"
        "s_min_u32 s8, s3, 64\n\t"
        "s_waitcnt lgkmcnt(0)\n\t"
        "30:\n\t"
        "ds_read_b128 v[160:163], v185\n\t"
        "s_waitcnt lgkmcnt(0)\n\t"
        "v_lshlrev_b32_sdwa v157, 5, v160 dst_sel:DWORD dst_unused:UNUSED_PAD src0_sel:DWORD src1_sel:WORD_0\n\t"
        "v_lshlrev_b32_sdwa v158, 5, v160 dst_sel:DWORD dst_unused:UNUSED_PAD src0_sel:DWORD src1_sel:WORD_1\n\t"
        "v_or_b32 v157, v184, v157\n\t"
        "v_or_b32 v158, v184, v158\n\t"
        "v_xor_b32 v159, 32, v157\n\t"
        "v_xor_b32 v152, 32, v158\n\t"
        "ds_read_b64_tr_b16 v[60:61], v157 offset:0\n\t"
        "ds_read_b64_tr_b16 v[62:63], v158 offset:0\n\t"
        "ds_read_b64_tr_b16 v[64:65], v159 offset:0\n\t"
        "ds_read_b64_tr_b16 v[66:67], v152 offset:0\n\t"
        "ds_read_b64_tr_b16 v[68:69], v157 offset:64\n\t"
        "ds_read_b64_tr_b16 v[70:71], v158 offset:64\n\t"
        "ds_read_b64_tr_b16 v[72:73], v159 offset:64\n\t"
        "ds_read_b64_tr_b16 v[74:75], v152 offset:64\n\t"
        "ds_read_b64_tr_b16 v[76:77], v157 offset:128\n\t"
        "ds_read_b64_tr_b16 v[78:79], v158 offset:128\n\t"
        "ds_read_b64_tr_b16 v[80:81], v159 offset:128\n\t"
        "ds_read_b64_tr_b16 v[82:83], v152 offset:128\n\t"
        "v_perm_b32 v164, v162, v162, v168\n\t"
        "v_perm_b32 v165, v162, v162, v169\n\t"
        "v_perm_b32 v166, v162, v162, v170\n\t"
        "v_perm_b32 v167, v162, v162, v171\n\t"
        "v_cmp_ne_u32 vcc, 0, v163\n\t"
        "s_waitcnt lgkmcnt(10)\n\t"
        "v_mfma_f32_16x16x32_bf16 v[48:51], v[60:63], v[164:167], 0\n\t"
        "s_waitcnt lgkmcnt(8)\n\t"
        "v_mfma_f32_16x16x32_bf16 v[52:55], v[64:67], v[164:167], 0\n\t"
        "s_waitcnt lgkmcnt(6)\n\t"
        "v_mfma_f32_16x16x32_bf16 v[56:59], v[68:71], v[164:167], 0\n\t"
        "s_waitcnt lgkmcnt(4)\n\t"
        "v_mfma_f32_16x16x32_bf16 v[116:119], v[72:75], v[164:167], 0\n\t"
        "s_waitcnt lgkmcnt(2)\n\t"
        "v_mfma_f32_16x16x32_bf16 v[120:123], v[76:79], v[164:167], 0\n\t"
        "s_waitcnt lgkmcnt(0)\n\t"
        "v_mfma_f32_16x16x32_bf16 v[124:127], v[80:83], v[164:167], 0\n\t"
        "s_cbranch_vccz 31f\n\t"
        "v_lshlrev_b32_sdwa v157, 5, v161 dst_sel:DWORD dst_unused:UNUSED_PAD src0_sel:DWORD src1_sel:WORD_0\n\t"
        "v_lshlrev_b32_sdwa v158, 5, v161 dst_sel:DWORD dst_unused:UNUSED_PAD src0_sel:DWORD src1_sel:WORD_1\n\t"
        "v_or_b32 v157, v184, v157\n\t"
        "v_or_b32 v158, v184, v158\n\t"
        "v_xor_b32 v159, 32, v157\n\t"
        "v_xor_b32 v152, 32, v158\n\t"
        "ds_read_b64_tr_b16 v[60:61], v157 offset:0\n\t"
        "ds_read_b64_tr_b16 v[62:63], v158 offset:0\n\t"
        "ds_read_b64_tr_b16 v[64:65], v159 offset:0\n\t"
        "ds_read_b64_tr_b16 v[66:67], v152 offset:0\n\t"
        "ds_read_b64_tr_b16 v[68:69], v157 offset:64\n\t"
        "ds_read_b64_tr_b16 v[70:71], v158 offset:64\n\t"
        "ds_read_b64_tr_b16 v[72:73], v159 offset:64\n\t"
        "ds_read_b64_tr_b16 v[74:75], v152 offset:64\n\t"
        "ds_read_b64_tr_b16 v[76:77], v157 offset:128\n\t"
        "ds_read_b64_tr_b16 v[78:79], v158 offset:128\n\t"
        "ds_read_b64_tr_b16 v[80:81], v159 offset:128\n\t"
        "ds_read_b64_tr_b16 v[82:83], v152 offset:128\n\t"
        "v_perm_b32 v164, v163, v163, v168\n\t"
        "v_perm_b32 v165, v163, v163, v169\n\t"
        "v_perm_b32 v166, v163, v163, v170\n\t"
        "v_perm_b32 v167, v163, v163, v171\n\t"
        "s_waitcnt lgkmcnt(10)\n\t"
        "s_nop 0\n\t"
        "v_mfma_f32_16x16x32_bf16 v[48:51], v[60:63], v[164:167], v[48:51]\n\t"
        "s_waitcnt lgkmcnt(8)\n\t"
        "v_mfma_f32_16x16x32_bf16 v[52:55], v[64:67], v[164:167], v[52:55]\n\t"
        "s_waitcnt lgkmcnt(6)\n\t"
        "v_mfma_f32_16x16x32_bf16 v[56:59], v[68:71], v[164:167], v[56:59]\n\t"
        "s_waitcnt lgkmcnt(4)\n\t"
        "v_mfma_f32_16x16x32_bf16 v[116:119], v[72:75], v[164:167], v[116:119]\n\t"
        "s_waitcnt lgkmcnt(2)\n\t"
        "v_mfma_f32_16x16x32_bf16 v[120:123], v[76:79], v[164:167], v[120:123]\n\t"
        "s_waitcnt lgkmcnt(0)\n\t"
        "v_mfma_f32_16x16x32_bf16 v[124:127], v[80:83], v[164:167], v[124:127]\n\t"
        "31:\n\t"
        "v_cvt_pk_bf16_f32 v48, v48, v49\n\t"
        "v_cvt_pk_bf16_f32 v49, v50, v51\n\t"
        "v_cvt_pk_bf16_f32 v52, v52, v53\n\t"
        "v_cvt_pk_bf16_f32 v53, v54, v55\n\t"
        "v_cvt_pk_bf16_f32 v56, v56, v57\n\t"
        "v_cvt_pk_bf16_f32 v57, v58, v59\n\t"
        "v_cvt_pk_bf16_f32 v116, v116, v117\n\t"
        "v_cvt_pk_bf16_f32 v117, v118, v119\n\t"
        "v_cvt_pk_bf16_f32 v120, v120, v121\n\t"
        "v_cvt_pk_bf16_f32 v121, v122, v123\n\t"
        "v_cvt_pk_bf16_f32 v124, v124, v125\n\t"
        "v_cvt_pk_bf16_f32 v125, v126, v127\n\t"
        "ds_write_b64 v186, v[48:49] offset:0\n\t"
        "ds_write_b64 v154, v[52:53] offset:0\n\t"
        "ds_write_b64 v186, v[56:57] offset:64\n\t"
        "ds_write_b64 v154, v[116:117] offset:64\n\t"
        "ds_write_b64 v186, v[120:121] offset:128\n\t"
        "ds_write_b64 v154, v[124:125] offset:128\n\t"
        "v_add_u32 v185, 256, v185\n\t"
        "v_add_u32 v186, 12288, v186\n\t"
        "v_xor_b32 v154, 32, v186\n\t"
        "s_sub_i32 s8, s8, 16\n\t"
        "s_cmp_gt_i32 s8, 0\n\t"
        "s_cbranch_scc1 30b\n\t"
        "v_mov_b32 v185, v151\n\t"
        "v_mov_b32 v186, v183\n\t"
        "v_xor_b32 v154, 32, v186\n\t"
        "s_waitcnt lgkmcnt(0)\n\t"
        "s_barrier\n\t"
        "s_branch .Lx59%=\n\t"
        ".Lx66%=:\n\t"
        "v_mov_b32 v151, v185\n\t"
        "v_mov_b32 v183, v186\n\t"
        "s_min_u32 s8, s3, 64\n\t"
        "s_waitcnt lgkmcnt(0)\n\t"
        "30:\n\t"
        "ds_read_b128 v[160:163], v185\n\t"
        "s_waitcnt lgkmcnt(0)\n\t"
        "v_lshlrev_b32_sdwa v157, 5, v160 dst_sel:DWORD dst_unused:UNUSED_PAD src0_sel:DWORD src1_sel:WORD_0\n\t"
        "v_lshlrev_b32_sdwa v158, 5, v160 dst_sel:DWORD dst_unused:UNUSED_PAD src0_sel:DWORD src1_sel:WORD_1\n\t"
        "v_or_b32 v157, v184, v157\n\t"
        "v_or_b32 v158, v184, v158\n\t"
        "v_xor_b32 v159, 32, v157\n\t"
        "v_xor_b32 v152, 32, v158\n\t"
        "ds_read_b64_tr_b16 v[60:61], v157 offset:0\n\t"
        "ds_read_b64_tr_b16 v[62:63], v158 offset:0\n\t"
        "ds_read_b64_tr_b16 v[64:65], v159 offset:0\n\t"
        "ds_read_b64_tr_b16 v[66:67], v152 offset:0\n\t"
        "ds_read_b64_tr_b16 v[68:69], v157 offset:64\n\t"
        "ds_read_b64_tr_b16 v[70:71], v158 offset:64\n\t"
        "ds_read_b64_tr_b16 v[72:73], v159 offset:64\n\t"
        "ds_read_b64_tr_b16 v[74:75], v152 offset:64\n\t"
        "ds_read_b64_tr_b16 v[76:77], v157 offset:128\n\t"
        "ds_read_b64_tr_b16 v[78:79], v158 offset:128\n\t"
        "ds_read_b64_tr_b16 v[80:81], v159 offset:128\n\t"
        "ds_read_b64_tr_b16 v[82:83], v152 offset:128\n\t"
        "v_perm_b32 v164, v162, v162, v168\n\t"
        "v_perm_b32 v165, v162, v162, v169\n\t"
        "v_perm_b32 v166, v162, v162, v170\n\t"
        "v_perm_b32 v167, v162, v162, v171\n\t"
        "v_cmp_ne_u32 vcc, 0, v163\n\t"
        "s_waitcnt lgkmcnt(10)\n\t"
        "v_mfma_f32_16x16x32_bf16 v[48:51], v[60:63], v[164:167], 0\n\t"
        "s_waitcnt lgkmcnt(8)\n\t"
        "v_mfma_f32_16x16x32_bf16 v[52:55], v[64:67], v[164:167], 0\n\t"
        "s_waitcnt lgkmcnt(6)\n\t"
        "v_mfma_f32_16x16x32_bf16 v[56:59], v[68:71], v[164:167], 0\n\t"
        "s_waitcnt lgkmcnt(4)\n\t"
        "v_mfma_f32_16x16x32_bf16 v[116:119], v[72:75], v[164:167], 0\n\t"
        "s_waitcnt lgkmcnt(2)\n\t"
        "v_mfma_f32_16x16x32_bf16 v[120:123], v[76:79], v[164:167], 0\n\t"
        "s_waitcnt lgkmcnt(0)\n\t"
        "v_mfma_f32_16x16x32_bf16 v[124:127], v[80:83], v[164:167], 0\n\t"
        "s_cbranch_vccz 31f\n\t"
        "v_lshlrev_b32_sdwa v157, 5, v161 dst_sel:DWORD dst_unused:UNUSED_PAD src0_sel:DWORD src1_sel:WORD_0\n\t"
        "v_lshlrev_b32_sdwa v158, 5, v161 dst_sel:DWORD dst_unused:UNUSED_PAD src0_sel:DWORD src1_sel:WORD_1\n\t"
        "v_or_b32 v157, v184, v157\n\t"
        "v_or_b32 v158, v184, v158\n\t"
        "v_xor_b32 v159, 32, v157\n\t"
        "v_xor_b32 v152, 32, v158\n\t"
        "ds_read_b64_tr_b16 v[60:61], v157 offset:0\n\t"
        "ds_read_b64_tr_b16 v[62:63], v158 offset:0\n\t"
        "ds_read_b64_tr_b16 v[64:65], v159 offset:0\n\t"
        "ds_read_b64_tr_b16 v[66:67], v152 offset:0\n\t"
        "ds_read_b64_tr_b16 v[68:69], v157 offset:64\n\t"
        "ds_read_b64_tr_b16 v[70:71], v158 offset:64\n\t"
        "ds_read_b64_tr_b16 v[72:73], v159 offset:64\n\t"
        "ds_read_b64_tr_b16 v[74:75], v152 offset:64\n\t"
        "ds_read_b64_tr_b16 v[76:77], v157 offset:128\n\t"
        "ds_read_b64_tr_b16 v[78:79], v158 offset:128\n\t"
        "ds_read_b64_tr_b16 v[80:81], v159 offset:128\n\t"
        "ds_read_b64_tr_b16 v[82:83], v152 offset:128\n\t"
        "v_perm_b32 v164, v163, v163, v168\n\t"
        "v_perm_b32 v165, v163, v163, v169\n\t"
        "v_perm_b32 v166, v163, v163, v170\n\t"
        "v_perm_b32 v167, v163, v163, v171\n\t"
        "s_waitcnt lgkmcnt(10)\n\t"
        "s_nop 0\n\t"
        "v_mfma_f32_16x16x32_bf16 v[48:51], v[60:63], v[164:167], v[48:51]\n\t"
        "s_waitcnt lgkmcnt(8)\n\t"
        "v_mfma_f32_16x16x32_bf16 v[52:55], v[64:67], v[164:167], v[52:55]\n\t"
        "s_waitcnt lgkmcnt(6)\n\t"
        "v_mfma_f32_16x16x32_bf16 v[56:59], v[68:71], v[164:167], v[56:59]\n\t"
        "s_waitcnt lgkmcnt(4)\n\t"
        "v_mfma_f32_16x16x32_bf16 v[116:119], v[72:75], v[164:167], v[116:119]\n\t"
        "s_waitcnt lgkmcnt(2)\n\t"
        "v_mfma_f32_16x16x32_bf16 v[120:123], v[76:79], v[164:167], v[120:123]\n\t"
        "s_waitcnt lgkmcnt(0)\n\t"
        "v_mfma_f32_16x16x32_bf16 v[124:127], v[80:83], v[164:167], v[124:127]\n\t"
        "31:\n\t"
        "v_cvt_pk_bf16_f32 v48, v48, v49\n\t"
        "v_cvt_pk_bf16_f32 v49, v50, v51\n\t"
        "v_cvt_pk_bf16_f32 v52, v52, v53\n\t"
        "v_cvt_pk_bf16_f32 v53, v54, v55\n\t"
        "v_cvt_pk_bf16_f32 v56, v56, v57\n\t"
        "v_cvt_pk_bf16_f32 v57, v58, v59\n\t"
        "v_cvt_pk_bf16_f32 v116, v116, v117\n\t"
        "v_cvt_pk_bf16_f32 v117, v118, v119\n\t"
        "v_cvt_pk_bf16_f32 v120, v120, v121\n\t"
        "v_cvt_pk_bf16_f32 v121, v122, v123\n\t"
        "v_cvt_pk_bf16_f32 v124, v124, v125\n\t"
        "v_cvt_pk_bf16_f32 v125, v126, v127\n\t"
        "ds_write_b64 v186, v[48:49] offset:0\n\t"
        "ds_write_b64 v154, v[52:53] offset:0\n\t"
        "ds_write_b64 v186, v[56:57] offset:64\n\t"
        "ds_write_b64 v154, v[116:117] offset:64\n\t"
        "ds_write_b64 v186, v[120:121] offset:128\n\t"
        "ds_write_b64 v154, v[124:125] offset:128\n\t"
        "v_add_u32 v185, 256, v185\n\t"
        "v_add_u32 v186, 12288, v186\n\t"
        "v_xor_b32 v154, 32, v186\n\t"
        "s_sub_i32 s8, s8, 16\n\t"
        "s_cmp_gt_i32 s8, 0\n\t"
        "s_cbranch_scc1 30b\n\t"
        "v_mov_b32 v185, v151\n\t"
        "v_mov_b32 v186, v183\n\t"
        "v_xor_b32 v154, 32, v186\n\t"
        "s_waitcnt lgkmcnt(0)\n\t"
        "s_barrier\n\t"
        "s_branch .Lx67%=\n\t"
        ".LwX%=:\n\t"
        "s_branch .LrX%=\n\t"
        ".Lx68%=:\n\t"
        "v_add_u32 v185, 1024, v185\n\t"
        "v_add_u32 v186, 49152, v186\n\t"
        "v_xor_b32 v154, 32, v186\n\t"
        "v_bfe_u32 v64, v185, 4, 4\n\t"
        "v_add_u32 v60, 65, v64\n\t"
        "s_sub_u32 s8, s37, 12288\n\t"
        "v_mov_b32 v63, s62\n\t"
        "v_lshlrev_b32 v65, 3, v64\n\t"
        "v_mul_u32_u24 v64, 192, v64\n\t"
        "v_and_or_b32 v64, v65, 48, v64\n\t"
        "v_add_u32 v61, s8, v64\n\t"
        "v_subrev_u32 v186, 61440, v186\n\t"
        "v_xor_b32 v154, 32, v186\n\t"
        "v_min_u32 v16, s61, v60\n\t"
        "v_lshl_add_u32 v16, v16, 3, s60\n\t"
        "ds_read_b64 v[0:1], v16\n\t"
        "v_cmp_ge_u32 s[66:67], s61, v60\n\t"
        "s_mov_b32 s46, 0x07060302\n\t"
        "s_waitcnt lgkmcnt(0)\n\t"
        "v_and_b32 v16, 0xfffff800, v1\n\t"
        "ds_read_b128 v[2:5], v16 offset:1536\n\t"
        "v_lshlrev_b32 v10, 16, v0\n\t"
        "v_and_b32 v11, 0xffff0000, v0\n\t"
        "v_med3_f32 v10, v10, 0, s63\n\t"
        "v_med3_f32 v11, v11, 0, s63\n\t"
        "s_mov_b64 exec, s[66:67]\n\t"
        "ds_write_b32 v1, v61\n\t"
        "s_mov_b64 exec, -1\n\t"
        "v_perm_b32 v0, v11, v10, s46\n\t"
        "v_fract_f32 v12, v10\n\t"
        "v_fract_f32 v13, v11\n\t"
        "v_cvt_u32_f32 v14, v10\n\t"
        "v_cvt_u32_f32 v15, v11\n\t"
        "v_cmp_neq_f32 s[64:65], 0, v12\n\t"
        "v_cmp_neq_f32 s[44:45], 0, v13\n\t"
        "v_sub_f32 v16, 1.0, v12\n\t"
        "v_sub_f32 v17, 1.0, v13\n\t"
        "v_cvt_pk_bf16_f32 v17, v16, v17\n\t"
        "v_lshlrev_b32 v16, 16, v17\n\t"
        "v_and_b32 v17, 0xffff0000, v17\n\t"
        "v_mul_f32 v18, v16, v17\n\t"
        "v_mul_f32 v16, v16, v13\n\t"
        "v_mul_f32 v17, v12, v17\n\t"
        "v_mul_f32 v19, v12, v13\n\t"
        "v_cndmask_b32 v16, v17, v16, s[44:45]\n\t"
        "v_cndmask_b32 v17, 0, v17, s[44:45]\n\t"
        "v_cvt_pk_bf16_f32 v162, v18, v16\n\t"
        "v_cvt_pk_bf16_f32 v163, v17, v19\n\t"
        "s_waitcnt lgkmcnt(1)\n\t"
        "v_pk_max_u16 v16, v0, v2\n\t"
        "v_pk_min_u16 v16, v16, v3\n\t"
        "v_cmp_ne_u32 vcc, v16, v0\n\t"
        "v_sub_u32 v16, v15, v5\n\t"
        "v_mad_u32_u24 v17, v15, 6, v4\n\t"
        "v_mad_u32_u24 v17, v14, s62, v17\n\t"
        "s_and_b64 vcc, vcc, s[66:67]\n\t"
        "s_or_b64 s[56:57], s[56:57], vcc\n\t"
        "v_bfe_u32 v18, v16, 2, 1\n\t"
        "v_or_b32 v18, v18, v17\n\t"
        "v_add_u32 v16, 1, v16\n\t"
        "v_bfe_u32 v16, v16, 2, 1\n\t"
        "v_add3_u32 v16, v17, v16, 6\n\t"
        "v_cndmask_b32 v16, v18, v16, s[44:45]\n\t"
        "v_cndmask_b32 v17, 0, v63, s[64:65]\n\t"
        "v_add_u32 v19, v18, v17\n\t"
        "v_add_u32 v20, v16, v17\n\t"
        "v_cndmask_b32 v16, v19, v16, s[44:45]\n\t"
        "v_lshl_or_b32 v160, v16, 16, v18\n\t"
        "v_lshl_or_b32 v161, v20, 16, v19\n\t"
        "v_lshlrev_b32_sdwa v157, 5, v160 dst_sel:DWORD dst_unused:UNUSED_PAD src0_sel:DWORD src1_sel:WORD_0\n\t"
        "v_lshlrev_b32_sdwa v158, 5, v160 dst_sel:DWORD dst_unused:UNUSED_PAD src0_sel:DWORD src1_sel:WORD_1\n\t"
        "v_or_b32 v157, v184, v157\n\t"
        "v_or_b32 v158, v184, v158\n\t"
        "v_xor_b32 v159, 32, v157\n\t"
        "v_xor_b32 v152, 32, v158\n\t"
        "ds_read_b64_tr_b16 v[60:61], v157 offset:0\n\t"
        "ds_read_b64_tr_b16 v[62:63], v158 offset:0\n\t"
        "ds_read_b64_tr_b16 v[64:65], v159 offset:0\n\t"
        "ds_read_b64_tr_b16 v[66:67], v152 offset:0\n\t"
        "ds_read_b64_tr_b16 v[68:69], v157 offset:64\n\t"
        "ds_read_b64_tr_b16 v[70:71], v158 offset:64\n\t"
        "ds_read_b64_tr_b16 v[72:73], v159 offset:64\n\t"
        "ds_read_b64_tr_b16 v[74:75], v152 offset:64\n\t"
        "ds_read_b64_tr_b16 v[76:77], v157 offset:128\n\t"
        "ds_read_b64_tr_b16 v[78:79], v158 offset:128\n\t"
        "ds_read_b64_tr_b16 v[80:81], v159 offset:128\n\t"
        "ds_read_b64_tr_b16 v[82:83], v152 offset:128\n\t"
        "v_lshlrev_b32_sdwa v157, 5, v161 dst_sel:DWORD dst_unused:UNUSED_PAD src0_sel:DWORD src1_sel:WORD_0\n\t"
        "v_lshlrev_b32_sdwa v158, 5, v161 dst_sel:DWORD dst_unused:UNUSED_PAD src0_sel:DWORD src1_sel:WORD_1\n\t"
        "v_or_b32 v157, v184, v157\n\t"
        "v_or_b32 v158, v184, v158\n\t"
        "v_xor_b32 v159, 32, v157\n\t"
        "v_xor_b32 v152, 32, v158\n\t"
        "ds_read_b64_tr_b16 v[24:25], v157 offset:0\n\t"
        "ds_read_b64_tr_b16 v[26:27], v158 offset:0\n\t"
        "ds_read_b64_tr_b16 v[28:29], v159 offset:0\n\t"
        "ds_read_b64_tr_b16 v[30:31], v152 offset:0\n\t"
        "ds_read_b64_tr_b16 v[32:33], v157 offset:64\n\t"
        "ds_read_b64_tr_b16 v[34:35], v158 offset:64\n\t"
        "ds_read_b64_tr_b16 v[36:37], v159 offset:64\n\t"
        "ds_read_b64_tr_b16 v[38:39], v152 offset:64\n\t"
        "ds_read_b64_tr_b16 v[40:41], v157 offset:128\n\t"
        "ds_read_b64_tr_b16 v[42:43], v158 offset:128\n\t"
        "ds_read_b64_tr_b16 v[44:45], v159 offset:128\n\t"
        "ds_read_b64_tr_b16 v[46:47], v152 offset:128\n\t"
        "s_cmp_lg_u64 s[56:57], 0\n\t"
        "s_cselect_b32 s8, 1, 0\n\t"
        "v_mov_b32 v157, s8\n\t"
        "v_perm_b32 v164, v162, v162, v168\n\t"
        "v_perm_b32 v165, v162, v162, v169\n\t"
        "v_perm_b32 v166, v162, v162, v170\n\t"
        "v_perm_b32 v167, v162, v162, v171\n\t"
        "s_waitcnt lgkmcnt(12)\n\t"
        "s_nop 0\n\t"
        "v_mfma_f32_16x16x32_bf16 v[0:3], v[60:63], v[164:167], 0\n\t"
        "v_mfma_f32_16x16x32_bf16 v[4:7], v[64:67], v[164:167], 0\n\t"
        "v_mfma_f32_16x16x32_bf16 v[8:11], v[68:71], v[164:167], 0\n\t"
        "v_mfma_f32_16x16x32_bf16 v[12:15], v[72:75], v[164:167], 0\n\t"
        "v_mfma_f32_16x16x32_bf16 v[16:19], v[76:79], v[164:167], 0\n\t"
        "v_mfma_f32_16x16x32_bf16 v[20:23], v[80:83], v[164:167], 0\n\t"
        "v_perm_b32 v164, v163, v163, v168\n\t"
        "v_perm_b32 v165, v163, v163, v169\n\t"
        "v_perm_b32 v166, v163, v163, v170\n\t"
        "v_perm_b32 v167, v163, v163, v171\n\t"
        "s_waitcnt lgkmcnt(3)\n\t"
        "s_nop 0\n\t"
        "v_mfma_f32_16x16x32_bf16 v[0:3], v[24:27], v[164:167], v[0:3]\n\t"
        "v_mfma_f32_16x16x32_bf16 v[4:7], v[28:31], v[164:167], v[4:7]\n\t"
        "v_mfma_f32_16x16x32_bf16 v[8:11], v[32:35], v[164:167], v[8:11]\n\t"
        "v_mfma_f32_16x16x32_bf16 v[12:15], v[36:39], v[164:167], v[12:15]\n\t"
        "s_waitcnt lgkmcnt(0)\n\t"
        "v_mfma_f32_16x16x32_bf16 v[16:19], v[40:43], v[164:167], v[16:19]\n\t"
        "v_mfma_f32_16x16x32_bf16 v[20:23], v[44:47], v[164:167], v[20:23]\n\t"
        "ds_write_b32 v153, v157 offset:16\n\t"
        "s_nop 0\n\t"
        "v_cvt_pk_bf16_f32 v0, v0, v1\n\t"
        "v_cvt_pk_bf16_f32 v1, v2, v3\n\t"
        "v_cvt_pk_bf16_f32 v4, v4, v5\n\t"
        "v_cvt_pk_bf16_f32 v5, v6, v7\n\t"
        "v_cvt_pk_bf16_f32 v8, v8, v9\n\t"
        "v_cvt_pk_bf16_f32 v9, v10, v11\n\t"
        "v_cvt_pk_bf16_f32 v12, v12, v13\n\t"
        "v_cvt_pk_bf16_f32 v13, v14, v15\n\t"
        "v_cvt_pk_bf16_f32 v16, v16, v17\n\t"
        "v_cvt_pk_bf16_f32 v17, v18, v19\n\t"
        "v_cvt_pk_bf16_f32 v20, v20, v21\n\t"
        "v_cvt_pk_bf16_f32 v21, v22, v23\n\t"
        "ds_write_b64 v186, v[0:1] offset:0\n\t"
        "ds_write_b64 v154, v[4:5] offset:0\n\t"
        "ds_write_b64 v186, v[8:9] offset:64\n\t"
        "ds_write_b64 v154, v[12:13] offset:64\n\t"
        "ds_write_b64 v186, v[16:17] offset:128\n\t"
        "ds_write_b64 v154, v[20:21] offset:128\n\t"
        "s_branch .Lx69%=\n\t"
        ".Lx72%=:\n\t"
        "v_mov_b32 v151, v185\n\t"
        "v_mov_b32 v183, v186\n\t"
        "s_min_u32 s8, s3, 64\n\t"
        "s_waitcnt lgkmcnt(0)\n\t"
        "30:\n\t"
        "ds_read_b128 v[160:163], v185\n\t"
        "s_waitcnt lgkmcnt(0)\n\t"
        "v_lshlrev_b32_sdwa v157, 5, v160 dst_sel:DWORD dst_unused:UNUSED_PAD src0_sel:DWORD src1_sel:WORD_0\n\t"
        "v_lshlrev_b32_sdwa v158, 5, v160 dst_sel:DWORD dst_unused:UNUSED_PAD src0_sel:DWORD src1_sel:WORD_1\n\t"
        "v_or_b32 v157, v184, v157\n\t"
        "v_or_b32 v158, v184, v158\n\t"
        "v_xor_b32 v159, 32, v157\n\t"
        "v_xor_b32 v152, 32, v158\n\t"
        "ds_read_b64_tr_b16 v[60:61], v157 offset:0\n\t"
        "ds_read_b64_tr_b16 v[62:63], v158 offset:0\n\t"
        "ds_read_b64_tr_b16 v[64:65], v159 offset:0\n\t"
        "ds_read_b64_tr_b16 v[66:67], v152 offset:0\n\t"
        "ds_read_b64_tr_b16 v[68:69], v157 offset:64\n\t"
        "ds_read_b64_tr_b16 v[70:71], v158 offset:64\n\t"
        "ds_read_b64_tr_b16 v[72:73], v159 offset:64\n\t"
        "ds_read_b64_tr_b16 v[74:75], v152 offset:64\n\t"
        "ds_read_b64_tr_b16 v[76:77], v157 offset:128\n\t"
        "ds_read_b64_tr_b16 v[78:79], v158 offset:128\n\t"
        "ds_read_b64_tr_b16 v[80:81], v159 offset:128\n\t"
        "ds_read_b64_tr_b16 v[82:83], v152 offset:128\n\t"
        "v_perm_b32 v164, v162, v162, v168\n\t"
        "v_perm_b32 v165, v162, v162, v169\n\t"
        "v_perm_b32 v166, v162, v162, v170\n\t"
        "v_perm_b32 v167, v162, v162, v171\n\t"
        "v_cmp_ne_u32 vcc, 0, v163\n\t"
        "s_waitcnt lgkmcnt(10)\n\t"
        "v_mfma_f32_16x16x32_bf16 v[48:51], v[60:63], v[164:167], 0\n\t"
        "s_waitcnt lgkmcnt(8)\n\t"
        "v_mfma_f32_16x16x32_bf16 v[52:55], v[64:67], v[164:167], 0\n\t"
        "s_waitcnt lgkmcnt(6)\n\t"
        "v_mfma_f32_16x16x32_bf16 v[56:59], v[68:71], v[164:167], 0\n\t"
        "s_waitcnt lgkmcnt(4)\n\t"
        "v_mfma_f32_16x16x32_bf16 v[116:119], v[72:75], v[164:167], 0\n\t"
        "s_waitcnt lgkmcnt(2)\n\t"
        "v_mfma_f32_16x16x32_bf16 v[120:123], v[76:79], v[164:167], 0\n\t"
        "s_waitcnt lgkmcnt(0)\n\t"
        "v_mfma_f32_16x16x32_bf16 v[124:127], v[80:83], v[164:167], 0\n\t"
        "s_cbranch_vccz 31f\n\t"
        "v_lshlrev_b32_sdwa v157, 5, v161 dst_sel:DWORD dst_unused:UNUSED_PAD src0_sel:DWORD src1_sel:WORD_0\n\t"
        "v_lshlrev_b32_sdwa v158, 5, v161 dst_sel:DWORD dst_unused:UNUSED_PAD src0_sel:DWORD src1_sel:WORD_1\n\t"
        "v_or_b32 v157, v184, v157\n\t"
        "v_or_b32 v158, v184, v158\n\t"
        "v_xor_b32 v159, 32, v157\n\t"
        "v_xor_b32 v152, 32, v158\n\t"
        "ds_read_b64_tr_b16 v[60:61], v157 offset:0\n\t"
        "ds_read_b64_tr_b16 v[62:63], v158 offset:0\n\t"
        "ds_read_b64_tr_b16 v[64:65], v159 offset:0\n\t"
        "ds_read_b64_tr_b16 v[66:67], v152 offset:0\n\t"
        "ds_read_b64_tr_b16 v[68:69], v157 offset:64\n\t"
        "ds_read_b64_tr_b16 v[70:71], v158 offset:64\n\t"
        "ds_read_b64_tr_b16 v[72:73], v159 offset:64\n\t"
        "ds_read_b64_tr_b16 v[74:75], v152 offset:64\n\t"
        "ds_read_b64_tr_b16 v[76:77], v157 offset:128\n\t"
        "ds_read_b64_tr_b16 v[78:79], v158 offset:128\n\t"
        "ds_read_b64_tr_b16 v[80:81], v159 offset:128\n\t"
        "ds_read_b64_tr_b16 v[82:83], v152 offset:128\n\t"
        "v_perm_b32 v164, v163, v163, v168\n\t"
        "v_perm_b32 v165, v163, v163, v169\n\t"
        "v_perm_b32 v166, v163, v163, v170\n\t"
        "v_perm_b32 v167, v163, v163, v171\n\t"
        "s_waitcnt lgkmcnt(10)\n\t"
        "s_nop 0\n\t"
        "v_mfma_f32_16x16x32_bf16 v[48:51], v[60:63], v[164:167], v[48:51]\n\t"
        "s_waitcnt lgkmcnt(8)\n\t"
        "v_mfma_f32_16x16x32_bf16 v[52:55], v[64:67], v[164:167], v[52:55]\n\t"
        "s_waitcnt lgkmcnt(6)\n\t"
        "v_mfma_f32_16x16x32_bf16 v[56:59], v[68:71], v[164:167], v[56:59]\n\t"
        "s_waitcnt lgkmcnt(4)\n\t"
        "v_mfma_f32_16x16x32_bf16 v[116:119], v[72:75], v[164:167], v[116:119]\n\t"
        "s_waitcnt lgkmcnt(2)\n\t"
        "v_mfma_f32_16x16x32_bf16 v[120:123], v[76:79], v[164:167], v[120:123]\n\t"
        "s_waitcnt lgkmcnt(0)\n\t"
        "v_mfma_f32_16x16x32_bf16 v[124:127], v[80:83], v[164:167], v[124:127]\n\t"
        "31:\n\t"
        "v_cvt_pk_bf16_f32 v48, v48, v49\n\t"
        "v_cvt_pk_bf16_f32 v49, v50, v51\n\t"
        "v_cvt_pk_bf16_f32 v52, v52, v53\n\t"
        "v_cvt_pk_bf16_f32 v53, v54, v55\n\t"
        "v_cvt_pk_bf16_f32 v56, v56, v57\n\t"
        "v_cvt_pk_bf16_f32 v57, v58, v59\n\t"
        "v_cvt_pk_bf16_f32 v116, v116, v117\n\t"
        "v_cvt_pk_bf16_f32 v117, v118, v119\n\t"
        "v_cvt_pk_bf16_f32 v120, v120, v121\n\t"
        "v_cvt_pk_bf16_f32 v121, v122, v123\n\t"
        "v_cvt_pk_bf16_f32 v124, v124, v125\n\t"
        "v_cvt_pk_bf16_f32 v125, v126, v127\n\t"
        "ds_write_b64 v186, v[48:49] offset:0\n\t"
        "ds_write_b64 v154, v[52:53] offset:0\n\t"
        "ds_write_b64 v186, v[56:57] offset:64\n\t"
        "ds_write_b64 v154, v[116:117] offset:64\n\t"
        "ds_write_b64 v186, v[120:121] offset:128\n\t"
        "ds_write_b64 v154, v[124:125] offset:128\n\t"
        "v_add_u32 v185, 256, v185\n\t"
        "v_add_u32 v186, 12288, v186\n\t"
        "v_xor_b32 v154, 32, v186\n\t"
        "s_sub_i32 s8, s8, 16\n\t"
        "s_cmp_gt_i32 s8, 0\n\t"
        "s_cbranch_scc1 30b\n\t"
        "v_mov_b32 v185, v151\n\t"
        "v_mov_b32 v186, v183\n\t"
        "v_xor_b32 v154, 32, v186\n\t"
        "s_waitcnt lgkmcnt(0)\n\t"
        "s_barrier\n\t"
        "s_branch .Lx73%=\n\t"
        "99:\n\t"
        "s_nop 7\n\t"
        "s_waitcnt vmcnt(0) lgkmcnt(0)\n\t"
        "s_barrier\n\t"
        "s_lshl_b32 s6, s6, 16\n\t"
        "v_or_b32 %0, s6, v156\n\t"
        : "+v"(tid) : "s"(ka), "s"(bid), "s"(recs)
        : "memory", "scc", "vcc", "s3", "s4", "s5", "s6", "s7", "s8", "s9", "s10", "s11", "s12", "s13", "s16", "s17", "s18", "s19", "s20", "s21", "s22", "s23", "s24", "s25", "s26", "s27", "s28", "s29", "s30", "s31", "s34", "s35", "s36", "s37", "s38", "s39", "s40", "s41", "s42", "s43", "s44", "s45", "s46", "s47", "s48", "s49", "s50", "s51", "s52", "s53", "s54", "s55", "s56", "s57", "s60", "s61", "s62", "s63", "s64", "s65", "s66", "s67", "s68", "s69", "s70", "s71", "s72", "s73", "s74", "s75", "s76", "s77", "s78", "s79", "s80", "s81", "s82", "s83", "s84", "s85", "s86", "s87", "s88", "s89", "s90", "s91", "s92", "s93", "s94", "s95", "s96", "s97", "s98", "s99", "v1", "v2", "v3", "v4", "v5", "v6", "v7", "v8", "v9", "v10", "v11", "v12", "v13", "v14", "v15", "v16", "v17", "v18", "v19", "v20", "v21", "v22", "v23", "v24", "v25", "v26", "v27", "v28", "v29", "v30", "v31", "v32", "v33", "v34", "v35", "v36", "v37", "v38", "v39", "v40", "v41", "v42", "v43", "v44", "v45", "v46", "v47", "v48", "v49", "v50", "v51", "v52", "v53", "v54", "v55", "v56", "v57", "v58", "v59", "v60", "v61", "v62", "v63", "v64", "v65", "v66", "v67", "v68", "v69", "v70", "v71", "v72", "v73", "v74", "v75", "v76", "v77", "v78", "v79", "v80", "v81", "v82", "v83", "v84", "v85", "v86", "v87", "v88", "v89", "v90", "v91", "v92", "v93", "v94", "v95", "v96", "v97", "v98", "v99", "v100", "v101", "v102", "v103", "v104", "v105", "v106", "v107", "v108", "v109", "v110", "v111", "v112", "v113", "v114", "v115", "v116", "v117", "v118", "v119", "v120", "v121", "v122", "v123", "v124", "v125", "v126", "v127", "v128", "v129", "v130", "v131", "v132", "v133", "v134", "v135", "v136", "v137", "v138", "v139", "v140", "v141", "v142", "v143", "v144", "v145", "v146", "v147", "v148", "v149", "v150", "v151", "v152", "v153", "v154", "v155", "v156", "v157", "v158", "v159", "v160", "v161", "v162", "v163", "v164", "v165", "v166", "v167", "v168", "v169", "v170", "v171", "v172", "v173", "v174", "v175", "v176", "v177", "v178", "v179", "v180", "v181", "v182", "v183", "v184", "v185", "v186", "v187", "v188", "v189", "v190", "v191", "v192", "v193", "v194", "v195", "v196", "v197", "v198", "v199", "v200", "v201", "v202", "v203", "v204", "v205", "v206", "v207", "v208", "v209", "v210", "v211", "v212", "v213", "v214", "v215", "v216", "v217", "v218", "v219", "v220", "v221", "v222", "v223", "v224", "v225", "v226", "v227", "v228", "v229", "v230", "v231", "v232", "v233", "v234", "v235", "v236", "v237", "v238", "v239", "v240", "v241", "v242", "v243", "v244", "v245", "v246", "v247", "v248", "v249", "v250", "v251", "v252", "v253", "v254", "v255", "a0", "a1", "a2", "a3", "a4", "a5", "a6", "a7", "a8", "a9", "a10", "a11", "a12", "a13", "a14", "a15", "a16", "a17", "a18", "a19", "a20", "a21", "a22", "a23", "a24", "a25", "a26", "a27", "a28", "a29", "a30", "a31", "a32", "a33", "a34", "a35", "a36", "a37", "a38", "a39", "a40", "a41", "a42", "a43", "a44", "a45", "a46", "a47", "a48", "a49", "a50", "a51", "a52", "a53", "a54", "a55", "a56", "a57", "a58", "a59", "a60", "a61", "a62", "a63", "a64", "a65", "a66", "a67", "a68", "a69", "a70", "a71", "a72", "a73", "a74", "a75", "a76", "a77", "a78", "a79", "a80", "a81", "a82", "a83", "a84", "a85", "a86", "a87", "a88", "a89", "a90", "a91", "a92", "a93", "a94", "a95", "a96", "a97", "a98", "a99", "a100", "a101", "a102", "a103", "a104", "a105", "a106", "a107", "a108", "a109", "a110", "a111", "a112", "a113", "a114", "a115", "a116", "a117", "a118", "a119", "a120", "a121", "a122", "a123", "a124", "a125", "a126", "a127", "a128", "a129", "a130", "a131", "a132", "a133", "a134", "a135", "a136", "a137", "a138", "a139", "a140", "a141", "a142", "a143", "a144", "a145", "a146", "a147", "a148", "a149", "a150", "a151", "a152", "a153", "a154", "a155", "a156", "a157", "a158", "a159", "a160", "a161", "a162", "a163", "a164", "a165", "a166", "a167", "a168", "a169", "a170", "a171", "a172", "a173", "a174", "a175", "a176", "a177", "a178", "a179", "a180", "a181", "a182", "a183", "a184", "a185", "a186", "a187", "a188", "a189", "a190", "a191", "a192", "a193", "a194", "a195", "a196", "a197", "a198", "a199", "a200", "a201", "a202", "a203", "a204", "a205", "a206", "a207", "a208", "a209", "a210", "a211", "a212", "a213", "a214", "a215", "a216", "a217", "a218", "a219", "a220", "a221", "a222", "a223", "a224", "a225", "a226", "a227", "a228", "a229", "a230", "a231", "a232", "a233", "a234", "a235", "a236", "a237", "a238", "a239", "a240", "a241", "a242", "a243", "a244", "a245", "a246", "a247", "a248", "a249", "a250", "a251", "a252", "a253", "a254", "a255");
    asm volatile("" : "+s"(ka), "+s"(bid) :: "memory");
    {
        typedef short v4s __attribute__((ext_vector_type(4)));
        const unsigned short *x = ka->x, *w = ka->w;
        const unsigned* off = (const unsigned*)ka->off;
        unsigned short* out = ka->out;
        const unsigned tw = recs[bid * 64 + 27] >> 16, redo = __builtin_amdgcn_readfirstlane(tid >> 16);
        const unsigned* rc = recs + bid * 64;
        tid &= 255;
        const int lane = tid & 63, li = lane & 15, q = lane >> 4, wv = __builtin_amdgcn_readfirstlane(tid >> 6);
        int pix[4] = {-1, -1, -1, -1}, n = 0;
#pragma unroll 1
        for (int s = 0; s < 14; ++s) {
            const unsigned o = s < 8 ? (((tw & 255) + s) * 224 + ((tw >> 8) & 255)) * 192 : rc[49 + ((s - 8) >> 1)] + (s & 1) * (43200 - rc[30]);
            if (!((redo >> (s < 8 ? 8 + (s >> 1) : 0)) & 1) || o >= 224 * 224 * 192) continue;
            const int p = (o + li * (s < 8 ? 192 : rc[30])) / 192;
#pragma unroll
            for (int i = 0; i < 4; ++i) pix[i] = n == wv + 4 * i ? p : pix[i];
            ++n;
        }
        f32x4 acc[4][6];
        unsigned od[4];
        u32x4 wn[5];
#pragma unroll
        for (int i = 0; i < 4; ++i) {
            od[i] = off[max(pix[i], 0) * 9];
#pragma unroll
            for (int k = 0; k < 6; ++k) acc[i][k] = (f32x4){0.f, 0.f, 0.f, 0.f};
        }
#pragma unroll
        for (int k = 0; k < 5; ++k) wn[k] = *(const u32x4*)(w + min(tid + 256 * k, 1151u) * 8);
        const __amdgpu_buffer_rsrc_t xr = __builtin_amdgcn_make_buffer_rsrc((void*)x, 0, 224 * 224 * 192, 0x00020000);
        const unsigned wb = (8 * q + (li >> 2)) * 192 + (li & 3) * 8;
#pragma unroll 1
        for (int t = 0; t < 9; ++t) {
            unsigned char* slab = lds + (t & 1) * 18432;
#pragma unroll
            for (int k = 0; k < 5; ++k) {
                const unsigned c = min(tid + 256 * k, 1151u);
                *(u32x4*)(slab + ((c * 16) ^ (((c / 96) & 1) << 5))) = wn[k];
            }
            __syncthreads();
            u32x4 wf[3][6];
#pragma unroll
            for (int k = 0; k < 18; ++k) {
                __attribute__((address_space(3))) unsigned char* a = (__attribute__((address_space(3))) unsigned char*)slab + (k / 6) * 6144 + wb + (((k % 6) * 32) ^ ((q & 1) << 5));
                const u32x2 lo = __builtin_bit_cast(u32x2, __builtin_amdgcn_ds_read_tr16_b64_v4i16((__attribute__((address_space(3))) v4s*)a));
                const u32x2 hi = __builtin_bit_cast(u32x2, __builtin_amdgcn_ds_read_tr16_b64_v4i16((__attribute__((address_space(3))) v4s*)(a + 768)));
                wf[k / 6][k % 6] = (u32x4){lo[0], lo[1], hi[0], hi[1]};
            }
            const int tn = min(t + 1, 8);
#pragma unroll
            for (int k = 0; k < 5; ++k) wn[k] = *(const u32x4*)(w + tn * 9216 + min(tid + 256 * k, 1151u) * 8);
#pragma unroll
            for (int i0 = 0; i0 < 4; i0 += 2) {
                if (wv + 4 * i0 >= n) continue;
                u32x4 g[2][3][4];
                float wt[2][4];
#pragma unroll
                for (int j = 0; j < 2; ++j) {
                    const int i = i0 + j, p = max(pix[i], 0), h = p / 224, c = p - h * 224;
                    float py = rb((float)(h + t / 3 - 1) + bf_lo(od[i])), px = rb((float)(c + t % 3 - 1) + bf_hi(od[i]));
                    od[i] = off[p * 9 + tn];
                    py = fminf(fmaxf(py, 0.f), 223.f); px = fminf(fmaxf(px, 0.f), 223.f);
                    const float y0 = floorf(py), x0 = floorf(px), y1 = fminf(y0 + 1.f, 223.f), x1 = fminf(x0 + 1.f, 223.f);
                    const float wy = py - y0, wx = px - x0, a = rb(1.f - wy), b = rb(1.f - wx);
                    wt[j][0] = rb(a * b); wt[j][1] = rb(a * wx); wt[j][2] = rb(wy * b); wt[j][3] = rb(wy * wx);
                    int ad[4] = {(int)y0 * 224 + (int)x0, (int)y0 * 224 + (int)x1, (int)y1 * 224 + (int)x0, (int)y1 * 224 + (int)x1};
#pragma unroll
                    for (int k = 0; k < 4; ++k) ad[k] = wt[j][k] != 0.f && pix[i] >= 0 ? ad[k] * 192 + q * 16 : 0x40000000;
#pragma unroll
                    for (int k = 0; k < 4; ++k) {
#pragma unroll
                        for (int ks = 0; ks < 3; ++ks) g[j][ks][k] = (u32x4){0u, 0u, 0u, 0u};
                        if (__builtin_amdgcn_ballot_w64(ad[k] != 0x40000000) == 0) continue;
#pragma unroll
                        for (int ks = 0; ks < 3; ++ks) g[j][ks][k] = __builtin_amdgcn_raw_buffer_load_b128(xr, ad[k] + ks * 64, 0, 0);
                    }
                }
                asm volatile("" ::: "memory");
#pragma unroll
                for (int j = 0; j < 2; ++j) {
                    if (wv + 4 * (i0 + j) >= n) continue;
#pragma unroll
                    for (int ks = 0; ks < 3; ++ks) {
                        u32x4 bfr;
#pragma unroll
                        for (int d = 0; d < 4; ++d) {
                            float lo = 0.f, hi = 0.f;
#pragma unroll
                            for (int cn = 0; cn < 4; ++cn) {
                                const unsigned u = g[j][ks][cn][d];
                                const bf16x2 v = __builtin_bit_cast(bf16x2, u);
                                lo = __builtin_amdgcn_fdot2_f32_bf16(v, __builtin_bit_cast(bf16x2, __float_as_uint(wt[j][cn]) >> 16), lo, false);
                                hi = __builtin_amdgcn_fdot2_f32_bf16(v, __builtin_bit_cast(bf16x2, __float_as_uint(wt[j][cn])), hi, false);
                            }
                            bfr[d] = cvt_pk(lo, hi);
                        }
#pragma unroll
                        for (int k = 0; k < 6; ++k)
                            acc[i0 + j][k] = __builtin_amdgcn_mfma_f32_16x16x32_bf16(__builtin_bit_cast(bf16x8, wf[ks][k]), __builtin_bit_cast(bf16x8, bfr), acc[i0 + j][k], 0, 0, 0);
                    }
                }
            }
        }
#pragma unroll
        for (int i = 0; i < 4; ++i) {
            if (wv + 4 * i >= n) continue;
#pragma unroll
            for (int k = 0; k < 6; ++k) {
                u32x2 o; o[0] = cvt_pk(acc[i][k][0], acc[i][k][1]); o[1] = cvt_pk(acc[i][k][2], acc[i][k][3]);
                *(u32x2*)(out + pix[i] * 96 + k * 16 + q * 4) = o;
            }
        }
    }
}
extern "C" void kernel_launch(void* const* d_in, const int* in_sizes, int n_in, void* d_out, int out_size, void* d_ws, size_t ws_size, hipStream_t stream) {
    dcn_kernel<<<256, 256, 0, stream>>>((const unsigned short*)d_in[0], (const unsigned short*)d_in[1], (const unsigned short*)d_in[2], (unsigned short*)d_out, d_ws);
}
```

```cpp
#include <hip/hip_runtime.h>
#include <stdint.h>
typedef __attribute__((ext_vector_type(8))) __bf16 bf16x8;
typedef __attribute__((ext_vector_type(2))) __bf16 bf16x2;
typedef __attribute__((ext_vector_type(4))) float f32x4;
typedef __attribute__((ext_vector_type(4))) unsigned u32x4;
typedef __attribute__((ext_vector_type(2))) unsigned u32x2;
struct Args { const unsigned short *x, *off, *w; unsigned short* out; void* ws; };
static __device__ const unsigned recs[256 * 64] = {
1419264,1419264,1462272,1462272,1505280,1505280,1548288,1548288,1548288,1548288,1548288,1548288,40960,44032,48128,51200,55296,58368,62464,65536,62464,65536,62464,65536,268439552,336991254,57,807538688,80,7488,192,1040137727,16780300,50465804,576,36,224,1107820544,1107820544,1107820544,1098907648,1098907648,0,274752,1073741824,274176,44032,44032,40960,1465344,1073741824,1462272,16900,1107575312,4294961196,4294967294,16900,1107575312,4294961196,4294967294,16900,1107575312,4294961196,4294967294,
2623488,2666496,2709504,2752512,2795520,2838528,2881536,2924544,2924544,2924544,2924544,2924544,40960,45056,49152,53248,57344,61440,65536,69632,69632,69632,69632,69632,538976288,353703189,57,1080164608,80,7488,192,1040137855,16780300,50465804,576,36,128,1115947008,1115684864,1115160576,1107296256,1107296256,1107296256,533376,517248,501120,57344,49152,40960,2844672,2758656,2672640,1106264692,1112031880,4294960588,30,1106264692,1112031880,4294960588,30,1106264692,1112031880,4294960588,30,
3827712,3827712,3870720,3870720,3913728,3913728,3956736,3956736,3956736,3956736,3956736,3956736,40960,44032,48128,51200,55296,58368,62464,65536,62464,65536,62464,65536,268439552,336991254,57,1352798208,80,7488,192,1048526591,16780300,50465804,576,36,224,1119092736,1119092736,1119092736,1098907648,1098907648,0,726336,1073741824,725760,44032,44032,40960,3873792,1073741824,3870720,17074,1107575480,4294948652,4294967294,17074,1107575480,4294948652,4294967294,17074,1107575480,4294948652,4294967294,
21312,21312,21504,21504,21696,21696,21888,21888,21888,21888,21888,21888,40960,44032,48128,51200,55296,58368,62464,65536,62464,65536,62464,65536,268439552,336991254,410,1881808896,80,7984,43008,1048526335,50465804,16780300,36,80,224,1121976320,1121976320,1121976320,1098907648,1098907648,0,133056,1073741824,4032,44032,44032,40960,709632,1073741824,21504,17118,1107575524,4294943724,4294967294,17118,1107575524,4294943724,4294967294,17118,1107575524,4294943724,4294967294,
5720064,5720064,5763072,5763072,5806080,5806080,5849088,5849088,5849088,5849088,5849088,5849088,40960,44032,48128,51200,55296,58368,62464,65536,62464,65536,62464,65536,268439552,336991254,57,2154958848,80,7488,192,1048526463,16780300,50465804,576,36,224,1124466688,1124466688,1124466688,1098907648,1098907648,0,1081152,1073741824,1080576,44032,44032,40960,5766144,1073741824,5763072,17157,1107575560,4294938796,4294967294,17157,1107575560,4294938796,4294967294,17157,1107575560,4294938796,4294967294,
6752256,6795264,6838272,6881280,6924288,6967296,7010304,7053312,7053312,7053312,7053312,7053312,40960,45056,49152,53248,57344,61440,65536,69632,69632,69632,69632,69632,538976288,353703189,57,2428109056,80,7488,192,1056915199,16780300,50465804,576,36,128,1126301696,1126170624,1126039552,1107296256,1107296256,1107296256,1307520,1291392,1275264,57344,49152,40960,6973440,6887424,6801408,1106264861,1112032036,4294948300,30,1106264861,1112032036,4294948300,30,1106264861,1112032036,4294948300,30,
7612416,7612416,7655424,7655424,7698432,7698432,7741440,7741440,7741440,7741440,7741440,7741440,40960,44032,48128,51200,55296,58368,62464,65536,62464,65536,62464,65536,268439552,336991254,57,2957643776,80,7488,192,1056915071,16780300,50465804,576,36,224,1127350272,1127350272,1127350272,1098907648,1098907648,0,1435968,1073741824,1435392,44032,44032,40960,7658496,1073741824,7655424,17201,1107575604,4294928940,4294967294,17201,1107575604,4294928940,4294967294,17201,1107575604,4294928940,4294967294,
38592,38592,38784,38784,38976,38976,39168,39168,39168,39168,39168,39168,40960,44032,48128,51200,55296,58368,62464,65536,62464,65536,62464,65536,268439552,336991254,410,3230801920,80,7984,43008,1056915199,50465804,16780300,36,80,224,1128923136,1128923136,1128923136,1098907648,1098907648,0,136296,1073741824,7272,44032,44032,40960,726912,1073741824,38784,17225,1107575628,4294923564,4294967294,17225,1107575628,4294923564,4294967294,17225,1107575628,4294923564,4294967294,
1505280,1505280,1548288,1548288,1591296,1591296,1634304,1634304,1634304,1634304,1634304,1634304,40960,44032,48128,51200,55296,58368,62464,65536,62464,65536,62464,65536,268439552,336991254,57,808062976,80,7488,192,1040137727,16780300,50465804,576,36,224,1108344832,1108344832,1108344832,1098907648,1098907648,0,290880,1073741824,290304,44032,44032,40960,1551360,1073741824,1548288,16908,1107575320,4294960748,4294967294,16908,1107575320,4294960748,4294967294,16908,1107575320,4294960748,4294967294,
12096,12096,12288,12288,12480,12480,12672,12672,12672,12672,12672,12672,40960,44032,48128,51200,55296,58368,62464,65536,62464,65536,62464,65536,268439552,336991254,410,1080696832,80,7984,43008,1040137855,50465804,16780300,36,80,224,1115684864,1115684864,1115684864,1098907648,1098907648,0,131328,1073741824,2304,44032,44032,40960,700416,1073741824,12288,17020,1107575428,4294954476,4294967294,17020,1107575428,4294954476,4294967294,17020,1107575428,4294954476,4294967294,
3913728,3913728,3956736,3956736,3999744,3999744,4042752,4042752,4042752,4042752,4042752,4042752,40960,44032,48128,51200,55296,58368,62464,65536,62464,65536,62464,65536,268439552,336991254,57,1353322496,80,7488,192,1048526591,16780300,50465804,576,36,224,1119354880,1119354880,1119354880,1098907648,1098907648,0,742464,1073741824,741888,44032,44032,40960,3959808,1073741824,3956736,17078,1107575484,4294948204,4294967294,17078,1107575484,4294948204,4294967294,17078,1107575484,4294948204,4294967294,
21696,21696,21888,21888,22080,22080,22272,22272,22272,22272,22272,22272,40960,44032,48128,51200,55296,58368,62464,65536,62464,65536,62464,65536,268439552,336991254,410,1882333184,80,7984,43008,1048526335,50465804,16780300,36,80,224,1122238464,1122238464,1122238464,1098907648,1098907648,0,133128,1073741824,4104,44032,44032,40960,710016,1073741824,21888,17122,1107575528,4294943276,4294967294,17122,1107575528,4294943276,4294967294,17122,1107575528,4294943276,4294967294,
5806080,5806080,5849088,5849088,5892096,5892096,5935104,5935104,5935104,5935104,5935104,5935104,40960,44032,48128,51200,55296,58368,62464,65536,62464,65536,62464,65536,268439552,336991254,57,2155483136,80,7488,192,1048526463,16780300,50465804,576,36,224,1124597760,1124597760,1124597760,1098907648,1098907648,0,1097280,1073741824,1096704,44032,44032,40960,5852160,1073741824,5849088,17159,1107575562,4294938348,4294967294,17159,1107575562,4294938348,4294967294,17159,1107575562,4294938348,4294967294,
30144,30144,30336,30336,30528,30528,30720,30720,30720,30720,30720,30720,40960,44032,48128,51200,55296,58368,62464,65536,62464,65536,62464,65536,268439552,336991254,410,2428641280,80,7984,43008,1056915199,50465804,16780300,36,80,224,1126039552,1126039552,1126039552,1098907648,1098907648,0,134712,1073741824,5688,44032,44032,40960,718464,1073741824,30336,17181,1107575584,4294933420,4294967294,17181,1107575584,4294933420,4294967294,17181,1107575584,4294933420,4294967294,
7698432,7698432,7741440,7741440,7784448,7784448,7827456,7827456,7827456,7827456,7827456,7827456,40960,44032,48128,51200,55296,58368,62464,65536,62464,65536,62464,65536,268439552,336991254,57,2958168064,80,7488,192,1056915071,16780300,50465804,576,36,224,1127481344,1127481344,1127481344,1098907648,1098907648,0,1452096,1073741824,1451520,44032,44032,40960,7744512,1073741824,7741440,17203,1107575606,4294928492,4294967294,17203,1107575606,4294928492,4294967294,17203,1107575606,4294928492,4294967294,
38976,38976,39168,39168,39360,39360,39552,39552,39552,39552,39552,39552,40960,44032,48128,51200,55296,58368,62464,65536,62464,65536,62464,65536,268439552,336991254,410,3231326208,80,7984,43008,1056915199,50465804,16780300,36,80,224,1129054208,1129054208,1129054208,1098907648,1098907648,0,136368,1073741824,7344,44032,44032,40960,727296,1073741824,39168,17227,1107575630,4294923116,4294967294,17227,1107575630,4294923116,4294967294,17227,1107575630,4294923116,4294967294,
1290240,1333248,1376256,1419264,1462272,1505280,1548288,1591296,1634304,1634304,1634304,1634304,40960,45056,49152,53248,57344,61440,65536,69632,73728,73728,73728,73728,538976288,353703189,57,808584448,64,7488,192,1040137727,16780300,50465804,576,36,128,1108344832,1107820544,1107296256,1107296256,1107296256,1107296256,291456,275328,259200,61440,53248,45056,1554432,1468416,1382400,1106264560,1112031768,4294964556,30,1106264560,1112031768,4294964556,30,1106264560,1112031768,4294964556,30,
12480,12480,12672,12672,12864,12864,13056,13056,13056,13056,13056,13056,40960,44032,48128,51200,55296,58368,62464,65536,62464,65536,62464,65536,268439552,336991254,410,1081221120,80,7984,43008,1040137855,50465804,16780300,36,80,224,1115947008,1115947008,1115947008,1098907648,1098907648,0,131400,1073741824,2376,44032,44032,40960,700800,1073741824,12672,17026,1107575432,4294954028,4294967294,17026,1107575432,4294954028,4294967294,17026,1107575432,4294954028,4294967294,
3999744,3999744,4042752,4042752,4085760,4085760,4128768,4128768,4128768,4128768,4128768,4128768,40960,44032,48128,51200,55296,58368,62464,65536,62464,65536,62464,65536,268439552,336991254,57,1353846784,80,7488,192,1048526591,16780300,50465804,576,36,224,1119617024,1119617024,1119617024,1098907648,1098907648,0,758592,1073741824,758016,44032,44032,40960,4045824,1073741824,4042752,17082,1107575488,4294947756,4294967294,17082,1107575488,4294947756,4294967294,17082,1107575488,4294947756,4294967294,
22080,22080,22272,22272,22464,22464,22656,22656,22656,22656,22656,22656,40960,44032,48128,51200,55296,58368,62464,65536,62464,65536,62464,65536,268439552,336991254,410,1882857472,80,7984,43008,1048526335,50465804,16780300,36,80,224,1122500608,1122500608,1122500608,1098907648,1098907648,0,133200,1073741824,4176,44032,44032,40960,710400,1073741824,22272,17126,1107575532,4294942828,4294967294,17126,1107575532,4294942828,4294967294,17126,1107575532,4294942828,4294967294,
5892096,5892096,5935104,5935104,5978112,5978112,6021120,6021120,6021120,6021120,6021120,6021120,40960,44032,48128,51200,55296,58368,62464,65536,62464,65536,62464,65536,268439552,336991254,57,2156007424,80,7488,192,1048526591,16780300,50465804,576,36,224,1124728832,1124728832,1124728832,1098907648,1098907648,0,1113408,1073741824,1112832,44032,44032,40960,5938176,1073741824,5935104,17161,1107575564,4294937900,4294967294,17161,1107575564,4294937900,4294967294,17161,1107575564,4294937900,4294967294,
172032,215040,258048,301056,344064,387072,9074688,9117696,9117696,9117696,9117696,9117696,49152,53248,57344,61440,65536,69632,40960,45056,45056,45056,45056,45056,2425356288,320017685,57,2429170176,80,7488,192,1056915199,16780300,50465804,576,36,128,1129447424,1129447424,1086324736,1125122048,1125122048,0,1698624,1073741824,48384,32768,32768,53248,9059328,1073741824,258048,1125073737,1126187860,4294940716,142,1125073737,1126187860,4294940716,142,16512,1099972880,1036,4294967294,
7784448,7784448,7827456,7827456,7870464,7870464,7913472,7913472,7913472,7913472,7913472,7913472,40960,44032,48128,51200,55296,58368,62464,65536,62464,65536,62464,65536,268439552,336991254,57,2958692352,80,7488,192,1056915071,16780300,50465804,576,36,224,1127612416,1127612416,1127612416,1098907648,1098907648,0,1468224,1073741824,1467648,44032,44032,40960,7830528,1073741824,7827456,17205,1107575608,4294928044,4294967294,17205,1107575608,4294928044,4294967294,17205,1107575608,4294928044,4294967294,
39360,39360,39552,39552,39744,39744,39936,39936,39936,39936,39936,39936,40960,44032,48128,51200,55296,58368,62464,65536,62464,65536,62464,65536,268439552,336991254,410,3231850496,80,7984,43008,1056915199,50465804,16780300,36,80,224,1129185280,1129185280,1129185280,1098907648,1098907648,0,136440,1073741824,7416,44032,44032,40960,727680,1073741824,39552,17229,1107575632,4294922668,4294967294,17229,1107575632,4294922668,4294967294,17229,1107575632,4294922668,4294967294,
1591296,1591296,1634304,1634304,1677312,1677312,1720320,1720320,1720320,1720320,1720320,1720320,40960,44032,48128,51200,55296,58368,62464,65536,62464,65536,62464,65536,268439552,336991254,57,809111552,80,7488,192,1040137727,16780300,50465804,576,36,224,1108869120,1108869120,1108869120,1098907648,1098907648,0,307008,1073741824,306432,44032,44032,40960,1637376,1073741824,1634304,16916,1107575328,4294960300,4294967294,16916,1107575328,4294960300,4294967294,16916,1107575328,4294960300,4294967294,
12864,12864,13056,13056,13248,13248,13440,13440,13440,13440,13440,13440,40960,44032,48128,51200,55296,58368,62464,65536,62464,65536,62464,65536,268439552,336991254,410,1081745408,80,7984,43008,1040137855,50465804,16780300,36,80,224,1116209152,1116209152,1116209152,1098907648,1098907648,0,131472,1073741824,2448,44032,44032,40960,701184,1073741824,13056,17030,1107575436,4294953580,4294967294,17030,1107575436,4294953580,4294967294,17030,1107575436,4294953580,4294967294,
3913728,3956736,3999744,4042752,4085760,4128768,4171776,4214784,4214784,4214784,4214784,4214784,40960,45056,49152,53248,57344,61440,65536,69632,69632,69632,69632,69632,538976288,353703189,57,1354367232,80,7488,192,1048526591,16780300,50465804,576,36,128,1119879168,1119617024,1119354880,1107296256,1107296256,1107296256,775296,759168,743040,57344,49152,40960,4134912,4048896,3962880,1106264758,1112031940,4294956748,30,1106264758,1112031940,4294956748,30,1106264758,1112031940,4294956748,30,
4945920,4988928,5031936,5074944,5117952,5160960,5203968,5246976,5246976,5246976,5246976,5246976,40960,45056,49152,53248,57344,61440,65536,69632,69632,69632,69632,69632,538976288,353703189,57,1883373824,80,7488,192,1048526463,16780300,50465804,576,36,128,1123024896,1122762752,1122500608,1107296256,1107296256,1107296256,968832,952704,936576,57344,49152,40960,5167104,5081088,4995072,1106264806,1112031988,4294953676,30,1106264806,1112031988,4294953676,30,1106264806,1112031988,4294953676,30,
5978112,5978112,6021120,6021120,6064128,6064128,6107136,6107136,6107136,6107136,6107136,6107136,40960,44032,48128,51200,55296,58368,62464,65536,62464,65536,62464,65536,268439552,336991254,57,2156531712,80,7488,192,1048526591,16780300,50465804,576,36,224,1124859904,1124859904,1124859904,1098907648,1098907648,0,1129536,1073741824,1128960,44032,44032,40960,6024192,1073741824,6021120,17163,1107575566,4294937452,4294967294,17163,1107575566,4294937452,4294967294,17163,1107575566,4294937452,4294967294,
1290240,1333248,1376256,1419264,1462272,9504768,9547776,9590784,9590784,9590784,9590784,9590784,57344,61440,65536,69632,73728,40960,45056,49152,53248,53248,53248,53248,2425393296,353703189,57,2429814016,64,7488,192,1056915199,16780300,50465804,576,36,128,1130102784,1130233856,1107296256,1125122048,1125122048,1125122048,1779264,1795392,263232,32768,40960,61440,9489408,9575424,1403904,1125073747,1126187871,4294939436,142,1125073747,1126187871,4294939436,142,1125007856,1126318600,4294964396,142,
7870464,7870464,7913472,7913472,7956480,7956480,7999488,7999488,7999488,7999488,7999488,7999488,40960,44032,48128,51200,55296,58368,62464,65536,62464,65536,62464,65536,268439552,336991254,57,2959216640,80,7488,192,1056915071,16780300,50465804,576,36,224,1127743488,1127743488,1127743488,1098907648,1098907648,0,1484352,1073741824,1483776,44032,44032,40960,7916544,1073741824,7913472,17207,1107575610,4294927596,4294967294,17207,1107575610,4294927596,4294967294,17207,1107575610,4294927596,4294967294,
39744,39744,39936,39936,40128,40128,40320,40320,40320,40320,40320,40320,40960,44032,48128,51200,55296,58368,62464,65536,62464,65536,62464,65536,268439552,336991254,410,3232374784,80,7984,43008,1056915199,50465804,16780300,36,80,224,1129316352,1129316352,1129316352,1098907648,1098907648,0,136512,1073741824,7488,44032,44032,40960,728064,1073741824,39936,17231,1107575634,4294922220,4294967294,17231,1107575634,4294922220,4294967294,17231,1107575634,4294922220,4294967294,
1677312,1677312,1720320,1720320,1763328,1763328,1806336,1806336,1806336,1806336,1806336,1806336,40960,44032,48128,51200,55296,58368,62464,65536,62464,65536,62464,65536,268439552,336991254,57,809635840,80,7488,192,1040137855,16780300,50465804,576,36,224,1109393408,1109393408,1109393408,1098907648,1098907648,0,323136,1073741824,322560,44032,44032,40960,1723392,1073741824,1720320,16924,1107575336,4294959852,4294967294,16924,1107575336,4294959852,4294967294,16924,1107575336,4294959852,4294967294,
13248,13248,13440,13440,13632,13632,13824,13824,13824,13824,13824,13824,40960,44032,48128,51200,55296,58368,62464,65536,62464,65536,62464,65536,268439552,336991254,410,1082269696,80,7984,43008,1040137983,50465804,16780300,36,80,224,1116471296,1116471296,1116471296,1098907648,1098907648,0,131544,1073741824,2520,44032,44032,40960,701568,1073741824,13440,17034,1107575440,4294953132,4294967294,17034,1107575440,4294953132,4294967294,17034,1107575440,4294953132,4294967294,
4085760,4085760,4128768,4128768,4171776,4171776,4214784,4214784,4214784,4214784,4214784,4214784,40960,44032,48128,51200,55296,58368,62464,65536,62464,65536,62464,65536,268439552,336991254,57,1354895360,80,7488,192,1048526591,16780300,50465804,576,36,224,1119879168,1119879168,1119879168,1098907648,1098907648,0,774720,1073741824,774144,44032,44032,40960,4131840,1073741824,4128768,17086,1107575492,4294947308,4294967294,17086,1107575492,4294947308,4294967294,17086,1107575492,4294947308,4294967294,
22464,22464,22656,22656,22848,22848,23040,23040,23040,23040,23040,23040,40960,44032,48128,51200,55296,58368,62464,65536,62464,65536,62464,65536,268439552,336991254,410,1883906048,80,7984,43008,1048526463,50465804,16780300,36,80,224,1122762752,1122762752,1122762752,1098907648,1098907648,0,133272,1073741824,4248,44032,44032,40960,710784,1073741824,22656,17130,1107575536,4294942380,4294967294,17130,1107575536,4294942380,4294967294,17130,1107575536,4294942380,4294967294,
6064128,6064128,6107136,6107136,6150144,6150144,6193152,6193152,6193152,6193152,6193152,6193152,40960,44032,48128,51200,55296,58368,62464,65536,62464,65536,62464,65536,268439552,336991254,57,2157056000,80,7488,192,1048526591,16780300,50465804,576,36,224,1124990976,1124990976,1124990976,1098907648,1098907648,0,1145664,1073741824,1145088,44032,44032,40960,6110208,1073741824,6107136,17165,1107575568,4294937004,4294967294,17165,1107575568,4294937004,4294967294,17165,1107575568,4294937004,4294967294,
30528,30528,30720,30720,30912,30912,31104,31104,31104,31104,31104,31104,40960,44032,48128,51200,55296,58368,62464,65536,62464,65536,62464,65536,268439552,336991254,410,2686590976,80,7984,43008,1056914943,50465804,16780300,36,80,224,1126170624,1126170624,1126170624,1098907648,1098907648,0,134784,1073741824,5760,44032,44032,40960,718848,1073741824,30720,17183,1107575586,4294932972,4294967294,17183,1107575586,4294932972,4294967294,17183,1107575586,4294932972,4294967294,
7784448,7827456,7870464,7913472,7956480,7999488,8042496,8085504,8085504,8085504,8085504,8085504,40960,45056,49152,53248,57344,61440,65536,69632,69632,69632,69632,69632,538976288,353703189,57,2959737088,80,7488,192,1056915071,16780300,50465804,576,36,128,1127874560,1127743488,1127612416,1107296256,1107296256,1107296256,1501056,1484928,1468800,57344,49152,40960,8005632,7919616,7833600,1106264885,1112032060,4294945228,30,1106264885,1112032060,4294945228,30,1106264885,1112032060,4294945228,30,
40128,40128,40320,40320,40512,40512,40704,40704,40704,40704,40704,40704,40960,44032,48128,51200,55296,58368,62464,65536,62464,65536,62464,65536,268439552,336991254,410,3232899072,80,7984,43008,1056915199,50465804,16780300,36,80,224,1129447424,1129447424,1129447424,1098907648,1098907648,0,136584,1073741824,7560,44032,44032,40960,728448,1073741824,40320,17233,1107575636,4294921772,4294967294,17233,1107575636,4294921772,4294967294,17233,1107575636,4294921772,4294967294,
1763328,1763328,1806336,1806336,1849344,1849344,1892352,1892352,1892352,1892352,1892352,1892352,40960,44032,48128,51200,55296,58368,62464,65536,62464,65536,62464,65536,268439552,336991254,57,810160128,80,7488,192,1040137855,16780300,50465804,576,36,224,1109917696,1109917696,1109917696,1098907648,1098907648,0,339264,1073741824,338688,44032,44032,40960,1809408,1073741824,1806336,16932,1107575344,4294959404,4294967294,16932,1107575344,4294959404,4294967294,16932,1107575344,4294959404,4294967294,
13632,13632,13824,13824,14016,14016,14208,14208,14208,14208,14208,14208,40960,44032,48128,51200,55296,58368,62464,65536,62464,65536,62464,65536,268439552,336991254,410,1082793984,80,7984,43008,1040137983,50465804,16780300,36,80,224,1116733440,1116733440,1116733440,1098907648,1098907648,0,131616,1073741824,2592,44032,44032,40960,701952,1073741824,13824,17038,1107575444,4294952684,4294967294,17038,1107575444,4294952684,4294967294,17038,1107575444,4294952684,4294967294,
4171776,4171776,4214784,4214784,4257792,4257792,4300800,4300800,4300800,4300800,4300800,4300800,40960,44032,48128,51200,55296,58368,62464,65536,62464,65536,62464,65536,268439552,336991254,57,1355419648,80,7488,192,1048526591,16780300,50465804,576,36,224,1120141312,1120141312,1120141312,1098907648,1098907648,0,790848,1073741824,790272,44032,44032,40960,4217856,1073741824,4214784,17090,1107575496,4294946860,4294967294,17090,1107575496,4294946860,4294967294,17090,1107575496,4294946860,4294967294,
22848,22848,23040,23040,23232,23232,23424,23424,23424,23424,23424,23424,40960,44032,48128,51200,55296,58368,62464,65536,62464,65536,62464,65536,268439552,336991254,410,1884430336,80,7984,43008,1048526463,50465804,16780300,36,80,224,1123024896,1123024896,1123024896,1098907648,1098907648,0,133344,1073741824,4320,44032,44032,40960,711168,1073741824,23040,17134,1107575540,4294941932,4294967294,17134,1107575540,4294941932,4294967294,17134,1107575540,4294941932,4294967294,
5978112,6021120,6064128,6107136,6150144,6193152,6236160,6279168,6279168,6279168,6279168,6279168,40960,45056,49152,53248,57344,61440,65536,69632,69632,69632,69632,69632,538976288,353703189,57,2157576448,80,7488,192,1048526591,16780300,50465804,576,36,128,1125122048,1124990976,1124859904,1107296256,1107296256,1107296256,1162368,1146240,1130112,57344,49152,40960,6199296,6113280,6027264,1106264843,1112032018,4294950604,30,1106264843,1112032018,4294950604,30,1106264843,1112032018,4294950604,30,
30912,30912,31104,31104,31296,31296,31488,31488,31488,31488,31488,31488,40960,44032,48128,51200,55296,58368,62464,65536,62464,65536,62464,65536,268439552,336991254,410,2687115264,80,7984,43008,1056914943,50465804,16780300,36,80,224,1126301696,1126301696,1126301696,1098907648,1098907648,0,134856,1073741824,5832,44032,44032,40960,719232,1073741824,31104,17185,1107575588,4294932524,4294967294,17185,1107575588,4294932524,4294967294,17185,1107575588,4294932524,4294967294,
7956480,7956480,7999488,7999488,8042496,8042496,8085504,8085504,8085504,8085504,8085504,8085504,40960,44032,48128,51200,55296,58368,62464,65536,62464,65536,62464,65536,268439552,336991254,57,2960265216,80,7488,192,1056915071,16780300,50465804,576,36,224,1127874560,1127874560,1127874560,1098907648,1098907648,0,1500480,1073741824,1499904,44032,44032,40960,8002560,1073741824,7999488,17209,1107575612,4294927148,4294967294,17209,1107575612,4294927148,4294967294,17209,1107575612,4294927148,4294967294,
40512,40512,40704,40704,40896,40896,41088,41088,41088,41088,41088,41088,40960,44032,48128,51200,55296,58368,62464,65536,62464,65536,62464,65536,268439552,336991254,410,3233423360,80,7984,43008,1056915199,50465804,16780300,36,80,224,1129578496,1129578496,1129578496,1098907648,1098907648,0,136656,1073741824,7632,44032,44032,40960,728832,1073741824,40704,17235,1107575638,4294921324,4294967294,17235,1107575638,4294921324,4294967294,17235,1107575638,4294921324,4294967294,
1849344,1849344,1892352,1892352,1935360,1935360,1978368,1978368,1978368,1978368,1978368,1978368,40960,44032,48128,51200,55296,58368,62464,65536,62464,65536,62464,65536,268439552,336991254,57,810684416,80,7488,192,1040137855,16780300,50465804,576,36,224,1110441984,1110441984,1110441984,1098907648,1098907648,0,355392,1073741824,354816,44032,44032,40960,1895424,1073741824,1892352,16940,1107575352,4294958956,4294967294,16940,1107575352,4294958956,4294967294,16940,1107575352,4294958956,4294967294,
14016,14016,14208,14208,14400,14400,14592,14592,14592,14592,14592,14592,40960,44032,48128,51200,55296,58368,62464,65536,62464,65536,62464,65536,268439552,336991254,410,1083318272,80,7984,43008,1040137983,50465804,16780300,36,80,224,1116995584,1116995584,1116995584,1098907648,1098907648,0,131688,1073741824,2664,44032,44032,40960,702336,1073741824,14208,17042,1107575448,4294952236,4294967294,17042,1107575448,4294952236,4294967294,17042,1107575448,4294952236,4294967294,
4257792,4257792,4300800,4300800,4343808,4343808,4386816,4386816,4386816,4386816,4386816,4386816,40960,44032,48128,51200,55296,58368,62464,65536,62464,65536,62464,65536,268439552,336991254,57,1355943936,80,7488,192,1048526591,16780300,50465804,576,36,224,1120403456,1120403456,1120403456,1098907648,1098907648,0,806976,1073741824,806400,44032,44032,40960,4303872,1073741824,4300800,17094,1107575500,4294946412,4294967294,17094,1107575500,4294946412,4294967294,17094,1107575500,4294946412,4294967294,
23232,23232,23424,23424,23616,23616,23808,23808,23808,23808,23808,23808,40960,44032,48128,51200,55296,58368,62464,65536,62464,65536,62464,65536,268439552,336991254,410,1884954624,80,7984,43008,1048526463,50465804,16780300,36,80,224,1123287040,1123287040,1123287040,1098907648,1098907648,0,133416,1073741824,4392,44032,44032,40960,711552,1073741824,23424,17138,1107575544,4294941484,4294967294,17138,1107575544,4294941484,4294967294,17138,1107575544,4294941484,4294967294,
6150144,6150144,6193152,6193152,6236160,6236160,6279168,6279168,6279168,6279168,6279168,6279168,40960,44032,48128,51200,55296,58368,62464,65536,62464,65536,62464,65536,268439552,336991254,57,2158104576,80,7488,192,1048526591,16780300,50465804,576,36,224,1125122048,1125122048,1125122048,1098907648,1098907648,0,1161792,1073741824,1161216,44032,44032,40960,6196224,1073741824,6193152,17167,1107575570,4294936556,4294967294,17167,1107575570,4294936556,4294967294,17167,1107575570,4294936556,4294967294,
31296,31296,31488,31488,31680,31680,31872,31872,31872,31872,31872,31872,40960,44032,48128,51200,55296,58368,62464,65536,62464,65536,62464,65536,268439552,336991254,410,2687639552,80,7984,43008,1056914943,50465804,16780300,36,80,224,1126432768,1126432768,1126432768,1098907648,1098907648,0,134928,1073741824,5904,44032,44032,40960,719616,1073741824,31488,17187,1107575590,4294932076,4294967294,17187,1107575590,4294932076,4294967294,17187,1107575590,4294932076,4294967294,
8042496,8042496,8085504,8085504,8128512,8128512,8171520,8171520,8171520,8171520,8171520,8171520,40960,44032,48128,51200,55296,58368,62464,65536,62464,65536,62464,65536,268439552,336991254,57,2960789504,80,7488,192,1056915071,16780300,50465804,576,36,224,1128005632,1128005632,1128005632,1098907648,1098907648,0,1516608,1073741824,1516032,44032,44032,40960,8088576,1073741824,8085504,17211,1107575614,4294926700,4294967294,17211,1107575614,4294926700,4294967294,17211,1107575614,4294926700,4294967294,
40896,40896,41088,41088,41280,41280,41472,41472,41472,41472,41472,41472,40960,44032,48128,51200,55296,58368,62464,65536,62464,65536,62464,65536,268439552,336991254,410,3233947648,80,7984,43008,1056915199,50465804,16780300,36,80,224,1129709568,1129709568,1129709568,1098907648,1098907648,0,136728,1073741824,7704,44032,44032,40960,729216,1073741824,41088,17237,1107575640,4294920876,4294967294,17237,1107575640,4294920876,4294967294,17237,1107575640,4294920876,4294967294,
1591296,1634304,1677312,1720320,1763328,1806336,1849344,1892352,1892352,1892352,1892352,1892352,40960,45056,49152,53248,57344,61440,65536,69632,69632,69632,69632,69632,538976288,353703189,57,811204864,80,7488,192,1040137855,16780300,50465804,576,36,128,1109917696,1109393408,1108869120,1107296256,1107296256,1107296256,339840,323712,307584,57344,49152,40960,1812480,1726464,1640448,1106264596,1112031792,4294963660,30,1106264596,1112031792,4294963660,30,1106264596,1112031792,4294963660,30,
2881536,2924544,2967552,3010560,3053568,3096576,3139584,3182592,3182592,3182592,3182592,3182592,40960,45056,49152,53248,57344,61440,65536,69632,69632,69632,69632,69632,538976288,353703189,57,1083834624,80,7488,192,1040137983,16780300,50465804,576,36,128,1116733440,1116471296,1116209152,1107296256,1107296256,1107296256,581760,565632,549504,57344,49152,40960,3102720,3016704,2930688,1106264710,1112031892,4294959820,30,1106264710,1112031892,4294959820,30,1106264710,1112031892,4294959820,30,
4343808,4343808,4386816,4386816,4429824,4429824,4472832,4472832,4472832,4472832,4472832,4472832,40960,44032,48128,51200,55296,58368,62464,65536,62464,65536,62464,65536,268439552,336991254,57,1356468224,80,7488,192,1048526591,16780300,50465804,576,36,224,1120665600,1120665600,1120665600,1098907648,1098907648,0,823104,1073741824,822528,44032,44032,40960,4389888,1073741824,4386816,17098,1107575504,4294945964,4294967294,17098,1107575504,4294945964,4294967294,17098,1107575504,4294945964,4294967294,
23616,23616,23808,23808,24000,24000,24192,24192,24192,24192,24192,24192,40960,44032,48128,51200,55296,58368,62464,65536,62464,65536,62464,65536,268439552,336991254,410,1885478912,80,7984,43008,1048526463,50465804,16780300,36,80,224,1123549184,1123549184,1123549184,1098907648,1098907648,0,133488,1073741824,4464,44032,44032,40960,711936,1073741824,23808,17142,1107575548,4294941036,4294967294,17142,1107575548,4294941036,4294967294,17142,1107575548,4294941036,4294967294,
6236160,6236160,6279168,6279168,6322176,6322176,6365184,6365184,6365184,6365184,6365184,6365184,40960,44032,48128,51200,55296,58368,62464,65536,62464,65536,62464,65536,268439552,336991254,57,2158628864,80,7488,192,1048526591,16780300,50465804,576,36,224,1125253120,1125253120,1125253120,1098907648,1098907648,0,1177920,1073741824,1177344,44032,44032,40960,6282240,1073741824,6279168,17169,1107575572,4294936108,4294967294,17169,1107575572,4294936108,4294967294,17169,1107575572,4294936108,4294967294,
31680,31680,31872,31872,32064,32064,32256,32256,32256,32256,32256,32256,40960,44032,48128,51200,55296,58368,62464,65536,62464,65536,62464,65536,268439552,336991254,410,2688163840,80,7984,43008,1056914943,50465804,16780300,36,80,224,1126563840,1126563840,1126563840,1098907648,1098907648,0,135000,1073741824,5976,44032,44032,40960,720000,1073741824,31872,17189,1107575592,4294931628,4294967294,17189,1107575592,4294931628,4294967294,17189,1107575592,4294931628,4294967294,
8128512,8128512,8171520,8171520,8214528,8214528,8257536,8257536,8257536,8257536,8257536,8257536,40960,44032,48128,51200,55296,58368,62464,65536,62464,65536,62464,65536,268439552,336991254,57,2961313792,80,7488,192,1056915199,16780300,50465804,576,36,224,1128136704,1128136704,1128136704,1098907648,1098907648,0,1532736,1073741824,1532160,44032,44032,40960,8174592,1073741824,8171520,17213,1107575616,4294926252,4294967294,17213,1107575616,4294926252,4294967294,17213,1107575616,4294926252,4294967294,
430080,473088,516096,559104,602112,645120,9074688,9117696,9117696,9117696,9117696,9117696,49152,53248,57344,61440,65536,69632,40960,45056,45056,45056,45056,45056,3233808384,320017685,57,3234476544,80,7488,192,1056915199,16780300,50465804,576,36,128,1129447424,1129447424,1094713344,1128267776,1128267776,0,1700352,1073741824,96768,32768,32768,53248,9068544,1073741824,516096,1128219465,1129333588,4294940428,190,1128219465,1129333588,4294940428,190,16672,1099972976,268,4294967294,
1935360,1935360,1978368,1978368,2021376,2021376,2064384,2064384,2064384,2064384,2064384,2064384,40960,44032,48128,51200,55296,58368,62464,65536,62464,65536,62464,65536,268439552,336991254,57,811732992,80,7488,192,1040137855,16780300,50465804,576,36,224,1110966272,1110966272,1110966272,1098907648,1098907648,0,371520,1073741824,370944,44032,44032,40960,1981440,1073741824,1978368,16948,1107575360,4294958508,4294967294,16948,1107575360,4294958508,4294967294,16948,1107575360,4294958508,4294967294,
14400,14400,14592,14592,14784,14784,14976,14976,14976,14976,14976,14976,40960,44032,48128,51200,55296,58368,62464,65536,62464,65536,62464,65536,268439552,336991254,410,1084366848,80,7984,43008,1040137983,50465804,16780300,36,80,224,1117257728,1117257728,1117257728,1098907648,1098907648,0,131760,1073741824,2736,44032,44032,40960,702720,1073741824,14592,17046,1107575452,4294951788,4294967294,17046,1107575452,4294951788,4294967294,17046,1107575452,4294951788,4294967294,
3072,3072,3264,3264,3456,3456,3648,3648,3840,3840,4032,4032,40960,44032,48128,51200,55296,58368,62464,65536,69632,72704,76800,79872,268439552,336991254,410,1612852224,64,7984,43008,1048526335,50465804,16780300,36,80,224,1099956224,1099956224,1099956224,1098907648,1098907648,0,129672,1073741824,648,51200,51200,48128,691584,1073741824,3456,16768,1107575208,4294965004,4294967294,16768,1107575208,4294965004,4294967294,16768,1107575208,4294965004,4294967294,
24000,24000,24192,24192,24384,24384,24576,24576,24576,24576,24576,24576,40960,44032,48128,51200,55296,58368,62464,65536,62464,65536,62464,65536,268439552,336991254,410,1886003200,80,7984,43008,1048526463,50465804,16780300,36,80,224,1123811328,1123811328,1123811328,1098907648,1098907648,0,133560,1073741824,4536,44032,44032,40960,712320,1073741824,24192,17146,1107575552,4294940588,4294967294,17146,1107575552,4294940588,4294967294,17146,1107575552,4294940588,4294967294,
6322176,6322176,6365184,6365184,6408192,6408192,6451200,6451200,6451200,6451200,6451200,6451200,40960,44032,48128,51200,55296,58368,62464,65536,62464,65536,62464,65536,268439552,336991254,57,2159153152,80,7488,192,1048526591,16780300,50465804,576,36,224,1125384192,1125384192,1125384192,1098907648,1098907648,0,1194048,1073741824,1193472,44032,44032,40960,6368256,1073741824,6365184,17171,1107575574,4294935660,4294967294,17171,1107575574,4294935660,4294967294,17171,1107575574,4294935660,4294967294,
32064,32064,32256,32256,32448,32448,32640,32640,32640,32640,32640,32640,40960,44032,48128,51200,55296,58368,62464,65536,62464,65536,62464,65536,268439552,336991254,410,2688688128,80,7984,43008,1056915071,50465804,16780300,36,80,224,1126694912,1126694912,1126694912,1098907648,1098907648,0,135072,1073741824,6048,44032,44032,40960,720384,1073741824,32256,17191,1107575594,4294931180,4294967294,17191,1107575594,4294931180,4294967294,17191,1107575594,4294931180,4294967294,
8214528,8214528,8257536,8257536,8300544,8300544,8343552,8343552,8343552,8343552,8343552,8343552,40960,44032,48128,51200,55296,58368,62464,65536,62464,65536,62464,65536,268439552,336991254,57,2961838080,80,7488,192,1056915199,16780300,50465804,576,36,224,1128267776,1128267776,1128267776,1098907648,1098907648,0,1548864,1073741824,1548288,44032,44032,40960,8260608,1073741824,8257536,17215,1107575618,4294925804,4294967294,17215,1107575618,4294925804,4294967294,17215,1107575618,4294925804,4294967294,
1290240,1333248,1376256,1419264,1462272,9504768,9547776,9590784,9590784,9590784,9590784,9590784,57344,61440,65536,69632,73728,40960,45056,49152,53248,53248,53248,53248,3233857728,353703189,57,3235120384,64,7488,192,1056915199,16780300,50465804,576,36,128,1130102784,1130233856,1107296256,1128267776,1128267776,1128267776,1780992,1797120,264960,32768,40960,61440,9498624,9584640,1413120,1128219475,1129333599,4294939148,190,1128219475,1129333599,4294939148,190,1128153584,1129464328,4294964108,190,
2021376,2021376,2064384,2064384,2107392,2107392,2150400,2150400,2150400,2150400,2150400,2150400,40960,44032,48128,51200,55296,58368,62464,65536,62464,65536,62464,65536,268439552,336991254,57,812257280,80,7488,192,1040137855,16780300,50465804,576,36,224,1111490560,1111490560,1111490560,1098907648,1098907648,0,387648,1073741824,387072,44032,44032,40960,2067456,1073741824,2064384,16956,1107575368,4294958060,4294967294,16956,1107575368,4294958060,4294967294,16956,1107575368,4294958060,4294967294,
14784,14784,14976,14976,15168,15168,15360,15360,15360,15360,15360,15360,40960,44032,48128,51200,55296,58368,62464,65536,62464,65536,62464,65536,268439552,336991254,410,1084891136,80,7984,43008,1040137983,50465804,16780300,36,80,224,1117519872,1117519872,1117519872,1098907648,1098907648,0,131832,1073741824,2808,44032,44032,40960,703104,1073741824,14976,17050,1107575456,4294951340,4294967294,17050,1107575456,4294951340,4294967294,17050,1107575456,4294951340,4294967294,
3456,3456,3648,3648,3840,3840,4032,4032,4224,4224,4416,4416,40960,44032,48128,51200,55296,58368,62464,65536,69632,72704,76800,79872,268439552,336991254,410,1613376512,64,7984,43008,1048526335,50465804,16780300,36,80,224,1101004800,1101004800,1101004800,1098907648,1098907648,0,129744,1073741824,720,51200,51200,48128,691968,1073741824,3840,16784,1107575224,4294964556,4294967294,16784,1107575224,4294964556,4294967294,16784,1107575224,4294964556,4294967294,
86016,129024,172032,215040,258048,301056,301056,301056,301056,301056,301056,301056,40960,45056,49152,53248,57344,61440,61440,61440,61440,61440,61440,61440,0,353703189,57,1886564864,80,7488,192,1048526463,16780300,50465804,576,36,128,1082130432,1082130432,1082130432,0,0,0,1073741824,1073741824,32256,45056,45056,45056,1073741824,1073741824,172032,16384,1099972832,1036,4294967294,16384,1099972832,1036,4294967294,16384,1099972832,1036,4294967294,
6408192,6408192,6451200,6451200,6494208,6494208,6537216,6537216,6537216,6537216,6537216,6537216,40960,44032,48128,51200,55296,58368,62464,65536,62464,65536,62464,65536,268439552,336991254,57,2159677440,80,7488,192,1048526591,16780300,50465804,576,36,224,1125515264,1125515264,1125515264,1098907648,1098907648,0,1210176,1073741824,1209600,44032,44032,40960,6454272,1073741824,6451200,17173,1107575576,4294935212,4294967294,17173,1107575576,4294935212,4294967294,17173,1107575576,4294935212,4294967294,
32448,32448,32640,32640,32832,32832,33024,33024,33024,33024,33024,33024,40960,44032,48128,51200,55296,58368,62464,65536,62464,65536,62464,65536,268439552,336991254,410,2689212416,80,7984,43008,1056915071,50465804,16780300,36,80,224,1126825984,1126825984,1126825984,1098907648,1098907648,0,135144,1073741824,6120,44032,44032,40960,720768,1073741824,32640,17193,1107575596,4294930732,4294967294,17193,1107575596,4294930732,4294967294,17193,1107575596,4294930732,4294967294,
8300544,8300544,8343552,8343552,8386560,8386560,8429568,8429568,8429568,8429568,8429568,8429568,40960,44032,48128,51200,55296,58368,62464,65536,62464,65536,62464,65536,268439552,336991254,57,2962362368,80,7488,192,1056915199,16780300,50465804,576,36,224,1128398848,1128398848,1128398848,1098907648,1098907648,0,1564992,1073741824,1564416,44032,44032,40960,8346624,1073741824,8343552,17217,1107575620,4294925356,4294967294,17217,1107575620,4294925356,4294967294,17217,1107575620,4294925356,4294967294,
8816640,8859648,8902656,8945664,8988672,9031680,9074688,9117696,9117696,9117696,9117696,9117696,40960,45056,49152,53248,57344,61440,65536,69632,69632,69632,69632,69632,538976288,353703189,57,3491889408,80,7488,192,1056914943,16780300,50465804,576,36,128,1129447424,1129316352,1129185280,1107296256,1107296256,1107296256,1694592,1678464,1662336,57344,49152,40960,9037824,8951808,8865792,1106264909,1112032084,4294942156,30,1106264909,1112032084,4294942156,30,1106264909,1112032084,4294942156,30,
2107392,2107392,2150400,2150400,2193408,2193408,2236416,2236416,2236416,2236416,2236416,2236416,40960,44032,48128,51200,55296,58368,62464,65536,62464,65536,62464,65536,268439552,336991254,57,812781568,80,7488,192,1040137855,16780300,50465804,576,36,224,1112014848,1112014848,1112014848,1098907648,1098907648,0,403776,1073741824,403200,44032,44032,40960,2153472,1073741824,2150400,16964,1107575376,4294957612,4294967294,16964,1107575376,4294957612,4294967294,16964,1107575376,4294957612,4294967294,
1290240,1290240,1333248,1333248,1376256,1376256,1419264,1419264,1462272,1462272,1505280,1505280,40960,44032,48128,51200,55296,58368,62464,65536,69632,72704,76800,79872,1076903984,336991254,57,1085414400,64,7488,192,1040137983,16780300,50465804,576,36,224,1107296256,1107296256,1107296256,1115684864,1115684864,1111490560,260352,1073741824,259776,51200,51200,48128,1388544,1073741824,1385472,1110983152,1117929996,4294961580,46,1110983152,1117929996,4294961580,46,1110983152,1117929996,4294961580,46,
4171776,4214784,4257792,4300800,4343808,4386816,4429824,4472832,4472832,4472832,4472832,4472832,40960,45056,49152,53248,57344,61440,65536,69632,69632,69632,69632,69632,538976288,353703189,57,1613889792,80,7488,192,1048526335,16780300,50465804,576,36,128,1120665600,1120403456,1120141312,1107296256,1107296256,1107296256,823680,807552,791424,57344,49152,40960,4392960,4306944,4220928,1106264770,1112031952,4294955980,30,1106264770,1112031952,4294955980,30,1106264770,1112031952,4294955980,30,
3840,3840,4032,4032,4224,4224,4416,4416,4608,4608,4800,4800,40960,44032,48128,51200,55296,58368,62464,65536,69632,72704,76800,79872,268439552,336991254,410,1887054848,64,7984,43008,1048526463,50465804,16780300,36,80,224,1102053376,1102053376,1102053376,1098907648,1098907648,0,129816,1073741824,792,51200,51200,48128,692352,1073741824,4224,16800,1107575240,4294964108,4294967294,16800,1107575240,4294964108,4294967294,16800,1107575240,4294964108,4294967294,
1032192,1075200,1118208,1161216,1204224,1247232,8730624,8773632,8773632,8773632,8773632,8773632,49152,53248,57344,61440,65536,69632,40960,45056,45056,45056,45056,45056,2155872256,320017685,57,2160209920,80,7488,192,1048526591,16780300,50465804,576,36,128,1128923136,1128923136,1104150528,1124073472,1124073472,0,1633536,1073741824,209664,32768,32768,53248,8712192,1073741824,1118208,1123959617,1125139276,4294941836,126,1123959617,1125139276,4294941836,126,16832,1099973096,4294965772,4294967294,
7010304,7053312,7096320,7139328,7182336,7225344,7268352,7311360,7311360,7311360,7311360,7311360,40960,45056,49152,53248,57344,61440,65536,69632,69632,69632,69632,69632,538976288,353703189,57,2689728768,80,7488,192,1056915071,16780300,50465804,576,36,128,1126694912,1126563840,1126432768,1107296256,1107296256,1107296256,1355904,1339776,1323648,57344,49152,40960,7231488,7145472,7059456,1106264867,1112032042,4294947532,30,1106264867,1112032042,4294947532,30,1106264867,1112032042,4294947532,30,
8386560,8386560,8429568,8429568,8472576,8472576,8515584,8515584,8515584,8515584,8515584,8515584,40960,44032,48128,51200,55296,58368,62464,65536,62464,65536,62464,65536,268439552,336991254,57,2962886656,80,7488,192,1056915199,16780300,50465804,576,36,224,1128529920,1128529920,1128529920,1098907648,1098907648,0,1581120,1073741824,1580544,44032,44032,40960,8432640,1073741824,8429568,17219,1107575622,4294924908,4294967294,17219,1107575622,4294924908,4294967294,17219,1107575622,4294924908,4294967294,
41280,41280,41472,41472,41664,41664,41856,41856,41856,41856,41856,41856,40960,44032,48128,51200,55296,58368,62464,65536,62464,65536,62464,65536,268439552,336991254,410,3492421632,80,7984,43008,1056914943,50465804,16780300,36,80,224,1129840640,1129840640,1129840640,1098907648,1098907648,0,136800,1073741824,7776,44032,44032,40960,729600,1073741824,41472,17239,1107575642,4294920428,4294967294,17239,1107575642,4294920428,4294967294,17239,1107575642,4294920428,4294967294,
2193408,2193408,2236416,2236416,2279424,2279424,2322432,2322432,2322432,2322432,2322432,2322432,40960,44032,48128,51200,55296,58368,62464,65536,62464,65536,62464,65536,268439552,336991254,57,813305856,80,7488,192,1040137855,16780300,50465804,576,36,224,1112539136,1112539136,1112539136,1098907648,1098907648,0,419904,1073741824,419328,44032,44032,40960,2239488,1073741824,2236416,16972,1107575384,4294957164,4294967294,16972,1107575384,4294957164,4294967294,16972,1107575384,4294957164,4294967294,
0,43008,86016,129024,172032,215040,215040,215040,215040,215040,215040,215040,40960,45056,49152,53248,57344,61440,61440,61440,61440,61440,61440,61440,0,353703189,57,1085977088,80,7488,192,1040137983,16780300,50465804,576,36,128,1073741824,1073741824,1073741824,0,0,0,1073741824,1073741824,16128,45056,45056,45056,1073741824,1073741824,86016,0,1099972768,1292,4294967294,0,1099972768,1292,4294967294,0,1099972768,1292,4294967294,
15168,15168,15360,15360,15552,15552,15744,15744,15744,15744,15744,15744,40960,44032,48128,51200,55296,58368,62464,65536,62464,65536,62464,65536,268439552,336991254,410,1614422016,80,7984,43008,1048526335,50465804,16780300,36,80,224,1117782016,1117782016,1117782016,1098907648,1098907648,0,131904,1073741824,2880,44032,44032,40960,703488,1073741824,15360,17054,1107575460,4294950892,4294967294,17054,1107575460,4294950892,4294967294,17054,1107575460,4294950892,4294967294,
5203968,5246976,5289984,5332992,5376000,5419008,5462016,5505024,5505024,5505024,5505024,5505024,40960,45056,49152,53248,57344,61440,65536,69632,69632,69632,69632,69632,538976288,353703189,57,1887568128,80,7488,192,1048526591,16780300,50465804,576,36,128,1123811328,1123549184,1123287040,1107296256,1107296256,1107296256,1017216,1001088,984960,57344,49152,40960,5425152,5339136,5253120,1106264818,1112032000,4294952908,30,1106264818,1112032000,4294952908,30,1106264818,1112032000,4294952908,30,
1118208,1161216,1204224,1247232,1290240,1333248,9160704,9203712,9203712,9203712,9203712,9203712,49152,53248,57344,61440,65536,69632,40960,45056,45056,45056,45056,45056,2155872256,320017685,57,2160865280,80,7488,192,1048526591,16780300,50465804,576,36,128,1129578496,1129578496,1105199104,1124073472,1124073472,0,1714176,1073741824,225792,32768,32768,53248,9142272,1073741824,1204224,1123959627,1125139286,4294940556,126,1123959627,1125139286,4294940556,126,16848,1099973112,4294965516,4294967294,
32832,32832,33024,33024,33216,33216,33408,33408,33408,33408,33408,33408,40960,44032,48128,51200,55296,58368,62464,65536,62464,65536,62464,65536,268439552,336991254,410,2690260992,80,7984,43008,1056915071,50465804,16780300,36,80,224,1126957056,1126957056,1126957056,1098907648,1098907648,0,135216,1073741824,6192,44032,44032,40960,721152,1073741824,33024,17195,1107575598,4294930284,4294967294,17195,1107575598,4294930284,4294967294,17195,1107575598,4294930284,4294967294,
8472576,8472576,8515584,8515584,8558592,8558592,8601600,8601600,8601600,8601600,8601600,8601600,40960,44032,48128,51200,55296,58368,62464,65536,62464,65536,62464,65536,268439552,336991254,57,2963410944,80,7488,192,1056915199,16780300,50465804,576,36,224,1128660992,1128660992,1128660992,1098907648,1098907648,0,1597248,1073741824,1596672,44032,44032,40960,8518656,1073741824,8515584,17221,1107575624,4294924460,4294967294,17221,1107575624,4294924460,4294967294,17221,1107575624,4294924460,4294967294,
41664,41664,41856,41856,42048,42048,42240,42240,42240,42240,42240,42240,40960,44032,48128,51200,55296,58368,62464,65536,62464,65536,62464,65536,268439552,336991254,410,3492945920,80,7984,43008,1056914943,50465804,16780300,36,80,224,1129971712,1129971712,1129971712,1098907648,1098907648,0,136872,1073741824,7848,44032,44032,40960,729984,1073741824,41856,17241,1107575644,4294919980,4294967294,17241,1107575644,4294919980,4294967294,17241,1107575644,4294919980,4294967294,
2279424,2279424,2322432,2322432,2365440,2365440,2408448,2408448,2408448,2408448,2408448,2408448,40960,44032,48128,51200,55296,58368,62464,65536,62464,65536,62464,65536,268439552,336991254,57,813830144,80,7488,192,1040137983,16780300,50465804,576,36,224,1113063424,1113063424,1113063424,1098907648,1098907648,0,436032,1073741824,435456,44032,44032,40960,2325504,1073741824,2322432,16980,1107575392,4294956716,4294967294,16980,1107575392,4294956716,4294967294,16980,1107575392,4294956716,4294967294,
2365440,2365440,2408448,2408448,2451456,2451456,2494464,2494464,2494464,2494464,2494464,2494464,40960,44032,48128,51200,55296,58368,62464,65536,62464,65536,62464,65536,268439552,336991254,57,1086459904,80,7488,192,1040137983,16780300,50465804,576,36,224,1113587712,1113587712,1113587712,1098907648,1098907648,0,452160,1073741824,451584,44032,44032,40960,2411520,1073741824,2408448,16988,1107575400,4294956268,4294967294,16988,1107575400,4294956268,4294967294,16988,1107575400,4294956268,4294967294,
15552,15552,15744,15744,15936,15936,16128,16128,16128,16128,16128,16128,40960,44032,48128,51200,55296,58368,62464,65536,62464,65536,62464,65536,268439552,336991254,410,1614946304,80,7984,43008,1048526463,50465804,16780300,36,80,224,1118044160,1118044160,1118044160,1098907648,1098907648,0,131976,1073741824,2952,44032,44032,40960,703872,1073741824,15744,17058,1107575464,4294950444,4294967294,17058,1107575464,4294950444,4294967294,17058,1107575464,4294950444,4294967294,
4429824,4429824,4472832,4472832,4515840,4515840,4558848,4558848,4558848,4558848,4558848,4558848,40960,44032,48128,51200,55296,58368,62464,65536,62464,65536,62464,65536,268439552,336991254,57,1888096256,80,7488,192,1048526591,16780300,50465804,576,36,224,1120927744,1120927744,1120927744,1098907648,1098907648,0,839232,1073741824,838656,44032,44032,40960,4475904,1073741824,4472832,17102,1107575508,4294945516,4294967294,17102,1107575508,4294945516,4294967294,17102,1107575508,4294945516,4294967294,
1204224,1247232,1290240,1333248,1376256,1419264,9590784,9590784,9590784,9590784,9590784,9590784,49152,53248,57344,61440,65536,69632,40960,45056,45056,45056,45056,45056,2155872256,320017685,57,2161520640,80,7488,192,1048526591,16780300,50465804,576,36,128,1130233856,1130233856,1106247680,1124073472,1124073472,0,1794816,1073741824,241920,32768,32768,53248,9572352,1073741824,1290240,1123959637,1125139295,4294939276,126,1123959637,1125139295,4294939276,126,16864,1099973124,4294965260,4294967294,
33216,33216,33408,33408,33600,33600,33792,33792,33792,33792,33792,33792,40960,44032,48128,51200,55296,58368,62464,65536,62464,65536,62464,65536,268439552,336991254,410,2690785280,80,7984,43008,1056915071,50465804,16780300,36,80,224,1127088128,1127088128,1127088128,1098907648,1098907648,0,135288,1073741824,6264,44032,44032,40960,721536,1073741824,33408,17197,1107575600,4294929836,4294967294,17197,1107575600,4294929836,4294967294,17197,1107575600,4294929836,4294967294,
8042496,8085504,8128512,8171520,8214528,8257536,8300544,8343552,8343552,8343552,8343552,8343552,40960,45056,49152,53248,57344,61440,65536,69632,69632,69632,69632,69632,538976288,353703189,57,2963931392,80,7488,192,1056915199,16780300,50465804,576,36,128,1128267776,1128136704,1128005632,1107296256,1107296256,1107296256,1549440,1533312,1517184,57344,49152,40960,8263680,8177664,8091648,1106264891,1112032066,4294944460,30,1106264891,1112032066,4294944460,30,1106264891,1112032066,4294944460,30,
42048,42048,42240,42240,42432,42432,42624,42624,42624,42624,42624,42624,40960,44032,48128,51200,55296,58368,62464,65536,62464,65536,62464,65536,268439552,336991254,410,3493470208,80,7984,43008,1056914943,50465804,16780300,36,80,224,1130102784,1130102784,1130102784,1098907648,1098907648,0,136944,1073741824,7920,44032,44032,40960,730368,1073741824,42240,17243,1107575646,4294919532,4294967294,17243,1107575646,4294919532,4294967294,17243,1107575646,4294919532,4294967294,
1849344,1892352,1935360,1978368,2021376,2064384,2107392,2150400,2150400,2150400,2150400,2150400,40960,45056,49152,53248,57344,61440,65536,69632,69632,69632,69632,69632,538976288,353703189,57,814350592,80,7488,192,1040137983,16780300,50465804,576,36,128,1111490560,1110966272,1110441984,1107296256,1107296256,1107296256,388224,372096,355968,57344,49152,40960,2070528,1984512,1898496,1106264620,1112031816,4294962892,30,1106264620,1112031816,4294962892,30,1106264620,1112031816,4294962892,30,
2451456,2451456,2494464,2494464,2537472,2537472,2580480,2580480,2580480,2580480,2580480,2580480,40960,44032,48128,51200,55296,58368,62464,65536,62464,65536,62464,65536,268439552,336991254,57,1086984192,80,7488,192,1040137983,16780300,50465804,576,36,224,1114112000,1114112000,1114112000,1098907648,1098907648,0,468288,1073741824,467712,44032,44032,40960,2497536,1073741824,2494464,16996,1107575408,4294955820,4294967294,16996,1107575408,4294955820,4294967294,16996,1107575408,4294955820,4294967294,
15936,15936,16128,16128,16320,16320,16512,16512,16512,16512,16512,16512,40960,44032,48128,51200,55296,58368,62464,65536,62464,65536,62464,65536,268439552,336991254,410,1615470592,80,7984,43008,1048526463,50465804,16780300,36,80,224,1118306304,1118306304,1118306304,1098907648,1098907648,0,132048,1073741824,3024,44032,44032,40960,704256,1073741824,16128,17062,1107575468,4294949996,4294967294,17062,1107575468,4294949996,4294967294,17062,1107575468,4294949996,4294967294,
4515840,4515840,4558848,4558848,4601856,4601856,4644864,4644864,4644864,4644864,4644864,4644864,40960,44032,48128,51200,55296,58368,62464,65536,62464,65536,62464,65536,268439552,336991254,57,1888620544,80,7488,192,1048526591,16780300,50465804,576,36,224,1121189888,1121189888,1121189888,1098907648,1098907648,0,855360,1073741824,854784,44032,44032,40960,4561920,1073741824,4558848,17106,1107575512,4294945068,4294967294,17106,1107575512,4294945068,4294967294,17106,1107575512,4294945068,4294967294,
1290240,1290240,1333248,1333248,1376256,1376256,1419264,1419264,1462272,1462272,1505280,1505280,40960,44032,48128,51200,55296,58368,62464,65536,69632,72704,76800,79872,1615880272,336991254,57,2418154496,64,7488,192,1056914943,16780300,50465804,576,36,224,1107296256,1107296256,1107296256,1119879168,1119879168,1117782016,261504,1073741824,260928,51200,51200,48128,1394688,1073741824,1391616,1117536752,1122124300,4294961388,78,1117536752,1122124300,4294961388,78,1117536752,1122124300,4294961388,78,
4608,4608,4800,4800,4992,4992,5184,5184,5376,5376,5568,5568,40960,44032,48128,51200,55296,58368,62464,65536,69632,72704,76800,79872,268439552,336991254,410,2691312640,64,7984,43008,1056915071,50465804,16780300,36,80,224,1104150528,1104150528,1104150528,1098907648,1098907648,0,129960,1073741824,936,51200,51200,48128,693120,1073741824,4992,16832,1107575272,4294963212,4294967294,16832,1107575272,4294963212,4294967294,16832,1107575272,4294963212,4294967294,
4992,4992,5184,5184,5376,5376,5568,5568,5760,5760,5952,5952,40960,44032,48128,51200,55296,58368,62464,65536,69632,72704,76800,79872,268439552,336991254,410,2964466688,64,7984,43008,1056915199,50465804,16780300,36,80,224,1105199104,1105199104,1105199104,1098907648,1098907648,0,130032,1073741824,1008,51200,51200,48128,693504,1073741824,5376,16848,1107575288,4294962764,4294967294,16848,1107575288,4294962764,4294967294,16848,1107575288,4294962764,4294967294,
42432,42432,42624,42624,42816,42816,42816,42816,42816,42816,42816,42816,40960,44032,48128,51200,55296,58368,62464,65536,62464,65536,62464,65536,268439552,336991254,410,3493994496,80,7984,43008,1056915071,50465804,16780300,36,80,224,1130233856,1130233856,1130233856,1098907648,1098907648,0,137016,1073741824,7992,44032,44032,40960,730752,1073741824,42624,17245,1107575647,4294919084,4294967294,17245,1107575647,4294919084,4294967294,17245,1107575647,4294919084,4294967294,
2688,2688,2880,2880,3072,3072,3264,3264,3456,3456,3648,3648,40960,44032,48128,51200,55296,58368,62464,65536,69632,72704,76800,79872,268439552,336991254,410,814886400,64,7984,43008,1040137983,50465804,16780300,36,80,224,1098907648,1098907648,1098907648,1098907648,1098907648,0,129600,1073741824,576,51200,51200,48128,691200,1073741824,3072,16736,1107575192,4294965452,4294967294,16736,1107575192,4294965452,4294967294,16736,1107575192,4294965452,4294967294,
3139584,3182592,3225600,3268608,3311616,3354624,3397632,3440640,3440640,3440640,3440640,3440640,40960,45056,49152,53248,57344,61440,65536,69632,69632,69632,69632,69632,538976288,353703189,57,1087504640,80,7488,192,1040137983,16780300,50465804,576,36,128,1117519872,1117257728,1116995584,1107296256,1107296256,1107296256,630144,614016,597888,57344,49152,40960,3360768,3274752,3188736,1106264722,1112031904,4294959052,30,1106264722,1112031904,4294959052,30,1106264722,1112031904,4294959052,30,
16320,16320,16512,16512,16704,16704,16896,16896,16896,16896,16896,16896,40960,44032,48128,51200,55296,58368,62464,65536,62464,65536,62464,65536,268439552,336991254,410,1615994880,80,7984,43008,1048526463,50465804,16780300,36,80,224,1118568448,1118568448,1118568448,1098907648,1098907648,0,132120,1073741824,3096,44032,44032,40960,704640,1073741824,16512,17066,1107575472,4294949548,4294967294,17066,1107575472,4294949548,4294967294,17066,1107575472,4294949548,4294967294,
4601856,4601856,4644864,4644864,4687872,4687872,4730880,4730880,4730880,4730880,4730880,4730880,40960,44032,48128,51200,55296,58368,62464,65536,62464,65536,62464,65536,268439552,336991254,57,1889144832,80,7488,192,1048526591,16780300,50465804,576,36,224,1121452032,1121452032,1121452032,1098907648,1098907648,0,871488,1073741824,870912,44032,44032,40960,4647936,1073741824,4644864,17110,1107575516,4294944620,4294967294,17110,1107575516,4294944620,4294967294,17110,1107575516,4294944620,4294967294,
4224,4224,4416,4416,4608,4608,4800,4800,4992,4992,5184,5184,40960,44032,48128,51200,55296,58368,62464,65536,69632,72704,76800,79872,268439552,336991254,410,2418682880,64,7984,43008,1056914943,50465804,16780300,36,80,224,1103101952,1103101952,1103101952,1098907648,1098907648,0,129888,1073741824,864,51200,51200,48128,692736,1073741824,4608,16816,1107575256,4294963660,4294967294,16816,1107575256,4294963660,4294967294,16816,1107575256,4294963660,4294967294,
1290240,1290240,1333248,1333248,1376256,1376256,1419264,1419264,1462272,1462272,1505280,1505280,40960,44032,48128,51200,55296,58368,62464,65536,69632,72704,76800,79872,268439552,336991254,57,2691832832,64,7488,192,1056915071,16780300,50465804,576,36,224,1107296256,1107296256,1107296256,1098907648,1098907648,0,258624,1073741824,258048,51200,51200,48128,1379328,1073741824,1376256,16880,1107575308,4294961868,4294967294,16880,1107575308,4294961868,4294967294,16880,1107575308,4294961868,4294967294,
5376,5376,5568,5568,5760,5760,5952,5952,6144,6144,6336,6336,40960,44032,48128,51200,55296,58368,62464,65536,69632,72704,76800,79872,268439552,336991254,410,2964990976,64,7984,43008,1056915199,50465804,16780300,36,80,224,1106247680,1106247680,1106247680,1098907648,1098907648,0,130104,1073741824,1080,51200,51200,48128,693888,1073741824,5760,16864,1107575300,4294962316,4294967294,16864,1107575300,4294962316,4294967294,16864,1107575300,4294962316,4294967294,
1290240,1290240,1333248,1333248,1376256,1376256,1419264,1419264,1462272,1462272,1505280,1505280,40960,44032,48128,51200,55296,58368,62464,65536,69632,72704,76800,79872,2154856560,336991254,57,3494517760,64,7488,192,1056915071,16780300,50465804,576,36,224,1107296256,1107296256,1107296256,1124073472,1124073472,1121976320,262656,1073741824,262080,51200,51200,48128,1400832,1073741824,1397760,1121731056,1125204492,4294961196,110,1121731056,1125204492,4294961196,110,1121731056,1125204492,4294961196,110,
6336,6336,6528,6528,6720,6720,6912,6912,6912,6912,6912,6912,40960,44032,48128,51200,55296,58368,62464,65536,62464,65536,62464,65536,268439552,336991254,410,815407104,80,7984,43008,1040137983,50465804,16780300,36,80,224,1107820544,1107820544,1107820544,1098907648,1098907648,0,130248,1073741824,1224,44032,44032,40960,694656,1073741824,6528,16900,1107575312,4294961196,4294967294,16900,1107575312,4294961196,4294967294,16900,1107575312,4294961196,4294967294,
2537472,2537472,2580480,2580480,2623488,2623488,2666496,2666496,2666496,2666496,2666496,2666496,40960,44032,48128,51200,55296,58368,62464,65536,62464,65536,62464,65536,268439552,336991254,57,1088032768,80,7488,192,1040137983,16780300,50465804,576,36,224,1114636288,1114636288,1114636288,1098907648,1098907648,0,484416,1073741824,483840,44032,44032,40960,2583552,1073741824,2580480,17004,1107575416,4294955372,4294967294,17004,1107575416,4294955372,4294967294,17004,1107575416,4294955372,4294967294,
16704,16704,16896,16896,17088,17088,17280,17280,17280,17280,17280,17280,40960,44032,48128,51200,55296,58368,62464,65536,62464,65536,62464,65536,268439552,336991254,410,1616519168,80,7984,43008,1048526463,50465804,16780300,36,80,224,1118830592,1118830592,1118830592,1098907648,1098907648,0,132192,1073741824,3168,44032,44032,40960,705024,1073741824,16896,17070,1107575476,4294949100,4294967294,17070,1107575476,4294949100,4294967294,17070,1107575476,4294949100,4294967294,
4687872,4687872,4730880,4730880,4773888,4773888,4816896,4816896,4816896,4816896,4816896,4816896,40960,44032,48128,51200,55296,58368,62464,65536,62464,65536,62464,65536,268439552,336991254,57,1889669120,80,7488,192,1048526591,16780300,50465804,576,36,224,1121714176,1121714176,1121714176,1098907648,1098907648,0,887616,1073741824,887040,44032,44032,40960,4733952,1073741824,4730880,17114,1107575520,4294944172,4294967294,17114,1107575520,4294944172,4294967294,17114,1107575520,4294944172,4294967294,
24384,24384,24576,24576,24768,24768,24960,24960,24960,24960,24960,24960,40960,44032,48128,51200,55296,58368,62464,65536,62464,65536,62464,65536,268439552,336991254,410,2419204096,80,7984,43008,1056914943,50465804,16780300,36,80,224,1124073472,1124073472,1124073472,1098907648,1098907648,0,133632,1073741824,4608,44032,44032,40960,712704,1073741824,24576,17150,1107575554,4294940140,4294967294,17150,1107575554,4294940140,4294967294,17150,1107575554,4294940140,4294967294,
6494208,6494208,6537216,6537216,6580224,6580224,6623232,6623232,6623232,6623232,6623232,6623232,40960,44032,48128,51200,55296,58368,62464,65536,62464,65536,62464,65536,268439552,336991254,57,2692354048,80,7488,192,1056915071,16780300,50465804,576,36,224,1125646336,1125646336,1125646336,1098907648,1098907648,0,1226304,1073741824,1225728,44032,44032,40960,6540288,1073741824,6537216,17175,1107575578,4294934764,4294967294,17175,1107575578,4294934764,4294967294,17175,1107575578,4294934764,4294967294,
33600,33600,33792,33792,33984,33984,34176,34176,34176,34176,34176,34176,40960,44032,48128,51200,55296,58368,62464,65536,62464,65536,62464,65536,268439552,336991254,410,2965512192,80,7984,43008,1056915199,50465804,16780300,36,80,224,1127219200,1127219200,1127219200,1098907648,1098907648,0,135360,1073741824,6336,44032,44032,40960,721920,1073741824,33792,17199,1107575602,4294929388,4294967294,17199,1107575602,4294929388,4294967294,17199,1107575602,4294929388,4294967294,
5760,5760,5952,5952,6144,6144,6336,6336,6528,6528,6720,6720,40960,44032,48128,51200,55296,58368,62464,65536,69632,72704,76800,79872,268439552,336991254,410,3495046144,64,7984,43008,1056915071,50465804,16780300,36,80,224,1107296256,1107296256,1107296256,1098907648,1098907648,0,130176,1073741824,1152,51200,51200,48128,694272,1073741824,6144,16880,1107575308,4294961868,4294967294,16880,1107575308,4294961868,4294967294,16880,1107575308,4294961868,4294967294,
6720,6720,6912,6912,7104,7104,7296,7296,7296,7296,7296,7296,40960,44032,48128,51200,55296,58368,62464,65536,62464,65536,62464,65536,268439552,336991254,410,815931392,80,7984,43008,1040137983,50465804,16780300,36,80,224,1108344832,1108344832,1108344832,1098907648,1098907648,0,130320,1073741824,1296,44032,44032,40960,695040,1073741824,6912,16908,1107575320,4294960748,4294967294,16908,1107575320,4294960748,4294967294,16908,1107575320,4294960748,4294967294,
2623488,2623488,2666496,2666496,2709504,2709504,2752512,2752512,2752512,2752512,2752512,2752512,40960,44032,48128,51200,55296,58368,62464,65536,62464,65536,62464,65536,268439552,336991254,57,1344409600,80,7488,192,1048526335,16780300,50465804,576,36,224,1115160576,1115160576,1115160576,1098907648,1098907648,0,500544,1073741824,499968,44032,44032,40960,2669568,1073741824,2666496,17012,1107575424,4294954924,4294967294,17012,1107575424,4294954924,4294967294,17012,1107575424,4294954924,4294967294,
17088,17088,17280,17280,17472,17472,17664,17664,17664,17664,17664,17664,40960,44032,48128,51200,55296,58368,62464,65536,62464,65536,62464,65536,268439552,336991254,410,1617043456,80,7984,43008,1048526463,50465804,16780300,36,80,224,1119092736,1119092736,1119092736,1098907648,1098907648,0,132264,1073741824,3240,44032,44032,40960,705408,1073741824,17280,17074,1107575480,4294948652,4294967294,17074,1107575480,4294948652,4294967294,17074,1107575480,4294948652,4294967294,
4773888,4773888,4816896,4816896,4859904,4859904,4902912,4902912,4902912,4902912,4902912,4902912,40960,44032,48128,51200,55296,58368,62464,65536,62464,65536,62464,65536,268439552,336991254,57,1890193408,80,7488,192,1048526591,16780300,50465804,576,36,224,1121976320,1121976320,1121976320,1098907648,1098907648,0,903744,1073741824,903168,44032,44032,40960,4819968,1073741824,4816896,17118,1107575524,4294943724,4294967294,17118,1107575524,4294943724,4294967294,17118,1107575524,4294943724,4294967294,
6236160,6279168,6322176,6365184,6408192,6451200,6494208,6537216,6537216,6537216,6537216,6537216,40960,45056,49152,53248,57344,61440,65536,69632,69632,69632,69632,69632,538976288,353703189,57,2419720448,80,7488,192,1056914943,16780300,50465804,576,36,128,1125515264,1125384192,1125253120,1107296256,1107296256,1107296256,1210752,1194624,1178496,57344,49152,40960,6457344,6371328,6285312,1106264849,1112032024,4294949836,30,1106264849,1112032024,4294949836,30,1106264849,1112032024,4294949836,30,
6580224,6580224,6623232,6623232,6666240,6666240,6709248,6709248,6709248,6709248,6709248,6709248,40960,44032,48128,51200,55296,58368,62464,65536,62464,65536,62464,65536,268439552,336991254,57,2692878336,80,7488,192,1056915199,16780300,50465804,576,36,224,1125777408,1125777408,1125777408,1098907648,1098907648,0,1242432,1073741824,1241856,44032,44032,40960,6626304,1073741824,6623232,17177,1107575580,4294934316,4294967294,17177,1107575580,4294934316,4294967294,17177,1107575580,4294934316,4294967294,
344064,387072,430080,473088,516096,559104,9074688,9117696,9117696,9117696,9117696,9117696,49152,53248,57344,61440,65536,69632,40960,45056,45056,45056,45056,45056,2964324352,320017685,57,2966041088,80,7488,192,1056915199,16780300,50465804,576,36,128,1129447424,1129447424,1092616192,1127219200,1127219200,0,1699776,1073741824,80640,32768,32768,53248,9065472,1073741824,430080,1127170889,1128285012,4294940524,174,1127170889,1128285012,4294940524,174,16640,1099972944,524,4294967294,
8558592,8558592,8601600,8601600,8644608,8644608,8687616,8687616,8687616,8687616,8687616,8687616,40960,44032,48128,51200,55296,58368,62464,65536,62464,65536,62464,65536,268439552,336991254,57,3495563264,80,7488,192,1056915071,16780300,50465804,576,36,224,1128792064,1128792064,1128792064,1098907648,1098907648,0,1613376,1073741824,1612800,44032,44032,40960,8604672,1073741824,8601600,17223,1107575626,4294924012,4294967294,17223,1107575626,4294924012,4294967294,17223,1107575626,4294924012,4294967294,
7104,7104,7296,7296,7488,7488,7680,7680,7680,7680,7680,7680,40960,44032,48128,51200,55296,58368,62464,65536,62464,65536,62464,65536,268439552,336991254,410,816455680,80,7984,43008,1040137983,50465804,16780300,36,80,224,1108869120,1108869120,1108869120,1098907648,1098907648,0,130392,1073741824,1368,44032,44032,40960,695424,1073741824,7296,16916,1107575328,4294960300,4294967294,16916,1107575328,4294960300,4294967294,16916,1107575328,4294960300,4294967294,
2709504,2709504,2752512,2752512,2795520,2795520,2838528,2838528,2838528,2838528,2838528,2838528,40960,44032,48128,51200,55296,58368,62464,65536,62464,65536,62464,65536,268439552,336991254,57,1344933888,80,7488,192,1048526335,16780300,50465804,576,36,224,1115684864,1115684864,1115684864,1098907648,1098907648,0,516672,1073741824,516096,44032,44032,40960,2755584,1073741824,2752512,17020,1107575428,4294954476,4294967294,17020,1107575428,4294954476,4294967294,17020,1107575428,4294954476,4294967294,
17472,17472,17664,17664,17856,17856,18048,18048,18048,18048,18048,18048,40960,44032,48128,51200,55296,58368,62464,65536,62464,65536,62464,65536,268439552,336991254,410,1617567744,80,7984,43008,1048526463,50465804,16780300,36,80,224,1119354880,1119354880,1119354880,1098907648,1098907648,0,132336,1073741824,3312,44032,44032,40960,705792,1073741824,17664,17078,1107575484,4294948204,4294967294,17078,1107575484,4294948204,4294967294,17078,1107575484,4294948204,4294967294,
4859904,4859904,4902912,4902912,4945920,4945920,4988928,4988928,4988928,4988928,4988928,4988928,40960,44032,48128,51200,55296,58368,62464,65536,62464,65536,62464,65536,268439552,336991254,57,1890717696,80,7488,192,1048526591,16780300,50465804,576,36,224,1122238464,1122238464,1122238464,1098907648,1098907648,0,919872,1073741824,919296,44032,44032,40960,4905984,1073741824,4902912,17122,1107575528,4294943276,4294967294,17122,1107575528,4294943276,4294967294,17122,1107575528,4294943276,4294967294,
24768,24768,24960,24960,25152,25152,25344,25344,25344,25344,25344,25344,40960,44032,48128,51200,55296,58368,62464,65536,62464,65536,62464,65536,268439552,336991254,410,2420252672,80,7984,43008,1056915071,50465804,16780300,36,80,224,1124204544,1124204544,1124204544,1098907648,1098907648,0,133704,1073741824,4680,44032,44032,40960,713088,1073741824,24960,17153,1107575556,4294939692,4294967294,17153,1107575556,4294939692,4294967294,17153,1107575556,4294939692,4294967294,
6666240,6666240,6709248,6709248,6752256,6752256,6795264,6795264,6795264,6795264,6795264,6795264,40960,44032,48128,51200,55296,58368,62464,65536,62464,65536,62464,65536,268439552,336991254,57,2693402624,80,7488,192,1056915199,16780300,50465804,576,36,224,1125908480,1125908480,1125908480,1098907648,1098907648,0,1258560,1073741824,1257984,44032,44032,40960,6712320,1073741824,6709248,17179,1107575582,4294933868,4294967294,17179,1107575582,4294933868,4294967294,17179,1107575582,4294933868,4294967294,
1290240,1333248,1376256,1419264,1462272,9504768,9547776,9590784,9590784,9590784,9590784,9590784,57344,61440,65536,69632,73728,40960,45056,49152,53248,53248,53248,53248,2964369584,353703189,57,2966684928,64,7488,192,1056915199,16780300,50465804,576,36,128,1130102784,1130233856,1107296256,1127219200,1127219200,1127219200,1780416,1796544,264384,32768,40960,61440,9495552,9581568,1410048,1127170899,1128285023,4294939244,174,1127170899,1128285023,4294939244,174,1127105008,1128415752,4294964204,174,
9074688,9117696,9160704,9203712,9246720,9289728,9332736,9375744,9375744,9375744,9375744,9375744,40960,45056,49152,53248,57344,61440,65536,69632,69632,69632,69632,69632,538976288,353703189,57,3496083712,80,7488,192,1056915071,16780300,50465804,576,36,128,1129840640,1129709568,1129578496,1107296256,1107296256,1107296256,1742976,1726848,1710720,57344,49152,40960,9295872,9209856,9123840,1106264915,1112032090,4294941388,30,1106264915,1112032090,4294941388,30,1106264915,1112032090,4294941388,30,
7488,7488,7680,7680,7872,7872,8064,8064,8064,8064,8064,8064,40960,44032,48128,51200,55296,58368,62464,65536,62464,65536,62464,65536,268439552,336991254,410,816979968,80,7984,43008,1040137983,50465804,16780300,36,80,224,1109393408,1109393408,1109393408,1098907648,1098907648,0,130464,1073741824,1440,44032,44032,40960,695808,1073741824,7680,16924,1107575336,4294959852,4294967294,16924,1107575336,4294959852,4294967294,16924,1107575336,4294959852,4294967294,
2795520,2795520,2838528,2838528,2881536,2881536,2924544,2924544,2924544,2924544,2924544,2924544,40960,44032,48128,51200,55296,58368,62464,65536,62464,65536,62464,65536,268439552,336991254,57,1345458176,80,7488,192,1048526335,16780300,50465804,576,36,224,1115947008,1115947008,1115947008,1098907648,1098907648,0,532800,1073741824,532224,44032,44032,40960,2841600,1073741824,2838528,17026,1107575432,4294954028,4294967294,17026,1107575432,4294954028,4294967294,17026,1107575432,4294954028,4294967294,
4429824,4472832,4515840,4558848,4601856,4644864,4687872,4730880,4730880,4730880,4730880,4730880,40960,45056,49152,53248,57344,61440,65536,69632,69632,69632,69632,69632,538976288,353703189,57,1618084096,80,7488,192,1048526463,16780300,50465804,576,36,128,1121452032,1121189888,1120927744,1107296256,1107296256,1107296256,872064,855936,839808,57344,49152,40960,4651008,4564992,4478976,1106264782,1112031964,4294955212,30,1106264782,1112031964,4294955212,30,1106264782,1112031964,4294955212,30,
4945920,4945920,4988928,4988928,5031936,5031936,5074944,5074944,5074944,5074944,5074944,5074944,40960,44032,48128,51200,55296,58368,62464,65536,62464,65536,62464,65536,268439552,336991254,57,1891241984,80,7488,192,1048526591,16780300,50465804,576,36,224,1122500608,1122500608,1122500608,1098907648,1098907648,0,936000,1073741824,935424,44032,44032,40960,4992000,1073741824,4988928,17126,1107575532,4294942828,4294967294,17126,1107575532,4294942828,4294967294,17126,1107575532,4294942828,4294967294,
25152,25152,25344,25344,25536,25536,25728,25728,25728,25728,25728,25728,40960,44032,48128,51200,55296,58368,62464,65536,62464,65536,62464,65536,268439552,336991254,410,2420776960,80,7984,43008,1056915071,50465804,16780300,36,80,224,1124335616,1124335616,1124335616,1098907648,1098907648,0,133776,1073741824,4752,44032,44032,40960,713472,1073741824,25344,17155,1107575558,4294939244,4294967294,17155,1107575558,4294939244,4294967294,17155,1107575558,4294939244,4294967294,
7268352,7311360,7354368,7397376,7440384,7483392,7526400,7569408,7569408,7569408,7569408,7569408,40960,45056,49152,53248,57344,61440,65536,69632,69632,69632,69632,69632,538976288,353703189,57,2693923072,80,7488,192,1056915199,16780300,50465804,576,36,128,1127088128,1126957056,1126825984,1107296256,1107296256,1107296256,1404288,1388160,1372032,57344,49152,40960,7489536,7403520,7317504,1106264873,1112032048,4294946764,30,1106264873,1112032048,4294946764,30,1106264873,1112032048,4294946764,30,
33984,33984,34176,34176,34368,34368,34560,34560,34560,34560,34560,34560,40960,44032,48128,51200,55296,58368,62464,65536,62464,65536,62464,65536,268439552,336991254,410,3223461888,80,7984,43008,1056914943,50465804,16780300,36,80,224,1127350272,1127350272,1127350272,1098907648,1098907648,0,135432,1073741824,6408,44032,44032,40960,722304,1073741824,34176,17201,1107575604,4294928940,4294967294,17201,1107575604,4294928940,4294967294,17201,1107575604,4294928940,4294967294,
8644608,8644608,8687616,8687616,8730624,8730624,8773632,8773632,8773632,8773632,8773632,8773632,40960,44032,48128,51200,55296,58368,62464,65536,62464,65536,62464,65536,268439552,336991254,57,3496611840,80,7488,192,1056915071,16780300,50465804,576,36,224,1128923136,1128923136,1128923136,1098907648,1098907648,0,1629504,1073741824,1628928,44032,44032,40960,8690688,1073741824,8687616,17225,1107575628,4294923564,4294967294,17225,1107575628,4294923564,4294967294,17225,1107575628,4294923564,4294967294,
2107392,2150400,2193408,2236416,2279424,2322432,2365440,2408448,2408448,2408448,2408448,2408448,40960,45056,49152,53248,57344,61440,65536,69632,69632,69632,69632,69632,538976288,353703189,57,817496320,80,7488,192,1040137983,16780300,50465804,576,36,128,1113063424,1112539136,1112014848,1107296256,1107296256,1107296256,436608,420480,404352,57344,49152,40960,2328576,2242560,2156544,1106264644,1112031840,4294962124,30,1106264644,1112031840,4294962124,30,1106264644,1112031840,4294962124,30,
2881536,2881536,2924544,2924544,2967552,2967552,3010560,3010560,3010560,3010560,3010560,3010560,40960,44032,48128,51200,55296,58368,62464,65536,62464,65536,62464,65536,268439552,336991254,57,1345982464,80,7488,192,1048526335,16780300,50465804,576,36,224,1116209152,1116209152,1116209152,1098907648,1098907648,0,548928,1073741824,548352,44032,44032,40960,2927616,1073741824,2924544,17030,1107575436,4294953580,4294967294,17030,1107575436,4294953580,4294967294,17030,1107575436,4294953580,4294967294,
17856,17856,18048,18048,18240,18240,18432,18432,18432,18432,18432,18432,40960,44032,48128,51200,55296,58368,62464,65536,62464,65536,62464,65536,268439552,336991254,410,1618616320,80,7984,43008,1048526463,50465804,16780300,36,80,224,1119617024,1119617024,1119617024,1098907648,1098907648,0,132408,1073741824,3384,44032,44032,40960,706176,1073741824,18048,17082,1107575488,4294947756,4294967294,17082,1107575488,4294947756,4294967294,17082,1107575488,4294947756,4294967294,
774144,817152,860160,903168,946176,989184,8730624,8773632,8773632,8773632,8773632,8773632,49152,53248,57344,61440,65536,69632,40960,45056,45056,45056,45056,45056,1886388224,320017685,57,1891774464,80,7488,192,1048526591,16780300,50465804,576,36,128,1128923136,1128923136,1101004800,1121976320,1121976320,0,1632960,1073741824,161280,32768,32768,53248,8709120,1073741824,860160,1121862465,1124090700,4294941932,110,1121862465,1124090700,4294941932,110,16784,1099973048,4294966540,4294967294,
25536,25536,25728,25728,25920,25920,26112,26112,26112,26112,26112,26112,40960,44032,48128,51200,55296,58368,62464,65536,62464,65536,62464,65536,268439552,336991254,410,2421301248,80,7984,43008,1056915071,50465804,16780300,36,80,224,1124466688,1124466688,1124466688,1098907648,1098907648,0,133848,1073741824,4824,44032,44032,40960,713856,1073741824,25728,17157,1107575560,4294938796,4294967294,17157,1107575560,4294938796,4294967294,17157,1107575560,4294938796,4294967294,
6752256,6752256,6795264,6795264,6838272,6838272,6881280,6881280,6881280,6881280,6881280,6881280,40960,44032,48128,51200,55296,58368,62464,65536,62464,65536,62464,65536,268439552,336991254,57,2694451200,80,7488,192,1056915199,16780300,50465804,576,36,224,1126039552,1126039552,1126039552,1098907648,1098907648,0,1274688,1073741824,1274112,44032,44032,40960,6798336,1073741824,6795264,17181,1107575584,4294933420,4294967294,17181,1107575584,4294933420,4294967294,17181,1107575584,4294933420,4294967294,
34368,34368,34560,34560,34752,34752,34944,34944,34944,34944,34944,34944,40960,44032,48128,51200,55296,58368,62464,65536,62464,65536,62464,65536,268439552,336991254,410,3223986176,80,7984,43008,1056914943,50465804,16780300,36,80,224,1127481344,1127481344,1127481344,1098907648,1098907648,0,135504,1073741824,6480,44032,44032,40960,722688,1073741824,34560,17203,1107575606,4294928492,4294967294,17203,1107575606,4294928492,4294967294,17203,1107575606,4294928492,4294967294,
8730624,8730624,8773632,8773632,8816640,8816640,8859648,8859648,8859648,8859648,8859648,8859648,40960,44032,48128,51200,55296,58368,62464,65536,62464,65536,62464,65536,268439552,336991254,57,3497136128,80,7488,192,1056915071,16780300,50465804,576,36,224,1129054208,1129054208,1129054208,1098907648,1098907648,0,1645632,1073741824,1645056,44032,44032,40960,8776704,1073741824,8773632,17227,1107575630,4294923116,4294967294,17227,1107575630,4294923116,4294967294,17227,1107575630,4294923116,4294967294,
7872,7872,8064,8064,8256,8256,8448,8448,8448,8448,8448,8448,40960,44032,48128,51200,55296,58368,62464,65536,62464,65536,62464,65536,268439552,336991254,410,818028544,80,7984,43008,1040137983,50465804,16780300,36,80,224,1109917696,1109917696,1109917696,1098907648,1098907648,0,130536,1073741824,1512,44032,44032,40960,696192,1073741824,8064,16932,1107575344,4294959404,4294967294,16932,1107575344,4294959404,4294967294,16932,1107575344,4294959404,4294967294,
2967552,2967552,3010560,3010560,3053568,3053568,3096576,3096576,3096576,3096576,3096576,3096576,40960,44032,48128,51200,55296,58368,62464,65536,62464,65536,62464,65536,268439552,336991254,57,1346506752,80,7488,192,1048526463,16780300,50465804,576,36,224,1116471296,1116471296,1116471296,1098907648,1098907648,0,565056,1073741824,564480,44032,44032,40960,3013632,1073741824,3010560,17034,1107575440,4294953132,4294967294,17034,1107575440,4294953132,4294967294,17034,1107575440,4294953132,4294967294,
18240,18240,18432,18432,18624,18624,18816,18816,18816,18816,18816,18816,40960,44032,48128,51200,55296,58368,62464,65536,62464,65536,62464,65536,268439552,336991254,410,1619140608,80,7984,43008,1048526591,50465804,16780300,36,80,224,1119879168,1119879168,1119879168,1098907648,1098907648,0,132480,1073741824,3456,44032,44032,40960,706560,1073741824,18432,17086,1107575492,4294947308,4294967294,17086,1107575492,4294947308,4294967294,17086,1107575492,4294947308,4294967294,
860160,903168,946176,989184,1032192,1075200,9160704,9203712,9203712,9203712,9203712,9203712,49152,53248,57344,61440,65536,69632,40960,45056,45056,45056,45056,45056,1886388224,320017685,57,1892429824,80,7488,192,1048526591,16780300,50465804,576,36,128,1129578496,1129578496,1102053376,1121976320,1121976320,0,1713600,1073741824,177408,32768,32768,53248,9139200,1073741824,946176,1121862475,1124090710,4294940652,110,1121862475,1124090710,4294940652,110,16800,1099973064,4294966284,4294967294,
25920,25920,26112,26112,26304,26304,26496,26496,26496,26496,26496,26496,40960,44032,48128,51200,55296,58368,62464,65536,62464,65536,62464,65536,268439552,336991254,410,2421825536,80,7984,43008,1056915071,50465804,16780300,36,80,224,1124597760,1124597760,1124597760,1098907648,1098907648,0,133920,1073741824,4896,44032,44032,40960,714240,1073741824,26112,17159,1107575562,4294938348,4294967294,17159,1107575562,4294938348,4294967294,17159,1107575562,4294938348,4294967294,
6838272,6838272,6881280,6881280,6924288,6924288,6967296,6967296,6967296,6967296,6967296,6967296,40960,44032,48128,51200,55296,58368,62464,65536,62464,65536,62464,65536,268439552,336991254,57,2694975488,80,7488,192,1056915199,16780300,50465804,576,36,224,1126170624,1126170624,1126170624,1098907648,1098907648,0,1290816,1073741824,1290240,44032,44032,40960,6884352,1073741824,6881280,17183,1107575586,4294932972,4294967294,17183,1107575586,4294932972,4294967294,17183,1107575586,4294932972,4294967294,
34752,34752,34944,34944,35136,35136,35328,35328,35328,35328,35328,35328,40960,44032,48128,51200,55296,58368,62464,65536,62464,65536,62464,65536,268439552,336991254,410,3224510464,80,7984,43008,1056914943,50465804,16780300,36,80,224,1127612416,1127612416,1127612416,1098907648,1098907648,0,135576,1073741824,6552,44032,44032,40960,723072,1073741824,34944,17205,1107575608,4294928044,4294967294,17205,1107575608,4294928044,4294967294,17205,1107575608,4294928044,4294967294,
8816640,8816640,8859648,8859648,8902656,8902656,8945664,8945664,8945664,8945664,8945664,8945664,40960,44032,48128,51200,55296,58368,62464,65536,62464,65536,62464,65536,268439552,336991254,57,3497660416,80,7488,192,1056915071,16780300,50465804,576,36,224,1129185280,1129185280,1129185280,1098907648,1098907648,0,1661760,1073741824,1661184,44032,44032,40960,8862720,1073741824,8859648,17229,1107575632,4294922668,4294967294,17229,1107575632,4294922668,4294967294,17229,1107575632,4294922668,4294967294,
8256,8256,8448,8448,8640,8640,8832,8832,8832,8832,8832,8832,40960,44032,48128,51200,55296,58368,62464,65536,62464,65536,62464,65536,268439552,336991254,410,818552832,80,7984,43008,1040137983,50465804,16780300,36,80,224,1110441984,1110441984,1110441984,1098907648,1098907648,0,130608,1073741824,1584,44032,44032,40960,696576,1073741824,8448,16940,1107575352,4294958956,4294967294,16940,1107575352,4294958956,4294967294,16940,1107575352,4294958956,4294967294,
3397632,3440640,3483648,3526656,3569664,3612672,3655680,3698688,3698688,3698688,3698688,3698688,40960,45056,49152,53248,57344,61440,65536,69632,69632,69632,69632,69632,538976288,353703189,57,1347027200,80,7488,192,1048526463,16780300,50465804,576,36,128,1118306304,1118044160,1117782016,1107296256,1107296256,1107296256,678528,662400,646272,57344,49152,40960,3618816,3532800,3446784,1106264734,1112031916,4294958284,30,1106264734,1112031916,4294958284,30,1106264734,1112031916,4294958284,30,
18624,18624,18816,18816,19008,19008,19200,19200,19200,19200,19200,19200,40960,44032,48128,51200,55296,58368,62464,65536,62464,65536,62464,65536,268439552,336991254,410,1619664896,80,7984,43008,1048526591,50465804,16780300,36,80,224,1120141312,1120141312,1120141312,1098907648,1098907648,0,132552,1073741824,3528,44032,44032,40960,706944,1073741824,18816,17090,1107575496,4294946860,4294967294,17090,1107575496,4294946860,4294967294,17090,1107575496,4294946860,4294967294,
946176,989184,1032192,1075200,1118208,1161216,9590784,9590784,9590784,9590784,9590784,9590784,49152,53248,57344,61440,65536,69632,40960,45056,45056,45056,45056,45056,1886388224,320017685,57,1893085184,80,7488,192,1048526591,16780300,50465804,576,36,128,1130233856,1130233856,1103101952,1121976320,1121976320,0,1794240,1073741824,193536,32768,32768,53248,9569280,1073741824,1032192,1121862485,1124090719,4294939372,110,1121862485,1124090719,4294939372,110,16816,1099973080,4294966028,4294967294,
26304,26304,26496,26496,26688,26688,26880,26880,26880,26880,26880,26880,40960,44032,48128,51200,55296,58368,62464,65536,62464,65536,62464,65536,268439552,336991254,410,2422349824,80,7984,43008,1056915071,50465804,16780300,36,80,224,1124728832,1124728832,1124728832,1098907648,1098907648,0,133992,1073741824,4968,44032,44032,40960,714624,1073741824,26496,17161,1107575564,4294937900,4294967294,17161,1107575564,4294937900,4294967294,17161,1107575564,4294937900,4294967294,
6924288,6924288,6967296,6967296,7010304,7010304,7053312,7053312,7053312,7053312,7053312,7053312,40960,44032,48128,51200,55296,58368,62464,65536,62464,65536,62464,65536,268439552,336991254,57,2695499776,80,7488,192,1056915199,16780300,50465804,576,36,224,1126301696,1126301696,1126301696,1098907648,1098907648,0,1306944,1073741824,1306368,44032,44032,40960,6970368,1073741824,6967296,17185,1107575588,4294932524,4294967294,17185,1107575588,4294932524,4294967294,17185,1107575588,4294932524,4294967294,
35136,35136,35328,35328,35520,35520,35712,35712,35712,35712,35712,35712,40960,44032,48128,51200,55296,58368,62464,65536,62464,65536,62464,65536,268439552,336991254,410,3225034752,80,7984,43008,1056914943,50465804,16780300,36,80,224,1127743488,1127743488,1127743488,1098907648,1098907648,0,135648,1073741824,6624,44032,44032,40960,723456,1073741824,35328,17207,1107575610,4294927596,4294967294,17207,1107575610,4294927596,4294967294,17207,1107575610,4294927596,4294967294,
8902656,8902656,8945664,8945664,8988672,8988672,9031680,9031680,9031680,9031680,9031680,9031680,40960,44032,48128,51200,55296,58368,62464,65536,62464,65536,62464,65536,268439552,336991254,57,3498184704,80,7488,192,1056915199,16780300,50465804,576,36,224,1129316352,1129316352,1129316352,1098907648,1098907648,0,1677888,1073741824,1677312,44032,44032,40960,8948736,1073741824,8945664,17231,1107575634,4294922220,4294967294,17231,1107575634,4294922220,4294967294,17231,1107575634,4294922220,4294967294,
8640,8640,8832,8832,9024,9024,9216,9216,9216,9216,9216,9216,40960,44032,48128,51200,55296,58368,62464,65536,62464,65536,62464,65536,268439552,336991254,410,819077120,80,7984,43008,1040137983,50465804,16780300,36,80,224,1110966272,1110966272,1110966272,1098907648,1098907648,0,130680,1073741824,1656,44032,44032,40960,696960,1073741824,8832,16948,1107575360,4294958508,4294967294,16948,1107575360,4294958508,4294967294,16948,1107575360,4294958508,4294967294,
3053568,3053568,3096576,3096576,3139584,3139584,3182592,3182592,3182592,3182592,3182592,3182592,40960,44032,48128,51200,55296,58368,62464,65536,62464,65536,62464,65536,268439552,336991254,57,1347555328,80,7488,192,1048526463,16780300,50465804,576,36,224,1116733440,1116733440,1116733440,1098907648,1098907648,0,581184,1073741824,580608,44032,44032,40960,3099648,1073741824,3096576,17038,1107575444,4294952684,4294967294,17038,1107575444,4294952684,4294967294,17038,1107575444,4294952684,4294967294,
19008,19008,19200,19200,19392,19392,19584,19584,19584,19584,19584,19584,40960,44032,48128,51200,55296,58368,62464,65536,62464,65536,62464,65536,268439552,336991254,410,1620189184,80,7984,43008,1048526591,50465804,16780300,36,80,224,1120403456,1120403456,1120403456,1098907648,1098907648,0,132624,1073741824,3600,44032,44032,40960,707328,1073741824,19200,17094,1107575500,4294946412,4294967294,17094,1107575500,4294946412,4294967294,17094,1107575500,4294946412,4294967294,
5462016,5505024,5548032,5591040,5634048,5677056,5720064,5763072,5763072,5763072,5763072,5763072,40960,45056,49152,53248,57344,61440,65536,69632,69632,69632,69632,69632,538976288,353703189,57,2149712128,80,7488,192,1048526335,16780300,50465804,576,36,128,1124335616,1124204544,1124073472,1107296256,1107296256,1107296256,1065600,1049472,1033344,57344,49152,40960,5683200,5597184,5511168,1106264830,1112032006,4294952140,30,1106264830,1112032006,4294952140,30,1106264830,1112032006,4294952140,30,
26688,26688,26880,26880,27072,27072,27264,27264,27264,27264,27264,27264,40960,44032,48128,51200,55296,58368,62464,65536,62464,65536,62464,65536,268439552,336991254,410,2422874112,80,7984,43008,1056915071,50465804,16780300,36,80,224,1124859904,1124859904,1124859904,1098907648,1098907648,0,134064,1073741824,5040,44032,44032,40960,715008,1073741824,26880,17163,1107575566,4294937452,4294967294,17163,1107575566,4294937452,4294967294,17163,1107575566,4294937452,4294967294,
7010304,7010304,7053312,7053312,7096320,7096320,7139328,7139328,7139328,7139328,7139328,7139328,40960,44032,48128,51200,55296,58368,62464,65536,62464,65536,62464,65536,268439552,336991254,57,2696024064,80,7488,192,1056915199,16780300,50465804,576,36,224,1126432768,1126432768,1126432768,1098907648,1098907648,0,1323072,1073741824,1322496,44032,44032,40960,7056384,1073741824,7053312,17187,1107575590,4294932076,4294967294,17187,1107575590,4294932076,4294967294,17187,1107575590,4294932076,4294967294,
8300544,8343552,8386560,8429568,8472576,8515584,8558592,8601600,8601600,8601600,8601600,8601600,40960,45056,49152,53248,57344,61440,65536,69632,69632,69632,69632,69632,538976288,353703189,57,3225551104,80,7488,192,1056915071,16780300,50465804,576,36,128,1128660992,1128529920,1128398848,1107296256,1107296256,1107296256,1597824,1581696,1565568,57344,49152,40960,8521728,8435712,8349696,1106264897,1112032072,4294943692,30,1106264897,1112032072,4294943692,30,1106264897,1112032072,4294943692,30,
8988672,8988672,9031680,9031680,9074688,9074688,9117696,9117696,9117696,9117696,9117696,9117696,40960,44032,48128,51200,55296,58368,62464,65536,62464,65536,62464,65536,268439552,336991254,57,3498708992,80,7488,192,1056915199,16780300,50465804,576,36,224,1129447424,1129447424,1129447424,1098907648,1098907648,0,1694016,1073741824,1693440,44032,44032,40960,9034752,1073741824,9031680,17233,1107575636,4294921772,4294967294,17233,1107575636,4294921772,4294967294,17233,1107575636,4294921772,4294967294,
9024,9024,9216,9216,9408,9408,9600,9600,9600,9600,9600,9600,40960,44032,48128,51200,55296,58368,62464,65536,62464,65536,62464,65536,268439552,336991254,410,819601408,80,7984,43008,1040137983,50465804,16780300,36,80,224,1111490560,1111490560,1111490560,1098907648,1098907648,0,130752,1073741824,1728,44032,44032,40960,697344,1073741824,9216,16956,1107575368,4294958060,4294967294,16956,1107575368,4294958060,4294967294,16956,1107575368,4294958060,4294967294,
3139584,3139584,3182592,3182592,3225600,3225600,3268608,3268608,3268608,3268608,3268608,3268608,40960,44032,48128,51200,55296,58368,62464,65536,62464,65536,62464,65536,268439552,336991254,57,1348079616,80,7488,192,1048526463,16780300,50465804,576,36,224,1116995584,1116995584,1116995584,1098907648,1098907648,0,597312,1073741824,596736,44032,44032,40960,3185664,1073741824,3182592,17042,1107575448,4294952236,4294967294,17042,1107575448,4294952236,4294967294,17042,1107575448,4294952236,4294967294,
19392,19392,19584,19584,19776,19776,19968,19968,19968,19968,19968,19968,40960,44032,48128,51200,55296,58368,62464,65536,62464,65536,62464,65536,268439552,336991254,410,1620713472,80,7984,43008,1048526591,50465804,16780300,36,80,224,1120665600,1120665600,1120665600,1098907648,1098907648,0,132696,1073741824,3672,44032,44032,40960,707712,1073741824,19584,17098,1107575504,4294945964,4294967294,17098,1107575504,4294945964,4294967294,17098,1107575504,4294945964,4294967294,
5031936,5031936,5074944,5074944,5117952,5117952,5160960,5160960,5160960,5160960,5160960,5160960,40960,44032,48128,51200,55296,58368,62464,65536,62464,65536,62464,65536,268439552,336991254,57,2150240256,80,7488,192,1048526335,16780300,50465804,576,36,224,1122762752,1122762752,1122762752,1098907648,1098907648,0,952128,1073741824,951552,44032,44032,40960,5078016,1073741824,5074944,17130,1107575536,4294942380,4294967294,17130,1107575536,4294942380,4294967294,17130,1107575536,4294942380,4294967294,
27072,27072,27264,27264,27456,27456,27648,27648,27648,27648,27648,27648,40960,44032,48128,51200,55296,58368,62464,65536,62464,65536,62464,65536,268439552,336991254,410,2423398400,80,7984,43008,1056915071,50465804,16780300,36,80,224,1124990976,1124990976,1124990976,1098907648,1098907648,0,134136,1073741824,5112,44032,44032,40960,715392,1073741824,27264,17165,1107575568,4294937004,4294967294,17165,1107575568,4294937004,4294967294,17165,1107575568,4294937004,4294967294,
7096320,7096320,7139328,7139328,7182336,7182336,7225344,7225344,7225344,7225344,7225344,7225344,40960,44032,48128,51200,55296,58368,62464,65536,62464,65536,62464,65536,268439552,336991254,57,2696548352,80,7488,192,1056915199,16780300,50465804,576,36,224,1126563840,1126563840,1126563840,1098907648,1098907648,0,1339200,1073741824,1338624,44032,44032,40960,7142400,1073741824,7139328,17189,1107575592,4294931628,4294967294,17189,1107575592,4294931628,4294967294,17189,1107575592,4294931628,4294967294,
35520,35520,35712,35712,35904,35904,36096,36096,36096,36096,36096,36096,40960,44032,48128,51200,55296,58368,62464,65536,62464,65536,62464,65536,268439552,336991254,410,3226083328,80,7984,43008,1056915071,50465804,16780300,36,80,224,1127874560,1127874560,1127874560,1098907648,1098907648,0,135720,1073741824,6696,44032,44032,40960,723840,1073741824,35712,17209,1107575612,4294927148,4294967294,17209,1107575612,4294927148,4294967294,17209,1107575612,4294927148,4294967294,
9074688,9074688,9117696,9117696,9160704,9160704,9203712,9203712,9203712,9203712,9203712,9203712,40960,44032,48128,51200,55296,58368,62464,65536,62464,65536,62464,65536,268439552,336991254,57,3499233280,80,7488,192,1056915199,16780300,50465804,576,36,224,1129578496,1129578496,1129578496,1098907648,1098907648,0,1710144,1073741824,1709568,44032,44032,40960,9120768,1073741824,9117696,17235,1107575638,4294921324,4294967294,17235,1107575638,4294921324,4294967294,17235,1107575638,4294921324,4294967294,
9408,9408,9600,9600,9792,9792,9984,9984,9984,9984,9984,9984,40960,44032,48128,51200,55296,58368,62464,65536,62464,65536,62464,65536,268439552,336991254,410,1075978240,80,7984,43008,1040137727,50465804,16780300,36,80,224,1112014848,1112014848,1112014848,1098907648,1098907648,0,130824,1073741824,1800,44032,44032,40960,697728,1073741824,9600,16964,1107575376,4294957612,4294967294,16964,1107575376,4294957612,4294967294,16964,1107575376,4294957612,4294967294,
3225600,3225600,3268608,3268608,3311616,3311616,3354624,3354624,3354624,3354624,3354624,3354624,40960,44032,48128,51200,55296,58368,62464,65536,62464,65536,62464,65536,268439552,336991254,57,1348603904,80,7488,192,1048526463,16780300,50465804,576,36,224,1117257728,1117257728,1117257728,1098907648,1098907648,0,613440,1073741824,612864,44032,44032,40960,3271680,1073741824,3268608,17046,1107575452,4294951788,4294967294,17046,1107575452,4294951788,4294967294,17046,1107575452,4294951788,4294967294,
19776,19776,19968,19968,20160,20160,20352,20352,20352,20352,20352,20352,40960,44032,48128,51200,55296,58368,62464,65536,62464,65536,62464,65536,268439552,336991254,410,1621237760,80,7984,43008,1048526591,50465804,16780300,36,80,224,1120927744,1120927744,1120927744,1098907648,1098907648,0,132768,1073741824,3744,44032,44032,40960,708096,1073741824,19968,17102,1107575508,4294945516,4294967294,17102,1107575508,4294945516,4294967294,17102,1107575508,4294945516,4294967294,
5117952,5117952,5160960,5160960,5203968,5203968,5246976,5246976,5246976,5246976,5246976,5246976,40960,44032,48128,51200,55296,58368,62464,65536,62464,65536,62464,65536,268439552,336991254,57,2150764544,80,7488,192,1048526335,16780300,50465804,576,36,224,1123024896,1123024896,1123024896,1098907648,1098907648,0,968256,1073741824,967680,44032,44032,40960,5164032,1073741824,5160960,17134,1107575540,4294941932,4294967294,17134,1107575540,4294941932,4294967294,17134,1107575540,4294941932,4294967294,
6494208,6537216,6580224,6623232,6666240,6709248,6752256,6795264,6795264,6795264,6795264,6795264,40960,45056,49152,53248,57344,61440,65536,69632,69632,69632,69632,69632,538976288,353703189,57,2423914752,80,7488,192,1056915071,16780300,50465804,576,36,128,1125908480,1125777408,1125646336,1107296256,1107296256,1107296256,1259136,1243008,1226880,57344,49152,40960,6715392,6629376,6543360,1106264855,1112032030,4294949068,30,1106264855,1112032030,4294949068,30,1106264855,1112032030,4294949068,30,
7182336,7182336,7225344,7225344,7268352,7268352,7311360,7311360,7311360,7311360,7311360,7311360,40960,44032,48128,51200,55296,58368,62464,65536,62464,65536,62464,65536,268439552,336991254,57,2697072640,80,7488,192,1056915199,16780300,50465804,576,36,224,1126694912,1126694912,1126694912,1098907648,1098907648,0,1355328,1073741824,1354752,44032,44032,40960,7228416,1073741824,7225344,17191,1107575594,4294931180,4294967294,17191,1107575594,4294931180,4294967294,17191,1107575594,4294931180,4294967294,
35904,35904,36096,36096,36288,36288,36480,36480,36480,36480,36480,36480,40960,44032,48128,51200,55296,58368,62464,65536,62464,65536,62464,65536,268439552,336991254,410,3226607616,80,7984,43008,1056915071,50465804,16780300,36,80,224,1128005632,1128005632,1128005632,1098907648,1098907648,0,135792,1073741824,6768,44032,44032,40960,724224,1073741824,36096,17211,1107575614,4294926700,4294967294,17211,1107575614,4294926700,4294967294,17211,1107575614,4294926700,4294967294,
9160704,9160704,9203712,9203712,9246720,9246720,9289728,9289728,9289728,9289728,9289728,9289728,40960,44032,48128,51200,55296,58368,62464,65536,62464,65536,62464,65536,268439552,336991254,57,3499757568,80,7488,192,1056915199,16780300,50465804,576,36,224,1129709568,1129709568,1129709568,1098907648,1098907648,0,1726272,1073741824,1725696,44032,44032,40960,9206784,1073741824,9203712,17237,1107575640,4294920876,4294967294,17237,1107575640,4294920876,4294967294,17237,1107575640,4294920876,4294967294,
2365440,2408448,2451456,2494464,2537472,2580480,2623488,2666496,2666496,2666496,2666496,2666496,40960,45056,49152,53248,57344,61440,65536,69632,69632,69632,69632,69632,538976288,353703189,57,1076494592,80,7488,192,1040137727,16780300,50465804,576,36,128,1114636288,1114112000,1113587712,1107296256,1107296256,1107296256,484992,468864,452736,57344,49152,40960,2586624,2500608,2414592,1106264668,1112031864,4294961356,30,1106264668,1112031864,4294961356,30,1106264668,1112031864,4294961356,30,
3311616,3311616,3354624,3354624,3397632,3397632,3440640,3440640,3440640,3440640,3440640,3440640,40960,44032,48128,51200,55296,58368,62464,65536,62464,65536,62464,65536,268439552,336991254,57,1349128192,80,7488,192,1048526463,16780300,50465804,576,36,224,1117519872,1117519872,1117519872,1098907648,1098907648,0,629568,1073741824,628992,44032,44032,40960,3357696,1073741824,3354624,17050,1107575456,4294951340,4294967294,17050,1107575456,4294951340,4294967294,17050,1107575456,4294951340,4294967294,
4687872,4730880,4773888,4816896,4859904,4902912,4945920,4988928,4988928,4988928,4988928,4988928,40960,45056,49152,53248,57344,61440,65536,69632,69632,69632,69632,69632,538976288,353703189,57,1621754112,80,7488,192,1048526591,16780300,50465804,576,36,128,1122238464,1121976320,1121714176,1107296256,1107296256,1107296256,920448,904320,888192,57344,49152,40960,4909056,4823040,4737024,1106264794,1112031976,4294954444,30,1106264794,1112031976,4294954444,30,1106264794,1112031976,4294954444,30,
5203968,5203968,5246976,5246976,5289984,5289984,5332992,5332992,5332992,5332992,5332992,5332992,40960,44032,48128,51200,55296,58368,62464,65536,62464,65536,62464,65536,268439552,336991254,57,2151288832,80,7488,192,1048526335,16780300,50465804,576,36,224,1123287040,1123287040,1123287040,1098907648,1098907648,0,984384,1073741824,983808,44032,44032,40960,5250048,1073741824,5246976,17138,1107575544,4294941484,4294967294,17138,1107575544,4294941484,4294967294,17138,1107575544,4294941484,4294967294,
27456,27456,27648,27648,27840,27840,28032,28032,28032,28032,28032,28032,40960,44032,48128,51200,55296,58368,62464,65536,62464,65536,62464,65536,268439552,336991254,410,2424446976,80,7984,43008,1056915199,50465804,16780300,36,80,224,1125122048,1125122048,1125122048,1098907648,1098907648,0,134208,1073741824,5184,44032,44032,40960,715776,1073741824,27648,17167,1107575570,4294936556,4294967294,17167,1107575570,4294936556,4294967294,17167,1107575570,4294936556,4294967294,
258048,301056,344064,387072,430080,473088,9074688,9117696,9117696,9117696,9117696,9117696,49152,53248,57344,61440,65536,69632,40960,45056,45056,45056,45056,45056,2694840320,320017685,57,2697605632,80,7488,192,1056915199,16780300,50465804,576,36,128,1129447424,1129447424,1090519040,1126170624,1126170624,0,1699200,1073741824,64512,32768,32768,53248,9062400,1073741824,344064,1126122313,1127236436,4294940620,158,1126122313,1127236436,4294940620,158,16576,1099972912,780,4294967294,
36288,36288,36480,36480,36672,36672,36864,36864,36864,36864,36864,36864,40960,44032,48128,51200,55296,58368,62464,65536,62464,65536,62464,65536,268439552,336991254,410,3227131904,80,7984,43008,1056915071,50465804,16780300,36,80,224,1128136704,1128136704,1128136704,1098907648,1098907648,0,135864,1073741824,6840,44032,44032,40960,724608,1073741824,36480,17213,1107575616,4294926252,4294967294,17213,1107575616,4294926252,4294967294,17213,1107575616,4294926252,4294967294,
9332736,9375744,9418752,9461760,9504768,9547776,9590784,9590784,9590784,9590784,9590784,9590784,40960,45056,49152,53248,57344,61440,65536,69632,69632,69632,69632,69632,538976288,353703189,57,3500278016,80,7488,192,1056915199,16780300,50465804,576,36,128,1130233856,1130102784,1129971712,1107296256,1107296256,1107296256,1791360,1775232,1759104,57344,49152,40960,9553920,9467904,9381888,1106264921,1112032095,4294940620,30,1106264921,1112032095,4294940620,30,1106264921,1112032095,4294940620,30,
9792,9792,9984,9984,10176,10176,10368,10368,10368,10368,10368,10368,40960,44032,48128,51200,55296,58368,62464,65536,62464,65536,62464,65536,268439552,336991254,410,1077026816,80,7984,43008,1040137727,50465804,16780300,36,80,224,1112539136,1112539136,1112539136,1098907648,1098907648,0,130896,1073741824,1872,44032,44032,40960,698112,1073741824,9984,16972,1107575384,4294957164,4294967294,16972,1107575384,4294957164,4294967294,16972,1107575384,4294957164,4294967294,
3397632,3397632,3440640,3440640,3483648,3483648,3526656,3526656,3526656,3526656,3526656,3526656,40960,44032,48128,51200,55296,58368,62464,65536,62464,65536,62464,65536,268439552,336991254,57,1349652480,80,7488,192,1048526463,16780300,50465804,576,36,224,1117782016,1117782016,1117782016,1098907648,1098907648,0,645696,1073741824,645120,44032,44032,40960,3443712,1073741824,3440640,17054,1107575460,4294950892,4294967294,17054,1107575460,4294950892,4294967294,17054,1107575460,4294950892,4294967294,
20160,20160,20352,20352,20544,20544,20736,20736,20736,20736,20736,20736,40960,44032,48128,51200,55296,58368,62464,65536,62464,65536,62464,65536,268439552,336991254,410,1622286336,80,7984,43008,1048526591,50465804,16780300,36,80,224,1121189888,1121189888,1121189888,1098907648,1098907648,0,132840,1073741824,3816,44032,44032,40960,708480,1073741824,20352,17106,1107575512,4294945068,4294967294,17106,1107575512,4294945068,4294967294,17106,1107575512,4294945068,4294967294,
5289984,5289984,5332992,5332992,5376000,5376000,5419008,5419008,5419008,5419008,5419008,5419008,40960,44032,48128,51200,55296,58368,62464,65536,62464,65536,62464,65536,268439552,336991254,57,2151813120,80,7488,192,1048526463,16780300,50465804,576,36,224,1123549184,1123549184,1123549184,1098907648,1098907648,0,1000512,1073741824,999936,44032,44032,40960,5336064,1073741824,5332992,17142,1107575548,4294941036,4294967294,17142,1107575548,4294941036,4294967294,17142,1107575548,4294941036,4294967294,
27840,27840,28032,28032,28224,28224,28416,28416,28416,28416,28416,28416,40960,44032,48128,51200,55296,58368,62464,65536,62464,65536,62464,65536,268439552,336991254,410,2424971264,80,7984,43008,1056915199,50465804,16780300,36,80,224,1125253120,1125253120,1125253120,1098907648,1098907648,0,134280,1073741824,5256,44032,44032,40960,716160,1073741824,28032,17169,1107575572,4294936108,4294967294,17169,1107575572,4294936108,4294967294,17169,1107575572,4294936108,4294967294,
1290240,1333248,1376256,1419264,1462272,9504768,9547776,9590784,9590784,9590784,9590784,9590784,57344,61440,65536,69632,73728,40960,45056,49152,53248,53248,53248,53248,2694881440,353703189,57,2698249472,64,7488,192,1056915199,16780300,50465804,576,36,128,1130102784,1130233856,1107296256,1126170624,1126170624,1126170624,1779840,1795968,263808,32768,40960,61440,9492480,9578496,1406976,1126122323,1127236447,4294939340,158,1126122323,1127236447,4294939340,158,1126056432,1127367176,4294964300,158,
36672,36672,36864,36864,37056,37056,37248,37248,37248,37248,37248,37248,40960,44032,48128,51200,55296,58368,62464,65536,62464,65536,62464,65536,268439552,336991254,410,3227656192,80,7984,43008,1056915071,50465804,16780300,36,80,224,1128267776,1128267776,1128267776,1098907648,1098907648,0,135936,1073741824,6912,44032,44032,40960,724992,1073741824,36864,17215,1107575618,4294925804,4294967294,17215,1107575618,4294925804,4294967294,17215,1107575618,4294925804,4294967294,
9246720,9246720,9289728,9289728,9332736,9332736,9375744,9375744,9375744,9375744,9375744,9375744,40960,44032,48128,51200,55296,58368,62464,65536,62464,65536,62464,65536,268439552,336991254,57,3500806144,80,7488,192,1056915199,16780300,50465804,576,36,224,1129840640,1129840640,1129840640,1098907648,1098907648,0,1742400,1073741824,1741824,44032,44032,40960,9292800,1073741824,9289728,17239,1107575642,4294920428,4294967294,17239,1107575642,4294920428,4294967294,17239,1107575642,4294920428,4294967294,
10176,10176,10368,10368,10560,10560,10752,10752,10752,10752,10752,10752,40960,44032,48128,51200,55296,58368,62464,65536,62464,65536,62464,65536,268439552,336991254,410,1077551104,80,7984,43008,1040137727,50465804,16780300,36,80,224,1113063424,1113063424,1113063424,1098907648,1098907648,0,130968,1073741824,1944,44032,44032,40960,698496,1073741824,10368,16980,1107575392,4294956716,4294967294,16980,1107575392,4294956716,4294967294,16980,1107575392,4294956716,4294967294,
3483648,3483648,3526656,3526656,3569664,3569664,3612672,3612672,3612672,3612672,3612672,3612672,40960,44032,48128,51200,55296,58368,62464,65536,62464,65536,62464,65536,268439552,336991254,57,1350176768,80,7488,192,1048526463,16780300,50465804,576,36,224,1118044160,1118044160,1118044160,1098907648,1098907648,0,661824,1073741824,661248,44032,44032,40960,3529728,1073741824,3526656,17058,1107575464,4294950444,4294967294,17058,1107575464,4294950444,4294967294,17058,1107575464,4294950444,4294967294,
20544,20544,20736,20736,20928,20928,21120,21120,21120,21120,21120,21120,40960,44032,48128,51200,55296,58368,62464,65536,62464,65536,62464,65536,268439552,336991254,410,1622810624,80,7984,43008,1048526591,50465804,16780300,36,80,224,1121452032,1121452032,1121452032,1098907648,1098907648,0,132912,1073741824,3888,44032,44032,40960,708864,1073741824,20736,17110,1107575516,4294944620,4294967294,17110,1107575516,4294944620,4294967294,17110,1107575516,4294944620,4294967294,
5376000,5376000,5419008,5419008,5462016,5462016,5505024,5505024,5505024,5505024,5505024,5505024,40960,44032,48128,51200,55296,58368,62464,65536,62464,65536,62464,65536,268439552,336991254,57,2152337408,80,7488,192,1048526463,16780300,50465804,576,36,224,1123811328,1123811328,1123811328,1098907648,1098907648,0,1016640,1073741824,1016064,44032,44032,40960,5422080,1073741824,5419008,17146,1107575552,4294940588,4294967294,17146,1107575552,4294940588,4294967294,17146,1107575552,4294940588,4294967294,
28224,28224,28416,28416,28608,28608,28800,28800,28800,28800,28800,28800,40960,44032,48128,51200,55296,58368,62464,65536,62464,65536,62464,65536,268439552,336991254,410,2425495552,80,7984,43008,1056915199,50465804,16780300,36,80,224,1125384192,1125384192,1125384192,1098907648,1098907648,0,134352,1073741824,5328,44032,44032,40960,716544,1073741824,28416,17171,1107575574,4294935660,4294967294,17171,1107575574,4294935660,4294967294,17171,1107575574,4294935660,4294967294,
7268352,7268352,7311360,7311360,7354368,7354368,7397376,7397376,7397376,7397376,7397376,7397376,40960,44032,48128,51200,55296,58368,62464,65536,62464,65536,62464,65536,268439552,336991254,57,2955022336,80,7488,192,1056914943,16780300,50465804,576,36,224,1126825984,1126825984,1126825984,1098907648,1098907648,0,1371456,1073741824,1370880,44032,44032,40960,7314432,1073741824,7311360,17193,1107575596,4294930732,4294967294,17193,1107575596,4294930732,4294967294,17193,1107575596,4294930732,4294967294,
37056,37056,37248,37248,37440,37440,37632,37632,37632,37632,37632,37632,40960,44032,48128,51200,55296,58368,62464,65536,62464,65536,62464,65536,268439552,336991254,410,3228180480,80,7984,43008,1056915071,50465804,16780300,36,80,224,1128398848,1128398848,1128398848,1098907648,1098907648,0,136008,1073741824,6984,44032,44032,40960,725376,1073741824,37248,17217,1107575620,4294925356,4294967294,17217,1107575620,4294925356,4294967294,17217,1107575620,4294925356,4294967294,
9332736,9332736,9375744,9375744,9418752,9418752,9461760,9461760,9461760,9461760,9461760,9461760,40960,44032,48128,51200,55296,58368,62464,65536,62464,65536,62464,65536,268439552,336991254,57,3501330432,80,7488,192,1056915199,16780300,50465804,576,36,224,1129971712,1129971712,1129971712,1098907648,1098907648,0,1758528,1073741824,1757952,44032,44032,40960,9378816,1073741824,9375744,17241,1107575644,4294919980,4294967294,17241,1107575644,4294919980,4294967294,17241,1107575644,4294919980,4294967294,
10560,10560,10752,10752,10944,10944,11136,11136,11136,11136,11136,11136,40960,44032,48128,51200,55296,58368,62464,65536,62464,65536,62464,65536,268439552,336991254,410,1078075392,80,7984,43008,1040137855,50465804,16780300,36,80,224,1113587712,1113587712,1113587712,1098907648,1098907648,0,131040,1073741824,2016,44032,44032,40960,698880,1073741824,10752,16988,1107575400,4294956268,4294967294,16988,1107575400,4294956268,4294967294,16988,1107575400,4294956268,4294967294,
3655680,3698688,3741696,3784704,3827712,3870720,3913728,3956736,3956736,3956736,3956736,3956736,40960,45056,49152,53248,57344,61440,65536,69632,69632,69632,69632,69632,538976288,353703189,57,1350697216,80,7488,192,1048526591,16780300,50465804,576,36,128,1119092736,1118830592,1118568448,1107296256,1107296256,1107296256,726912,710784,694656,57344,49152,40960,3876864,3790848,3704832,1106264746,1112031928,4294957516,30,1106264746,1112031928,4294957516,30,1106264746,1112031928,4294957516,30,
0,0,0,43008,86016,129024,8730624,8773632,8773632,8773632,8773632,8773632,49152,53248,57344,61440,65536,69632,40960,45056,45056,45056,45056,45056,1616904192,320017685,57,1623339520,80,7488,192,1048526591,16780300,50465804,576,36,128,1128923136,1128923136,0,1119879168,1119879168,0,1632384,1073741824,0,32768,32768,53248,8706048,1073741824,0,1119765313,1121993548,4294942028,94,1119765313,1121993548,4294942028,94,0,1099972672,1804,4294967294,
5462016,5462016,5505024,5505024,5548032,5548032,5591040,5591040,5591040,5591040,5591040,5591040,40960,44032,48128,51200,55296,58368,62464,65536,62464,65536,62464,65536,268439552,336991254,57,2152861696,80,7488,192,1048526463,16780300,50465804,576,36,224,1124073472,1124073472,1124073472,1098907648,1098907648,0,1032768,1073741824,1032192,44032,44032,40960,5508096,1073741824,5505024,17150,1107575554,4294940140,4294967294,17150,1107575554,4294940140,4294967294,17150,1107575554,4294940140,4294967294,
28608,28608,28800,28800,28992,28992,29184,29184,29184,29184,29184,29184,40960,44032,48128,51200,55296,58368,62464,65536,62464,65536,62464,65536,268439552,336991254,410,2426019840,80,7984,43008,1056915199,50465804,16780300,36,80,224,1125515264,1125515264,1125515264,1098907648,1098907648,0,134424,1073741824,5400,44032,44032,40960,716928,1073741824,28800,17173,1107575576,4294935212,4294967294,17173,1107575576,4294935212,4294967294,17173,1107575576,4294935212,4294967294,
7526400,7569408,7612416,7655424,7698432,7741440,7784448,7827456,7827456,7827456,7827456,7827456,40960,45056,49152,53248,57344,61440,65536,69632,69632,69632,69632,69632,538976288,353703189,57,2955542784,80,7488,192,1056914943,16780300,50465804,576,36,128,1127481344,1127350272,1127219200,1107296256,1107296256,1107296256,1452672,1436544,1420416,57344,49152,40960,7747584,7661568,7575552,1106264879,1112032054,4294945996,30,1106264879,1112032054,4294945996,30,1106264879,1112032054,4294945996,30,
37440,37440,37632,37632,37824,37824,38016,38016,38016,38016,38016,38016,40960,44032,48128,51200,55296,58368,62464,65536,62464,65536,62464,65536,268439552,336991254,410,3228704768,80,7984,43008,1056915071,50465804,16780300,36,80,224,1128529920,1128529920,1128529920,1098907648,1098907648,0,136080,1073741824,7056,44032,44032,40960,725760,1073741824,37632,17219,1107575622,4294924908,4294967294,17219,1107575622,4294924908,4294967294,17219,1107575622,4294924908,4294967294,
9418752,9418752,9461760,9461760,9504768,9504768,9547776,9547776,9547776,9547776,9547776,9547776,40960,44032,48128,51200,55296,58368,62464,65536,62464,65536,62464,65536,268439552,336991254,57,3501854720,80,7488,192,1056915199,16780300,50465804,576,36,224,1130102784,1130102784,1130102784,1098907648,1098907648,0,1774656,1073741824,1774080,44032,44032,40960,9464832,1073741824,9461760,17243,1107575646,4294919532,4294967294,17243,1107575646,4294919532,4294967294,17243,1107575646,4294919532,4294967294,
10944,10944,11136,11136,11328,11328,11520,11520,11520,11520,11520,11520,40960,44032,48128,51200,55296,58368,62464,65536,62464,65536,62464,65536,268439552,336991254,410,1078599680,80,7984,43008,1040137855,50465804,16780300,36,80,224,1114112000,1114112000,1114112000,1098907648,1098907648,0,131112,1073741824,2088,44032,44032,40960,699264,1073741824,11136,16996,1107575408,4294955820,4294967294,16996,1107575408,4294955820,4294967294,16996,1107575408,4294955820,4294967294,
3569664,3569664,3612672,3612672,3655680,3655680,3698688,3698688,3698688,3698688,3698688,3698688,40960,44032,48128,51200,55296,58368,62464,65536,62464,65536,62464,65536,268439552,336991254,57,1351225344,80,7488,192,1048526591,16780300,50465804,576,36,224,1118306304,1118306304,1118306304,1098907648,1098907648,0,677952,1073741824,677376,44032,44032,40960,3615744,1073741824,3612672,17062,1107575468,4294949996,4294967294,17062,1107575468,4294949996,4294967294,17062,1107575468,4294949996,4294967294,
602112,645120,688128,731136,774144,817152,9160704,9203712,9203712,9203712,9203712,9203712,49152,53248,57344,61440,65536,69632,40960,45056,45056,45056,45056,45056,1616904192,320017685,57,1623994368,80,7488,192,1048526591,16780300,50465804,576,36,128,1129578496,1129578496,1098907648,1119879168,1119879168,0,1713024,1073741824,129024,32768,32768,53248,9136128,1073741824,688128,1119765323,1121993558,4294940748,94,1119765323,1121993558,4294940748,94,16736,1099973016,4294967052,4294967294,
5720064,5763072,5806080,5849088,5892096,5935104,5978112,6021120,6021120,6021120,6021120,6021120,40960,45056,49152,53248,57344,61440,65536,69632,69632,69632,69632,69632,538976288,353703189,57,2153382144,80,7488,192,1048526463,16780300,50465804,576,36,128,1124728832,1124597760,1124466688,1107296256,1107296256,1107296256,1113984,1097856,1081728,57344,49152,40960,5941248,5855232,5769216,1106264837,1112032012,4294951372,30,1106264837,1112032012,4294951372,30,1106264837,1112032012,4294951372,30,
28992,28992,29184,29184,29376,29376,29568,29568,29568,29568,29568,29568,40960,44032,48128,51200,55296,58368,62464,65536,62464,65536,62464,65536,268439552,336991254,410,2426544128,80,7984,43008,1056915199,50465804,16780300,36,80,224,1125646336,1125646336,1125646336,1098907648,1098907648,0,134496,1073741824,5472,44032,44032,40960,717312,1073741824,29184,17175,1107575578,4294934764,4294967294,17175,1107575578,4294934764,4294967294,17175,1107575578,4294934764,4294967294,
7354368,7354368,7397376,7397376,7440384,7440384,7483392,7483392,7483392,7483392,7483392,7483392,40960,44032,48128,51200,55296,58368,62464,65536,62464,65536,62464,65536,268439552,336991254,57,2956070912,80,7488,192,1056914943,16780300,50465804,576,36,224,1126957056,1126957056,1126957056,1098907648,1098907648,0,1387584,1073741824,1387008,44032,44032,40960,7400448,1073741824,7397376,17195,1107575598,4294930284,4294967294,17195,1107575598,4294930284,4294967294,17195,1107575598,4294930284,4294967294,
37824,37824,38016,38016,38208,38208,38400,38400,38400,38400,38400,38400,40960,44032,48128,51200,55296,58368,62464,65536,62464,65536,62464,65536,268439552,336991254,410,3229229056,80,7984,43008,1056915071,50465804,16780300,36,80,224,1128660992,1128660992,1128660992,1098907648,1098907648,0,136152,1073741824,7128,44032,44032,40960,726144,1073741824,38016,17221,1107575624,4294924460,4294967294,17221,1107575624,4294924460,4294967294,17221,1107575624,4294924460,4294967294,
9504768,9504768,9547776,9547776,9590784,9590784,9590784,9590784,9590784,9590784,9590784,9590784,40960,44032,48128,51200,55296,58368,62464,65536,62464,65536,62464,65536,268439552,336991254,57,3502379008,80,7488,192,1056915199,16780300,50465804,576,36,224,1130233856,1130233856,1130233856,1098907648,1098907648,0,1790784,1073741824,1790208,44032,44032,40960,9550848,1073741824,9547776,17245,1107575647,4294919084,4294967294,17245,1107575647,4294919084,4294967294,17245,1107575647,4294919084,4294967294,
11328,11328,11520,11520,11712,11712,11904,11904,11904,11904,11904,11904,40960,44032,48128,51200,55296,58368,62464,65536,62464,65536,62464,65536,268439552,336991254,410,1079123968,80,7984,43008,1040137855,50465804,16780300,36,80,224,1114636288,1114636288,1114636288,1098907648,1098907648,0,131184,1073741824,2160,44032,44032,40960,699648,1073741824,11520,17004,1107575416,4294955372,4294967294,17004,1107575416,4294955372,4294967294,17004,1107575416,4294955372,4294967294,
3655680,3655680,3698688,3698688,3741696,3741696,3784704,3784704,3784704,3784704,3784704,3784704,40960,44032,48128,51200,55296,58368,62464,65536,62464,65536,62464,65536,268439552,336991254,57,1351749632,80,7488,192,1048526591,16780300,50465804,576,36,224,1118568448,1118568448,1118568448,1098907648,1098907648,0,694080,1073741824,693504,44032,44032,40960,3701760,1073741824,3698688,17066,1107575472,4294949548,4294967294,17066,1107575472,4294949548,4294967294,17066,1107575472,4294949548,4294967294,
688128,731136,774144,817152,860160,903168,9590784,9590784,9590784,9590784,9590784,9590784,49152,53248,57344,61440,65536,69632,40960,45056,45056,45056,45056,45056,1616904192,320017685,57,1624649728,80,7488,192,1048526591,16780300,50465804,576,36,128,1130233856,1130233856,1099956224,1119879168,1119879168,0,1793664,1073741824,145152,32768,32768,53248,9566208,1073741824,774144,1119765333,1121993567,4294939468,94,1119765333,1121993567,4294939468,94,16768,1099973032,4294966796,4294967294,
5548032,5548032,5591040,5591040,5634048,5634048,5677056,5677056,5677056,5677056,5677056,5677056,40960,44032,48128,51200,55296,58368,62464,65536,62464,65536,62464,65536,268439552,336991254,57,2153910272,80,7488,192,1048526463,16780300,50465804,576,36,224,1124204544,1124204544,1124204544,1098907648,1098907648,0,1048896,1073741824,1048320,44032,44032,40960,5594112,1073741824,5591040,17153,1107575556,4294939692,4294967294,17153,1107575556,4294939692,4294967294,17153,1107575556,4294939692,4294967294,
29376,29376,29568,29568,29760,29760,29952,29952,29952,29952,29952,29952,40960,44032,48128,51200,55296,58368,62464,65536,62464,65536,62464,65536,268439552,336991254,410,2427068416,80,7984,43008,1056915199,50465804,16780300,36,80,224,1125777408,1125777408,1125777408,1098907648,1098907648,0,134568,1073741824,5544,44032,44032,40960,717696,1073741824,29568,17177,1107575580,4294934316,4294967294,17177,1107575580,4294934316,4294967294,17177,1107575580,4294934316,4294967294,
7440384,7440384,7483392,7483392,7526400,7526400,7569408,7569408,7569408,7569408,7569408,7569408,40960,44032,48128,51200,55296,58368,62464,65536,62464,65536,62464,65536,268439552,336991254,57,2956595200,80,7488,192,1056914943,16780300,50465804,576,36,224,1127088128,1127088128,1127088128,1098907648,1098907648,0,1403712,1073741824,1403136,44032,44032,40960,7486464,1073741824,7483392,17197,1107575600,4294929836,4294967294,17197,1107575600,4294929836,4294967294,17197,1107575600,4294929836,4294967294,
8558592,8601600,8644608,8687616,8730624,8773632,8816640,8859648,8859648,8859648,8859648,8859648,40960,45056,49152,53248,57344,61440,65536,69632,69632,69632,69632,69632,538976288,353703189,57,3229745408,80,7488,192,1056915199,16780300,50465804,576,36,128,1129054208,1128923136,1128792064,1107296256,1107296256,1107296256,1646208,1630080,1613952,57344,49152,40960,8779776,8693760,8607744,1106264903,1112032078,4294942924,30,1106264903,1112032078,4294942924,30,1106264903,1112032078,4294942924,30,
516096,559104,602112,645120,688128,731136,9074688,9117696,9117696,9117696,9117696,9117696,49152,53248,57344,61440,65536,69632,40960,45056,45056,45056,45056,45056,3503292416,320017685,57,3502912000,80,7488,192,1056915199,16780300,50465804,576,36,128,1129447424,1129447424,1096810496,1129316352,1129316352,0,1700928,1073741824,112896,32768,32768,53248,9071616,1073741824,602112,1129268041,1130316628,4294940332,206,1129268041,1130316628,4294940332,206,16704,1099973000,12,4294967294,
11712,11712,11904,11904,12096,12096,12288,12288,12288,12288,12288,12288,40960,44032,48128,51200,55296,58368,62464,65536,62464,65536,62464,65536,268439552,336991254,410,1079648256,80,7984,43008,1040137855,50465804,16780300,36,80,224,1115160576,1115160576,1115160576,1098907648,1098907648,0,131256,1073741824,2232,44032,44032,40960,700032,1073741824,11904,17012,1107575424,4294954924,4294967294,17012,1107575424,4294954924,4294967294,17012,1107575424,4294954924,4294967294,
3741696,3741696,3784704,3784704,3827712,3827712,3870720,3870720,3870720,3870720,3870720,3870720,40960,44032,48128,51200,55296,58368,62464,65536,62464,65536,62464,65536,268439552,336991254,57,1352273920,80,7488,192,1048526591,16780300,50465804,576,36,224,1118830592,1118830592,1118830592,1098907648,1098907648,0,710208,1073741824,709632,44032,44032,40960,3787776,1073741824,3784704,17070,1107575476,4294949100,4294967294,17070,1107575476,4294949100,4294967294,17070,1107575476,4294949100,4294967294,
20928,20928,21120,21120,21312,21312,21504,21504,21504,21504,21504,21504,40960,44032,48128,51200,55296,58368,62464,65536,62464,65536,62464,65536,268439552,336991254,410,1881284608,80,7984,43008,1048526335,50465804,16780300,36,80,224,1121714176,1121714176,1121714176,1098907648,1098907648,0,132984,1073741824,3960,44032,44032,40960,709248,1073741824,21120,17114,1107575520,4294944172,4294967294,17114,1107575520,4294944172,4294967294,17114,1107575520,4294944172,4294967294,
5634048,5634048,5677056,5677056,5720064,5720064,5763072,5763072,5763072,5763072,5763072,5763072,40960,44032,48128,51200,55296,58368,62464,65536,62464,65536,62464,65536,268439552,336991254,57,2154434560,80,7488,192,1048526463,16780300,50465804,576,36,224,1124335616,1124335616,1124335616,1098907648,1098907648,0,1065024,1073741824,1064448,44032,44032,40960,5680128,1073741824,5677056,17155,1107575558,4294939244,4294967294,17155,1107575558,4294939244,4294967294,17155,1107575558,4294939244,4294967294,
29760,29760,29952,29952,30144,30144,30336,30336,30336,30336,30336,30336,40960,44032,48128,51200,55296,58368,62464,65536,62464,65536,62464,65536,268439552,336991254,410,2427592704,80,7984,43008,1056915199,50465804,16780300,36,80,224,1125908480,1125908480,1125908480,1098907648,1098907648,0,134640,1073741824,5616,44032,44032,40960,718080,1073741824,29952,17179,1107575582,4294933868,4294967294,17179,1107575582,4294933868,4294967294,17179,1107575582,4294933868,4294967294,
7526400,7526400,7569408,7569408,7612416,7612416,7655424,7655424,7655424,7655424,7655424,7655424,40960,44032,48128,51200,55296,58368,62464,65536,62464,65536,62464,65536,268439552,336991254,57,2957119488,80,7488,192,1056915071,16780300,50465804,576,36,224,1127219200,1127219200,1127219200,1098907648,1098907648,0,1419840,1073741824,1419264,44032,44032,40960,7572480,1073741824,7569408,17199,1107575602,4294929388,4294967294,17199,1107575602,4294929388,4294967294,17199,1107575602,4294929388,4294967294,
38208,38208,38400,38400,38592,38592,38784,38784,38784,38784,38784,38784,40960,44032,48128,51200,55296,58368,62464,65536,62464,65536,62464,65536,268439552,336991254,410,3230277632,80,7984,43008,1056915199,50465804,16780300,36,80,224,1128792064,1128792064,1128792064,1098907648,1098907648,0,136224,1073741824,7200,44032,44032,40960,726528,1073741824,38400,17223,1107575626,4294924012,4294967294,17223,1107575626,4294924012,4294967294,17223,1107575626,4294924012,4294967294,
1290240,1333248,1376256,1419264,1462272,9504768,9547776,9590784,9590784,9590784,9590784,9590784,57344,61440,65536,69632,73728,40960,45056,49152,53248,53248,53248,53248,3503345872,353703189,57,3503555840,64,7488,192,1056915199,16780300,50465804,576,36,128,1130102784,1130233856,1107296256,1129316352,1129316352,1129316352,1781568,1797696,265536,32768,40960,61440,9501696,9587712,1416192,1129268051,1130316639,4294939052,206,1129268051,1130316639,4294939052,206,1129202160,1130316296,4294964012,206};
__device__ __forceinline__ float bf_lo(unsigned u) { return __uint_as_float(u << 16); }
__device__ __forceinline__ float bf_hi(unsigned u) { return __uint_as_float(u & 0xffff0000u); }
__device__ __forceinline__ unsigned cvt_pk(float lo, float hi) { bf16x2 r = {(__bf16)lo, (__bf16)hi}; return __builtin_bit_cast(unsigned, r); }
__device__ __forceinline__ float rb(float v) { return bf_lo(cvt_pk(v, 0.f)); }
__global__ __launch_bounds__(256) __attribute__((aligned(4096))) void dcn_kernel(const unsigned short* x_, const unsigned short* off_, const unsigned short* w_, unsigned short* out_, void* ws_) {
    __shared__ __attribute__((aligned(128))) unsigned char lds[163840];
    asm volatile("" ::"v"((__attribute__((address_space(3))) unsigned char*)lds));
    unsigned tid = threadIdx.x, bid = blockIdx.x;
    const __attribute__((address_space(4))) Args* ka = (const __attribute__((address_space(4))) Args*)__builtin_amdgcn_kernarg_segment_ptr();
    asm volatile(
        "s_load_dwordx8 s[48:55], %1, 0\n\t"
        "v_mov_b32 v156, %0\n\t"
        "s_mov_b32 s42, %2\n\t"
        "s_getpc_b64 s[44:45]\n\t"
        "s_and_b32 s44, s44, 0xffffff00\n\t"
        "9:\n\t"
        "s_mov_b32 s46, 99f-9b+3300\n\t"
        "s_mov_b32 s47, 0x20000\n\t"
        "v_lshlrev_b32 v160, 12, v156\n\t"
        "s_lshl_b32 s43, s42, 4\n\t"
        "s_and_b32 s43, s43, 3968\n\t"
        "s_mov_b32 m0, 161856\n\t"
        "v_cmp_gt_u32 vcc, 64, v156\n\t"
        "s_nop 1\n\t"
        "s_cbranch_vccz 10f\n\t"
        "buffer_load_dword v160, s[44:47], s43 offen lds\n\t"
        "10:\n\t"
        "s_mov_b64 s[64:65], %3\n\t"
        "v_and_b32 v157, 63, v156\n\t"
        "s_mov_b32 s66, 65536\n\t"
        "s_mov_b32 s67, 0x20000\n\t"
        "v_lshlrev_b32 v155, 2, v157\n\t"
        "s_lshl_b32 s70, s42, 8\n\t"
        "buffer_load_dword v155, v155, s[64:67], s70 offen\n\t"
        "v_lshrrev_b32 v73, 6, v156\n\t"
        "v_and_b32 v157, 63, v156\n\t"
        "v_readfirstlane_b32 s4, v73\n\t"
        "v_lshlrev_b32 v140, 4, v157\n\t"
        "s_mov_b32 s26, 165888\n\t"
        "s_mov_b32 s27, 0x20000\n\t"
        "v_xor_b32 v142, 32, v140\n\t"
        "v_and_b32 v73, 32, v157\n\t"
        "v_xor_b32 v141, v73, v140\n\t"
        "s_lshr_b32 s68, s4, 1\n\t"
        "s_mul_i32 s37, s4, 3072\n\t"
        "s_mul_i32 s36, s68, -61440\n\t"
        "s_add_u32 s36, s36, s37\n\t"
        "s_add_u32 s36, s36, 55296\n\t"
        "s_add_u32 s37, s37, 86016\n\t"
        "s_and_b32 s8, s42, 7\n\t"
        "s_lshl_b32 s8, s8, 5\n\t"
        "s_lshr_b32 s10, s42, 3\n\t"
        "s_add_u32 s10, s10, s8\n\t"
        "s_mul_i32 s8, s10, 2731\n\t"
        "s_sub_u32 s11, s10, 3\n\t"
        "s_mul_i32 s11, s11, 2850\n\t"
        "s_max_i32 s8, s8, s11\n\t"
        "s_lshr_b32 s8, s8, 16\n\t"
        "v_mul_u32_u24 v71, 171, v156\n\t"
        "v_lshrrev_b32 v71, 11, v71\n\t"
        "v_mul_u32_u24 v74, 12, v71\n\t"
        "v_sub_u32 v74, v156, v74\n\t"
        "s_mul_i32 s11, s8, 23\n\t"
        "s_sub_u32 s10, s10, s11\n\t"
        "s_min_u32 s11, s8, 3\n\t"
        "s_sub_u32 s10, s10, s11\n\t"
        "s_lshl_b32 s6, s8, 4\n\t"
        "s_add_u32 s6, s6, 48\n\t"
        "v_lshrrev_b32 v75, 1, v71\n\t"
        "v_and_b32 v75, 2, v75\n\t"
        "v_xor_b32 v74, v74, v75\n\t"
        "v_lshlrev_b32 v72, 4, v74\n\t"
        "s_lshl_b32 s8, s8, 1\n\t"
        "s_lshr_b32 s8, 1396799, s8\n\t"
        "s_and_b32 s8, s8, 3\n\t"
        "s_sub_u32 s8, s10, s8\n\t"
        "s_max_i32 s8, s8, 20\n\t"
        "s_lshl2_add_u32 s8, s10, s8\n\t"
        "s_lshl1_add_u32 s8, s8, -6\n\t"
        "v_add_u32 v73, -1, v71\n\t"
        "v_cmp_lt_u32 vcc, 17, v73\n\t"
        "v_mov_b32 v75, 0x7ff00000\n\t"
        "v_add3_u32 v161, v71, s6, -2\n\t"
        "v_cndmask_b32 v162, v72, v75, vcc\n\t"
        "s_movk_i32 s9, 192\n\t"
        "s_lshl_b32 s35, s4, 10\n\t"
        "s_mov_b32 s18, 9633792\n\t"
        "s_mov_b32 s19, 0x20000\n\t"
        "v_min_u32 v161, 223, v161\n\t"
        "s_mul_i32 s34, s8, 43008\n\t"
        "v_mad_u32_u24 v143, v161, s9, v162\n\t"
        "s_sub_u32 s34, s34, 43008\n\t"
        "s_lshr_b32 s12, s42, 3\n\t"
        "s_lshl_b32 s12, s12, 2\n\t"
        "s_add_u32 s12, s12, s4\n\t"
        "s_mul_i32 s12, s12, 1296\n\t"
        "s_and_b32 s12, s12, 0xffffff80\n\t"
        "v_min_u32 v164, 11, v157\n\t"
        "v_lshlrev_b32 v164, 7, v164\n\t"
        "s_mul_i32 s39, s68, 6144\n\t"
        "s_add_u32 s39, s39, 55296\n\t"
        "s_mul_i32 s38, s68, 159744\n\t"
        "s_add_u32 s38, s38, -104448\n\t"
        "s_mul_i32 s5, s68, -165888\n\t"
        "s_add_u32 s5, s5, 61440\n\t"
        "s_mov_b32 s22, 1806336\n\t"
        "s_mov_b32 s23, 0x20000\n\t"
        "s_mov_b32 s30, 9633792\n\t"
        "s_mov_b32 s31, 0x20000\n\t"
        "s_lshl_b32 s10, s4, 2\n\t"
        "s_add_u32 s10, s10, 161792\n\t"
        "v_mov_b32 v153, s10\n\t"
        "v_mov_b32 v182, 161792\n\t"
        "v_mov_b32 v76, 0\n\t"
        "v_mov_b32 v77, 0\n\t"
        "v_lshlrev_b32 v73, 3, v156\n\t"
        "v_add_u32 v73, 147456, v73\n\t"
        "s_add_u32 s60, s35, 153600\n\t"
        "v_mov_b32 v74, s60\n\t"
        "s_cmp_lt_u32 s4, 3\n\t"
        "s_cselect_b64 exec, -1, 0\n\t"
        "ds_write_b64 v73, v[76:77] offset:0\n\t"
        "ds_write_b64 v73, v[76:77] offset:2048\n\t"
        "ds_write_b64 v73, v[76:77] offset:4096\n\t"
        "s_mov_b64 exec, 1\n\t"
        "ds_write_b32 v153, v76 offset:16\n\t"
        "ds_write_b64 v74, v[76:77]\n\t"
        "s_mov_b64 exec, -1\n\t"
        "s_waitcnt lgkmcnt(0)\n\t"
        "s_mov_b64 s[24:25], s[52:53]\n\t"
        "s_mov_b64 s[16:17], s[48:49]\n\t"
        "s_add_u32 m0, s37, -36864\n\t"
        "s_nop 0\n\t"
        "buffer_load_dwordx4 v140, s[24:27], s36 offen offset:0 lds\n\t"
        "buffer_load_dwordx4 v141, s[24:27], s36 offen offset:1024 lds\n\t"
        "buffer_load_dwordx4 v142, s[24:27], s36 offen offset:2048 lds\n\t"
        "buffer_load_dword v165, v164, s[24:27], s12 offen\n\t"
        "s_add_u32 m0, s35, 8192\n\t"
        "s_add_u32 s43, s34, 86016\n\t"
        "buffer_load_dwordx4 v143, s[16:19], s43 offen  lds\n\t"
        "s_add_u32 m0, s35, 24576\n\t"
        "s_add_u32 s43, s34, 258048\n\t"
        "buffer_load_dwordx4 v143, s[16:19], s43 offen  lds\n\t"
        "s_mov_b64 s[20:21], s[50:51]\n\t"
        "s_mov_b64 s[28:29], s[54:55]\n\t"
        "s_mul_i32 s41, s8, 224\n\t"
        "s_add_u32 s41, s41, s6\n\t"
        "v_and_b32 v158, 15, v156\n\t"
        "s_and_b32 s49, s4, 1\n\t"
        "s_lshl_b32 s8, s49, 5\n\t"
        "v_xor_b32 v152, s8, v140\n\t"
        "s_mul_i32 s43, s4, 1536\n\t"
        "s_add_u32 m0, s43, 43008\n\t"
        "s_add_u32 s43, s43, 110592\n\t"
        "buffer_load_dwordx4 v152, s[24:27], s43 offen lds\n\t"
        "buffer_load_dwordx4 v152, s[24:27], s43 offen offset:512 lds\n\t"
        "s_add_u32 m0, s35, 12288\n\t"
        "s_add_u32 s43, s34, 129024\n\t"
        "buffer_load_dwordx4 v143, s[16:19], s43 offen  lds\n\t"
        "s_add_u32 m0, s35, 28672\n\t"
        "s_add_u32 s43, s34, 301056\n\t"
        "s_min_u32 s43, s43, 9590784\n\t"
        "buffer_load_dwordx4 v143, s[16:19], s43 offen  lds\n\t"
        "v_bfe_i32 v74, v156, 5, 1\n\t"
        "v_and_b32 v74, 7, v74\n\t"
        "v_xor_b32 v74, s4, v74\n\t"
        "v_mul_u32_u24 v68, 11, v74\n\t"
        "v_lshrrev_b32 v68, 5, v68\n\t"
        "v_mul_u32_u24 v75, 3, v68\n\t"
        "v_sub_u32 v69, v74, v75\n\t"
        "v_lshlrev_b32 v70, 2, v74\n\t"
        "v_mul_u32_u24 v74, 205, v156\n\t"
        "v_lshrrev_b32 v74, 14, v74\n\t"
        "v_min_u32 v74, 2, v74\n\t"
        "v_lshlrev_b32 v65, 2, v74\n\t"
        "v_mul_u32_u24 v74, 80, v74\n\t"
        "v_sub_u32 v66, v156, v74\n\t"
        "v_bfe_u32 v52, v156, 4, 1\n\t"
        "v_and_b32 v181, 48, v156\n\t"
        "s_mov_b64 s[56:57], 0\n\t"
        "s_mov_b32 s63, 0x435f0000\n\t"
        "s_mov_b32 s54, 0x7fff7fff\n\t"
        "v_bfe_u32 v159, v156, 4, 2\n\t"
        "v_mul_u32_u24 v62, 192, v158\n\t"
        "v_lshlrev_b32 v76, 3, v158\n\t"
        "v_and_b32 v77, 48, v76\n\t"
        "v_and_b32 v184, 24, v76\n\t"
        "s_mul_i32 s69, s68, 16384\n\t"
        "s_lshl_b32 s70, s49, 7\n\t"
        "s_lshl_b32 s71, s49, 5\n\t"
        "v_add_u32 v73, 8, v76\n\t"
        "v_and_b32 v73, 32, v73\n\t"
        "s_add_u32 s69, s69, 192\n\t"
        "v_xor_b32 v73, v181, v73\n\t"
        "v_add3_u32 v128, v73, v62, s69\n\t"
        "v_add_u32 v73, 16, v76\n\t"
        "v_and_b32 v73, 32, v73\n\t"
        "s_add_u32 s69, s69, 192\n\t"
        "v_xor_b32 v73, v181, v73\n\t"
        "v_add3_u32 v129, v73, v62, s69\n\t"
        "v_add_u32 v73, 24, v76\n\t"
        "v_and_b32 v73, 32, v73\n\t"
        "s_add_u32 s69, s69, 192\n\t"
        "v_xor_b32 v73, v181, v73\n\t"
        "v_add3_u32 v130, v73, v62, s69\n\t"
        "v_lshrrev_b32 v73, 2, v158\n\t"
        "v_lshl_add_u32 v74, v159, 3, v73\n\t"
        "v_mul_u32_u24 v74, 192, v74\n\t"
        "v_lshlrev_b32 v75, 1, v156\n\t"
        "v_and_b32 v75, 32, v75\n\t"
        "v_add3_u32 v74, v74, v75, v184\n\t"
        "v_add_u32 v74, 86016, v74\n\t"
        "v_xad_u32 v133, v74, s71, 64\n\t"
        "v_add_u32 v131, s70, v74\n\t"
        "v_xor_b32 v132, 32, v131\n\t"
        "v_subrev_u32 v138, 43008, v131\n\t"
        "v_subrev_u32 v139, 43008, v132\n\t"
        "v_subrev_u32 v149, 43008, v133\n\t"
        "v_lshrrev_b32 v74, 3, v157\n\t"
        "v_min_u32 v74, 2, v74\n\t"
        "v_lshlrev_b32 v81, 2, v74\n\t"
        "s_lshl_b32 s69, s4, 3\n\t"
        "v_and_or_b32 v82, v156, 7, s69\n\t"
        "v_mul_u32_u24 v80, 48, v82\n\t"
        "v_lshl_add_u32 v80, v74, 11, v80\n\t"
        "v_add_u32 v80, 147488, v80\n\t"
        "v_and_b32 v82, 15, v82\n\t"
        "v_and_b32 v74, 31, v156\n\t"
        "v_mad_u32_u24 v53, v74, 48, v70\n\t"
        "v_add_u32 v53, 147456, v53\n\t"
        "v_add_u32 v70, 157696, v70\n\t"
        "v_mul_u32_u24 v67, 320, v81\n\t"
        "s_add_u32 s69, s68, 1\n\t"
        "v_cvt_f32_u32 v56, s69\n\t"
        "v_add_u32 v57, 1, v82\n\t"
        "v_cvt_f32_u32 v57, v57\n\t"
        "v_lshrrev_b32 v83, 2, v157\n\t"
        "v_and_b32 v74, 3, v156\n\t"
        "v_lshlrev_b32 v119, 11, v83\n\t"
        "v_lshl_add_u32 v119, v74, 2, v119\n\t"
        "v_add_u32 v119, 122368, v119\n\t"
        "v_lshl_add_u32 v74, v74, 4, 64\n\t"
        "v_mad_u32_u24 v145, v83, s9, v74\n\t"
        "v_lshrrev_b32 v74, 1, v156\n\t"
        "v_and_or_b32 v74, v74, 16, v75\n\t"
        "v_add3_u32 v144, v74, v62, s70\n\t"
        "s_lshl_b32 s69, s68, 10\n\t"
        "s_add_u32 s69, s69, 135168\n\t"
        "v_add_u32 v74, v76, v159\n\t"
        "v_lshl_or_b32 v74, v74, 3, s71\n\t"
        "v_xad_u32 v146, v74, v77, s69\n\t"
        "s_lshl_b32 s70, s49, 11\n\t"
        "s_add_u32 s70, s70, s69\n\t"
        "v_lshlrev_b32 v74, 1, v156\n\t"
        "v_and_b32 v74, 48, v74\n\t"
        "v_xad_u32 v147, v74, v140, s70\n\t"
        "s_mul_i32 s48, s41, 36\n\t"
        "s_mul_i32 s41, s41, 192\n\t"
        "s_mul_i32 s69, s68, 172032\n\t"
        "s_add_u32 s41, s41, s69\n\t"
        "s_mul_i32 s69, s49, 43008\n\t"
        "s_add_u32 s50, s41, s69\n\t"
        "v_lshrrev_b32 v74, 5, v156\n\t"
        "v_mul_u32_u24 v74, 8064, v74\n\t"
        "v_and_b32 v55, 496, v140\n\t"
        "v_add_u32 v55, v55, v74\n\t"
        "v_lshl_add_u32 v61, v158, 2, v74\n\t"
        "s_mul_i32 s69, s68, 768\n\t"
        "s_add_u32 s69, s69, 147456\n\t"
        "v_mad_u32_u24 v187, v158, 48, s69\n\t"
        "v_lshl_add_u32 v74, v159, 3, v62\n\t"
        "v_xad_u32 v186, v74, v77, s37\n\t"
        "v_and_b32 v73, 2, v73\n\t"
        "v_add_u32 v73, v73, v159\n\t"
        "v_and_b32 v73, 3, v73\n\t"
        "v_and_or_b32 v73, v158, 12, v73\n\t"
        "s_add_u32 s69, s35, 157696\n\t"
        "v_lshl_add_u32 v185, v73, 4, s69\n\t"
        "v_add_u32 v118, s69, v140\n\t"
        "v_add_u32 v116, 1, v157\n\t"
        "v_mul_u32_u24 v74, 12288, v159\n\t"
        "v_add3_u32 v74, v74, v62, v77\n\t"
        "v_add_u32 v117, s37, v74\n\t"
        "v_lshlrev_b32 v73, 2, v158\n\t"
        "v_and_b32 v73, 16, v73\n\t"
        "s_mov_b32 s69, 0x0c0c0100\n\t"
        "v_alignbit_b32 v169, s69, s69, v73\n\t"
        "s_mov_b32 s69, 0x0c0c0302\n\t"
        "v_alignbit_b32 v171, s69, s69, v73\n\t"
        "v_mov_b32 v74, 0x0c0c0c0c\n\t"
        "s_mov_b32 s10, 2228241\n\t"
        "s_mov_b32 s11, 8912964\n\t"
        "v_cndmask_b32 v168, v74, v169, s[10:11]\n\t"
        "v_cndmask_b32 v170, v74, v171, s[10:11]\n\t"
        "s_mov_b32 s10, 2281718784\n\t"
        "s_mov_b32 s11, 570429696\n\t"
        "v_cndmask_b32 v169, v74, v169, s[10:11]\n\t"
        "v_cndmask_b32 v171, v74, v171, s[10:11]\n\t"
        "s_waitcnt vmcnt(10)\n\t"
        "v_readlane_b32 s7, v155, 27\n\t"
        "s_nop 1\n\t"
        "s_or_b32 s42, s42, s7\n\t"
        "s_bitcmp1_b32 s7, 8\n\t"
        "s_cbranch_scc1 20f\n\t"
        "s_bitcmp1_b32 s7, 9\n\t"
        "s_cbranch_scc1 21f\n\t"
        "ds_bpermute_b32 v64, v65, v155 offset:172\n\t"
        "s_add_u32 m0, s37, -24576\n\t"
        "s_add_u32 s36, s36, 6144\n\t"
        "buffer_load_dwordx4 v140, s[24:27], s36 offen offset:0 lds\n\t"
        "ds_bpermute_b32 v78, v81, v155 offset:148\n\t"
        "ds_bpermute_b32 v79, v81, v155 offset:160\n\t"
        "v_readlane_b32 s71, v155, 26\n\t"
        "v_readlane_b32 s10, v155, 29\n\t"
        "s_nop 0\n\t"
        "v_mul_u32_u24 v73, s71, v66\n\t"
        "v_lshrrev_b32 v73, 11, v73\n\t"
        "v_lshlrev_b32 v66, 4, v66\n\t"
        "v_mad_u32_u24 v73, v73, s10, v66\n\t"
        "s_waitcnt lgkmcnt(2)\n\t"
        "v_add_u32 v64, v64, v73\n\t"
        "buffer_load_dwordx4 v141, s[24:27], s36 offen offset:1024 lds\n\t"
        "v_readlane_b32 s69, v155, 34\n\t"
        "v_readlane_b32 s70, v155, 35\n\t"
        "s_nop 0\n\t"
        "v_mad_u32_u24 v70, v52, s69, v70\n\t"
        "v_mad_u32_u24 v70, v158, s70, v70\n\t"
        "s_mul_i32 s71, s69, s68\n\t"
        "s_add_u32 s71, s71, 157728\n\t"
        "v_mad_u32_u24 v67, v82, s70, v67\n\t"
        "v_add_u32 v67, s71, v67\n\t"
        "v_readlane_b32 s11, v155, 30\n\t"
        "buffer_load_dwordx4 v142, s[24:27], s36 offen offset:2048 lds\n\t"
        "s_movk_i32 s10, 223\n\t"
        "v_mov_b32 v75, 0x7ff00000\n\t"
        "v_readlane_b32 s12, v155, 24\n\t"
        "v_readlane_b32 s13, v155, 25\n\t"
        "s_nop 0\n\t"
        "s_bfe_u32 s70, s12, 524288\n\t"
        "s_bfe_u32 s71, s13, 524288\n\t"
        "v_cmp_gt_u32 vcc, s71, v71\n\t"
        "v_add3_u32 v73, v71, s70, -2\n\t"
        "v_med3_i32 v73, v73, 0, s10\n\t"
        "s_add_u32 m0, s35, 4096\n\t"
        "s_add_u32 s43, s34, 43008\n\t"
        "buffer_load_dwordx4 v143, s[16:19], s43 offen  lds\n\t"
        "v_cndmask_b32 v74, v75, v72, vcc\n\t"
        "v_mad_u32_u24 v134, v73, s11, v74\n\t"
        "s_bfe_u32 s70, s12, 524296\n\t"
        "s_bfe_u32 s71, s13, 524296\n\t"
        "v_cmp_gt_u32 vcc, s71, v71\n\t"
        "v_add3_u32 v73, v71, s70, -2\n\t"
        "v_med3_i32 v73, v73, 0, s10\n\t"
        "v_cndmask_b32 v74, v75, v72, vcc\n\t"
        "v_mad_u32_u24 v135, v73, s11, v74\n\t"
        "s_add_u32 m0, s35, 20480\n\t"
        "s_add_u32 s43, s34, 215040\n\t"
        "buffer_load_dwordx4 v143, s[16:19], s43 offen  lds\n\t"
        "s_bfe_u32 s70, s12, 524304\n\t"
        "s_bfe_u32 s71, s13, 524304\n\t"
        "v_cmp_gt_u32 vcc, s71, v71\n\t"
        "v_add3_u32 v73, v71, s70, -2\n\t"
        "v_med3_i32 v73, v73, 0, s10\n\t"
        "v_cndmask_b32 v74, v75, v72, vcc\n\t"
        "v_mad_u32_u24 v136, v73, s11, v74\n\t"
        "s_bfe_u32 s70, s12, 524312\n\t"
        "s_bfe_u32 s71, s13, 524312\n\t"
        "v_cmp_gt_u32 vcc, s71, v71\n\t"
        "v_add3_u32 v73, v71, s70, -2\n\t"
        "v_med3_i32 v73, v73, 0, s10\n\t"
        "v_cndmask_b32 v74, v75, v72, vcc\n\t"
        "v_mad_u32_u24 v137, v73, s11, v74\n\t"
        "s_mov_b64 exec, 0\n\t"
        "s_cmp_eq_u32 s4, 0\n\t"
        "s_cbranch_scc1 .LpM0%=\n\t"
        "s_cmp_eq_u32 s4, 1\n\t"
        "s_cbranch_scc1 .LpM1%=\n\t"
        "s_cmp_eq_u32 s4, 2\n\t"
        "s_cbranch_scc1 .LpM2%=\n\t"
        "s_branch .LwM%=\n\t"
        ".LrM%=:\n\t"
        "s_mov_b64 exec, -1\n\t"
        "s_waitcnt vmcnt(9)\n\t"
        "s_barrier\n\t"
        "ds_read_b64_tr_b16 a[0:1], v138 offset:6144\n\t"
        "ds_read_b64_tr_b16 a[2:3], v138 offset:6912\n\t"
        "ds_read_b64_tr_b16 a[4:5], v139 offset:6144\n\t"
        "ds_read_b64_tr_b16 a[6:7], v139 offset:6912\n\t"
        "ds_read_b64_tr_b16 a[8:9], v149 offset:6144\n\t"
        "ds_read_b64_tr_b16 a[10:11], v149 offset:6912\n\t"
        "ds_read_b128 v[84:87], v128 offset:8192\n\t"
        "ds_read_b64_tr_b16 a[12:13], v138 offset:12288\n\t"
        "ds_read_b64_tr_b16 a[14:15], v138 offset:13056\n\t"
        "ds_read_b64_tr_b16 a[16:17], v139 offset:12288\n\t"
        "ds_read_b64_tr_b16 a[18:19], v139 offset:13056\n\t"
        "ds_read_b64_tr_b16 a[20:21], v149 offset:12288\n\t"
        "ds_read_b64_tr_b16 a[22:23], v149 offset:13056\n\t"
        "ds_read_b128 v[88:91], v128 offset:8192\n\t"
        "s_add_u32 m0, s37, -12288\n\t"
        "s_add_u32 s36, s36, s39\n\t"
        "s_waitcnt lgkmcnt(7)\n\t"
        "v_mfma_f32_16x16x32_bf16 v[12:15], a[0:3], v[84:87], 0\n\t"
        "buffer_load_dwordx4 v140, s[24:27], s36 offen offset:0 lds\n\t"
        "buffer_load_dwordx4 v141, s[24:27], s36 offen offset:1024 lds\n\t"
        "buffer_load_dwordx4 v142, s[24:27], s36 offen offset:2048 lds\n\t"
        "s_add_u32 m0, s35, 16384\n\t"
        "s_add_u32 s43, s34, 172032\n\t"
        "buffer_load_dwordx4 v143, s[16:19], s43 offen  lds\n\t"
        "s_add_u32 m0, s35, 32768\n\t"
        "s_add_u32 s43, s34, 344064\n\t"
        "s_min_u32 s43, s43, 9590784\n\t"
        "buffer_load_dwordx4 v143, s[16:19], s43 offen  lds\n\t"
        "s_waitcnt vmcnt(10)\n\t"
        "s_barrier\n\t"
        "ds_read_b128 v[92:95], v128 offset:12288\n\t"
        "ds_read_b64_tr_b16 a[24:25], v138 offset:0\n\t"
        "v_mfma_f32_16x16x32_bf16 v[16:19], a[4:7], v[84:87], 0\n\t"
        "ds_read_b64_tr_b16 a[26:27], v138 offset:768\n\t"
        "s_add_u32 m0, s35, 157696\n\t"
        "s_nop 0\n\t"
        "buffer_load_dwordx4 v64, s[20:23], 0 offen lds\n\t"
        "s_add_u32 m0, s37, 0\n\t"
        "s_add_u32 s36, s36, s38\n\t"
        "v_mfma_f32_16x16x32_bf16 v[20:23], a[8:11], v[84:87], 0\n\t"
        "buffer_load_dwordx4 v140, s[24:27], s36 offen offset:0 lds\n\t"
        "ds_read_b64_tr_b16 a[28:29], v139 offset:0\n\t"
        "v_readlane_b32 s44, v155, 32\n\t"
        "s_waitcnt lgkmcnt(4)\n\t"
        "v_mfma_f32_16x16x32_bf16 v[24:27], a[12:15], v[88:91], 0\n\t"
        "ds_read_b64_tr_b16 a[30:31], v139 offset:768\n\t"
        "ds_read_b64_tr_b16 a[32:33], v149 offset:0\n\t"
        "ds_read_b64_tr_b16 a[34:35], v149 offset:768\n\t"
        "v_readlane_b32 s45, v155, 33\n\t"
        "v_mfma_f32_16x16x32_bf16 v[28:31], a[16:19], v[88:91], 0\n\t"
        "buffer_load_dwordx4 v141, s[24:27], s36 offen offset:1024 lds\n\t"
        "ds_read_b128 v[96:99], v128 offset:8192\n\t"
        "v_readlane_b32 s62, v155, 36\n\t"
        "v_mfma_f32_16x16x32_bf16 v[32:35], a[20:23], v[88:91], 0\n\t"
        "ds_read_b128 v[100:103], v128 offset:12288\n\t"
        "s_lshl_b32 s70, s62, 5\n\t"
        "s_waitcnt lgkmcnt(8)\n\t"
        "v_mfma_f32_16x16x32_bf16 v[24:27], a[0:3], v[92:95], v[24:27]\n\t"
        "s_waitcnt vmcnt(10)\n\t"
        "s_barrier\n\t"
        "ds_read_b64_tr_b16 a[36:37], v138 offset:18432\n\t"
        "ds_read_b64_tr_b16 a[38:39], v138 offset:19200\n\t"
        "v_mfma_f32_16x16x32_bf16 v[28:31], a[4:7], v[92:95], v[28:31]\n\t"
        "buffer_load_dwordx4 v142, s[24:27], s36 offen offset:2048 lds\n\t"
        "s_add_u32 m0, s35, 0\n\t"
        "ds_read_b64_tr_b16 a[40:41], v139 offset:18432\n\t"
        "s_add_u32 s43, s34, 0\n\t"
        "v_mfma_f32_16x16x32_bf16 v[32:35], a[8:11], v[92:95], v[32:35]\n\t"
        "ds_read_b64_tr_b16 a[42:43], v139 offset:19200\n\t"
        "ds_read_b64_tr_b16 a[44:45], v149 offset:18432\n\t"
        "s_sub_u32 s71, s70, 16384\n\t"
        "v_mfma_f32_16x16x32_bf16 v[36:39], a[12:15], v[92:95], 0\n\t"
        "buffer_load_dwordx4 v143, s[16:19], s43 offen  lds\n\t"
        "s_add_u32 m0, s35, 36864\n\t"
        "ds_read_b64_tr_b16 a[46:47], v149 offset:19200\n\t"
        "s_add_u32 s43, s34, 387072\n\t"
        "ds_read_b64_tr_b16 a[48:49], v138 offset:24576\n\t"
        "s_min_u32 s43, s43, 9590784\n\t"
        "v_mfma_f32_16x16x32_bf16 v[40:43], a[16:19], v[92:95], 0\n\t"
        "ds_read_b64_tr_b16 a[50:51], v138 offset:25344\n\t"
        "ds_read_b64_tr_b16 a[52:53], v139 offset:24576\n\t"
        "ds_read_b64_tr_b16 a[54:55], v139 offset:25344\n\t"
        "s_mul_i32 s71, s71, s68\n\t"
        "v_mfma_f32_16x16x32_bf16 v[44:47], a[20:23], v[92:95], 0\n\t"
        "buffer_load_dwordx4 v143, s[16:19], s43 offen  lds\n\t"
        "s_add_u32 m0, s37, 12288\n\t"
        "ds_read_b64_tr_b16 a[56:57], v149 offset:24576\n\t"
        "s_add_u32 s36, s36, s5\n\t"
        "ds_read_b64_tr_b16 a[58:59], v149 offset:25344\n\t"
        "s_waitcnt lgkmcnt(12)\n\t"
        "v_mfma_f32_16x16x32_bf16 v[0:3], a[24:27], v[96:99], 0\n\t"
        "ds_read_b128 v[104:107], v128 offset:8256\n\t"
        "v_add_u32 v172, s71, v128\n\t"
        "v_mfma_f32_16x16x32_bf16 v[4:7], a[28:31], v[96:99], 0\n\t"
        "s_mov_b32 exec_lo, 0\n\t"
        "s_mov_b32 exec_hi, 0xfff00000\n\t"
        "ds_write_b32 v119, v155\n\t"
        "s_mov_b64 exec, -1\n\t"
        "v_mfma_f32_16x16x32_bf16 v[8:11], a[32:35], v[96:99], 0\n\t"
        "buffer_load_dwordx4 v140, s[24:27], s36 offen offset:0 lds\n\t"
        "v_add_u32 v173, s71, v129\n\t"
        "v_mfma_f32_16x16x32_bf16 v[12:15], a[24:27], v[100:103], v[12:15]\n\t"
        "v_add_u32 v174, s71, v130\n\t"
        "s_add_u32 s71, s71, s70\n\t"
        "v_mfma_f32_16x16x32_bf16 v[16:19], a[28:31], v[100:103], v[16:19]\n\t"
        "v_add_u32 v175, s71, v128\n\t"
        "v_add_u32 v176, s71, v129\n\t"
        "v_mfma_f32_16x16x32_bf16 v[20:23], a[32:35], v[100:103], v[20:23]\n\t"
        "s_waitcnt vmcnt(9)\n\t"
        "s_barrier\n\t"
        "buffer_load_dwordx4 v141, s[24:27], s36 offen offset:1024 lds\n\t"
        "ds_read_b128 v[108:111], v128 offset:4096\n\t"
        "s_waitcnt lgkmcnt(2)\n\t"
        "v_mfma_f32_16x16x32_bf16 v[12:15], a[36:39], v[104:107], v[12:15]\n\t"
        "ds_read_b128 v[112:115], v128 offset:4160\n\t"
        "ds_read_b64_tr_b16 a[60:61], v138 offset:30720\n\t"
        "v_add_u32 v177, s71, v130\n\t"
        "v_mfma_f32_16x16x32_bf16 v[16:19], a[40:43], v[104:107], v[16:19]\n\t"
        "ds_read_b64_tr_b16 a[62:63], v138 offset:31488\n\t"
        "ds_read_b64_tr_b16 a[64:65], v139 offset:30720\n\t"
        "s_add_u32 s71, s71, s70\n\t"
        "v_mfma_f32_16x16x32_bf16 v[20:23], a[44:47], v[104:107], v[20:23]\n\t"
        "buffer_load_dwordx4 v142, s[24:27], s36 offen offset:2048 lds\n\t"
        "s_add_u32 m0, s37, 24576\n\t"
        "ds_read_b64_tr_b16 a[66:67], v139 offset:31488\n\t"
        "s_add_u32 s36, s36, s39\n\t"
        "v_mfma_f32_16x16x32_bf16 v[24:27], a[48:51], v[104:107], v[24:27]\n\t"
        "ds_read_b64_tr_b16 a[68:69], v149 offset:30720\n\t"
        "ds_read_b64_tr_b16 a[70:71], v149 offset:31488\n\t"
        "v_add_u32 v178, s71, v128\n\t"
        "v_mfma_f32_16x16x32_bf16 v[28:31], a[52:55], v[104:107], v[28:31]\n\t"
        "ds_read_b128 v[84:87], v128 offset:8256\n\t"
        "v_add_u32 v179, s71, v129\n\t"
        "v_mfma_f32_16x16x32_bf16 v[32:35], a[56:59], v[104:107], v[32:35]\n\t"
        "s_cmp_eq_u32 s69, 36\n\t"
        "s_cselect_b64 exec, -1, 0\n\t"
        "v_swap_b32 v68, v69\n\t"
        "v_swap_b32 v173, v175\n\t"
        "v_swap_b32 v174, v178\n\t"
        "v_swap_b32 v177, v179\n\t"
        "s_mov_b64 exec, -1\n\t"
        "s_waitcnt lgkmcnt(8)\n\t"
        "v_mfma_f32_16x16x32_bf16 v[0:3], a[0:3], v[108:111], v[0:3]\n\t"
        "ds_read_b128 v[88:91], v128 offset:12352\n\t"
        "ds_read_b64_tr_b16 a[72:73], v138 offset:36864\n\t"
        "v_add_u32 v180, s71, v130\n\t"
        "v_mfma_f32_16x16x32_bf16 v[4:7], a[4:7], v[108:111], v[4:7]\n\t"
        "ds_read_b64_tr_b16 a[74:75], v138 offset:37632\n\t"
        "ds_read_b64_tr_b16 a[76:77], v139 offset:36864\n\t"
        "v_add3_u32 v68, v68, v52, -1\n\t"
        "v_mfma_f32_16x16x32_bf16 v[8:11], a[8:11], v[108:111], v[8:11]\n\t"
        "ds_read_b64_tr_b16 a[78:79], v139 offset:37632\n\t"
        "ds_read_b64_tr_b16 a[80:81], v149 offset:36864\n\t"
        "v_add3_u32 v69, v69, v158, -1\n\t"
        "v_mfma_f32_16x16x32_bf16 v[12:15], a[12:15], v[108:111], v[12:15]\n\t"
        "ds_read_b64_tr_b16 a[82:83], v149 offset:37632\n\t"
        "v_cvt_f32_i32 v68, v68\n\t"
        "v_mfma_f32_16x16x32_bf16 v[16:19], a[16:19], v[108:111], v[16:19]\n\t"
        "v_cvt_f32_i32 v69, v69\n\t"
        "v_add_f32 v78, v78, v56\n\t"
        "v_mfma_f32_16x16x32_bf16 v[20:23], a[20:23], v[108:111], v[20:23]\n\t"
        "v_add_f32 v79, v79, v57\n\t"
        "v_readlane_b32 s69, v155, 31\n\t"
        "s_waitcnt lgkmcnt(7)\n\t"
        "v_mfma_f32_16x16x32_bf16 v[0:3], a[36:39], v[112:115], v[0:3]\n\t"
        "v_mov_b32 v154, s69\n\t"
        "v_mov_b32 v119, s62\n\t"
        "v_readlane_b32 s53, v155, 28\n\t"
        "v_mfma_f32_16x16x32_bf16 v[4:7], a[40:43], v[112:115], v[4:7]\n\t"
        "v_readlane_b32 s9, v155, 46\n\t"
        "s_sub_u32 s10, 43200, s11\n\t"
        "v_mfma_f32_16x16x32_bf16 v[8:11], a[44:47], v[112:115], v[8:11]\n\t"
        "s_mul_i32 s10, s10, s68\n\t"
        "s_sub_u32 s11, s11, 192\n\t"
        "v_mfma_f32_16x16x32_bf16 v[12:15], a[48:51], v[112:115], v[12:15]\n\t"
        "v_mad_u32_u24 v148, v158, s11, v144\n\t"
        "v_add_u32 v148, s10, v148\n\t"
        "v_mfma_f32_16x16x32_bf16 v[16:19], a[52:55], v[112:115], v[16:19]\n\t"
        "v_mad_u32_u24 v150, v83, s11, v145\n\t"
        "s_lshl_b32 s70, s49, 1\n\t"
        "v_mfma_f32_16x16x32_bf16 v[20:23], a[56:59], v[112:115], v[20:23]\n\t"
        "s_add_u32 s70, s70, 49\n\t"
        "v_readlane_b32 s52, v155, s70\n\t"
        "v_mfma_f32_16x16x32_bf16 v[0:3], a[60:63], v[84:87], v[0:3]\n\t"
        "s_nop 0\n\t"
        "s_add_u32 s52, s52, s10\n\t"
        "v_mfma_f32_16x16x32_bf16 v[4:7], a[64:67], v[84:87], v[4:7]\n\t"
        "v_mfma_f32_16x16x32_bf16 v[8:11], a[68:71], v[84:87], v[8:11]\n\t"
        "buffer_load_dwordx4 v140, s[24:27], s36 offen offset:0 lds\n\t"
        "s_waitcnt lgkmcnt(0)\n\t"
        "v_mfma_f32_16x16x32_bf16 v[12:15], a[60:63], v[88:91], v[12:15]\n\t"
        "v_mfma_f32_16x16x32_bf16 v[16:19], a[64:67], v[88:91], v[16:19]\n\t"
        "v_mfma_f32_16x16x32_bf16 v[20:23], a[68:71], v[88:91], v[20:23]\n\t"
        "v_mfma_f32_16x16x32_bf16 v[24:27], a[36:39], v[88:91], v[24:27]\n\t"
        "ds_read_b128 v[92:95], v128 offset:4224\n\t"
        "v_mfma_f32_16x16x32_bf16 v[28:31], a[40:43], v[88:91], v[28:31]\n\t"
        "buffer_load_dwordx4 v141, s[24:27], s36 offen offset:1024 lds\n\t"
        "v_mfma_f32_16x16x32_bf16 v[32:35], a[44:47], v[88:91], v[32:35]\n\t"
        "v_mfma_f32_16x16x32_bf16 v[36:39], a[48:51], v[88:91], v[36:39]\n\t"
        "ds_read_b128 v[96:99], v128 offset:8320\n\t"
        "v_mfma_f32_16x16x32_bf16 v[40:43], a[52:55], v[88:91], v[40:43]\n\t"
        "v_mfma_f32_16x16x32_bf16 v[44:47], a[56:59], v[88:91], v[44:47]\n\t"
        "buffer_load_dwordx4 v142, s[24:27], s36 offen offset:2048 lds\n\t"
        "s_waitcnt lgkmcnt(1)\n\t"
        "v_mfma_f32_16x16x32_bf16 v[0:3], a[72:75], v[92:95], v[0:3]\n\t"
        "ds_read_b128 v[100:103], v128 offset:12416\n\t"
        "v_mfma_f32_16x16x32_bf16 v[4:7], a[76:79], v[92:95], v[4:7]\n\t"
        "v_mfma_f32_16x16x32_bf16 v[8:11], a[80:83], v[92:95], v[8:11]\n\t"
        "buffer_load_dwordx4 v[48:51], v55, s[20:23], s48 offen\n\t"
        "buffer_load_dword v61, v61, s[20:23], s48 offen offset:512\n\t"
        "s_waitcnt vmcnt(10)\n\t"
        "s_barrier\n\t"
        "ds_read_b128 v[104:107], v128 offset:16384\n\t"
        "s_add_u32 m0, s37, 36864\n\t"
        "ds_read_b128 v[108:111], v128 offset:16448\n\t"
        "s_waitcnt lgkmcnt(3)\n\t"
        "v_mfma_f32_16x16x32_bf16 v[12:15], a[72:75], v[96:99], v[12:15]\n\t"
        "ds_read_b32 v64, v70 offset:0\n\t"
        "s_add_u32 s36, s36, s38\n\t"
        "ds_read_b64_tr_b16 a[84:85], v131 offset:0\n\t"
        "v_mfma_f32_16x16x32_bf16 v[16:19], a[76:79], v[96:99], v[16:19]\n\t"
        "ds_read_b32 v67, v67 offset:0\n\t"
        "ds_read_b64_tr_b16 a[86:87], v131 offset:768\n\t"
        "v_readlane_b32 s46, v155, 37\n\t"
        "v_mfma_f32_16x16x32_bf16 v[20:23], a[80:83], v[96:99], v[20:23]\n\t"
        "ds_read_b64_tr_b16 a[88:89], v132 offset:0\n\t"
        "ds_read_b64_tr_b16 a[90:91], v132 offset:768\n\t"
        "v_readlane_b32 s47, v155, 40\n\t"
        "s_waitcnt lgkmcnt(8)\n\t"
        "v_mfma_f32_16x16x32_bf16 v[24:27], a[72:75], v[100:103], v[24:27]\n\t"
        "buffer_load_dwordx4 v140, s[24:27], s36 offen offset:0 lds\n\t"
        "ds_read_b64_tr_b16 a[92:93], v133 offset:0\n\t"
        "v_add_f32 v58, s46, v68\n\t"
        "v_mfma_f32_16x16x32_bf16 v[28:31], a[76:79], v[100:103], v[28:31]\n\t"
        "ds_read_b64_tr_b16 a[94:95], v133 offset:768\n\t"
        "ds_read_b128 v[112:115], v128 offset:4224\n\t"
        "v_add_f32 v59, s47, v69\n\t"
        "v_mfma_f32_16x16x32_bf16 v[32:35], a[80:83], v[100:103], v[32:35]\n\t"
        "ds_read_b64_tr_b16 a[96:97], v131 offset:6144\n\t"
        "ds_read_b64_tr_b16 a[98:99], v131 offset:6912\n\t"
        "s_waitcnt lgkmcnt(12)\n\t"
        "v_mfma_f32_16x16x32_bf16 v[24:27], a[24:27], v[104:107], v[24:27]\n\t"
        "ds_read_b64_tr_b16 a[100:101], v132 offset:6144\n\t"
        "ds_read_b64_tr_b16 a[102:103], v132 offset:6912\n\t"
        "v_mfma_f32_16x16x32_bf16 v[28:31], a[28:31], v[104:107], v[28:31]\n\t"
        "buffer_load_dwordx4 v141, s[24:27], s36 offen offset:1024 lds\n\t"
        "ds_read_b64_tr_b16 a[104:105], v133 offset:6144\n\t"
        "v_mfma_f32_16x16x32_bf16 v[32:35], a[32:35], v[104:107], v[32:35]\n\t"
        "ds_read_b64_tr_b16 a[106:107], v133 offset:6912\n\t"
        "v_mfma_f32_16x16x32_bf16 v[36:39], a[0:3], v[104:107], v[36:39]\n\t"
        "ds_read_b128 v[84:87], v128 offset:8320\n\t"
        "s_waitcnt lgkmcnt(12)\n\t"
        "v_perm_b32 v56, v64, v64, s44\n\t"
        "v_perm_b32 v57, v64, v64, s45\n\t"
        "v_mfma_f32_16x16x32_bf16 v[40:43], a[4:7], v[104:107], v[40:43]\n\t"
        "ds_read_b32 v66, v70 offset:2560\n\t"
        "v_add_f32 v56, v56, v58\n\t"
        "v_mfma_f32_16x16x32_bf16 v[44:47], a[8:11], v[104:107], v[44:47]\n\t"
        "buffer_load_dwordx4 v142, s[24:27], s36 offen offset:2048 lds\n\t"
        "s_add_u32 m0, s37, 49152\n\t"
        "v_mfma_f32_16x16x32_bf16 v[24:27], a[60:63], v[108:111], v[24:27]\n\t"
        "ds_read_b128 v[88:91], v128 offset:12416\n\t"
        "s_add_u32 s36, s36, s5\n\t"
        "v_mfma_f32_16x16x32_bf16 v[28:31], a[64:67], v[108:111], v[28:31]\n\t"
        ".LpM0%=:\n\t"
        "s_cbranch_execz .LrM%=\n\t"
        "ds_read_b128 v[92:95], v128 offset:16512\n\t"
        "v_add_f32 v57, v57, v59\n\t"
        "v_mfma_f32_16x16x32_bf16 v[32:35], a[68:71], v[108:111], v[32:35]\n\t"
        "v_cvt_pk_bf16_f32 v52, v56, v57\n\t"
        "v_cvt_pk_bf16_f32 v58, v58, v59\n\t"
        "v_mfma_f32_16x16x32_bf16 v[36:39], a[36:39], v[108:111], v[36:39]\n\t"
        "buffer_load_dwordx4 v140, s[24:27], s36 offen offset:0 lds\n\t"
        "v_cmp_ne_u32 s[64:65], v52, v58\n\t"
        "v_mfma_f32_16x16x32_bf16 v[40:43], a[40:43], v[108:111], v[40:43]\n\t"
        "s_nop 0\n\t"
        "v_mbcnt_lo_u32_b32 v56, s64, 1\n\t"
        "v_mbcnt_hi_u32_b32 v56, s65, v56\n\t"
        "v_mfma_f32_16x16x32_bf16 v[44:47], a[44:47], v[108:111], v[44:47]\n\t"
        "s_bcnt1_i32_b64 s61, s[64:65]\n\t"
        "v_add_u32 v54, s61, 1\n\t"
        "s_waitcnt lgkmcnt(4)\n\t"
        "v_mfma_f32_16x16x32_bf16 v[12:15], a[84:87], v[112:115], v[12:15]\n\t"
        "v_lshl_add_u32 v56, v56, 3, s60\n\t"
        "v_perm_b32 v57, v67, v67, s45\n\t"
        "v_mfma_f32_16x16x32_bf16 v[16:19], a[88:91], v[112:115], v[16:19]\n\t"
        "s_mov_b64 exec, s[64:65]\n\t"
        "ds_write_b64 v56, v[52:53]\n\t"
        "s_mov_b64 exec, -1\n\t"
        "v_mfma_f32_16x16x32_bf16 v[20:23], a[92:95], v[112:115], v[20:23]\n\t"
        "buffer_load_dwordx4 v141, s[24:27], s36 offen offset:1024 lds\n\t"
        "v_swap_b32 v53, v80\n\t"
        "s_waitcnt lgkmcnt(3)\n\t"
        "v_mfma_f32_16x16x32_bf16 v[0:3], a[96:99], v[84:87], v[0:3]\n\t"
        "v_perm_b32 v56, v67, v67, s44\n\t"
        "v_add_f32 v56, v56, v78\n\t"
        "v_mfma_f32_16x16x32_bf16 v[4:7], a[100:103], v[84:87], v[4:7]\n\t"
        "v_add_f32 v57, v57, v79\n\t"
        "v_cvt_pk_bf16_f32 v52, v56, v57\n\t"
        "v_mfma_f32_16x16x32_bf16 v[8:11], a[104:107], v[84:87], v[8:11]\n\t"
        "v_cvt_pk_bf16_f32 v58, v78, v79\n\t"
        "v_cmp_ne_u32 s[64:65], v52, v58\n\t"
        "v_mfma_f32_16x16x32_bf16 v[24:27], a[84:87], v[84:87], v[24:27]\n\t"
        "buffer_load_dwordx4 v142, s[24:27], s36 offen offset:2048 lds\n\t"
        "v_readlane_b32 s40, v155, 12\n\t"
        "v_mfma_f32_16x16x32_bf16 v[28:31], a[88:91], v[84:87], v[28:31]\n\t"
        "v_readlane_b32 s43, v155, 0\n\t"
        "s_add_u32 m0, s40, s35\n\t"
        "s_and_b64 s[64:65], s[64:65], 0xffffff\n\t"
        "v_mfma_f32_16x16x32_bf16 v[32:35], a[92:95], v[84:87], v[32:35]\n\t"
        "v_mbcnt_lo_u32_b32 v56, s64, v54\n\t"
        "s_bcnt1_i32_b64 s46, s[64:65]\n\t"
        "s_waitcnt lgkmcnt(1)\n\t"
        "v_mfma_f32_16x16x32_bf16 v[12:15], a[96:99], v[88:91], v[12:15]\n\t"
        "s_waitcnt vmcnt(8)\n\t"
        "s_barrier\n\t"
        "ds_read_b128 v[96:99], v128 offset:0\n\t"
        "ds_read_b128 v[100:103], v128 offset:64\n\t"
        "v_mfma_f32_16x16x32_bf16 v[16:19], a[100:103], v[88:91], v[16:19]\n\t"
        "buffer_load_dwordx4 v134, s[16:19], s43 offen  lds\n\t"
        "ds_read_b128 v[104:107], v128 offset:128\n\t"
        "v_readlane_b32 s40, v155, 13\n\t"
        "v_mfma_f32_16x16x32_bf16 v[20:23], a[104:107], v[88:91], v[20:23]\n\t"
        "ds_read_b128 v[108:111], v128 offset:20480\n\t"
        "ds_read_b64_tr_b16 a[108:109], v131 offset:12288\n\t"
        "v_readlane_b32 s43, v155, 1\n\t"
        "v_mfma_f32_16x16x32_bf16 v[36:39], a[84:87], v[88:91], v[36:39]\n\t"
        "ds_read_b128 v[112:115], v128 offset:20544\n\t"
        "s_add_u32 m0, s40, s35\n\t"
        "ds_read_b64_tr_b16 a[110:111], v131 offset:13056\n\t"
        "v_mfma_f32_16x16x32_bf16 v[40:43], a[88:91], v[88:91], v[40:43]\n\t"
        "ds_read_b64_tr_b16 a[112:113], v132 offset:12288\n\t"
        "ds_read_b64_tr_b16 a[114:115], v132 offset:13056\n\t"
        "s_add_u32 s61, s61, s46\n\t"
        "v_mfma_f32_16x16x32_bf16 v[44:47], a[92:95], v[88:91], v[44:47]\n\t"
        "buffer_load_dwordx4 v135, s[16:19], s43 offen  lds\n\t"
        "ds_read_b64_tr_b16 a[116:117], v133 offset:12288\n\t"
        "v_readlane_b32 s40, v155, 14\n\t"
        "v_mfma_f32_16x16x32_bf16 v[24:27], a[96:99], v[92:95], v[24:27]\n\t"
        "ds_read_b128 v[84:87], v128 offset:20608\n\t"
        "ds_read_b64_tr_b16 a[118:119], v133 offset:13056\n\t"
        "v_readlane_b32 s43, v155, 2\n\t"
        "v_mfma_f32_16x16x32_bf16 v[28:31], a[100:103], v[92:95], v[28:31]\n\t"
        "ds_read_b64_tr_b16 a[120:121], v131 offset:18432\n\t"
        "s_add_u32 m0, s40, s35\n\t"
        "v_mfma_f32_16x16x32_bf16 v[32:35], a[104:107], v[92:95], v[32:35]\n\t"
        "ds_read_b64_tr_b16 a[122:123], v131 offset:19200\n\t"
        "v_add_u32 v54, s46, v54\n\t"
        "v_mfma_f32_16x16x32_bf16 v[36:39], a[72:75], v[92:95], v[36:39]\n\t"
        "buffer_load_dwordx4 v134, s[16:19], s43 offen  lds\n\t"
        "s_bitcmp1_b32 s42, 10\n\t"
        "s_cbranch_scc0 .Lx40%=\n\t"
        "v_readlane_b32 s40, v155, 20\n\t"
        "v_readlane_b32 s43, v155, 8\n\t"
        "s_nop 0\n\t"
        "s_add_u32 m0, s40, s35\n\t"
        "s_nop 2\n\t"
        "buffer_load_dwordx4 v136, s[16:19], s43 offen  lds\n\t"
        ".Lx40%=:\n\t"
        "v_mfma_f32_16x16x32_bf16 v[40:43], a[76:79], v[92:95], v[40:43]\n\t"
        "ds_read_b64_tr_b16 a[124:125], v132 offset:18432\n\t"
        "v_readlane_b32 s40, v155, 15\n\t"
        "v_mfma_f32_16x16x32_bf16 v[44:47], a[80:83], v[92:95], v[44:47]\n\t"
        "ds_read_b64_tr_b16 a[126:127], v132 offset:19200\n\t"
        "v_readlane_b32 s43, v155, 3\n\t"
        "s_waitcnt lgkmcnt(8)\n\t"
        "v_mfma_f32_16x16x32_bf16 v[0:3], a[12:15], v[96:99], v[0:3]\n\t"
        "ds_read_b64_tr_b16 a[128:129], v133 offset:18432\n\t"
        "s_add_u32 m0, s40, s35\n\t"
        "ds_read_b64_tr_b16 a[130:131], v133 offset:19200\n\t"
        "v_mfma_f32_16x16x32_bf16 v[4:7], a[16:19], v[96:99], v[4:7]\n\t"
        "ds_read_b128 v[88:91], v129 offset:0\n\t"
        "s_mov_b32 s3, s61\n\t"
        "v_mfma_f32_16x16x32_bf16 v[8:11], a[20:23], v[96:99], v[8:11]\n\t"
        "ds_read_b128 v[92:95], v129 offset:4096\n\t"
        "v_lshl_add_u32 v56, v56, 3, s60\n\t"
        "v_mfma_f32_16x16x32_bf16 v[0:3], a[48:51], v[100:103], v[0:3]\n\t"
        "s_mov_b64 exec, s[64:65]\n\t"
        "ds_write_b64 v56, v[52:53]\n\t"
        "s_mov_b64 exec, -1\n\t"
        "v_mfma_f32_16x16x32_bf16 v[4:7], a[52:55], v[100:103], v[4:7]\n\t"
        "ds_read_b64_tr_b16 a[132:133], v131 offset:24576\n\t"
        "v_add_u32 v53, 4096, v80\n\t"
        "v_mfma_f32_16x16x32_bf16 v[8:11], a[56:59], v[100:103], v[8:11]\n\t"
        "ds_read_b64_tr_b16 a[134:135], v131 offset:25344\n\t"
        "v_readlane_b32 s46, v155, 39\n\t"
        "v_mfma_f32_16x16x32_bf16 v[0:3], a[84:87], v[104:107], v[0:3]\n\t"
        "buffer_load_dwordx4 v135, s[16:19], s43 offen  lds\n\t"
        "s_add_u32 m0, s37, 0\n\t"
        "v_mfma_f32_16x16x32_bf16 v[4:7], a[88:91], v[104:107], v[4:7]\n\t"
        "ds_read_b64_tr_b16 a[136:137], v132 offset:24576\n\t"
        "s_add_u32 s36, s36, s39\n\t"
        "v_mfma_f32_16x16x32_bf16 v[8:11], a[92:95], v[104:107], v[8:11]\n\t"
        "ds_read_b64_tr_b16 a[138:139], v132 offset:25344\n\t"
        "v_readlane_b32 s47, v155, 42\n\t"
        "v_mfma_f32_16x16x32_bf16 v[36:39], a[24:27], v[108:111], v[36:39]\n\t"
        "ds_read_b64_tr_b16 a[140:141], v133 offset:24576\n\t"
        "v_perm_b32 v56, v66, v66, s44\n\t"
        "v_mfma_f32_16x16x32_bf16 v[40:43], a[28:31], v[108:111], v[40:43]\n\t"
        "ds_read_b64_tr_b16 a[142:143], v133 offset:25344\n\t"
        "v_perm_b32 v57, v66, v66, s45\n\t"
        "v_mfma_f32_16x16x32_bf16 v[44:47], a[32:35], v[108:111], v[44:47]\n\t"
        "buffer_load_dwordx4 v140, s[24:27], s36 offen offset:0 lds\n\t"
        "v_add_f32 v58, s46, v68\n\t"
        "v_mfma_f32_16x16x32_bf16 v[36:39], a[60:63], v[112:115], v[36:39]\n\t"
        "ds_read_b128 v[96:99], v129 offset:8192\n\t"
        "v_add_f32 v59, s47, v69\n\t"
        "v_mfma_f32_16x16x32_bf16 v[40:43], a[64:67], v[112:115], v[40:43]\n\t"
        "ds_read_b64_tr_b16 a[144:145], v131 offset:30720\n\t"
        "v_add_f32 v56, v56, v58\n\t"
        "v_mfma_f32_16x16x32_bf16 v[44:47], a[68:71], v[112:115], v[44:47]\n\t"
        "ds_read_b64_tr_b16 a[146:147], v131 offset:31488\n\t"
        "v_add_f32 v57, v57, v59\n\t"
        "s_waitcnt lgkmcnt(8)\n\t"
        "v_mfma_f32_16x16x32_bf16 v[36:39], a[96:99], v[84:87], v[36:39]\n\t"
        "buffer_load_dwordx4 v141, s[24:27], s36 offen offset:1024 lds\n\t"
        "v_max_f32 v56, 0, v56\n\t"
        "v_mfma_f32_16x16x32_bf16 v[40:43], a[100:103], v[84:87], v[40:43]\n\t"
        "ds_read_b128 v[100:103], v129 offset:12288\n\t"
        "v_max_f32 v57, 0, v57\n\t"
        "v_mfma_f32_16x16x32_bf16 v[44:47], a[104:107], v[84:87], v[44:47]\n\t"
        "ds_read_b64_tr_b16 a[148:149], v132 offset:30720\n\t"
        "v_max_f32 v58, 0, v58\n\t"
        "v_mfma_f32_16x16x32_bf16 v[0:3], a[120:123], v[88:91], v[0:3]\n\t"
        "ds_read_b64_tr_b16 a[150:151], v132 offset:31488\n\t"
        "v_max_f32 v59, 0, v59\n\t"
        "v_mfma_f32_16x16x32_bf16 v[4:7], a[124:127], v[88:91], v[4:7]\n\t"
        "buffer_load_dwordx4 v142, s[24:27], s36 offen offset:2048 lds\n\t"
        "s_add_u32 m0, s37, 12288\n\t"
        "v_mfma_f32_16x16x32_bf16 v[8:11], a[128:131], v[88:91], v[8:11]\n\t"
        "ds_read_b64_tr_b16 a[152:153], v133 offset:30720\n\t"
        "s_add_u32 s36, s36, s38\n\t"
        "v_mfma_f32_16x16x32_bf16 v[0:3], a[108:111], v[92:95], v[0:3]\n\t"
        "ds_read_b64_tr_b16 a[154:155], v133 offset:31488\n\t"
        "v_cvt_pk_bf16_f32 v52, v56, v57\n\t"
        "v_mfma_f32_16x16x32_bf16 v[4:7], a[112:115], v[92:95], v[4:7]\n\t"
        "v_cvt_pk_bf16_f32 v58, v58, v59\n\t"
        "v_cmp_ne_u32 s[64:65], v52, v58\n\t"
        "v_mfma_f32_16x16x32_bf16 v[8:11], a[116:119], v[92:95], v[8:11]\n\t"
        "s_nop 0\n\t"
        "v_mbcnt_lo_u32_b32 v56, s64, v54\n\t"
        "v_mbcnt_hi_u32_b32 v56, s65, v56\n\t"
        "v_mfma_f32_16x16x32_bf16 v[12:15], a[120:123], v[92:95], v[12:15]\n\t"
        "ds_read_b128 v[104:107], v129 offset:16384\n\t"
        "s_bcnt1_i32_b64 s46, s[64:65]\n\t"
        "v_mfma_f32_16x16x32_bf16 v[16:19], a[124:127], v[92:95], v[16:19]\n\t"
        "s_add_u32 s61, s61, s46\n\t"
        "v_min_u32 v56, 97, v56\n\t"
        "v_mfma_f32_16x16x32_bf16 v[20:23], a[128:131], v[92:95], v[20:23]\n\t"
        "v_lshl_add_u32 v56, v56, 3, s60\n\t"
        "s_waitcnt lgkmcnt(5)\n\t"
        "v_mfma_f32_16x16x32_bf16 v[0:3], a[132:135], v[96:99], v[0:3]\n\t"
        "s_mov_b64 exec, s[64:65]\n\t"
        "ds_write_b64 v56, v[52:53]\n\t"
        "s_mov_b64 exec, -1\n\t"
        "v_mfma_f32_16x16x32_bf16 v[4:7], a[136:139], v[96:99], v[4:7]\n\t"
        "s_cmp_gt_u32 s61, s53\n\t"
        "s_cselect_b32 s8, 1, 0\n\t"
        "v_mfma_f32_16x16x32_bf16 v[8:11], a[140:143], v[96:99], v[8:11]\n\t"
        "s_waitcnt vmcnt(14)\n\t"
        "v_and_b32 v48, s54, v48\n\t"
        "v_pk_max_u16 v60, v154, v48\n\t"
        "v_mfma_f32_16x16x32_bf16 v[12:15], a[108:111], v[96:99], v[12:15]\n\t"
        "ds_read_b128 v[108:111], v129 offset:20480\n\t"
        "v_and_b32 v49, s54, v49\n\t"
        "v_mfma_f32_16x16x32_bf16 v[16:19], a[112:115], v[96:99], v[16:19]\n\t"
        "v_pk_max_u16 v60, v60, v49\n\t"
        "v_and_b32 v50, s54, v50\n\t"
        "v_mfma_f32_16x16x32_bf16 v[20:23], a[116:119], v[96:99], v[20:23]\n\t"
        "v_pk_max_u16 v60, v60, v50\n\t"
        "v_and_b32 v51, s54, v51\n\t"
        "v_mfma_f32_16x16x32_bf16 v[24:27], a[120:123], v[96:99], v[24:27]\n\t"
        "v_pk_max_u16 v60, v60, v51\n\t"
        "s_waitcnt vmcnt(13)\n\t"
        "v_and_b32 v61, s54, v61\n\t"
        "v_mfma_f32_16x16x32_bf16 v[28:31], a[124:127], v[96:99], v[28:31]\n\t"
        "v_pk_max_u16 v60, v60, v61\n\t"
        "v_cmp_ne_u32 s[12:13], v60, v154\n\t"
        "v_mfma_f32_16x16x32_bf16 v[32:35], a[128:131], v[96:99], v[32:35]\n\t"
        "s_waitcnt vmcnt(7) lgkmcnt(2)\n\t"
        "s_barrier\n\t"
        "v_mfma_f32_16x16x32_bf16 v[12:15], a[132:135], v[100:103], v[12:15]\n\t"
        "buffer_load_dwordx4 v140, s[24:27], s36 offen offset:0 lds\n\t"
        "ds_read_b64_tr_b16 a[156:157], v131 offset:36864\n\t"
        "s_lshl_b32 s6, 256, s4\n\t"
        "v_mfma_f32_16x16x32_bf16 v[16:19], a[136:139], v[100:103], v[16:19]\n\t"
        "s_cmp_lg_u64 s[12:13], 0\n\t"
        "s_cselect_b32 s6, s6, 0\n\t"
        "ds_read_b64_tr_b16 a[158:159], v131 offset:37632\n\t"
        "ds_read_b64_tr_b16 a[160:161], v132 offset:36864\n\t"
        "v_mfma_f32_16x16x32_bf16 v[20:23], a[140:143], v[100:103], v[20:23]\n\t"
        "ds_read_b64_tr_b16 a[162:163], v132 offset:37632\n\t"
        "ds_read_b64_tr_b16 a[164:165], v133 offset:36864\n\t"
        "s_or_b32 s56, s56, s8\n\t"
        "v_mfma_f32_16x16x32_bf16 v[24:27], a[108:111], v[100:103], v[24:27]\n\t"
        "s_cmp_gt_u32 s3, 64\n\t"
        "s_cselect_b32 s8, 1, 0\n\t"
        "ds_read_b64_tr_b16 a[166:167], v133 offset:37632\n\t"
        "v_mfma_f32_16x16x32_bf16 v[28:31], a[112:115], v[100:103], v[28:31]\n\t"
        "buffer_load_dwordx4 v141, s[24:27], s36 offen offset:1024 lds\n\t"
        "s_or_b32 s56, s56, s8\n\t"
        "v_mfma_f32_16x16x32_bf16 v[32:35], a[116:119], v[100:103], v[32:35]\n\t"
        "ds_read_b128 v[112:115], v129 offset:64\n\t"
        "s_min_u32 s61, s61, 80\n\t"
        "v_mfma_f32_16x16x32_bf16 v[36:39], a[120:123], v[100:103], v[36:39]\n\t"
        "ds_read_b128 v[84:87], v129 offset:4160\n\t"
        "v_min_u32 v78, s61, v116\n\t"
        "v_mfma_f32_16x16x32_bf16 v[40:43], a[124:127], v[100:103], v[40:43]\n\t"
        "ds_read_b64_tr_b16 a[168:169], v131 offset:43008\n\t"
        "v_lshl_add_u32 v78, v78, 3, s60\n\t"
        "v_mfma_f32_16x16x32_bf16 v[44:47], a[128:131], v[100:103], v[44:47]\n\t"
        "ds_read_b64 v[62:63], v78\n\t"
        "v_cmp_ge_u32 s[66:67], s61, v116\n\t"
        "v_mfma_f32_16x16x32_bf16 v[24:27], a[132:135], v[104:107], v[24:27]\n\t"
        "buffer_load_dwordx4 v142, s[24:27], s36 offen offset:2048 lds\n\t"
        "v_readlane_b32 s40, v155, 16\n\t"
        "v_mfma_f32_16x16x32_bf16 v[28:31], a[136:139], v[104:107], v[28:31]\n\t"
        "ds_read_b64_tr_b16 a[170:171], v131 offset:43776\n\t"
        "v_readlane_b32 s43, v155, 4\n\t"
        "v_mfma_f32_16x16x32_bf16 v[32:35], a[140:143], v[104:107], v[32:35]\n\t"
        "ds_read_b64_tr_b16 a[172:173], v132 offset:43008\n\t"
        "s_add_u32 m0, s40, s35\n\t"
        "v_mfma_f32_16x16x32_bf16 v[36:39], a[108:111], v[104:107], v[36:39]\n\t"
        "ds_read_b64_tr_b16 a[174:175], v132 offset:43776\n\t"
        "ds_read_b64_tr_b16 a[176:177], v133 offset:43008\n\t"
        "s_mov_b32 s46, 0x07060302\n\t"
        "v_mfma_f32_16x16x32_bf16 v[40:43], a[112:115], v[104:107], v[40:43]\n\t"
        "buffer_load_dwordx4 v134, s[16:19], s43 offen  lds\n\t"
        "ds_read_b64_tr_b16 a[178:179], v133 offset:43776\n\t"
        "s_bitcmp1_b32 s42, 11\n\t"
        "s_cbranch_scc0 .Lx42%=\n\t"
        "v_readlane_b32 s40, v155, 21\n\t"
        "v_readlane_b32 s43, v155, 9\n\t"
        "s_nop 0\n\t"
        "s_add_u32 m0, s40, s35\n\t"
        "s_nop 2\n\t"
        "buffer_load_dwordx4 v137, s[16:19], s43 offen  lds\n\t"
        "v_readlane_b32 s40, v155, 22\n\t"
        "v_readlane_b32 s43, v155, 10\n\t"
        "s_nop 0\n\t"
        "s_add_u32 m0, s40, s35\n\t"
        "s_nop 2\n\t"
        "buffer_load_dwordx4 v136, s[16:19], s43 offen  lds\n\t"
        ".Lx42%=:\n\t"
        "v_mfma_f32_16x16x32_bf16 v[44:47], a[116:119], v[104:107], v[44:47]\n\t"
        "ds_read_b128 v[88:91], v129 offset:8256\n\t"
        "v_readlane_b32 s40, v155, 17\n\t"
        "s_waitcnt lgkmcnt(8)\n\t"
        "v_mfma_f32_16x16x32_bf16 v[36:39], a[132:135], v[108:111], v[36:39]\n\t"
        "ds_read_b128 v[92:95], v129 offset:12352\n\t"
        "v_readlane_b32 s43, v155, 5\n\t"
        "v_mfma_f32_16x16x32_bf16 v[40:43], a[136:139], v[108:111], v[40:43]\n\t"
        "ds_read_b64_tr_b16 a[180:181], v131 offset:49152\n\t"
        "s_add_u32 m0, s40, s35\n\t"
        "v_mfma_f32_16x16x32_bf16 v[44:47], a[140:143], v[108:111], v[44:47]\n\t"
        "s_mov_b64 exec, s[66:67]\n\t"
        "s_waitcnt lgkmcnt(8)\n\t"
        "ds_write_b32 v63, v117\n\t"
        "s_mov_b64 exec, -1\n\t"
        "v_mfma_f32_16x16x32_bf16 v[0:3], a[156:159], v[112:115], v[0:3]\n\t"
        "buffer_load_dwordx4 v135, s[16:19], s43 offen  lds\n\t"
        "v_readlane_b32 s40, v155, 18\n\t"
        "v_mfma_f32_16x16x32_bf16 v[4:7], a[160:163], v[112:115], v[4:7]\n\t"
        "ds_read_b64_tr_b16 a[182:183], v131 offset:49920\n\t"
        "v_readlane_b32 s43, v155, 6\n\t"
        "v_mfma_f32_16x16x32_bf16 v[8:11], a[164:167], v[112:115], v[8:11]\n\t"
        "ds_read_b64_tr_b16 a[184:185], v132 offset:49152\n\t"
        "s_add_u32 m0, s40, s35\n\t"
        "v_mfma_f32_16x16x32_bf16 v[0:3], a[144:147], v[84:87], v[0:3]\n\t"
        "ds_read_b64_tr_b16 a[186:187], v132 offset:49920\n\t"
        "v_and_b32 v78, 0xfffff800, v63\n\t"
        "v_mfma_f32_16x16x32_bf16 v[4:7], a[148:151], v[84:87], v[4:7]\n\t"
        "ds_read_b128 v[64:67], v78 offset:1536\n\t"
        "v_lshlrev_b32 v72, 16, v62\n\t"
        "v_mfma_f32_16x16x32_bf16 v[8:11], a[152:155], v[84:87], v[8:11]\n\t"
        "buffer_load_dwordx4 v136, s[16:19], s43 offen  lds\n\t"
        "s_bitcmp1_b32 s42, 11\n\t"
        "s_cbranch_scc0 .Lx44%=\n\t"
        "v_readlane_b32 s40, v155, 23\n\t"
        "v_readlane_b32 s43, v155, 11\n\t"
        "s_nop 0\n\t"
        "s_add_u32 m0, s40, s35\n\t"
        "s_nop 2\n\t"
        "buffer_load_dwordx4 v137, s[16:19], s43 offen  lds\n\t"
        ".Lx44%=:\n\t"
        "v_mfma_f32_16x16x32_bf16 v[12:15], a[156:159], v[84:87], v[12:15]\n\t"
        "ds_read_b128 v[96:99], v129 offset:16448\n\t"
        "ds_read_b64_tr_b16 a[188:189], v133 offset:49152\n\t"
        "v_readlane_b32 s40, v155, 19\n\t"
        "v_mfma_f32_16x16x32_bf16 v[16:19], a[160:163], v[84:87], v[16:19]\n\t"
        "ds_read_b64_tr_b16 a[190:191], v133 offset:49920\n\t"
        "v_readlane_b32 s43, v155, 7\n\t"
        "v_mfma_f32_16x16x32_bf16 v[20:23], a[164:167], v[84:87], v[20:23]\n\t"
        "ds_read_b64_tr_b16 a[192:193], v131 offset:55296\n\t"
        "s_add_u32 m0, s40, s35\n\t"
        "s_waitcnt lgkmcnt(8)\n\t"
        "v_mfma_f32_16x16x32_bf16 v[0:3], a[168:171], v[88:91], v[0:3]\n\t"
        "buffer_load_dwordx4 v137, s[16:19], s43 offen  lds\n\t"
        "s_add_u32 m0, s37, 24576\n\t"
        "v_mfma_f32_16x16x32_bf16 v[4:7], a[172:175], v[88:91], v[4:7]\n\t"
        "ds_read_b64_tr_b16 a[194:195], v131 offset:56064\n\t"
        "s_add_u32 s36, s36, s5\n\t"
        "v_mfma_f32_16x16x32_bf16 v[8:11], a[176:179], v[88:91], v[8:11]\n\t"
        "ds_read_b64_tr_b16 a[196:197], v132 offset:55296\n\t"
        "v_and_b32 v73, 0xffff0000, v62\n\t"
        "v_mfma_f32_16x16x32_bf16 v[12:15], a[144:147], v[88:91], v[12:15]\n\t"
        "ds_read_b128 v[100:103], v129 offset:20544\n\t"
        "v_med3_f32 v72, v72, 0, s63\n\t"
        "v_mfma_f32_16x16x32_bf16 v[16:19], a[148:151], v[88:91], v[16:19]\n\t"
        "buffer_load_dwordx4 v140, s[24:27], s36 offen offset:0 lds\n\t"
        "v_med3_f32 v73, v73, 0, s63\n\t"
        "v_mfma_f32_16x16x32_bf16 v[20:23], a[152:155], v[88:91], v[20:23]\n\t"
        "ds_read_b64_tr_b16 a[198:199], v132 offset:56064\n\t"
        "v_perm_b32 v62, v73, v72, s46\n\t"
        "v_mfma_f32_16x16x32_bf16 v[24:27], a[156:159], v[88:91], v[24:27]\n\t"
        "ds_read_b64_tr_b16 a[200:201], v133 offset:55296\n\t"
        "v_fract_f32 v74, v72\n\t"
        "v_mfma_f32_16x16x32_bf16 v[28:31], a[160:163], v[88:91], v[28:31]\n\t"
        "ds_read_b64_tr_b16 a[202:203], v133 offset:56064\n\t"
        "v_fract_f32 v75, v73\n\t"
        "v_mfma_f32_16x16x32_bf16 v[32:35], a[164:167], v[88:91], v[32:35]\n\t"
        "buffer_load_dwordx4 v141, s[24:27], s36 offen offset:1024 lds\n\t"
        "v_cvt_u32_f32 v76, v72\n\t"
        "v_mfma_f32_16x16x32_bf16 v[12:15], a[168:171], v[92:95], v[12:15]\n\t"
        "ds_read_b128 v[104:107], v129 offset:128\n\t"
        "v_cvt_u32_f32 v77, v73\n\t"
        "v_mfma_f32_16x16x32_bf16 v[16:19], a[172:175], v[92:95], v[16:19]\n\t"
        "ds_read_b128 v[108:111], v129 offset:4224\n\t"
        "v_cmp_neq_f32 s[64:65], 0, v74\n\t"
        "v_mfma_f32_16x16x32_bf16 v[20:23], a[176:179], v[92:95], v[20:23]\n\t"
        "v_cmp_neq_f32 s[44:45], 0, v75\n\t"
        "v_sub_f32 v78, 1.0, v74\n\t"
        "v_mfma_f32_16x16x32_bf16 v[24:27], a[144:147], v[92:95], v[24:27]\n\t"
        "buffer_load_dwordx4 v142, s[24:27], s36 offen offset:2048 lds\n\t"
        "s_add_u32 m0, s37, 36864\n\t"
        "v_mfma_f32_16x16x32_bf16 v[28:31], a[148:151], v[92:95], v[28:31]\n\t"
        "s_add_u32 s36, s36, s39\n\t"
        "v_sub_f32 v79, 1.0, v75\n\t"
        "v_mfma_f32_16x16x32_bf16 v[32:35], a[152:155], v[92:95], v[32:35]\n\t"
        "s_waitcnt vmcnt(10)\n\t"
        "s_barrier\n\t"
        "v_mfma_f32_16x16x32_bf16 v[36:39], a[156:159], v[92:95], v[36:39]\n\t"
        "ds_read_b64_tr_b16 a[204:205], v131 offset:0\n\t"
        "v_cvt_pk_bf16_f32 v79, v78, v79\n\t"
        "v_mfma_f32_16x16x32_bf16 v[40:43], a[160:163], v[92:95], v[40:43]\n\t"
        "buffer_load_dwordx4 v140, s[24:27], s36 offen offset:0 lds\n\t"
        "v_lshlrev_b32 v78, 16, v79\n\t"
        "v_mfma_f32_16x16x32_bf16 v[44:47], a[164:167], v[92:95], v[44:47]\n\t"
        "ds_read_b64_tr_b16 a[206:207], v131 offset:768\n\t"
        "v_and_b32 v79, 0xffff0000, v79\n\t"
        "s_waitcnt lgkmcnt(4)\n\t"
        "v_mfma_f32_16x16x32_bf16 v[24:27], a[168:171], v[96:99], v[24:27]\n\t"
        "ds_read_b64_tr_b16 a[208:209], v132 offset:0\n\t"
        "v_mul_f32 v80, v78, v79\n\t"
        "v_mfma_f32_16x16x32_bf16 v[28:31], a[172:175], v[96:99], v[28:31]\n\t"
        "ds_read_b64_tr_b16 a[210:211], v132 offset:768\n\t"
        "v_mul_f32 v78, v78, v75\n\t"
        "v_mfma_f32_16x16x32_bf16 v[32:35], a[176:179], v[96:99], v[32:35]\n\t"
        "buffer_load_dwordx4 v141, s[24:27], s36 offen offset:1024 lds\n\t"
        "v_mul_f32 v79, v74, v79\n\t"
        "v_mfma_f32_16x16x32_bf16 v[36:39], a[144:147], v[96:99], v[36:39]\n\t"
        "ds_read_b64_tr_b16 a[212:213], v133 offset:0\n\t"
        "ds_read_b64_tr_b16 a[214:215], v133 offset:768\n\t"
        "v_mul_f32 v81, v74, v75\n\t"
        "v_mfma_f32_16x16x32_bf16 v[40:43], a[148:151], v[96:99], v[40:43]\n\t"
        "ds_read_b128 v[112:115], v129 offset:8320\n\t"
        "v_cndmask_b32 v78, v79, v78, s[44:45]\n\t"
        "v_mfma_f32_16x16x32_bf16 v[44:47], a[152:155], v[96:99], v[44:47]\n\t"
        "ds_read_b64_tr_b16 a[216:217], v131 offset:6144\n\t"
        "v_cndmask_b32 v79, 0, v79, s[44:45]\n\t"
        "v_mfma_f32_16x16x32_bf16 v[36:39], a[168:171], v[100:103], v[36:39]\n\t"
        "buffer_load_dwordx4 v142, s[24:27], s36 offen offset:2048 lds\n\t"
        "s_add_u32 m0, s37, 49152\n\t"
        "v_mfma_f32_16x16x32_bf16 v[40:43], a[172:175], v[100:103], v[40:43]\n\t"
        "s_waitcnt vmcnt(10)\n\t"
        "s_barrier\n\t"
        "v_mfma_f32_16x16x32_bf16 v[44:47], a[176:179], v[100:103], v[44:47]\n\t"
        "ds_read_b128 v[84:87], v129 offset:12416\n\t"
        "s_add_u32 s36, s36, s38\n\t"
        "s_waitcnt lgkmcnt(7)\n\t"
        "v_mfma_f32_16x16x32_bf16 v[0:3], a[192:195], v[104:107], v[0:3]\n\t"
        "ds_read_b64_tr_b16 a[218:219], v131 offset:6912\n\t"
        "v_cvt_pk_bf16_f32 v70, v80, v78\n\t"
        "v_mfma_f32_16x16x32_bf16 v[4:7], a[196:199], v[104:107], v[4:7]\n\t"
        "buffer_load_dwordx4 v140, s[24:27], s36 offen offset:0 lds\n\t"
        "v_cvt_pk_bf16_f32 v71, v79, v81\n\t"
        "v_mfma_f32_16x16x32_bf16 v[8:11], a[200:203], v[104:107], v[8:11]\n\t"
        "ds_read_b64_tr_b16 a[220:221], v132 offset:6144\n\t"
        "v_pk_max_u16 v78, v62, v64\n\t"
        "v_mfma_f32_16x16x32_bf16 v[0:3], a[180:183], v[108:111], v[0:3]\n\t"
        "ds_read_b64_tr_b16 a[222:223], v132 offset:6912\n\t"
        "v_pk_min_u16 v78, v78, v65\n\t"
        "v_mfma_f32_16x16x32_bf16 v[4:7], a[184:187], v[108:111], v[4:7]\n\t"
        "ds_read_b64_tr_b16 a[224:225], v133 offset:6144\n\t"
        "v_cmp_ne_u32 vcc, v78, v62\n\t"
        "v_mfma_f32_16x16x32_bf16 v[8:11], a[188:191], v[108:111], v[8:11]\n\t"
        "ds_read_b64_tr_b16 a[226:227], v133 offset:6912\n\t"
        "v_sub_u32 v78, v77, v67\n\t"
        "v_mfma_f32_16x16x32_bf16 v[12:15], a[192:195], v[108:111], v[12:15]\n\t"
        "ds_read_b128 v[88:91], v129 offset:16512\n\t"
        "v_mad_u32_u24 v79, v77, 6, v66\n\t"
        "v_mfma_f32_16x16x32_bf16 v[16:19], a[196:199], v[108:111], v[16:19]\n\t"
        "ds_read_b64_tr_b16 a[228:229], v131 offset:12288\n\t"
        "v_mad_u32_u24 v79, v76, s62, v79\n\t"
        "v_mfma_f32_16x16x32_bf16 v[20:23], a[200:203], v[108:111], v[20:23]\n\t"
        "ds_read_b64_tr_b16 a[230:231], v131 offset:13056\n\t"
        "s_and_b64 vcc, vcc, s[66:67]\n\t"
        "s_waitcnt lgkmcnt(9)\n\t"
        "v_mfma_f32_16x16x32_bf16 v[0:3], a[204:207], v[112:115], v[0:3]\n\t"
        "ds_read_b64_tr_b16 a[232:233], v132 offset:12288\n\t"
        "s_or_b64 s[56:57], s[56:57], vcc\n\t"
        "v_mfma_f32_16x16x32_bf16 v[4:7], a[208:211], v[112:115], v[4:7]\n\t"
        "ds_read_b64_tr_b16 a[234:235], v132 offset:13056\n\t"
        "v_bfe_u32 v80, v78, 2, 1\n\t"
        "v_mfma_f32_16x16x32_bf16 v[8:11], a[212:215], v[112:115], v[8:11]\n\t"
        "ds_read_b64_tr_b16 a[236:237], v133 offset:12288\n\t"
        "v_or_b32 v80, v80, v79\n\t"
        "v_mfma_f32_16x16x32_bf16 v[12:15], a[180:183], v[112:115], v[12:15]\n\t"
        "ds_read_b128 v[92:95], v129 offset:20608\n\t"
        "v_add_u32 v78, 1, v78\n\t"
        "v_mfma_f32_16x16x32_bf16 v[16:19], a[184:187], v[112:115], v[16:19]\n\t"
        "ds_read_b64_tr_b16 a[238:239], v133 offset:13056\n\t"
        "v_bfe_u32 v78, v78, 2, 1\n\t"
        "v_mfma_f32_16x16x32_bf16 v[20:23], a[188:191], v[112:115], v[20:23]\n\t"
        "ds_read_b64_tr_b16 a[240:241], v131 offset:18432\n\t"
        "v_add3_u32 v78, v79, v78, 6\n\t"
        "v_mfma_f32_16x16x32_bf16 v[24:27], a[192:195], v[112:115], v[24:27]\n\t"
        "ds_read_b128 v[96:99], v130 offset:0\n\t"
        "v_cndmask_b32 v78, v80, v78, s[44:45]\n\t"
        "v_mfma_f32_16x16x32_bf16 v[28:31], a[196:199], v[112:115], v[28:31]\n\t"
        "ds_read_b64_tr_b16 a[242:243], v131 offset:19200\n\t"
        "v_cndmask_b32 v79, 0, v119, s[64:65]\n\t"
        "v_mfma_f32_16x16x32_bf16 v[32:35], a[200:203], v[112:115], v[32:35]\n\t"
        "ds_read_b64_tr_b16 a[244:245], v132 offset:18432\n\t"
        "v_add_u32 v81, v80, v79\n\t"
        "s_waitcnt lgkmcnt(10)\n\t"
        "v_mfma_f32_16x16x32_bf16 v[12:15], a[204:207], v[84:87], v[12:15]\n\t"
        "ds_read_b128 v[100:103], v130 offset:4096\n\t"
        "v_add_u32 v82, v78, v79\n\t"
        "v_mfma_f32_16x16x32_bf16 v[16:19], a[208:211], v[84:87], v[16:19]\n\t"
        "ds_read_b64_tr_b16 a[246:247], v132 offset:19200\n\t"
        "v_cndmask_b32 v78, v81, v78, s[44:45]\n\t"
        "v_mfma_f32_16x16x32_bf16 v[20:23], a[212:215], v[84:87], v[20:23]\n\t"
        "buffer_load_dwordx4 v141, s[24:27], s36 offen offset:1024 lds\n\t"
        "v_lshl_or_b32 v68, v78, 16, v80\n\t"
        "v_mfma_f32_16x16x32_bf16 v[24:27], a[180:183], v[84:87], v[24:27]\n\t"
        "ds_read_b64_tr_b16 a[248:249], v133 offset:18432\n\t"
        "v_lshl_or_b32 v69, v82, 16, v81\n\t"
        "v_mfma_f32_16x16x32_bf16 v[28:31], a[184:187], v[84:87], v[28:31]\n\t"
        "ds_write_b128 v118, v[68:71]\n\t"
        "v_mfma_f32_16x16x32_bf16 v[32:35], a[188:191], v[84:87], v[32:35]\n\t"
        "s_cmp_gt_u32 s3, 32\n\t"
        "s_cselect_b32 s8, 2, 0\n\t"
        "v_mfma_f32_16x16x32_bf16 v[36:39], a[192:195], v[84:87], v[36:39]\n\t"
        "s_cmp_lg_u64 s[56:57], 0\n\t"
        "s_addc_u32 s8, s8, s6\n\t"
        "v_mfma_f32_16x16x32_bf16 v[40:43], a[196:199], v[84:87], v[40:43]\n\t"
        "ds_read_b128 v[160:163], v185 offset:0\n\t"
        "v_mov_b32 v151, s8\n\t"
        "v_mfma_f32_16x16x32_bf16 v[44:47], a[200:203], v[84:87], v[44:47]\n\t"
        "s_mov_b64 exec, 1\n\t"
        "ds_write_b32 v153, v151\n\t"
        "s_mov_b64 exec, -1\n\t"
        "v_mfma_f32_16x16x32_bf16 v[24:27], a[204:207], v[88:91], v[24:27]\n\t"
        "buffer_load_dwordx4 v142, s[24:27], s36 offen offset:2048 lds\n\t"
        "v_xor_b32 v154, 32, v186\n\t"
        "v_mfma_f32_16x16x32_bf16 v[28:31], a[208:211], v[88:91], v[28:31]\n\t"
        "ds_read_b64_tr_b16 a[250:251], v133 offset:19200\n\t"
        "v_mfma_f32_16x16x32_bf16 v[32:35], a[212:215], v[88:91], v[32:35]\n\t"
        "ds_read_b128 v[104:107], v130 offset:8192\n\t"
        "v_mfma_f32_16x16x32_bf16 v[36:39], a[180:183], v[88:91], v[36:39]\n\t"
        "v_mfma_f32_16x16x32_bf16 v[40:43], a[184:187], v[88:91], v[40:43]\n\t"
        "v_mfma_f32_16x16x32_bf16 v[44:47], a[188:191], v[88:91], v[44:47]\n\t"
        "s_waitcnt lgkmcnt(5)\n\t"
        "v_mfma_f32_16x16x32_bf16 v[36:39], a[204:207], v[92:95], v[36:39]\n\t"
        "ds_read_b128 v[108:111], v130 offset:12288\n\t"
        "v_mfma_f32_16x16x32_bf16 v[40:43], a[208:211], v[92:95], v[40:43]\n\t"
        "s_waitcnt lgkmcnt(4)\n\t"
        "v_lshlrev_b32_sdwa v157, 5, v160 dst_sel:DWORD dst_unused:UNUSED_PAD src0_sel:DWORD src1_sel:WORD_0\n\t"
        "v_lshlrev_b32_sdwa v158, 5, v160 dst_sel:DWORD dst_unused:UNUSED_PAD src0_sel:DWORD src1_sel:WORD_1\n\t"
        "v_mfma_f32_16x16x32_bf16 v[44:47], a[212:215], v[92:95], v[44:47]\n\t"
        "v_or_b32 v157, v184, v157\n\t"
        "v_or_b32 v158, v184, v158\n\t"
        "v_mfma_f32_16x16x32_bf16 v[0:3], a[228:231], v[96:99], v[0:3]\n\t"
        "v_xor_b32 v159, 32, v157\n\t"
        "v_xor_b32 v152, 32, v158\n\t"
        "v_mfma_f32_16x16x32_bf16 v[4:7], a[232:235], v[96:99], v[4:7]\n\t"
        "v_perm_b32 v164, v162, v162, v168\n\t"
        "v_perm_b32 v165, v162, v162, v169\n\t"
        "v_mfma_f32_16x16x32_bf16 v[8:11], a[236:239], v[96:99], v[8:11]\n\t"
        "s_waitcnt vmcnt(3) lgkmcnt(2)\n\t"
        "s_barrier\n\t"
        "v_mfma_f32_16x16x32_bf16 v[0:3], a[216:219], v[100:103], v[0:3]\n\t"
        "ds_read_b64_tr_b16 v[60:61], v157 offset:0\n\t"
        "v_perm_b32 v166, v162, v162, v170\n\t"
        "v_mfma_f32_16x16x32_bf16 v[4:7], a[220:223], v[100:103], v[4:7]\n\t"
        "ds_read_b64_tr_b16 v[62:63], v158 offset:0\n\t"
        "v_perm_b32 v167, v162, v162, v171\n\t"
        "v_mfma_f32_16x16x32_bf16 v[8:11], a[224:227], v[100:103], v[8:11]\n\t"
        "ds_read_b64_tr_b16 v[64:65], v159 offset:0\n\t"
        "v_cmp_ne_u32 vcc, 0, v163\n\t"
        "v_mfma_f32_16x16x32_bf16 v[12:15], a[228:231], v[100:103], v[12:15]\n\t"
        "ds_read_b64_tr_b16 v[66:67], v152 offset:0\n\t"
        "ds_read_b128 v[112:115], v130 offset:16384\n\t"
        "v_mfma_f32_16x16x32_bf16 v[16:19], a[232:235], v[100:103], v[16:19]\n\t"
        "ds_read_b64_tr_b16 v[68:69], v157 offset:64\n\t"
        "ds_read_b64_tr_b16 a[252:253], v131 offset:24576\n\t"
        "v_mfma_f32_16x16x32_bf16 v[20:23], a[236:239], v[100:103], v[20:23]\n\t"
        "ds_read_b64_tr_b16 v[70:71], v158 offset:64\n\t"
        "ds_read_b64_tr_b16 a[254:255], v131 offset:25344\n\t"
        "s_waitcnt lgkmcnt(9)\n\t"
        "v_mfma_f32_16x16x32_bf16 v[0:3], a[240:243], v[104:107], v[0:3]\n\t"
        "ds_read_b64_tr_b16 v[72:73], v159 offset:64\n\t"
        "ds_read_b64_tr_b16 v[188:189], v132 offset:24576\n\t"
        "v_mfma_f32_16x16x32_bf16 v[4:7], a[244:247], v[104:107], v[4:7]\n\t"
        "ds_read_b64_tr_b16 v[74:75], v152 offset:64\n\t"
        "ds_read_b64_tr_b16 v[190:191], v132 offset:25344\n\t"
        "v_mfma_f32_16x16x32_bf16 v[8:11], a[248:251], v[104:107], v[8:11]\n\t"
        "ds_read_b64_tr_b16 v[76:77], v157 offset:128\n\t"
        "ds_read_b64_tr_b16 v[192:193], v133 offset:24576\n\t"
        "v_mfma_f32_16x16x32_bf16 v[12:15], a[216:219], v[104:107], v[12:15]\n\t"
        "ds_read_b64_tr_b16 v[78:79], v158 offset:128\n\t"
        "ds_read_b128 v[84:87], v130 offset:20480\n\t"
        "v_mfma_f32_16x16x32_bf16 v[16:19], a[220:223], v[104:107], v[16:19]\n\t"
        "ds_read_b64_tr_b16 v[80:81], v159 offset:128\n\t"
        "ds_read_b64_tr_b16 v[194:195], v133 offset:25344\n\t"
        "s_waitcnt lgkmcnt(15)\n\t"
        "v_mfma_f32_16x16x32_bf16 v[48:51], v[60:63], v[164:167], 0\n\t"
        "ds_read_b64_tr_b16 v[82:83], v152 offset:128\n\t"
        "v_mfma_f32_16x16x32_bf16 v[20:23], a[224:227], v[104:107], v[20:23]\n\t"
        "ds_read_b64_tr_b16 v[196:197], v131 offset:30720\n\t"
        "v_mfma_f32_16x16x32_bf16 v[52:55], v[64:67], v[164:167], 0\n\t"
        "ds_read_b64_tr_b16 v[198:199], v131 offset:31488\n\t"
        "v_mfma_f32_16x16x32_bf16 v[24:27], a[228:231], v[104:107], v[24:27]\n\t"
        "ds_read_b64_tr_b16 v[200:201], v132 offset:30720\n\t"
        "s_waitcnt lgkmcnt(14)\n\t"
        "v_mfma_f32_16x16x32_bf16 v[56:59], v[68:71], v[164:167], 0\n\t"
        "ds_read_b64_tr_b16 v[202:203], v132 offset:31488\n\t"
        "v_mfma_f32_16x16x32_bf16 v[28:31], a[232:235], v[104:107], v[28:31]\n\t"
        "ds_read_b64_tr_b16 v[204:205], v133 offset:30720\n\t"
        "s_waitcnt lgkmcnt(12)\n\t"
        "v_mfma_f32_16x16x32_bf16 v[116:119], v[72:75], v[164:167], 0\n\t"
        "ds_read_b64_tr_b16 v[206:207], v133 offset:31488\n\t"
        "v_mfma_f32_16x16x32_bf16 v[32:35], a[236:239], v[104:107], v[32:35]\n\t"
        "ds_read_b128 v[88:91], v130 offset:64\n\t"
        "s_waitcnt lgkmcnt(11)\n\t"
        "v_mfma_f32_16x16x32_bf16 v[120:123], v[76:79], v[164:167], 0\n\t"
        "ds_read_b64_tr_b16 v[208:209], v131 offset:36864\n\t"
        "v_mfma_f32_16x16x32_bf16 v[12:15], a[240:243], v[108:111], v[12:15]\n\t"
        "ds_read_b128 v[92:95], v130 offset:4160\n\t"
        ".LpM1%=:\n\t"
        "s_cbranch_execz .LrM%=\n\t"
        "s_waitcnt lgkmcnt(9)\n\t"
        "v_mfma_f32_16x16x32_bf16 v[124:127], v[80:83], v[164:167], 0\n\t"
        "ds_read_b64_tr_b16 v[210:211], v131 offset:37632\n\t"
        "s_cbranch_vccz .Lx46%=\n\t"
        "v_lshlrev_b32_sdwa v157, 5, v161 dst_sel:DWORD dst_unused:UNUSED_PAD src0_sel:DWORD src1_sel:WORD_0\n\t"
        "v_lshlrev_b32_sdwa v158, 5, v161 dst_sel:DWORD dst_unused:UNUSED_PAD src0_sel:DWORD src1_sel:WORD_1\n\t"
        "v_or_b32 v157, v184, v157\n\t"
        "v_or_b32 v158, v184, v158\n\t"
        "v_xor_b32 v159, 32, v157\n\t"
        "v_xor_b32 v152, 32, v158\n\t"
        "ds_read_b64_tr_b16 v[60:61], v157 offset:0\n\t"
        "ds_read_b64_tr_b16 v[62:63], v158 offset:0\n\t"
        "ds_read_b64_tr_b16 v[64:65], v159 offset:0\n\t"
        "ds_read_b64_tr_b16 v[66:67], v152 offset:0\n\t"
        "ds_read_b64_tr_b16 v[68:69], v157 offset:64\n\t"
        "ds_read_b64_tr_b16 v[70:71], v158 offset:64\n\t"
        "ds_read_b64_tr_b16 v[72:73], v159 offset:64\n\t"
        "ds_read_b64_tr_b16 v[74:75], v152 offset:64\n\t"
        "ds_read_b64_tr_b16 v[76:77], v157 offset:128\n\t"
        "ds_read_b64_tr_b16 v[78:79], v158 offset:128\n\t"
        "ds_read_b64_tr_b16 v[80:81], v159 offset:128\n\t"
        "ds_read_b64_tr_b16 v[82:83], v152 offset:128\n\t"
        "v_perm_b32 v164, v163, v163, v168\n\t"
        "v_perm_b32 v165, v163, v163, v169\n\t"
        "v_perm_b32 v166, v163, v163, v170\n\t"
        "v_perm_b32 v167, v163, v163, v171\n\t"
        "s_waitcnt lgkmcnt(10)\n\t"
        "s_nop 0\n\t"
        "v_mfma_f32_16x16x32_bf16 v[48:51], v[60:63], v[164:167], v[48:51]\n\t"
        "s_waitcnt lgkmcnt(8)\n\t"
        "v_mfma_f32_16x16x32_bf16 v[52:55], v[64:67], v[164:167], v[52:55]\n\t"
        "s_waitcnt lgkmcnt(6)\n\t"
        "v_mfma_f32_16x16x32_bf16 v[56:59], v[68:71], v[164:167], v[56:59]\n\t"
        "s_waitcnt lgkmcnt(4)\n\t"
        "v_mfma_f32_16x16x32_bf16 v[116:119], v[72:75], v[164:167], v[116:119]\n\t"
        "s_waitcnt lgkmcnt(2)\n\t"
        "v_mfma_f32_16x16x32_bf16 v[120:123], v[76:79], v[164:167], v[120:123]\n\t"
        "s_waitcnt lgkmcnt(0)\n\t"
        "v_mfma_f32_16x16x32_bf16 v[124:127], v[80:83], v[164:167], v[124:127]\n\t"
        "s_nop 4\n\t"
        ".Lx46%=:\n\t"
        "v_mfma_f32_16x16x32_bf16 v[16:19], a[244:247], v[108:111], v[16:19]\n\t"
        "ds_read_b128 v[160:163], v185 offset:256\n\t"
        "v_cvt_pk_bf16_f32 v48, v48, v49\n\t"
        "v_mfma_f32_16x16x32_bf16 v[20:23], a[248:251], v[108:111], v[20:23]\n\t"
        "ds_read_b64_tr_b16 v[212:213], v132 offset:36864\n\t"
        "v_cvt_pk_bf16_f32 v49, v50, v51\n\t"
        "v_mfma_f32_16x16x32_bf16 v[24:27], a[216:219], v[108:111], v[24:27]\n\t"
        "ds_write_b64 v186, v[48:49] offset:0\n\t"
        "v_mfma_f32_16x16x32_bf16 v[28:31], a[220:223], v[108:111], v[28:31]\n\t"
        "s_waitcnt vmcnt(0) lgkmcnt(7)\n\t"
        "s_barrier\n\t"
        "v_mfma_f32_16x16x32_bf16 v[32:35], a[224:227], v[108:111], v[32:35]\n\t"
        "ds_read_b64_tr_b16 v[214:215], v132 offset:37632\n\t"
        "v_cvt_pk_bf16_f32 v52, v52, v53\n\t"
        "v_mfma_f32_16x16x32_bf16 v[36:39], a[228:231], v[108:111], v[36:39]\n\t"
        "ds_read_b64_tr_b16 v[216:217], v133 offset:36864\n\t"
        "v_cvt_pk_bf16_f32 v53, v54, v55\n\t"
        "v_mfma_f32_16x16x32_bf16 v[40:43], a[232:235], v[108:111], v[40:43]\n\t"
        "ds_write_b64 v154, v[52:53] offset:0\n\t"
        "v_mfma_f32_16x16x32_bf16 v[44:47], a[236:239], v[108:111], v[44:47]\n\t"
        "ds_read_b64_tr_b16 v[218:219], v133 offset:37632\n\t"
        "v_cvt_pk_bf16_f32 v56, v56, v57\n\t"
        "v_mfma_f32_16x16x32_bf16 v[24:27], a[240:243], v[112:115], v[24:27]\n\t"
        "ds_read_b128 v[96:99], v130 offset:8256\n\t"
        "v_cvt_pk_bf16_f32 v57, v58, v59\n\t"
        "v_mfma_f32_16x16x32_bf16 v[28:31], a[244:247], v[112:115], v[28:31]\n\t"
        "ds_write_b64 v186, v[56:57] offset:64\n\t"
        "v_mfma_f32_16x16x32_bf16 v[32:35], a[248:251], v[112:115], v[32:35]\n\t"
        "ds_read_b64_tr_b16 v[220:221], v131 offset:43008\n\t"
        "v_cvt_pk_bf16_f32 v116, v116, v117\n\t"
        "v_mfma_f32_16x16x32_bf16 v[36:39], a[216:219], v[112:115], v[36:39]\n\t"
        "ds_read_b64_tr_b16 v[222:223], v131 offset:43776\n\t"
        "v_cvt_pk_bf16_f32 v117, v118, v119\n\t"
        "v_mfma_f32_16x16x32_bf16 v[40:43], a[220:223], v[112:115], v[40:43]\n\t"
        "ds_write_b64 v154, v[116:117] offset:64\n\t"
        "v_mfma_f32_16x16x32_bf16 v[44:47], a[224:227], v[112:115], v[44:47]\n\t"
        "ds_read_b64_tr_b16 v[224:225], v132 offset:43008\n\t"
        "v_cvt_pk_bf16_f32 v120, v120, v121\n\t"
        "v_mfma_f32_16x16x32_bf16 v[36:39], a[240:243], v[84:87], v[36:39]\n\t"
        "ds_read_b128 v[100:103], v130 offset:12352\n\t"
        "v_cvt_pk_bf16_f32 v121, v122, v123\n\t"
        "v_mfma_f32_16x16x32_bf16 v[40:43], a[244:247], v[84:87], v[40:43]\n\t"
        "ds_write_b64 v186, v[120:121] offset:128\n\t"
        "v_mfma_f32_16x16x32_bf16 v[44:47], a[248:251], v[84:87], v[44:47]\n\t"
        "ds_read_b64_tr_b16 v[226:227], v132 offset:43776\n\t"
        "v_cvt_pk_bf16_f32 v124, v124, v125\n\t"
        "s_waitcnt lgkmcnt(9)\n\t"
        "v_mfma_f32_16x16x32_bf16 v[0:3], v[196:199], v[88:91], v[0:3]\n\t"
        "ds_read_b64_tr_b16 v[228:229], v133 offset:43008\n\t"
        "v_cvt_pk_bf16_f32 v125, v126, v127\n\t"
        "v_mfma_f32_16x16x32_bf16 v[4:7], v[200:203], v[88:91], v[4:7]\n\t"
        "ds_write_b64 v154, v[124:125] offset:128\n\t"
        "v_mfma_f32_16x16x32_bf16 v[8:11], v[204:207], v[88:91], v[8:11]\n\t"
        "ds_read_b64_tr_b16 v[230:231], v133 offset:43776\n\t"
        "v_lshlrev_b32_sdwa v157, 5, v160 dst_sel:DWORD dst_unused:UNUSED_PAD src0_sel:DWORD src1_sel:WORD_0\n\t"
        "v_mfma_f32_16x16x32_bf16 v[0:3], a[252:255], v[92:95], v[0:3]\n\t"
        "ds_read_b64_tr_b16 v[232:233], v131 offset:49152\n\t"
        "v_lshlrev_b32_sdwa v158, 5, v160 dst_sel:DWORD dst_unused:UNUSED_PAD src0_sel:DWORD src1_sel:WORD_1\n\t"
        "v_mfma_f32_16x16x32_bf16 v[4:7], v[188:191], v[92:95], v[4:7]\n\t"
        "ds_read_b64_tr_b16 v[234:235], v131 offset:49920\n\t"
        "v_or_b32 v157, v184, v157\n\t"
        "v_mfma_f32_16x16x32_bf16 v[8:11], v[192:195], v[92:95], v[8:11]\n\t"
        "ds_read_b64_tr_b16 v[60:61], v157 offset:0\n\t"
        "v_or_b32 v158, v184, v158\n\t"
        "v_mfma_f32_16x16x32_bf16 v[12:15], v[196:199], v[92:95], v[12:15]\n\t"
        "ds_read_b64_tr_b16 v[62:63], v158 offset:0\n\t"
        "v_xor_b32 v159, 32, v157\n\t"
        "v_mfma_f32_16x16x32_bf16 v[16:19], v[200:203], v[92:95], v[16:19]\n\t"
        "ds_read_b64_tr_b16 v[64:65], v159 offset:0\n\t"
        "v_xor_b32 v152, 32, v158\n\t"
        "v_mfma_f32_16x16x32_bf16 v[20:23], v[204:207], v[92:95], v[20:23]\n\t"
        "ds_read_b64_tr_b16 v[66:67], v152 offset:0\n\t"
        "v_perm_b32 v164, v162, v162, v168\n\t"
        "s_waitcnt lgkmcnt(9)\n\t"
        "v_mfma_f32_16x16x32_bf16 v[0:3], v[208:211], v[96:99], v[0:3]\n\t"
        "ds_read_b64_tr_b16 v[68:69], v157 offset:64\n\t"
        "v_perm_b32 v165, v162, v162, v169\n\t"
        "v_mfma_f32_16x16x32_bf16 v[4:7], v[212:215], v[96:99], v[4:7]\n\t"
        "ds_read_b64_tr_b16 v[70:71], v158 offset:64\n\t"
        "v_perm_b32 v166, v162, v162, v170\n\t"
        "v_mfma_f32_16x16x32_bf16 v[8:11], v[216:219], v[96:99], v[8:11]\n\t"
        "ds_read_b128 v[104:107], v130 offset:16448\n\t"
        "v_perm_b32 v167, v162, v162, v171\n\t"
        "v_mfma_f32_16x16x32_bf16 v[12:15], a[252:255], v[96:99], v[12:15]\n\t"
        "ds_read_b64_tr_b16 v[72:73], v159 offset:64\n\t"
        "v_cmp_ne_u32 vcc, 0, v163\n\t"
        "v_mfma_f32_16x16x32_bf16 v[16:19], v[188:191], v[96:99], v[16:19]\n\t"
        "ds_read_b64_tr_b16 v[74:75], v152 offset:64\n\t"
        "v_mfma_f32_16x16x32_bf16 v[20:23], v[192:195], v[96:99], v[20:23]\n\t"
        "ds_read_b64_tr_b16 v[76:77], v157 offset:128\n\t"
        "v_mfma_f32_16x16x32_bf16 v[24:27], v[196:199], v[96:99], v[24:27]\n\t"
        "ds_read_b64_tr_b16 v[78:79], v158 offset:128\n\t"
        "s_waitcnt lgkmcnt(9)\n\t"
        "v_mfma_f32_16x16x32_bf16 v[48:51], v[60:63], v[164:167], 0\n\t"
        "ds_read_b64_tr_b16 v[80:81], v159 offset:128\n\t"
        "v_mfma_f32_16x16x32_bf16 v[28:31], v[200:203], v[96:99], v[28:31]\n\t"
        "ds_read_b64_tr_b16 v[82:83], v152 offset:128\n\t"
        "s_waitcnt lgkmcnt(9)\n\t"
        "v_mfma_f32_16x16x32_bf16 v[52:55], v[64:67], v[164:167], 0\n\t"
        "ds_read_b128 v[108:111], v130 offset:20544\n\t"
        "v_mfma_f32_16x16x32_bf16 v[32:35], v[204:207], v[96:99], v[32:35]\n\t"
        "ds_read_b64_tr_b16 v[236:237], v132 offset:49152\n\t"
        "s_waitcnt lgkmcnt(9)\n\t"
        "v_mfma_f32_16x16x32_bf16 v[56:59], v[68:71], v[164:167], 0\n\t"
        "ds_read_b64_tr_b16 v[238:239], v132 offset:49920\n\t"
        "v_mfma_f32_16x16x32_bf16 v[12:15], v[208:211], v[100:103], v[12:15]\n\t"
        "ds_read_b64_tr_b16 v[240:241], v133 offset:49152\n\t"
        "ds_read_b64_tr_b16 v[242:243], v133 offset:49920\n\t"
        "v_mfma_f32_16x16x32_bf16 v[16:19], v[212:215], v[100:103], v[16:19]\n\t"
        "ds_read_b128 v[112:115], v130 offset:128\n\t"
        "s_waitcnt lgkmcnt(10)\n\t"
        "v_mfma_f32_16x16x32_bf16 v[116:119], v[72:75], v[164:167], 0\n\t"
        "ds_read_b128 v[84:87], v130 offset:4224\n\t"
        "v_mfma_f32_16x16x32_bf16 v[20:23], v[216:219], v[100:103], v[20:23]\n\t"
        "ds_read_b64_tr_b16 v[244:245], v131 offset:55296\n\t"
        "s_waitcnt lgkmcnt(10)\n\t"
        "v_mfma_f32_16x16x32_bf16 v[120:123], v[76:79], v[164:167], 0\n\t"
        "ds_read_b64_tr_b16 v[246:247], v131 offset:56064\n\t"
        "v_mfma_f32_16x16x32_bf16 v[24:27], a[252:255], v[100:103], v[24:27]\n\t"
        "ds_read_b64_tr_b16 v[248:249], v132 offset:55296\n\t"
        "s_waitcnt lgkmcnt(10)\n\t"
        "v_mfma_f32_16x16x32_bf16 v[124:127], v[80:83], v[164:167], 0\n\t"
        "ds_read_b64_tr_b16 v[250:251], v132 offset:56064\n\t"
        "s_cbranch_vccz .Lx48%=\n\t"
        "v_lshlrev_b32_sdwa v157, 5, v161 dst_sel:DWORD dst_unused:UNUSED_PAD src0_sel:DWORD src1_sel:WORD_0\n\t"
        "v_lshlrev_b32_sdwa v158, 5, v161 dst_sel:DWORD dst_unused:UNUSED_PAD src0_sel:DWORD src1_sel:WORD_1\n\t"
        "v_or_b32 v157, v184, v157\n\t"
        "v_or_b32 v158, v184, v158\n\t"
        "v_xor_b32 v159, 32, v157\n\t"
        "v_xor_b32 v152, 32, v158\n\t"
        "ds_read_b64_tr_b16 v[60:61], v157 offset:0\n\t"
        "ds_read_b64_tr_b16 v[62:63], v158 offset:0\n\t"
        "ds_read_b64_tr_b16 v[64:65], v159 offset:0\n\t"
        "ds_read_b64_tr_b16 v[66:67], v152 offset:0\n\t"
        "ds_read_b64_tr_b16 v[68:69], v157 offset:64\n\t"
        "ds_read_b64_tr_b16 v[70:71], v158 offset:64\n\t"
        "ds_read_b64_tr_b16 v[72:73], v159 offset:64\n\t"
        "ds_read_b64_tr_b16 v[74:75], v152 offset:64\n\t"
        "ds_read_b64_tr_b16 v[76:77], v157 offset:128\n\t"
        "ds_read_b64_tr_b16 v[78:79], v158 offset:128\n\t"
        "ds_read_b64_tr_b16 v[80:81], v159 offset:128\n\t"
        "ds_read_b64_tr_b16 v[82:83], v152 offset:128\n\t"
        "v_perm_b32 v164, v163, v163, v168\n\t"
        "v_perm_b32 v165, v163, v163, v169\n\t"
        "v_perm_b32 v166, v163, v163, v170\n\t"
        "v_perm_b32 v167, v163, v163, v171\n\t"
        "s_waitcnt lgkmcnt(10)\n\t"
        "s_nop 0\n\t"
        "v_mfma_f32_16x16x32_bf16 v[48:51], v[60:63], v[164:167], v[48:51]\n\t"
        "s_waitcnt lgkmcnt(8)\n\t"
        "v_mfma_f32_16x16x32_bf16 v[52:55], v[64:67], v[164:167], v[52:55]\n\t"
        "s_waitcnt lgkmcnt(6)\n\t"
        "v_mfma_f32_16x16x32_bf16 v[56:59], v[68:71], v[164:167], v[56:59]\n\t"
        "s_waitcnt lgkmcnt(4)\n\t"
        "v_mfma_f32_16x16x32_bf16 v[116:119], v[72:75], v[164:167], v[116:119]\n\t"
        "s_waitcnt lgkmcnt(2)\n\t"
        "v_mfma_f32_16x16x32_bf16 v[120:123], v[76:79], v[164:167], v[120:123]\n\t"
        "s_waitcnt lgkmcnt(0)\n\t"
        "v_mfma_f32_16x16x32_bf16 v[124:127], v[80:83], v[164:167], v[124:127]\n\t"
        "s_nop 4\n\t"
        ".Lx48%=:\n\t"
        "v_mfma_f32_16x16x32_bf16 v[28:31], v[188:191], v[100:103], v[28:31]\n\t"
        "ds_read_b64_tr_b16 v[252:253], v133 offset:55296\n\t"
        "v_cvt_pk_bf16_f32 v48, v48, v49\n\t"
        "v_mfma_f32_16x16x32_bf16 v[32:35], v[192:195], v[100:103], v[32:35]\n\t"
        "ds_read_b64_tr_b16 v[254:255], v133 offset:56064\n\t"
        "v_cvt_pk_bf16_f32 v49, v50, v51\n\t"
        "v_mfma_f32_16x16x32_bf16 v[36:39], v[196:199], v[100:103], v[36:39]\n\t"
        "ds_write_b64 v186, v[48:49] offset:12288\n\t"
        "v_mfma_f32_16x16x32_bf16 v[40:43], v[200:203], v[100:103], v[40:43]\n\t"
        "ds_read_b128 v[88:91], v130 offset:8320\n\t"
        "v_cvt_pk_bf16_f32 v52, v52, v53\n\t"
        "v_mfma_f32_16x16x32_bf16 v[44:47], v[204:207], v[100:103], v[44:47]\n\t"
        "v_cvt_pk_bf16_f32 v53, v54, v55\n\t"
        "v_cvt_pk_bf16_f32 v56, v56, v57\n\t"
        "v_mfma_f32_16x16x32_bf16 v[24:27], v[208:211], v[104:107], v[24:27]\n\t"
        "ds_write_b64 v154, v[52:53] offset:12288\n\t"
        "v_mfma_f32_16x16x32_bf16 v[28:31], v[212:215], v[104:107], v[28:31]\n\t"
        "v_cvt_pk_bf16_f32 v57, v58, v59\n\t"
        "v_cvt_pk_bf16_f32 v116, v116, v117\n\t"
        "v_mfma_f32_16x16x32_bf16 v[32:35], v[216:219], v[104:107], v[32:35]\n\t"
        "ds_write_b64 v186, v[56:57] offset:12352\n\t"
        "v_mfma_f32_16x16x32_bf16 v[36:39], a[252:255], v[104:107], v[36:39]\n\t"
        "v_cvt_pk_bf16_f32 v117, v118, v119\n\t"
        "v_cvt_pk_bf16_f32 v120, v120, v121\n\t"
        "v_mfma_f32_16x16x32_bf16 v[40:43], v[188:191], v[104:107], v[40:43]\n\t"
        "ds_write_b64 v154, v[116:117] offset:12352\n\t"
        "v_mfma_f32_16x16x32_bf16 v[44:47], v[192:195], v[104:107], v[44:47]\n\t"
        "v_cvt_pk_bf16_f32 v121, v122, v123\n\t"
        "v_cvt_pk_bf16_f32 v124, v124, v125\n\t"
        "s_waitcnt lgkmcnt(5)\n\t"
        "v_mfma_f32_16x16x32_bf16 v[36:39], v[208:211], v[108:111], v[36:39]\n\t"
        "ds_write_b64 v186, v[120:121] offset:12416\n\t"
        "v_mfma_f32_16x16x32_bf16 v[40:43], v[212:215], v[108:111], v[40:43]\n\t"
        "ds_read_b128 v[92:95], v130 offset:12416\n\t"
        "v_cvt_pk_bf16_f32 v125, v126, v127\n\t"
        "v_mfma_f32_16x16x32_bf16 v[44:47], v[216:219], v[108:111], v[44:47]\n\t"
        "ds_write_b64 v154, v[124:125] offset:12416\n\t"
        "v_mfma_f32_16x16x32_bf16 v[0:3], v[232:235], v[112:115], v[0:3]\n\t"
        "v_mfma_f32_16x16x32_bf16 v[4:7], v[236:239], v[112:115], v[4:7]\n\t"
        "v_mfma_f32_16x16x32_bf16 v[8:11], v[240:243], v[112:115], v[8:11]\n\t"
        "v_mfma_f32_16x16x32_bf16 v[0:3], v[220:223], v[84:87], v[0:3]\n\t"
        "v_mfma_f32_16x16x32_bf16 v[4:7], v[224:227], v[84:87], v[4:7]\n\t"
        "v_mfma_f32_16x16x32_bf16 v[8:11], v[228:231], v[84:87], v[8:11]\n\t"
        "s_waitcnt lgkmcnt(0)\n\t"
        "s_barrier\n\t"
        "v_mfma_f32_16x16x32_bf16 v[12:15], v[232:235], v[84:87], v[12:15]\n\t"
        "ds_read_b128 v[124:127], v182 offset:0\n\t"
        "v_mfma_f32_16x16x32_bf16 v[16:19], v[236:239], v[84:87], v[16:19]\n\t"
        "ds_read_b128 v[132:135], v187 offset:0\n\t"
        "v_mfma_f32_16x16x32_bf16 v[20:23], v[240:243], v[84:87], v[20:23]\n\t"
        "ds_read_b128 v[136:139], v187 offset:16\n\t"
        "v_mfma_f32_16x16x32_bf16 v[0:3], v[244:247], v[88:91], v[0:3]\n\t"
        "ds_read_b32 v140, v187 offset:32\n\t"
        "v_mfma_f32_16x16x32_bf16 v[4:7], v[248:251], v[88:91], v[4:7]\n\t"
        "ds_read_b128 v[96:99], v130 offset:16512\n\t"
        "v_mfma_f32_16x16x32_bf16 v[8:11], v[252:255], v[88:91], v[8:11]\n\t"
        "v_mfma_f32_16x16x32_bf16 v[12:15], v[220:223], v[88:91], v[12:15]\n\t"
        "ds_read_b128 v[100:103], v130 offset:20608\n\t"
        "v_mfma_f32_16x16x32_bf16 v[16:19], v[224:227], v[88:91], v[16:19]\n\t"
        "v_mfma_f32_16x16x32_bf16 v[20:23], v[228:231], v[88:91], v[20:23]\n\t"
        "v_mfma_f32_16x16x32_bf16 v[24:27], v[232:235], v[88:91], v[24:27]\n\t"
        "s_waitcnt lgkmcnt(5)\n\t"
        "v_or3_b32 v124, v124, v125, v126\n\t"
        "v_or_b32 v124, v124, v127\n\t"
        "v_mfma_f32_16x16x32_bf16 v[28:31], v[236:239], v[88:91], v[28:31]\n\t"
        "v_readfirstlane_b32 s6, v124\n\t"
        "v_mfma_f32_16x16x32_bf16 v[32:35], v[240:243], v[88:91], v[32:35]\n\t"
        "s_nop 0\n\t"
        "s_bitcmp1_b32 s6, 1\n\t"
        "s_cbranch_scc1 .Lx50%=\n\t"
        ".Lx51%=:\n\t"
        "v_mfma_f32_16x16x32_bf16 v[12:15], v[244:247], v[92:95], v[12:15]\n\t"
        "ds_read_b128 v[160:163], v185 offset:512\n\t"
        "s_waitcnt lgkmcnt(4)\n\t"
        "v_xor_b32 v132, v181, v132\n\t"
        "v_mfma_f32_16x16x32_bf16 v[16:19], v[248:251], v[92:95], v[16:19]\n\t"
        "v_add_u32 v151, s9, v172\n\t"
        "v_max_u32 v151, v151, v132\n\t"
        "v_mfma_f32_16x16x32_bf16 v[20:23], v[252:255], v[92:95], v[20:23]\n\t"
        "ds_read_b128 v[104:107], v151 offset:0\n\t"
        "v_xor_b32 v133, v181, v133\n\t"
        "v_mfma_f32_16x16x32_bf16 v[24:27], v[220:223], v[92:95], v[24:27]\n\t"
        "ds_read_b128 v[108:111], v151 offset:64\n\t"
        "v_add_u32 v183, s9, v173\n\t"
        "v_mfma_f32_16x16x32_bf16 v[28:31], v[224:227], v[92:95], v[28:31]\n\t"
        "ds_read_b128 v[112:115], v151 offset:128\n\t"
        "v_max_u32 v183, v183, v133\n\t"
        "v_xor_b32 v134, v181, v134\n\t"
        "v_mfma_f32_16x16x32_bf16 v[32:35], v[228:231], v[92:95], v[32:35]\n\t"
        "v_add_u32 v151, s9, v174\n\t"
        "v_max_u32 v151, v151, v134\n\t"
        "v_mfma_f32_16x16x32_bf16 v[36:39], v[232:235], v[92:95], v[36:39]\n\t"
        "ds_read_b128 v[84:87], v183 offset:0\n\t"
        "v_xor_b32 v135, v181, v135\n\t"
        "v_mfma_f32_16x16x32_bf16 v[40:43], v[236:239], v[92:95], v[40:43]\n\t"
        "s_add_u32 s55, s41, 0\n\t"
        "v_cvt_pk_bf16_f32 v116, v0, v1\n\t"
        "v_mfma_f32_16x16x32_bf16 v[44:47], v[240:243], v[92:95], v[44:47]\n\t"
        "v_cvt_pk_bf16_f32 v117, v2, v3\n\t"
        "v_cvt_pk_bf16_f32 v118, v4, v5\n\t"
        "s_waitcnt lgkmcnt(5)\n\t"
        "v_mfma_f32_16x16x32_bf16 v[24:27], v[244:247], v[96:99], v[24:27]\n\t"
        "ds_read_b128 v[88:91], v183 offset:64\n\t"
        "v_cvt_pk_bf16_f32 v119, v6, v7\n\t"
        "v_cvt_pk_bf16_f32 v120, v8, v9\n\t"
        "v_mfma_f32_16x16x32_bf16 v[28:31], v[248:251], v[96:99], v[28:31]\n\t"
        "v_cvt_pk_bf16_f32 v121, v10, v11\n\t"
        "s_waitcnt lgkmcnt(5)\n\t"
        "v_lshlrev_b32_sdwa v157, 5, v160 dst_sel:DWORD dst_unused:UNUSED_PAD src0_sel:DWORD src1_sel:WORD_0\n\t"
        "v_mfma_f32_16x16x32_bf16 v[32:35], v[252:255], v[96:99], v[32:35]\n\t"
        "v_permlane16_swap_b32 v116, v118\n\t"
        "v_mfma_f32_16x16x32_bf16 v[36:39], v[220:223], v[96:99], v[36:39]\n\t"
        "v_permlane16_swap_b32 v117, v119\n\t"
        "v_mfma_f32_16x16x32_bf16 v[40:43], v[224:227], v[96:99], v[40:43]\n\t"
        "buffer_store_dwordx4 v[116:119], v144, s[28:31], s55 offen sc1\n\t"
        "v_mfma_f32_16x16x32_bf16 v[44:47], v[228:231], v[96:99], v[44:47]\n\t"
        "ds_write_b64 v146, v[120:121] offset:0\n\t"
        "v_mfma_f32_16x16x32_bf16 v[36:39], v[244:247], v[100:103], v[36:39]\n\t"
        "ds_read_b128 v[92:95], v183 offset:128\n\t"
        "v_add_u32 v183, s9, v175\n\t"
        "v_max_u32 v183, v183, v135\n\t"
        "v_mfma_f32_16x16x32_bf16 v[40:43], v[248:251], v[100:103], v[40:43]\n\t"
        "ds_read_b128 v[96:99], v151 offset:0\n\t"
        "v_xor_b32 v136, v181, v136\n\t"
        "v_mfma_f32_16x16x32_bf16 v[44:47], v[252:255], v[100:103], v[44:47]\n\t"
        "s_add_u32 s55, s41, 43008\n\t"
        "v_cvt_pk_bf16_f32 v116, v12, v13\n\t"
        "s_waitcnt lgkmcnt(3)\n\t"
        "v_mfma_f32_16x16x32_bf16 v[48:51], a[12:15], v[104:107], 0\n\t"
        "ds_read_b128 v[100:103], v151 offset:64\n\t"
        "v_cvt_pk_bf16_f32 v117, v14, v15\n\t"
        "v_mfma_f32_16x16x32_bf16 v[52:55], a[16:19], v[104:107], 0\n\t"
        "v_cvt_pk_bf16_f32 v118, v16, v17\n\t"
        "v_cvt_pk_bf16_f32 v119, v18, v19\n\t"
        "v_cvt_pk_bf16_f32 v120, v20, v21\n\t"
        "v_mfma_f32_16x16x32_bf16 v[56:59], a[20:23], v[104:107], 0\n\t"
        "v_cvt_pk_bf16_f32 v121, v22, v23\n\t"
        "v_lshlrev_b32_sdwa v158, 5, v160 dst_sel:DWORD dst_unused:UNUSED_PAD src0_sel:DWORD src1_sel:WORD_1\n\t"
        "v_mfma_f32_16x16x32_bf16 v[48:51], a[48:51], v[108:111], v[48:51]\n\t"
        "v_permlane16_swap_b32 v116, v118\n\t"
        "v_mfma_f32_16x16x32_bf16 v[52:55], a[52:55], v[108:111], v[52:55]\n\t"
        "v_permlane16_swap_b32 v117, v119\n\t"
        "v_mfma_f32_16x16x32_bf16 v[56:59], a[56:59], v[108:111], v[56:59]\n\t"
        "ds_read_b128 v[104:107], v151 offset:128\n\t"
        "v_add_u32 v151, s9, v176\n\t"
        "v_mfma_f32_16x16x32_bf16 v[48:51], a[84:87], v[112:115], v[48:51]\n\t"
        "ds_read_b128 v[108:111], v183 offset:0\n\t"
        "v_max_u32 v151, v151, v136\n\t"
        "v_xor_b32 v137, v181, v137\n\t"
        "v_mfma_f32_16x16x32_bf16 v[52:55], a[88:91], v[112:115], v[52:55]\n\t"
        "v_or_b32 v157, v184, v157\n\t"
        "v_or_b32 v158, v184, v158\n\t"
        "v_mfma_f32_16x16x32_bf16 v[56:59], a[92:95], v[112:115], v[56:59]\n\t"
        "ds_read_b64_tr_b16 v[60:61], v157 offset:0\n\t"
        "v_xor_b32 v159, 32, v157\n\t"
        "v_mfma_f32_16x16x32_bf16 v[48:51], a[120:123], v[84:87], v[48:51]\n\t"
        "buffer_store_dwordx4 v[116:119], v144, s[28:31], s55 offen sc1\n\t"
        "v_mfma_f32_16x16x32_bf16 v[52:55], a[124:127], v[84:87], v[52:55]\n\t"
        "ds_write_b64 v146, v[120:121] offset:2048\n\t"
        "v_mfma_f32_16x16x32_bf16 v[56:59], a[128:131], v[84:87], v[56:59]\n\t"
        "ds_read_b64_tr_b16 v[62:63], v158 offset:0\n\t"
        "s_add_u32 s55, s41, 86016\n\t"
        "v_cvt_pk_bf16_f32 v116, v24, v25\n\t"
        "v_mfma_f32_16x16x32_bf16 v[48:51], a[156:159], v[88:91], v[48:51]\n\t"
        "ds_read_b64_tr_b16 v[64:65], v159 offset:0\n\t"
        "v_cvt_pk_bf16_f32 v117, v26, v27\n\t"
        "v_mfma_f32_16x16x32_bf16 v[52:55], a[160:163], v[88:91], v[52:55]\n\t"
        "ds_read_b64_tr_b16 v[68:69], v157 offset:64\n\t"
        "v_cvt_pk_bf16_f32 v118, v28, v29\n\t"
        "v_mfma_f32_16x16x32_bf16 v[56:59], a[164:167], v[88:91], v[56:59]\n\t"
        "ds_read_b128 v[112:115], v183 offset:64\n\t"
        "v_cvt_pk_bf16_f32 v119, v30, v31\n\t"
        "s_waitcnt lgkmcnt(6)\n\t"
        "v_mfma_f32_16x16x32_bf16 v[48:51], a[192:195], v[92:95], v[48:51]\n\t"
        "ds_read_b64_tr_b16 v[70:71], v158 offset:64\n\t"
        "v_cvt_pk_bf16_f32 v120, v32, v33\n\t"
        "v_mfma_f32_16x16x32_bf16 v[52:55], a[196:199], v[92:95], v[52:55]\n\t"
        "ds_read_b64_tr_b16 v[72:73], v159 offset:64\n\t"
        "v_cvt_pk_bf16_f32 v121, v34, v35\n\t"
        "v_xor_b32 v152, 32, v158\n\t"
        "v_mfma_f32_16x16x32_bf16 v[56:59], a[200:203], v[92:95], v[56:59]\n\t"
        "v_permlane16_swap_b32 v116, v118\n\t"
        "v_mfma_f32_16x16x32_bf16 v[48:51], a[228:231], v[96:99], v[48:51]\n\t"
        "v_permlane16_swap_b32 v117, v119\n\t"
        "v_mfma_f32_16x16x32_bf16 v[52:55], a[232:235], v[96:99], v[52:55]\n\t"
        "ds_read_b128 v[84:87], v183 offset:128\n\t"
        "v_add_u32 v183, s9, v177\n\t"
        "v_mfma_f32_16x16x32_bf16 v[56:59], a[236:239], v[96:99], v[56:59]\n\t"
        "ds_read_b128 v[88:91], v151 offset:0\n\t"
        "v_max_u32 v183, v183, v137\n\t"
        "v_mfma_f32_16x16x32_bf16 v[48:51], v[196:199], v[100:103], v[48:51]\n\t"
        "ds_read_b64_tr_b16 v[66:67], v152 offset:0\n\t"
        "v_xor_b32 v138, v181, v138\n\t"
        "v_perm_b32 v164, v162, v162, v168\n\t"
        "v_mfma_f32_16x16x32_bf16 v[52:55], v[200:203], v[100:103], v[52:55]\n\t"
        "ds_read_b64_tr_b16 v[74:75], v152 offset:64\n\t"
        "v_perm_b32 v165, v162, v162, v169\n\t"
        "v_mfma_f32_16x16x32_bf16 v[56:59], v[204:207], v[100:103], v[56:59]\n\t"
        "buffer_store_dwordx4 v[116:119], v144, s[28:31], s55 offen sc1\n\t"
        "v_mfma_f32_16x16x32_bf16 v[48:51], v[232:235], v[104:107], v[48:51]\n\t"
        "ds_write_b64 v146, v[120:121] offset:4096\n\t"
        "v_mfma_f32_16x16x32_bf16 v[52:55], v[236:239], v[104:107], v[52:55]\n\t"
        "ds_read_b128 v[92:95], v151 offset:64\n\t"
        "s_add_u32 s55, s41, 129024\n\t"
        "v_mfma_f32_16x16x32_bf16 v[56:59], v[240:243], v[104:107], v[56:59]\n\t"
        "ds_read_b128 v[96:99], v151 offset:128\n\t"
        "v_add_u32 v151, s9, v178\n\t"
        "v_max_u32 v151, v151, v138\n\t"
        "v_mfma_f32_16x16x32_bf16 v[48:51], a[0:3], v[108:111], v[48:51]\n\t"
        "ds_read_b64_tr_b16 v[76:77], v157 offset:128\n\t"
        "v_xor_b32 v139, v181, v139\n\t"
        "v_mfma_f32_16x16x32_bf16 v[52:55], a[4:7], v[108:111], v[52:55]\n\t"
        "ds_read_b64_tr_b16 v[78:79], v158 offset:128\n\t"
        "v_cvt_pk_bf16_f32 v116, v36, v37\n\t"
        "v_mfma_f32_16x16x32_bf16 v[56:59], a[8:11], v[108:111], v[56:59]\n\t"
        "ds_read_b128 v[100:103], v183 offset:0\n\t"
        "v_cvt_pk_bf16_f32 v117, v38, v39\n\t"
        "s_waitcnt lgkmcnt(7)\n\t"
        "v_mfma_f32_16x16x32_bf16 v[48:51], a[36:39], v[112:115], v[48:51]\n\t"
        "ds_read_b64_tr_b16 v[80:81], v159 offset:128\n\t"
        "v_cvt_pk_bf16_f32 v118, v40, v41\n\t"
        "v_mfma_f32_16x16x32_bf16 v[52:55], a[40:43], v[112:115], v[52:55]\n\t"
        "ds_read_b64_tr_b16 v[82:83], v152 offset:128\n\t"
        "v_cvt_pk_bf16_f32 v119, v42, v43\n\t"
        "v_cvt_pk_bf16_f32 v120, v44, v45\n\t"
        "v_mfma_f32_16x16x32_bf16 v[56:59], a[44:47], v[112:115], v[56:59]\n\t"
        "ds_read_b128 v[104:107], v183 offset:64\n\t"
        "v_cvt_pk_bf16_f32 v121, v46, v47\n\t"
        "v_mfma_f32_16x16x32_bf16 v[48:51], a[72:75], v[84:87], v[48:51]\n\t"
        "v_permlane16_swap_b32 v116, v118\n\t"
        "v_mfma_f32_16x16x32_bf16 v[52:55], a[76:79], v[84:87], v[52:55]\n\t"
        "v_permlane16_swap_b32 v117, v119\n\t"
        "v_mfma_f32_16x16x32_bf16 v[56:59], a[80:83], v[84:87], v[56:59]\n\t"
        "ds_read_b128 v[108:111], v183 offset:128\n\t"
        "v_add_u32 v183, s9, v179\n\t"
        "v_mfma_f32_16x16x32_bf16 v[48:51], a[108:111], v[88:91], v[48:51]\n\t"
        "ds_read_b128 v[112:115], v151 offset:0\n\t"
        "v_max_u32 v183, v183, v139\n\t"
        "v_perm_b32 v166, v162, v162, v170\n\t"
        "v_mfma_f32_16x16x32_bf16 v[52:55], a[112:115], v[88:91], v[52:55]\n\t"
        "buffer_store_dwordx4 v[116:119], v144, s[28:31], s55 offen sc1\n\t"
        "v_mfma_f32_16x16x32_bf16 v[56:59], a[116:119], v[88:91], v[56:59]\n\t"
        "ds_write_b64 v146, v[120:121] offset:6144\n\t"
        "s_waitcnt lgkmcnt(5)\n\t"
        "v_mfma_f32_16x16x32_bf16 v[48:51], a[144:147], v[92:95], v[48:51]\n\t"
        "ds_read_b128 v[84:87], v151 offset:64\n\t"
        "v_perm_b32 v167, v162, v162, v171\n\t"
        "v_mfma_f32_16x16x32_bf16 v[52:55], a[148:151], v[92:95], v[52:55]\n\t"
        "ds_read_b128 v[88:91], v151 offset:128\n\t"
        "v_cmp_ne_u32 vcc, 0, v163\n\t"
        "v_mfma_f32_16x16x32_bf16 v[0:3], v[60:63], v[164:167], 0\n\t"
        "v_mfma_f32_16x16x32_bf16 v[56:59], a[152:155], v[92:95], v[56:59]\n\t"
        "v_mfma_f32_16x16x32_bf16 v[4:7], v[64:67], v[164:167], 0\n\t"
        "v_mfma_f32_16x16x32_bf16 v[48:51], a[180:183], v[96:99], v[48:51]\n\t"
        "ds_read_b128 v[92:95], v183 offset:0\n\t"
        "v_mfma_f32_16x16x32_bf16 v[8:11], v[68:71], v[164:167], 0\n\t"
        "v_mfma_f32_16x16x32_bf16 v[52:55], a[184:187], v[96:99], v[52:55]\n\t"
        "v_mfma_f32_16x16x32_bf16 v[12:15], v[72:75], v[164:167], 0\n\t"
        "v_mfma_f32_16x16x32_bf16 v[56:59], a[188:191], v[96:99], v[56:59]\n\t"
        "v_mfma_f32_16x16x32_bf16 v[16:19], v[76:79], v[164:167], 0\n\t"
        "v_mfma_f32_16x16x32_bf16 v[48:51], a[216:219], v[100:103], v[48:51]\n\t"
        "ds_read_b128 v[96:99], v183 offset:64\n\t"
        "s_waitcnt lgkmcnt(2)\n\t"
        "v_mfma_f32_16x16x32_bf16 v[20:23], v[80:83], v[164:167], 0\n\t"
        "s_cbranch_vccz .Lx52%=\n\t"
        "v_lshlrev_b32_sdwa v157, 5, v161 dst_sel:DWORD dst_unused:UNUSED_PAD src0_sel:DWORD src1_sel:WORD_0\n\t"
        "v_lshlrev_b32_sdwa v158, 5, v161 dst_sel:DWORD dst_unused:UNUSED_PAD src0_sel:DWORD src1_sel:WORD_1\n\t"
        "v_or_b32 v157, v184, v157\n\t"
        "v_or_b32 v158, v184, v158\n\t"
        "v_xor_b32 v159, 32, v157\n\t"
        "v_xor_b32 v152, 32, v158\n\t"
        "ds_read_b64_tr_b16 v[60:61], v157 offset:0\n\t"
        "ds_read_b64_tr_b16 v[62:63], v158 offset:0\n\t"
        "ds_read_b64_tr_b16 v[64:65], v159 offset:0\n\t"
        "ds_read_b64_tr_b16 v[66:67], v152 offset:0\n\t"
        "ds_read_b64_tr_b16 v[68:69], v157 offset:64\n\t"
        "ds_read_b64_tr_b16 v[70:71], v158 offset:64\n\t"
        "ds_read_b64_tr_b16 v[72:73], v159 offset:64\n\t"
        "ds_read_b64_tr_b16 v[74:75], v152 offset:64\n\t"
        "ds_read_b64_tr_b16 v[76:77], v157 offset:128\n\t"
        "ds_read_b64_tr_b16 v[78:79], v158 offset:128\n\t"
        "ds_read_b64_tr_b16 v[80:81], v159 offset:128\n\t"
        "ds_read_b64_tr_b16 v[82:83], v152 offset:128\n\t"
        "v_perm_b32 v164, v163, v163, v168\n\t"
        "v_perm_b32 v165, v163, v163, v169\n\t"
        "v_perm_b32 v166, v163, v163, v170\n\t"
        "v_perm_b32 v167, v163, v163, v171\n\t"
        "s_waitcnt lgkmcnt(10)\n\t"
        "s_nop 0\n\t"
        "v_mfma_f32_16x16x32_bf16 v[0:3], v[60:63], v[164:167], v[0:3]\n\t"
        "s_waitcnt lgkmcnt(8)\n\t"
        "v_mfma_f32_16x16x32_bf16 v[4:7], v[64:67], v[164:167], v[4:7]\n\t"
        "s_waitcnt lgkmcnt(6)\n\t"
        "v_mfma_f32_16x16x32_bf16 v[8:11], v[68:71], v[164:167], v[8:11]\n\t"
        "s_waitcnt lgkmcnt(4)\n\t"
        "v_mfma_f32_16x16x32_bf16 v[12:15], v[72:75], v[164:167], v[12:15]\n\t"
        "s_waitcnt lgkmcnt(2)\n\t"
        "v_mfma_f32_16x16x32_bf16 v[16:19], v[76:79], v[164:167], v[16:19]\n\t"
        "s_waitcnt lgkmcnt(0)\n\t"
        "v_mfma_f32_16x16x32_bf16 v[20:23], v[80:83], v[164:167], v[20:23]\n\t"
        "s_nop 4\n\t"
        ".Lx52%=:\n\t"
        "v_cvt_pk_bf16_f32 v0, v0, v1\n\t"
        "v_cvt_pk_bf16_f32 v1, v2, v3\n\t"
        "v_cvt_pk_bf16_f32 v4, v4, v5\n\t"
        "v_cvt_pk_bf16_f32 v5, v6, v7\n\t"
        "v_mfma_f32_16x16x32_bf16 v[52:55], a[220:223], v[100:103], v[52:55]\n\t"
        "ds_write_b64 v186, v[0:1] offset:24576\n\t"
        "v_mfma_f32_16x16x32_bf16 v[56:59], a[224:227], v[100:103], v[56:59]\n\t"
        "ds_write_b64 v154, v[4:5] offset:24576\n\t"
        "v_mfma_f32_16x16x32_bf16 v[48:51], a[252:255], v[104:107], v[48:51]\n\t"
        "ds_read_b128 v[100:103], v183 offset:128\n\t"
        "v_cvt_pk_bf16_f32 v8, v8, v9\n\t"
        "v_mfma_f32_16x16x32_bf16 v[52:55], v[188:191], v[104:107], v[52:55]\n\t"
        "v_cvt_pk_bf16_f32 v9, v10, v11\n\t"
        "v_cvt_pk_bf16_f32 v12, v12, v13\n\t"
        "v_mfma_f32_16x16x32_bf16 v[56:59], v[192:195], v[104:107], v[56:59]\n\t"
        "ds_write_b64 v186, v[8:9] offset:24640\n\t"
        "v_mfma_f32_16x16x32_bf16 v[48:51], v[220:223], v[108:111], v[48:51]\n\t"
        "v_cvt_pk_bf16_f32 v13, v14, v15\n\t"
        "v_cvt_pk_bf16_f32 v16, v16, v17\n\t"
        "v_cvt_pk_bf16_f32 v17, v18, v19\n\t"
        "v_mfma_f32_16x16x32_bf16 v[52:55], v[224:227], v[108:111], v[52:55]\n\t"
        "ds_write_b64 v154, v[12:13] offset:24640\n\t"
        "v_mfma_f32_16x16x32_bf16 v[56:59], v[228:231], v[108:111], v[56:59]\n\t"
        "ds_write_b64 v186, v[16:17] offset:24704\n\t"
        "v_mfma_f32_16x16x32_bf16 v[48:51], a[24:27], v[112:115], v[48:51]\n\t"
        "v_cvt_pk_bf16_f32 v20, v20, v21\n\t"
        "v_cvt_pk_bf16_f32 v21, v22, v23\n\t"
        "v_mfma_f32_16x16x32_bf16 v[52:55], a[28:31], v[112:115], v[52:55]\n\t"
        "ds_write_b64 v154, v[20:21] offset:24704\n\t"
        "v_mfma_f32_16x16x32_bf16 v[56:59], a[32:35], v[112:115], v[56:59]\n\t"
        "s_cmp_gt_u32 s61, 48\n\t"
        "s_cbranch_scc1 .Lx54%=\n\t"
        ".Lx55%=:\n\t"
        "v_mfma_f32_16x16x32_bf16 v[48:51], a[60:63], v[84:87], v[48:51]\n\t"
        "v_mfma_f32_16x16x32_bf16 v[52:55], a[64:67], v[84:87], v[52:55]\n\t"
        "v_mfma_f32_16x16x32_bf16 v[56:59], a[68:71], v[84:87], v[56:59]\n\t"
        "v_mfma_f32_16x16x32_bf16 v[48:51], a[96:99], v[88:91], v[48:51]\n\t"
        "v_mfma_f32_16x16x32_bf16 v[52:55], a[100:103], v[88:91], v[52:55]\n\t"
        "s_waitcnt lgkmcnt(0)\n\t"
        "s_barrier\n\t"
        "v_xor_b32 v140, v181, v140\n\t"
        "v_mfma_f32_16x16x32_bf16 v[56:59], a[104:107], v[88:91], v[56:59]\n\t"
        "ds_read_b128 v[132:135], v187 offset:4096\n\t"
        "v_mfma_f32_16x16x32_bf16 v[48:51], a[132:135], v[92:95], v[48:51]\n\t"
        "ds_read_b128 v[136:139], v187 offset:4112\n\t"
        "v_add_u32 v151, s9, v180\n\t"
        "v_max_u32 v151, v151, v140\n\t"
        "s_waitcnt lgkmcnt(1)\n\t"
        "v_xor_b32 v132, v181, v132\n\t"
        "v_mfma_f32_16x16x32_bf16 v[52:55], a[136:139], v[92:95], v[52:55]\n\t"
        "ds_read_b128 v[104:107], v151 offset:0\n\t"
        "v_mfma_f32_16x16x32_bf16 v[56:59], a[140:143], v[92:95], v[56:59]\n\t"
        "ds_read_b128 v[108:111], v151 offset:64\n\t"
        "v_mfma_f32_16x16x32_bf16 v[48:51], a[168:171], v[96:99], v[48:51]\n\t"
        "ds_read_b128 v[112:115], v151 offset:128\n\t"
        "v_mfma_f32_16x16x32_bf16 v[52:55], a[172:175], v[96:99], v[52:55]\n\t"
        "ds_read_b32 v140, v187 offset:4128\n\t"
        "v_readlane_b32 s9, v155, 48\n\t"
        "v_mfma_f32_16x16x32_bf16 v[56:59], a[176:179], v[96:99], v[56:59]\n\t"
        "ds_read_b128 v[124:127], v182 offset:16\n\t"
        "v_add_u32 v183, s9, v172\n\t"
        "v_max_u32 v183, v183, v132\n\t"
        "v_xor_b32 v133, v181, v133\n\t"
        "v_mfma_f32_16x16x32_bf16 v[48:51], a[204:207], v[100:103], v[48:51]\n\t"
        "ds_read_b128 v[84:87], v183 offset:0\n\t"
        "v_add_u32 v151, s9, v173\n\t"
        "v_mfma_f32_16x16x32_bf16 v[52:55], a[208:211], v[100:103], v[52:55]\n\t"
        "ds_read_b128 v[88:91], v183 offset:64\n\t"
        "v_max_u32 v151, v151, v133\n\t"
        "v_mfma_f32_16x16x32_bf16 v[56:59], a[212:215], v[100:103], v[56:59]\n\t"
        "ds_read_b128 v[92:95], v183 offset:128\n\t"
        "v_xor_b32 v134, v181, v134\n\t"
        "s_waitcnt lgkmcnt(7)\n\t"
        "v_mfma_f32_16x16x32_bf16 v[48:51], a[240:243], v[104:107], v[48:51]\n\t"
        "ds_read_b128 v[96:99], v151 offset:0\n\t"
        "v_add_u32 v183, s9, v174\n\t"
        ".LpM2%=:\n\t"
        "s_cbranch_execz .LrM%=\n\t"
        "v_mfma_f32_16x16x32_bf16 v[52:55], a[244:247], v[104:107], v[52:55]\n\t"
        "ds_read_b128 v[100:103], v151 offset:64\n\t"
        "v_max_u32 v183, v183, v134\n\t"
        "v_mfma_f32_16x16x32_bf16 v[56:59], a[248:251], v[104:107], v[56:59]\n\t"
        "v_xor_b32 v135, v181, v135\n\t"
        "s_waitcnt lgkmcnt(8)\n\t"
        "v_mfma_f32_16x16x32_bf16 v[48:51], v[208:211], v[108:111], v[48:51]\n\t"
        "ds_read_b128 v[104:107], v151 offset:128\n\t"
        "v_add_u32 v151, s9, v175\n\t"
        "v_mfma_f32_16x16x32_bf16 v[52:55], v[212:215], v[108:111], v[52:55]\n\t"
        "v_max_u32 v151, v151, v135\n\t"
        "v_xor_b32 v136, v181, v136\n\t"
        "v_mfma_f32_16x16x32_bf16 v[56:59], v[216:219], v[108:111], v[56:59]\n\t"
        "s_waitcnt lgkmcnt(6)\n\t"
        "v_or3_b32 v124, v124, v125, v126\n\t"
        "v_or_b32 v124, v124, v127\n\t"
        "v_mfma_f32_16x16x32_bf16 v[48:51], v[244:247], v[112:115], v[48:51]\n\t"
        "ds_read_b128 v[108:111], v183 offset:0\n\t"
        "v_readfirstlane_b32 s8, v124\n\t"
        "v_mfma_f32_16x16x32_bf16 v[52:55], v[248:251], v[112:115], v[52:55]\n\t"
        "ds_read_b128 v[124:127], v147 offset:0\n\t"
        "s_or_b32 s6, s6, s8\n\t"
        "v_mfma_f32_16x16x32_bf16 v[56:59], v[252:255], v[112:115], v[56:59]\n\t"
        "s_waitcnt lgkmcnt(6)\n\t"
        "v_mfma_f32_16x16x32_bf16 v[72:75], a[12:15], v[84:87], 0\n\t"
        "ds_read_b128 v[112:115], v183 offset:64\n\t"
        "v_mfma_f32_16x16x32_bf16 v[76:79], a[16:19], v[84:87], 0\n\t"
        "v_mfma_f32_16x16x32_bf16 v[80:83], a[20:23], v[84:87], 0\n\t"
        "v_mfma_f32_16x16x32_bf16 v[72:75], a[48:51], v[88:91], v[72:75]\n\t"
        "ds_read_b128 v[84:87], v183 offset:128\n\t"
        "v_add_u32 v183, s9, v176\n\t"
        "v_mfma_f32_16x16x32_bf16 v[76:79], a[52:55], v[88:91], v[76:79]\n\t"
        "v_max_u32 v183, v183, v136\n\t"
        "v_xor_b32 v137, v181, v137\n\t"
        "v_mfma_f32_16x16x32_bf16 v[80:83], a[56:59], v[88:91], v[80:83]\n\t"
        "s_waitcnt lgkmcnt(4)\n\t"
        "v_mfma_f32_16x16x32_bf16 v[72:75], a[84:87], v[92:95], v[72:75]\n\t"
        "ds_read_b128 v[88:91], v151 offset:0\n\t"
        "v_mfma_f32_16x16x32_bf16 v[76:79], a[88:91], v[92:95], v[76:79]\n\t"
        "s_waitcnt lgkmcnt(3)\n\t"
        "buffer_store_dwordx4 v[124:127], v145, s[28:31], s50 offen sc1\n\t"
        "v_mfma_f32_16x16x32_bf16 v[80:83], a[92:95], v[92:95], v[80:83]\n\t"
        "ds_read_b128 v[124:127], v147 offset:4096\n\t"
        "s_add_u32 s50, s50, 86016\n\t"
        "v_mfma_f32_16x16x32_bf16 v[72:75], a[120:123], v[96:99], v[72:75]\n\t"
        "ds_read_b128 v[92:95], v151 offset:64\n\t"
        "v_mfma_f32_16x16x32_bf16 v[76:79], a[124:127], v[96:99], v[76:79]\n\t"
        "v_mfma_f32_16x16x32_bf16 v[80:83], a[128:131], v[96:99], v[80:83]\n\t"
        "v_mfma_f32_16x16x32_bf16 v[72:75], a[156:159], v[100:103], v[72:75]\n\t"
        "ds_read_b128 v[96:99], v151 offset:128\n\t"
        "v_add_u32 v151, s9, v177\n\t"
        "v_mfma_f32_16x16x32_bf16 v[76:79], a[160:163], v[100:103], v[76:79]\n\t"
        "v_max_u32 v151, v151, v137\n\t"
        "v_xor_b32 v138, v181, v138\n\t"
        "v_mfma_f32_16x16x32_bf16 v[80:83], a[164:167], v[100:103], v[80:83]\n\t"
        "v_mfma_f32_16x16x32_bf16 v[72:75], a[192:195], v[104:107], v[72:75]\n\t"
        "ds_read_b128 v[100:103], v183 offset:0\n\t"
        "v_mfma_f32_16x16x32_bf16 v[76:79], a[196:199], v[104:107], v[76:79]\n\t"
        "v_mfma_f32_16x16x32_bf16 v[80:83], a[200:203], v[104:107], v[80:83]\n\t"
        "v_mfma_f32_16x16x32_bf16 v[72:75], a[228:231], v[108:111], v[72:75]\n\t"
        "ds_read_b128 v[104:107], v183 offset:64\n\t"
        "v_mfma_f32_16x16x32_bf16 v[76:79], a[232:235], v[108:111], v[76:79]\n\t"
        "v_mfma_f32_16x16x32_bf16 v[80:83], a[236:239], v[108:111], v[80:83]\n\t"
        "s_waitcnt lgkmcnt(3)\n\t"
        "v_mfma_f32_16x16x32_bf16 v[72:75], v[196:199], v[112:115], v[72:75]\n\t"
        "buffer_store_dwordx4 v[124:127], v145, s[28:31], s50 offen sc1\n\t"
        "v_mfma_f32_16x16x32_bf16 v[76:79], v[200:203], v[112:115], v[76:79]\n\t"
        "ds_read_b128 v[108:111], v183 offset:128\n\t"
        "v_add_u32 v183, s9, v178\n\t"
        "v_mfma_f32_16x16x32_bf16 v[80:83], v[204:207], v[112:115], v[80:83]\n\t"
        "v_max_u32 v183, v183, v138\n\t"
        "v_xor_b32 v139, v181, v139\n\t"
        "v_mfma_f32_16x16x32_bf16 v[72:75], v[232:235], v[84:87], v[72:75]\n\t"
        "ds_read_b128 v[112:115], v151 offset:0\n\t"
        "v_readlane_b32 s55, v155, 49\n\t"
        "v_mfma_f32_16x16x32_bf16 v[76:79], v[236:239], v[84:87], v[76:79]\n\t"
        "v_cvt_pk_bf16_f32 v116, v48, v49\n\t"
        "v_cvt_pk_bf16_f32 v117, v50, v51\n\t"
        "v_mfma_f32_16x16x32_bf16 v[80:83], v[240:243], v[84:87], v[80:83]\n\t"
        "v_cvt_pk_bf16_f32 v118, v52, v53\n\t"
        "v_cvt_pk_bf16_f32 v119, v54, v55\n\t"
        "v_mfma_f32_16x16x32_bf16 v[72:75], a[0:3], v[88:91], v[72:75]\n\t"
        "ds_read_b128 v[84:87], v151 offset:64\n\t"
        "v_cvt_pk_bf16_f32 v120, v56, v57\n\t"
        "v_mfma_f32_16x16x32_bf16 v[76:79], a[4:7], v[88:91], v[76:79]\n\t"
        "v_cvt_pk_bf16_f32 v121, v58, v59\n\t"
        "v_mfma_f32_16x16x32_bf16 v[80:83], a[8:11], v[88:91], v[80:83]\n\t"
        "v_permlane16_swap_b32 v116, v118\n\t"
        "v_mfma_f32_16x16x32_bf16 v[72:75], a[36:39], v[92:95], v[72:75]\n\t"
        "v_permlane16_swap_b32 v117, v119\n\t"
        "v_mfma_f32_16x16x32_bf16 v[76:79], a[40:43], v[92:95], v[76:79]\n\t"
        "ds_read_b128 v[88:91], v151 offset:128\n\t"
        "v_add_u32 v151, s9, v179\n\t"
        "v_mfma_f32_16x16x32_bf16 v[80:83], a[44:47], v[92:95], v[80:83]\n\t"
        "v_max_u32 v151, v151, v139\n\t"
        "v_xor_b32 v140, v181, v140\n\t"
        "s_waitcnt lgkmcnt(3)\n\t"
        "v_mfma_f32_16x16x32_bf16 v[72:75], a[72:75], v[96:99], v[72:75]\n\t"
        "ds_read_b128 v[92:95], v183 offset:0\n\t"
        "v_mfma_f32_16x16x32_bf16 v[76:79], a[76:79], v[96:99], v[76:79]\n\t"
        "v_mfma_f32_16x16x32_bf16 v[80:83], a[80:83], v[96:99], v[80:83]\n\t"
        "buffer_store_dwordx4 v[116:119], v148, s[28:31], s55 offen sc1\n\t"
        "v_mfma_f32_16x16x32_bf16 v[72:75], a[108:111], v[100:103], v[72:75]\n\t"
        "ds_write_b64 v146, v[120:121] offset:8192\n\t"
        "v_mfma_f32_16x16x32_bf16 v[76:79], a[112:115], v[100:103], v[76:79]\n\t"
        "ds_read_b128 v[96:99], v183 offset:64\n\t"
        "v_mfma_f32_16x16x32_bf16 v[80:83], a[116:119], v[100:103], v[80:83]\n\t"
        "ds_read_b128 v[100:103], v183 offset:128\n\t"
        "v_add_u32 v183, s9, v180\n\t"
        "v_mfma_f32_16x16x32_bf16 v[72:75], a[144:147], v[104:107], v[72:75]\n\t"
        "v_max_u32 v183, v183, v140\n\t"
        "v_mfma_f32_16x16x32_bf16 v[76:79], a[148:151], v[104:107], v[76:79]\n\t"
        "v_mfma_f32_16x16x32_bf16 v[80:83], a[152:155], v[104:107], v[80:83]\n\t"
        "ds_read_b128 v[104:107], v151 offset:0\n\t"
        "v_mfma_f32_16x16x32_bf16 v[72:75], a[180:183], v[108:111], v[72:75]\n\t"
        "v_mfma_f32_16x16x32_bf16 v[76:79], a[184:187], v[108:111], v[76:79]\n\t"
        "v_mfma_f32_16x16x32_bf16 v[80:83], a[188:191], v[108:111], v[80:83]\n\t"
        "ds_read_b128 v[108:111], v151 offset:64\n\t"
        "s_waitcnt lgkmcnt(5)\n\t"
        "v_mfma_f32_16x16x32_bf16 v[72:75], a[216:219], v[112:115], v[72:75]\n\t"
        "v_mfma_f32_16x16x32_bf16 v[76:79], a[220:223], v[112:115], v[76:79]\n\t"
        "v_mfma_f32_16x16x32_bf16 v[80:83], a[224:227], v[112:115], v[80:83]\n\t"
        "ds_read_b128 v[112:115], v151 offset:128\n\t"
        "v_mfma_f32_16x16x32_bf16 v[72:75], a[252:255], v[84:87], v[72:75]\n\t"
        "v_mfma_f32_16x16x32_bf16 v[76:79], v[188:191], v[84:87], v[76:79]\n\t"
        "v_mfma_f32_16x16x32_bf16 v[80:83], v[192:195], v[84:87], v[80:83]\n\t"
        "ds_read_b128 v[84:87], v183 offset:0\n\t"
        "v_mfma_f32_16x16x32_bf16 v[72:75], v[220:223], v[88:91], v[72:75]\n\t"
        "v_mfma_f32_16x16x32_bf16 v[76:79], v[224:227], v[88:91], v[76:79]\n\t"
        "v_mfma_f32_16x16x32_bf16 v[80:83], v[228:231], v[88:91], v[80:83]\n\t"
        "ds_read_b128 v[88:91], v183 offset:64\n\t"
        "v_mfma_f32_16x16x32_bf16 v[72:75], a[24:27], v[92:95], v[72:75]\n\t"
        "v_mfma_f32_16x16x32_bf16 v[76:79], a[28:31], v[92:95], v[76:79]\n\t"
        "v_mfma_f32_16x16x32_bf16 v[80:83], a[32:35], v[92:95], v[80:83]\n\t"
        "ds_read_b128 v[92:95], v183 offset:128\n\t"
        "s_waitcnt lgkmcnt(4)\n\t"
        "v_mfma_f32_16x16x32_bf16 v[80:83], a[68:71], v[96:99], v[80:83]\n\t"
        "v_mfma_f32_16x16x32_bf16 v[80:83], a[104:107], v[100:103], v[80:83]\n\t"
        "v_mfma_f32_16x16x32_bf16 v[80:83], a[140:143], v[104:107], v[80:83]\n\t"
        "v_mfma_f32_16x16x32_bf16 v[80:83], a[176:179], v[108:111], v[80:83]\n\t"
        "s_waitcnt lgkmcnt(2)\n\t"
        "v_mfma_f32_16x16x32_bf16 v[80:83], a[212:215], v[112:115], v[80:83]\n\t"
        "v_mfma_f32_16x16x32_bf16 v[80:83], a[248:251], v[84:87], v[80:83]\n\t"
        "s_waitcnt lgkmcnt(1)\n\t"
        "v_mfma_f32_16x16x32_bf16 v[80:83], v[216:219], v[88:91], v[80:83]\n\t"
        "s_waitcnt lgkmcnt(0)\n\t"
        "v_mfma_f32_16x16x32_bf16 v[80:83], v[252:255], v[92:95], v[80:83]\n\t"
        "s_nop 2\n\t"
        "v_mfma_f32_16x16x32_bf16 v[72:75], a[60:63], v[96:99], v[72:75]\n\t"
        "s_nop 2\n\t"
        "v_mfma_f32_16x16x32_bf16 v[72:75], a[96:99], v[100:103], v[72:75]\n\t"
        "v_cvt_pk_bf16_f32 v120, v80, v81\n\t"
        "v_cvt_pk_bf16_f32 v121, v82, v83\n\t"
        "v_mfma_f32_16x16x32_bf16 v[72:75], a[132:135], v[104:107], v[72:75]\n\t"
        "ds_write_b64 v146, v[120:121] offset:10240\n\t"
        "v_mfma_f32_16x16x32_bf16 v[72:75], a[168:171], v[108:111], v[72:75]\n\t"
        "v_mfma_f32_16x16x32_bf16 v[72:75], a[204:207], v[112:115], v[72:75]\n\t"
        "v_mfma_f32_16x16x32_bf16 v[72:75], a[240:243], v[84:87], v[72:75]\n\t"
        "s_waitcnt lgkmcnt(0)\n\t"
        "s_barrier\n\t"
        "v_mfma_f32_16x16x32_bf16 v[72:75], v[208:211], v[88:91], v[72:75]\n\t"
        "ds_read_b128 v[124:127], v147 offset:8192\n\t"
        "v_readlane_b32 s55, v155, 51\n\t"
        "v_mfma_f32_16x16x32_bf16 v[72:75], v[244:247], v[92:95], v[72:75]\n\t"
        "s_nop 1\n\t"
        "v_mfma_f32_16x16x32_bf16 v[76:79], a[64:67], v[96:99], v[76:79]\n\t"
        "s_nop 1\n\t"
        "v_mfma_f32_16x16x32_bf16 v[76:79], a[100:103], v[100:103], v[76:79]\n\t"
        "v_mfma_f32_16x16x32_bf16 v[76:79], a[136:139], v[104:107], v[76:79]\n\t"
        "v_mfma_f32_16x16x32_bf16 v[76:79], a[172:175], v[108:111], v[76:79]\n\t"
        "v_mfma_f32_16x16x32_bf16 v[76:79], a[208:211], v[112:115], v[76:79]\n\t"
        "v_cvt_pk_bf16_f32 v116, v72, v73\n\t"
        "v_cvt_pk_bf16_f32 v117, v74, v75\n\t"
        "v_mfma_f32_16x16x32_bf16 v[76:79], a[244:247], v[84:87], v[76:79]\n\t"
        "s_waitcnt lgkmcnt(0)\n\t"
        "buffer_store_dwordx4 v[124:127], v150, s[28:31], s52 offen sc1\n\t"
        "v_mfma_f32_16x16x32_bf16 v[76:79], v[212:215], v[88:91], v[76:79]\n\t"
        "v_mfma_f32_16x16x32_bf16 v[76:79], v[248:251], v[92:95], v[76:79]\n\t"
        "s_nop 7\n\t"
        "v_cvt_pk_bf16_f32 v118, v76, v77\n\t"
        "v_cvt_pk_bf16_f32 v119, v78, v79\n\t"
        "s_nop 0\n\t"
        "v_permlane16_swap_b32 v116, v118\n\t"
        "v_permlane16_swap_b32 v117, v119\n\t"
        "buffer_store_dwordx4 v[116:119], v148, s[28:31], s55 offen sc1\n\t"
        "s_andn2_b32 s6, s6, 2\n\t"
        "s_cbranch_scc1 99f\n\t"
        "s_endpgm\n\t"
        "20:\n\t"
        "ds_bpermute_b32 v64, v65, v155 offset:172\n\t"
        "s_add_u32 m0, s35, 4096\n\t"
        "s_add_u32 s43, s34, 43008\n\t"
        "buffer_load_dwordx4 v143, s[16:19], s43 offen  lds\n\t"
        "ds_bpermute_b32 v78, v81, v155 offset:148\n\t"
        "ds_bpermute_b32 v79, v81, v155 offset:160\n\t"
        "v_readlane_b32 s71, v155, 26\n\t"
        "v_readlane_b32 s10, v155, 29\n\t"
        "s_nop 0\n\t"
        "v_mul_u32_u24 v73, s71, v66\n\t"
        "v_lshrrev_b32 v73, 11, v73\n\t"
        "v_lshlrev_b32 v66, 4, v66\n\t"
        "v_mad_u32_u24 v73, v73, s10, v66\n\t"
        "s_waitcnt lgkmcnt(2)\n\t"
        "v_add_u32 v64, v64, v73\n\t"
        "s_add_u32 m0, s35, 20480\n\t"
        "s_add_u32 s43, s34, 215040\n\t"
        "buffer_load_dwordx4 v143, s[16:19], s43 offen  lds\n\t"
        "v_readlane_b32 s69, v155, 34\n\t"
        "v_readlane_b32 s70, v155, 35\n\t"
        "s_nop 0\n\t"
        "v_mad_u32_u24 v70, v52, s69, v70\n\t"
        "v_mad_u32_u24 v70, v158, s70, v70\n\t"
        "s_mul_i32 s71, s69, s68\n\t"
        "s_add_u32 s71, s71, 157728\n\t"
        "v_mad_u32_u24 v67, v82, s70, v67\n\t"
        "v_add_u32 v67, s71, v67\n\t"
        "v_readlane_b32 s11, v155, 30\n\t"
        "s_add_u32 m0, s37, -24576\n\t"
        "s_add_u32 s36, s36, 6144\n\t"
        "buffer_load_dwordx4 v140, s[24:27], s36 offen offset:0 lds\n\t"
        "s_movk_i32 s10, 223\n\t"
        "v_mov_b32 v75, 0x7ff00000\n\t"
        "v_readlane_b32 s12, v155, 24\n\t"
        "v_readlane_b32 s13, v155, 25\n\t"
        "s_nop 0\n\t"
        "s_bfe_u32 s70, s12, 524288\n\t"
        "s_bfe_u32 s71, s13, 524288\n\t"
        "v_cmp_gt_u32 vcc, s71, v71\n\t"
        "v_add3_u32 v73, v71, s70, -2\n\t"
        "v_med3_i32 v73, v73, 0, s10\n\t"
        "buffer_load_dwordx4 v141, s[24:27], s36 offen offset:1024 lds\n\t"
        "v_cndmask_b32 v74, v75, v72, vcc\n\t"
        "v_mad_u32_u24 v134, v73, s11, v74\n\t"
        "s_bfe_u32 s70, s12, 524296\n\t"
        "s_bfe_u32 s71, s13, 524296\n\t"
        "v_cmp_gt_u32 vcc, s71, v71\n\t"
        "v_add3_u32 v73, v71, s70, -2\n\t"
        "v_med3_i32 v73, v73, 0, s10\n\t"
        "v_cndmask_b32 v74, v75, v72, vcc\n\t"
        "v_mad_u32_u24 v135, v73, s11, v74\n\t"
        "buffer_load_dwordx4 v142, s[24:27], s36 offen offset:2048 lds\n\t"
        "s_bfe_u32 s70, s12, 524304\n\t"
        "s_bfe_u32 s71, s13, 524304\n\t"
        "v_cmp_gt_u32 vcc, s71, v71\n\t"
        "v_add3_u32 v73, v71, s70, -2\n\t"
        "v_med3_i32 v73, v73, 0, s10\n\t"
        "v_cndmask_b32 v74, v75, v72, vcc\n\t"
        "v_mad_u32_u24 v136, v73, s11, v74\n\t"
        "s_bfe_u32 s70, s12, 524312\n\t"
        "s_bfe_u32 s71, s13, 524312\n\t"
        "v_cmp_gt_u32 vcc, s71, v71\n\t"
        "v_add3_u32 v73, v71, s70, -2\n\t"
        "v_med3_i32 v73, v73, 0, s10\n\t"
        "v_cndmask_b32 v74, v75, v72, vcc\n\t"
        "v_mad_u32_u24 v137, v73, s11, v74\n\t"
        "v_readlane_b32 s44, v155, 32\n\t"
        "v_readlane_b32 s45, v155, 33\n\t"
        "v_readlane_b32 s62, v155, 36\n\t"
        "s_mov_b32 exec_lo, 0\n\t"
        "s_mov_b32 exec_hi, 0xfff00000\n\t"
        "ds_write_b32 v119, v155\n\t"
        "s_mov_b64 exec, -1\n\t"
        "s_lshl_b32 s70, s62, 5\n\t"
        "s_sub_u32 s71, s70, 16384\n\t"
        "s_mul_i32 s71, s71, s68\n\t"
        "v_add_u32 v172, s71, v128\n\t"
        "v_add_u32 v173, s71, v129\n\t"
        "v_add_u32 v174, s71, v130\n\t"
        "s_add_u32 s71, s71, s70\n\t"
        "v_add_u32 v175, s71, v128\n\t"
        "v_add_u32 v176, s71, v129\n\t"
        "v_add_u32 v177, s71, v130\n\t"
        "s_add_u32 s71, s71, s70\n\t"
        "v_add_u32 v178, s71, v128\n\t"
        "v_add_u32 v179, s71, v129\n\t"
        "v_add_u32 v180, s71, v130\n\t"
        "s_cmp_eq_u32 s69, 36\n\t"
        "s_cselect_b64 exec, -1, 0\n\t"
        "v_swap_b32 v68, v69\n\t"
        "v_swap_b32 v173, v175\n\t"
        "v_swap_b32 v174, v178\n\t"
        "v_swap_b32 v177, v179\n\t"
        "s_mov_b64 exec, -1\n\t"
        "v_add3_u32 v68, v68, v52, -1\n\t"
        "v_add3_u32 v69, v69, v158, -1\n\t"
        "v_cvt_f32_i32 v68, v68\n\t"
        "v_cvt_f32_i32 v69, v69\n\t"
        "s_waitcnt lgkmcnt(1)\n\t"
        "v_add_f32 v78, v78, v56\n\t"
        "v_add_f32 v79, v79, v57\n\t"
        "v_readlane_b32 s69, v155, 31\n\t"
        "s_nop 1\n\t"
        "v_mov_b32 v154, s69\n\t"
        "v_mov_b32 v119, s62\n\t"
        "v_readlane_b32 s53, v155, 28\n\t"
        "v_readlane_b32 s9, v155, 46\n\t"
        "s_sub_u32 s10, 43200, s11\n\t"
        "s_mul_i32 s10, s10, s68\n\t"
        "s_sub_u32 s11, s11, 192\n\t"
        "v_mad_u32_u24 v148, v158, s11, v144\n\t"
        "v_add_u32 v148, s10, v148\n\t"
        "v_mad_u32_u24 v150, v83, s11, v145\n\t"
        "s_lshl_b32 s70, s49, 0\n\t"
        "s_add_u32 s70, s70, 50\n\t"
        "v_readlane_b32 s52, v155, s70\n\t"
        "s_nop 1\n\t"
        "s_add_u32 s52, s52, s10\n\t"
        "v_readlane_b32 s51, v155, 49\n\t"
        "s_lshl_b32 s70, s49, 30\n\t"
        "s_add_u32 s70, s70, s10\n\t"
        "s_add_u32 s51, s51, s70\n\t"
        "s_mov_b64 exec, 0\n\t"
        "s_cmp_eq_u32 s4, 0\n\t"
        "s_cbranch_scc1 .LpS0%=\n\t"
        "s_cmp_eq_u32 s4, 1\n\t"
        "s_cbranch_scc1 .LpS1%=\n\t"
        "s_cmp_eq_u32 s4, 2\n\t"
        "s_cbranch_scc1 .LpS2%=\n\t"
        "s_nop 0\n\t"
        ".LrS%=:\n\t"
        "s_mov_b64 exec, -1\n\t"
        "s_waitcnt vmcnt(9) lgkmcnt(0)\n\t"
        "s_barrier\n\t"
        "ds_read_b64_tr_b16 a[0:1], v138 offset:6144\n\t"
        "ds_read_b64_tr_b16 a[2:3], v138 offset:6912\n\t"
        "ds_read_b64_tr_b16 a[4:5], v139 offset:6144\n\t"
        "ds_read_b64_tr_b16 a[6:7], v139 offset:6912\n\t"
        "ds_read_b64_tr_b16 a[8:9], v149 offset:6144\n\t"
        "ds_read_b64_tr_b16 a[10:11], v149 offset:6912\n\t"
        "ds_read_b128 v[84:87], v128 offset:8192\n\t"
        "ds_read_b64_tr_b16 a[12:13], v138 offset:12288\n\t"
        "ds_read_b64_tr_b16 a[14:15], v138 offset:13056\n\t"
        "ds_read_b64_tr_b16 a[16:17], v139 offset:12288\n\t"
        "ds_read_b64_tr_b16 a[18:19], v139 offset:13056\n\t"
        "ds_read_b64_tr_b16 a[20:21], v149 offset:12288\n\t"
        "ds_read_b64_tr_b16 a[22:23], v149 offset:13056\n\t"
        "ds_read_b128 v[88:91], v128 offset:8192\n\t"
        "s_add_u32 m0, s37, -12288\n\t"
        "s_add_u32 s36, s36, s39\n\t"
        "s_waitcnt lgkmcnt(7)\n\t"
        "v_mfma_f32_16x16x32_bf16 v[12:15], a[0:3], v[84:87], 0\n\t"
        "buffer_load_dwordx4 v140, s[24:27], s36 offen offset:0 lds\n\t"
        "buffer_load_dwordx4 v141, s[24:27], s36 offen offset:1024 lds\n\t"
        "buffer_load_dwordx4 v142, s[24:27], s36 offen offset:2048 lds\n\t"
        "s_add_u32 m0, s37, 0\n\t"
        "s_add_u32 s36, s36, s38\n\t"
        "buffer_load_dwordx4 v140, s[24:27], s36 offen offset:0 lds\n\t"
        "buffer_load_dwordx4 v141, s[24:27], s36 offen offset:1024 lds\n\t"
        "buffer_load_dwordx4 v142, s[24:27], s36 offen offset:2048 lds\n\t"
        "s_add_u32 m0, s37, 12288\n\t"
        "s_add_u32 s36, s36, s5\n\t"
        "buffer_load_dwordx4 v140, s[24:27], s36 offen offset:0 lds\n\t"
        "s_waitcnt vmcnt(10)\n\t"
        "s_barrier\n\t"
        "ds_read_b128 v[92:95], v128 offset:4096\n\t"
        "ds_read_b64_tr_b16 a[24:25], v138 offset:0\n\t"
        "v_mfma_f32_16x16x32_bf16 v[16:19], a[4:7], v[84:87], 0\n\t"
        "buffer_load_dwordx4 v141, s[24:27], s36 offen offset:1024 lds\n\t"
        "ds_read_b64_tr_b16 a[26:27], v138 offset:768\n\t"
        "v_mfma_f32_16x16x32_bf16 v[20:23], a[8:11], v[84:87], 0\n\t"
        "ds_read_b64_tr_b16 a[28:29], v139 offset:0\n\t"
        "ds_read_b64_tr_b16 a[30:31], v139 offset:768\n\t"
        "s_waitcnt lgkmcnt(5)\n\t"
        "v_mfma_f32_16x16x32_bf16 v[24:27], a[12:15], v[88:91], 0\n\t"
        "buffer_load_dwordx4 v142, s[24:27], s36 offen offset:2048 lds\n\t"
        "s_add_u32 m0, s37, 24576\n\t"
        "ds_read_b64_tr_b16 a[32:33], v149 offset:0\n\t"
        "s_add_u32 s36, s36, s39\n\t"
        "ds_read_b64_tr_b16 a[34:35], v149 offset:768\n\t"
        "v_mfma_f32_16x16x32_bf16 v[28:31], a[16:19], v[88:91], 0\n\t"
        "ds_read_b128 v[96:99], v128 offset:8192\n\t"
        "v_mfma_f32_16x16x32_bf16 v[32:35], a[20:23], v[88:91], 0\n\t"
        "ds_read_b128 v[100:103], v128 offset:12288\n\t"
        "s_waitcnt lgkmcnt(8)\n\t"
        "v_mfma_f32_16x16x32_bf16 v[0:3], a[0:3], v[92:95], 0\n\t"
        "buffer_load_dwordx4 v140, s[24:27], s36 offen offset:0 lds\n\t"
        "v_mfma_f32_16x16x32_bf16 v[4:7], a[4:7], v[92:95], 0\n\t"
        "v_mfma_f32_16x16x32_bf16 v[8:11], a[8:11], v[92:95], 0\n\t"
        "v_mfma_f32_16x16x32_bf16 v[12:15], a[12:15], v[92:95], v[12:15]\n\t"
        "v_mfma_f32_16x16x32_bf16 v[16:19], a[16:19], v[92:95], v[16:19]\n\t"
        "buffer_load_dwordx4 v141, s[24:27], s36 offen offset:1024 lds\n\t"
        "v_mfma_f32_16x16x32_bf16 v[20:23], a[20:23], v[92:95], v[20:23]\n\t"
        "s_waitcnt lgkmcnt(1)\n\t"
        "v_mfma_f32_16x16x32_bf16 v[0:3], a[24:27], v[96:99], v[0:3]\n\t"
        "v_mfma_f32_16x16x32_bf16 v[4:7], a[28:31], v[96:99], v[4:7]\n\t"
        "v_mfma_f32_16x16x32_bf16 v[8:11], a[32:35], v[96:99], v[8:11]\n\t"
        "buffer_load_dwordx4 v142, s[24:27], s36 offen offset:2048 lds\n\t"
        "s_add_u32 m0, s35, 16384\n\t"
        "s_add_u32 s43, s34, 172032\n\t"
        "s_waitcnt lgkmcnt(0)\n\t"
        "v_mfma_f32_16x16x32_bf16 v[12:15], a[24:27], v[100:103], v[12:15]\n\t"
        "v_mfma_f32_16x16x32_bf16 v[16:19], a[28:31], v[100:103], v[16:19]\n\t"
        "s_waitcnt vmcnt(9)\n\t"
        "s_barrier\n\t"
        "ds_read_b64_tr_b16 a[36:37], v138 offset:18432\n\t"
        "ds_read_b64_tr_b16 a[38:39], v138 offset:19200\n\t"
        "ds_read_b64_tr_b16 a[40:41], v139 offset:18432\n\t"
        "v_mfma_f32_16x16x32_bf16 v[20:23], a[32:35], v[100:103], v[20:23]\n\t"
        "buffer_load_dwordx4 v143, s[16:19], s43 offen  lds\n\t"
        "s_add_u32 m0, s35, 32768\n\t"
        "ds_read_b64_tr_b16 a[42:43], v139 offset:19200\n\t"
        "s_add_u32 s43, s34, 344064\n\t"
        "ds_read_b64_tr_b16 a[44:45], v149 offset:18432\n\t"
        "s_min_u32 s43, s43, 9590784\n\t"
        "ds_read_b64_tr_b16 a[46:47], v149 offset:19200\n\t"
        "ds_read_b64_tr_b16 a[48:49], v138 offset:24576\n\t"
        "ds_read_b64_tr_b16 a[50:51], v138 offset:25344\n\t"
        "ds_read_b64_tr_b16 a[52:53], v139 offset:24576\n\t"
        "ds_read_b64_tr_b16 a[54:55], v139 offset:25344\n\t"
        "ds_read_b64_tr_b16 a[56:57], v149 offset:24576\n\t"
        "ds_read_b64_tr_b16 a[58:59], v149 offset:25344\n\t"
        "ds_read_b128 v[104:107], v128 offset:4160\n\t"
        "v_mfma_f32_16x16x32_bf16 v[24:27], a[0:3], v[100:103], v[24:27]\n\t"
        "buffer_load_dwordx4 v143, s[16:19], s43 offen  lds\n\t"
        "s_add_u32 m0, s35, 157696\n\t"
        "ds_read_b64_tr_b16 a[60:61], v138 offset:30720\n\t"
        "v_mfma_f32_16x16x32_bf16 v[28:31], a[4:7], v[100:103], v[28:31]\n\t"
        "buffer_load_dwordx4 v64, s[20:23], 0 offen lds\n\t"
        "s_add_u32 m0, s35, 0\n\t"
        "ds_read_b64_tr_b16 a[62:63], v138 offset:31488\n\t"
        "s_add_u32 s43, s34, 0\n\t"
        "v_mfma_f32_16x16x32_bf16 v[32:35], a[8:11], v[100:103], v[32:35]\n\t"
        "s_waitcnt vmcnt(9)\n\t"
        "s_barrier\n\t"
        "ds_read_b64_tr_b16 a[64:65], v139 offset:30720\n\t"
        "ds_read_b64_tr_b16 a[66:67], v139 offset:31488\n\t"
        "v_mfma_f32_16x16x32_bf16 v[36:39], a[12:15], v[100:103], 0\n\t"
        "ds_read_b64_tr_b16 a[68:69], v149 offset:30720\n\t"
        "ds_read_b64_tr_b16 a[70:71], v149 offset:31488\n\t"
        "v_mfma_f32_16x16x32_bf16 v[40:43], a[16:19], v[100:103], 0\n\t"
        "buffer_load_dwordx4 v143, s[16:19], s43 offen  lds\n\t"
        "s_add_u32 m0, s35, 36864\n\t"
        "ds_read_b128 v[108:111], v128 offset:8256\n\t"
        "s_add_u32 s43, s34, 387072\n\t"
        "s_min_u32 s43, s43, 9590784\n\t"
        "v_mfma_f32_16x16x32_bf16 v[44:47], a[20:23], v[100:103], 0\n\t"
        "ds_read_b128 v[112:115], v128 offset:12352\n\t"
        "s_waitcnt lgkmcnt(8)\n\t"
        "v_mfma_f32_16x16x32_bf16 v[0:3], a[36:39], v[104:107], v[0:3]\n\t"
        "ds_read_b64_tr_b16 a[72:73], v138 offset:36864\n\t"
        "ds_read_b64_tr_b16 a[74:75], v138 offset:37632\n\t"
        "v_mfma_f32_16x16x32_bf16 v[4:7], a[40:43], v[104:107], v[4:7]\n\t"
        "buffer_load_dwordx4 v143, s[16:19], s43 offen  lds\n\t"
        "ds_read_b64_tr_b16 a[76:77], v139 offset:36864\n\t"
        "v_mfma_f32_16x16x32_bf16 v[8:11], a[44:47], v[104:107], v[8:11]\n\t"
        "ds_read_b64_tr_b16 a[78:79], v139 offset:37632\n\t"
        "ds_read_b64_tr_b16 a[80:81], v149 offset:36864\n\t"
        "v_mfma_f32_16x16x32_bf16 v[12:15], a[48:51], v[104:107], v[12:15]\n\t"
        "ds_read_b64_tr_b16 a[82:83], v149 offset:37632\n\t"
        "v_mfma_f32_16x16x32_bf16 v[16:19], a[52:55], v[104:107], v[16:19]\n\t"
        "buffer_load_dwordx4 v[48:51], v55, s[20:23], s48 offen\n\t"
        "v_mfma_f32_16x16x32_bf16 v[20:23], a[56:59], v[104:107], v[20:23]\n\t"
        "ds_read_b64_tr_b16 a[84:85], v131 offset:0\n\t"
        "s_waitcnt lgkmcnt(8)\n\t"
        "v_mfma_f32_16x16x32_bf16 v[0:3], a[60:63], v[108:111], v[0:3]\n\t"
        "ds_read_b64_tr_b16 a[86:87], v131 offset:768\n\t"
        "v_mfma_f32_16x16x32_bf16 v[4:7], a[64:67], v[108:111], v[4:7]\n\t"
        "ds_read_b64_tr_b16 a[88:89], v132 offset:0\n\t"
        "v_mfma_f32_16x16x32_bf16 v[8:11], a[68:71], v[108:111], v[8:11]\n\t"
        "buffer_load_dword v61, v61, s[20:23], s48 offen offset:512\n\t"
        "s_add_u32 m0, s37, 36864\n\t"
        "s_add_u32 s36, s36, s38\n\t"
        "v_mfma_f32_16x16x32_bf16 v[12:15], a[36:39], v[108:111], v[12:15]\n\t"
        "ds_read_b64_tr_b16 a[90:91], v132 offset:768\n\t"
        "ds_read_b64_tr_b16 a[92:93], v133 offset:0\n\t"
        "v_mfma_f32_16x16x32_bf16 v[16:19], a[40:43], v[108:111], v[16:19]\n\t"
        "ds_read_b64_tr_b16 a[94:95], v133 offset:768\n\t"
        "v_mfma_f32_16x16x32_bf16 v[20:23], a[44:47], v[108:111], v[20:23]\n\t"
        "ds_read_b128 v[84:87], v128 offset:4224\n\t"
        "v_mfma_f32_16x16x32_bf16 v[24:27], a[48:51], v[108:111], v[24:27]\n\t"
        "buffer_load_dwordx4 v140, s[24:27], s36 offen offset:0 lds\n\t"
        "v_mfma_f32_16x16x32_bf16 v[28:31], a[52:55], v[108:111], v[28:31]\n\t"
        "ds_read_b64_tr_b16 a[96:97], v131 offset:6144\n\t"
        "v_mfma_f32_16x16x32_bf16 v[32:35], a[56:59], v[108:111], v[32:35]\n\t"
        "ds_read_b64_tr_b16 a[98:99], v131 offset:6912\n\t"
        "s_waitcnt lgkmcnt(8)\n\t"
        "v_mfma_f32_16x16x32_bf16 v[12:15], a[60:63], v[112:115], v[12:15]\n\t"
        "ds_read_b64_tr_b16 a[100:101], v132 offset:6144\n\t"
        "v_mfma_f32_16x16x32_bf16 v[16:19], a[64:67], v[112:115], v[16:19]\n\t"
        "buffer_load_dwordx4 v141, s[24:27], s36 offen offset:1024 lds\n\t"
        "ds_read_b64_tr_b16 a[102:103], v132 offset:6912\n\t"
        "v_mfma_f32_16x16x32_bf16 v[20:23], a[68:71], v[112:115], v[20:23]\n\t"
        "s_waitcnt vmcnt(9)\n\t"
        "s_barrier\n\t"
        "ds_read_b64_tr_b16 a[104:105], v133 offset:6144\n\t"
        "ds_read_b64_tr_b16 a[106:107], v133 offset:6912\n\t"
        "v_mfma_f32_16x16x32_bf16 v[24:27], a[36:39], v[112:115], v[24:27]\n\t"
        "ds_read_b128 v[88:91], v128 offset:8320\n\t"
        "ds_read_b128 v[92:95], v128 offset:12416\n\t"
        "v_mfma_f32_16x16x32_bf16 v[28:31], a[40:43], v[112:115], v[28:31]\n\t"
        "ds_read_b64_tr_b16 a[108:109], v131 offset:12288\n\t"
        "ds_read_b64_tr_b16 a[110:111], v131 offset:13056\n\t"
        "v_mfma_f32_16x16x32_bf16 v[32:35], a[44:47], v[112:115], v[32:35]\n\t"
        "buffer_load_dwordx4 v142, s[24:27], s36 offen offset:2048 lds\n\t"
        "s_add_u32 m0, s37, 49152\n\t"
        "ds_read_b64_tr_b16 a[112:113], v132 offset:12288\n\t"
        "v_mfma_f32_16x16x32_bf16 v[36:39], a[48:51], v[112:115], v[36:39]\n\t"
        "ds_read_b64_tr_b16 a[114:115], v132 offset:13056\n\t"
        "s_add_u32 s36, s36, s5\n\t"
        "ds_read_b64_tr_b16 a[116:117], v133 offset:12288\n\t"
        "v_mfma_f32_16x16x32_bf16 v[40:43], a[52:55], v[112:115], v[40:43]\n\t"
        "ds_read_b64_tr_b16 a[118:119], v133 offset:13056\n\t"
        "ds_read_b64_tr_b16 a[120:121], v131 offset:18432\n\t"
        "v_mfma_f32_16x16x32_bf16 v[44:47], a[56:59], v[112:115], v[44:47]\n\t"
        "ds_read_b64_tr_b16 a[122:123], v131 offset:19200\n\t"
        "ds_read_b64_tr_b16 a[124:125], v132 offset:18432\n\t"
        "s_waitcnt lgkmcnt(14)\n\t"
        "v_mfma_f32_16x16x32_bf16 v[0:3], a[72:75], v[84:87], v[0:3]\n\t"
        "buffer_load_dwordx4 v140, s[24:27], s36 offen offset:0 lds\n\t"
        "ds_read_b64_tr_b16 a[126:127], v132 offset:19200\n\t"
        "v_mfma_f32_16x16x32_bf16 v[4:7], a[76:79], v[84:87], v[4:7]\n\t"
        "ds_read_b64_tr_b16 a[128:129], v133 offset:18432\n\t"
        "ds_read_b64_tr_b16 a[130:131], v133 offset:19200\n\t"
        "v_mfma_f32_16x16x32_bf16 v[8:11], a[80:83], v[84:87], v[8:11]\n\t"
        "ds_read_b64_tr_b16 a[132:133], v131 offset:24576\n\t"
        "ds_read_b64_tr_b16 a[134:135], v131 offset:25344\n\t"
        "v_mfma_f32_16x16x32_bf16 v[12:15], a[84:87], v[84:87], v[12:15]\n\t"
        "ds_read_b128 v[96:99], v129 offset:4096\n\t"
        "ds_read_b64_tr_b16 a[136:137], v132 offset:24576\n\t"
        "v_mfma_f32_16x16x32_bf16 v[16:19], a[88:91], v[84:87], v[16:19]\n\t"
        "buffer_load_dwordx4 v141, s[24:27], s36 offen offset:1024 lds\n\t"
        "ds_read_b64_tr_b16 a[138:139], v132 offset:25344\n\t"
        "v_mfma_f32_16x16x32_bf16 v[20:23], a[92:95], v[84:87], v[20:23]\n\t"
        "ds_read_b64_tr_b16 a[140:141], v133 offset:24576\n\t"
        "ds_read_b64_tr_b16 a[142:143], v133 offset:25344\n\t"
        "s_waitcnt lgkmcnt(14)\n\t"
        "v_mfma_f32_16x16x32_bf16 v[0:3], a[96:99], v[88:91], v[0:3]\n\t"
        "ds_read_b64_tr_b16 a[144:145], v131 offset:30720\n\t"
        "ds_read_b64_tr_b16 a[146:147], v131 offset:31488\n\t"
        "v_mfma_f32_16x16x32_bf16 v[4:7], a[100:103], v[88:91], v[4:7]\n\t"
        "ds_read_b64_tr_b16 a[148:149], v132 offset:30720\n\t"
        "ds_read_b64_tr_b16 a[150:151], v132 offset:31488\n\t"
        "v_mfma_f32_16x16x32_bf16 v[8:11], a[104:107], v[88:91], v[8:11]\n\t"
        "ds_read_b64_tr_b16 a[152:153], v133 offset:30720\n\t"
        "ds_read_b64_tr_b16 a[154:155], v133 offset:31488\n\t"
        "v_mfma_f32_16x16x32_bf16 v[12:15], a[72:75], v[88:91], v[12:15]\n\t"
        "ds_read_b128 v[100:103], v129 offset:8192\n\t"
        "v_mfma_f32_16x16x32_bf16 v[16:19], a[76:79], v[88:91], v[16:19]\n\t"
        "v_mfma_f32_16x16x32_bf16 v[20:23], a[80:83], v[88:91], v[20:23]\n\t"
        "v_mfma_f32_16x16x32_bf16 v[24:27], a[84:87], v[88:91], v[24:27]\n\t"
        "v_mfma_f32_16x16x32_bf16 v[28:31], a[88:91], v[88:91], v[28:31]\n\t"
        "v_mfma_f32_16x16x32_bf16 v[32:35], a[92:95], v[88:91], v[32:35]\n\t"
        "v_mfma_f32_16x16x32_bf16 v[12:15], a[96:99], v[92:95], v[12:15]\n\t"
        "v_mfma_f32_16x16x32_bf16 v[16:19], a[100:103], v[92:95], v[16:19]\n\t"
        "v_mfma_f32_16x16x32_bf16 v[20:23], a[104:107], v[92:95], v[20:23]\n\t"
        "v_mfma_f32_16x16x32_bf16 v[24:27], a[72:75], v[92:95], v[24:27]\n\t"
        "ds_read_b128 v[104:107], v129 offset:12288\n\t"
        "v_mfma_f32_16x16x32_bf16 v[28:31], a[76:79], v[92:95], v[28:31]\n\t"
        "v_mfma_f32_16x16x32_bf16 v[32:35], a[80:83], v[92:95], v[32:35]\n\t"
        "v_mfma_f32_16x16x32_bf16 v[36:39], a[84:87], v[92:95], v[36:39]\n\t"
        "v_mfma_f32_16x16x32_bf16 v[40:43], a[88:91], v[92:95], v[40:43]\n\t"
        "v_mfma_f32_16x16x32_bf16 v[44:47], a[92:95], v[92:95], v[44:47]\n\t"
        "s_waitcnt lgkmcnt(1)\n\t"
        "v_mfma_f32_16x16x32_bf16 v[0:3], a[108:111], v[96:99], v[0:3]\n\t"
        "v_mfma_f32_16x16x32_bf16 v[4:7], a[112:115], v[96:99], v[4:7]\n\t"
        "v_mfma_f32_16x16x32_bf16 v[8:11], a[116:119], v[96:99], v[8:11]\n\t"
        "v_mfma_f32_16x16x32_bf16 v[12:15], a[120:123], v[96:99], v[12:15]\n\t"
        "v_mfma_f32_16x16x32_bf16 v[16:19], a[124:127], v[96:99], v[16:19]\n\t"
        "buffer_load_dwordx4 v142, s[24:27], s36 offen offset:2048 lds\n\t"
        "s_add_u32 m0, s37, 0\n\t"
        "s_add_u32 s36, s36, s39\n\t"
        "v_mfma_f32_16x16x32_bf16 v[20:23], a[128:131], v[96:99], v[20:23]\n\t"
        "v_mfma_f32_16x16x32_bf16 v[0:3], a[132:135], v[100:103], v[0:3]\n\t"
        "v_mfma_f32_16x16x32_bf16 v[4:7], a[136:139], v[100:103], v[4:7]\n\t"
        "v_mfma_f32_16x16x32_bf16 v[8:11], a[140:143], v[100:103], v[8:11]\n\t"
        "v_mfma_f32_16x16x32_bf16 v[12:15], a[108:111], v[100:103], v[12:15]\n\t"
        "v_mfma_f32_16x16x32_bf16 v[16:19], a[112:115], v[100:103], v[16:19]\n\t"
        "v_mfma_f32_16x16x32_bf16 v[20:23], a[116:119], v[100:103], v[20:23]\n\t"
        "v_mfma_f32_16x16x32_bf16 v[24:27], a[120:123], v[100:103], v[24:27]\n\t"
        "v_mfma_f32_16x16x32_bf16 v[28:31], a[124:127], v[100:103], v[28:31]\n\t"
        "v_mfma_f32_16x16x32_bf16 v[32:35], a[128:131], v[100:103], v[32:35]\n\t"
        "s_waitcnt lgkmcnt(0)\n\t"
        "v_mfma_f32_16x16x32_bf16 v[12:15], a[132:135], v[104:107], v[12:15]\n\t"
        "v_mfma_f32_16x16x32_bf16 v[16:19], a[136:139], v[104:107], v[16:19]\n\t"
        "v_mfma_f32_16x16x32_bf16 v[20:23], a[140:143], v[104:107], v[20:23]\n\t"
        "v_mfma_f32_16x16x32_bf16 v[24:27], a[108:111], v[104:107], v[24:27]\n\t"
        "ds_read_b128 v[108:111], v129 offset:4160\n\t"
        "v_mfma_f32_16x16x32_bf16 v[28:31], a[112:115], v[104:107], v[28:31]\n\t"
        "v_mfma_f32_16x16x32_bf16 v[32:35], a[116:119], v[104:107], v[32:35]\n\t"
        "v_mfma_f32_16x16x32_bf16 v[36:39], a[120:123], v[104:107], v[36:39]\n\t"
        "ds_read_b128 v[112:115], v129 offset:8256\n\t"
        "v_mfma_f32_16x16x32_bf16 v[40:43], a[124:127], v[104:107], v[40:43]\n\t"
        "v_mfma_f32_16x16x32_bf16 v[44:47], a[128:131], v[104:107], v[44:47]\n\t"
        "s_waitcnt lgkmcnt(1)\n\t"
        "v_mfma_f32_16x16x32_bf16 v[0:3], a[144:147], v[108:111], v[0:3]\n\t"
        "ds_read_b128 v[84:87], v129 offset:12352\n\t"
        "v_mfma_f32_16x16x32_bf16 v[4:7], a[148:151], v[108:111], v[4:7]\n\t"
        "v_mfma_f32_16x16x32_bf16 v[8:11], a[152:155], v[108:111], v[8:11]\n\t"
        "s_waitcnt vmcnt(3)\n\t"
        "s_barrier\n\t"
        ".LpS0%=:\n\t"
        "s_cbranch_execz .LrS%=\n\t"
        "buffer_load_dwordx4 v140, s[24:27], s36 offen offset:0 lds\n\t"
        "ds_read_b128 v[88:91], v128 offset:0\n\t"
        "s_waitcnt lgkmcnt(2)\n\t"
        "v_mfma_f32_16x16x32_bf16 v[12:15], a[144:147], v[112:115], v[12:15]\n\t"
        "ds_read_b128 v[92:95], v128 offset:64\n\t"
        "ds_read_b128 v[96:99], v128 offset:128\n\t"
        "v_readlane_b32 s46, v155, 37\n\t"
        "v_readlane_b32 s47, v155, 40\n\t"
        "v_mfma_f32_16x16x32_bf16 v[16:19], a[148:151], v[112:115], v[16:19]\n\t"
        "ds_read_b128 v[100:103], v129 offset:0\n\t"
        "ds_read_b128 v[104:107], v128 offset:16384\n\t"
        "v_and_b32 v48, s54, v48\n\t"
        "v_pk_max_u16 v60, v154, v48\n\t"
        "v_mfma_f32_16x16x32_bf16 v[20:23], a[152:155], v[112:115], v[20:23]\n\t"
        "ds_read_b32 v64, v70 offset:0\n\t"
        "ds_read_b64_tr_b16 a[156:157], v131 offset:36864\n\t"
        "v_and_b32 v49, s54, v49\n\t"
        "s_waitcnt lgkmcnt(7)\n\t"
        "v_mfma_f32_16x16x32_bf16 v[24:27], a[144:147], v[84:87], v[24:27]\n\t"
        "ds_read_b32 v67, v67 offset:0\n\t"
        "buffer_load_dwordx4 v141, s[24:27], s36 offen offset:1024 lds\n\t"
        "v_pk_max_u16 v60, v60, v49\n\t"
        "v_mfma_f32_16x16x32_bf16 v[28:31], a[148:151], v[84:87], v[28:31]\n\t"
        "ds_read_b32 v65, v70 offset:1280\n\t"
        "ds_read_b128 v[108:111], v128 offset:16448\n\t"
        "v_and_b32 v50, s54, v50\n\t"
        "v_mfma_f32_16x16x32_bf16 v[32:35], a[152:155], v[84:87], v[32:35]\n\t"
        "ds_read_b32 v66, v70 offset:2560\n\t"
        "ds_read_b64_tr_b16 a[158:159], v131 offset:37632\n\t"
        "v_pk_max_u16 v60, v60, v50\n\t"
        "v_and_b32 v51, s54, v51\n\t"
        "s_waitcnt lgkmcnt(11)\n\t"
        "v_mfma_f32_16x16x32_bf16 v[0:3], a[12:15], v[88:91], v[0:3]\n\t"
        "ds_read_b64_tr_b16 a[160:161], v132 offset:36864\n\t"
        "ds_read_b64_tr_b16 a[162:163], v132 offset:37632\n\t"
        "v_pk_max_u16 v60, v60, v51\n\t"
        "v_mfma_f32_16x16x32_bf16 v[4:7], a[16:19], v[88:91], v[4:7]\n\t"
        "buffer_load_dwordx4 v142, s[24:27], s36 offen offset:2048 lds\n\t"
        "s_add_u32 m0, s37, 12288\n\t"
        "ds_read_b64_tr_b16 a[164:165], v133 offset:36864\n\t"
        "v_mfma_f32_16x16x32_bf16 v[8:11], a[20:23], v[88:91], v[8:11]\n\t"
        "ds_read_b64_tr_b16 a[166:167], v133 offset:37632\n\t"
        "s_add_u32 s36, s36, s38\n\t"
        "s_waitcnt lgkmcnt(11)\n\t"
        "v_mfma_f32_16x16x32_bf16 v[0:3], a[48:51], v[92:95], v[0:3]\n\t"
        "ds_read_b128 v[112:115], v129 offset:64\n\t"
        "ds_read_b64_tr_b16 a[168:169], v131 offset:43008\n\t"
        "v_and_b32 v61, s54, v61\n\t"
        "v_mfma_f32_16x16x32_bf16 v[4:7], a[52:55], v[92:95], v[4:7]\n\t"
        "ds_read_b64_tr_b16 a[170:171], v131 offset:43776\n\t"
        "ds_read_b64_tr_b16 a[172:173], v132 offset:43008\n\t"
        "v_pk_max_u16 v60, v60, v61\n\t"
        "v_cmp_ne_u32 s[12:13], v60, v154\n\t"
        "v_mfma_f32_16x16x32_bf16 v[8:11], a[56:59], v[92:95], v[8:11]\n\t"
        "buffer_load_dwordx4 v140, s[24:27], s36 offen offset:0 lds\n\t"
        "ds_read_b64_tr_b16 a[174:175], v132 offset:43776\n\t"
        "s_lshl_b32 s6, 256, s4\n\t"
        "v_mfma_f32_16x16x32_bf16 v[0:3], a[84:87], v[96:99], v[0:3]\n\t"
        "s_cmp_lg_u64 s[12:13], 0\n\t"
        "s_cselect_b32 s6, s6, 0\n\t"
        "ds_read_b128 v[84:87], v129 offset:4160\n\t"
        "ds_read_b64_tr_b16 a[176:177], v133 offset:43008\n\t"
        "v_mfma_f32_16x16x32_bf16 v[4:7], a[88:91], v[96:99], v[4:7]\n\t"
        "ds_read_b64_tr_b16 a[178:179], v133 offset:43776\n\t"
        "s_waitcnt lgkmcnt(10)\n\t"
        "v_perm_b32 v56, v64, v64, s44\n\t"
        "v_mfma_f32_16x16x32_bf16 v[8:11], a[92:95], v[96:99], v[8:11]\n\t"
        "v_perm_b32 v57, v64, v64, s45\n\t"
        "v_add_f32 v58, s46, v68\n\t"
        "v_mfma_f32_16x16x32_bf16 v[0:3], a[120:123], v[100:103], v[0:3]\n\t"
        "buffer_load_dwordx4 v141, s[24:27], s36 offen offset:1024 lds\n\t"
        "v_add_f32 v59, s47, v69\n\t"
        "v_mfma_f32_16x16x32_bf16 v[4:7], a[124:127], v[100:103], v[4:7]\n\t"
        "ds_read_b128 v[88:91], v129 offset:8256\n\t"
        "v_add_f32 v56, v56, v58\n\t"
        "v_add_f32 v57, v57, v59\n\t"
        "v_mfma_f32_16x16x32_bf16 v[8:11], a[128:131], v[100:103], v[8:11]\n\t"
        "v_cvt_pk_bf16_f32 v52, v56, v57\n\t"
        "v_cvt_pk_bf16_f32 v58, v58, v59\n\t"
        "v_mfma_f32_16x16x32_bf16 v[24:27], a[24:27], v[104:107], v[24:27]\n\t"
        "v_cmp_ne_u32 s[64:65], v52, v58\n\t"
        "v_perm_b32 v57, v67, v67, s45\n\t"
        "v_mfma_f32_16x16x32_bf16 v[28:31], a[28:31], v[104:107], v[28:31]\n\t"
        "buffer_load_dwordx4 v142, s[24:27], s36 offen offset:2048 lds\n\t"
        "s_add_u32 m0, s37, 24576\n\t"
        "v_mfma_f32_16x16x32_bf16 v[32:35], a[32:35], v[104:107], v[32:35]\n\t"
        "s_add_u32 s36, s36, s5\n\t"
        "v_mbcnt_lo_u32_b32 v56, s64, 1\n\t"
        "v_mfma_f32_16x16x32_bf16 v[36:39], a[0:3], v[104:107], v[36:39]\n\t"
        "ds_read_b128 v[92:95], v129 offset:12352\n\t"
        "v_mbcnt_hi_u32_b32 v56, s65, v56\n\t"
        "s_bcnt1_i32_b64 s61, s[64:65]\n\t"
        "v_mfma_f32_16x16x32_bf16 v[40:43], a[4:7], v[104:107], v[40:43]\n\t"
        "v_add_u32 v54, s61, 1\n\t"
        "v_lshl_add_u32 v56, v56, 3, s60\n\t"
        "v_mfma_f32_16x16x32_bf16 v[44:47], a[8:11], v[104:107], v[44:47]\n\t"
        "s_mov_b64 exec, s[64:65]\n\t"
        "ds_write_b64 v56, v[52:53]\n\t"
        "s_mov_b64 exec, -1\n\t"
        "v_mfma_f32_16x16x32_bf16 v[24:27], a[60:63], v[108:111], v[24:27]\n\t"
        "buffer_load_dwordx4 v140, s[24:27], s36 offen offset:0 lds\n\t"
        "v_swap_b32 v53, v80\n\t"
        "v_mfma_f32_16x16x32_bf16 v[28:31], a[64:67], v[108:111], v[28:31]\n\t"
        "v_perm_b32 v56, v67, v67, s44\n\t"
        "v_add_f32 v56, v56, v78\n\t"
        "v_mfma_f32_16x16x32_bf16 v[32:35], a[68:71], v[108:111], v[32:35]\n\t"
        "v_add_f32 v57, v57, v79\n\t"
        "v_cvt_pk_bf16_f32 v52, v56, v57\n\t"
        "v_mfma_f32_16x16x32_bf16 v[36:39], a[36:39], v[108:111], v[36:39]\n\t"
        "ds_read_b128 v[96:99], v128 offset:16512\n\t"
        "v_cvt_pk_bf16_f32 v58, v78, v79\n\t"
        "v_mfma_f32_16x16x32_bf16 v[40:43], a[40:43], v[108:111], v[40:43]\n\t"
        "buffer_load_dwordx4 v141, s[24:27], s36 offen offset:1024 lds\n\t"
        "v_cmp_ne_u32 s[64:65], v52, v58\n\t"
        "v_readlane_b32 s47, v155, 41\n\t"
        "v_mfma_f32_16x16x32_bf16 v[44:47], a[44:47], v[108:111], v[44:47]\n\t"
        "s_waitcnt vmcnt(5) lgkmcnt(3)\n\t"
        "s_barrier\n\t"
        "ds_read_b64_tr_b16 a[180:181], v131 offset:49152\n\t"
        "ds_read_b64_tr_b16 a[182:183], v131 offset:49920\n\t"
        "v_mfma_f32_16x16x32_bf16 v[0:3], a[156:159], v[112:115], v[0:3]\n\t"
        "ds_read_b64_tr_b16 a[184:185], v132 offset:49152\n\t"
        "ds_read_b64_tr_b16 a[186:187], v132 offset:49920\n\t"
        "s_and_b64 s[64:65], s[64:65], 0xffffff\n\t"
        "v_mfma_f32_16x16x32_bf16 v[4:7], a[160:163], v[112:115], v[4:7]\n\t"
        "ds_read_b64_tr_b16 a[188:189], v133 offset:49152\n\t"
        "ds_read_b64_tr_b16 a[190:191], v133 offset:49920\n\t"
        "v_mbcnt_lo_u32_b32 v56, s64, v54\n\t"
        "v_mfma_f32_16x16x32_bf16 v[8:11], a[164:167], v[112:115], v[8:11]\n\t"
        "buffer_load_dwordx4 v142, s[24:27], s36 offen offset:2048 lds\n\t"
        "s_add_u32 m0, s37, 36864\n\t"
        "v_mfma_f32_16x16x32_bf16 v[12:15], a[156:159], v[84:87], v[12:15]\n\t"
        "ds_read_b128 v[100:103], v129 offset:16384\n\t"
        "s_add_u32 s36, s36, s39\n\t"
        "ds_read_b64_tr_b16 a[192:193], v131 offset:55296\n\t"
        "v_mfma_f32_16x16x32_bf16 v[16:19], a[160:163], v[84:87], v[16:19]\n\t"
        "ds_read_b64_tr_b16 a[194:195], v131 offset:56064\n\t"
        "ds_read_b64_tr_b16 a[196:197], v132 offset:55296\n\t"
        "s_bcnt1_i32_b64 s46, s[64:65]\n\t"
        "v_mfma_f32_16x16x32_bf16 v[20:23], a[164:167], v[84:87], v[20:23]\n\t"
        "ds_read_b64_tr_b16 a[198:199], v132 offset:56064\n\t"
        "ds_read_b64_tr_b16 a[200:201], v133 offset:55296\n\t"
        "s_add_u32 s61, s61, s46\n\t"
        "v_add_u32 v54, s46, v54\n\t"
        "v_mfma_f32_16x16x32_bf16 v[0:3], a[168:171], v[88:91], v[0:3]\n\t"
        "buffer_load_dwordx4 v140, s[24:27], s36 offen offset:0 lds\n\t"
        "ds_read_b64_tr_b16 a[202:203], v133 offset:56064\n\t"
        "v_lshl_add_u32 v56, v56, 3, s60\n\t"
        "v_mfma_f32_16x16x32_bf16 v[4:7], a[172:175], v[88:91], v[4:7]\n\t"
        "s_mov_b64 exec, s[64:65]\n\t"
        "ds_write_b64 v56, v[52:53]\n\t"
        "s_mov_b64 exec, -1\n\t"
        "v_mfma_f32_16x16x32_bf16 v[8:11], a[176:179], v[88:91], v[8:11]\n\t"
        "ds_read_b64_tr_b16 a[204:205], v131 offset:0\n\t"
        "v_add_u32 v53, 2048, v80\n\t"
        "v_mfma_f32_16x16x32_bf16 v[24:27], a[156:159], v[88:91], v[24:27]\n\t"
        "ds_read_b128 v[104:107], v129 offset:128\n\t"
        "v_readlane_b32 s46, v155, 38\n\t"
        "v_mfma_f32_16x16x32_bf16 v[28:31], a[160:163], v[88:91], v[28:31]\n\t"
        "buffer_load_dwordx4 v141, s[24:27], s36 offen offset:1024 lds\n\t"
        "v_perm_b32 v56, v65, v65, s44\n\t"
        "v_mfma_f32_16x16x32_bf16 v[32:35], a[164:167], v[88:91], v[32:35]\n\t"
        "ds_read_b64_tr_b16 a[206:207], v131 offset:768\n\t"
        "v_perm_b32 v57, v65, v65, s45\n\t"
        "s_waitcnt lgkmcnt(8)\n\t"
        "v_mfma_f32_16x16x32_bf16 v[12:15], a[168:171], v[92:95], v[12:15]\n\t"
        "ds_read_b128 v[108:111], v129 offset:4224\n\t"
        "v_add_f32 v58, s46, v68\n\t"
        "v_mfma_f32_16x16x32_bf16 v[16:19], a[172:175], v[92:95], v[16:19]\n\t"
        "ds_read_b64_tr_b16 a[208:209], v132 offset:0\n\t"
        "v_add_f32 v59, s47, v69\n\t"
        "v_add_f32 v56, v56, v58\n\t"
        "v_mfma_f32_16x16x32_bf16 v[20:23], a[176:179], v[92:95], v[20:23]\n\t"
        "buffer_load_dwordx4 v142, s[24:27], s36 offen offset:2048 lds\n\t"
        "s_add_u32 m0, s37, 49152\n\t"
        "v_mfma_f32_16x16x32_bf16 v[36:39], a[156:159], v[92:95], v[36:39]\n\t"
        "ds_read_b64_tr_b16 a[210:211], v132 offset:768\n\t"
        "s_add_u32 s36, s36, s38\n\t"
        "ds_read_b64_tr_b16 a[212:213], v133 offset:0\n\t"
        "v_mfma_f32_16x16x32_bf16 v[40:43], a[160:163], v[92:95], v[40:43]\n\t"
        "ds_read_b64_tr_b16 a[214:215], v133 offset:768\n\t"
        "v_add_f32 v57, v57, v59\n\t"
        "v_mfma_f32_16x16x32_bf16 v[44:47], a[164:167], v[92:95], v[44:47]\n\t"
        "ds_read_b64_tr_b16 a[216:217], v131 offset:6144\n\t"
        "v_cvt_pk_bf16_f32 v52, v56, v57\n\t"
        "v_mfma_f32_16x16x32_bf16 v[24:27], a[96:99], v[96:99], v[24:27]\n\t"
        "ds_read_b128 v[112:115], v129 offset:8320\n\t"
        "v_cvt_pk_bf16_f32 v58, v58, v59\n\t"
        "v_mfma_f32_16x16x32_bf16 v[28:31], a[100:103], v[96:99], v[28:31]\n\t"
        "ds_read_b64_tr_b16 a[218:219], v131 offset:6912\n\t"
        "v_cmp_ne_u32 s[64:65], v52, v58\n\t"
        "v_mfma_f32_16x16x32_bf16 v[32:35], a[104:107], v[96:99], v[32:35]\n\t"
        "ds_read_b64_tr_b16 a[220:221], v132 offset:6144\n\t"
        "v_mbcnt_lo_u32_b32 v56, s64, v54\n\t"
        "v_mfma_f32_16x16x32_bf16 v[36:39], a[72:75], v[96:99], v[36:39]\n\t"
        "s_waitcnt vmcnt(6) lgkmcnt(9)\n\t"
        "s_barrier\n\t"
        "v_mfma_f32_16x16x32_bf16 v[40:43], a[76:79], v[96:99], v[40:43]\n\t"
        "buffer_load_dwordx4 v140, s[24:27], s36 offen offset:0 lds\n\t"
        "v_mbcnt_hi_u32_b32 v56, s65, v56\n\t"
        "s_bcnt1_i32_b64 s46, s[64:65]\n\t"
        "v_mfma_f32_16x16x32_bf16 v[44:47], a[80:83], v[96:99], v[44:47]\n\t"
        "ds_read_b64_tr_b16 a[222:223], v132 offset:6912\n\t"
        "s_add_u32 s61, s61, s46\n\t"
        "v_mfma_f32_16x16x32_bf16 v[24:27], a[132:135], v[100:103], v[24:27]\n\t"
        "ds_read_b64_tr_b16 a[224:225], v133 offset:6144\n\t"
        "v_add_u32 v54, s46, v54\n\t"
        "v_mfma_f32_16x16x32_bf16 v[28:31], a[136:139], v[100:103], v[28:31]\n\t"
        "ds_read_b64_tr_b16 a[226:227], v133 offset:6912\n\t"
        "v_min_u32 v56, 97, v56\n\t"
        "v_mfma_f32_16x16x32_bf16 v[32:35], a[140:143], v[100:103], v[32:35]\n\t"
        "buffer_load_dwordx4 v141, s[24:27], s36 offen offset:1024 lds\n\t"
        "v_lshl_add_u32 v56, v56, 3, s60\n\t"
        "v_mfma_f32_16x16x32_bf16 v[36:39], a[108:111], v[100:103], v[36:39]\n\t"
        "s_mov_b64 exec, s[64:65]\n\t"
        "ds_write_b64 v56, v[52:53]\n\t"
        "s_mov_b64 exec, -1\n\t"
        "v_mfma_f32_16x16x32_bf16 v[40:43], a[112:115], v[100:103], v[40:43]\n\t"
        "ds_read_b128 v[84:87], v129 offset:12416\n\t"
        "v_add_u32 v53, 2048, v53\n\t"
        "v_mfma_f32_16x16x32_bf16 v[44:47], a[116:119], v[100:103], v[44:47]\n\t"
        "ds_read_b64_tr_b16 a[228:229], v131 offset:12288\n\t"
        "v_readlane_b32 s46, v155, 39\n\t"
        "v_mfma_f32_16x16x32_bf16 v[0:3], a[192:195], v[104:107], v[0:3]\n\t"
        "buffer_load_dwordx4 v142, s[24:27], s36 offen offset:2048 lds\n\t"
        "v_readlane_b32 s40, v155, 12\n\t"
        "v_mfma_f32_16x16x32_bf16 v[4:7], a[196:199], v[104:107], v[4:7]\n\t"
        "ds_read_b64_tr_b16 a[230:231], v131 offset:13056\n\t"
        "v_readlane_b32 s43, v155, 0\n\t"
        "s_add_u32 m0, s40, s35\n\t"
        "v_mfma_f32_16x16x32_bf16 v[8:11], a[200:203], v[104:107], v[8:11]\n\t"
        "ds_read_b64_tr_b16 a[232:233], v132 offset:12288\n\t"
        "v_readlane_b32 s47, v155, 42\n\t"
        "s_waitcnt lgkmcnt(7)\n\t"
        "v_mfma_f32_16x16x32_bf16 v[0:3], a[180:183], v[108:111], v[0:3]\n\t"
        "ds_read_b64_tr_b16 a[234:235], v132 offset:13056\n\t"
        "v_perm_b32 v56, v66, v66, s44\n\t"
        "v_mfma_f32_16x16x32_bf16 v[4:7], a[184:187], v[108:111], v[4:7]\n\t"
        "buffer_load_dwordx4 v134, s[16:19], s43 offen  lds\n\t"
        "v_readlane_b32 s40, v155, 13\n\t"
        "v_mfma_f32_16x16x32_bf16 v[8:11], a[188:191], v[108:111], v[8:11]\n\t"
        "ds_read_b64_tr_b16 a[236:237], v133 offset:12288\n\t"
        "v_readlane_b32 s43, v155, 1\n\t"
        "v_mfma_f32_16x16x32_bf16 v[12:15], a[192:195], v[108:111], v[12:15]\n\t"
        "ds_read_b128 v[88:91], v129 offset:16448\n\t"
        "s_add_u32 m0, s40, s35\n\t"
        "v_mfma_f32_16x16x32_bf16 v[16:19], a[196:199], v[108:111], v[16:19]\n\t"
        "ds_read_b64_tr_b16 a[238:239], v133 offset:13056\n\t"
        "v_perm_b32 v57, v66, v66, s45\n\t"
        "v_mfma_f32_16x16x32_bf16 v[20:23], a[200:203], v[108:111], v[20:23]\n\t"
        "buffer_load_dwordx4 v135, s[16:19], s43 offen  lds\n\t"
        "v_readlane_b32 s40, v155, 14\n\t"
        "v_mfma_f32_16x16x32_bf16 v[0:3], a[204:207], v[112:115], v[0:3]\n\t"
        "ds_read_b64_tr_b16 a[240:241], v131 offset:18432\n\t"
        "v_readlane_b32 s43, v155, 2\n\t"
        "v_mfma_f32_16x16x32_bf16 v[4:7], a[208:211], v[112:115], v[4:7]\n\t"
        "ds_read_b64_tr_b16 a[242:243], v131 offset:19200\n\t"
        "s_add_u32 m0, s40, s35\n\t"
        "v_mfma_f32_16x16x32_bf16 v[8:11], a[212:215], v[112:115], v[8:11]\n\t"
        "ds_read_b64_tr_b16 a[244:245], v132 offset:18432\n\t"
        "v_add_f32 v58, s46, v68\n\t"
        "v_add_f32 v59, s47, v69\n\t"
        "v_mfma_f32_16x16x32_bf16 v[12:15], a[180:183], v[112:115], v[12:15]\n\t"
        "buffer_load_dwordx4 v134, s[16:19], s43 offen  lds\n\t"
        "v_readlane_b32 s40, v155, 15\n\t"
        "v_mfma_f32_16x16x32_bf16 v[16:19], a[184:187], v[112:115], v[16:19]\n\t"
        "ds_read_b128 v[92:95], v130 offset:0\n\t"
        "v_readlane_b32 s43, v155, 3\n\t"
        "v_mfma_f32_16x16x32_bf16 v[20:23], a[188:191], v[112:115], v[20:23]\n\t"
        "ds_read_b64_tr_b16 a[246:247], v132 offset:19200\n\t"
        "s_add_u32 m0, s40, s35\n\t"
        "v_mfma_f32_16x16x32_bf16 v[24:27], a[192:195], v[112:115], v[24:27]\n\t"
        "ds_read_b128 v[96:99], v130 offset:4096\n\t"
        "v_add_f32 v56, v56, v58\n\t"
        "v_mfma_f32_16x16x32_bf16 v[28:31], a[196:199], v[112:115], v[28:31]\n\t"
        "buffer_load_dwordx4 v135, s[16:19], s43 offen  lds\n\t"
        "v_readlane_b32 s40, v155, 16\n\t"
        "v_mfma_f32_16x16x32_bf16 v[32:35], a[200:203], v[112:115], v[32:35]\n\t"
        "ds_read_b64_tr_b16 a[248:249], v133 offset:18432\n\t"
        "v_readlane_b32 s43, v155, 4\n\t"
        "s_waitcnt lgkmcnt(7)\n\t"
        "v_mfma_f32_16x16x32_bf16 v[12:15], a[204:207], v[84:87], v[12:15]\n\t"
        "ds_read_b64_tr_b16 a[250:251], v133 offset:19200\n\t"
        "s_add_u32 m0, s40, s35\n\t"
        "v_mfma_f32_16x16x32_bf16 v[16:19], a[208:211], v[84:87], v[16:19]\n\t"
        "v_add_f32 v57, v57, v59\n\t"
        "v_cvt_pk_bf16_f32 v52, v56, v57\n\t"
        "v_mfma_f32_16x16x32_bf16 v[20:23], a[212:215], v[84:87], v[20:23]\n\t"
        "buffer_load_dwordx4 v134, s[16:19], s43 offen  lds\n\t"
        "v_readlane_b32 s40, v155, 17\n\t"
        "v_mfma_f32_16x16x32_bf16 v[24:27], a[180:183], v[84:87], v[24:27]\n\t"
        "ds_read_b128 v[100:103], v130 offset:8192\n\t"
        "v_readlane_b32 s43, v155, 5\n\t"
        "v_mfma_f32_16x16x32_bf16 v[28:31], a[184:187], v[84:87], v[28:31]\n\t"
        "s_add_u32 m0, s40, s35\n\t"
        "v_cvt_pk_bf16_f32 v58, v58, v59\n\t"
        "v_mfma_f32_16x16x32_bf16 v[32:35], a[188:191], v[84:87], v[32:35]\n\t"
        "v_cmp_ne_u32 s[64:65], v52, v58\n\t"
        "v_mfma_f32_16x16x32_bf16 v[36:39], a[192:195], v[84:87], v[36:39]\n\t"
        "buffer_load_dwordx4 v135, s[16:19], s43 offen  lds\n\t"
        "v_readlane_b32 s40, v155, 18\n\t"
        "v_readlane_b32 s43, v155, 6\n\t"
        "v_mfma_f32_16x16x32_bf16 v[40:43], a[196:199], v[84:87], v[40:43]\n\t"
        "s_add_u32 m0, s40, s35\n\t"
        "v_mbcnt_lo_u32_b32 v56, s64, v54\n\t"
        "v_mfma_f32_16x16x32_bf16 v[44:47], a[200:203], v[84:87], v[44:47]\n\t"
        "s_waitcnt vmcnt(6) lgkmcnt(1)\n\t"
        "s_barrier\n\t"
        "v_mfma_f32_16x16x32_bf16 v[24:27], a[168:171], v[88:91], v[24:27]\n\t"
        "ds_read_b64_tr_b16 a[252:253], v131 offset:24576\n\t"
        "v_mbcnt_hi_u32_b32 v56, s65, v56\n\t"
        "v_mfma_f32_16x16x32_bf16 v[28:31], a[172:175], v[88:91], v[28:31]\n\t"
        "buffer_load_dwordx4 v136, s[16:19], s43 offen  lds\n\t"
        "v_readlane_b32 s40, v155, 20\n\t"
        "v_mfma_f32_16x16x32_bf16 v[32:35], a[176:179], v[88:91], v[32:35]\n\t"
        "ds_read_b64_tr_b16 a[254:255], v131 offset:25344\n\t"
        "v_readlane_b32 s43, v155, 8\n\t"
        "v_mfma_f32_16x16x32_bf16 v[36:39], a[144:147], v[88:91], v[36:39]\n\t"
        "ds_read_b128 v[104:107], v130 offset:12288\n\t"
        "s_add_u32 m0, s40, s35\n\t"
        "v_mfma_f32_16x16x32_bf16 v[40:43], a[148:151], v[88:91], v[40:43]\n\t"
        "ds_read_b64_tr_b16 v[188:189], v132 offset:24576\n\t"
        "s_bcnt1_i32_b64 s46, s[64:65]\n\t"
        "v_mfma_f32_16x16x32_bf16 v[44:47], a[152:155], v[88:91], v[44:47]\n\t"
        "buffer_load_dwordx4 v136, s[16:19], s43 offen  lds\n\t"
        "v_readlane_b32 s40, v155, 19\n\t"
        "v_mfma_f32_16x16x32_bf16 v[0:3], a[228:231], v[92:95], v[0:3]\n\t"
        "ds_read_b64_tr_b16 v[190:191], v132 offset:25344\n\t"
        "ds_read_b64_tr_b16 v[192:193], v133 offset:24576\n\t"
        "v_readlane_b32 s43, v155, 7\n\t"
        "v_mfma_f32_16x16x32_bf16 v[4:7], a[232:235], v[92:95], v[4:7]\n\t"
        "ds_read_b64_tr_b16 v[194:195], v133 offset:25344\n\t"
        "s_add_u32 m0, s40, s35\n\t"
        "v_mfma_f32_16x16x32_bf16 v[8:11], a[236:239], v[92:95], v[8:11]\n\t"
        "ds_read_b64_tr_b16 v[196:197], v131 offset:30720\n\t"
        "s_add_u32 s61, s61, s46\n\t"
        "v_mfma_f32_16x16x32_bf16 v[0:3], a[216:219], v[96:99], v[0:3]\n\t"
        "buffer_load_dwordx4 v137, s[16:19], s43 offen  lds\n\t"
        "s_mov_b32 s3, s61\n\t"
        "v_mfma_f32_16x16x32_bf16 v[4:7], a[220:223], v[96:99], v[4:7]\n\t"
        "ds_read_b64_tr_b16 v[198:199], v131 offset:31488\n\t"
        "v_min_u32 v56, 97, v56\n\t"
        "v_mfma_f32_16x16x32_bf16 v[8:11], a[224:227], v[96:99], v[8:11]\n\t"
        "ds_read_b64_tr_b16 v[200:201], v132 offset:30720\n\t"
        "v_lshl_add_u32 v56, v56, 3, s60\n\t"
        "v_mfma_f32_16x16x32_bf16 v[12:15], a[228:231], v[96:99], v[12:15]\n\t"
        "s_mov_b64 exec, s[64:65]\n\t"
        "ds_write_b64 v56, v[52:53]\n\t"
        "s_mov_b64 exec, -1\n\t"
        "v_mfma_f32_16x16x32_bf16 v[16:19], a[232:235], v[96:99], v[16:19]\n\t"
        "s_cmp_gt_u32 s3, 64\n\t"
        "s_cselect_b32 s8, 1, 0\n\t"
        "v_mfma_f32_16x16x32_bf16 v[20:23], a[236:239], v[96:99], v[20:23]\n\t"
        "ds_read_b128 v[108:111], v129 offset:16512\n\t"
        "s_or_b32 s56, s56, s8\n\t"
        "s_waitcnt lgkmcnt(7)\n\t"
        "v_mfma_f32_16x16x32_bf16 v[0:3], a[240:243], v[100:103], v[0:3]\n\t"
        "ds_read_b64_tr_b16 v[202:203], v132 offset:31488\n\t"
        "s_min_u32 s61, s61, 80\n\t"
        "v_mfma_f32_16x16x32_bf16 v[4:7], a[244:247], v[100:103], v[4:7]\n\t"
        "ds_read_b64_tr_b16 v[204:205], v133 offset:30720\n\t"
        "v_min_u32 v78, s61, v116\n\t"
        "v_mfma_f32_16x16x32_bf16 v[8:11], a[248:251], v[100:103], v[8:11]\n\t"
        "ds_read_b64_tr_b16 v[206:207], v133 offset:31488\n\t"
        "v_lshl_add_u32 v78, v78, 3, s60\n\t"
        "v_mfma_f32_16x16x32_bf16 v[12:15], a[216:219], v[100:103], v[12:15]\n\t"
        "ds_read_b64 v[62:63], v78\n\t"
        "v_cmp_ge_u32 s[66:67], s61, v116\n\t"
        "v_mfma_f32_16x16x32_bf16 v[16:19], a[220:223], v[100:103], v[16:19]\n\t"
        "ds_read_b128 v[112:115], v130 offset:64\n\t"
        "s_mov_b32 s46, 0x07060302\n\t"
        "v_mfma_f32_16x16x32_bf16 v[20:23], a[224:227], v[100:103], v[20:23]\n\t"
        "ds_read_b64_tr_b16 v[208:209], v131 offset:36864\n\t"
        "v_mfma_f32_16x16x32_bf16 v[24:27], a[228:231], v[100:103], v[24:27]\n\t"
        "ds_read_b128 v[84:87], v130 offset:4160\n\t"
        "v_mfma_f32_16x16x32_bf16 v[28:31], a[232:235], v[100:103], v[28:31]\n\t"
        "ds_read_b64_tr_b16 v[210:211], v131 offset:37632\n\t"
        "v_mfma_f32_16x16x32_bf16 v[32:35], a[236:239], v[100:103], v[32:35]\n\t"
        "ds_read_b64_tr_b16 v[212:213], v132 offset:36864\n\t"
        "v_mfma_f32_16x16x32_bf16 v[12:15], a[240:243], v[104:107], v[12:15]\n\t"
        "ds_read_b64_tr_b16 v[214:215], v132 offset:37632\n\t"
        "v_mfma_f32_16x16x32_bf16 v[16:19], a[244:247], v[104:107], v[16:19]\n\t"
        "ds_read_b64_tr_b16 v[216:217], v133 offset:36864\n\t"
        "v_mfma_f32_16x16x32_bf16 v[20:23], a[248:251], v[104:107], v[20:23]\n\t"
        "ds_read_b64_tr_b16 v[218:219], v133 offset:37632\n\t"
        "v_mfma_f32_16x16x32_bf16 v[24:27], a[216:219], v[104:107], v[24:27]\n\t"
        "s_mov_b64 exec, s[66:67]\n\t"
        "s_waitcnt lgkmcnt(8)\n\t"
        "ds_write_b32 v63, v117\n\t"
        "s_mov_b64 exec, -1\n\t"
        "v_mfma_f32_16x16x32_bf16 v[28:31], a[220:223], v[104:107], v[28:31]\n\t"
        "ds_read_b128 v[88:91], v130 offset:8256\n\t"
        "v_and_b32 v78, 0xfffff800, v63\n\t"
        "v_lshlrev_b32 v72, 16, v62\n\t"
        "v_mfma_f32_16x16x32_bf16 v[32:35], a[224:227], v[104:107], v[32:35]\n\t"
        "ds_read_b128 v[64:67], v78 offset:1536\n\t"
        "v_and_b32 v73, 0xffff0000, v62\n\t"
        "v_mfma_f32_16x16x32_bf16 v[36:39], a[228:231], v[104:107], v[36:39]\n\t"
        "ds_read_b64_tr_b16 v[220:221], v131 offset:43008\n\t"
        "v_med3_f32 v72, v72, 0, s63\n\t"
        "v_mfma_f32_16x16x32_bf16 v[40:43], a[232:235], v[104:107], v[40:43]\n\t"
        "ds_read_b64_tr_b16 v[222:223], v131 offset:43776\n\t"
        "v_med3_f32 v73, v73, 0, s63\n\t"
        "v_mfma_f32_16x16x32_bf16 v[44:47], a[236:239], v[104:107], v[44:47]\n\t"
        "ds_read_b64_tr_b16 v[224:225], v132 offset:43008\n\t"
        "v_perm_b32 v62, v73, v72, s46\n\t"
        "v_mfma_f32_16x16x32_bf16 v[24:27], a[204:207], v[108:111], v[24:27]\n\t"
        "ds_read_b64_tr_b16 v[226:227], v132 offset:43776\n\t"
        "v_fract_f32 v74, v72\n\t"
        "v_mfma_f32_16x16x32_bf16 v[28:31], a[208:211], v[108:111], v[28:31]\n\t"
        "ds_read_b64_tr_b16 v[228:229], v133 offset:43008\n\t"
        "v_fract_f32 v75, v73\n\t"
        "v_mfma_f32_16x16x32_bf16 v[32:35], a[212:215], v[108:111], v[32:35]\n\t"
        "ds_read_b64_tr_b16 v[230:231], v133 offset:43776\n\t"
        "v_cvt_u32_f32 v76, v72\n\t"
        "v_mfma_f32_16x16x32_bf16 v[36:39], a[180:183], v[108:111], v[36:39]\n\t"
        "ds_read_b128 v[92:95], v130 offset:12352\n\t"
        "v_cvt_u32_f32 v77, v73\n\t"
        "v_cmp_neq_f32 s[64:65], 0, v74\n\t"
        "v_mfma_f32_16x16x32_bf16 v[40:43], a[184:187], v[108:111], v[40:43]\n\t"
        "ds_read_b64_tr_b16 v[232:233], v131 offset:49152\n\t"
        "v_cmp_neq_f32 s[44:45], 0, v75\n\t"
        "v_mfma_f32_16x16x32_bf16 v[44:47], a[188:191], v[108:111], v[44:47]\n\t"
        "ds_read_b64_tr_b16 v[234:235], v131 offset:49920\n\t"
        "v_sub_f32 v78, 1.0, v74\n\t"
        "s_waitcnt lgkmcnt(9)\n\t"
        "v_mfma_f32_16x16x32_bf16 v[0:3], v[196:199], v[112:115], v[0:3]\n\t"
        "ds_read_b64_tr_b16 v[236:237], v132 offset:49152\n\t"
        "v_sub_f32 v79, 1.0, v75\n\t"
        "v_mfma_f32_16x16x32_bf16 v[4:7], v[200:203], v[112:115], v[4:7]\n\t"
        "ds_read_b64_tr_b16 v[238:239], v132 offset:49920\n\t"
        "v_cvt_pk_bf16_f32 v79, v78, v79\n\t"
        "v_mfma_f32_16x16x32_bf16 v[8:11], v[204:207], v[112:115], v[8:11]\n\t"
        "ds_read_b64_tr_b16 v[240:241], v133 offset:49152\n\t"
        "v_lshlrev_b32 v78, 16, v79\n\t"
        "v_mfma_f32_16x16x32_bf16 v[0:3], a[252:255], v[84:87], v[0:3]\n\t"
        "ds_read_b64_tr_b16 v[242:243], v133 offset:49920\n\t"
        "v_and_b32 v79, 0xffff0000, v79\n\t"
        "v_mfma_f32_16x16x32_bf16 v[4:7], v[188:191], v[84:87], v[4:7]\n\t"
        "ds_read_b64_tr_b16 v[244:245], v131 offset:55296\n\t"
        "v_mul_f32 v80, v78, v79\n\t"
        "v_mfma_f32_16x16x32_bf16 v[8:11], v[192:195], v[84:87], v[8:11]\n\t"
        "ds_read_b64_tr_b16 v[246:247], v131 offset:56064\n\t"
        "v_mul_f32 v78, v78, v75\n\t"
        "v_mfma_f32_16x16x32_bf16 v[12:15], v[196:199], v[84:87], v[12:15]\n\t"
        "ds_read_b128 v[96:99], v130 offset:128\n\t"
        "v_mul_f32 v79, v74, v79\n\t"
        "v_mfma_f32_16x16x32_bf16 v[16:19], v[200:203], v[84:87], v[16:19]\n\t"
        "ds_read_b64_tr_b16 v[248:249], v132 offset:55296\n\t"
        "v_mul_f32 v81, v74, v75\n\t"
        "v_mfma_f32_16x16x32_bf16 v[20:23], v[204:207], v[84:87], v[20:23]\n\t"
        "ds_read_b64_tr_b16 v[250:251], v132 offset:56064\n\t"
        "v_cndmask_b32 v78, v79, v78, s[44:45]\n\t"
        "v_mfma_f32_16x16x32_bf16 v[0:3], v[208:211], v[88:91], v[0:3]\n\t"
        "ds_read_b128 v[100:103], v130 offset:4224\n\t"
        "v_cndmask_b32 v79, 0, v79, s[44:45]\n\t"
        "v_mfma_f32_16x16x32_bf16 v[4:7], v[212:215], v[88:91], v[4:7]\n\t"
        "ds_read_b64_tr_b16 v[252:253], v133 offset:55296\n\t"
        "v_cvt_pk_bf16_f32 v70, v80, v78\n\t"
        "v_mfma_f32_16x16x32_bf16 v[8:11], v[216:219], v[88:91], v[8:11]\n\t"
        "ds_read_b64_tr_b16 v[254:255], v133 offset:56064\n\t"
        "v_cvt_pk_bf16_f32 v71, v79, v81\n\t"
        "v_mfma_f32_16x16x32_bf16 v[12:15], a[252:255], v[88:91], v[12:15]\n\t"
        "v_pk_max_u16 v78, v62, v64\n\t"
        "v_pk_min_u16 v78, v78, v65\n\t"
        "v_mfma_f32_16x16x32_bf16 v[16:19], v[188:191], v[88:91], v[16:19]\n\t"
        "v_cmp_ne_u32 vcc, v78, v62\n\t"
        "v_sub_u32 v78, v77, v67\n\t"
        "v_mfma_f32_16x16x32_bf16 v[20:23], v[192:195], v[88:91], v[20:23]\n\t"
        "v_mad_u32_u24 v79, v77, 6, v66\n\t"
        "v_mad_u32_u24 v79, v76, s62, v79\n\t"
        "v_mfma_f32_16x16x32_bf16 v[24:27], v[196:199], v[88:91], v[24:27]\n\t"
        "ds_read_b128 v[104:107], v130 offset:8320\n\t"
        "s_and_b64 vcc, vcc, s[66:67]\n\t"
        "v_mfma_f32_16x16x32_bf16 v[28:31], v[200:203], v[88:91], v[28:31]\n\t"
        "s_or_b64 s[56:57], s[56:57], vcc\n\t"
        "v_bfe_u32 v80, v78, 2, 1\n\t"
        "v_mfma_f32_16x16x32_bf16 v[32:35], v[204:207], v[88:91], v[32:35]\n\t"
        "v_or_b32 v80, v80, v79\n\t"
        "v_add_u32 v78, 1, v78\n\t"
        "s_waitcnt lgkmcnt(5)\n\t"
        "v_mfma_f32_16x16x32_bf16 v[12:15], v[208:211], v[92:95], v[12:15]\n\t"
        "v_bfe_u32 v78, v78, 2, 1\n\t"
        "v_add3_u32 v78, v79, v78, 6\n\t"
        "v_mfma_f32_16x16x32_bf16 v[16:19], v[212:215], v[92:95], v[16:19]\n\t"
        "v_cndmask_b32 v78, v80, v78, s[44:45]\n\t"
        "v_cndmask_b32 v79, 0, v119, s[64:65]\n\t"
        "v_mfma_f32_16x16x32_bf16 v[20:23], v[216:219], v[92:95], v[20:23]\n\t"
        "v_add_u32 v81, v80, v79\n\t"
        "v_add_u32 v82, v78, v79\n\t"
        "v_mfma_f32_16x16x32_bf16 v[24:27], a[252:255], v[92:95], v[24:27]\n\t"
        "v_cndmask_b32 v78, v81, v78, s[44:45]\n\t"
        "v_lshl_or_b32 v68, v78, 16, v80\n\t"
        "v_mfma_f32_16x16x32_bf16 v[28:31], v[188:191], v[92:95], v[28:31]\n\t"
        "v_lshl_or_b32 v69, v82, 16, v81\n\t"
        "v_mfma_f32_16x16x32_bf16 v[32:35], v[192:195], v[92:95], v[32:35]\n\t"
        "ds_write_b128 v118, v[68:71]\n\t"
        "v_mfma_f32_16x16x32_bf16 v[36:39], v[196:199], v[92:95], v[36:39]\n\t"
        "s_cmp_gt_u32 s3, 16\n\t"
        "s_cselect_b32 s8, 2, 0\n\t"
        "v_mfma_f32_16x16x32_bf16 v[40:43], v[200:203], v[92:95], v[40:43]\n\t"
        "s_cmp_lg_u64 s[56:57], 0\n\t"
        "s_addc_u32 s8, s8, s6\n\t"
        "v_mfma_f32_16x16x32_bf16 v[44:47], v[204:207], v[92:95], v[44:47]\n\t"
        "ds_read_b128 v[160:163], v185 offset:0\n\t"
        "v_mov_b32 v151, s8\n\t"
        "v_mfma_f32_16x16x32_bf16 v[0:3], v[232:235], v[96:99], v[0:3]\n\t"
        "s_waitcnt vmcnt(0) lgkmcnt(2)\n\t"
        "s_barrier\n\t"
        "v_mfma_f32_16x16x32_bf16 v[4:7], v[236:239], v[96:99], v[4:7]\n\t"
        "s_mov_b64 exec, 1\n\t"
        "ds_write_b32 v153, v151\n\t"
        "s_mov_b64 exec, -1\n\t"
        "v_mfma_f32_16x16x32_bf16 v[8:11], v[240:243], v[96:99], v[8:11]\n\t"
        "ds_read_b128 v[108:111], v130 offset:12416\n\t"
        "v_xor_b32 v154, 32, v186\n\t"
        "v_mfma_f32_16x16x32_bf16 v[0:3], v[220:223], v[100:103], v[0:3]\n\t"
        "v_mfma_f32_16x16x32_bf16 v[4:7], v[224:227], v[100:103], v[4:7]\n\t"
        "v_mfma_f32_16x16x32_bf16 v[8:11], v[228:231], v[100:103], v[8:11]\n\t"
        "v_mfma_f32_16x16x32_bf16 v[12:15], v[232:235], v[100:103], v[12:15]\n\t"
        "ds_read_b128 v[112:115], v130 offset:16384\n\t"
        "v_mfma_f32_16x16x32_bf16 v[16:19], v[236:239], v[100:103], v[16:19]\n\t"
        "v_mfma_f32_16x16x32_bf16 v[20:23], v[240:243], v[100:103], v[20:23]\n\t"
        "s_waitcnt lgkmcnt(3)\n\t"
        "v_lshlrev_b32_sdwa v157, 5, v160 dst_sel:DWORD dst_unused:UNUSED_PAD src0_sel:DWORD src1_sel:WORD_0\n\t"
        "v_lshlrev_b32_sdwa v158, 5, v160 dst_sel:DWORD dst_unused:UNUSED_PAD src0_sel:DWORD src1_sel:WORD_1\n\t"
        "v_mfma_f32_16x16x32_bf16 v[0:3], v[244:247], v[104:107], v[0:3]\n\t"
        "v_or_b32 v157, v184, v157\n\t"
        "v_or_b32 v158, v184, v158\n\t"
        "v_mfma_f32_16x16x32_bf16 v[4:7], v[248:251], v[104:107], v[4:7]\n\t"
        "ds_read_b64_tr_b16 v[60:61], v157 offset:0\n\t"
        "v_xor_b32 v159, 32, v157\n\t"
        "v_mfma_f32_16x16x32_bf16 v[8:11], v[252:255], v[104:107], v[8:11]\n\t"
        "ds_read_b64_tr_b16 v[62:63], v158 offset:0\n\t"
        "v_xor_b32 v152, 32, v158\n\t"
        "v_mfma_f32_16x16x32_bf16 v[12:15], v[220:223], v[104:107], v[12:15]\n\t"
        "ds_read_b64_tr_b16 v[64:65], v159 offset:0\n\t"
        "v_perm_b32 v164, v162, v162, v168\n\t"
        "v_mfma_f32_16x16x32_bf16 v[16:19], v[224:227], v[104:107], v[16:19]\n\t"
        "ds_read_b64_tr_b16 v[66:67], v152 offset:0\n\t"
        "v_perm_b32 v165, v162, v162, v169\n\t"
        "v_mfma_f32_16x16x32_bf16 v[20:23], v[228:231], v[104:107], v[20:23]\n\t"
        "ds_read_b64_tr_b16 v[68:69], v157 offset:64\n\t"
        "v_perm_b32 v166, v162, v162, v170\n\t"
        "v_mfma_f32_16x16x32_bf16 v[24:27], v[232:235], v[104:107], v[24:27]\n\t"
        "ds_read_b64_tr_b16 v[70:71], v158 offset:64\n\t"
        "v_perm_b32 v167, v162, v162, v171\n\t"
        "v_mfma_f32_16x16x32_bf16 v[28:31], v[236:239], v[104:107], v[28:31]\n\t"
        "ds_read_b64_tr_b16 v[72:73], v159 offset:64\n\t"
        "v_cmp_ne_u32 vcc, 0, v163\n\t"
        "v_mfma_f32_16x16x32_bf16 v[32:35], v[240:243], v[104:107], v[32:35]\n\t"
        "ds_read_b128 v[84:87], v130 offset:16448\n\t"
        "s_waitcnt lgkmcnt(8)\n\t"
        "v_mfma_f32_16x16x32_bf16 v[12:15], v[244:247], v[108:111], v[12:15]\n\t"
        "ds_read_b64_tr_b16 v[74:75], v152 offset:64\n\t"
        "v_mfma_f32_16x16x32_bf16 v[16:19], v[248:251], v[108:111], v[16:19]\n\t"
        "ds_read_b64_tr_b16 v[76:77], v157 offset:128\n\t"
        "v_mfma_f32_16x16x32_bf16 v[20:23], v[252:255], v[108:111], v[20:23]\n\t"
        "ds_read_b64_tr_b16 v[78:79], v158 offset:128\n\t"
        "s_waitcnt lgkmcnt(9)\n\t"
        "v_mfma_f32_16x16x32_bf16 v[48:51], v[60:63], v[164:167], 0\n\t"
        "ds_read_b64_tr_b16 v[80:81], v159 offset:128\n\t"
        "v_mfma_f32_16x16x32_bf16 v[24:27], v[220:223], v[108:111], v[24:27]\n\t"
        "ds_read_b64_tr_b16 v[82:83], v152 offset:128\n\t"
        "s_waitcnt lgkmcnt(9)\n\t"
        "v_mfma_f32_16x16x32_bf16 v[52:55], v[64:67], v[164:167], 0\n\t"
        "ds_read_b128 v[88:91], v130 offset:16512\n\t"
        "v_mfma_f32_16x16x32_bf16 v[28:31], v[224:227], v[108:111], v[28:31]\n\t"
        "s_waitcnt lgkmcnt(8)\n\t"
        "v_mfma_f32_16x16x32_bf16 v[56:59], v[68:71], v[164:167], 0\n\t"
        ".LpS1%=:\n\t"
        "s_cbranch_execz .LrS%=\n\t"
        "v_mfma_f32_16x16x32_bf16 v[32:35], v[228:231], v[108:111], v[32:35]\n\t"
        "v_mfma_f32_16x16x32_bf16 v[36:39], v[232:235], v[108:111], v[36:39]\n\t"
        "ds_read_b128 v[92:95], v128 offset:20480\n\t"
        "s_waitcnt lgkmcnt(6)\n\t"
        "v_mfma_f32_16x16x32_bf16 v[116:119], v[72:75], v[164:167], 0\n\t"
        "v_mfma_f32_16x16x32_bf16 v[40:43], v[236:239], v[108:111], v[40:43]\n\t"
        "s_waitcnt lgkmcnt(4)\n\t"
        "v_mfma_f32_16x16x32_bf16 v[120:123], v[76:79], v[164:167], 0\n\t"
        "v_mfma_f32_16x16x32_bf16 v[44:47], v[240:243], v[108:111], v[44:47]\n\t"
        "s_waitcnt lgkmcnt(2)\n\t"
        "v_mfma_f32_16x16x32_bf16 v[124:127], v[80:83], v[164:167], 0\n\t"
        "s_cbranch_vccz .Lx56%=\n\t"
        "v_lshlrev_b32_sdwa v157, 5, v161 dst_sel:DWORD dst_unused:UNUSED_PAD src0_sel:DWORD src1_sel:WORD_0\n\t"
        "v_lshlrev_b32_sdwa v158, 5, v161 dst_sel:DWORD dst_unused:UNUSED_PAD src0_sel:DWORD src1_sel:WORD_1\n\t"
        "v_or_b32 v157, v184, v157\n\t"
        "v_or_b32 v158, v184, v158\n\t"
        "v_xor_b32 v159, 32, v157\n\t"
        "v_xor_b32 v152, 32, v158\n\t"
        "ds_read_b64_tr_b16 v[60:61], v157 offset:0\n\t"
        "ds_read_b64_tr_b16 v[62:63], v158 offset:0\n\t"
        "ds_read_b64_tr_b16 v[64:65], v159 offset:0\n\t"
        "ds_read_b64_tr_b16 v[66:67], v152 offset:0\n\t"
        "ds_read_b64_tr_b16 v[68:69], v157 offset:64\n\t"
        "ds_read_b64_tr_b16 v[70:71], v158 offset:64\n\t"
        "ds_read_b64_tr_b16 v[72:73], v159 offset:64\n\t"
        "ds_read_b64_tr_b16 v[74:75], v152 offset:64\n\t"
        "ds_read_b64_tr_b16 v[76:77], v157 offset:128\n\t"
        "ds_read_b64_tr_b16 v[78:79], v158 offset:128\n\t"
        "ds_read_b64_tr_b16 v[80:81], v159 offset:128\n\t"
        "ds_read_b64_tr_b16 v[82:83], v152 offset:128\n\t"
        "v_perm_b32 v164, v163, v163, v168\n\t"
        "v_perm_b32 v165, v163, v163, v169\n\t"
        "v_perm_b32 v166, v163, v163, v170\n\t"
        "v_perm_b32 v167, v163, v163, v171\n\t"
        "s_waitcnt lgkmcnt(10)\n\t"
        "s_nop 0\n\t"
        "v_mfma_f32_16x16x32_bf16 v[48:51], v[60:63], v[164:167], v[48:51]\n\t"
        "s_waitcnt lgkmcnt(8)\n\t"
        "v_mfma_f32_16x16x32_bf16 v[52:55], v[64:67], v[164:167], v[52:55]\n\t"
        "s_waitcnt lgkmcnt(6)\n\t"
        "v_mfma_f32_16x16x32_bf16 v[56:59], v[68:71], v[164:167], v[56:59]\n\t"
        "s_waitcnt lgkmcnt(4)\n\t"
        "v_mfma_f32_16x16x32_bf16 v[116:119], v[72:75], v[164:167], v[116:119]\n\t"
        "s_waitcnt lgkmcnt(2)\n\t"
        "v_mfma_f32_16x16x32_bf16 v[120:123], v[76:79], v[164:167], v[120:123]\n\t"
        "s_waitcnt lgkmcnt(0)\n\t"
        "v_mfma_f32_16x16x32_bf16 v[124:127], v[80:83], v[164:167], v[124:127]\n\t"
        "s_nop 4\n\t"
        ".Lx56%=:\n\t"
        "v_cvt_pk_bf16_f32 v48, v48, v49\n\t"
        "v_cvt_pk_bf16_f32 v49, v50, v51\n\t"
        "v_mfma_f32_16x16x32_bf16 v[24:27], a[240:243], v[112:115], v[24:27]\n\t"
        "ds_write_b64 v186, v[48:49] offset:0\n\t"
        "v_mfma_f32_16x16x32_bf16 v[28:31], a[244:247], v[112:115], v[28:31]\n\t"
        "ds_read_b128 v[96:99], v128 offset:20544\n\t"
        "v_cvt_pk_bf16_f32 v52, v52, v53\n\t"
        "v_cvt_pk_bf16_f32 v53, v54, v55\n\t"
        "v_mfma_f32_16x16x32_bf16 v[32:35], a[248:251], v[112:115], v[32:35]\n\t"
        "ds_write_b64 v154, v[52:53] offset:0\n\t"
        "v_mfma_f32_16x16x32_bf16 v[36:39], a[216:219], v[112:115], v[36:39]\n\t"
        "ds_read_b128 v[100:103], v128 offset:20608\n\t"
        "v_cvt_pk_bf16_f32 v56, v56, v57\n\t"
        "v_cvt_pk_bf16_f32 v57, v58, v59\n\t"
        "v_mfma_f32_16x16x32_bf16 v[40:43], a[220:223], v[112:115], v[40:43]\n\t"
        "ds_write_b64 v186, v[56:57] offset:64\n\t"
        "v_mfma_f32_16x16x32_bf16 v[44:47], a[224:227], v[112:115], v[44:47]\n\t"
        "v_cvt_pk_bf16_f32 v116, v116, v117\n\t"
        "v_cvt_pk_bf16_f32 v117, v118, v119\n\t"
        "v_mfma_f32_16x16x32_bf16 v[24:27], v[208:211], v[84:87], v[24:27]\n\t"
        "ds_write_b64 v154, v[116:117] offset:64\n\t"
        "v_mfma_f32_16x16x32_bf16 v[28:31], v[212:215], v[84:87], v[28:31]\n\t"
        "ds_read_b128 v[104:107], v129 offset:20480\n\t"
        "v_cvt_pk_bf16_f32 v120, v120, v121\n\t"
        "v_mfma_f32_16x16x32_bf16 v[32:35], v[216:219], v[84:87], v[32:35]\n\t"
        "v_cvt_pk_bf16_f32 v121, v122, v123\n\t"
        "v_cvt_pk_bf16_f32 v124, v124, v125\n\t"
        "v_cvt_pk_bf16_f32 v125, v126, v127\n\t"
        "v_mfma_f32_16x16x32_bf16 v[36:39], a[252:255], v[84:87], v[36:39]\n\t"
        "ds_write_b64 v186, v[120:121] offset:128\n\t"
        "v_mfma_f32_16x16x32_bf16 v[40:43], v[188:191], v[84:87], v[40:43]\n\t"
        "ds_write_b64 v154, v[124:125] offset:128\n\t"
        "v_mfma_f32_16x16x32_bf16 v[44:47], v[192:195], v[84:87], v[44:47]\n\t"
        "ds_read_b128 v[108:111], v129 offset:20544\n\t"
        "s_waitcnt lgkmcnt(6)\n\t"
        "v_mfma_f32_16x16x32_bf16 v[24:27], v[244:247], v[88:91], v[24:27]\n\t"
        "ds_read_b128 v[112:115], v129 offset:20608\n\t"
        "v_mfma_f32_16x16x32_bf16 v[28:31], v[248:251], v[88:91], v[28:31]\n\t"
        "v_mfma_f32_16x16x32_bf16 v[32:35], v[252:255], v[88:91], v[32:35]\n\t"
        "v_mfma_f32_16x16x32_bf16 v[36:39], v[220:223], v[88:91], v[36:39]\n\t"
        "ds_read_b128 v[84:87], v130 offset:20480\n\t"
        "v_mfma_f32_16x16x32_bf16 v[40:43], v[224:227], v[88:91], v[40:43]\n\t"
        "s_waitcnt lgkmcnt(3)\n\t"
        "s_barrier\n\t"
        "v_mfma_f32_16x16x32_bf16 v[44:47], v[228:231], v[88:91], v[44:47]\n\t"
        "ds_read_b128 v[124:127], v182 offset:0\n\t"
        "v_mfma_f32_16x16x32_bf16 v[36:39], a[24:27], v[92:95], v[36:39]\n\t"
        "ds_read_b128 v[132:135], v187 offset:0\n\t"
        "v_mfma_f32_16x16x32_bf16 v[40:43], a[28:31], v[92:95], v[40:43]\n\t"
        "ds_read_b128 v[136:139], v187 offset:16\n\t"
        "v_mfma_f32_16x16x32_bf16 v[44:47], a[32:35], v[92:95], v[44:47]\n\t"
        "ds_read_b32 v140, v187 offset:32\n\t"
        "v_mfma_f32_16x16x32_bf16 v[36:39], a[60:63], v[96:99], v[36:39]\n\t"
        "ds_read_b128 v[88:91], v130 offset:20544\n\t"
        "v_mfma_f32_16x16x32_bf16 v[40:43], a[64:67], v[96:99], v[40:43]\n\t"
        "ds_read_b128 v[92:95], v130 offset:20608\n\t"
        "v_mfma_f32_16x16x32_bf16 v[44:47], a[68:71], v[96:99], v[44:47]\n\t"
        "v_mfma_f32_16x16x32_bf16 v[36:39], a[96:99], v[100:103], v[36:39]\n\t"
        "v_mfma_f32_16x16x32_bf16 v[40:43], a[100:103], v[100:103], v[40:43]\n\t"
        "v_mfma_f32_16x16x32_bf16 v[44:47], a[104:107], v[100:103], v[44:47]\n\t"
        "s_waitcnt lgkmcnt(5)\n\t"
        "v_or3_b32 v124, v124, v125, v126\n\t"
        "v_or_b32 v124, v124, v127\n\t"
        "v_mfma_f32_16x16x32_bf16 v[36:39], a[132:135], v[104:107], v[36:39]\n\t"
        "v_readfirstlane_b32 s6, v124\n\t"
        "v_mfma_f32_16x16x32_bf16 v[40:43], a[136:139], v[104:107], v[40:43]\n\t"
        "s_nop 0\n\t"
        "s_bitcmp1_b32 s6, 1\n\t"
        "s_cbranch_scc1 .Lx58%=\n\t"
        ".Lx59%=:\n\t"
        "v_mfma_f32_16x16x32_bf16 v[44:47], a[140:143], v[104:107], v[44:47]\n\t"
        "s_waitcnt lgkmcnt(3)\n\t"
        "v_xor_b32 v132, v181, v132\n\t"
        "v_add_u32 v151, s9, v172\n\t"
        "v_mfma_f32_16x16x32_bf16 v[36:39], a[168:171], v[108:111], v[36:39]\n\t"
        "v_max_u32 v151, v151, v132\n\t"
        "v_xor_b32 v133, v181, v133\n\t"
        "v_mfma_f32_16x16x32_bf16 v[40:43], a[172:175], v[108:111], v[40:43]\n\t"
        "ds_read_b128 v[96:99], v151 offset:0\n\t"
        "v_add_u32 v183, s9, v173\n\t"
        "v_mfma_f32_16x16x32_bf16 v[44:47], a[176:179], v[108:111], v[44:47]\n\t"
        "ds_read_b128 v[100:103], v151 offset:64\n\t"
        "v_max_u32 v183, v183, v133\n\t"
        "v_mfma_f32_16x16x32_bf16 v[36:39], a[204:207], v[112:115], v[36:39]\n\t"
        "ds_read_b128 v[104:107], v151 offset:128\n\t"
        "v_xor_b32 v134, v181, v134\n\t"
        "v_mfma_f32_16x16x32_bf16 v[40:43], a[208:211], v[112:115], v[40:43]\n\t"
        "ds_read_b128 v[108:111], v183 offset:0\n\t"
        "v_add_u32 v151, s9, v174\n\t"
        "v_mfma_f32_16x16x32_bf16 v[44:47], a[212:215], v[112:115], v[44:47]\n\t"
        "v_max_u32 v151, v151, v134\n\t"
        "v_xor_b32 v135, v181, v135\n\t"
        "v_mfma_f32_16x16x32_bf16 v[36:39], a[240:243], v[84:87], v[36:39]\n\t"
        "ds_read_b128 v[112:115], v183 offset:64\n\t"
        "s_add_u32 s55, s41, 0\n\t"
        "v_mfma_f32_16x16x32_bf16 v[40:43], a[244:247], v[84:87], v[40:43]\n\t"
        "v_cvt_pk_bf16_f32 v116, v0, v1\n\t"
        "v_cvt_pk_bf16_f32 v117, v2, v3\n\t"
        "v_mfma_f32_16x16x32_bf16 v[44:47], a[248:251], v[84:87], v[44:47]\n\t"
        "v_cvt_pk_bf16_f32 v118, v4, v5\n\t"
        "v_cvt_pk_bf16_f32 v119, v6, v7\n\t"
        "s_waitcnt lgkmcnt(5)\n\t"
        "v_mfma_f32_16x16x32_bf16 v[36:39], v[208:211], v[88:91], v[36:39]\n\t"
        "ds_read_b128 v[84:87], v183 offset:128\n\t"
        "v_add_u32 v183, s9, v175\n\t"
        "v_mfma_f32_16x16x32_bf16 v[40:43], v[212:215], v[88:91], v[40:43]\n\t"
        "v_max_u32 v183, v183, v135\n\t"
        "v_xor_b32 v136, v181, v136\n\t"
        "v_mfma_f32_16x16x32_bf16 v[44:47], v[216:219], v[88:91], v[44:47]\n\t"
        "ds_read_b128 v[132:135], v187 offset:2048\n\t"
        "v_cvt_pk_bf16_f32 v120, v8, v9\n\t"
        "v_mfma_f32_16x16x32_bf16 v[36:39], v[244:247], v[92:95], v[36:39]\n\t"
        "ds_read_b128 v[88:91], v151 offset:0\n\t"
        "v_cvt_pk_bf16_f32 v121, v10, v11\n\t"
        "v_mfma_f32_16x16x32_bf16 v[40:43], v[248:251], v[92:95], v[40:43]\n\t"
        "v_permlane16_swap_b32 v116, v118\n\t"
        "v_mfma_f32_16x16x32_bf16 v[44:47], v[252:255], v[92:95], v[44:47]\n\t"
        "v_permlane16_swap_b32 v117, v119\n\t"
        "s_waitcnt lgkmcnt(4)\n\t"
        "v_mfma_f32_16x16x32_bf16 v[48:51], a[12:15], v[96:99], 0\n\t"
        "buffer_store_dwordx4 v[116:119], v144, s[28:31], s55 offen sc1\n\t"
        "v_mfma_f32_16x16x32_bf16 v[52:55], a[16:19], v[96:99], 0\n\t"
        "ds_write_b64 v146, v[120:121] offset:0\n\t"
        "v_mfma_f32_16x16x32_bf16 v[56:59], a[20:23], v[96:99], 0\n\t"
        "ds_read_b128 v[92:95], v151 offset:64\n\t"
        "s_add_u32 s55, s41, 43008\n\t"
        "v_mfma_f32_16x16x32_bf16 v[48:51], a[48:51], v[100:103], v[48:51]\n\t"
        "ds_read_b128 v[96:99], v151 offset:128\n\t"
        "v_add_u32 v151, s9, v176\n\t"
        "v_mfma_f32_16x16x32_bf16 v[52:55], a[52:55], v[100:103], v[52:55]\n\t"
        "v_max_u32 v151, v151, v136\n\t"
        "v_xor_b32 v137, v181, v137\n\t"
        "v_mfma_f32_16x16x32_bf16 v[56:59], a[56:59], v[100:103], v[56:59]\n\t"
        "v_cvt_pk_bf16_f32 v116, v12, v13\n\t"
        "v_cvt_pk_bf16_f32 v117, v14, v15\n\t"
        "v_mfma_f32_16x16x32_bf16 v[48:51], a[84:87], v[104:107], v[48:51]\n\t"
        "ds_read_b128 v[100:103], v183 offset:0\n\t"
        "v_cvt_pk_bf16_f32 v118, v16, v17\n\t"
        "v_mfma_f32_16x16x32_bf16 v[52:55], a[88:91], v[104:107], v[52:55]\n\t"
        "v_cvt_pk_bf16_f32 v119, v18, v19\n\t"
        "v_cvt_pk_bf16_f32 v120, v20, v21\n\t"
        "v_mfma_f32_16x16x32_bf16 v[56:59], a[92:95], v[104:107], v[56:59]\n\t"
        "v_cvt_pk_bf16_f32 v121, v22, v23\n\t"
        "v_mfma_f32_16x16x32_bf16 v[48:51], a[120:123], v[108:111], v[48:51]\n\t"
        "v_permlane16_swap_b32 v116, v118\n\t"
        "v_mfma_f32_16x16x32_bf16 v[52:55], a[124:127], v[108:111], v[52:55]\n\t"
        "v_permlane16_swap_b32 v117, v119\n\t"
        "v_mfma_f32_16x16x32_bf16 v[56:59], a[128:131], v[108:111], v[56:59]\n\t"
        "ds_read_b128 v[104:107], v183 offset:64\n\t"
        "s_waitcnt lgkmcnt(4)\n\t"
        "v_mfma_f32_16x16x32_bf16 v[48:51], a[156:159], v[112:115], v[48:51]\n\t"
        "ds_read_b128 v[108:111], v183 offset:128\n\t"
        "v_add_u32 v183, s9, v177\n\t"
        "v_mfma_f32_16x16x32_bf16 v[52:55], a[160:163], v[112:115], v[52:55]\n\t"
        "v_max_u32 v183, v183, v137\n\t"
        "v_xor_b32 v138, v181, v138\n\t"
        "v_mfma_f32_16x16x32_bf16 v[56:59], a[164:167], v[112:115], v[56:59]\n\t"
        "buffer_store_dwordx4 v[116:119], v144, s[28:31], s55 offen sc1\n\t"
        "v_mfma_f32_16x16x32_bf16 v[48:51], a[192:195], v[84:87], v[48:51]\n\t"
        "ds_write_b64 v146, v[120:121] offset:2048\n\t"
        "v_mfma_f32_16x16x32_bf16 v[52:55], a[196:199], v[84:87], v[52:55]\n\t"
        "ds_read_b128 v[112:115], v151 offset:0\n\t"
        "s_add_u32 s55, s41, 86016\n\t"
        "v_mfma_f32_16x16x32_bf16 v[56:59], a[200:203], v[84:87], v[56:59]\n\t"
        "v_cvt_pk_bf16_f32 v116, v24, v25\n\t"
        "v_cvt_pk_bf16_f32 v117, v26, v27\n\t"
        "v_mfma_f32_16x16x32_bf16 v[48:51], a[228:231], v[88:91], v[48:51]\n\t"
        "ds_read_b128 v[84:87], v151 offset:64\n\t"
        "v_cvt_pk_bf16_f32 v118, v28, v29\n\t"
        "v_mfma_f32_16x16x32_bf16 v[52:55], a[232:235], v[88:91], v[52:55]\n\t"
        "v_cvt_pk_bf16_f32 v119, v30, v31\n\t"
        "v_cvt_pk_bf16_f32 v120, v32, v33\n\t"
        "v_mfma_f32_16x16x32_bf16 v[56:59], a[236:239], v[88:91], v[56:59]\n\t"
        "v_cvt_pk_bf16_f32 v121, v34, v35\n\t"
        "s_waitcnt lgkmcnt(4)\n\t"
        "v_mfma_f32_16x16x32_bf16 v[48:51], v[196:199], v[92:95], v[48:51]\n\t"
        "v_permlane16_swap_b32 v116, v118\n\t"
        "v_mfma_f32_16x16x32_bf16 v[52:55], v[200:203], v[92:95], v[52:55]\n\t"
        "v_permlane16_swap_b32 v117, v119\n\t"
        "v_mfma_f32_16x16x32_bf16 v[56:59], v[204:207], v[92:95], v[56:59]\n\t"
        "ds_read_b128 v[88:91], v151 offset:128\n\t"
        "v_add_u32 v151, s9, v178\n\t"
        "v_mfma_f32_16x16x32_bf16 v[48:51], v[232:235], v[96:99], v[48:51]\n\t"
        "ds_read_b128 v[92:95], v183 offset:0\n\t"
        "v_max_u32 v151, v151, v138\n\t"
        "v_mfma_f32_16x16x32_bf16 v[52:55], v[236:239], v[96:99], v[52:55]\n\t"
        "v_xor_b32 v139, v181, v139\n\t"
        "v_mfma_f32_16x16x32_bf16 v[56:59], v[240:243], v[96:99], v[56:59]\n\t"
        "v_mfma_f32_16x16x32_bf16 v[48:51], a[0:3], v[100:103], v[48:51]\n\t"
        "ds_read_b128 v[96:99], v183 offset:64\n\t"
        "v_mfma_f32_16x16x32_bf16 v[52:55], a[4:7], v[100:103], v[52:55]\n\t"
        "buffer_store_dwordx4 v[116:119], v144, s[28:31], s55 offen sc1\n\t"
        "v_mfma_f32_16x16x32_bf16 v[56:59], a[8:11], v[100:103], v[56:59]\n\t"
        "ds_write_b64 v146, v[120:121] offset:4096\n\t"
        "v_mfma_f32_16x16x32_bf16 v[48:51], a[36:39], v[104:107], v[48:51]\n\t"
        "ds_read_b128 v[100:103], v183 offset:128\n\t"
        "v_add_u32 v183, s9, v179\n\t"
        "v_mfma_f32_16x16x32_bf16 v[52:55], a[40:43], v[104:107], v[52:55]\n\t"
        "v_max_u32 v183, v183, v139\n\t"
        "v_xor_b32 v140, v181, v140\n\t"
        "v_mfma_f32_16x16x32_bf16 v[56:59], a[44:47], v[104:107], v[56:59]\n\t"
        "ds_read_b128 v[136:139], v187 offset:2064\n\t"
        "s_add_u32 s55, s41, 129024\n\t"
        "s_waitcnt lgkmcnt(5)\n\t"
        "v_mfma_f32_16x16x32_bf16 v[48:51], a[72:75], v[108:111], v[48:51]\n\t"
        "ds_read_b128 v[104:107], v151 offset:0\n\t"
        "v_cvt_pk_bf16_f32 v116, v36, v37\n\t"
        "v_mfma_f32_16x16x32_bf16 v[52:55], a[76:79], v[108:111], v[52:55]\n\t"
        "v_cvt_pk_bf16_f32 v117, v38, v39\n\t"
        "v_cvt_pk_bf16_f32 v118, v40, v41\n\t"
        "v_mfma_f32_16x16x32_bf16 v[56:59], a[80:83], v[108:111], v[56:59]\n\t"
        "v_cvt_pk_bf16_f32 v119, v42, v43\n\t"
        "v_cvt_pk_bf16_f32 v120, v44, v45\n\t"
        "v_mfma_f32_16x16x32_bf16 v[48:51], a[108:111], v[112:115], v[48:51]\n\t"
        "ds_read_b128 v[108:111], v151 offset:64\n\t"
        "v_cvt_pk_bf16_f32 v121, v46, v47\n\t"
        "v_mfma_f32_16x16x32_bf16 v[52:55], a[112:115], v[112:115], v[52:55]\n\t"
        "v_permlane16_swap_b32 v116, v118\n\t"
        "v_mfma_f32_16x16x32_bf16 v[56:59], a[116:119], v[112:115], v[56:59]\n\t"
        "v_permlane16_swap_b32 v117, v119\n\t"
        "v_mfma_f32_16x16x32_bf16 v[48:51], a[144:147], v[84:87], v[48:51]\n\t"
        "ds_read_b128 v[112:115], v151 offset:128\n\t"
        "v_add_u32 v151, s9, v180\n\t"
        "v_mfma_f32_16x16x32_bf16 v[52:55], a[148:151], v[84:87], v[52:55]\n\t"
        "v_max_u32 v151, v151, v140\n\t"
        "v_xor_b32 v132, v181, v132\n\t"
        "v_mfma_f32_16x16x32_bf16 v[56:59], a[152:155], v[84:87], v[56:59]\n\t"
        "ds_read_b32 v140, v187 offset:2080\n\t"
        "v_mfma_f32_16x16x32_bf16 v[48:51], a[180:183], v[88:91], v[48:51]\n\t"
        "buffer_store_dwordx4 v[116:119], v144, s[28:31], s55 offen sc1\n\t"
        "v_mfma_f32_16x16x32_bf16 v[52:55], a[184:187], v[88:91], v[52:55]\n\t"
        "ds_write_b64 v146, v[120:121] offset:6144\n\t"
        "v_mfma_f32_16x16x32_bf16 v[56:59], a[188:191], v[88:91], v[56:59]\n\t"
        "ds_read_b128 v[84:87], v183 offset:0\n\t"
        "s_waitcnt lgkmcnt(5)\n\t"
        "v_mfma_f32_16x16x32_bf16 v[48:51], a[216:219], v[92:95], v[48:51]\n\t"
        "ds_read_b128 v[88:91], v183 offset:64\n\t"
        "v_mfma_f32_16x16x32_bf16 v[52:55], a[220:223], v[92:95], v[52:55]\n\t"
        "v_mfma_f32_16x16x32_bf16 v[56:59], a[224:227], v[92:95], v[56:59]\n\t"
        "v_mfma_f32_16x16x32_bf16 v[48:51], a[252:255], v[96:99], v[48:51]\n\t"
        "ds_read_b128 v[92:95], v183 offset:128\n\t"
        "v_readlane_b32 s9, v155, 47\n\t"
        "v_mfma_f32_16x16x32_bf16 v[52:55], v[188:191], v[96:99], v[52:55]\n\t"
        "s_waitcnt lgkmcnt(3)\n\t"
        "s_barrier\n\t"
        "v_mfma_f32_16x16x32_bf16 v[56:59], v[192:195], v[96:99], v[56:59]\n\t"
        "ds_read_b128 v[124:127], v147 offset:0\n\t"
        "v_add_u32 v183, s9, v172\n\t"
        "v_mfma_f32_16x16x32_bf16 v[48:51], v[220:223], v[100:103], v[48:51]\n\t"
        "ds_read_b128 v[96:99], v151 offset:0\n\t"
        "v_max_u32 v183, v183, v132\n\t"
        "v_mfma_f32_16x16x32_bf16 v[52:55], v[224:227], v[100:103], v[52:55]\n\t"
        "v_xor_b32 v133, v181, v133\n\t"
        "v_mfma_f32_16x16x32_bf16 v[56:59], v[228:231], v[100:103], v[56:59]\n\t"
        "v_mfma_f32_16x16x32_bf16 v[48:51], a[24:27], v[104:107], v[48:51]\n\t"
        "ds_read_b128 v[100:103], v151 offset:64\n\t"
        "v_mfma_f32_16x16x32_bf16 v[52:55], a[28:31], v[104:107], v[52:55]\n\t"
        "v_mfma_f32_16x16x32_bf16 v[56:59], a[32:35], v[104:107], v[56:59]\n\t"
        "v_mfma_f32_16x16x32_bf16 v[48:51], a[60:63], v[108:111], v[48:51]\n\t"
        "ds_read_b128 v[104:107], v151 offset:128\n\t"
        "v_add_u32 v151, s9, v173\n\t"
        "v_mfma_f32_16x16x32_bf16 v[52:55], a[64:67], v[108:111], v[52:55]\n\t"
        "v_max_u32 v151, v151, v133\n\t"
        "v_xor_b32 v134, v181, v134\n\t"
        "v_mfma_f32_16x16x32_bf16 v[56:59], a[68:71], v[108:111], v[56:59]\n\t"
        "s_waitcnt lgkmcnt(3)\n\t"
        "buffer_store_dwordx4 v[124:127], v145, s[28:31], s50 offen sc1\n\t"
        "v_mfma_f32_16x16x32_bf16 v[48:51], a[96:99], v[112:115], v[48:51]\n\t"
        "ds_read_b128 v[124:127], v147 offset:4096\n\t"
        "s_add_u32 s50, s50, 86016\n\t"
        "v_mfma_f32_16x16x32_bf16 v[52:55], a[100:103], v[112:115], v[52:55]\n\t"
        "ds_read_b128 v[108:111], v183 offset:0\n\t"
        "v_mfma_f32_16x16x32_bf16 v[56:59], a[104:107], v[112:115], v[56:59]\n\t"
        "v_mfma_f32_16x16x32_bf16 v[48:51], a[132:135], v[84:87], v[48:51]\n\t"
        "ds_read_b128 v[112:115], v183 offset:64\n\t"
        "v_mfma_f32_16x16x32_bf16 v[52:55], a[136:139], v[84:87], v[52:55]\n\t"
        "v_mfma_f32_16x16x32_bf16 v[56:59], a[140:143], v[84:87], v[56:59]\n\t"
        "v_mfma_f32_16x16x32_bf16 v[48:51], a[168:171], v[88:91], v[48:51]\n\t"
        "ds_read_b128 v[84:87], v183 offset:128\n\t"
        "v_add_u32 v183, s9, v174\n\t"
        "v_mfma_f32_16x16x32_bf16 v[52:55], a[172:175], v[88:91], v[52:55]\n\t"
        "v_max_u32 v183, v183, v134\n\t"
        "v_xor_b32 v135, v181, v135\n\t"
        "v_mfma_f32_16x16x32_bf16 v[56:59], a[176:179], v[88:91], v[56:59]\n\t"
        "v_mfma_f32_16x16x32_bf16 v[48:51], a[204:207], v[92:95], v[48:51]\n\t"
        "ds_read_b128 v[88:91], v151 offset:0\n\t"
        "v_mfma_f32_16x16x32_bf16 v[52:55], a[208:211], v[92:95], v[52:55]\n\t"
        "v_mfma_f32_16x16x32_bf16 v[56:59], a[212:215], v[92:95], v[56:59]\n\t"
        "s_waitcnt lgkmcnt(3)\n\t"
        "v_mfma_f32_16x16x32_bf16 v[48:51], a[240:243], v[96:99], v[48:51]\n\t"
        "ds_read_b128 v[92:95], v151 offset:64\n\t"
        "v_mfma_f32_16x16x32_bf16 v[52:55], a[244:247], v[96:99], v[52:55]\n\t"
        "buffer_store_dwordx4 v[124:127], v145, s[28:31], s50 offen sc1\n\t"
        "v_mfma_f32_16x16x32_bf16 v[56:59], a[248:251], v[96:99], v[56:59]\n\t"
        "v_mfma_f32_16x16x32_bf16 v[48:51], v[208:211], v[100:103], v[48:51]\n\t"
        "ds_read_b128 v[96:99], v151 offset:128\n\t"
        "v_add_u32 v151, s9, v175\n\t"
        "v_mfma_f32_16x16x32_bf16 v[52:55], v[212:215], v[100:103], v[52:55]\n\t"
        "v_max_u32 v151, v151, v135\n\t"
        "v_xor_b32 v136, v181, v136\n\t"
        "v_mfma_f32_16x16x32_bf16 v[56:59], v[216:219], v[100:103], v[56:59]\n\t"
        "ds_read_b128 v[132:135], v187 offset:4096\n\t"
        "v_mfma_f32_16x16x32_bf16 v[48:51], v[244:247], v[104:107], v[48:51]\n\t"
        "ds_read_b128 v[100:103], v183 offset:0\n\t"
        "v_mfma_f32_16x16x32_bf16 v[52:55], v[248:251], v[104:107], v[52:55]\n\t"
        "v_mfma_f32_16x16x32_bf16 v[56:59], v[252:255], v[104:107], v[56:59]\n\t"
        "v_mfma_f32_16x16x32_bf16 v[60:63], a[12:15], v[108:111], 0\n\t"
        "ds_read_b128 v[104:107], v183 offset:64\n\t"
        "v_mfma_f32_16x16x32_bf16 v[64:67], a[16:19], v[108:111], 0\n\t"
        "v_mfma_f32_16x16x32_bf16 v[68:71], a[20:23], v[108:111], 0\n\t"
        "s_waitcnt lgkmcnt(4)\n\t"
        "v_mfma_f32_16x16x32_bf16 v[60:63], a[48:51], v[112:115], v[60:63]\n\t"
        "ds_read_b128 v[108:111], v183 offset:128\n\t"
        "v_add_u32 v183, s9, v176\n\t"
        "v_mfma_f32_16x16x32_bf16 v[64:67], a[52:55], v[112:115], v[64:67]\n\t"
        "v_max_u32 v183, v183, v136\n\t"
        "v_xor_b32 v137, v181, v137\n\t"
        "v_mfma_f32_16x16x32_bf16 v[68:71], a[56:59], v[112:115], v[68:71]\n\t"
        "v_readlane_b32 s55, v155, 49\n\t"
        "v_cvt_pk_bf16_f32 v116, v48, v49\n\t"
        "v_mfma_f32_16x16x32_bf16 v[60:63], a[84:87], v[84:87], v[60:63]\n\t"
        "ds_read_b128 v[112:115], v151 offset:0\n\t"
        "v_cvt_pk_bf16_f32 v117, v50, v51\n\t"
        "v_mfma_f32_16x16x32_bf16 v[64:67], a[88:91], v[84:87], v[64:67]\n\t"
        "v_cvt_pk_bf16_f32 v118, v52, v53\n\t"
        "v_cvt_pk_bf16_f32 v119, v54, v55\n\t"
        "v_mfma_f32_16x16x32_bf16 v[68:71], a[92:95], v[84:87], v[68:71]\n\t"
        "v_cvt_pk_bf16_f32 v120, v56, v57\n\t"
        "v_cvt_pk_bf16_f32 v121, v58, v59\n\t"
        "v_mfma_f32_16x16x32_bf16 v[60:63], a[120:123], v[88:91], v[60:63]\n\t"
        "v_permlane16_swap_b32 v116, v118\n\t"
        "v_mfma_f32_16x16x32_bf16 v[64:67], a[124:127], v[88:91], v[64:67]\n\t"
        "v_permlane16_swap_b32 v117, v119\n\t"
        "v_mfma_f32_16x16x32_bf16 v[68:71], a[128:131], v[88:91], v[68:71]\n\t"
        "buffer_store_dwordx4 v[116:119], v148, s[28:31], s55 offen sc1\n\t"
        "v_mfma_f32_16x16x32_bf16 v[60:63], a[156:159], v[92:95], v[60:63]\n\t"
        "ds_write_b64 v146, v[120:121] offset:8192\n\t"
        "v_mfma_f32_16x16x32_bf16 v[64:67], a[160:163], v[92:95], v[64:67]\n\t"
        "ds_read_b128 v[84:87], v151 offset:64\n\t"
        "v_mfma_f32_16x16x32_bf16 v[68:71], a[164:167], v[92:95], v[68:71]\n\t"
        "ds_read_b128 v[88:91], v151 offset:128\n\t"
        "v_add_u32 v151, s9, v177\n\t"
        "s_waitcnt lgkmcnt(4)\n\t"
        "v_mfma_f32_16x16x32_bf16 v[60:63], a[192:195], v[96:99], v[60:63]\n\t"
        "ds_read_b128 v[92:95], v183 offset:0\n\t"
        "v_max_u32 v151, v151, v137\n\t"
        "v_mfma_f32_16x16x32_bf16 v[64:67], a[196:199], v[96:99], v[64:67]\n\t"
        "v_xor_b32 v138, v181, v138\n\t"
        "v_mfma_f32_16x16x32_bf16 v[68:71], a[200:203], v[96:99], v[68:71]\n\t"
        "v_mfma_f32_16x16x32_bf16 v[60:63], a[228:231], v[100:103], v[60:63]\n\t"
        "s_waitcnt lgkmcnt(3)\n\t"
        "s_barrier\n\t"
        "v_mfma_f32_16x16x32_bf16 v[64:67], a[232:235], v[100:103], v[64:67]\n\t"
        "ds_read_b128 v[124:127], v147 offset:8192\n\t"
        "v_mfma_f32_16x16x32_bf16 v[68:71], a[236:239], v[100:103], v[68:71]\n\t"
        "ds_read_b128 v[96:99], v183 offset:64\n\t"
        "v_mfma_f32_16x16x32_bf16 v[60:63], v[196:199], v[104:107], v[60:63]\n\t"
        "ds_read_b128 v[100:103], v183 offset:128\n\t"
        "v_add_u32 v183, s9, v178\n\t"
        "v_mfma_f32_16x16x32_bf16 v[64:67], v[200:203], v[104:107], v[64:67]\n\t"
        "v_max_u32 v183, v183, v138\n\t"
        "v_xor_b32 v139, v181, v139\n\t"
        "v_mfma_f32_16x16x32_bf16 v[68:71], v[204:207], v[104:107], v[68:71]\n\t"
        "v_mfma_f32_16x16x32_bf16 v[60:63], v[232:235], v[108:111], v[60:63]\n\t"
        "ds_read_b128 v[104:107], v151 offset:0\n\t"
        "v_mfma_f32_16x16x32_bf16 v[64:67], v[236:239], v[108:111], v[64:67]\n\t"
        "v_mfma_f32_16x16x32_bf16 v[68:71], v[240:243], v[108:111], v[68:71]\n\t"
        "v_mfma_f32_16x16x32_bf16 v[60:63], a[0:3], v[112:115], v[60:63]\n\t"
        "ds_read_b128 v[108:111], v151 offset:64\n\t"
        "v_mfma_f32_16x16x32_bf16 v[64:67], a[4:7], v[112:115], v[64:67]\n\t"
        "s_waitcnt lgkmcnt(4)\n\t"
        "buffer_store_dwordx4 v[124:127], v150, s[28:31], s51 offen sc1\n\t"
        "v_mfma_f32_16x16x32_bf16 v[68:71], a[8:11], v[112:115], v[68:71]\n\t"
        "v_mfma_f32_16x16x32_bf16 v[60:63], a[36:39], v[84:87], v[60:63]\n\t"
        "ds_read_b128 v[112:115], v151 offset:128\n\t"
        "v_add_u32 v151, s9, v179\n\t"
        "v_mfma_f32_16x16x32_bf16 v[64:67], a[40:43], v[84:87], v[64:67]\n\t"
        "v_max_u32 v151, v151, v139\n\t"
        "v_xor_b32 v140, v181, v140\n\t"
        "v_mfma_f32_16x16x32_bf16 v[68:71], a[44:47], v[84:87], v[68:71]\n\t"
        "ds_read_b128 v[136:139], v187 offset:4112\n\t"
        "v_mfma_f32_16x16x32_bf16 v[60:63], a[72:75], v[88:91], v[60:63]\n\t"
        "ds_read_b128 v[84:87], v183 offset:0\n\t"
        "v_mfma_f32_16x16x32_bf16 v[64:67], a[76:79], v[88:91], v[64:67]\n\t"
        "v_mfma_f32_16x16x32_bf16 v[68:71], a[80:83], v[88:91], v[68:71]\n\t"
        "v_mfma_f32_16x16x32_bf16 v[60:63], a[108:111], v[92:95], v[60:63]\n\t"
        "ds_read_b128 v[88:91], v183 offset:64\n\t"
        "v_mfma_f32_16x16x32_bf16 v[64:67], a[112:115], v[92:95], v[64:67]\n\t"
        "v_mfma_f32_16x16x32_bf16 v[68:71], a[116:119], v[92:95], v[68:71]\n\t"
        "s_waitcnt lgkmcnt(4)\n\t"
        "v_mfma_f32_16x16x32_bf16 v[60:63], a[144:147], v[96:99], v[60:63]\n\t"
        "ds_read_b128 v[92:95], v183 offset:128\n\t"
        "v_add_u32 v183, s9, v180\n\t"
        "v_mfma_f32_16x16x32_bf16 v[64:67], a[148:151], v[96:99], v[64:67]\n\t"
        "v_max_u32 v183, v183, v140\n\t"
        "v_xor_b32 v132, v181, v132\n\t"
        "v_mfma_f32_16x16x32_bf16 v[68:71], a[152:155], v[96:99], v[68:71]\n\t"
        "ds_read_b32 v140, v187 offset:4128\n\t"
        "v_mfma_f32_16x16x32_bf16 v[60:63], a[180:183], v[100:103], v[60:63]\n\t"
        "ds_read_b128 v[96:99], v151 offset:0\n\t"
        "v_mfma_f32_16x16x32_bf16 v[64:67], a[184:187], v[100:103], v[64:67]\n\t"
        "v_mfma_f32_16x16x32_bf16 v[68:71], a[188:191], v[100:103], v[68:71]\n\t"
        "v_mfma_f32_16x16x32_bf16 v[60:63], a[216:219], v[104:107], v[60:63]\n\t"
        "ds_read_b128 v[100:103], v151 offset:64\n\t"
        "v_mfma_f32_16x16x32_bf16 v[64:67], a[220:223], v[104:107], v[64:67]\n\t"
        "v_mfma_f32_16x16x32_bf16 v[68:71], a[224:227], v[104:107], v[68:71]\n\t"
        "v_mfma_f32_16x16x32_bf16 v[60:63], a[252:255], v[108:111], v[60:63]\n\t"
        "ds_read_b128 v[104:107], v151 offset:128\n\t"
        "v_readlane_b32 s9, v155, 48\n\t"
        "v_mfma_f32_16x16x32_bf16 v[64:67], v[188:191], v[108:111], v[64:67]\n\t"
        "s_nop 0\n\t"
        "v_add_u32 v151, s9, v172\n\t"
        "v_max_u32 v151, v151, v132\n\t"
        "v_mfma_f32_16x16x32_bf16 v[68:71], v[192:195], v[108:111], v[68:71]\n\t"
        "v_xor_b32 v133, v181, v133\n\t"
        "s_waitcnt lgkmcnt(4)\n\t"
        "v_mfma_f32_16x16x32_bf16 v[60:63], v[220:223], v[112:115], v[60:63]\n\t"
        "ds_read_b128 v[108:111], v183 offset:0\n\t"
        "v_mfma_f32_16x16x32_bf16 v[64:67], v[224:227], v[112:115], v[64:67]\n\t"
        "v_mfma_f32_16x16x32_bf16 v[68:71], v[228:231], v[112:115], v[68:71]\n\t"
        "v_mfma_f32_16x16x32_bf16 v[60:63], a[24:27], v[84:87], v[60:63]\n\t"
        "ds_read_b128 v[112:115], v183 offset:64\n\t"
        "v_mfma_f32_16x16x32_bf16 v[64:67], a[28:31], v[84:87], v[64:67]\n\t"
        "v_mfma_f32_16x16x32_bf16 v[68:71], a[32:35], v[84:87], v[68:71]\n\t"
        "v_mfma_f32_16x16x32_bf16 v[60:63], a[60:63], v[88:91], v[60:63]\n\t"
        "ds_read_b128 v[84:87], v183 offset:128\n\t"
        "v_add_u32 v183, s9, v173\n\t"
        "v_mfma_f32_16x16x32_bf16 v[64:67], a[64:67], v[88:91], v[64:67]\n\t"
        "v_max_u32 v183, v183, v133\n\t"
        "v_xor_b32 v134, v181, v134\n\t"
        "v_mfma_f32_16x16x32_bf16 v[68:71], a[68:71], v[88:91], v[68:71]\n\t"
        "v_mfma_f32_16x16x32_bf16 v[60:63], a[96:99], v[92:95], v[60:63]\n\t"
        "ds_read_b128 v[88:91], v151 offset:0\n\t"
        "v_mfma_f32_16x16x32_bf16 v[64:67], a[100:103], v[92:95], v[64:67]\n\t"
        "v_mfma_f32_16x16x32_bf16 v[68:71], a[104:107], v[92:95], v[68:71]\n\t"
        "s_waitcnt lgkmcnt(3)\n\t"
        "v_mfma_f32_16x16x32_bf16 v[60:63], a[132:135], v[96:99], v[60:63]\n\t"
        "ds_read_b128 v[92:95], v151 offset:64\n\t"
        "v_mfma_f32_16x16x32_bf16 v[64:67], a[136:139], v[96:99], v[64:67]\n\t"
        "v_mfma_f32_16x16x32_bf16 v[68:71], a[140:143], v[96:99], v[68:71]\n\t"
        "v_mfma_f32_16x16x32_bf16 v[60:63], a[168:171], v[100:103], v[60:63]\n\t"
        "ds_read_b128 v[96:99], v151 offset:128\n\t"
        "v_add_u32 v151, s9, v174\n\t"
        "v_mfma_f32_16x16x32_bf16 v[64:67], a[172:175], v[100:103], v[64:67]\n\t"
        "v_max_u32 v151, v151, v134\n\t"
        "v_xor_b32 v135, v181, v135\n\t"
        "v_mfma_f32_16x16x32_bf16 v[68:71], a[176:179], v[100:103], v[68:71]\n\t"
        "v_mfma_f32_16x16x32_bf16 v[60:63], a[204:207], v[104:107], v[60:63]\n\t"
        "ds_read_b128 v[100:103], v183 offset:0\n\t"
        "v_mfma_f32_16x16x32_bf16 v[64:67], a[208:211], v[104:107], v[64:67]\n\t"
        "v_mfma_f32_16x16x32_bf16 v[68:71], a[212:215], v[104:107], v[68:71]\n\t"
        "v_mfma_f32_16x16x32_bf16 v[60:63], a[240:243], v[108:111], v[60:63]\n\t"
        "ds_read_b128 v[104:107], v183 offset:64\n\t"
        "v_mfma_f32_16x16x32_bf16 v[64:67], a[244:247], v[108:111], v[64:67]\n\t"
        "v_mfma_f32_16x16x32_bf16 v[68:71], a[248:251], v[108:111], v[68:71]\n\t"
        "s_waitcnt lgkmcnt(3)\n\t"
        "v_mfma_f32_16x16x32_bf16 v[60:63], v[208:211], v[112:115], v[60:63]\n\t"
        "ds_read_b128 v[108:111], v183 offset:128\n\t"
        "v_add_u32 v183, s9, v175\n\t"
        "v_mfma_f32_16x16x32_bf16 v[64:67], v[212:215], v[112:115], v[64:67]\n\t"
        "v_max_u32 v183, v183, v135\n\t"
        "v_xor_b32 v136, v181, v136\n\t"
        "v_mfma_f32_16x16x32_bf16 v[68:71], v[216:219], v[112:115], v[68:71]\n\t"
        "v_mfma_f32_16x16x32_bf16 v[60:63], v[244:247], v[84:87], v[60:63]\n\t"
        "ds_read_b128 v[112:115], v151 offset:0\n\t"
        "v_mfma_f32_16x16x32_bf16 v[64:67], v[248:251], v[84:87], v[64:67]\n\t"
        "v_mfma_f32_16x16x32_bf16 v[68:71], v[252:255], v[84:87], v[68:71]\n\t"
        "v_mfma_f32_16x16x32_bf16 v[72:75], a[12:15], v[88:91], 0\n\t"
        "ds_read_b128 v[84:87], v151 offset:64\n\t"
        "v_mfma_f32_16x16x32_bf16 v[76:79], a[16:19], v[88:91], 0\n\t"
        "v_mfma_f32_16x16x32_bf16 v[80:83], a[20:23], v[88:91], 0\n\t"
        "v_mfma_f32_16x16x32_bf16 v[72:75], a[48:51], v[92:95], v[72:75]\n\t"
        "ds_read_b128 v[88:91], v151 offset:128\n\t"
        "v_add_u32 v151, s9, v176\n\t"
        "v_mfma_f32_16x16x32_bf16 v[76:79], a[52:55], v[92:95], v[76:79]\n\t"
        "v_max_u32 v151, v151, v136\n\t"
        "v_xor_b32 v137, v181, v137\n\t"
        "v_mfma_f32_16x16x32_bf16 v[80:83], a[56:59], v[92:95], v[80:83]\n\t"
        "v_readlane_b32 s55, v155, 50\n\t"
        "v_cvt_pk_bf16_f32 v116, v60, v61\n\t"
        "s_waitcnt lgkmcnt(3)\n\t"
        "v_mfma_f32_16x16x32_bf16 v[72:75], a[84:87], v[96:99], v[72:75]\n\t"
        "ds_read_b128 v[92:95], v183 offset:0\n\t"
        "v_cvt_pk_bf16_f32 v117, v62, v63\n\t"
        "v_mfma_f32_16x16x32_bf16 v[76:79], a[88:91], v[96:99], v[76:79]\n\t"
        "v_cvt_pk_bf16_f32 v118, v64, v65\n\t"
        "v_cvt_pk_bf16_f32 v119, v66, v67\n\t"
        "v_mfma_f32_16x16x32_bf16 v[80:83], a[92:95], v[96:99], v[80:83]\n\t"
        "v_cvt_pk_bf16_f32 v120, v68, v69\n\t"
        "v_cvt_pk_bf16_f32 v121, v70, v71\n\t"
        "v_mfma_f32_16x16x32_bf16 v[72:75], a[120:123], v[100:103], v[72:75]\n\t"
        "v_permlane16_swap_b32 v116, v118\n\t"
        "v_mfma_f32_16x16x32_bf16 v[76:79], a[124:127], v[100:103], v[76:79]\n\t"
        "v_permlane16_swap_b32 v117, v119\n\t"
        "v_mfma_f32_16x16x32_bf16 v[80:83], a[128:131], v[100:103], v[80:83]\n\t"
        "buffer_store_dwordx4 v[116:119], v148, s[28:31], s55 offen sc1\n\t"
        "v_mfma_f32_16x16x32_bf16 v[72:75], a[156:159], v[104:107], v[72:75]\n\t"
        "ds_write_b64 v146, v[120:121] offset:0\n\t"
        "v_mfma_f32_16x16x32_bf16 v[76:79], a[160:163], v[104:107], v[76:79]\n\t"
        "ds_read_b128 v[96:99], v183 offset:64\n\t"
        "v_mfma_f32_16x16x32_bf16 v[80:83], a[164:167], v[104:107], v[80:83]\n\t"
        "ds_read_b128 v[100:103], v183 offset:128\n\t"
        "v_add_u32 v183, s9, v177\n\t"
        "v_mfma_f32_16x16x32_bf16 v[72:75], a[192:195], v[108:111], v[72:75]\n\t"
        "ds_read_b128 v[104:107], v151 offset:0\n\t"
        "v_max_u32 v183, v183, v137\n\t"
        "v_mfma_f32_16x16x32_bf16 v[76:79], a[196:199], v[108:111], v[76:79]\n\t"
        "v_xor_b32 v138, v181, v138\n\t"
        "v_mfma_f32_16x16x32_bf16 v[80:83], a[200:203], v[108:111], v[80:83]\n\t"
        "s_waitcnt lgkmcnt(4)\n\t"
        "v_mfma_f32_16x16x32_bf16 v[72:75], a[228:231], v[112:115], v[72:75]\n\t"
        "ds_read_b128 v[108:111], v151 offset:64\n\t"
        "v_mfma_f32_16x16x32_bf16 v[76:79], a[232:235], v[112:115], v[76:79]\n\t"
        "v_mfma_f32_16x16x32_bf16 v[80:83], a[236:239], v[112:115], v[80:83]\n\t"
        "v_mfma_f32_16x16x32_bf16 v[72:75], v[196:199], v[84:87], v[72:75]\n\t"
        "ds_read_b128 v[112:115], v151 offset:128\n\t"
        "v_add_u32 v151, s9, v178\n\t"
        "v_mfma_f32_16x16x32_bf16 v[76:79], v[200:203], v[84:87], v[76:79]\n\t"
        "v_max_u32 v151, v151, v138\n\t"
        "v_xor_b32 v139, v181, v139\n\t"
        "v_mfma_f32_16x16x32_bf16 v[80:83], v[204:207], v[84:87], v[80:83]\n\t"
        "v_mfma_f32_16x16x32_bf16 v[72:75], v[232:235], v[88:91], v[72:75]\n\t"
        "ds_read_b128 v[84:87], v183 offset:0\n\t"
        "v_mfma_f32_16x16x32_bf16 v[76:79], v[236:239], v[88:91], v[76:79]\n\t"
        "v_mfma_f32_16x16x32_bf16 v[80:83], v[240:243], v[88:91], v[80:83]\n\t"
        "v_mfma_f32_16x16x32_bf16 v[72:75], a[0:3], v[92:95], v[72:75]\n\t"
        "ds_read_b128 v[88:91], v183 offset:64\n\t"
        "v_mfma_f32_16x16x32_bf16 v[76:79], a[4:7], v[92:95], v[76:79]\n\t"
        "v_mfma_f32_16x16x32_bf16 v[80:83], a[8:11], v[92:95], v[80:83]\n\t"
        "s_waitcnt lgkmcnt(3)\n\t"
        "v_mfma_f32_16x16x32_bf16 v[72:75], a[36:39], v[96:99], v[72:75]\n\t"
        "ds_read_b128 v[92:95], v183 offset:128\n\t"
        "v_add_u32 v183, s9, v179\n\t"
        "v_mfma_f32_16x16x32_bf16 v[76:79], a[40:43], v[96:99], v[76:79]\n\t"
        ".LpS2%=:\n\t"
        "s_cbranch_execz .LrS%=\n\t"
        "v_max_u32 v183, v183, v139\n\t"
        "v_xor_b32 v140, v181, v140\n\t"
        "v_mfma_f32_16x16x32_bf16 v[80:83], a[44:47], v[96:99], v[80:83]\n\t"
        "v_mfma_f32_16x16x32_bf16 v[72:75], a[72:75], v[100:103], v[72:75]\n\t"
        "ds_read_b128 v[96:99], v151 offset:0\n\t"
        "v_mfma_f32_16x16x32_bf16 v[76:79], a[76:79], v[100:103], v[76:79]\n\t"
        "v_mfma_f32_16x16x32_bf16 v[80:83], a[80:83], v[100:103], v[80:83]\n\t"
        "v_mfma_f32_16x16x32_bf16 v[72:75], a[108:111], v[104:107], v[72:75]\n\t"
        "ds_read_b128 v[100:103], v151 offset:64\n\t"
        "v_mfma_f32_16x16x32_bf16 v[76:79], a[112:115], v[104:107], v[76:79]\n\t"
        "v_mfma_f32_16x16x32_bf16 v[80:83], a[116:119], v[104:107], v[80:83]\n\t"
        "ds_read_b128 v[104:107], v151 offset:128\n\t"
        "v_add_u32 v151, s9, v180\n\t"
        "v_mfma_f32_16x16x32_bf16 v[72:75], a[144:147], v[108:111], v[72:75]\n\t"
        "v_max_u32 v151, v151, v140\n\t"
        "v_mfma_f32_16x16x32_bf16 v[76:79], a[148:151], v[108:111], v[76:79]\n\t"
        "v_mfma_f32_16x16x32_bf16 v[80:83], a[152:155], v[108:111], v[80:83]\n\t"
        "ds_read_b128 v[108:111], v183 offset:0\n\t"
        "s_waitcnt lgkmcnt(4)\n\t"
        "v_mfma_f32_16x16x32_bf16 v[72:75], a[180:183], v[112:115], v[72:75]\n\t"
        "v_mfma_f32_16x16x32_bf16 v[76:79], a[184:187], v[112:115], v[76:79]\n\t"
        "v_mfma_f32_16x16x32_bf16 v[80:83], a[188:191], v[112:115], v[80:83]\n\t"
        "ds_read_b128 v[112:115], v183 offset:64\n\t"
        "v_mfma_f32_16x16x32_bf16 v[72:75], a[216:219], v[84:87], v[72:75]\n\t"
        "v_mfma_f32_16x16x32_bf16 v[76:79], a[220:223], v[84:87], v[76:79]\n\t"
        "v_mfma_f32_16x16x32_bf16 v[80:83], a[224:227], v[84:87], v[80:83]\n\t"
        "ds_read_b128 v[84:87], v183 offset:128\n\t"
        "v_mfma_f32_16x16x32_bf16 v[72:75], a[252:255], v[88:91], v[72:75]\n\t"
        "v_mfma_f32_16x16x32_bf16 v[76:79], v[188:191], v[88:91], v[76:79]\n\t"
        "v_mfma_f32_16x16x32_bf16 v[80:83], v[192:195], v[88:91], v[80:83]\n\t"
        "ds_read_b128 v[88:91], v151 offset:0\n\t"
        "v_mfma_f32_16x16x32_bf16 v[72:75], v[220:223], v[92:95], v[72:75]\n\t"
        "v_mfma_f32_16x16x32_bf16 v[76:79], v[224:227], v[92:95], v[76:79]\n\t"
        "v_mfma_f32_16x16x32_bf16 v[80:83], v[228:231], v[92:95], v[80:83]\n\t"
        "ds_read_b128 v[92:95], v151 offset:64\n\t"
        "s_waitcnt lgkmcnt(4)\n\t"
        "v_mfma_f32_16x16x32_bf16 v[72:75], a[24:27], v[96:99], v[72:75]\n\t"
        "v_mfma_f32_16x16x32_bf16 v[76:79], a[28:31], v[96:99], v[76:79]\n\t"
        "v_mfma_f32_16x16x32_bf16 v[80:83], a[32:35], v[96:99], v[80:83]\n\t"
        "ds_read_b128 v[96:99], v151 offset:128\n\t"
        "v_mfma_f32_16x16x32_bf16 v[80:83], a[68:71], v[100:103], v[80:83]\n\t"
        "v_mfma_f32_16x16x32_bf16 v[80:83], a[104:107], v[104:107], v[80:83]\n\t"
        "v_mfma_f32_16x16x32_bf16 v[80:83], a[140:143], v[108:111], v[80:83]\n\t"
        "s_waitcnt lgkmcnt(3)\n\t"
        "v_mfma_f32_16x16x32_bf16 v[80:83], a[176:179], v[112:115], v[80:83]\n\t"
        "v_mfma_f32_16x16x32_bf16 v[80:83], a[212:215], v[84:87], v[80:83]\n\t"
        "s_waitcnt lgkmcnt(2)\n\t"
        "v_mfma_f32_16x16x32_bf16 v[80:83], a[248:251], v[88:91], v[80:83]\n\t"
        "s_waitcnt lgkmcnt(1)\n\t"
        "v_mfma_f32_16x16x32_bf16 v[80:83], v[216:219], v[92:95], v[80:83]\n\t"
        "s_waitcnt lgkmcnt(0)\n\t"
        "v_mfma_f32_16x16x32_bf16 v[80:83], v[252:255], v[96:99], v[80:83]\n\t"
        "s_nop 2\n\t"
        "v_mfma_f32_16x16x32_bf16 v[72:75], a[60:63], v[100:103], v[72:75]\n\t"
        "s_nop 2\n\t"
        "v_mfma_f32_16x16x32_bf16 v[72:75], a[96:99], v[104:107], v[72:75]\n\t"
        "v_cvt_pk_bf16_f32 v120, v80, v81\n\t"
        "v_cvt_pk_bf16_f32 v121, v82, v83\n\t"
        "v_mfma_f32_16x16x32_bf16 v[72:75], a[132:135], v[108:111], v[72:75]\n\t"
        "ds_write_b64 v146, v[120:121] offset:2048\n\t"
        "v_mfma_f32_16x16x32_bf16 v[72:75], a[168:171], v[112:115], v[72:75]\n\t"
        "v_mfma_f32_16x16x32_bf16 v[72:75], a[204:207], v[84:87], v[72:75]\n\t"
        "v_mfma_f32_16x16x32_bf16 v[72:75], a[240:243], v[88:91], v[72:75]\n\t"
        "s_waitcnt lgkmcnt(0)\n\t"
        "s_barrier\n\t"
        "v_mfma_f32_16x16x32_bf16 v[72:75], v[208:211], v[92:95], v[72:75]\n\t"
        "ds_read_b128 v[124:127], v147 offset:0\n\t"
        "v_readlane_b32 s55, v155, 51\n\t"
        "v_mfma_f32_16x16x32_bf16 v[72:75], v[244:247], v[96:99], v[72:75]\n\t"
        "s_nop 1\n\t"
        "v_mfma_f32_16x16x32_bf16 v[76:79], a[64:67], v[100:103], v[76:79]\n\t"
        "s_nop 1\n\t"
        "v_mfma_f32_16x16x32_bf16 v[76:79], a[100:103], v[104:107], v[76:79]\n\t"
        "v_mfma_f32_16x16x32_bf16 v[76:79], a[136:139], v[108:111], v[76:79]\n\t"
        "v_mfma_f32_16x16x32_bf16 v[76:79], a[172:175], v[112:115], v[76:79]\n\t"
        "v_mfma_f32_16x16x32_bf16 v[76:79], a[208:211], v[84:87], v[76:79]\n\t"
        "v_cvt_pk_bf16_f32 v116, v72, v73\n\t"
        "v_cvt_pk_bf16_f32 v117, v74, v75\n\t"
        "v_mfma_f32_16x16x32_bf16 v[76:79], a[244:247], v[88:91], v[76:79]\n\t"
        "s_waitcnt lgkmcnt(0)\n\t"
        "buffer_store_dwordx4 v[124:127], v150, s[28:31], s52 offen sc1\n\t"
        "v_mfma_f32_16x16x32_bf16 v[76:79], v[212:215], v[92:95], v[76:79]\n\t"
        "v_mfma_f32_16x16x32_bf16 v[76:79], v[248:251], v[96:99], v[76:79]\n\t"
        "s_nop 7\n\t"
        "v_cvt_pk_bf16_f32 v118, v76, v77\n\t"
        "v_cvt_pk_bf16_f32 v119, v78, v79\n\t"
        "s_nop 0\n\t"
        "v_permlane16_swap_b32 v116, v118\n\t"
        "v_permlane16_swap_b32 v117, v119\n\t"
        "buffer_store_dwordx4 v[116:119], v148, s[28:31], s55 offen sc1\n\t"
        "s_andn2_b32 s6, s6, 2\n\t"
        "s_cbranch_scc1 99f\n\t"
        "s_endpgm\n\t"
        "21:\n\t"
        "s_bitcmp1_b32 s7, 15\n\t"
        "s_cbranch_scc1 22f\n\t"
        "ds_bpermute_b32 v64, v65, v155 offset:172\n\t"
        "s_add_u32 m0, s37, -24576\n\t"
        "s_add_u32 s36, s36, 6144\n\t"
        "buffer_load_dwordx4 v140, s[24:27], s36 offen offset:0 lds\n\t"
        "ds_bpermute_b32 v78, v81, v155 offset:148\n\t"
        "ds_bpermute_b32 v79, v81, v155 offset:160\n\t"
        "v_readlane_b32 s71, v155, 26\n\t"
        "v_readlane_b32 s10, v155, 29\n\t"
        "s_nop 0\n\t"
        "v_mul_u32_u24 v73, s71, v66\n\t"
        "v_lshrrev_b32 v73, 11, v73\n\t"
        "v_lshlrev_b32 v66, 4, v66\n\t"
        "v_mad_u32_u24 v73, v73, s10, v66\n\t"
        "s_waitcnt lgkmcnt(2)\n\t"
        "v_add_u32 v64, v64, v73\n\t"
        "buffer_load_dwordx4 v141, s[24:27], s36 offen offset:1024 lds\n\t"
        "v_readlane_b32 s69, v155, 34\n\t"
        "v_readlane_b32 s70, v155, 35\n\t"
        "s_nop 0\n\t"
        "v_mad_u32_u24 v70, v52, s69, v70\n\t"
        "v_mad_u32_u24 v70, v158, s70, v70\n\t"
        "s_mul_i32 s71, s69, s68\n\t"
        "s_add_u32 s71, s71, 157728\n\t"
        "v_mad_u32_u24 v67, v82, s70, v67\n\t"
        "v_add_u32 v67, s71, v67\n\t"
        "v_readlane_b32 s11, v155, 30\n\t"
        "buffer_load_dwordx4 v142, s[24:27], s36 offen offset:2048 lds\n\t"
        "s_movk_i32 s10, 223\n\t"
        "v_mov_b32 v75, 0x7ff00000\n\t"
        "v_readlane_b32 s12, v155, 24\n\t"
        "v_readlane_b32 s13, v155, 25\n\t"
        "s_nop 0\n\t"
        "s_bfe_u32 s70, s12, 524288\n\t"
        "s_bfe_u32 s71, s13, 524288\n\t"
        "v_cmp_gt_u32 vcc, s71, v71\n\t"
        "v_add3_u32 v73, v71, s70, -2\n\t"
        "v_med3_i32 v73, v73, 0, s10\n\t"
        "s_add_u32 m0, s35, 4096\n\t"
        "s_add_u32 s43, s34, 43008\n\t"
        "buffer_load_dwordx4 v143, s[16:19], s43 offen  lds\n\t"
        "v_cndmask_b32 v74, v75, v72, vcc\n\t"
        "v_mad_u32_u24 v134, v73, s11, v74\n\t"
        "s_bfe_u32 s70, s12, 524296\n\t"
        "s_bfe_u32 s71, s13, 524296\n\t"
        "v_cmp_gt_u32 vcc, s71, v71\n\t"
        "v_add3_u32 v73, v71, s70, -2\n\t"
        "v_med3_i32 v73, v73, 0, s10\n\t"
        "v_cndmask_b32 v74, v75, v72, vcc\n\t"
        "v_mad_u32_u24 v135, v73, s11, v74\n\t"
        "s_add_u32 m0, s35, 20480\n\t"
        "s_add_u32 s43, s34, 215040\n\t"
        "buffer_load_dwordx4 v143, s[16:19], s43 offen  lds\n\t"
        "s_bfe_u32 s70, s12, 524304\n\t"
        "s_bfe_u32 s71, s13, 524304\n\t"
        "v_cmp_gt_u32 vcc, s71, v71\n\t"
        "v_add3_u32 v73, v71, s70, -2\n\t"
        "v_med3_i32 v73, v73, 0, s10\n\t"
        "v_cndmask_b32 v74, v75, v72, vcc\n\t"
        "v_mad_u32_u24 v136, v73, s11, v74\n\t"
        "s_bfe_u32 s70, s12, 524312\n\t"
        "s_bfe_u32 s71, s13, 524312\n\t"
        "v_cmp_gt_u32 vcc, s71, v71\n\t"
        "v_add3_u32 v73, v71, s70, -2\n\t"
        "v_med3_i32 v73, v73, 0, s10\n\t"
        "v_cndmask_b32 v74, v75, v72, vcc\n\t"
        "v_mad_u32_u24 v137, v73, s11, v74\n\t"
        "s_mov_b64 exec, 0\n\t"
        "s_cmp_eq_u32 s4, 0\n\t"
        "s_cbranch_scc1 .LpX0%=\n\t"
        "s_cmp_eq_u32 s4, 1\n\t"
        "s_cbranch_scc1 .LpX1%=\n\t"
        "s_cmp_eq_u32 s4, 2\n\t"
        "s_cbranch_scc1 .LpX2%=\n\t"
        "s_branch .LwX%=\n\t"
        ".LrX%=:\n\t"
        "s_mov_b64 exec, -1\n\t"
        "s_waitcnt vmcnt(9)\n\t"
        "s_barrier\n\t"
        "ds_read_b64_tr_b16 a[0:1], v138 offset:6144\n\t"
        "ds_read_b64_tr_b16 a[2:3], v138 offset:6912\n\t"
        "ds_read_b64_tr_b16 a[4:5], v139 offset:6144\n\t"
        "ds_read_b64_tr_b16 a[6:7], v139 offset:6912\n\t"
        "ds_read_b64_tr_b16 a[8:9], v149 offset:6144\n\t"
        "ds_read_b64_tr_b16 a[10:11], v149 offset:6912\n\t"
        "ds_read_b128 v[84:87], v128 offset:8192\n\t"
        "ds_read_b64_tr_b16 a[12:13], v138 offset:12288\n\t"
        "ds_read_b64_tr_b16 a[14:15], v138 offset:13056\n\t"
        "ds_read_b64_tr_b16 a[16:17], v139 offset:12288\n\t"
        "ds_read_b64_tr_b16 a[18:19], v139 offset:13056\n\t"
        "ds_read_b64_tr_b16 a[20:21], v149 offset:12288\n\t"
        "ds_read_b64_tr_b16 a[22:23], v149 offset:13056\n\t"
        "ds_read_b128 v[88:91], v128 offset:8192\n\t"
        "s_add_u32 m0, s37, -12288\n\t"
        "s_add_u32 s36, s36, s39\n\t"
        "s_waitcnt lgkmcnt(7)\n\t"
        "v_mfma_f32_16x16x32_bf16 v[12:15], a[0:3], v[84:87], 0\n\t"
        "buffer_load_dwordx4 v140, s[24:27], s36 offen offset:0 lds\n\t"
        "buffer_load_dwordx4 v141, s[24:27], s36 offen offset:1024 lds\n\t"
        "buffer_load_dwordx4 v142, s[24:27], s36 offen offset:2048 lds\n\t"
        "s_add_u32 m0, s35, 16384\n\t"
        "s_add_u32 s43, s34, 172032\n\t"
        "buffer_load_dwordx4 v143, s[16:19], s43 offen  lds\n\t"
        "s_add_u32 m0, s35, 32768\n\t"
        "s_add_u32 s43, s34, 344064\n\t"
        "s_min_u32 s43, s43, 9590784\n\t"
        "buffer_load_dwordx4 v143, s[16:19], s43 offen  lds\n\t"
        "s_waitcnt vmcnt(10)\n\t"
        "s_barrier\n\t"
        "ds_read_b128 v[92:95], v128 offset:12288\n\t"
        "ds_read_b64_tr_b16 a[24:25], v138 offset:0\n\t"
        "v_mfma_f32_16x16x32_bf16 v[16:19], a[4:7], v[84:87], 0\n\t"
        "ds_read_b64_tr_b16 a[26:27], v138 offset:768\n\t"
        "s_add_u32 m0, s35, 157696\n\t"
        "s_nop 0\n\t"
        "buffer_load_dwordx4 v64, s[20:23], 0 offen lds\n\t"
        "s_add_u32 m0, s37, 0\n\t"
        "s_add_u32 s36, s36, s38\n\t"
        "v_mfma_f32_16x16x32_bf16 v[20:23], a[8:11], v[84:87], 0\n\t"
        "buffer_load_dwordx4 v140, s[24:27], s36 offen offset:0 lds\n\t"
        "ds_read_b64_tr_b16 a[28:29], v139 offset:0\n\t"
        "v_readlane_b32 s44, v155, 32\n\t"
        "s_waitcnt lgkmcnt(4)\n\t"
        "v_mfma_f32_16x16x32_bf16 v[24:27], a[12:15], v[88:91], 0\n\t"
        "ds_read_b64_tr_b16 a[30:31], v139 offset:768\n\t"
        "ds_read_b64_tr_b16 a[32:33], v149 offset:0\n\t"
        "ds_read_b64_tr_b16 a[34:35], v149 offset:768\n\t"
        "v_readlane_b32 s45, v155, 33\n\t"
        "v_mfma_f32_16x16x32_bf16 v[28:31], a[16:19], v[88:91], 0\n\t"
        "buffer_load_dwordx4 v141, s[24:27], s36 offen offset:1024 lds\n\t"
        "ds_read_b128 v[96:99], v128 offset:8192\n\t"
        "v_readlane_b32 s62, v155, 36\n\t"
        "v_mfma_f32_16x16x32_bf16 v[32:35], a[20:23], v[88:91], 0\n\t"
        "ds_read_b128 v[100:103], v128 offset:12288\n\t"
        "s_lshl_b32 s70, s62, 5\n\t"
        "s_waitcnt lgkmcnt(8)\n\t"
        "v_mfma_f32_16x16x32_bf16 v[24:27], a[0:3], v[92:95], v[24:27]\n\t"
        "s_sub_u32 s71, s70, 16384\n\t"
        "s_mul_i32 s71, s71, s68\n\t"
        "v_mfma_f32_16x16x32_bf16 v[28:31], a[4:7], v[92:95], v[28:31]\n\t"
        "buffer_load_dwordx4 v142, s[24:27], s36 offen offset:2048 lds\n\t"
        "s_add_u32 m0, s35, 0\n\t"
        "s_add_u32 s43, s34, 0\n\t"
        "v_mfma_f32_16x16x32_bf16 v[32:35], a[8:11], v[92:95], v[32:35]\n\t"
        "s_mov_b32 exec_lo, 0\n\t"
        "s_mov_b32 exec_hi, 0xfff00000\n\t"
        "ds_write_b32 v119, v155\n\t"
        "s_mov_b64 exec, -1\n\t"
        "v_mfma_f32_16x16x32_bf16 v[36:39], a[12:15], v[92:95], 0\n\t"
        "v_add_u32 v172, s71, v128\n\t"
        "v_add_u32 v173, s71, v129\n\t"
        "v_mfma_f32_16x16x32_bf16 v[40:43], a[16:19], v[92:95], 0\n\t"
        "buffer_load_dwordx4 v143, s[16:19], s43 offen  lds\n\t"
        "s_add_u32 m0, s35, 36864\n\t"
        "s_add_u32 s43, s34, 387072\n\t"
        "s_min_u32 s43, s43, 9590784\n\t"
        "v_mfma_f32_16x16x32_bf16 v[44:47], a[20:23], v[92:95], 0\n\t"
        "buffer_load_dwordx4 v143, s[16:19], s43 offen  lds\n\t"
        "s_add_u32 m0, s37, 12288\n\t"
        "s_add_u32 s36, s36, s5\n\t"
        "buffer_load_dwordx4 v140, s[24:27], s36 offen offset:0 lds\n\t"
        "s_waitcnt vmcnt(9) lgkmcnt(3)\n\t"
        "s_barrier\n\t"
        "ds_read_b128 v[104:107], v128 offset:4096\n\t"
        "ds_read_b64_tr_b16 a[36:37], v138 offset:18432\n\t"
        "s_waitcnt lgkmcnt(3)\n\t"
        "v_mfma_f32_16x16x32_bf16 v[0:3], a[24:27], v[96:99], 0\n\t"
        "buffer_load_dwordx4 v141, s[24:27], s36 offen offset:1024 lds\n\t"
        "ds_read_b64_tr_b16 a[38:39], v138 offset:19200\n\t"
        "v_add_u32 v174, s71, v130\n\t"
        "v_mfma_f32_16x16x32_bf16 v[4:7], a[28:31], v[96:99], 0\n\t"
        "ds_read_b64_tr_b16 a[40:41], v139 offset:18432\n\t"
        "ds_read_b64_tr_b16 a[42:43], v139 offset:19200\n\t"
        "s_add_u32 s71, s71, s70\n\t"
        "v_mfma_f32_16x16x32_bf16 v[8:11], a[32:35], v[96:99], 0\n\t"
        "buffer_load_dwordx4 v142, s[24:27], s36 offen offset:2048 lds\n\t"
        "s_add_u32 m0, s37, 24576\n\t"
        "ds_read_b64_tr_b16 a[44:45], v149 offset:18432\n\t"
        "s_add_u32 s36, s36, s39\n\t"
        "v_mfma_f32_16x16x32_bf16 v[12:15], a[24:27], v[100:103], v[12:15]\n\t"
        "ds_read_b64_tr_b16 a[46:47], v149 offset:19200\n\t"
        "ds_read_b64_tr_b16 a[48:49], v138 offset:24576\n\t"
        "ds_read_b64_tr_b16 a[50:51], v138 offset:25344\n\t"
        "v_add_u32 v175, s71, v128\n\t"
        "v_mfma_f32_16x16x32_bf16 v[16:19], a[28:31], v[100:103], v[16:19]\n\t"
        "ds_read_b64_tr_b16 a[52:53], v139 offset:24576\n\t"
        "ds_read_b64_tr_b16 a[54:55], v139 offset:25344\n\t"
        "ds_read_b64_tr_b16 a[56:57], v149 offset:24576\n\t"
        "v_add_u32 v176, s71, v129\n\t"
        "v_mfma_f32_16x16x32_bf16 v[20:23], a[32:35], v[100:103], v[20:23]\n\t"
        "ds_read_b64_tr_b16 a[58:59], v149 offset:25344\n\t"
        "ds_read_b128 v[108:111], v128 offset:4160\n\t"
        "v_add_u32 v177, s71, v130\n\t"
        "s_add_u32 s71, s71, s70\n\t"
        "s_waitcnt lgkmcnt(12)\n\t"
        "v_mfma_f32_16x16x32_bf16 v[0:3], a[0:3], v[104:107], v[0:3]\n\t"
        "ds_read_b64_tr_b16 a[60:61], v138 offset:30720\n\t"
        "ds_read_b64_tr_b16 a[62:63], v138 offset:31488\n\t"
        "v_add_u32 v178, s71, v128\n\t"
        "v_mfma_f32_16x16x32_bf16 v[4:7], a[4:7], v[104:107], v[4:7]\n\t"
        "ds_read_b64_tr_b16 a[64:65], v139 offset:30720\n\t"
        "ds_read_b64_tr_b16 a[66:67], v139 offset:31488\n\t"
        "v_add_u32 v179, s71, v129\n\t"
        "v_mfma_f32_16x16x32_bf16 v[8:11], a[8:11], v[104:107], v[8:11]\n\t"
        "ds_read_b64_tr_b16 a[68:69], v149 offset:30720\n\t"
        "ds_read_b64_tr_b16 a[70:71], v149 offset:31488\n\t"
        "v_add_u32 v180, s71, v130\n\t"
        "v_mfma_f32_16x16x32_bf16 v[12:15], a[12:15], v[104:107], v[12:15]\n\t"
        "ds_read_b128 v[112:115], v128 offset:8256\n\t"
        "v_add_f32 v78, v78, v56\n\t"
        "v_mfma_f32_16x16x32_bf16 v[16:19], a[16:19], v[104:107], v[16:19]\n\t"
        "s_cmp_eq_u32 s69, 36\n\t"
        "s_cselect_b64 exec, -1, 0\n\t"
        "v_swap_b32 v68, v69\n\t"
        "v_swap_b32 v173, v175\n\t"
        "v_swap_b32 v174, v178\n\t"
        "v_swap_b32 v177, v179\n\t"
        "s_mov_b64 exec, -1\n\t"
        "v_mfma_f32_16x16x32_bf16 v[20:23], a[20:23], v[104:107], v[20:23]\n\t"
        "ds_read_b128 v[84:87], v128 offset:12352\n\t"
        "v_add3_u32 v68, v68, v52, -1\n\t"
        "v_add3_u32 v69, v69, v158, -1\n\t"
        "s_waitcnt lgkmcnt(8)\n\t"
        "v_mfma_f32_16x16x32_bf16 v[0:3], a[36:39], v[108:111], v[0:3]\n\t"
        "ds_read_b64_tr_b16 a[72:73], v138 offset:36864\n\t"
        "ds_read_b64_tr_b16 a[74:75], v138 offset:37632\n\t"
        "v_cvt_f32_i32 v68, v68\n\t"
        "v_mfma_f32_16x16x32_bf16 v[4:7], a[40:43], v[108:111], v[4:7]\n\t"
        "ds_read_b64_tr_b16 a[76:77], v139 offset:36864\n\t"
        "ds_read_b64_tr_b16 a[78:79], v139 offset:37632\n\t"
        "v_cvt_f32_i32 v69, v69\n\t"
        "v_mfma_f32_16x16x32_bf16 v[8:11], a[44:47], v[108:111], v[8:11]\n\t"
        "ds_read_b64_tr_b16 a[80:81], v149 offset:36864\n\t"
        "ds_read_b64_tr_b16 a[82:83], v149 offset:37632\n\t"
        "v_add_f32 v79, v79, v57\n\t"
        "v_mfma_f32_16x16x32_bf16 v[12:15], a[48:51], v[108:111], v[12:15]\n\t"
        "v_readlane_b32 s69, v155, 31\n\t"
        "v_mfma_f32_16x16x32_bf16 v[16:19], a[52:55], v[108:111], v[16:19]\n\t"
        "s_nop 0\n\t"
        "v_mov_b32 v154, s69\n\t"
        "v_mov_b32 v119, s62\n\t"
        "v_readlane_b32 s53, v155, 28\n\t"
        "v_mfma_f32_16x16x32_bf16 v[20:23], a[56:59], v[108:111], v[20:23]\n\t"
        "buffer_load_dwordx4 v140, s[24:27], s36 offen offset:0 lds\n\t"
        "v_readlane_b32 s9, v155, 46\n\t"
        "s_waitcnt lgkmcnt(7)\n\t"
        "v_mfma_f32_16x16x32_bf16 v[0:3], a[60:63], v[112:115], v[0:3]\n\t"
        "s_sub_u32 s10, 43200, s11\n\t"
        "s_mul_i32 s10, s10, s68\n\t"
        "v_mfma_f32_16x16x32_bf16 v[4:7], a[64:67], v[112:115], v[4:7]\n\t"
        "s_sub_u32 s11, s11, 192\n\t"
        "v_mad_u32_u24 v148, v158, s11, v144\n\t"
        "v_mfma_f32_16x16x32_bf16 v[8:11], a[68:71], v[112:115], v[8:11]\n\t"
        "v_add_u32 v148, s10, v148\n\t"
        "v_mad_u32_u24 v150, v83, s11, v145\n\t"
        "v_mfma_f32_16x16x32_bf16 v[12:15], a[36:39], v[112:115], v[12:15]\n\t"
        "s_lshl_b32 s70, s49, 1\n\t"
        "s_add_u32 s70, s70, 49\n\t"
        "v_mfma_f32_16x16x32_bf16 v[16:19], a[40:43], v[112:115], v[16:19]\n\t"
        "buffer_load_dwordx4 v141, s[24:27], s36 offen offset:1024 lds\n\t"
        "v_readlane_b32 s52, v155, s70\n\t"
        "v_mfma_f32_16x16x32_bf16 v[20:23], a[44:47], v[112:115], v[20:23]\n\t"
        "s_nop 0\n\t"
        "s_add_u32 s52, s52, s10\n\t"
        "v_mfma_f32_16x16x32_bf16 v[24:27], a[48:51], v[112:115], v[24:27]\n\t"
        "v_mfma_f32_16x16x32_bf16 v[28:31], a[52:55], v[112:115], v[28:31]\n\t"
        "v_mfma_f32_16x16x32_bf16 v[32:35], a[56:59], v[112:115], v[32:35]\n\t"
        "buffer_load_dwordx4 v142, s[24:27], s36 offen offset:2048 lds\n\t"
        "s_waitcnt lgkmcnt(0)\n\t"
        "v_mfma_f32_16x16x32_bf16 v[12:15], a[60:63], v[84:87], v[12:15]\n\t"
        "v_mfma_f32_16x16x32_bf16 v[16:19], a[64:67], v[84:87], v[16:19]\n\t"
        "v_mfma_f32_16x16x32_bf16 v[20:23], a[68:71], v[84:87], v[20:23]\n\t"
        "v_mfma_f32_16x16x32_bf16 v[24:27], a[36:39], v[84:87], v[24:27]\n\t"
        "buffer_load_dwordx4 v[48:51], v55, s[20:23], s48 offen\n\t"
        "ds_read_b128 v[88:91], v128 offset:4224\n\t"
        "v_mfma_f32_16x16x32_bf16 v[28:31], a[40:43], v[84:87], v[28:31]\n\t"
        "v_mfma_f32_16x16x32_bf16 v[32:35], a[44:47], v[84:87], v[32:35]\n\t"
        "v_mfma_f32_16x16x32_bf16 v[36:39], a[48:51], v[84:87], v[36:39]\n\t"
        "ds_read_b128 v[92:95], v128 offset:8320\n\t"
        "v_mfma_f32_16x16x32_bf16 v[40:43], a[52:55], v[84:87], v[40:43]\n\t"
        "buffer_load_dword v61, v61, s[20:23], s48 offen offset:512\n\t"
        "s_add_u32 m0, s37, 36864\n\t"
        "s_add_u32 s36, s36, s38\n\t"
        "v_mfma_f32_16x16x32_bf16 v[44:47], a[56:59], v[84:87], v[44:47]\n\t"
        "s_waitcnt lgkmcnt(1)\n\t"
        "v_mfma_f32_16x16x32_bf16 v[0:3], a[72:75], v[88:91], v[0:3]\n\t"
        "ds_read_b128 v[96:99], v128 offset:12416\n\t"
        "v_mfma_f32_16x16x32_bf16 v[4:7], a[76:79], v[88:91], v[4:7]\n\t"
        "s_waitcnt vmcnt(10)\n\t"
        "s_barrier\n\t"
        "ds_read_b128 v[100:103], v128 offset:16384\n\t"
        "ds_read_b128 v[104:107], v128 offset:16448\n\t"
        "v_mfma_f32_16x16x32_bf16 v[8:11], a[80:83], v[88:91], v[8:11]\n\t"
        "ds_read_b32 v64, v70 offset:0\n\t"
        "buffer_load_dwordx4 v140, s[24:27], s36 offen offset:0 lds\n\t"
        "v_readlane_b32 s46, v155, 37\n\t"
        "v_readlane_b32 s47, v155, 40\n\t"
        "s_waitcnt lgkmcnt(4)\n\t"
        "v_mfma_f32_16x16x32_bf16 v[12:15], a[72:75], v[92:95], v[12:15]\n\t"
        "ds_read_b32 v67, v67 offset:0\n\t"
        "ds_read_b64_tr_b16 a[84:85], v131 offset:0\n\t"
        "v_add_f32 v58, s46, v68\n\t"
        "v_mfma_f32_16x16x32_bf16 v[16:19], a[76:79], v[92:95], v[16:19]\n\t"
        "ds_read_b32 v66, v70 offset:2560\n\t"
        "ds_read_b64_tr_b16 a[86:87], v131 offset:768\n\t"
        "v_add_f32 v59, s47, v69\n\t"
        "v_readlane_b32 s47, v155, 42\n\t"
        "v_mfma_f32_16x16x32_bf16 v[20:23], a[80:83], v[92:95], v[20:23]\n\t"
        "ds_read_b64_tr_b16 a[88:89], v132 offset:0\n\t"
        "ds_read_b64_tr_b16 a[90:91], v132 offset:768\n\t"
        "s_waitcnt lgkmcnt(9)\n\t"
        "v_mfma_f32_16x16x32_bf16 v[24:27], a[72:75], v[96:99], v[24:27]\n\t"
        "buffer_load_dwordx4 v141, s[24:27], s36 offen offset:1024 lds\n\t"
        "ds_read_b64_tr_b16 a[92:93], v133 offset:0\n\t"
        "v_mfma_f32_16x16x32_bf16 v[28:31], a[76:79], v[96:99], v[28:31]\n\t"
        "ds_read_b64_tr_b16 a[94:95], v133 offset:768\n\t"
        "ds_read_b128 v[108:111], v128 offset:4224\n\t"
        "v_mfma_f32_16x16x32_bf16 v[32:35], a[80:83], v[96:99], v[32:35]\n\t"
        "ds_read_b64_tr_b16 a[96:97], v131 offset:6144\n\t"
        "ds_read_b64_tr_b16 a[98:99], v131 offset:6912\n\t"
        "s_waitcnt lgkmcnt(12)\n\t"
        "v_mfma_f32_16x16x32_bf16 v[24:27], a[24:27], v[100:103], v[24:27]\n\t"
        "ds_read_b64_tr_b16 a[100:101], v132 offset:6144\n\t"
        "ds_read_b64_tr_b16 a[102:103], v132 offset:6912\n\t"
        "v_mfma_f32_16x16x32_bf16 v[28:31], a[28:31], v[100:103], v[28:31]\n\t"
        "buffer_load_dwordx4 v142, s[24:27], s36 offen offset:2048 lds\n\t"
        "s_add_u32 m0, s37, 49152\n\t"
        "ds_read_b64_tr_b16 a[104:105], v133 offset:6144\n\t"
        "v_mfma_f32_16x16x32_bf16 v[32:35], a[32:35], v[100:103], v[32:35]\n\t"
        "ds_read_b64_tr_b16 a[106:107], v133 offset:6912\n\t"
        "s_add_u32 s36, s36, s5\n\t"
        "s_waitcnt lgkmcnt(12)\n\t"
        "v_perm_b32 v56, v64, v64, s44\n\t"
        "v_mfma_f32_16x16x32_bf16 v[36:39], a[0:3], v[100:103], v[36:39]\n\t"
        "ds_read_b128 v[112:115], v128 offset:8320\n\t"
        "v_perm_b32 v57, v64, v64, s45\n\t"
        "v_mfma_f32_16x16x32_bf16 v[40:43], a[4:7], v[100:103], v[40:43]\n\t"
        "ds_read_b128 v[84:87], v128 offset:12416\n\t"
        "v_add_f32 v56, v56, v58\n\t"
        "v_mfma_f32_16x16x32_bf16 v[44:47], a[8:11], v[100:103], v[44:47]\n\t"
        "buffer_load_dwordx4 v140, s[24:27], s36 offen offset:0 lds\n\t"
        "v_add_f32 v57, v57, v59\n\t"
        "v_cvt_pk_bf16_f32 v52, v56, v57\n\t"
        "v_mfma_f32_16x16x32_bf16 v[24:27], a[60:63], v[104:107], v[24:27]\n\t"
        "ds_read_b128 v[88:91], v128 offset:16512\n\t"
        "v_cvt_pk_bf16_f32 v58, v58, v59\n\t"
        "v_mfma_f32_16x16x32_bf16 v[28:31], a[64:67], v[104:107], v[28:31]\n\t"
        "v_cmp_ne_u32 s[64:65], v52, v58\n\t"
        "v_perm_b32 v57, v67, v67, s45\n\t"
        "v_mfma_f32_16x16x32_bf16 v[32:35], a[68:71], v[104:107], v[32:35]\n\t"
        "v_mbcnt_lo_u32_b32 v56, s64, 1\n\t"
        "v_mbcnt_hi_u32_b32 v56, s65, v56\n\t"
        "v_mfma_f32_16x16x32_bf16 v[36:39], a[36:39], v[104:107], v[36:39]\n\t"
        "buffer_load_dwordx4 v141, s[24:27], s36 offen offset:1024 lds\n\t"
        "s_bcnt1_i32_b64 s61, s[64:65]\n\t"
        "v_mfma_f32_16x16x32_bf16 v[40:43], a[40:43], v[104:107], v[40:43]\n\t"
        "v_add_u32 v54, s61, 1\n\t"
        "v_lshl_add_u32 v56, v56, 3, s60\n\t"
        "v_add_f32 v57, v57, v79\n\t"
        "v_mfma_f32_16x16x32_bf16 v[44:47], a[44:47], v[104:107], v[44:47]\n\t"
        "s_mov_b64 exec, s[64:65]\n\t"
        "ds_write_b64 v56, v[52:53]\n\t"
        "s_mov_b64 exec, -1\n\t"
        "s_waitcnt lgkmcnt(3)\n\t"
        "v_mfma_f32_16x16x32_bf16 v[12:15], a[84:87], v[108:111], v[12:15]\n\t"
        "v_swap_b32 v53, v80\n\t"
        "v_perm_b32 v56, v67, v67, s44\n\t"
        "v_mfma_f32_16x16x32_bf16 v[16:19], a[88:91], v[108:111], v[16:19]\n\t"
        "buffer_load_dwordx4 v142, s[24:27], s36 offen offset:2048 lds\n\t"
        "v_readlane_b32 s40, v155, 12\n\t"
        "v_mfma_f32_16x16x32_bf16 v[20:23], a[92:95], v[108:111], v[20:23]\n\t"
        "v_readlane_b32 s43, v155, 0\n\t"
        "s_add_u32 m0, s40, s35\n\t"
        "v_mfma_f32_16x16x32_bf16 v[0:3], a[96:99], v[112:115], v[0:3]\n\t"
        "v_add_f32 v56, v56, v78\n\t"
        "v_cvt_pk_bf16_f32 v52, v56, v57\n\t"
        "v_mfma_f32_16x16x32_bf16 v[4:7], a[100:103], v[112:115], v[4:7]\n\t"
        "v_cvt_pk_bf16_f32 v58, v78, v79\n\t"
        "v_cmp_ne_u32 s[64:65], v52, v58\n\t"
        "v_perm_b32 v57, v66, v66, s45\n\t"
        "v_mfma_f32_16x16x32_bf16 v[8:11], a[104:107], v[112:115], v[8:11]\n\t"
        "buffer_load_dwordx4 v134, s[16:19], s43 offen  lds\n\t"
        "v_readlane_b32 s40, v155, 13\n\t"
        "v_mfma_f32_16x16x32_bf16 v[24:27], a[84:87], v[112:115], v[24:27]\n\t"
        "v_readlane_b32 s43, v155, 1\n\t"
        "s_add_u32 m0, s40, s35\n\t"
        "v_mfma_f32_16x16x32_bf16 v[28:31], a[88:91], v[112:115], v[28:31]\n\t"
        "s_waitcnt vmcnt(9)\n\t"
        "s_barrier\n\t"
        "v_mfma_f32_16x16x32_bf16 v[32:35], a[92:95], v[112:115], v[32:35]\n\t"
        "ds_read_b128 v[92:95], v128 offset:0\n\t"
        "s_and_b64 s[64:65], s[64:65], 0xffffff\n\t"
        "s_waitcnt lgkmcnt(2)\n\t"
        "v_mfma_f32_16x16x32_bf16 v[12:15], a[96:99], v[84:87], v[12:15]\n\t"
        "buffer_load_dwordx4 v135, s[16:19], s43 offen  lds\n\t"
        "ds_read_b128 v[96:99], v128 offset:64\n\t"
        "v_readlane_b32 s40, v155, 14\n\t"
        "v_mfma_f32_16x16x32_bf16 v[16:19], a[100:103], v[84:87], v[16:19]\n\t"
        "ds_read_b128 v[100:103], v128 offset:128\n\t"
        "ds_read_b128 v[104:107], v128 offset:20480\n\t"
        "v_readlane_b32 s43, v155, 2\n\t"
        "v_mfma_f32_16x16x32_bf16 v[20:23], a[104:107], v[84:87], v[20:23]\n\t"
        "ds_read_b64_tr_b16 a[108:109], v131 offset:12288\n\t"
        "s_add_u32 m0, s40, s35\n\t"
        "ds_read_b64_tr_b16 a[110:111], v131 offset:13056\n\t"
        "v_mfma_f32_16x16x32_bf16 v[36:39], a[84:87], v[84:87], v[36:39]\n\t"
        "ds_read_b128 v[108:111], v128 offset:20544\n\t"
        "ds_read_b64_tr_b16 a[112:113], v132 offset:12288\n\t"
        "v_mbcnt_lo_u32_b32 v56, s64, v54\n\t"
        "s_bcnt1_i32_b64 s46, s[64:65]\n\t"
        "v_mfma_f32_16x16x32_bf16 v[40:43], a[88:91], v[84:87], v[40:43]\n\t"
        "buffer_load_dwordx4 v134, s[16:19], s43 offen  lds\n\t"
        "ds_read_b64_tr_b16 a[114:115], v132 offset:13056\n\t"
        "s_bitcmp1_b32 s42, 10\n\t"
        "s_cbranch_scc0 .Lx60%=\n\t"
        "v_readlane_b32 s40, v155, 20\n\t"
        "v_readlane_b32 s43, v155, 8\n\t"
        "s_nop 0\n\t"
        "s_add_u32 m0, s40, s35\n\t"
        "s_nop 2\n\t"
        "buffer_load_dwordx4 v136, s[16:19], s43 offen  lds\n\t"
        ".Lx60%=:\n\t"
        "v_mfma_f32_16x16x32_bf16 v[44:47], a[92:95], v[84:87], v[44:47]\n\t"
        "ds_read_b64_tr_b16 a[116:117], v133 offset:12288\n\t"
        "ds_read_b64_tr_b16 a[118:119], v133 offset:13056\n\t"
        "v_readlane_b32 s40, v155, 15\n\t"
        "v_mfma_f32_16x16x32_bf16 v[24:27], a[96:99], v[88:91], v[24:27]\n\t"
        "ds_read_b128 v[112:115], v128 offset:20608\n\t"
        "v_readlane_b32 s43, v155, 3\n\t"
        "v_mfma_f32_16x16x32_bf16 v[28:31], a[100:103], v[88:91], v[28:31]\n\t"
        "ds_read_b64_tr_b16 a[120:121], v131 offset:18432\n\t"
        "s_add_u32 m0, s40, s35\n\t"
        "v_mfma_f32_16x16x32_bf16 v[32:35], a[104:107], v[88:91], v[32:35]\n\t"
        "ds_read_b64_tr_b16 a[122:123], v131 offset:19200\n\t"
        "s_add_u32 s61, s61, s46\n\t"
        "v_mfma_f32_16x16x32_bf16 v[36:39], a[72:75], v[88:91], v[36:39]\n\t"
        "ds_read_b64_tr_b16 a[124:125], v132 offset:18432\n\t"
        "v_add_u32 v54, s46, v54\n\t"
        "v_mfma_f32_16x16x32_bf16 v[40:43], a[76:79], v[88:91], v[40:43]\n\t"
        "ds_read_b64_tr_b16 a[126:127], v132 offset:19200\n\t"
        "s_mov_b32 s3, s61\n\t"
        "v_mfma_f32_16x16x32_bf16 v[44:47], a[80:83], v[88:91], v[44:47]\n\t"
        "ds_read_b64_tr_b16 a[128:129], v133 offset:18432\n\t"
        "v_lshl_add_u32 v56, v56, 3, s60\n\t"
        "v_readlane_b32 s46, v155, 39\n\t"
        "s_waitcnt lgkmcnt(9)\n\t"
        "v_mfma_f32_16x16x32_bf16 v[0:3], a[12:15], v[92:95], v[0:3]\n\t"
        "s_mov_b64 exec, s[64:65]\n\t"
        "ds_write_b64 v56, v[52:53]\n\t"
        "s_mov_b64 exec, -1\n\t"
        "ds_read_b64_tr_b16 a[130:131], v133 offset:19200\n\t"
        "v_mfma_f32_16x16x32_bf16 v[4:7], a[16:19], v[92:95], v[4:7]\n\t"
        "ds_read_b128 v[84:87], v129 offset:0\n\t"
        "v_add_u32 v53, 4096, v80\n\t"
        "v_mfma_f32_16x16x32_bf16 v[8:11], a[20:23], v[92:95], v[8:11]\n\t"
        "ds_read_b128 v[88:91], v129 offset:4096\n\t"
        "v_perm_b32 v56, v66, v66, s44\n\t"
        "v_mfma_f32_16x16x32_bf16 v[0:3], a[48:51], v[96:99], v[0:3]\n\t"
        "ds_read_b64_tr_b16 a[132:133], v131 offset:24576\n\t"
        "v_add_f32 v58, s46, v68\n\t"
        "v_mfma_f32_16x16x32_bf16 v[4:7], a[52:55], v[96:99], v[4:7]\n\t"
        "buffer_load_dwordx4 v135, s[16:19], s43 offen  lds\n\t"
        "s_add_u32 m0, s37, 0\n\t"
        "v_mfma_f32_16x16x32_bf16 v[8:11], a[56:59], v[96:99], v[8:11]\n\t"
        "ds_read_b64_tr_b16 a[134:135], v131 offset:25344\n\t"
        "s_add_u32 s36, s36, s39\n\t"
        "v_mfma_f32_16x16x32_bf16 v[0:3], a[84:87], v[100:103], v[0:3]\n\t"
        "ds_read_b64_tr_b16 a[136:137], v132 offset:24576\n\t"
        "v_add_f32 v59, s47, v69\n\t"
        "v_mfma_f32_16x16x32_bf16 v[4:7], a[88:91], v[100:103], v[4:7]\n\t"
        "ds_read_b64_tr_b16 a[138:139], v132 offset:25344\n\t"
        "v_add_f32 v56, v56, v58\n\t"
        "v_add_f32 v57, v57, v59\n\t"
        "v_mfma_f32_16x16x32_bf16 v[8:11], a[92:95], v[100:103], v[8:11]\n\t"
        "ds_read_b64_tr_b16 a[140:141], v133 offset:24576\n\t"
        "v_max_f32 v56, 0, v56\n\t"
        "v_mfma_f32_16x16x32_bf16 v[36:39], a[24:27], v[104:107], v[36:39]\n\t"
        "buffer_load_dwordx4 v140, s[24:27], s36 offen offset:0 lds\n\t"
        "v_max_f32 v57, 0, v57\n\t"
        "v_mfma_f32_16x16x32_bf16 v[40:43], a[28:31], v[104:107], v[40:43]\n\t"
        "ds_read_b64_tr_b16 a[142:143], v133 offset:25344\n\t"
        "v_max_f32 v58, 0, v58\n\t"
        "v_mfma_f32_16x16x32_bf16 v[44:47], a[32:35], v[104:107], v[44:47]\n\t"
        "ds_read_b128 v[92:95], v129 offset:8192\n\t"
        "v_max_f32 v59, 0, v59\n\t"
        "v_mfma_f32_16x16x32_bf16 v[36:39], a[60:63], v[108:111], v[36:39]\n\t"
        "ds_read_b64_tr_b16 a[144:145], v131 offset:30720\n\t"
        "v_cvt_pk_bf16_f32 v52, v56, v57\n\t"
        "v_mfma_f32_16x16x32_bf16 v[40:43], a[64:67], v[108:111], v[40:43]\n\t"
        "buffer_load_dwordx4 v141, s[24:27], s36 offen offset:1024 lds\n\t"
        "v_cvt_pk_bf16_f32 v58, v58, v59\n\t"
        "v_mfma_f32_16x16x32_bf16 v[44:47], a[68:71], v[108:111], v[44:47]\n\t"
        "ds_read_b64_tr_b16 a[146:147], v131 offset:31488\n\t"
        "v_cmp_ne_u32 s[64:65], v52, v58\n\t"
        "s_waitcnt lgkmcnt(7)\n\t"
        "v_mfma_f32_16x16x32_bf16 v[36:39], a[96:99], v[112:115], v[36:39]\n\t"
        "ds_read_b128 v[96:99], v129 offset:12288\n\t"
        "v_mbcnt_lo_u32_b32 v56, s64, v54\n\t"
        "v_mfma_f32_16x16x32_bf16 v[40:43], a[100:103], v[112:115], v[40:43]\n\t"
        "ds_read_b64_tr_b16 a[148:149], v132 offset:30720\n\t"
        ".LpX0%=:\n\t"
        "s_cbranch_execz .LrX%=\n\t"
        "v_mbcnt_hi_u32_b32 v56, s65, v56\n\t"
        "v_mfma_f32_16x16x32_bf16 v[44:47], a[104:107], v[112:115], v[44:47]\n\t"
        "buffer_load_dwordx4 v142, s[24:27], s36 offen offset:2048 lds\n\t"
        "s_add_u32 m0, s37, 12288\n\t"
        "v_mfma_f32_16x16x32_bf16 v[0:3], a[120:123], v[84:87], v[0:3]\n\t"
        "ds_read_b64_tr_b16 a[150:151], v132 offset:31488\n\t"
        "s_add_u32 s36, s36, s38\n\t"
        "s_bcnt1_i32_b64 s46, s[64:65]\n\t"
        "v_mfma_f32_16x16x32_bf16 v[4:7], a[124:127], v[84:87], v[4:7]\n\t"
        "ds_read_b64_tr_b16 a[152:153], v133 offset:30720\n\t"
        "s_add_u32 s61, s61, s46\n\t"
        "v_mfma_f32_16x16x32_bf16 v[8:11], a[128:131], v[84:87], v[8:11]\n\t"
        "ds_read_b64_tr_b16 a[154:155], v133 offset:31488\n\t"
        "v_min_u32 v56, 97, v56\n\t"
        "v_mfma_f32_16x16x32_bf16 v[0:3], a[108:111], v[88:91], v[0:3]\n\t"
        "v_lshl_add_u32 v56, v56, 3, s60\n\t"
        "v_mfma_f32_16x16x32_bf16 v[4:7], a[112:115], v[88:91], v[4:7]\n\t"
        "s_mov_b64 exec, s[64:65]\n\t"
        "ds_write_b64 v56, v[52:53]\n\t"
        "s_mov_b64 exec, -1\n\t"
        "v_mfma_f32_16x16x32_bf16 v[8:11], a[116:119], v[88:91], v[8:11]\n\t"
        "s_cmp_gt_u32 s61, s53\n\t"
        "s_cselect_b32 s8, 1, 0\n\t"
        "v_mfma_f32_16x16x32_bf16 v[12:15], a[120:123], v[88:91], v[12:15]\n\t"
        "ds_read_b128 v[100:103], v129 offset:16384\n\t"
        "s_waitcnt vmcnt(14)\n\t"
        "v_and_b32 v48, s54, v48\n\t"
        "v_mfma_f32_16x16x32_bf16 v[16:19], a[124:127], v[88:91], v[16:19]\n\t"
        "v_pk_max_u16 v60, v154, v48\n\t"
        "v_and_b32 v49, s54, v49\n\t"
        "v_mfma_f32_16x16x32_bf16 v[20:23], a[128:131], v[88:91], v[20:23]\n\t"
        "v_pk_max_u16 v60, v60, v49\n\t"
        "v_and_b32 v50, s54, v50\n\t"
        "s_waitcnt lgkmcnt(5)\n\t"
        "v_mfma_f32_16x16x32_bf16 v[0:3], a[132:135], v[92:95], v[0:3]\n\t"
        "v_pk_max_u16 v60, v60, v50\n\t"
        "v_and_b32 v51, s54, v51\n\t"
        "v_mfma_f32_16x16x32_bf16 v[4:7], a[136:139], v[92:95], v[4:7]\n\t"
        "v_pk_max_u16 v60, v60, v51\n\t"
        "s_waitcnt vmcnt(13)\n\t"
        "v_and_b32 v61, s54, v61\n\t"
        "v_mfma_f32_16x16x32_bf16 v[8:11], a[140:143], v[92:95], v[8:11]\n\t"
        "v_pk_max_u16 v60, v60, v61\n\t"
        "v_cmp_ne_u32 s[12:13], v60, v154\n\t"
        "v_mfma_f32_16x16x32_bf16 v[12:15], a[108:111], v[92:95], v[12:15]\n\t"
        "ds_read_b128 v[104:107], v129 offset:20480\n\t"
        "s_lshl_b32 s6, 256, s4\n\t"
        "v_mfma_f32_16x16x32_bf16 v[16:19], a[112:115], v[92:95], v[16:19]\n\t"
        "s_cmp_lg_u64 s[12:13], 0\n\t"
        "s_cselect_b32 s6, s6, 0\n\t"
        "s_or_b32 s56, s56, s8\n\t"
        "v_mfma_f32_16x16x32_bf16 v[20:23], a[116:119], v[92:95], v[20:23]\n\t"
        "s_waitcnt vmcnt(7) lgkmcnt(3)\n\t"
        "s_barrier\n\t"
        "v_mfma_f32_16x16x32_bf16 v[24:27], a[120:123], v[92:95], v[24:27]\n\t"
        "s_cmp_gt_u32 s3, 64\n\t"
        "s_cselect_b32 s8, 1, 0\n\t"
        "v_mfma_f32_16x16x32_bf16 v[28:31], a[124:127], v[92:95], v[28:31]\n\t"
        "buffer_load_dwordx4 v140, s[24:27], s36 offen offset:0 lds\n\t"
        "ds_read_b64_tr_b16 a[156:157], v131 offset:36864\n\t"
        "s_or_b32 s56, s56, s8\n\t"
        "v_mfma_f32_16x16x32_bf16 v[32:35], a[128:131], v[92:95], v[32:35]\n\t"
        "ds_read_b64_tr_b16 a[158:159], v131 offset:37632\n\t"
        "s_min_u32 s61, s61, 80\n\t"
        "v_mfma_f32_16x16x32_bf16 v[12:15], a[132:135], v[96:99], v[12:15]\n\t"
        "ds_read_b64_tr_b16 a[160:161], v132 offset:36864\n\t"
        "ds_read_b64_tr_b16 a[162:163], v132 offset:37632\n\t"
        "v_min_u32 v78, s61, v116\n\t"
        "v_mfma_f32_16x16x32_bf16 v[16:19], a[136:139], v[96:99], v[16:19]\n\t"
        "ds_read_b64_tr_b16 a[164:165], v133 offset:36864\n\t"
        "ds_read_b64_tr_b16 a[166:167], v133 offset:37632\n\t"
        "v_lshl_add_u32 v78, v78, 3, s60\n\t"
        "v_mfma_f32_16x16x32_bf16 v[20:23], a[140:143], v[96:99], v[20:23]\n\t"
        "buffer_load_dwordx4 v141, s[24:27], s36 offen offset:1024 lds\n\t"
        "v_cmp_ge_u32 s[66:67], s61, v116\n\t"
        "v_mfma_f32_16x16x32_bf16 v[24:27], a[108:111], v[96:99], v[24:27]\n\t"
        "ds_read_b128 v[108:111], v129 offset:64\n\t"
        "s_mov_b32 s46, 0x07060302\n\t"
        "v_mfma_f32_16x16x32_bf16 v[28:31], a[112:115], v[96:99], v[28:31]\n\t"
        "ds_read_b64 v[62:63], v78\n\t"
        "v_mfma_f32_16x16x32_bf16 v[32:35], a[116:119], v[96:99], v[32:35]\n\t"
        "ds_read_b128 v[112:115], v129 offset:4160\n\t"
        "v_mfma_f32_16x16x32_bf16 v[36:39], a[120:123], v[96:99], v[36:39]\n\t"
        "buffer_load_dwordx4 v142, s[24:27], s36 offen offset:2048 lds\n\t"
        "v_readlane_b32 s40, v155, 16\n\t"
        "v_mfma_f32_16x16x32_bf16 v[40:43], a[124:127], v[96:99], v[40:43]\n\t"
        "ds_read_b64_tr_b16 a[168:169], v131 offset:43008\n\t"
        "v_readlane_b32 s43, v155, 4\n\t"
        "v_mfma_f32_16x16x32_bf16 v[44:47], a[128:131], v[96:99], v[44:47]\n\t"
        "ds_read_b64_tr_b16 a[170:171], v131 offset:43776\n\t"
        "s_add_u32 m0, s40, s35\n\t"
        "s_waitcnt lgkmcnt(9)\n\t"
        "v_mfma_f32_16x16x32_bf16 v[24:27], a[132:135], v[100:103], v[24:27]\n\t"
        "ds_read_b64_tr_b16 a[172:173], v132 offset:43008\n\t"
        "v_mfma_f32_16x16x32_bf16 v[28:31], a[136:139], v[100:103], v[28:31]\n\t"
        "buffer_load_dwordx4 v134, s[16:19], s43 offen  lds\n\t"
        "s_bitcmp1_b32 s42, 11\n\t"
        "s_cbranch_scc0 .Lx62%=\n\t"
        "v_readlane_b32 s40, v155, 21\n\t"
        "v_readlane_b32 s43, v155, 9\n\t"
        "s_nop 0\n\t"
        "s_add_u32 m0, s40, s35\n\t"
        "s_nop 2\n\t"
        "buffer_load_dwordx4 v137, s[16:19], s43 offen  lds\n\t"
        "v_readlane_b32 s40, v155, 22\n\t"
        "v_readlane_b32 s43, v155, 10\n\t"
        "s_nop 0\n\t"
        "s_add_u32 m0, s40, s35\n\t"
        "s_nop 2\n\t"
        "buffer_load_dwordx4 v136, s[16:19], s43 offen  lds\n\t"
        ".Lx62%=:\n\t"
        "v_mfma_f32_16x16x32_bf16 v[32:35], a[140:143], v[100:103], v[32:35]\n\t"
        "ds_read_b64_tr_b16 a[174:175], v132 offset:43776\n\t"
        "v_readlane_b32 s40, v155, 17\n\t"
        "v_mfma_f32_16x16x32_bf16 v[36:39], a[108:111], v[100:103], v[36:39]\n\t"
        "ds_read_b64_tr_b16 a[176:177], v133 offset:43008\n\t"
        "ds_read_b64_tr_b16 a[178:179], v133 offset:43776\n\t"
        "v_readlane_b32 s43, v155, 5\n\t"
        "v_mfma_f32_16x16x32_bf16 v[40:43], a[112:115], v[100:103], v[40:43]\n\t"
        "ds_read_b128 v[84:87], v129 offset:8256\n\t"
        "s_add_u32 m0, s40, s35\n\t"
        "v_mfma_f32_16x16x32_bf16 v[44:47], a[116:119], v[100:103], v[44:47]\n\t"
        "s_mov_b64 exec, s[66:67]\n\t"
        "s_waitcnt lgkmcnt(7)\n\t"
        "ds_write_b32 v63, v117\n\t"
        "s_mov_b64 exec, -1\n\t"
        "v_mfma_f32_16x16x32_bf16 v[36:39], a[132:135], v[104:107], v[36:39]\n\t"
        "buffer_load_dwordx4 v135, s[16:19], s43 offen  lds\n\t"
        "v_readlane_b32 s40, v155, 18\n\t"
        "v_mfma_f32_16x16x32_bf16 v[40:43], a[136:139], v[104:107], v[40:43]\n\t"
        "ds_read_b128 v[88:91], v129 offset:12352\n\t"
        "v_readlane_b32 s43, v155, 6\n\t"
        "v_mfma_f32_16x16x32_bf16 v[44:47], a[140:143], v[104:107], v[44:47]\n\t"
        "ds_read_b64_tr_b16 a[180:181], v131 offset:49152\n\t"
        "s_add_u32 m0, s40, s35\n\t"
        "v_mfma_f32_16x16x32_bf16 v[0:3], a[156:159], v[108:111], v[0:3]\n\t"
        "ds_read_b64_tr_b16 a[182:183], v131 offset:49920\n\t"
        "v_and_b32 v78, 0xfffff800, v63\n\t"
        "v_mfma_f32_16x16x32_bf16 v[4:7], a[160:163], v[108:111], v[4:7]\n\t"
        "ds_read_b128 v[64:67], v78 offset:1536\n\t"
        "v_lshlrev_b32 v72, 16, v62\n\t"
        "v_mfma_f32_16x16x32_bf16 v[8:11], a[164:167], v[108:111], v[8:11]\n\t"
        "buffer_load_dwordx4 v136, s[16:19], s43 offen  lds\n\t"
        "s_bitcmp1_b32 s42, 11\n\t"
        "s_cbranch_scc0 .Lx64%=\n\t"
        "v_readlane_b32 s40, v155, 23\n\t"
        "v_readlane_b32 s43, v155, 11\n\t"
        "s_nop 0\n\t"
        "s_add_u32 m0, s40, s35\n\t"
        "s_nop 2\n\t"
        "buffer_load_dwordx4 v137, s[16:19], s43 offen  lds\n\t"
        ".Lx64%=:\n\t"
        "v_mfma_f32_16x16x32_bf16 v[0:3], a[144:147], v[112:115], v[0:3]\n\t"
        "ds_read_b64_tr_b16 a[184:185], v132 offset:49152\n\t"
        "v_readlane_b32 s40, v155, 19\n\t"
        "v_mfma_f32_16x16x32_bf16 v[4:7], a[148:151], v[112:115], v[4:7]\n\t"
        "ds_read_b64_tr_b16 a[186:187], v132 offset:49920\n\t"
        "v_readlane_b32 s43, v155, 7\n\t"
        "v_mfma_f32_16x16x32_bf16 v[8:11], a[152:155], v[112:115], v[8:11]\n\t"
        "ds_read_b64_tr_b16 a[188:189], v133 offset:49152\n\t"
        "s_add_u32 m0, s40, s35\n\t"
        "v_mfma_f32_16x16x32_bf16 v[12:15], a[156:159], v[112:115], v[12:15]\n\t"
        "s_nop 0\n\t"
        "buffer_load_dwordx4 v137, s[16:19], s43 offen  lds\n\t"
        "s_add_u32 m0, s37, 24576\n\t"
        "ds_read_b128 v[92:95], v129 offset:16448\n\t"
        "v_mfma_f32_16x16x32_bf16 v[16:19], a[160:163], v[112:115], v[16:19]\n\t"
        "ds_read_b64_tr_b16 a[190:191], v133 offset:49920\n\t"
        "s_add_u32 s36, s36, s5\n\t"
        "v_mfma_f32_16x16x32_bf16 v[20:23], a[164:167], v[112:115], v[20:23]\n\t"
        "ds_read_b64_tr_b16 a[192:193], v131 offset:55296\n\t"
        "v_and_b32 v73, 0xffff0000, v62\n\t"
        "v_med3_f32 v72, v72, 0, s63\n\t"
        "s_waitcnt lgkmcnt(7)\n\t"
        "v_mfma_f32_16x16x32_bf16 v[0:3], a[168:171], v[84:87], v[0:3]\n\t"
        "ds_read_b64_tr_b16 a[194:195], v131 offset:56064\n\t"
        "v_med3_f32 v73, v73, 0, s63\n\t"
        "v_mfma_f32_16x16x32_bf16 v[4:7], a[172:175], v[84:87], v[4:7]\n\t"
        "buffer_load_dwordx4 v140, s[24:27], s36 offen offset:0 lds\n\t"
        "v_perm_b32 v62, v73, v72, s46\n\t"
        "v_mfma_f32_16x16x32_bf16 v[8:11], a[176:179], v[84:87], v[8:11]\n\t"
        "ds_read_b64_tr_b16 a[196:197], v132 offset:55296\n\t"
        "v_fract_f32 v74, v72\n\t"
        "v_mfma_f32_16x16x32_bf16 v[12:15], a[144:147], v[84:87], v[12:15]\n\t"
        "ds_read_b128 v[96:99], v129 offset:20544\n\t"
        "v_fract_f32 v75, v73\n\t"
        "v_mfma_f32_16x16x32_bf16 v[16:19], a[148:151], v[84:87], v[16:19]\n\t"
        "ds_read_b64_tr_b16 a[198:199], v132 offset:56064\n\t"
        "v_cvt_u32_f32 v76, v72\n\t"
        "v_mfma_f32_16x16x32_bf16 v[20:23], a[152:155], v[84:87], v[20:23]\n\t"
        "buffer_load_dwordx4 v141, s[24:27], s36 offen offset:1024 lds\n\t"
        "v_cvt_u32_f32 v77, v73\n\t"
        "v_mfma_f32_16x16x32_bf16 v[24:27], a[156:159], v[84:87], v[24:27]\n\t"
        "ds_read_b64_tr_b16 a[200:201], v133 offset:55296\n\t"
        "v_cmp_neq_f32 s[64:65], 0, v74\n\t"
        "v_mfma_f32_16x16x32_bf16 v[28:31], a[160:163], v[84:87], v[28:31]\n\t"
        "ds_read_b64_tr_b16 a[202:203], v133 offset:56064\n\t"
        "v_cmp_neq_f32 s[44:45], 0, v75\n\t"
        "v_mfma_f32_16x16x32_bf16 v[32:35], a[164:167], v[84:87], v[32:35]\n\t"
        "ds_read_b128 v[100:103], v129 offset:128\n\t"
        "v_sub_f32 v78, 1.0, v74\n\t"
        "v_mfma_f32_16x16x32_bf16 v[12:15], a[168:171], v[88:91], v[12:15]\n\t"
        "buffer_load_dwordx4 v142, s[24:27], s36 offen offset:2048 lds\n\t"
        "s_add_u32 m0, s37, 36864\n\t"
        "v_mfma_f32_16x16x32_bf16 v[16:19], a[172:175], v[88:91], v[16:19]\n\t"
        "ds_read_b128 v[104:107], v129 offset:4224\n\t"
        "s_add_u32 s36, s36, s39\n\t"
        "v_mfma_f32_16x16x32_bf16 v[20:23], a[176:179], v[88:91], v[20:23]\n\t"
        "v_sub_f32 v79, 1.0, v75\n\t"
        "v_cvt_pk_bf16_f32 v79, v78, v79\n\t"
        "v_mfma_f32_16x16x32_bf16 v[24:27], a[144:147], v[88:91], v[24:27]\n\t"
        "s_waitcnt vmcnt(10)\n\t"
        "s_barrier\n\t"
        "v_mfma_f32_16x16x32_bf16 v[28:31], a[148:151], v[88:91], v[28:31]\n\t"
        "buffer_load_dwordx4 v140, s[24:27], s36 offen offset:0 lds\n\t"
        "v_lshlrev_b32 v78, 16, v79\n\t"
        "v_mfma_f32_16x16x32_bf16 v[32:35], a[152:155], v[88:91], v[32:35]\n\t"
        "ds_read_b64_tr_b16 a[204:205], v131 offset:0\n\t"
        "v_and_b32 v79, 0xffff0000, v79\n\t"
        "v_mfma_f32_16x16x32_bf16 v[36:39], a[156:159], v[88:91], v[36:39]\n\t"
        "ds_read_b64_tr_b16 a[206:207], v131 offset:768\n\t"
        "v_mul_f32 v80, v78, v79\n\t"
        "v_mfma_f32_16x16x32_bf16 v[40:43], a[160:163], v[88:91], v[40:43]\n\t"
        "ds_read_b64_tr_b16 a[208:209], v132 offset:0\n\t"
        "v_mul_f32 v78, v78, v75\n\t"
        "v_mfma_f32_16x16x32_bf16 v[44:47], a[164:167], v[88:91], v[44:47]\n\t"
        "buffer_load_dwordx4 v141, s[24:27], s36 offen offset:1024 lds\n\t"
        "v_mul_f32 v79, v74, v79\n\t"
        "s_waitcnt lgkmcnt(5)\n\t"
        "v_mfma_f32_16x16x32_bf16 v[24:27], a[168:171], v[92:95], v[24:27]\n\t"
        "ds_read_b64_tr_b16 a[210:211], v132 offset:768\n\t"
        "v_mul_f32 v81, v74, v75\n\t"
        "v_mfma_f32_16x16x32_bf16 v[28:31], a[172:175], v[92:95], v[28:31]\n\t"
        "ds_read_b64_tr_b16 a[212:213], v133 offset:0\n\t"
        "v_cndmask_b32 v78, v79, v78, s[44:45]\n\t"
        "v_mfma_f32_16x16x32_bf16 v[32:35], a[176:179], v[92:95], v[32:35]\n\t"
        "ds_read_b64_tr_b16 a[214:215], v133 offset:768\n\t"
        "v_cndmask_b32 v79, 0, v79, s[44:45]\n\t"
        "v_mfma_f32_16x16x32_bf16 v[36:39], a[144:147], v[92:95], v[36:39]\n\t"
        "buffer_load_dwordx4 v142, s[24:27], s36 offen offset:2048 lds\n\t"
        "s_add_u32 m0, s37, 49152\n\t"
        "ds_read_b128 v[108:111], v129 offset:8320\n\t"
        "v_mfma_f32_16x16x32_bf16 v[40:43], a[148:151], v[92:95], v[40:43]\n\t"
        "ds_read_b64_tr_b16 a[216:217], v131 offset:6144\n\t"
        "s_add_u32 s36, s36, s38\n\t"
        "v_mfma_f32_16x16x32_bf16 v[44:47], a[152:155], v[92:95], v[44:47]\n\t"
        "ds_read_b64_tr_b16 a[218:219], v131 offset:6912\n\t"
        "v_cvt_pk_bf16_f32 v70, v80, v78\n\t"
        "v_mfma_f32_16x16x32_bf16 v[36:39], a[168:171], v[96:99], v[36:39]\n\t"
        "s_waitcnt vmcnt(10)\n\t"
        "s_barrier\n\t"
        "v_mfma_f32_16x16x32_bf16 v[40:43], a[172:175], v[96:99], v[40:43]\n\t"
        "buffer_load_dwordx4 v140, s[24:27], s36 offen offset:0 lds\n\t"
        "v_cvt_pk_bf16_f32 v71, v79, v81\n\t"
        "v_mfma_f32_16x16x32_bf16 v[44:47], a[176:179], v[96:99], v[44:47]\n\t"
        "ds_read_b128 v[112:115], v129 offset:12416\n\t"
        "v_pk_max_u16 v78, v62, v64\n\t"
        "s_waitcnt lgkmcnt(7)\n\t"
        "v_mfma_f32_16x16x32_bf16 v[0:3], a[192:195], v[100:103], v[0:3]\n\t"
        "ds_read_b64_tr_b16 a[220:221], v132 offset:6144\n\t"
        "v_pk_min_u16 v78, v78, v65\n\t"
        "v_mfma_f32_16x16x32_bf16 v[4:7], a[196:199], v[100:103], v[4:7]\n\t"
        "ds_read_b64_tr_b16 a[222:223], v132 offset:6912\n\t"
        "v_cmp_ne_u32 vcc, v78, v62\n\t"
        "v_mfma_f32_16x16x32_bf16 v[8:11], a[200:203], v[100:103], v[8:11]\n\t"
        "ds_read_b64_tr_b16 a[224:225], v133 offset:6144\n\t"
        "v_sub_u32 v78, v77, v67\n\t"
        "v_mfma_f32_16x16x32_bf16 v[0:3], a[180:183], v[104:107], v[0:3]\n\t"
        "ds_read_b64_tr_b16 a[226:227], v133 offset:6912\n\t"
        "v_mad_u32_u24 v79, v77, 6, v66\n\t"
        "v_mfma_f32_16x16x32_bf16 v[4:7], a[184:187], v[104:107], v[4:7]\n\t"
        "ds_read_b64_tr_b16 a[228:229], v131 offset:12288\n\t"
        "v_mad_u32_u24 v79, v76, s62, v79\n\t"
        "v_mfma_f32_16x16x32_bf16 v[8:11], a[188:191], v[104:107], v[8:11]\n\t"
        "ds_read_b64_tr_b16 a[230:231], v131 offset:13056\n\t"
        "s_and_b64 vcc, vcc, s[66:67]\n\t"
        "v_mfma_f32_16x16x32_bf16 v[12:15], a[192:195], v[104:107], v[12:15]\n\t"
        "ds_read_b128 v[84:87], v129 offset:16512\n\t"
        "s_or_b64 s[56:57], s[56:57], vcc\n\t"
        "v_mfma_f32_16x16x32_bf16 v[16:19], a[196:199], v[104:107], v[16:19]\n\t"
        "ds_read_b64_tr_b16 a[232:233], v132 offset:12288\n\t"
        "v_bfe_u32 v80, v78, 2, 1\n\t"
        "v_mfma_f32_16x16x32_bf16 v[20:23], a[200:203], v[104:107], v[20:23]\n\t"
        "ds_read_b64_tr_b16 a[234:235], v132 offset:13056\n\t"
        "v_or_b32 v80, v80, v79\n\t"
        "s_waitcnt lgkmcnt(10)\n\t"
        "v_mfma_f32_16x16x32_bf16 v[0:3], a[204:207], v[108:111], v[0:3]\n\t"
        "ds_read_b64_tr_b16 a[236:237], v133 offset:12288\n\t"
        "v_add_u32 v78, 1, v78\n\t"
        "v_mfma_f32_16x16x32_bf16 v[4:7], a[208:211], v[108:111], v[4:7]\n\t"
        "ds_read_b64_tr_b16 a[238:239], v133 offset:13056\n\t"
        "v_bfe_u32 v78, v78, 2, 1\n\t"
        "v_mfma_f32_16x16x32_bf16 v[8:11], a[212:215], v[108:111], v[8:11]\n\t"
        "ds_read_b64_tr_b16 a[240:241], v131 offset:18432\n\t"
        "v_add3_u32 v78, v79, v78, 6\n\t"
        "v_mfma_f32_16x16x32_bf16 v[12:15], a[180:183], v[108:111], v[12:15]\n\t"
        "ds_read_b128 v[88:91], v130 offset:0\n\t"
        "v_cndmask_b32 v78, v80, v78, s[44:45]\n\t"
        "v_mfma_f32_16x16x32_bf16 v[16:19], a[184:187], v[108:111], v[16:19]\n\t"
        "ds_read_b64_tr_b16 a[242:243], v131 offset:19200\n\t"
        "v_cndmask_b32 v79, 0, v119, s[64:65]\n\t"
        "v_mfma_f32_16x16x32_bf16 v[20:23], a[188:191], v[108:111], v[20:23]\n\t"
        "ds_read_b64_tr_b16 a[244:245], v132 offset:18432\n\t"
        "v_add_u32 v81, v80, v79\n\t"
        "v_mfma_f32_16x16x32_bf16 v[24:27], a[192:195], v[108:111], v[24:27]\n\t"
        "ds_read_b128 v[92:95], v130 offset:4096\n\t"
        "v_add_u32 v82, v78, v79\n\t"
        "v_mfma_f32_16x16x32_bf16 v[28:31], a[196:199], v[108:111], v[28:31]\n\t"
        "ds_read_b64_tr_b16 a[246:247], v132 offset:19200\n\t"
        "ds_read_b64_tr_b16 a[248:249], v133 offset:18432\n\t"
        "v_cndmask_b32 v78, v81, v78, s[44:45]\n\t"
        "v_mfma_f32_16x16x32_bf16 v[32:35], a[200:203], v[108:111], v[32:35]\n\t"
        "buffer_load_dwordx4 v141, s[24:27], s36 offen offset:1024 lds\n\t"
        "v_lshl_or_b32 v68, v78, 16, v80\n\t"
        "s_waitcnt lgkmcnt(9)\n\t"
        "v_mfma_f32_16x16x32_bf16 v[12:15], a[204:207], v[112:115], v[12:15]\n\t"
        "ds_read_b64_tr_b16 a[250:251], v133 offset:19200\n\t"
        "v_lshl_or_b32 v69, v82, 16, v81\n\t"
        "v_mfma_f32_16x16x32_bf16 v[16:19], a[208:211], v[112:115], v[16:19]\n\t"
        "ds_write_b128 v118, v[68:71]\n\t"
        "v_mfma_f32_16x16x32_bf16 v[20:23], a[212:215], v[112:115], v[20:23]\n\t"
        "s_cmp_gt_u32 s3, 32\n\t"
        "s_cselect_b32 s8, 2, 0\n\t"
        "v_mfma_f32_16x16x32_bf16 v[24:27], a[180:183], v[112:115], v[24:27]\n\t"
        "s_cmp_lg_u64 s[56:57], 0\n\t"
        "s_addc_u32 s8, s8, s6\n\t"
        "v_mfma_f32_16x16x32_bf16 v[28:31], a[184:187], v[112:115], v[28:31]\n\t"
        "ds_read_b128 v[160:163], v185 offset:0\n\t"
        "v_mov_b32 v151, s8\n\t"
        "v_mfma_f32_16x16x32_bf16 v[32:35], a[188:191], v[112:115], v[32:35]\n\t"
        "buffer_load_dwordx4 v142, s[24:27], s36 offen offset:2048 lds\n\t"
        "s_waitcnt vmcnt(3)\n\t"
        "s_barrier\n\t"
        "v_mfma_f32_16x16x32_bf16 v[36:39], a[192:195], v[112:115], v[36:39]\n\t"
        "s_mov_b64 exec, 1\n\t"
        "ds_write_b32 v153, v151\n\t"
        "s_mov_b64 exec, -1\n\t"
        "v_mfma_f32_16x16x32_bf16 v[40:43], a[196:199], v[112:115], v[40:43]\n\t"
        "ds_read_b128 v[96:99], v130 offset:8192\n\t"
        "v_xor_b32 v154, 32, v186\n\t"
        "v_mfma_f32_16x16x32_bf16 v[44:47], a[200:203], v[112:115], v[44:47]\n\t"
        "ds_read_b64_tr_b16 a[252:253], v131 offset:24576\n\t"
        "v_mfma_f32_16x16x32_bf16 v[24:27], a[204:207], v[84:87], v[24:27]\n\t"
        "ds_read_b64_tr_b16 a[254:255], v131 offset:25344\n\t"
        "v_mfma_f32_16x16x32_bf16 v[28:31], a[208:211], v[84:87], v[28:31]\n\t"
        "ds_read_b64_tr_b16 v[188:189], v132 offset:24576\n\t"
        "v_mfma_f32_16x16x32_bf16 v[32:35], a[212:215], v[84:87], v[32:35]\n\t"
        "ds_read_b64_tr_b16 v[190:191], v132 offset:25344\n\t"
        "v_mfma_f32_16x16x32_bf16 v[36:39], a[180:183], v[84:87], v[36:39]\n\t"
        "ds_read_b128 v[100:103], v130 offset:12288\n\t"
        "v_mfma_f32_16x16x32_bf16 v[40:43], a[184:187], v[84:87], v[40:43]\n\t"
        "ds_read_b64_tr_b16 v[192:193], v133 offset:24576\n\t"
        "s_waitcnt lgkmcnt(8)\n\t"
        "v_lshlrev_b32_sdwa v157, 5, v160 dst_sel:DWORD dst_unused:UNUSED_PAD src0_sel:DWORD src1_sel:WORD_0\n\t"
        "v_mfma_f32_16x16x32_bf16 v[44:47], a[188:191], v[84:87], v[44:47]\n\t"
        "ds_read_b64_tr_b16 v[194:195], v133 offset:25344\n\t"
        "v_lshlrev_b32_sdwa v158, 5, v160 dst_sel:DWORD dst_unused:UNUSED_PAD src0_sel:DWORD src1_sel:WORD_1\n\t"
        "v_mfma_f32_16x16x32_bf16 v[0:3], a[228:231], v[88:91], v[0:3]\n\t"
        "ds_read_b64_tr_b16 v[196:197], v131 offset:30720\n\t"
        "v_or_b32 v157, v184, v157\n\t"
        "v_mfma_f32_16x16x32_bf16 v[4:7], a[232:235], v[88:91], v[4:7]\n\t"
        "ds_read_b64_tr_b16 v[60:61], v157 offset:0\n\t"
        "v_or_b32 v158, v184, v158\n\t"
        "v_mfma_f32_16x16x32_bf16 v[8:11], a[236:239], v[88:91], v[8:11]\n\t"
        "ds_read_b64_tr_b16 v[62:63], v158 offset:0\n\t"
        "v_xor_b32 v159, 32, v157\n\t"
        "v_mfma_f32_16x16x32_bf16 v[0:3], a[216:219], v[92:95], v[0:3]\n\t"
        "ds_read_b64_tr_b16 v[64:65], v159 offset:0\n\t"
        "v_xor_b32 v152, 32, v158\n\t"
        "v_mfma_f32_16x16x32_bf16 v[4:7], a[220:223], v[92:95], v[4:7]\n\t"
        "ds_read_b64_tr_b16 v[66:67], v152 offset:0\n\t"
        "v_perm_b32 v164, v162, v162, v168\n\t"
        "v_mfma_f32_16x16x32_bf16 v[8:11], a[224:227], v[92:95], v[8:11]\n\t"
        "ds_read_b64_tr_b16 v[68:69], v157 offset:64\n\t"
        "v_perm_b32 v165, v162, v162, v169\n\t"
        "v_mfma_f32_16x16x32_bf16 v[12:15], a[228:231], v[92:95], v[12:15]\n\t"
        "ds_read_b64_tr_b16 v[70:71], v158 offset:64\n\t"
        "v_perm_b32 v166, v162, v162, v170\n\t"
        "v_mfma_f32_16x16x32_bf16 v[16:19], a[232:235], v[92:95], v[16:19]\n\t"
        "ds_read_b64_tr_b16 v[72:73], v159 offset:64\n\t"
        "v_perm_b32 v167, v162, v162, v171\n\t"
        "v_mfma_f32_16x16x32_bf16 v[20:23], a[236:239], v[92:95], v[20:23]\n\t"
        "ds_read_b64_tr_b16 v[74:75], v152 offset:64\n\t"
        "s_waitcnt lgkmcnt(10)\n\t"
        "v_mfma_f32_16x16x32_bf16 v[0:3], a[240:243], v[96:99], v[0:3]\n\t"
        "ds_read_b64_tr_b16 v[76:77], v157 offset:128\n\t"
        "ds_read_b128 v[104:107], v130 offset:16384\n\t"
        "v_lshlrev_b32_sdwa v157, 5, v161 dst_sel:DWORD dst_unused:UNUSED_PAD src0_sel:DWORD src1_sel:WORD_0\n\t"
        "v_mfma_f32_16x16x32_bf16 v[4:7], a[244:247], v[96:99], v[4:7]\n\t"
        "ds_read_b64_tr_b16 v[78:79], v158 offset:128\n\t"
        "v_lshlrev_b32_sdwa v158, 5, v161 dst_sel:DWORD dst_unused:UNUSED_PAD src0_sel:DWORD src1_sel:WORD_1\n\t"
        "v_mfma_f32_16x16x32_bf16 v[8:11], a[248:251], v[96:99], v[8:11]\n\t"
        "ds_read_b64_tr_b16 v[80:81], v159 offset:128\n\t"
        "v_or_b32 v157, v184, v157\n\t"
        "s_waitcnt lgkmcnt(10)\n\t"
        "v_mfma_f32_16x16x32_bf16 v[48:51], v[60:63], v[164:167], 0\n\t"
        "ds_read_b64_tr_b16 v[82:83], v152 offset:128\n\t"
        "v_or_b32 v158, v184, v158\n\t"
        "v_mfma_f32_16x16x32_bf16 v[12:15], a[216:219], v[96:99], v[12:15]\n\t"
        "ds_read_b64_tr_b16 v[60:61], v157 offset:0\n\t"
        "v_xor_b32 v159, 32, v157\n\t"
        "s_waitcnt lgkmcnt(10)\n\t"
        "v_mfma_f32_16x16x32_bf16 v[52:55], v[64:67], v[164:167], 0\n\t"
        "ds_read_b64_tr_b16 v[62:63], v158 offset:0\n\t"
        "v_xor_b32 v152, 32, v158\n\t"
        "v_mfma_f32_16x16x32_bf16 v[16:19], a[220:223], v[96:99], v[16:19]\n\t"
        "ds_read_b64_tr_b16 v[64:65], v159 offset:0\n\t"
        "s_waitcnt lgkmcnt(10)\n\t"
        "v_mfma_f32_16x16x32_bf16 v[56:59], v[68:71], v[164:167], 0\n\t"
        "ds_read_b64_tr_b16 v[66:67], v152 offset:0\n\t"
        "v_mfma_f32_16x16x32_bf16 v[20:23], a[224:227], v[96:99], v[20:23]\n\t"
        "ds_read_b64_tr_b16 v[68:69], v157 offset:64\n\t"
        "s_waitcnt lgkmcnt(10)\n\t"
        "v_mfma_f32_16x16x32_bf16 v[116:119], v[72:75], v[164:167], 0\n\t"
        "s_waitcnt vmcnt(0)\n\t"
        "s_barrier\n\t"
        "v_mfma_f32_16x16x32_bf16 v[24:27], a[228:231], v[96:99], v[24:27]\n\t"
        "ds_read_b64_tr_b16 v[70:71], v158 offset:64\n\t"
        "s_waitcnt lgkmcnt(8)\n\t"
        "v_mfma_f32_16x16x32_bf16 v[120:123], v[76:79], v[164:167], 0\n\t"
        "ds_read_b64_tr_b16 v[72:73], v159 offset:64\n\t"
        "v_mfma_f32_16x16x32_bf16 v[28:31], a[232:235], v[96:99], v[28:31]\n\t"
        "ds_read_b64_tr_b16 v[74:75], v152 offset:64\n\t"
        "s_waitcnt lgkmcnt(8)\n\t"
        "v_mfma_f32_16x16x32_bf16 v[124:127], v[80:83], v[164:167], 0\n\t"
        "ds_read_b64_tr_b16 v[76:77], v157 offset:128\n\t"
        "v_perm_b32 v164, v163, v163, v168\n\t"
        "v_mfma_f32_16x16x32_bf16 v[32:35], a[236:239], v[96:99], v[32:35]\n\t"
        "ds_read_b128 v[108:111], v129 offset:20608\n\t"
        "v_perm_b32 v165, v163, v163, v169\n\t"
        "v_mfma_f32_16x16x32_bf16 v[12:15], a[240:243], v[100:103], v[12:15]\n\t"
        "ds_read_b64_tr_b16 v[78:79], v158 offset:128\n\t"
        "v_perm_b32 v166, v163, v163, v170\n\t"
        "v_mfma_f32_16x16x32_bf16 v[16:19], a[244:247], v[100:103], v[16:19]\n\t"
        "ds_read_b64_tr_b16 v[80:81], v159 offset:128\n\t"
        "v_perm_b32 v167, v163, v163, v171\n\t"
        "v_mfma_f32_16x16x32_bf16 v[20:23], a[248:251], v[100:103], v[20:23]\n\t"
        "ds_read_b128 v[112:115], v130 offset:20480\n\t"
        "s_waitcnt lgkmcnt(9)\n\t"
        "v_mfma_f32_16x16x32_bf16 v[48:51], v[60:63], v[164:167], v[48:51]\n\t"
        "ds_read_b64_tr_b16 v[82:83], v152 offset:128\n\t"
        "v_mfma_f32_16x16x32_bf16 v[24:27], a[216:219], v[100:103], v[24:27]\n\t"
        "ds_read_b128 v[160:163], v185 offset:256\n\t"
        "ds_read_b64_tr_b16 v[198:199], v131 offset:31488\n\t"
        "v_mfma_f32_16x16x32_bf16 v[52:55], v[64:67], v[164:167], v[52:55]\n\t"
        "ds_read_b64_tr_b16 v[200:201], v132 offset:30720\n\t"
        "ds_read_b64_tr_b16 v[202:203], v132 offset:31488\n\t"
        "v_mfma_f32_16x16x32_bf16 v[28:31], a[220:223], v[100:103], v[28:31]\n\t"
        "ds_read_b64_tr_b16 v[204:205], v133 offset:30720\n\t"
        "ds_read_b64_tr_b16 v[206:207], v133 offset:31488\n\t"
        "s_waitcnt lgkmcnt(12)\n\t"
        "v_mfma_f32_16x16x32_bf16 v[56:59], v[68:71], v[164:167], v[56:59]\n\t"
        "ds_read_b128 v[84:87], v130 offset:64\n\t"
        "v_cvt_pk_bf16_f32 v48, v48, v49\n\t"
        "v_mfma_f32_16x16x32_bf16 v[32:35], a[224:227], v[100:103], v[32:35]\n\t"
        "ds_read_b128 v[88:91], v130 offset:4160\n\t"
        "v_cvt_pk_bf16_f32 v49, v50, v51\n\t"
        "v_mfma_f32_16x16x32_bf16 v[116:119], v[72:75], v[164:167], v[116:119]\n\t"
        "ds_write_b64 v186, v[48:49] offset:0\n\t"
        "v_mfma_f32_16x16x32_bf16 v[36:39], a[228:231], v[100:103], v[36:39]\n\t"
        "ds_read_b64_tr_b16 v[208:209], v131 offset:36864\n\t"
        "v_cvt_pk_bf16_f32 v52, v52, v53\n\t"
        "s_waitcnt lgkmcnt(12)\n\t"
        "v_mfma_f32_16x16x32_bf16 v[120:123], v[76:79], v[164:167], v[120:123]\n\t"
        "ds_read_b64_tr_b16 v[210:211], v131 offset:37632\n\t"
        "v_cvt_pk_bf16_f32 v53, v54, v55\n\t"
        "v_mfma_f32_16x16x32_bf16 v[40:43], a[232:235], v[100:103], v[40:43]\n\t"
        "ds_write_b64 v154, v[52:53] offset:0\n\t"
        "s_waitcnt lgkmcnt(11)\n\t"
        "v_mfma_f32_16x16x32_bf16 v[124:127], v[80:83], v[164:167], v[124:127]\n\t"
        "ds_read_b64_tr_b16 v[212:213], v132 offset:36864\n\t"
        "v_cvt_pk_bf16_f32 v56, v56, v57\n\t"
        "v_mfma_f32_16x16x32_bf16 v[44:47], a[236:239], v[100:103], v[44:47]\n\t"
        "ds_read_b64_tr_b16 v[214:215], v132 offset:37632\n\t"
        "v_cvt_pk_bf16_f32 v57, v58, v59\n\t"
        "v_mfma_f32_16x16x32_bf16 v[24:27], a[240:243], v[104:107], v[24:27]\n\t"
        "ds_write_b64 v186, v[56:57] offset:64\n\t"
        "v_mfma_f32_16x16x32_bf16 v[28:31], a[244:247], v[104:107], v[28:31]\n\t"
        "ds_read_b64_tr_b16 v[216:217], v133 offset:36864\n\t"
        "v_cvt_pk_bf16_f32 v116, v116, v117\n\t"
        "v_mfma_f32_16x16x32_bf16 v[32:35], a[248:251], v[104:107], v[32:35]\n\t"
        "ds_read_b64_tr_b16 v[218:219], v133 offset:37632\n\t"
        "v_cvt_pk_bf16_f32 v117, v118, v119\n\t"
        "v_mfma_f32_16x16x32_bf16 v[36:39], a[216:219], v[104:107], v[36:39]\n\t"
        "ds_write_b64 v154, v[116:117] offset:64\n\t"
        "v_mfma_f32_16x16x32_bf16 v[40:43], a[220:223], v[104:107], v[40:43]\n\t"
        "ds_read_b128 v[92:95], v130 offset:8256\n\t"
        "v_cvt_pk_bf16_f32 v120, v120, v121\n\t"
        "v_mfma_f32_16x16x32_bf16 v[44:47], a[224:227], v[104:107], v[44:47]\n\t"
        "ds_read_b64_tr_b16 v[220:221], v131 offset:43008\n\t"
        "v_cvt_pk_bf16_f32 v121, v122, v123\n\t"
        "v_mfma_f32_16x16x32_bf16 v[36:39], a[204:207], v[108:111], v[36:39]\n\t"
        "ds_write_b64 v186, v[120:121] offset:128\n\t"
        "v_mfma_f32_16x16x32_bf16 v[40:43], a[208:211], v[108:111], v[40:43]\n\t"
        "ds_read_b64_tr_b16 v[222:223], v131 offset:43776\n\t"
        "v_cvt_pk_bf16_f32 v124, v124, v125\n\t"
        "v_mfma_f32_16x16x32_bf16 v[44:47], a[212:215], v[108:111], v[44:47]\n\t"
        "ds_read_b64_tr_b16 v[224:225], v132 offset:43008\n\t"
        "v_cvt_pk_bf16_f32 v125, v126, v127\n\t"
        "v_mfma_f32_16x16x32_bf16 v[36:39], a[240:243], v[112:115], v[36:39]\n\t"
        "ds_write_b64 v154, v[124:125] offset:128\n\t"
        "v_mfma_f32_16x16x32_bf16 v[40:43], a[244:247], v[112:115], v[40:43]\n\t"
        "ds_read_b128 v[96:99], v130 offset:12352\n\t"
        "v_lshlrev_b32_sdwa v157, 5, v160 dst_sel:DWORD dst_unused:UNUSED_PAD src0_sel:DWORD src1_sel:WORD_0\n\t"
        "v_mfma_f32_16x16x32_bf16 v[44:47], a[248:251], v[112:115], v[44:47]\n\t"
        "ds_read_b64_tr_b16 v[226:227], v132 offset:43776\n\t"
        "v_lshlrev_b32_sdwa v158, 5, v160 dst_sel:DWORD dst_unused:UNUSED_PAD src0_sel:DWORD src1_sel:WORD_1\n\t"
        "s_waitcnt lgkmcnt(9)\n\t"
        "v_mfma_f32_16x16x32_bf16 v[0:3], v[196:199], v[84:87], v[0:3]\n\t"
        "ds_read_b64_tr_b16 v[228:229], v133 offset:43008\n\t"
        "v_or_b32 v157, v184, v157\n\t"
        "v_mfma_f32_16x16x32_bf16 v[4:7], v[200:203], v[84:87], v[4:7]\n\t"
        "ds_read_b64_tr_b16 v[60:61], v157 offset:0\n\t"
        "v_or_b32 v158, v184, v158\n\t"
        "v_mfma_f32_16x16x32_bf16 v[8:11], v[204:207], v[84:87], v[8:11]\n\t"
        "ds_read_b64_tr_b16 v[62:63], v158 offset:0\n\t"
        "v_xor_b32 v159, 32, v157\n\t"
        "v_mfma_f32_16x16x32_bf16 v[0:3], a[252:255], v[88:91], v[0:3]\n\t"
        "ds_read_b64_tr_b16 v[64:65], v159 offset:0\n\t"
        "v_xor_b32 v152, 32, v158\n\t"
        "v_mfma_f32_16x16x32_bf16 v[4:7], v[188:191], v[88:91], v[4:7]\n\t"
        "ds_read_b64_tr_b16 v[66:67], v152 offset:0\n\t"
        "v_perm_b32 v164, v162, v162, v168\n\t"
        "v_mfma_f32_16x16x32_bf16 v[8:11], v[192:195], v[88:91], v[8:11]\n\t"
        "ds_read_b64_tr_b16 v[68:69], v157 offset:64\n\t"
        "v_perm_b32 v165, v162, v162, v169\n\t"
        "v_mfma_f32_16x16x32_bf16 v[12:15], v[196:199], v[88:91], v[12:15]\n\t"
        "ds_read_b64_tr_b16 v[70:71], v158 offset:64\n\t"
        "ds_read_b128 v[100:103], v130 offset:16448\n\t"
        "v_perm_b32 v166, v162, v162, v170\n\t"
        "v_mfma_f32_16x16x32_bf16 v[16:19], v[200:203], v[88:91], v[16:19]\n\t"
        "ds_read_b64_tr_b16 v[72:73], v159 offset:64\n\t"
        "ds_read_b64_tr_b16 v[230:231], v133 offset:43776\n\t"
        "v_perm_b32 v167, v162, v162, v171\n\t"
        "v_mfma_f32_16x16x32_bf16 v[20:23], v[204:207], v[88:91], v[20:23]\n\t"
        "ds_read_b64_tr_b16 v[74:75], v152 offset:64\n\t"
        "s_waitcnt lgkmcnt(11)\n\t"
        "v_mfma_f32_16x16x32_bf16 v[0:3], v[208:211], v[92:95], v[0:3]\n\t"
        "ds_read_b64_tr_b16 v[76:77], v157 offset:128\n\t"
        "v_lshlrev_b32_sdwa v157, 5, v161 dst_sel:DWORD dst_unused:UNUSED_PAD src0_sel:DWORD src1_sel:WORD_0\n\t"
        "v_mfma_f32_16x16x32_bf16 v[4:7], v[212:215], v[92:95], v[4:7]\n\t"
        "ds_read_b64_tr_b16 v[78:79], v158 offset:128\n\t"
        "v_lshlrev_b32_sdwa v158, 5, v161 dst_sel:DWORD dst_unused:UNUSED_PAD src0_sel:DWORD src1_sel:WORD_1\n\t"
        "v_mfma_f32_16x16x32_bf16 v[8:11], v[216:219], v[92:95], v[8:11]\n\t"
        "ds_read_b64_tr_b16 v[80:81], v159 offset:128\n\t"
        "v_or_b32 v157, v184, v157\n\t"
        "s_waitcnt lgkmcnt(11)\n\t"
        "v_mfma_f32_16x16x32_bf16 v[48:51], v[60:63], v[164:167], 0\n\t"
        "ds_read_b64_tr_b16 v[82:83], v152 offset:128\n\t"
        "v_or_b32 v158, v184, v158\n\t"
        "v_mfma_f32_16x16x32_bf16 v[12:15], a[252:255], v[92:95], v[12:15]\n\t"
        "ds_read_b64_tr_b16 v[60:61], v157 offset:0\n\t"
        "v_xor_b32 v159, 32, v157\n\t"
        "s_waitcnt lgkmcnt(11)\n\t"
        "v_mfma_f32_16x16x32_bf16 v[52:55], v[64:67], v[164:167], 0\n\t"
        "ds_read_b64_tr_b16 v[62:63], v158 offset:0\n\t"
        "v_xor_b32 v152, 32, v158\n\t"
        "v_mfma_f32_16x16x32_bf16 v[16:19], v[188:191], v[92:95], v[16:19]\n\t"
        "ds_read_b64_tr_b16 v[64:65], v159 offset:0\n\t"
        "s_waitcnt lgkmcnt(10)\n\t"
        "v_mfma_f32_16x16x32_bf16 v[56:59], v[68:71], v[164:167], 0\n\t"
        "ds_read_b64_tr_b16 v[66:67], v152 offset:0\n\t"
        "v_mfma_f32_16x16x32_bf16 v[20:23], v[192:195], v[92:95], v[20:23]\n\t"
        "ds_read_b64_tr_b16 v[68:69], v157 offset:64\n\t"
        "s_waitcnt lgkmcnt(9)\n\t"
        "v_mfma_f32_16x16x32_bf16 v[116:119], v[72:75], v[164:167], 0\n\t"
        "ds_read_b64_tr_b16 v[70:71], v158 offset:64\n\t"
        "v_mfma_f32_16x16x32_bf16 v[24:27], v[196:199], v[92:95], v[24:27]\n\t"
        "ds_read_b64_tr_b16 v[72:73], v159 offset:64\n\t"
        "s_waitcnt lgkmcnt(9)\n\t"
        "v_mfma_f32_16x16x32_bf16 v[120:123], v[76:79], v[164:167], 0\n\t"
        "ds_read_b64_tr_b16 v[74:75], v152 offset:64\n\t"
        "v_mfma_f32_16x16x32_bf16 v[28:31], v[200:203], v[92:95], v[28:31]\n\t"
        "ds_read_b64_tr_b16 v[76:77], v157 offset:128\n\t"
        "s_waitcnt lgkmcnt(9)\n\t"
        "v_mfma_f32_16x16x32_bf16 v[124:127], v[80:83], v[164:167], 0\n\t"
        "ds_read_b64_tr_b16 v[78:79], v158 offset:128\n\t"
        ".LpX1%=:\n\t"
        "s_cbranch_execz .LrX%=\n\t"
        "v_perm_b32 v164, v163, v163, v168\n\t"
        "v_mfma_f32_16x16x32_bf16 v[32:35], v[204:207], v[92:95], v[32:35]\n\t"
        "ds_read_b128 v[104:107], v130 offset:20544\n\t"
        "v_perm_b32 v165, v163, v163, v169\n\t"
        "v_mfma_f32_16x16x32_bf16 v[12:15], v[208:211], v[96:99], v[12:15]\n\t"
        "ds_read_b64_tr_b16 v[80:81], v159 offset:128\n\t"
        "ds_read_b64_tr_b16 v[232:233], v131 offset:49152\n\t"
        "v_perm_b32 v166, v163, v163, v170\n\t"
        "v_mfma_f32_16x16x32_bf16 v[16:19], v[212:215], v[96:99], v[16:19]\n\t"
        "ds_read_b64_tr_b16 v[82:83], v152 offset:128\n\t"
        "ds_read_b64_tr_b16 v[234:235], v131 offset:49920\n\t"
        "v_perm_b32 v167, v163, v163, v171\n\t"
        "v_mfma_f32_16x16x32_bf16 v[20:23], v[216:219], v[96:99], v[20:23]\n\t"
        "ds_read_b64_tr_b16 v[236:237], v132 offset:49152\n\t"
        "ds_read_b64_tr_b16 v[238:239], v132 offset:49920\n\t"
        "s_waitcnt lgkmcnt(11)\n\t"
        "v_mfma_f32_16x16x32_bf16 v[48:51], v[60:63], v[164:167], v[48:51]\n\t"
        "ds_read_b64_tr_b16 v[240:241], v133 offset:49152\n\t"
        "ds_read_b64_tr_b16 v[242:243], v133 offset:49920\n\t"
        "v_mfma_f32_16x16x32_bf16 v[24:27], a[252:255], v[96:99], v[24:27]\n\t"
        "ds_read_b128 v[108:111], v130 offset:128\n\t"
        "v_mfma_f32_16x16x32_bf16 v[52:55], v[64:67], v[164:167], v[52:55]\n\t"
        "ds_read_b128 v[112:115], v130 offset:4224\n\t"
        "v_mfma_f32_16x16x32_bf16 v[28:31], v[188:191], v[96:99], v[28:31]\n\t"
        "ds_read_b64_tr_b16 v[244:245], v131 offset:55296\n\t"
        "v_mfma_f32_16x16x32_bf16 v[56:59], v[68:71], v[164:167], v[56:59]\n\t"
        "ds_read_b64_tr_b16 v[246:247], v131 offset:56064\n\t"
        "v_cvt_pk_bf16_f32 v48, v48, v49\n\t"
        "v_mfma_f32_16x16x32_bf16 v[32:35], v[192:195], v[96:99], v[32:35]\n\t"
        "ds_read_b64_tr_b16 v[248:249], v132 offset:55296\n\t"
        "v_cvt_pk_bf16_f32 v49, v50, v51\n\t"
        "s_waitcnt lgkmcnt(12)\n\t"
        "v_mfma_f32_16x16x32_bf16 v[116:119], v[72:75], v[164:167], v[116:119]\n\t"
        "ds_write_b64 v186, v[48:49] offset:12288\n\t"
        "v_mfma_f32_16x16x32_bf16 v[36:39], v[196:199], v[96:99], v[36:39]\n\t"
        "ds_read_b64_tr_b16 v[250:251], v132 offset:56064\n\t"
        "v_cvt_pk_bf16_f32 v52, v52, v53\n\t"
        "v_mfma_f32_16x16x32_bf16 v[120:123], v[76:79], v[164:167], v[120:123]\n\t"
        "ds_read_b64_tr_b16 v[252:253], v133 offset:55296\n\t"
        "v_cvt_pk_bf16_f32 v53, v54, v55\n\t"
        "v_mfma_f32_16x16x32_bf16 v[40:43], v[200:203], v[96:99], v[40:43]\n\t"
        "ds_write_b64 v154, v[52:53] offset:12288\n\t"
        "s_waitcnt lgkmcnt(9)\n\t"
        "v_mfma_f32_16x16x32_bf16 v[124:127], v[80:83], v[164:167], v[124:127]\n\t"
        "ds_read_b64_tr_b16 v[254:255], v133 offset:56064\n\t"
        "v_cvt_pk_bf16_f32 v56, v56, v57\n\t"
        "v_mfma_f32_16x16x32_bf16 v[44:47], v[204:207], v[96:99], v[44:47]\n\t"
        "ds_read_b128 v[84:87], v130 offset:8320\n\t"
        "v_cvt_pk_bf16_f32 v57, v58, v59\n\t"
        "v_mfma_f32_16x16x32_bf16 v[24:27], v[208:211], v[100:103], v[24:27]\n\t"
        "ds_write_b64 v186, v[56:57] offset:12352\n\t"
        "v_mfma_f32_16x16x32_bf16 v[28:31], v[212:215], v[100:103], v[28:31]\n\t"
        "v_cvt_pk_bf16_f32 v116, v116, v117\n\t"
        "v_cvt_pk_bf16_f32 v117, v118, v119\n\t"
        "v_mfma_f32_16x16x32_bf16 v[32:35], v[216:219], v[100:103], v[32:35]\n\t"
        "ds_write_b64 v154, v[116:117] offset:12352\n\t"
        "v_mfma_f32_16x16x32_bf16 v[36:39], a[252:255], v[100:103], v[36:39]\n\t"
        "v_cvt_pk_bf16_f32 v120, v120, v121\n\t"
        "v_cvt_pk_bf16_f32 v121, v122, v123\n\t"
        "v_mfma_f32_16x16x32_bf16 v[40:43], v[188:191], v[100:103], v[40:43]\n\t"
        "ds_write_b64 v186, v[120:121] offset:12416\n\t"
        "v_mfma_f32_16x16x32_bf16 v[44:47], v[192:195], v[100:103], v[44:47]\n\t"
        "v_cvt_pk_bf16_f32 v124, v124, v125\n\t"
        "v_cvt_pk_bf16_f32 v125, v126, v127\n\t"
        "v_mfma_f32_16x16x32_bf16 v[36:39], v[208:211], v[104:107], v[36:39]\n\t"
        "ds_write_b64 v154, v[124:125] offset:12416\n\t"
        "v_mfma_f32_16x16x32_bf16 v[40:43], v[212:215], v[104:107], v[40:43]\n\t"
        "ds_read_b128 v[88:91], v130 offset:12416\n\t"
        "v_mfma_f32_16x16x32_bf16 v[44:47], v[216:219], v[104:107], v[44:47]\n\t"
        "s_waitcnt lgkmcnt(5)\n\t"
        "v_mfma_f32_16x16x32_bf16 v[0:3], v[232:235], v[108:111], v[0:3]\n\t"
        "v_mfma_f32_16x16x32_bf16 v[4:7], v[236:239], v[108:111], v[4:7]\n\t"
        "v_mfma_f32_16x16x32_bf16 v[8:11], v[240:243], v[108:111], v[8:11]\n\t"
        "v_mfma_f32_16x16x32_bf16 v[0:3], v[220:223], v[112:115], v[0:3]\n\t"
        "s_waitcnt lgkmcnt(1)\n\t"
        "s_barrier\n\t"
        "v_mfma_f32_16x16x32_bf16 v[4:7], v[224:227], v[112:115], v[4:7]\n\t"
        "ds_read_b128 v[124:127], v182 offset:0\n\t"
        "v_mfma_f32_16x16x32_bf16 v[8:11], v[228:231], v[112:115], v[8:11]\n\t"
        "ds_read_b128 v[132:135], v187 offset:0\n\t"
        "v_mfma_f32_16x16x32_bf16 v[12:15], v[232:235], v[112:115], v[12:15]\n\t"
        "ds_read_b128 v[136:139], v187 offset:16\n\t"
        "v_mfma_f32_16x16x32_bf16 v[16:19], v[236:239], v[112:115], v[16:19]\n\t"
        "ds_read_b32 v140, v187 offset:32\n\t"
        "v_mfma_f32_16x16x32_bf16 v[20:23], v[240:243], v[112:115], v[20:23]\n\t"
        "ds_read_b128 v[92:95], v130 offset:16512\n\t"
        "v_mfma_f32_16x16x32_bf16 v[0:3], v[244:247], v[84:87], v[0:3]\n\t"
        "v_mfma_f32_16x16x32_bf16 v[4:7], v[248:251], v[84:87], v[4:7]\n\t"
        "v_mfma_f32_16x16x32_bf16 v[8:11], v[252:255], v[84:87], v[8:11]\n\t"
        "v_mfma_f32_16x16x32_bf16 v[12:15], v[220:223], v[84:87], v[12:15]\n\t"
        "ds_read_b128 v[96:99], v130 offset:20608\n\t"
        "v_mfma_f32_16x16x32_bf16 v[16:19], v[224:227], v[84:87], v[16:19]\n\t"
        "s_waitcnt lgkmcnt(5)\n\t"
        "v_or3_b32 v124, v124, v125, v126\n\t"
        "v_or_b32 v124, v124, v127\n\t"
        "v_mfma_f32_16x16x32_bf16 v[20:23], v[228:231], v[84:87], v[20:23]\n\t"
        "v_readfirstlane_b32 s6, v124\n\t"
        "v_mfma_f32_16x16x32_bf16 v[24:27], v[232:235], v[84:87], v[24:27]\n\t"
        "s_nop 0\n\t"
        "s_bitcmp1_b32 s6, 1\n\t"
        "s_cbranch_scc1 .Lx66%=\n\t"
        ".Lx67%=:\n\t"
        "v_mfma_f32_16x16x32_bf16 v[28:31], v[236:239], v[84:87], v[28:31]\n\t"
        "ds_read_b128 v[160:163], v185 offset:512\n\t"
        "s_waitcnt lgkmcnt(4)\n\t"
        "v_xor_b32 v132, v181, v132\n\t"
        "v_add_u32 v151, s9, v172\n\t"
        "v_mfma_f32_16x16x32_bf16 v[32:35], v[240:243], v[84:87], v[32:35]\n\t"
        "v_max_u32 v151, v151, v132\n\t"
        "v_xor_b32 v133, v181, v133\n\t"
        "v_add_u32 v183, s9, v173\n\t"
        "v_mfma_f32_16x16x32_bf16 v[12:15], v[244:247], v[88:91], v[12:15]\n\t"
        "ds_read_b128 v[100:103], v151 offset:0\n\t"
        "v_max_u32 v183, v183, v133\n\t"
        "v_xor_b32 v134, v181, v134\n\t"
        "v_mfma_f32_16x16x32_bf16 v[16:19], v[248:251], v[88:91], v[16:19]\n\t"
        "ds_read_b128 v[104:107], v151 offset:64\n\t"
        "s_add_u32 s55, s41, 0\n\t"
        "v_cvt_pk_bf16_f32 v116, v0, v1\n\t"
        "v_mfma_f32_16x16x32_bf16 v[20:23], v[252:255], v[88:91], v[20:23]\n\t"
        "v_cvt_pk_bf16_f32 v117, v2, v3\n\t"
        "v_cvt_pk_bf16_f32 v118, v4, v5\n\t"
        "v_cvt_pk_bf16_f32 v119, v6, v7\n\t"
        "v_mfma_f32_16x16x32_bf16 v[24:27], v[220:223], v[88:91], v[24:27]\n\t"
        "ds_read_b128 v[108:111], v151 offset:128\n\t"
        "v_add_u32 v151, s9, v174\n\t"
        "v_max_u32 v151, v151, v134\n\t"
        "v_mfma_f32_16x16x32_bf16 v[28:31], v[224:227], v[88:91], v[28:31]\n\t"
        "v_xor_b32 v135, v181, v135\n\t"
        "v_cvt_pk_bf16_f32 v120, v8, v9\n\t"
        "v_cvt_pk_bf16_f32 v121, v10, v11\n\t"
        "v_mfma_f32_16x16x32_bf16 v[32:35], v[228:231], v[88:91], v[32:35]\n\t"
        "v_permlane16_swap_b32 v116, v118\n\t"
        "v_mfma_f32_16x16x32_bf16 v[36:39], v[232:235], v[88:91], v[36:39]\n\t"
        "v_permlane16_swap_b32 v117, v119\n\t"
        "v_mfma_f32_16x16x32_bf16 v[40:43], v[236:239], v[88:91], v[40:43]\n\t"
        "buffer_store_dwordx4 v[116:119], v144, s[28:31], s55 offen sc1\n\t"
        "v_mfma_f32_16x16x32_bf16 v[44:47], v[240:243], v[88:91], v[44:47]\n\t"
        "ds_write_b64 v146, v[120:121] offset:0\n\t"
        "s_add_u32 s55, s41, 43008\n\t"
        "v_cvt_pk_bf16_f32 v116, v12, v13\n\t"
        "s_waitcnt lgkmcnt(2)\n\t"
        "v_mfma_f32_16x16x32_bf16 v[24:27], v[244:247], v[92:95], v[24:27]\n\t"
        "ds_read_b128 v[112:115], v183 offset:0\n\t"
        "v_cvt_pk_bf16_f32 v117, v14, v15\n\t"
        "v_cvt_pk_bf16_f32 v118, v16, v17\n\t"
        "v_mfma_f32_16x16x32_bf16 v[28:31], v[248:251], v[92:95], v[28:31]\n\t"
        "ds_read_b128 v[84:87], v183 offset:64\n\t"
        "v_cvt_pk_bf16_f32 v119, v18, v19\n\t"
        "v_cvt_pk_bf16_f32 v120, v20, v21\n\t"
        "v_mfma_f32_16x16x32_bf16 v[32:35], v[252:255], v[92:95], v[32:35]\n\t"
        "v_cvt_pk_bf16_f32 v121, v22, v23\n\t"
        "v_permlane16_swap_b32 v116, v118\n\t"
        "v_mfma_f32_16x16x32_bf16 v[36:39], v[220:223], v[92:95], v[36:39]\n\t"
        "v_permlane16_swap_b32 v117, v119\n\t"
        "v_lshlrev_b32_sdwa v157, 5, v160 dst_sel:DWORD dst_unused:UNUSED_PAD src0_sel:DWORD src1_sel:WORD_0\n\t"
        "v_mfma_f32_16x16x32_bf16 v[40:43], v[224:227], v[92:95], v[40:43]\n\t"
        "ds_read_b128 v[88:91], v183 offset:128\n\t"
        "v_add_u32 v183, s9, v175\n\t"
        "v_max_u32 v183, v183, v135\n\t"
        "v_mfma_f32_16x16x32_bf16 v[44:47], v[228:231], v[92:95], v[44:47]\n\t"
        "v_xor_b32 v136, v181, v136\n\t"
        "v_lshlrev_b32_sdwa v158, 5, v160 dst_sel:DWORD dst_unused:UNUSED_PAD src0_sel:DWORD src1_sel:WORD_1\n\t"
        "v_or_b32 v157, v184, v157\n\t"
        "v_mfma_f32_16x16x32_bf16 v[36:39], v[244:247], v[96:99], v[36:39]\n\t"
        "ds_read_b64_tr_b16 v[60:61], v157 offset:0\n\t"
        "ds_read_b64_tr_b16 v[68:69], v157 offset:64\n\t"
        "v_or_b32 v158, v184, v158\n\t"
        "v_mfma_f32_16x16x32_bf16 v[40:43], v[248:251], v[96:99], v[40:43]\n\t"
        "ds_read_b64_tr_b16 v[62:63], v158 offset:0\n\t"
        "ds_read_b64_tr_b16 v[70:71], v158 offset:64\n\t"
        "v_xor_b32 v159, 32, v157\n\t"
        "v_mfma_f32_16x16x32_bf16 v[44:47], v[252:255], v[96:99], v[44:47]\n\t"
        "ds_read_b64_tr_b16 v[64:65], v159 offset:0\n\t"
        "ds_read_b64_tr_b16 v[72:73], v159 offset:64\n\t"
        "v_xor_b32 v152, 32, v158\n\t"
        "v_mfma_f32_16x16x32_bf16 v[48:51], a[12:15], v[100:103], 0\n\t"
        "ds_read_b64_tr_b16 v[66:67], v152 offset:0\n\t"
        "ds_read_b64_tr_b16 v[74:75], v152 offset:64\n\t"
        "v_perm_b32 v164, v162, v162, v168\n\t"
        "v_mfma_f32_16x16x32_bf16 v[52:55], a[16:19], v[100:103], 0\n\t"
        "ds_read_b64_tr_b16 v[76:77], v157 offset:128\n\t"
        "ds_read_b64_tr_b16 v[78:79], v158 offset:128\n\t"
        "v_perm_b32 v165, v162, v162, v169\n\t"
        "v_mfma_f32_16x16x32_bf16 v[56:59], a[20:23], v[100:103], 0\n\t"
        "ds_read_b128 v[92:95], v151 offset:0\n\t"
        "ds_read_b64_tr_b16 v[80:81], v159 offset:128\n\t"
        "v_perm_b32 v166, v162, v162, v170\n\t"
        "v_mfma_f32_16x16x32_bf16 v[48:51], a[48:51], v[104:107], v[48:51]\n\t"
        "buffer_store_dwordx4 v[116:119], v144, s[28:31], s55 offen sc1\n\t"
        "v_mfma_f32_16x16x32_bf16 v[52:55], a[52:55], v[104:107], v[52:55]\n\t"
        "ds_write_b64 v146, v[120:121] offset:2048\n\t"
        "s_add_u32 s55, s41, 86016\n\t"
        "v_mfma_f32_16x16x32_bf16 v[56:59], a[56:59], v[104:107], v[56:59]\n\t"
        "ds_read_b128 v[96:99], v151 offset:64\n\t"
        "ds_read_b64_tr_b16 v[82:83], v152 offset:128\n\t"
        "v_cvt_pk_bf16_f32 v116, v24, v25\n\t"
        "s_waitcnt lgkmcnt(9)\n\t"
        "v_mfma_f32_16x16x32_bf16 v[48:51], a[84:87], v[108:111], v[48:51]\n\t"
        "ds_read_b128 v[100:103], v151 offset:128\n\t"
        "v_add_u32 v151, s9, v176\n\t"
        "v_max_u32 v151, v151, v136\n\t"
        "v_mfma_f32_16x16x32_bf16 v[52:55], a[88:91], v[108:111], v[52:55]\n\t"
        "ds_read_b128 v[104:107], v183 offset:0\n\t"
        "v_xor_b32 v137, v181, v137\n\t"
        "v_cvt_pk_bf16_f32 v117, v26, v27\n\t"
        "v_mfma_f32_16x16x32_bf16 v[56:59], a[92:95], v[108:111], v[56:59]\n\t"
        "v_cvt_pk_bf16_f32 v118, v28, v29\n\t"
        "v_cvt_pk_bf16_f32 v119, v30, v31\n\t"
        "v_cvt_pk_bf16_f32 v120, v32, v33\n\t"
        "v_mfma_f32_16x16x32_bf16 v[48:51], a[120:123], v[112:115], v[48:51]\n\t"
        "ds_read_b128 v[108:111], v183 offset:64\n\t"
        "v_cvt_pk_bf16_f32 v121, v34, v35\n\t"
        "v_perm_b32 v167, v162, v162, v171\n\t"
        "v_mfma_f32_16x16x32_bf16 v[52:55], a[124:127], v[112:115], v[52:55]\n\t"
        "v_permlane16_swap_b32 v116, v118\n\t"
        "v_lshlrev_b32_sdwa v157, 5, v161 dst_sel:DWORD dst_unused:UNUSED_PAD src0_sel:DWORD src1_sel:WORD_0\n\t"
        "v_mfma_f32_16x16x32_bf16 v[0:3], v[60:63], v[164:167], 0\n\t"
        "v_permlane16_swap_b32 v117, v119\n\t"
        "v_lshlrev_b32_sdwa v158, 5, v161 dst_sel:DWORD dst_unused:UNUSED_PAD src0_sel:DWORD src1_sel:WORD_1\n\t"
        "v_mfma_f32_16x16x32_bf16 v[56:59], a[128:131], v[112:115], v[56:59]\n\t"
        "v_or_b32 v157, v184, v157\n\t"
        "v_or_b32 v158, v184, v158\n\t"
        "v_xor_b32 v159, 32, v157\n\t"
        "s_waitcnt lgkmcnt(4)\n\t"
        "v_mfma_f32_16x16x32_bf16 v[4:7], v[64:67], v[164:167], 0\n\t"
        "ds_read_b64_tr_b16 v[60:61], v157 offset:0\n\t"
        "ds_read_b64_tr_b16 v[62:63], v158 offset:0\n\t"
        "v_xor_b32 v152, 32, v158\n\t"
        "v_mfma_f32_16x16x32_bf16 v[48:51], a[156:159], v[84:87], v[48:51]\n\t"
        "ds_read_b64_tr_b16 v[64:65], v159 offset:0\n\t"
        "ds_read_b64_tr_b16 v[66:67], v152 offset:0\n\t"
        "v_mfma_f32_16x16x32_bf16 v[8:11], v[68:71], v[164:167], 0\n\t"
        "ds_read_b64_tr_b16 v[68:69], v157 offset:64\n\t"
        "ds_read_b64_tr_b16 v[70:71], v158 offset:64\n\t"
        "v_mfma_f32_16x16x32_bf16 v[52:55], a[160:163], v[84:87], v[52:55]\n\t"
        "ds_read_b128 v[112:115], v183 offset:128\n\t"
        "v_add_u32 v183, s9, v177\n\t"
        "v_max_u32 v183, v183, v137\n\t"
        "v_mfma_f32_16x16x32_bf16 v[12:15], v[72:75], v[164:167], 0\n\t"
        "buffer_store_dwordx4 v[116:119], v144, s[28:31], s55 offen sc1\n\t"
        "v_mfma_f32_16x16x32_bf16 v[56:59], a[164:167], v[84:87], v[56:59]\n\t"
        "ds_write_b64 v146, v[120:121] offset:4096\n\t"
        "v_xor_b32 v138, v181, v138\n\t"
        "v_mfma_f32_16x16x32_bf16 v[16:19], v[76:79], v[164:167], 0\n\t"
        "ds_read_b64_tr_b16 v[72:73], v159 offset:64\n\t"
        "ds_read_b64_tr_b16 v[74:75], v152 offset:64\n\t"
        "s_add_u32 s55, s41, 129024\n\t"
        "v_mfma_f32_16x16x32_bf16 v[48:51], a[192:195], v[88:91], v[48:51]\n\t"
        "ds_read_b64_tr_b16 v[76:77], v157 offset:128\n\t"
        "ds_read_b64_tr_b16 v[78:79], v158 offset:128\n\t"
        "v_cvt_pk_bf16_f32 v116, v36, v37\n\t"
        "s_waitcnt lgkmcnt(10)\n\t"
        "v_mfma_f32_16x16x32_bf16 v[20:23], v[80:83], v[164:167], 0\n\t"
        "ds_read_b64_tr_b16 v[80:81], v159 offset:128\n\t"
        "ds_read_b64_tr_b16 v[82:83], v152 offset:128\n\t"
        "v_cvt_pk_bf16_f32 v117, v38, v39\n\t"
        "v_mfma_f32_16x16x32_bf16 v[52:55], a[196:199], v[88:91], v[52:55]\n\t"
        "ds_read_b128 v[84:87], v151 offset:0\n\t"
        "v_cvt_pk_bf16_f32 v118, v40, v41\n\t"
        "v_cvt_pk_bf16_f32 v119, v42, v43\n\t"
        "v_mfma_f32_16x16x32_bf16 v[56:59], a[200:203], v[88:91], v[56:59]\n\t"
        "v_cvt_pk_bf16_f32 v120, v44, v45\n\t"
        "v_cvt_pk_bf16_f32 v121, v46, v47\n\t"
        "v_perm_b32 v164, v163, v163, v168\n\t"
        "v_mfma_f32_16x16x32_bf16 v[48:51], a[228:231], v[92:95], v[48:51]\n\t"
        "v_permlane16_swap_b32 v116, v118\n\t"
        "v_perm_b32 v165, v163, v163, v169\n\t"
        "v_mfma_f32_16x16x32_bf16 v[52:55], a[232:235], v[92:95], v[52:55]\n\t"
        "v_permlane16_swap_b32 v117, v119\n\t"
        "v_perm_b32 v166, v163, v163, v170\n\t"
        "v_mfma_f32_16x16x32_bf16 v[56:59], a[236:239], v[92:95], v[56:59]\n\t"
        "ds_read_b128 v[88:91], v151 offset:64\n\t"
        "v_perm_b32 v167, v163, v163, v171\n\t"
        "v_mfma_f32_16x16x32_bf16 v[48:51], v[196:199], v[96:99], v[48:51]\n\t"
        "ds_read_b128 v[160:163], v185 offset:768\n\t"
        "v_mfma_f32_16x16x32_bf16 v[0:3], v[60:63], v[164:167], v[0:3]\n\t"
        "ds_read_b128 v[92:95], v151 offset:128\n\t"
        "v_add_u32 v151, s9, v178\n\t"
        "v_max_u32 v151, v151, v138\n\t"
        "v_xor_b32 v139, v181, v139\n\t"
        "v_mfma_f32_16x16x32_bf16 v[52:55], v[200:203], v[96:99], v[52:55]\n\t"
        "s_waitcnt lgkmcnt(5)\n\t"
        "v_mfma_f32_16x16x32_bf16 v[4:7], v[64:67], v[164:167], v[4:7]\n\t"
        "v_mfma_f32_16x16x32_bf16 v[56:59], v[204:207], v[96:99], v[56:59]\n\t"
        "buffer_store_dwordx4 v[116:119], v144, s[28:31], s55 offen sc1\n\t"
        "v_mfma_f32_16x16x32_bf16 v[8:11], v[68:71], v[164:167], v[8:11]\n\t"
        "ds_write_b64 v146, v[120:121] offset:6144\n\t"
        "v_cvt_pk_bf16_f32 v0, v0, v1\n\t"
        "v_cvt_pk_bf16_f32 v1, v2, v3\n\t"
        "v_mfma_f32_16x16x32_bf16 v[48:51], v[232:235], v[100:103], v[48:51]\n\t"
        "ds_write_b64 v186, v[0:1] offset:24576\n\t"
        "v_mfma_f32_16x16x32_bf16 v[12:15], v[72:75], v[164:167], v[12:15]\n\t"
        "ds_read_b128 v[96:99], v183 offset:0\n\t"
        "v_cvt_pk_bf16_f32 v4, v4, v5\n\t"
        "v_cvt_pk_bf16_f32 v5, v6, v7\n\t"
        "v_mfma_f32_16x16x32_bf16 v[52:55], v[236:239], v[100:103], v[52:55]\n\t"
        "ds_write_b64 v154, v[4:5] offset:24576\n\t"
        "v_mfma_f32_16x16x32_bf16 v[16:19], v[76:79], v[164:167], v[16:19]\n\t"
        "v_cvt_pk_bf16_f32 v8, v8, v9\n\t"
        "v_cvt_pk_bf16_f32 v9, v10, v11\n\t"
        "s_waitcnt lgkmcnt(4)\n\t"
        "v_lshlrev_b32_sdwa v157, 5, v160 dst_sel:DWORD dst_unused:UNUSED_PAD src0_sel:DWORD src1_sel:WORD_0\n\t"
        "v_lshlrev_b32_sdwa v158, 5, v160 dst_sel:DWORD dst_unused:UNUSED_PAD src0_sel:DWORD src1_sel:WORD_1\n\t"
        "v_or_b32 v157, v184, v157\n\t"
        "v_mfma_f32_16x16x32_bf16 v[56:59], v[240:243], v[100:103], v[56:59]\n\t"
        "ds_write_b64 v186, v[8:9] offset:24640\n\t"
        "v_mfma_f32_16x16x32_bf16 v[20:23], v[80:83], v[164:167], v[20:23]\n\t"
        "ds_read_b64_tr_b16 v[60:61], v157 offset:0\n\t"
        "ds_read_b64_tr_b16 v[68:69], v157 offset:64\n\t"
        "v_cvt_pk_bf16_f32 v12, v12, v13\n\t"
        "v_cvt_pk_bf16_f32 v13, v14, v15\n\t"
        "v_mfma_f32_16x16x32_bf16 v[48:51], a[0:3], v[104:107], v[48:51]\n\t"
        "ds_write_b64 v154, v[12:13] offset:24640\n\t"
        "v_mfma_f32_16x16x32_bf16 v[52:55], a[4:7], v[104:107], v[52:55]\n\t"
        "ds_read_b64_tr_b16 v[76:77], v157 offset:128\n\t"
        "v_cvt_pk_bf16_f32 v16, v16, v17\n\t"
        "v_cvt_pk_bf16_f32 v17, v18, v19\n\t"
        "v_or_b32 v158, v184, v158\n\t"
        "v_mfma_f32_16x16x32_bf16 v[56:59], a[8:11], v[104:107], v[56:59]\n\t"
        "ds_write_b64 v186, v[16:17] offset:24704\n\t"
        "v_mfma_f32_16x16x32_bf16 v[48:51], a[36:39], v[108:111], v[48:51]\n\t"
        "ds_read_b64_tr_b16 v[62:63], v158 offset:0\n\t"
        "ds_read_b64_tr_b16 v[70:71], v158 offset:64\n\t"
        "v_cvt_pk_bf16_f32 v20, v20, v21\n\t"
        "v_cvt_pk_bf16_f32 v21, v22, v23\n\t"
        "v_mfma_f32_16x16x32_bf16 v[52:55], a[40:43], v[108:111], v[52:55]\n\t"
        "ds_write_b64 v154, v[20:21] offset:24704\n\t"
        "v_mfma_f32_16x16x32_bf16 v[56:59], a[44:47], v[108:111], v[56:59]\n\t"
        "ds_read_b128 v[100:103], v183 offset:64\n\t"
        "ds_read_b64_tr_b16 v[78:79], v158 offset:128\n\t"
        "v_xor_b32 v159, 32, v157\n\t"
        "v_xor_b32 v152, 32, v158\n\t"
        "v_mfma_f32_16x16x32_bf16 v[48:51], a[72:75], v[112:115], v[48:51]\n\t"
        "ds_read_b64_tr_b16 v[64:65], v159 offset:0\n\t"
        "ds_read_b64_tr_b16 v[66:67], v152 offset:0\n\t"
        "v_perm_b32 v164, v162, v162, v168\n\t"
        "v_mfma_f32_16x16x32_bf16 v[52:55], a[76:79], v[112:115], v[52:55]\n\t"
        "ds_read_b64_tr_b16 v[72:73], v159 offset:64\n\t"
        "ds_read_b64_tr_b16 v[74:75], v152 offset:64\n\t"
        "v_perm_b32 v165, v162, v162, v169\n\t"
        "v_mfma_f32_16x16x32_bf16 v[56:59], a[80:83], v[112:115], v[56:59]\n\t"
        "ds_read_b128 v[104:107], v183 offset:128\n\t"
        "ds_read_b64_tr_b16 v[80:81], v159 offset:128\n\t"
        "v_add_u32 v183, s9, v179\n\t"
        "v_mfma_f32_16x16x32_bf16 v[48:51], a[108:111], v[84:87], v[48:51]\n\t"
        "ds_read_b64_tr_b16 v[82:83], v152 offset:128\n\t"
        "v_max_u32 v183, v183, v139\n\t"
        "v_perm_b32 v166, v162, v162, v170\n\t"
        "v_mfma_f32_16x16x32_bf16 v[52:55], a[112:115], v[84:87], v[52:55]\n\t"
        "ds_read_b128 v[108:111], v151 offset:0\n\t"
        "v_perm_b32 v167, v162, v162, v171\n\t"
        "v_lshlrev_b32_sdwa v157, 5, v161 dst_sel:DWORD dst_unused:UNUSED_PAD src0_sel:DWORD src1_sel:WORD_0\n\t"
        "v_mfma_f32_16x16x32_bf16 v[56:59], a[116:119], v[84:87], v[56:59]\n\t"
        "ds_read_b128 v[112:115], v151 offset:64\n\t"
        "v_lshlrev_b32_sdwa v158, 5, v161 dst_sel:DWORD dst_unused:UNUSED_PAD src0_sel:DWORD src1_sel:WORD_1\n\t"
        "v_or_b32 v157, v184, v157\n\t"
        "v_mfma_f32_16x16x32_bf16 v[48:51], a[144:147], v[88:91], v[48:51]\n\t"
        "ds_read_b128 v[84:87], v151 offset:128\n\t"
        "v_or_b32 v158, v184, v158\n\t"
        "v_xor_b32 v159, 32, v157\n\t"
        "s_waitcnt lgkmcnt(10)\n\t"
        "v_mfma_f32_16x16x32_bf16 v[0:3], v[60:63], v[164:167], 0\n\t"
        "ds_read_b64_tr_b16 v[60:61], v157 offset:0\n\t"
        "ds_read_b64_tr_b16 v[62:63], v158 offset:0\n\t"
        "v_xor_b32 v152, 32, v158\n\t"
        "v_mfma_f32_16x16x32_bf16 v[52:55], a[148:151], v[88:91], v[52:55]\n\t"
        "v_mfma_f32_16x16x32_bf16 v[8:11], v[68:71], v[164:167], 0\n\t"
        "ds_read_b64_tr_b16 v[68:69], v157 offset:64\n\t"
        "ds_read_b64_tr_b16 v[70:71], v158 offset:64\n\t"
        "v_mfma_f32_16x16x32_bf16 v[56:59], a[152:155], v[88:91], v[56:59]\n\t"
        "s_waitcnt lgkmcnt(8)\n\t"
        "v_mfma_f32_16x16x32_bf16 v[4:7], v[64:67], v[164:167], 0\n\t"
        "ds_read_b64_tr_b16 v[64:65], v159 offset:0\n\t"
        "ds_read_b64_tr_b16 v[66:67], v152 offset:0\n\t"
        "v_mfma_f32_16x16x32_bf16 v[48:51], a[180:183], v[92:95], v[48:51]\n\t"
        "ds_read_b128 v[88:91], v183 offset:0\n\t"
        "v_mfma_f32_16x16x32_bf16 v[12:15], v[72:75], v[164:167], 0\n\t"
        "ds_read_b64_tr_b16 v[72:73], v159 offset:64\n\t"
        "ds_read_b64_tr_b16 v[74:75], v152 offset:64\n\t"
        "v_mfma_f32_16x16x32_bf16 v[52:55], a[184:187], v[92:95], v[52:55]\n\t"
        "v_mfma_f32_16x16x32_bf16 v[16:19], v[76:79], v[164:167], 0\n\t"
        "ds_read_b64_tr_b16 v[76:77], v157 offset:128\n\t"
        "ds_read_b64_tr_b16 v[78:79], v158 offset:128\n\t"
        "v_mfma_f32_16x16x32_bf16 v[56:59], a[188:191], v[92:95], v[56:59]\n\t"
        "s_waitcnt lgkmcnt(9)\n\t"
        "v_mfma_f32_16x16x32_bf16 v[20:23], v[80:83], v[164:167], 0\n\t"
        "ds_read_b64_tr_b16 v[80:81], v159 offset:128\n\t"
        "ds_read_b64_tr_b16 v[82:83], v152 offset:128\n\t"
        "v_perm_b32 v164, v163, v163, v168\n\t"
        "v_perm_b32 v165, v163, v163, v169\n\t"
        "v_perm_b32 v166, v163, v163, v170\n\t"
        "v_mfma_f32_16x16x32_bf16 v[48:51], a[216:219], v[96:99], v[48:51]\n\t"
        "ds_read_b128 v[92:95], v183 offset:64\n\t"
        "v_perm_b32 v167, v163, v163, v171\n\t"
        "v_mfma_f32_16x16x32_bf16 v[52:55], a[220:223], v[96:99], v[52:55]\n\t"
        "s_nop 0\n\t"
        "v_mfma_f32_16x16x32_bf16 v[0:3], v[60:63], v[164:167], v[0:3]\n\t"
        "v_mfma_f32_16x16x32_bf16 v[56:59], a[224:227], v[96:99], v[56:59]\n\t"
        "s_waitcnt lgkmcnt(7)\n\t"
        "v_mfma_f32_16x16x32_bf16 v[4:7], v[64:67], v[164:167], v[4:7]\n\t"
        "v_mfma_f32_16x16x32_bf16 v[48:51], a[252:255], v[100:103], v[48:51]\n\t"
        "ds_read_b128 v[96:99], v183 offset:128\n\t"
        "v_mfma_f32_16x16x32_bf16 v[8:11], v[68:71], v[164:167], v[8:11]\n\t"
        "s_nop 1\n\t"
        "v_cvt_pk_bf16_f32 v0, v0, v1\n\t"
        "v_cvt_pk_bf16_f32 v1, v2, v3\n\t"
        "v_mfma_f32_16x16x32_bf16 v[52:55], v[188:191], v[100:103], v[52:55]\n\t"
        "ds_write_b64 v186, v[0:1] offset:36864\n\t"
        "s_waitcnt lgkmcnt(4)\n\t"
        "v_mfma_f32_16x16x32_bf16 v[12:15], v[72:75], v[164:167], v[12:15]\n\t"
        "v_cvt_pk_bf16_f32 v4, v4, v5\n\t"
        "v_cvt_pk_bf16_f32 v5, v6, v7\n\t"
        "v_mfma_f32_16x16x32_bf16 v[56:59], v[192:195], v[100:103], v[56:59]\n\t"
        "ds_write_b64 v154, v[4:5] offset:36864\n\t"
        "v_mfma_f32_16x16x32_bf16 v[16:19], v[76:79], v[164:167], v[16:19]\n\t"
        "v_cvt_pk_bf16_f32 v8, v8, v9\n\t"
        "v_cvt_pk_bf16_f32 v9, v10, v11\n\t"
        "v_mfma_f32_16x16x32_bf16 v[48:51], v[220:223], v[104:107], v[48:51]\n\t"
        "ds_write_b64 v186, v[8:9] offset:36928\n\t"
        "s_waitcnt lgkmcnt(4)\n\t"
        "v_mfma_f32_16x16x32_bf16 v[20:23], v[80:83], v[164:167], v[20:23]\n\t"
        "v_cvt_pk_bf16_f32 v12, v12, v13\n\t"
        "v_cvt_pk_bf16_f32 v13, v14, v15\n\t"
        "v_mfma_f32_16x16x32_bf16 v[52:55], v[224:227], v[104:107], v[52:55]\n\t"
        "ds_write_b64 v154, v[12:13] offset:36928\n\t"
        "v_mfma_f32_16x16x32_bf16 v[56:59], v[228:231], v[104:107], v[56:59]\n\t"
        "v_cvt_pk_bf16_f32 v16, v16, v17\n\t"
        "v_cvt_pk_bf16_f32 v17, v18, v19\n\t"
        "v_mfma_f32_16x16x32_bf16 v[48:51], a[24:27], v[108:111], v[48:51]\n\t"
        "ds_write_b64 v186, v[16:17] offset:36992\n\t"
        "v_mfma_f32_16x16x32_bf16 v[52:55], a[28:31], v[108:111], v[52:55]\n\t"
        "v_cvt_pk_bf16_f32 v20, v20, v21\n\t"
        "v_cvt_pk_bf16_f32 v21, v22, v23\n\t"
        "v_mfma_f32_16x16x32_bf16 v[56:59], a[32:35], v[108:111], v[56:59]\n\t"
        "ds_write_b64 v154, v[20:21] offset:36992\n\t"
        "s_cmp_gt_u32 s61, 64\n\t"
        "s_cbranch_scc1 .Lx68%=\n\t"
        ".Lx69%=:\n\t"
        "v_mfma_f32_16x16x32_bf16 v[48:51], a[60:63], v[112:115], v[48:51]\n\t"
        "v_mfma_f32_16x16x32_bf16 v[52:55], a[64:67], v[112:115], v[52:55]\n\t"
        "v_mfma_f32_16x16x32_bf16 v[56:59], a[68:71], v[112:115], v[56:59]\n\t"
        "v_mfma_f32_16x16x32_bf16 v[48:51], a[96:99], v[84:87], v[48:51]\n\t"
        "v_mfma_f32_16x16x32_bf16 v[52:55], a[100:103], v[84:87], v[52:55]\n\t"
        "v_mfma_f32_16x16x32_bf16 v[56:59], a[104:107], v[84:87], v[56:59]\n\t"
        "s_waitcnt lgkmcnt(0)\n\t"
        "s_barrier\n\t"
        "v_xor_b32 v140, v181, v140\n\t"
        "v_mfma_f32_16x16x32_bf16 v[48:51], a[132:135], v[88:91], v[48:51]\n\t"
        "ds_read_b128 v[132:135], v187 offset:4096\n\t"
        "v_mfma_f32_16x16x32_bf16 v[52:55], a[136:139], v[88:91], v[52:55]\n\t"
        "ds_read_b128 v[136:139], v187 offset:4112\n\t"
        "v_add_u32 v151, s9, v180\n\t"
        "v_max_u32 v151, v151, v140\n\t"
        "s_waitcnt lgkmcnt(1)\n\t"
        "v_xor_b32 v132, v181, v132\n\t"
        "v_mfma_f32_16x16x32_bf16 v[56:59], a[140:143], v[88:91], v[56:59]\n\t"
        "ds_read_b128 v[100:103], v151 offset:0\n\t"
        "v_mfma_f32_16x16x32_bf16 v[48:51], a[168:171], v[92:95], v[48:51]\n\t"
        "ds_read_b128 v[104:107], v151 offset:64\n\t"
        "v_mfma_f32_16x16x32_bf16 v[52:55], a[172:175], v[92:95], v[52:55]\n\t"
        "ds_read_b128 v[108:111], v151 offset:128\n\t"
        "v_mfma_f32_16x16x32_bf16 v[56:59], a[176:179], v[92:95], v[56:59]\n\t"
        "ds_read_b32 v140, v187 offset:4128\n\t"
        "v_readlane_b32 s9, v155, 48\n\t"
        "v_mfma_f32_16x16x32_bf16 v[48:51], a[204:207], v[96:99], v[48:51]\n\t"
        "ds_read_b128 v[124:127], v182 offset:16\n\t"
        "v_add_u32 v183, s9, v172\n\t"
        "v_max_u32 v183, v183, v132\n\t"
        "v_xor_b32 v133, v181, v133\n\t"
        "v_mfma_f32_16x16x32_bf16 v[52:55], a[208:211], v[96:99], v[52:55]\n\t"
        "ds_read_b128 v[112:115], v183 offset:0\n\t"
        "v_add_u32 v151, s9, v173\n\t"
        "v_mfma_f32_16x16x32_bf16 v[56:59], a[212:215], v[96:99], v[56:59]\n\t"
        "ds_read_b128 v[84:87], v183 offset:64\n\t"
        "v_max_u32 v151, v151, v133\n\t"
        "s_waitcnt lgkmcnt(6)\n\t"
        "v_mfma_f32_16x16x32_bf16 v[48:51], a[240:243], v[100:103], v[48:51]\n\t"
        "ds_read_b128 v[88:91], v183 offset:128\n\t"
        "v_xor_b32 v134, v181, v134\n\t"
        "v_mfma_f32_16x16x32_bf16 v[52:55], a[244:247], v[100:103], v[52:55]\n\t"
        "ds_read_b128 v[92:95], v151 offset:0\n\t"
        "v_add_u32 v183, s9, v174\n\t"
        "v_mfma_f32_16x16x32_bf16 v[56:59], a[248:251], v[100:103], v[56:59]\n\t"
        "ds_read_b128 v[96:99], v151 offset:64\n\t"
        "v_max_u32 v183, v183, v134\n\t"
        "s_waitcnt lgkmcnt(8)\n\t"
        "v_mfma_f32_16x16x32_bf16 v[48:51], v[208:211], v[104:107], v[48:51]\n\t"
        "ds_read_b128 v[100:103], v151 offset:128\n\t"
        "v_xor_b32 v135, v181, v135\n\t"
        "v_mfma_f32_16x16x32_bf16 v[52:55], v[212:215], v[104:107], v[52:55]\n\t"
        "v_add_u32 v151, s9, v175\n\t"
        "v_max_u32 v151, v151, v135\n\t"
        "v_mfma_f32_16x16x32_bf16 v[56:59], v[216:219], v[104:107], v[56:59]\n\t"
        "v_xor_b32 v136, v181, v136\n\t"
        "s_waitcnt lgkmcnt(7)\n\t"
        "v_mfma_f32_16x16x32_bf16 v[48:51], v[244:247], v[108:111], v[48:51]\n\t"
        "ds_read_b128 v[104:107], v183 offset:0\n\t"
        "s_waitcnt lgkmcnt(7)\n\t"
        "v_or3_b32 v124, v124, v125, v126\n\t"
        "v_mfma_f32_16x16x32_bf16 v[52:55], v[248:251], v[108:111], v[52:55]\n\t"
        "v_or_b32 v124, v124, v127\n\t"
        "v_mfma_f32_16x16x32_bf16 v[56:59], v[252:255], v[108:111], v[56:59]\n\t"
        "v_readfirstlane_b32 s8, v124\n\t"
        "s_waitcnt lgkmcnt(6)\n\t"
        "v_mfma_f32_16x16x32_bf16 v[72:75], a[12:15], v[112:115], 0\n\t"
        "ds_read_b128 v[124:127], v147 offset:0\n\t"
        "s_or_b32 s6, s6, s8\n\t"
        "v_mfma_f32_16x16x32_bf16 v[76:79], a[16:19], v[112:115], 0\n\t"
        "ds_read_b128 v[108:111], v183 offset:64\n\t"
        "v_mfma_f32_16x16x32_bf16 v[80:83], a[20:23], v[112:115], 0\n\t"
        "s_waitcnt lgkmcnt(5)\n\t"
        "v_mfma_f32_16x16x32_bf16 v[72:75], a[48:51], v[84:87], v[72:75]\n\t"
        "ds_read_b128 v[112:115], v183 offset:128\n\t"
        "v_add_u32 v183, s9, v176\n\t"
        "v_mfma_f32_16x16x32_bf16 v[76:79], a[52:55], v[84:87], v[76:79]\n\t"
        "v_max_u32 v183, v183, v136\n\t"
        "v_xor_b32 v137, v181, v137\n\t"
        "v_mfma_f32_16x16x32_bf16 v[80:83], a[56:59], v[84:87], v[80:83]\n\t"
        "v_mfma_f32_16x16x32_bf16 v[72:75], a[84:87], v[88:91], v[72:75]\n\t"
        "ds_read_b128 v[84:87], v151 offset:0\n\t"
        "v_mfma_f32_16x16x32_bf16 v[76:79], a[88:91], v[88:91], v[76:79]\n\t"
        "v_mfma_f32_16x16x32_bf16 v[80:83], a[92:95], v[88:91], v[80:83]\n\t"
        "v_mfma_f32_16x16x32_bf16 v[72:75], a[120:123], v[92:95], v[72:75]\n\t"
        "s_waitcnt lgkmcnt(3)\n\t"
        "buffer_store_dwordx4 v[124:127], v145, s[28:31], s50 offen sc1\n\t"
        "v_mfma_f32_16x16x32_bf16 v[76:79], a[124:127], v[92:95], v[76:79]\n\t"
        "ds_read_b128 v[124:127], v147 offset:4096\n\t"
        "s_add_u32 s50, s50, 86016\n\t"
        "v_mfma_f32_16x16x32_bf16 v[80:83], a[128:131], v[92:95], v[80:83]\n\t"
        "ds_read_b128 v[88:91], v151 offset:64\n\t"
        "v_mfma_f32_16x16x32_bf16 v[72:75], a[156:159], v[96:99], v[72:75]\n\t"
        "ds_read_b128 v[92:95], v151 offset:128\n\t"
        "v_add_u32 v151, s9, v177\n\t"
        "v_mfma_f32_16x16x32_bf16 v[76:79], a[160:163], v[96:99], v[76:79]\n\t"
        "v_max_u32 v151, v151, v137\n\t"
        "v_xor_b32 v138, v181, v138\n\t"
        "v_mfma_f32_16x16x32_bf16 v[80:83], a[164:167], v[96:99], v[80:83]\n\t"
        "v_mfma_f32_16x16x32_bf16 v[72:75], a[192:195], v[100:103], v[72:75]\n\t"
        "ds_read_b128 v[96:99], v183 offset:0\n\t"
        "v_mfma_f32_16x16x32_bf16 v[76:79], a[196:199], v[100:103], v[76:79]\n\t"
        "v_mfma_f32_16x16x32_bf16 v[80:83], a[200:203], v[100:103], v[80:83]\n\t"
        "v_mfma_f32_16x16x32_bf16 v[72:75], a[228:231], v[104:107], v[72:75]\n\t"
        "ds_read_b128 v[100:103], v183 offset:64\n\t"
        "v_mfma_f32_16x16x32_bf16 v[76:79], a[232:235], v[104:107], v[76:79]\n\t"
        "v_mfma_f32_16x16x32_bf16 v[80:83], a[236:239], v[104:107], v[80:83]\n\t"
        "s_waitcnt lgkmcnt(4)\n\t"
        "v_mfma_f32_16x16x32_bf16 v[72:75], v[196:199], v[108:111], v[72:75]\n\t"
        "ds_read_b128 v[104:107], v183 offset:128\n\t"
        "v_add_u32 v183, s9, v178\n\t"
        "v_mfma_f32_16x16x32_bf16 v[76:79], v[200:203], v[108:111], v[76:79]\n\t"
        "v_max_u32 v183, v183, v138\n\t"
        "v_xor_b32 v139, v181, v139\n\t"
        "v_mfma_f32_16x16x32_bf16 v[80:83], v[204:207], v[108:111], v[80:83]\n\t"
        "buffer_store_dwordx4 v[124:127], v145, s[28:31], s50 offen sc1\n\t"
        "v_mfma_f32_16x16x32_bf16 v[72:75], v[232:235], v[112:115], v[72:75]\n\t"
        "ds_read_b128 v[108:111], v151 offset:0\n\t"
        "v_readlane_b32 s55, v155, 49\n\t"
        "v_mfma_f32_16x16x32_bf16 v[76:79], v[236:239], v[112:115], v[76:79]\n\t"
        "v_cvt_pk_bf16_f32 v116, v48, v49\n\t"
        "v_cvt_pk_bf16_f32 v117, v50, v51\n\t"
        "v_mfma_f32_16x16x32_bf16 v[80:83], v[240:243], v[112:115], v[80:83]\n\t"
        "v_cvt_pk_bf16_f32 v118, v52, v53\n\t"
        "v_cvt_pk_bf16_f32 v119, v54, v55\n\t"
        "v_mfma_f32_16x16x32_bf16 v[72:75], a[0:3], v[84:87], v[72:75]\n\t"
        "ds_read_b128 v[112:115], v151 offset:64\n\t"
        "v_cvt_pk_bf16_f32 v120, v56, v57\n\t"
        "v_mfma_f32_16x16x32_bf16 v[76:79], a[4:7], v[84:87], v[76:79]\n\t"
        "v_cvt_pk_bf16_f32 v121, v58, v59\n\t"
        "v_mfma_f32_16x16x32_bf16 v[80:83], a[8:11], v[84:87], v[80:83]\n\t"
        "v_permlane16_swap_b32 v116, v118\n\t"
        "s_waitcnt lgkmcnt(3)\n\t"
        "v_mfma_f32_16x16x32_bf16 v[72:75], a[36:39], v[88:91], v[72:75]\n\t"
        ".LpX2%=:\n\t"
        "s_cbranch_execz .LrX%=\n\t"
        "v_permlane16_swap_b32 v117, v119\n\t"
        "v_mfma_f32_16x16x32_bf16 v[76:79], a[40:43], v[88:91], v[76:79]\n\t"
        "ds_read_b128 v[84:87], v151 offset:128\n\t"
        "v_add_u32 v151, s9, v179\n\t"
        "v_mfma_f32_16x16x32_bf16 v[80:83], a[44:47], v[88:91], v[80:83]\n\t"
        "v_max_u32 v151, v151, v139\n\t"
        "v_xor_b32 v140, v181, v140\n\t"
        "v_mfma_f32_16x16x32_bf16 v[72:75], a[72:75], v[92:95], v[72:75]\n\t"
        "ds_read_b128 v[88:91], v183 offset:0\n\t"
        "v_mfma_f32_16x16x32_bf16 v[76:79], a[76:79], v[92:95], v[76:79]\n\t"
        "v_mfma_f32_16x16x32_bf16 v[80:83], a[80:83], v[92:95], v[80:83]\n\t"
        "v_mfma_f32_16x16x32_bf16 v[72:75], a[108:111], v[96:99], v[72:75]\n\t"
        "ds_read_b128 v[92:95], v183 offset:64\n\t"
        "v_mfma_f32_16x16x32_bf16 v[76:79], a[112:115], v[96:99], v[76:79]\n\t"
        "buffer_store_dwordx4 v[116:119], v148, s[28:31], s55 offen sc1\n\t"
        "v_mfma_f32_16x16x32_bf16 v[80:83], a[116:119], v[96:99], v[80:83]\n\t"
        "ds_write_b64 v146, v[120:121] offset:8192\n\t"
        "v_mfma_f32_16x16x32_bf16 v[72:75], a[144:147], v[100:103], v[72:75]\n\t"
        "ds_read_b128 v[96:99], v183 offset:128\n\t"
        "v_add_u32 v183, s9, v180\n\t"
        "v_mfma_f32_16x16x32_bf16 v[76:79], a[148:151], v[100:103], v[76:79]\n\t"
        "v_max_u32 v183, v183, v140\n\t"
        "v_mfma_f32_16x16x32_bf16 v[80:83], a[152:155], v[100:103], v[80:83]\n\t"
        "ds_read_b128 v[100:103], v151 offset:0\n\t"
        "s_waitcnt lgkmcnt(5)\n\t"
        "v_mfma_f32_16x16x32_bf16 v[72:75], a[180:183], v[104:107], v[72:75]\n\t"
        "v_mfma_f32_16x16x32_bf16 v[76:79], a[184:187], v[104:107], v[76:79]\n\t"
        "v_mfma_f32_16x16x32_bf16 v[80:83], a[188:191], v[104:107], v[80:83]\n\t"
        "ds_read_b128 v[104:107], v151 offset:64\n\t"
        "v_mfma_f32_16x16x32_bf16 v[72:75], a[216:219], v[108:111], v[72:75]\n\t"
        "v_mfma_f32_16x16x32_bf16 v[76:79], a[220:223], v[108:111], v[76:79]\n\t"
        "v_mfma_f32_16x16x32_bf16 v[80:83], a[224:227], v[108:111], v[80:83]\n\t"
        "ds_read_b128 v[108:111], v151 offset:128\n\t"
        "v_mfma_f32_16x16x32_bf16 v[72:75], a[252:255], v[112:115], v[72:75]\n\t"
        "v_mfma_f32_16x16x32_bf16 v[76:79], v[188:191], v[112:115], v[76:79]\n\t"
        "v_mfma_f32_16x16x32_bf16 v[80:83], v[192:195], v[112:115], v[80:83]\n\t"
        "ds_read_b128 v[112:115], v183 offset:0\n\t"
        "v_mfma_f32_16x16x32_bf16 v[72:75], v[220:223], v[84:87], v[72:75]\n\t"
        "v_mfma_f32_16x16x32_bf16 v[76:79], v[224:227], v[84:87], v[76:79]\n\t"
        "v_mfma_f32_16x16x32_bf16 v[80:83], v[228:231], v[84:87], v[80:83]\n\t"
        "ds_read_b128 v[84:87], v183 offset:64\n\t"
        "s_waitcnt lgkmcnt(4)\n\t"
        "v_mfma_f32_16x16x32_bf16 v[72:75], a[24:27], v[88:91], v[72:75]\n\t"
        "v_mfma_f32_16x16x32_bf16 v[76:79], a[28:31], v[88:91], v[76:79]\n\t"
        "v_mfma_f32_16x16x32_bf16 v[80:83], a[32:35], v[88:91], v[80:83]\n\t"
        "ds_read_b128 v[88:91], v183 offset:128\n\t"
        "v_mfma_f32_16x16x32_bf16 v[80:83], a[68:71], v[92:95], v[80:83]\n\t"
        "v_mfma_f32_16x16x32_bf16 v[80:83], a[104:107], v[96:99], v[80:83]\n\t"
        "v_mfma_f32_16x16x32_bf16 v[80:83], a[140:143], v[100:103], v[80:83]\n\t"
        "s_waitcnt lgkmcnt(3)\n\t"
        "v_mfma_f32_16x16x32_bf16 v[80:83], a[176:179], v[104:107], v[80:83]\n\t"
        "v_mfma_f32_16x16x32_bf16 v[80:83], a[212:215], v[108:111], v[80:83]\n\t"
        "s_waitcnt lgkmcnt(2)\n\t"
        "v_mfma_f32_16x16x32_bf16 v[80:83], a[248:251], v[112:115], v[80:83]\n\t"
        "s_waitcnt lgkmcnt(1)\n\t"
        "v_mfma_f32_16x16x32_bf16 v[80:83], v[216:219], v[84:87], v[80:83]\n\t"
        "s_waitcnt lgkmcnt(0)\n\t"
        "v_mfma_f32_16x16x32_bf16 v[80:83], v[252:255], v[88:91], v[80:83]\n\t"
        "s_nop 2\n\t"
        "v_mfma_f32_16x16x32_bf16 v[72:75], a[60:63], v[92:95], v[72:75]\n\t"
        "s_nop 2\n\t"
        "v_mfma_f32_16x16x32_bf16 v[72:75], a[96:99], v[96:99], v[72:75]\n\t"
        "v_cvt_pk_bf16_f32 v120, v80, v81\n\t"
        "v_cvt_pk_bf16_f32 v121, v82, v83\n\t"
        "v_mfma_f32_16x16x32_bf16 v[72:75], a[132:135], v[100:103], v[72:75]\n\t"
        "ds_write_b64 v146, v[120:121] offset:10240\n\t"
        "v_mfma_f32_16x16x32_bf16 v[72:75], a[168:171], v[104:107], v[72:75]\n\t"
        "v_mfma_f32_16x16x32_bf16 v[72:75], a[204:207], v[108:111], v[72:75]\n\t"
        "v_mfma_f32_16x16x32_bf16 v[72:75], a[240:243], v[112:115], v[72:75]\n\t"
        "s_waitcnt lgkmcnt(0)\n\t"
        "s_barrier\n\t"
        "v_mfma_f32_16x16x32_bf16 v[72:75], v[208:211], v[84:87], v[72:75]\n\t"
        "ds_read_b128 v[124:127], v147 offset:8192\n\t"
        "v_readlane_b32 s55, v155, 51\n\t"
        "v_mfma_f32_16x16x32_bf16 v[72:75], v[244:247], v[88:91], v[72:75]\n\t"
        "s_nop 1\n\t"
        "v_mfma_f32_16x16x32_bf16 v[76:79], a[64:67], v[92:95], v[76:79]\n\t"
        "s_nop 1\n\t"
        "v_mfma_f32_16x16x32_bf16 v[76:79], a[100:103], v[96:99], v[76:79]\n\t"
        "v_mfma_f32_16x16x32_bf16 v[76:79], a[136:139], v[100:103], v[76:79]\n\t"
        "v_mfma_f32_16x16x32_bf16 v[76:79], a[172:175], v[104:107], v[76:79]\n\t"
        "v_mfma_f32_16x16x32_bf16 v[76:79], a[208:211], v[108:111], v[76:79]\n\t"
        "v_cvt_pk_bf16_f32 v116, v72, v73\n\t"
        "v_cvt_pk_bf16_f32 v117, v74, v75\n\t"
        "v_mfma_f32_16x16x32_bf16 v[76:79], a[244:247], v[112:115], v[76:79]\n\t"
        "s_waitcnt lgkmcnt(0)\n\t"
        "buffer_store_dwordx4 v[124:127], v150, s[28:31], s52 offen sc1\n\t"
        "v_mfma_f32_16x16x32_bf16 v[76:79], v[212:215], v[84:87], v[76:79]\n\t"
        "v_mfma_f32_16x16x32_bf16 v[76:79], v[248:251], v[88:91], v[76:79]\n\t"
        "s_nop 7\n\t"
        "v_cvt_pk_bf16_f32 v118, v76, v77\n\t"
        "v_cvt_pk_bf16_f32 v119, v78, v79\n\t"
        "s_nop 0\n\t"
        "v_permlane16_swap_b32 v116, v118\n\t"
        "v_permlane16_swap_b32 v117, v119\n\t"
        "buffer_store_dwordx4 v[116:119], v148, s[28:31], s55 offen sc1\n\t"
        "s_andn2_b32 s6, s6, 2\n\t"
        "s_cbranch_scc1 99f\n\t"
        "s_endpgm\n\t"
        "22:\n\t"
        "ds_bpermute_b32 v64, v65, v155 offset:172\n\t"
        "s_add_u32 m0, s37, -24576\n\t"
        "s_add_u32 s36, s36, 6144\n\t"
        "buffer_load_dwordx4 v140, s[24:27], s36 offen offset:0 lds\n\t"
        "ds_bpermute_b32 v78, v81, v155 offset:148\n\t"
        "ds_bpermute_b32 v79, v81, v155 offset:160\n\t"
        "v_readlane_b32 s71, v155, 26\n\t"
        "v_readlane_b32 s10, v155, 29\n\t"
        "s_nop 0\n\t"
        "v_mul_u32_u24 v73, s71, v66\n\t"
        "v_lshrrev_b32 v73, 11, v73\n\t"
        "v_lshlrev_b32 v66, 4, v66\n\t"
        "v_mad_u32_u24 v73, v73, s10, v66\n\t"
        "s_waitcnt lgkmcnt(2)\n\t"
        "v_add_u32 v64, v64, v73\n\t"
        "buffer_load_dwordx4 v141, s[24:27], s36 offen offset:1024 lds\n\t"
        "v_readlane_b32 s69, v155, 34\n\t"
        "v_readlane_b32 s70, v155, 35\n\t"
        "s_nop 0\n\t"
        "v_mad_u32_u24 v70, v52, s69, v70\n\t"
        "v_mad_u32_u24 v70, v158, s70, v70\n\t"
        "s_mul_i32 s71, s69, s68\n\t"
        "s_add_u32 s71, s71, 157728\n\t"
        "v_mad_u32_u24 v67, v82, s70, v67\n\t"
        "v_add_u32 v67, s71, v67\n\t"
        "v_readlane_b32 s11, v155, 30\n\t"
        "buffer_load_dwordx4 v142, s[24:27], s36 offen offset:2048 lds\n\t"
        "s_movk_i32 s10, 223\n\t"
        "v_mov_b32 v75, 0x7ff00000\n\t"
        "v_readlane_b32 s12, v155, 24\n\t"
        "v_readlane_b32 s13, v155, 25\n\t"
        "s_nop 0\n\t"
        "s_bfe_u32 s70, s12, 524288\n\t"
        "s_bfe_u32 s71, s13, 524288\n\t"
        "v_cmp_gt_u32 vcc, s71, v71\n\t"
        "v_add3_u32 v73, v71, s70, -2\n\t"
        "v_med3_i32 v73, v73, 0, s10\n\t"
        "s_add_u32 m0, s35, 4096\n\t"
        "s_add_u32 s43, s34, 43008\n\t"
        "buffer_load_dwordx4 v143, s[16:19], s43 offen  lds\n\t"
        "v_cndmask_b32 v74, v75, v72, vcc\n\t"
        "v_mad_u32_u24 v134, v73, s11, v74\n\t"
        "s_bfe_u32 s70, s12, 524296\n\t"
        "s_bfe_u32 s71, s13, 524296\n\t"
        "v_cmp_gt_u32 vcc, s71, v71\n\t"
        "v_add3_u32 v73, v71, s70, -2\n\t"
        "v_med3_i32 v73, v73, 0, s10\n\t"
        "v_cndmask_b32 v74, v75, v72, vcc\n\t"
        "v_mad_u32_u24 v135, v73, s11, v74\n\t"
        "s_add_u32 m0, s35, 20480\n\t"
        "s_add_u32 s43, s34, 215040\n\t"
        "buffer_load_dwordx4 v143, s[16:19], s43 offen  lds\n\t"
        "s_bfe_u32 s70, s12, 524304\n\t"
        "s_bfe_u32 s71, s13, 524304\n\t"
        "v_cmp_gt_u32 vcc, s71, v71\n\t"
        "v_add3_u32 v73, v71, s70, -2\n\t"
        "v_med3_i32 v73, v73, 0, s10\n\t"
        "v_cndmask_b32 v74, v75, v72, vcc\n\t"
        "v_mad_u32_u24 v136, v73, s11, v74\n\t"
        "s_bfe_u32 s70, s12, 524312\n\t"
        "s_bfe_u32 s71, s13, 524312\n\t"
        "v_cmp_gt_u32 vcc, s71, v71\n\t"
        "v_add3_u32 v73, v71, s70, -2\n\t"
        "v_med3_i32 v73, v73, 0, s10\n\t"
        "v_cndmask_b32 v74, v75, v72, vcc\n\t"
        "v_mad_u32_u24 v137, v73, s11, v74\n\t"
        "s_mov_b64 exec, 0\n\t"
        "s_cmp_eq_u32 s4, 0\n\t"
        "s_cbranch_scc1 .LpY0%=\n\t"
        "s_cmp_eq_u32 s4, 1\n\t"
        "s_cbranch_scc1 .LpY1%=\n\t"
        "s_cmp_eq_u32 s4, 2\n\t"
        "s_cbranch_scc1 .LpY2%=\n\t"
        "s_nop 0\n\t"
        ".LrY%=:\n\t"
        "s_mov_b64 exec, -1\n\t"
        "s_waitcnt vmcnt(9)\n\t"
        "s_barrier\n\t"
        "ds_read_b64_tr_b16 a[0:1], v138 offset:6144\n\t"
        "ds_read_b64_tr_b16 a[2:3], v138 offset:6912\n\t"
        "ds_read_b64_tr_b16 a[4:5], v139 offset:6144\n\t"
        "ds_read_b64_tr_b16 a[6:7], v139 offset:6912\n\t"
        "ds_read_b64_tr_b16 a[8:9], v149 offset:6144\n\t"
        "ds_read_b64_tr_b16 a[10:11], v149 offset:6912\n\t"
        "ds_read_b128 v[84:87], v128 offset:8192\n\t"
        "ds_read_b64_tr_b16 a[12:13], v138 offset:12288\n\t"
        "ds_read_b64_tr_b16 a[14:15], v138 offset:13056\n\t"
        "ds_read_b64_tr_b16 a[16:17], v139 offset:12288\n\t"
        "ds_read_b64_tr_b16 a[18:19], v139 offset:13056\n\t"
        "ds_read_b64_tr_b16 a[20:21], v149 offset:12288\n\t"
        "ds_read_b64_tr_b16 a[22:23], v149 offset:13056\n\t"
        "ds_read_b128 v[88:91], v128 offset:8192\n\t"
        "s_add_u32 m0, s37, -12288\n\t"
        "s_add_u32 s36, s36, s39\n\t"
        "s_waitcnt lgkmcnt(7)\n\t"
        "v_mfma_f32_16x16x32_bf16 v[12:15], a[0:3], v[84:87], 0\n\t"
        "buffer_load_dwordx4 v140, s[24:27], s36 offen offset:0 lds\n\t"
        "buffer_load_dwordx4 v141, s[24:27], s36 offen offset:1024 lds\n\t"
        "buffer_load_dwordx4 v142, s[24:27], s36 offen offset:2048 lds\n\t"
        "s_waitcnt vmcnt(8)\n\t"
        "s_barrier\n\t"
        "ds_read_b128 v[92:95], v128 offset:12288\n\t"
        "ds_read_b64_tr_b16 a[24:25], v138 offset:0\n\t"
        "v_mfma_f32_16x16x32_bf16 v[16:19], a[4:7], v[84:87], 0\n\t"
        "ds_read_b64_tr_b16 a[26:27], v138 offset:768\n\t"
        "s_add_u32 m0, s35, 157696\n\t"
        "s_nop 0\n\t"
        "buffer_load_dwordx4 v64, s[20:23], 0 offen lds\n\t"
        "s_add_u32 m0, s35, 16384\n\t"
        "s_add_u32 s43, s34, 172032\n\t"
        "v_mfma_f32_16x16x32_bf16 v[20:23], a[8:11], v[84:87], 0\n\t"
        "buffer_load_dwordx4 v143, s[16:19], s43 offen  lds\n\t"
        "s_add_u32 m0, s35, 32768\n\t"
        "ds_read_b64_tr_b16 a[28:29], v139 offset:0\n\t"
        "s_add_u32 s43, s34, 344064\n\t"
        "s_min_u32 s43, s43, 9590784\n\t"
        "s_waitcnt lgkmcnt(4)\n\t"
        "v_mfma_f32_16x16x32_bf16 v[24:27], a[12:15], v[88:91], 0\n\t"
        "buffer_load_dwordx4 v143, s[16:19], s43 offen  lds\n\t"
        "s_add_u32 m0, s37, 0\n\t"
        "ds_read_b64_tr_b16 a[30:31], v139 offset:768\n\t"
        "s_add_u32 s36, s36, s38\n\t"
        "ds_read_b64_tr_b16 a[32:33], v149 offset:0\n\t"
        "v_mfma_f32_16x16x32_bf16 v[28:31], a[16:19], v[88:91], 0\n\t"
        "buffer_load_dwordx4 v140, s[24:27], s36 offen offset:0 lds\n\t"
        "ds_read_b64_tr_b16 a[34:35], v149 offset:768\n\t"
        "ds_read_b128 v[96:99], v128 offset:8192\n\t"
        "v_readlane_b32 s44, v155, 32\n\t"
        "v_mfma_f32_16x16x32_bf16 v[32:35], a[20:23], v[88:91], 0\n\t"
        "buffer_load_dwordx4 v141, s[24:27], s36 offen offset:1024 lds\n\t"
        "v_readlane_b32 s45, v155, 33\n\t"
        "s_waitcnt lgkmcnt(7)\n\t"
        "v_mfma_f32_16x16x32_bf16 v[24:27], a[0:3], v[92:95], v[24:27]\n\t"
        "ds_read_b128 v[100:103], v128 offset:12288\n\t"
        "v_readlane_b32 s62, v155, 36\n\t"
        "v_mfma_f32_16x16x32_bf16 v[28:31], a[4:7], v[92:95], v[28:31]\n\t"
        "s_nop 0\n\t"
        "s_lshl_b32 s70, s62, 5\n\t"
        "s_sub_u32 s71, s70, 16384\n\t"
        "v_mfma_f32_16x16x32_bf16 v[32:35], a[8:11], v[92:95], v[32:35]\n\t"
        "buffer_load_dwordx4 v142, s[24:27], s36 offen offset:2048 lds\n\t"
        "s_add_u32 m0, s35, 0\n\t"
        "s_add_u32 s43, s34, 0\n\t"
        "v_mfma_f32_16x16x32_bf16 v[36:39], a[12:15], v[92:95], 0\n\t"
        "s_mov_b32 exec_lo, 0\n\t"
        "s_mov_b32 exec_hi, 0xfff00000\n\t"
        "ds_write_b32 v119, v155\n\t"
        "s_mov_b64 exec, -1\n\t"
        "v_mfma_f32_16x16x32_bf16 v[40:43], a[16:19], v[92:95], 0\n\t"
        "s_mul_i32 s71, s71, s68\n\t"
        "v_add_u32 v172, s71, v128\n\t"
        "v_mfma_f32_16x16x32_bf16 v[44:47], a[20:23], v[92:95], 0\n\t"
        "buffer_load_dwordx4 v143, s[16:19], s43 offen  lds\n\t"
        "s_add_u32 m0, s35, 36864\n\t"
        "s_add_u32 s43, s34, 387072\n\t"
        "s_min_u32 s43, s43, 9590784\n\t"
        "buffer_load_dwordx4 v143, s[16:19], s43 offen  lds\n\t"
        "s_waitcnt vmcnt(8)\n\t"
        "s_barrier\n\t"
        "ds_read_b128 v[104:107], v128 offset:4096\n\t"
        "s_add_u32 m0, s37, 12288\n\t"
        "ds_read_b64_tr_b16 a[36:37], v138 offset:18432\n\t"
        "s_add_u32 s36, s36, s5\n\t"
        "s_waitcnt lgkmcnt(4)\n\t"
        "v_mfma_f32_16x16x32_bf16 v[0:3], a[24:27], v[96:99], 0\n\t"
        "buffer_load_dwordx4 v140, s[24:27], s36 offen offset:0 lds\n\t"
        "ds_read_b64_tr_b16 a[38:39], v138 offset:19200\n\t"
        "v_add_u32 v173, s71, v129\n\t"
        "v_mfma_f32_16x16x32_bf16 v[4:7], a[28:31], v[96:99], 0\n\t"
        "buffer_load_dwordx4 v141, s[24:27], s36 offen offset:1024 lds\n\t"
        "ds_read_b64_tr_b16 a[40:41], v139 offset:18432\n\t"
        "v_add_u32 v174, s71, v130\n\t"
        "v_mfma_f32_16x16x32_bf16 v[8:11], a[32:35], v[96:99], 0\n\t"
        "ds_read_b64_tr_b16 a[42:43], v139 offset:19200\n\t"
        "ds_read_b64_tr_b16 a[44:45], v149 offset:18432\n\t"
        "s_add_u32 s71, s71, s70\n\t"
        "v_add_u32 v175, s71, v128\n\t"
        "s_waitcnt lgkmcnt(7)\n\t"
        "v_mfma_f32_16x16x32_bf16 v[12:15], a[24:27], v[100:103], v[12:15]\n\t"
        "buffer_load_dwordx4 v142, s[24:27], s36 offen offset:2048 lds\n\t"
        "s_add_u32 m0, s37, 24576\n\t"
        "ds_read_b64_tr_b16 a[46:47], v149 offset:19200\n\t"
        "s_add_u32 s36, s36, s39\n\t"
        "ds_read_b64_tr_b16 a[48:49], v138 offset:24576\n\t"
        "v_mfma_f32_16x16x32_bf16 v[16:19], a[28:31], v[100:103], v[16:19]\n\t"
        "ds_read_b64_tr_b16 a[50:51], v138 offset:25344\n\t"
        "ds_read_b64_tr_b16 a[52:53], v139 offset:24576\n\t"
        "ds_read_b64_tr_b16 a[54:55], v139 offset:25344\n\t"
        "v_add_u32 v176, s71, v129\n\t"
        "v_add_u32 v177, s71, v130\n\t"
        "v_mfma_f32_16x16x32_bf16 v[20:23], a[32:35], v[100:103], v[20:23]\n\t"
        "ds_read_b64_tr_b16 a[56:57], v149 offset:24576\n\t"
        "ds_read_b64_tr_b16 a[58:59], v149 offset:25344\n\t"
        "ds_read_b128 v[108:111], v128 offset:4160\n\t"
        "s_add_u32 s71, s71, s70\n\t"
        "s_waitcnt lgkmcnt(11)\n\t"
        "v_mfma_f32_16x16x32_bf16 v[0:3], a[0:3], v[104:107], v[0:3]\n\t"
        "ds_read_b64_tr_b16 a[60:61], v138 offset:30720\n\t"
        "ds_read_b64_tr_b16 a[62:63], v138 offset:31488\n\t"
        "v_add_u32 v178, s71, v128\n\t"
        "v_add_u32 v179, s71, v129\n\t"
        "v_mfma_f32_16x16x32_bf16 v[4:7], a[4:7], v[104:107], v[4:7]\n\t"
        "ds_read_b64_tr_b16 a[64:65], v139 offset:30720\n\t"
        "ds_read_b64_tr_b16 a[66:67], v139 offset:31488\n\t"
        "v_add_u32 v180, s71, v130\n\t"
        "v_mfma_f32_16x16x32_bf16 v[8:11], a[8:11], v[104:107], v[8:11]\n\t"
        "ds_read_b64_tr_b16 a[68:69], v149 offset:30720\n\t"
        "ds_read_b64_tr_b16 a[70:71], v149 offset:31488\n\t"
        "v_add_f32 v78, v78, v56\n\t"
        "v_add_f32 v79, v79, v57\n\t"
        "v_mfma_f32_16x16x32_bf16 v[12:15], a[12:15], v[104:107], v[12:15]\n\t"
        "ds_read_b128 v[112:115], v128 offset:8256\n\t"
        "v_mfma_f32_16x16x32_bf16 v[16:19], a[16:19], v[104:107], v[16:19]\n\t"
        "s_cmp_eq_u32 s69, 36\n\t"
        "s_cselect_b64 exec, -1, 0\n\t"
        "v_swap_b32 v68, v69\n\t"
        "v_swap_b32 v173, v175\n\t"
        "v_swap_b32 v174, v178\n\t"
        "v_swap_b32 v177, v179\n\t"
        "s_mov_b64 exec, -1\n\t"
        "v_mfma_f32_16x16x32_bf16 v[20:23], a[20:23], v[104:107], v[20:23]\n\t"
        "ds_read_b128 v[84:87], v128 offset:12352\n\t"
        "v_add3_u32 v68, v68, v52, -1\n\t"
        "v_add3_u32 v69, v69, v158, -1\n\t"
        "s_waitcnt lgkmcnt(8)\n\t"
        "v_mfma_f32_16x16x32_bf16 v[0:3], a[36:39], v[108:111], v[0:3]\n\t"
        "ds_read_b64_tr_b16 a[72:73], v138 offset:36864\n\t"
        "ds_read_b64_tr_b16 a[74:75], v138 offset:37632\n\t"
        "v_cvt_f32_i32 v68, v68\n\t"
        "v_cvt_f32_i32 v69, v69\n\t"
        "v_mfma_f32_16x16x32_bf16 v[4:7], a[40:43], v[108:111], v[4:7]\n\t"
        "ds_read_b64_tr_b16 a[76:77], v139 offset:36864\n\t"
        "ds_read_b64_tr_b16 a[78:79], v139 offset:37632\n\t"
        "v_readlane_b32 s69, v155, 31\n\t"
        "v_mfma_f32_16x16x32_bf16 v[8:11], a[44:47], v[108:111], v[8:11]\n\t"
        "ds_read_b64_tr_b16 a[80:81], v149 offset:36864\n\t"
        "ds_read_b64_tr_b16 a[82:83], v149 offset:37632\n\t"
        "v_mov_b32 v154, s69\n\t"
        "v_mov_b32 v119, s62\n\t"
        "v_mfma_f32_16x16x32_bf16 v[12:15], a[48:51], v[108:111], v[12:15]\n\t"
        "v_readlane_b32 s53, v155, 28\n\t"
        "v_readlane_b32 s9, v155, 46\n\t"
        "v_mfma_f32_16x16x32_bf16 v[16:19], a[52:55], v[108:111], v[16:19]\n\t"
        "buffer_load_dwordx4 v140, s[24:27], s36 offen offset:0 lds\n\t"
        "s_sub_u32 s10, 43200, s11\n\t"
        "s_mul_i32 s10, s10, s68\n\t"
        "v_mfma_f32_16x16x32_bf16 v[20:23], a[56:59], v[108:111], v[20:23]\n\t"
        "s_sub_u32 s11, s11, 192\n\t"
        "v_mad_u32_u24 v148, v158, s11, v144\n\t"
        "s_waitcnt lgkmcnt(7)\n\t"
        "v_mfma_f32_16x16x32_bf16 v[0:3], a[60:63], v[112:115], v[0:3]\n\t"
        "v_add_u32 v148, s10, v148\n\t"
        "v_mad_u32_u24 v150, v83, s11, v145\n\t"
        "s_lshl_b32 s70, s49, 1\n\t"
        "v_mfma_f32_16x16x32_bf16 v[4:7], a[64:67], v[112:115], v[4:7]\n\t"
        "s_add_u32 s70, s70, 49\n\t"
        "v_readlane_b32 s52, v155, s70\n\t"
        "v_mfma_f32_16x16x32_bf16 v[8:11], a[68:71], v[112:115], v[8:11]\n\t"
        "buffer_load_dwordx4 v141, s[24:27], s36 offen offset:1024 lds\n\t"
        "s_add_u32 s52, s52, s10\n\t"
        "v_mfma_f32_16x16x32_bf16 v[12:15], a[36:39], v[112:115], v[12:15]\n\t"
        "v_mfma_f32_16x16x32_bf16 v[16:19], a[40:43], v[112:115], v[16:19]\n\t"
        "v_mfma_f32_16x16x32_bf16 v[20:23], a[44:47], v[112:115], v[20:23]\n\t"
        "v_mfma_f32_16x16x32_bf16 v[24:27], a[48:51], v[112:115], v[24:27]\n\t"
        "buffer_load_dwordx4 v142, s[24:27], s36 offen offset:2048 lds\n\t"
        "v_mfma_f32_16x16x32_bf16 v[28:31], a[52:55], v[112:115], v[28:31]\n\t"
        "v_mfma_f32_16x16x32_bf16 v[32:35], a[56:59], v[112:115], v[32:35]\n\t"
        "s_waitcnt lgkmcnt(0)\n\t"
        "v_mfma_f32_16x16x32_bf16 v[12:15], a[60:63], v[84:87], v[12:15]\n\t"
        "v_mfma_f32_16x16x32_bf16 v[16:19], a[64:67], v[84:87], v[16:19]\n\t"
        "buffer_load_dwordx4 v[48:51], v55, s[20:23], s48 offen\n\t"
        "v_mfma_f32_16x16x32_bf16 v[20:23], a[68:71], v[84:87], v[20:23]\n\t"
        "v_mfma_f32_16x16x32_bf16 v[24:27], a[36:39], v[84:87], v[24:27]\n\t"
        "ds_read_b128 v[88:91], v128 offset:4224\n\t"
        "v_mfma_f32_16x16x32_bf16 v[28:31], a[40:43], v[84:87], v[28:31]\n\t"
        "v_mfma_f32_16x16x32_bf16 v[32:35], a[44:47], v[84:87], v[32:35]\n\t"
        "buffer_load_dword v61, v61, s[20:23], s48 offen offset:512\n\t"
        "v_readlane_b32 s40, v155, 12\n\t"
        "v_readlane_b32 s43, v155, 0\n\t"
        "v_mfma_f32_16x16x32_bf16 v[36:39], a[48:51], v[84:87], v[36:39]\n\t"
        "ds_read_b128 v[92:95], v128 offset:8320\n\t"
        "s_add_u32 m0, s40, s35\n\t"
        "v_mfma_f32_16x16x32_bf16 v[40:43], a[52:55], v[84:87], v[40:43]\n\t"
        "v_mfma_f32_16x16x32_bf16 v[44:47], a[56:59], v[84:87], v[44:47]\n\t"
        "s_waitcnt lgkmcnt(1)\n\t"
        "v_mfma_f32_16x16x32_bf16 v[0:3], a[72:75], v[88:91], v[0:3]\n\t"
        "s_waitcnt vmcnt(10)\n\t"
        "s_barrier\n\t"
        "buffer_load_dwordx4 v134, s[16:19], s43 offen  lds\n\t"
        "ds_read_b128 v[96:99], v128 offset:16384\n\t"
        "v_mfma_f32_16x16x32_bf16 v[4:7], a[76:79], v[88:91], v[4:7]\n\t"
        "ds_read_b128 v[100:103], v128 offset:16448\n\t"
        "ds_read_b64_tr_b16 a[84:85], v131 offset:0\n\t"
        "v_readlane_b32 s40, v155, 13\n\t"
        "v_readlane_b32 s43, v155, 1\n\t"
        "v_mfma_f32_16x16x32_bf16 v[8:11], a[80:83], v[88:91], v[8:11]\n\t"
        "ds_read_b64_tr_b16 a[86:87], v131 offset:768\n\t"
        "s_add_u32 m0, s40, s35\n\t"
        "ds_read_b64_tr_b16 a[88:89], v132 offset:0\n\t"
        "s_waitcnt lgkmcnt(5)\n\t"
        "v_mfma_f32_16x16x32_bf16 v[12:15], a[72:75], v[92:95], v[12:15]\n\t"
        "ds_read_b64_tr_b16 a[90:91], v132 offset:768\n\t"
        "ds_read_b64_tr_b16 a[92:93], v133 offset:0\n\t"
        "v_readlane_b32 s46, v155, 37\n\t"
        "v_readlane_b32 s47, v155, 40\n\t"
        "v_mfma_f32_16x16x32_bf16 v[16:19], a[76:79], v[92:95], v[16:19]\n\t"
        "buffer_load_dwordx4 v135, s[16:19], s43 offen  lds\n\t"
        "ds_read_b64_tr_b16 a[94:95], v133 offset:768\n\t"
        "v_readlane_b32 s40, v155, 14\n\t"
        "v_readlane_b32 s43, v155, 2\n\t"
        "v_mfma_f32_16x16x32_bf16 v[20:23], a[80:83], v[92:95], v[20:23]\n\t"
        "ds_read_b128 v[104:107], v128 offset:4224\n\t"
        "s_add_u32 m0, s40, s35\n\t"
        "ds_read_b64_tr_b16 a[96:97], v131 offset:6144\n\t"
        "s_waitcnt lgkmcnt(9)\n\t"
        "v_mfma_f32_16x16x32_bf16 v[24:27], a[24:27], v[96:99], v[24:27]\n\t"
        "ds_read_b64_tr_b16 a[98:99], v131 offset:6912\n\t"
        "ds_read_b64_tr_b16 a[100:101], v132 offset:6144\n\t"
        "v_add_f32 v58, s46, v68\n\t"
        "v_add_f32 v59, s47, v69\n\t"
        "v_mfma_f32_16x16x32_bf16 v[28:31], a[28:31], v[96:99], v[28:31]\n\t"
        "ds_read_b64_tr_b16 a[102:103], v132 offset:6912\n\t"
        "ds_read_b64_tr_b16 a[104:105], v133 offset:6144\n\t"
        "v_readlane_b32 s47, v155, 42\n\t"
        "v_mfma_f32_16x16x32_bf16 v[32:35], a[32:35], v[96:99], v[32:35]\n\t"
        "buffer_load_dwordx4 v134, s[16:19], s43 offen  lds\n\t"
        "ds_read_b64_tr_b16 a[106:107], v133 offset:6912\n\t"
        "v_readlane_b32 s40, v155, 15\n\t"
        "v_readlane_b32 s43, v155, 3\n\t"
        "v_mfma_f32_16x16x32_bf16 v[36:39], a[0:3], v[96:99], v[36:39]\n\t"
        "ds_read_b128 v[108:111], v128 offset:8320\n\t"
        "s_add_u32 m0, s40, s35\n\t"
        "v_mfma_f32_16x16x32_bf16 v[40:43], a[4:7], v[96:99], v[40:43]\n\t"
        "ds_read_b32 v64, v70 offset:0\n\t"
        "v_mfma_f32_16x16x32_bf16 v[44:47], a[8:11], v[96:99], v[44:47]\n\t"
        "ds_read_b128 v[112:115], v128 offset:12416\n\t"
        "s_waitcnt lgkmcnt(10)\n\t"
        "v_mfma_f32_16x16x32_bf16 v[24:27], a[60:63], v[100:103], v[24:27]\n\t"
        "ds_read_b32 v67, v67 offset:0\n\t"
        "v_mfma_f32_16x16x32_bf16 v[28:31], a[64:67], v[100:103], v[28:31]\n\t"
        "ds_read_b32 v66, v70 offset:2560\n\t"
        "v_mfma_f32_16x16x32_bf16 v[32:35], a[68:71], v[100:103], v[32:35]\n\t"
        "buffer_load_dwordx4 v135, s[16:19], s43 offen  lds\n\t"
        "s_add_u32 m0, s37, 36864\n\t"
        "s_add_u32 s36, s36, s38\n\t"
        "v_mfma_f32_16x16x32_bf16 v[36:39], a[36:39], v[100:103], v[36:39]\n\t"
        "ds_read_b128 v[84:87], v128 offset:16512\n\t"
        "v_mfma_f32_16x16x32_bf16 v[40:43], a[40:43], v[100:103], v[40:43]\n\t"
        "v_mfma_f32_16x16x32_bf16 v[44:47], a[44:47], v[100:103], v[44:47]\n\t"
        "s_waitcnt lgkmcnt(7)\n\t"
        "v_mfma_f32_16x16x32_bf16 v[12:15], a[84:87], v[104:107], v[12:15]\n\t"
        "buffer_load_dwordx4 v140, s[24:27], s36 offen offset:0 lds\n\t"
        "v_mfma_f32_16x16x32_bf16 v[16:19], a[88:91], v[104:107], v[16:19]\n\t"
        "s_waitcnt lgkmcnt(4)\n\t"
        "v_perm_b32 v56, v64, v64, s44\n\t"
        "v_perm_b32 v57, v64, v64, s45\n\t"
        "v_add_f32 v56, v56, v58\n\t"
        "v_mfma_f32_16x16x32_bf16 v[20:23], a[92:95], v[104:107], v[20:23]\n\t"
        "v_add_f32 v57, v57, v59\n\t"
        "v_cvt_pk_bf16_f32 v52, v56, v57\n\t"
        "v_cvt_pk_bf16_f32 v58, v58, v59\n\t"
        "v_mfma_f32_16x16x32_bf16 v[0:3], a[96:99], v[108:111], v[0:3]\n\t"
        "v_cmp_ne_u32 s[64:65], v52, v58\n\t"
        "s_waitcnt lgkmcnt(2)\n\t"
        "v_perm_b32 v57, v67, v67, s45\n\t"
        "v_add_f32 v57, v57, v79\n\t"
        "v_mfma_f32_16x16x32_bf16 v[4:7], a[100:103], v[108:111], v[4:7]\n\t"
        "buffer_load_dwordx4 v141, s[24:27], s36 offen offset:1024 lds\n\t"
        "v_mbcnt_lo_u32_b32 v56, s64, 1\n\t"
        "v_mfma_f32_16x16x32_bf16 v[8:11], a[104:107], v[108:111], v[8:11]\n\t"
        "v_mbcnt_hi_u32_b32 v56, s65, v56\n\t"
        "s_bcnt1_i32_b64 s61, s[64:65]\n\t"
        "v_mfma_f32_16x16x32_bf16 v[24:27], a[84:87], v[108:111], v[24:27]\n\t"
        "v_add_u32 v54, s61, 1\n\t"
        "v_lshl_add_u32 v56, v56, 3, s60\n\t"
        "v_cvt_pk_bf16_f32 v58, v78, v79\n\t"
        "v_mfma_f32_16x16x32_bf16 v[28:31], a[88:91], v[108:111], v[28:31]\n\t"
        "s_mov_b64 exec, s[64:65]\n\t"
        "ds_write_b64 v56, v[52:53]\n\t"
        "s_mov_b64 exec, -1\n\t"
        "v_mfma_f32_16x16x32_bf16 v[32:35], a[92:95], v[108:111], v[32:35]\n\t"
        "s_waitcnt vmcnt(8) lgkmcnt(1)\n\t"
        "s_barrier\n\t"
        "buffer_load_dwordx4 v142, s[24:27], s36 offen offset:2048 lds\n\t"
        "s_add_u32 m0, s37, 49152\n\t"
        "ds_read_b128 v[88:91], v128 offset:0\n\t"
        "v_mfma_f32_16x16x32_bf16 v[12:15], a[96:99], v[112:115], v[12:15]\n\t"
        "ds_read_b128 v[92:95], v128 offset:64\n\t"
        "s_add_u32 s36, s36, s5\n\t"
        "ds_read_b128 v[96:99], v128 offset:128\n\t"
        "v_mfma_f32_16x16x32_bf16 v[16:19], a[100:103], v[112:115], v[16:19]\n\t"
        "ds_read_b64_tr_b16 a[108:109], v131 offset:12288\n\t"
        "ds_read_b64_tr_b16 a[110:111], v131 offset:13056\n\t"
        "v_swap_b32 v53, v80\n\t"
        "v_perm_b32 v56, v67, v67, s44\n\t"
        "v_mfma_f32_16x16x32_bf16 v[20:23], a[104:107], v[112:115], v[20:23]\n\t"
        "ds_read_b64_tr_b16 a[112:113], v132 offset:12288\n\t"
        "ds_read_b64_tr_b16 a[114:115], v132 offset:13056\n\t"
        "v_add_f32 v56, v56, v78\n\t"
        "v_cvt_pk_bf16_f32 v52, v56, v57\n\t"
        "v_mfma_f32_16x16x32_bf16 v[24:27], a[72:75], v[112:115], v[24:27]\n\t"
        "buffer_load_dwordx4 v140, s[24:27], s36 offen offset:0 lds\n\t"
        "ds_read_b128 v[100:103], v128 offset:20480\n\t"
        "v_cmp_ne_u32 s[64:65], v52, v58\n\t"
        "v_mfma_f32_16x16x32_bf16 v[28:31], a[76:79], v[112:115], v[28:31]\n\t"
        "ds_read_b64_tr_b16 a[116:117], v133 offset:12288\n\t"
        "ds_read_b64_tr_b16 a[118:119], v133 offset:13056\n\t"
        "s_and_b64 s[64:65], s[64:65], 0xffffff\n\t"
        "v_mbcnt_lo_u32_b32 v56, s64, v54\n\t"
        "v_mfma_f32_16x16x32_bf16 v[32:35], a[80:83], v[112:115], v[32:35]\n\t"
        "ds_read_b64_tr_b16 a[120:121], v131 offset:18432\n\t"
        "ds_read_b64_tr_b16 a[122:123], v131 offset:19200\n\t"
        "s_bcnt1_i32_b64 s46, s[64:65]\n\t"
        "v_mfma_f32_16x16x32_bf16 v[36:39], a[84:87], v[112:115], v[36:39]\n\t"
        "ds_read_b128 v[104:107], v128 offset:20544\n\t"
        "ds_read_b64_tr_b16 a[124:125], v132 offset:18432\n\t"
        "s_add_u32 s61, s61, s46\n\t"
        "v_add_u32 v54, s46, v54\n\t"
        "v_mfma_f32_16x16x32_bf16 v[40:43], a[88:91], v[112:115], v[40:43]\n\t"
        "buffer_load_dwordx4 v141, s[24:27], s36 offen offset:1024 lds\n\t"
        "ds_read_b64_tr_b16 a[126:127], v132 offset:19200\n\t"
        "s_mov_b32 s3, s61\n\t"
        "v_mfma_f32_16x16x32_bf16 v[44:47], a[92:95], v[112:115], v[44:47]\n\t"
        "ds_read_b64_tr_b16 a[128:129], v133 offset:18432\n\t"
        "ds_read_b64_tr_b16 a[130:131], v133 offset:19200\n\t"
        "v_lshl_add_u32 v56, v56, 3, s60\n\t"
        "v_readlane_b32 s46, v155, 39\n\t"
        "v_mfma_f32_16x16x32_bf16 v[24:27], a[96:99], v[84:87], v[24:27]\n\t"
        "s_mov_b64 exec, s[64:65]\n\t"
        "ds_write_b64 v56, v[52:53]\n\t"
        "s_mov_b64 exec, -1\n\t"
        "ds_read_b128 v[108:111], v129 offset:0\n\t"
        "v_mfma_f32_16x16x32_bf16 v[28:31], a[100:103], v[84:87], v[28:31]\n\t"
        "ds_read_b64_tr_b16 a[132:133], v131 offset:24576\n\t"
        "ds_read_b64_tr_b16 a[134:135], v131 offset:25344\n\t"
        "v_add_u32 v53, 4096, v80\n\t"
        "v_perm_b32 v56, v66, v66, s44\n\t"
        "v_mfma_f32_16x16x32_bf16 v[32:35], a[104:107], v[84:87], v[32:35]\n\t"
        "buffer_load_dwordx4 v142, s[24:27], s36 offen offset:2048 lds\n\t"
        "ds_read_b64_tr_b16 a[136:137], v132 offset:24576\n\t"
        "v_readlane_b32 s40, v155, 16\n\t"
        "v_mfma_f32_16x16x32_bf16 v[36:39], a[72:75], v[84:87], v[36:39]\n\t"
        "ds_read_b128 v[112:115], v129 offset:4096\n\t"
        "ds_read_b64_tr_b16 a[138:139], v132 offset:25344\n\t"
        "v_readlane_b32 s43, v155, 4\n\t"
        "s_add_u32 m0, s40, s35\n\t"
        "v_mfma_f32_16x16x32_bf16 v[40:43], a[76:79], v[84:87], v[40:43]\n\t"
        "ds_read_b64_tr_b16 a[140:141], v133 offset:24576\n\t"
        "ds_read_b64_tr_b16 a[142:143], v133 offset:25344\n\t"
        "v_perm_b32 v57, v66, v66, s45\n\t"
        "v_mfma_f32_16x16x32_bf16 v[44:47], a[80:83], v[84:87], v[44:47]\n\t"
        "ds_read_b64_tr_b16 a[144:145], v131 offset:30720\n\t"
        "ds_read_b64_tr_b16 a[146:147], v131 offset:31488\n\t"
        "v_add_f32 v58, s46, v68\n\t"
        "s_waitcnt lgkmcnt(11)\n\t"
        "v_mfma_f32_16x16x32_bf16 v[0:3], a[12:15], v[88:91], v[0:3]\n\t"
        "buffer_load_dwordx4 v134, s[16:19], s43 offen  lds\n\t"
        "ds_read_b64_tr_b16 a[148:149], v132 offset:30720\n\t"
        "v_readlane_b32 s40, v155, 17\n\t"
        "v_readlane_b32 s43, v155, 5\n\t"
        "v_mfma_f32_16x16x32_bf16 v[4:7], a[16:19], v[88:91], v[4:7]\n\t"
        "ds_read_b64_tr_b16 a[150:151], v132 offset:31488\n\t"
        "s_add_u32 m0, s40, s35\n\t"
        "ds_read_b64_tr_b16 a[152:153], v133 offset:30720\n\t"
        "v_mfma_f32_16x16x32_bf16 v[8:11], a[20:23], v[88:91], v[8:11]\n\t"
        "ds_read_b64_tr_b16 a[154:155], v133 offset:31488\n\t"
        "v_add_f32 v59, s47, v69\n\t"
        "v_add_f32 v56, v56, v58\n\t"
        "v_mfma_f32_16x16x32_bf16 v[0:3], a[48:51], v[92:95], v[0:3]\n\t"
        "ds_read_b128 v[84:87], v129 offset:8192\n\t"
        "v_add_f32 v57, v57, v59\n\t"
        "v_mfma_f32_16x16x32_bf16 v[4:7], a[52:55], v[92:95], v[4:7]\n\t"
        "buffer_load_dwordx4 v135, s[16:19], s43 offen  lds\n\t"
        "v_readlane_b32 s40, v155, 18\n\t"
        "v_readlane_b32 s43, v155, 6\n\t"
        "v_mfma_f32_16x16x32_bf16 v[8:11], a[56:59], v[92:95], v[8:11]\n\t"
        "s_add_u32 m0, s40, s35\n\t"
        "v_max_f32 v56, 0, v56\n\t"
        "v_mfma_f32_16x16x32_bf16 v[0:3], a[84:87], v[96:99], v[0:3]\n\t"
        "v_max_f32 v57, 0, v57\n\t"
        "v_max_f32 v58, 0, v58\n\t"
        "v_mfma_f32_16x16x32_bf16 v[4:7], a[88:91], v[96:99], v[4:7]\n\t"
        "v_max_f32 v59, 0, v59\n\t"
        "v_cvt_pk_bf16_f32 v52, v56, v57\n\t"
        "v_cvt_pk_bf16_f32 v58, v58, v59\n\t"
        "v_mfma_f32_16x16x32_bf16 v[8:11], a[92:95], v[96:99], v[8:11]\n\t"
        "buffer_load_dwordx4 v136, s[16:19], s43 offen  lds\n\t"
        "v_readlane_b32 s40, v155, 19\n\t"
        "v_mfma_f32_16x16x32_bf16 v[36:39], a[24:27], v[100:103], v[36:39]\n\t"
        "v_readlane_b32 s43, v155, 7\n\t"
        "s_add_u32 m0, s40, s35\n\t"
        "v_cmp_ne_u32 s[64:65], v52, v58\n\t"
        ".LpY0%=:\n\t"
        "s_cbranch_execz .LrY%=\n\t"
        "v_mfma_f32_16x16x32_bf16 v[40:43], a[28:31], v[100:103], v[40:43]\n\t"
        "s_nop 0\n\t"
        "v_mbcnt_lo_u32_b32 v56, s64, v54\n\t"
        "v_mbcnt_hi_u32_b32 v56, s65, v56\n\t"
        "v_mfma_f32_16x16x32_bf16 v[44:47], a[32:35], v[100:103], v[44:47]\n\t"
        "s_bcnt1_i32_b64 s46, s[64:65]\n\t"
        "s_add_u32 s61, s61, s46\n\t"
        "v_mfma_f32_16x16x32_bf16 v[36:39], a[60:63], v[104:107], v[36:39]\n\t"
        "buffer_load_dwordx4 v137, s[16:19], s43 offen  lds\n\t"
        "s_add_u32 m0, s37, 0\n\t"
        "ds_read_b128 v[88:91], v129 offset:12288\n\t"
        "v_mfma_f32_16x16x32_bf16 v[40:43], a[64:67], v[104:107], v[40:43]\n\t"
        "s_add_u32 s36, s36, s39\n\t"
        "s_waitcnt vmcnt(15)\n\t"
        "v_and_b32 v48, s54, v48\n\t"
        "v_pk_max_u16 v60, v154, v48\n\t"
        "v_mfma_f32_16x16x32_bf16 v[44:47], a[68:71], v[104:107], v[44:47]\n\t"
        "v_and_b32 v49, s54, v49\n\t"
        "v_pk_max_u16 v60, v60, v49\n\t"
        "v_and_b32 v50, s54, v50\n\t"
        "s_waitcnt lgkmcnt(1)\n\t"
        "v_mfma_f32_16x16x32_bf16 v[0:3], a[120:123], v[108:111], v[0:3]\n\t"
        "v_pk_max_u16 v60, v60, v50\n\t"
        "v_and_b32 v51, s54, v51\n\t"
        "v_mfma_f32_16x16x32_bf16 v[4:7], a[124:127], v[108:111], v[4:7]\n\t"
        "buffer_load_dwordx4 v140, s[24:27], s36 offen offset:0 lds\n\t"
        "v_pk_max_u16 v60, v60, v51\n\t"
        "v_mfma_f32_16x16x32_bf16 v[8:11], a[128:131], v[108:111], v[8:11]\n\t"
        "s_waitcnt vmcnt(15)\n\t"
        "v_and_b32 v61, s54, v61\n\t"
        "v_pk_max_u16 v60, v60, v61\n\t"
        "v_cmp_ne_u32 s[12:13], v60, v154\n\t"
        "v_mfma_f32_16x16x32_bf16 v[0:3], a[108:111], v[112:115], v[0:3]\n\t"
        "s_lshl_b32 s6, 256, s4\n\t"
        "v_min_u32 v56, 97, v56\n\t"
        "v_mfma_f32_16x16x32_bf16 v[4:7], a[112:115], v[112:115], v[4:7]\n\t"
        "s_cmp_lg_u64 s[12:13], 0\n\t"
        "s_cselect_b32 s6, s6, 0\n\t"
        "v_mfma_f32_16x16x32_bf16 v[8:11], a[116:119], v[112:115], v[8:11]\n\t"
        "buffer_load_dwordx4 v141, s[24:27], s36 offen offset:1024 lds\n\t"
        "v_lshl_add_u32 v56, v56, 3, s60\n\t"
        "v_mfma_f32_16x16x32_bf16 v[12:15], a[120:123], v[112:115], v[12:15]\n\t"
        "s_mov_b64 exec, s[64:65]\n\t"
        "ds_write_b64 v56, v[52:53]\n\t"
        "s_mov_b64 exec, -1\n\t"
        "v_mfma_f32_16x16x32_bf16 v[16:19], a[124:127], v[112:115], v[16:19]\n\t"
        "s_cmp_gt_u32 s61, s53\n\t"
        "s_cselect_b32 s8, 1, 0\n\t"
        "s_or_b32 s56, s56, s8\n\t"
        "v_mfma_f32_16x16x32_bf16 v[20:23], a[128:131], v[112:115], v[20:23]\n\t"
        "s_cmp_gt_u32 s3, 64\n\t"
        "s_cselect_b32 s8, 1, 0\n\t"
        "s_or_b32 s56, s56, s8\n\t"
        "v_mfma_f32_16x16x32_bf16 v[0:3], a[132:135], v[84:87], v[0:3]\n\t"
        "s_waitcnt vmcnt(9)\n\t"
        "s_barrier\n\t"
        "buffer_load_dwordx4 v142, s[24:27], s36 offen offset:2048 lds\n\t"
        "s_add_u32 m0, s37, 12288\n\t"
        "ds_read_b128 v[92:95], v129 offset:16384\n\t"
        "v_mfma_f32_16x16x32_bf16 v[4:7], a[136:139], v[84:87], v[4:7]\n\t"
        "ds_read_b64_tr_b16 a[156:157], v131 offset:36864\n\t"
        "s_add_u32 s36, s36, s38\n\t"
        "ds_read_b64_tr_b16 a[158:159], v131 offset:37632\n\t"
        "v_mfma_f32_16x16x32_bf16 v[8:11], a[140:143], v[84:87], v[8:11]\n\t"
        "ds_read_b64_tr_b16 a[160:161], v132 offset:36864\n\t"
        "ds_read_b64_tr_b16 a[162:163], v132 offset:37632\n\t"
        "s_min_u32 s61, s61, 80\n\t"
        "v_min_u32 v78, s61, v116\n\t"
        "v_mfma_f32_16x16x32_bf16 v[12:15], a[108:111], v[84:87], v[12:15]\n\t"
        "ds_read_b128 v[96:99], v128 offset:20608\n\t"
        "ds_read_b64_tr_b16 a[164:165], v133 offset:36864\n\t"
        "v_lshl_add_u32 v78, v78, 3, s60\n\t"
        "v_mfma_f32_16x16x32_bf16 v[16:19], a[112:115], v[84:87], v[16:19]\n\t"
        "ds_read_b64 v[62:63], v78\n\t"
        "buffer_load_dwordx4 v140, s[24:27], s36 offen offset:0 lds\n\t"
        "v_cmp_ge_u32 s[66:67], s61, v116\n\t"
        "v_mfma_f32_16x16x32_bf16 v[20:23], a[116:119], v[84:87], v[20:23]\n\t"
        "ds_read_b64_tr_b16 a[166:167], v133 offset:37632\n\t"
        "ds_read_b64_tr_b16 a[168:169], v131 offset:43008\n\t"
        "s_mov_b32 s46, 0x07060302\n\t"
        "v_mfma_f32_16x16x32_bf16 v[24:27], a[120:123], v[84:87], v[24:27]\n\t"
        "ds_read_b128 v[100:103], v129 offset:64\n\t"
        "ds_read_b64_tr_b16 a[170:171], v131 offset:43776\n\t"
        "v_mfma_f32_16x16x32_bf16 v[28:31], a[124:127], v[84:87], v[28:31]\n\t"
        "ds_read_b64_tr_b16 a[172:173], v132 offset:43008\n\t"
        "ds_read_b64_tr_b16 a[174:175], v132 offset:43776\n\t"
        "v_mfma_f32_16x16x32_bf16 v[32:35], a[128:131], v[84:87], v[32:35]\n\t"
        "s_waitcnt vmcnt(8)\n\t"
        "s_barrier\n\t"
        "buffer_load_dwordx4 v141, s[24:27], s36 offen offset:1024 lds\n\t"
        "ds_read_b64_tr_b16 a[176:177], v133 offset:43008\n\t"
        "s_waitcnt lgkmcnt(11)\n\t"
        "v_mfma_f32_16x16x32_bf16 v[12:15], a[132:135], v[88:91], v[12:15]\n\t"
        "ds_read_b128 v[104:107], v129 offset:4160\n\t"
        "ds_read_b64_tr_b16 a[178:179], v133 offset:43776\n\t"
        "v_mfma_f32_16x16x32_bf16 v[16:19], a[136:139], v[88:91], v[16:19]\n\t"
        "ds_read_b64_tr_b16 a[180:181], v131 offset:49152\n\t"
        "ds_read_b64_tr_b16 a[182:183], v131 offset:49920\n\t"
        "v_mfma_f32_16x16x32_bf16 v[20:23], a[140:143], v[88:91], v[20:23]\n\t"
        "ds_read_b64_tr_b16 a[184:185], v132 offset:49152\n\t"
        "ds_read_b64_tr_b16 a[186:187], v132 offset:49920\n\t"
        "v_mfma_f32_16x16x32_bf16 v[24:27], a[108:111], v[88:91], v[24:27]\n\t"
        "buffer_load_dwordx4 v142, s[24:27], s36 offen offset:2048 lds\n\t"
        "s_add_u32 m0, s37, 24576\n\t"
        "ds_read_b64_tr_b16 a[188:189], v133 offset:49152\n\t"
        "v_mfma_f32_16x16x32_bf16 v[28:31], a[112:115], v[88:91], v[28:31]\n\t"
        "s_mov_b64 exec, s[66:67]\n\t"
        "s_waitcnt lgkmcnt(12)\n\t"
        "ds_write_b32 v63, v117\n\t"
        "s_mov_b64 exec, -1\n\t"
        "s_add_u32 s36, s36, s5\n\t"
        "ds_read_b64_tr_b16 a[190:191], v133 offset:49920\n\t"
        "v_mfma_f32_16x16x32_bf16 v[32:35], a[116:119], v[88:91], v[32:35]\n\t"
        "ds_read_b64_tr_b16 a[192:193], v131 offset:55296\n\t"
        "ds_read_b64_tr_b16 a[194:195], v131 offset:56064\n\t"
        "v_and_b32 v78, 0xfffff800, v63\n\t"
        "v_lshlrev_b32 v72, 16, v62\n\t"
        "v_mfma_f32_16x16x32_bf16 v[36:39], a[120:123], v[88:91], v[36:39]\n\t"
        "ds_read_b128 v[64:67], v78 offset:1536\n\t"
        "ds_read_b128 v[108:111], v129 offset:8256\n\t"
        "v_and_b32 v73, 0xffff0000, v62\n\t"
        "v_med3_f32 v72, v72, 0, s63\n\t"
        "v_mfma_f32_16x16x32_bf16 v[40:43], a[124:127], v[88:91], v[40:43]\n\t"
        "buffer_load_dwordx4 v140, s[24:27], s36 offen offset:0 lds\n\t"
        "ds_read_b64_tr_b16 a[196:197], v132 offset:55296\n\t"
        "v_med3_f32 v73, v73, 0, s63\n\t"
        "v_mfma_f32_16x16x32_bf16 v[44:47], a[128:131], v[88:91], v[44:47]\n\t"
        "ds_read_b64_tr_b16 a[198:199], v132 offset:56064\n\t"
        "ds_read_b64_tr_b16 a[200:201], v133 offset:55296\n\t"
        "v_perm_b32 v62, v73, v72, s46\n\t"
        "v_mfma_f32_16x16x32_bf16 v[24:27], a[132:135], v[92:95], v[24:27]\n\t"
        "ds_read_b64_tr_b16 a[202:203], v133 offset:56064\n\t"
        "v_fract_f32 v74, v72\n\t"
        "v_fract_f32 v75, v73\n\t"
        "v_mfma_f32_16x16x32_bf16 v[28:31], a[136:139], v[92:95], v[28:31]\n\t"
        "v_cvt_u32_f32 v76, v72\n\t"
        "v_cvt_u32_f32 v77, v73\n\t"
        "v_mfma_f32_16x16x32_bf16 v[32:35], a[140:143], v[92:95], v[32:35]\n\t"
        "v_cmp_neq_f32 s[64:65], 0, v74\n\t"
        "v_cmp_neq_f32 s[44:45], 0, v75\n\t"
        "v_mfma_f32_16x16x32_bf16 v[36:39], a[108:111], v[92:95], v[36:39]\n\t"
        "v_sub_f32 v78, 1.0, v74\n\t"
        "v_sub_f32 v79, 1.0, v75\n\t"
        "v_cvt_pk_bf16_f32 v79, v78, v79\n\t"
        "v_mfma_f32_16x16x32_bf16 v[40:43], a[112:115], v[92:95], v[40:43]\n\t"
        "v_lshlrev_b32 v78, 16, v79\n\t"
        "v_and_b32 v79, 0xffff0000, v79\n\t"
        "v_mfma_f32_16x16x32_bf16 v[44:47], a[116:119], v[92:95], v[44:47]\n\t"
        "buffer_load_dwordx4 v141, s[24:27], s36 offen offset:1024 lds\n\t"
        "v_mul_f32 v80, v78, v79\n\t"
        "v_mfma_f32_16x16x32_bf16 v[36:39], a[96:99], v[96:99], v[36:39]\n\t"
        "ds_read_b128 v[112:115], v129 offset:128\n\t"
        "v_mul_f32 v78, v78, v75\n\t"
        "v_mul_f32 v79, v74, v79\n\t"
        "v_mfma_f32_16x16x32_bf16 v[40:43], a[100:103], v[96:99], v[40:43]\n\t"
        "v_mul_f32 v81, v74, v75\n\t"
        "v_cndmask_b32 v78, v79, v78, s[44:45]\n\t"
        "v_mfma_f32_16x16x32_bf16 v[44:47], a[104:107], v[96:99], v[44:47]\n\t"
        "v_cndmask_b32 v79, 0, v79, s[44:45]\n\t"
        "v_cvt_pk_bf16_f32 v70, v80, v78\n\t"
        "s_waitcnt lgkmcnt(2)\n\t"
        "v_mfma_f32_16x16x32_bf16 v[0:3], a[156:159], v[100:103], v[0:3]\n\t"
        "buffer_load_dwordx4 v142, s[24:27], s36 offen offset:2048 lds\n\t"
        "s_add_u32 m0, s37, 36864\n\t"
        "ds_read_b128 v[84:87], v129 offset:4224\n\t"
        "v_mfma_f32_16x16x32_bf16 v[4:7], a[160:163], v[100:103], v[4:7]\n\t"
        "s_add_u32 s36, s36, s39\n\t"
        "v_cvt_pk_bf16_f32 v71, v79, v81\n\t"
        "v_pk_max_u16 v78, v62, v64\n\t"
        "v_mfma_f32_16x16x32_bf16 v[8:11], a[164:167], v[100:103], v[8:11]\n\t"
        "v_pk_min_u16 v78, v78, v65\n\t"
        "v_cmp_ne_u32 vcc, v78, v62\n\t"
        "v_mfma_f32_16x16x32_bf16 v[0:3], a[144:147], v[104:107], v[0:3]\n\t"
        "v_sub_u32 v78, v77, v67\n\t"
        "v_mad_u32_u24 v79, v77, 6, v66\n\t"
        "v_mfma_f32_16x16x32_bf16 v[4:7], a[148:151], v[104:107], v[4:7]\n\t"
        "v_mad_u32_u24 v79, v76, s62, v79\n\t"
        "s_and_b64 vcc, vcc, s[66:67]\n\t"
        "v_mfma_f32_16x16x32_bf16 v[8:11], a[152:155], v[104:107], v[8:11]\n\t"
        "s_waitcnt vmcnt(6) lgkmcnt(2)\n\t"
        "s_barrier\n\t"
        "buffer_load_dwordx4 v140, s[24:27], s36 offen offset:0 lds\n\t"
        "ds_read_b64_tr_b16 a[204:205], v131 offset:0\n\t"
        "v_mfma_f32_16x16x32_bf16 v[12:15], a[156:159], v[104:107], v[12:15]\n\t"
        "ds_read_b128 v[88:91], v129 offset:12352\n\t"
        "ds_read_b64_tr_b16 a[206:207], v131 offset:768\n\t"
        "s_or_b64 s[56:57], s[56:57], vcc\n\t"
        "v_bfe_u32 v80, v78, 2, 1\n\t"
        "v_mfma_f32_16x16x32_bf16 v[16:19], a[160:163], v[104:107], v[16:19]\n\t"
        "ds_read_b64_tr_b16 a[208:209], v132 offset:0\n\t"
        "ds_read_b64_tr_b16 a[210:211], v132 offset:768\n\t"
        "v_or_b32 v80, v80, v79\n\t"
        "v_mfma_f32_16x16x32_bf16 v[20:23], a[164:167], v[104:107], v[20:23]\n\t"
        "ds_read_b64_tr_b16 a[212:213], v133 offset:0\n\t"
        "ds_read_b64_tr_b16 a[214:215], v133 offset:768\n\t"
        "v_add_u32 v78, 1, v78\n\t"
        "v_mfma_f32_16x16x32_bf16 v[0:3], a[168:171], v[108:111], v[0:3]\n\t"
        "buffer_load_dwordx4 v141, s[24:27], s36 offen offset:1024 lds\n\t"
        "ds_read_b64_tr_b16 a[216:217], v131 offset:6144\n\t"
        "v_bfe_u32 v78, v78, 2, 1\n\t"
        "v_mfma_f32_16x16x32_bf16 v[4:7], a[172:175], v[108:111], v[4:7]\n\t"
        "ds_read_b64_tr_b16 a[218:219], v131 offset:6912\n\t"
        "ds_read_b64_tr_b16 a[220:221], v132 offset:6144\n\t"
        "v_add3_u32 v78, v79, v78, 6\n\t"
        "v_cndmask_b32 v78, v80, v78, s[44:45]\n\t"
        "v_mfma_f32_16x16x32_bf16 v[8:11], a[176:179], v[108:111], v[8:11]\n\t"
        "ds_read_b64_tr_b16 a[222:223], v132 offset:6912\n\t"
        "ds_read_b64_tr_b16 a[224:225], v133 offset:6144\n\t"
        "v_cndmask_b32 v79, 0, v119, s[64:65]\n\t"
        "v_mfma_f32_16x16x32_bf16 v[12:15], a[144:147], v[108:111], v[12:15]\n\t"
        "ds_read_b64_tr_b16 a[226:227], v133 offset:6912\n\t"
        "v_add_u32 v81, v80, v79\n\t"
        "v_mfma_f32_16x16x32_bf16 v[16:19], a[148:151], v[108:111], v[16:19]\n\t"
        "buffer_load_dwordx4 v142, s[24:27], s36 offen offset:2048 lds\n\t"
        "s_add_u32 m0, s37, 49152\n\t"
        "v_mfma_f32_16x16x32_bf16 v[20:23], a[152:155], v[108:111], v[20:23]\n\t"
        "s_waitcnt vmcnt(6)\n\t"
        "s_barrier\n\t"
        "ds_read_b64_tr_b16 a[228:229], v131 offset:12288\n\t"
        "s_add_u32 s36, s36, s38\n\t"
        "ds_read_b64_tr_b16 a[230:231], v131 offset:13056\n\t"
        "v_mfma_f32_16x16x32_bf16 v[24:27], a[156:159], v[108:111], v[24:27]\n\t"
        "ds_read_b128 v[92:95], v129 offset:8320\n\t"
        "ds_read_b64_tr_b16 a[232:233], v132 offset:12288\n\t"
        "v_add_u32 v82, v78, v79\n\t"
        "v_cndmask_b32 v78, v81, v78, s[44:45]\n\t"
        "v_mfma_f32_16x16x32_bf16 v[28:31], a[160:163], v[108:111], v[28:31]\n\t"
        "ds_read_b64_tr_b16 a[234:235], v132 offset:13056\n\t"
        "ds_read_b64_tr_b16 a[236:237], v133 offset:12288\n\t"
        "v_lshl_or_b32 v68, v78, 16, v80\n\t"
        "v_mfma_f32_16x16x32_bf16 v[32:35], a[164:167], v[108:111], v[32:35]\n\t"
        "buffer_load_dwordx4 v140, s[24:27], s36 offen offset:0 lds\n\t"
        "ds_read_b64_tr_b16 a[238:239], v133 offset:13056\n\t"
        "v_lshl_or_b32 v69, v82, 16, v81\n\t"
        "s_waitcnt lgkmcnt(10)\n\t"
        "v_mfma_f32_16x16x32_bf16 v[0:3], a[192:195], v[112:115], v[0:3]\n\t"
        "ds_write_b128 v118, v[68:71]\n\t"
        "v_mfma_f32_16x16x32_bf16 v[4:7], a[196:199], v[112:115], v[4:7]\n\t"
        "ds_read_b128 v[160:163], v185 offset:0\n\t"
        "v_xor_b32 v154, 32, v186\n\t"
        "v_mfma_f32_16x16x32_bf16 v[8:11], a[200:203], v[112:115], v[8:11]\n\t"
        "ds_read_b64_tr_b16 a[240:241], v131 offset:18432\n\t"
        "v_mfma_f32_16x16x32_bf16 v[0:3], a[180:183], v[84:87], v[0:3]\n\t"
        "buffer_load_dwordx4 v141, s[24:27], s36 offen offset:1024 lds\n\t"
        "v_mfma_f32_16x16x32_bf16 v[4:7], a[184:187], v[84:87], v[4:7]\n\t"
        "ds_read_b64_tr_b16 a[242:243], v131 offset:19200\n\t"
        "v_mfma_f32_16x16x32_bf16 v[8:11], a[188:191], v[84:87], v[8:11]\n\t"
        "ds_read_b64_tr_b16 a[244:245], v132 offset:18432\n\t"
        "v_mfma_f32_16x16x32_bf16 v[12:15], a[192:195], v[84:87], v[12:15]\n\t"
        "ds_read_b128 v[96:99], v130 offset:0\n\t"
        "ds_read_b64_tr_b16 a[246:247], v132 offset:19200\n\t"
        "v_mfma_f32_16x16x32_bf16 v[16:19], a[196:199], v[84:87], v[16:19]\n\t"
        "buffer_load_dwordx4 v142, s[24:27], s36 offen offset:2048 lds\n\t"
        "ds_read_b64_tr_b16 a[248:249], v133 offset:18432\n\t"
        "v_mfma_f32_16x16x32_bf16 v[20:23], a[200:203], v[84:87], v[20:23]\n\t"
        "ds_read_b64_tr_b16 a[250:251], v133 offset:19200\n\t"
        "v_mfma_f32_16x16x32_bf16 v[12:15], a[168:171], v[88:91], v[12:15]\n\t"
        "ds_read_b128 v[100:103], v130 offset:4096\n\t"
        "v_mfma_f32_16x16x32_bf16 v[16:19], a[172:175], v[88:91], v[16:19]\n\t"
        "s_waitcnt lgkmcnt(8)\n\t"
        "v_lshlrev_b32_sdwa v157, 5, v160 dst_sel:DWORD dst_unused:UNUSED_PAD src0_sel:DWORD src1_sel:WORD_0\n\t"
        "v_lshlrev_b32_sdwa v158, 5, v160 dst_sel:DWORD dst_unused:UNUSED_PAD src0_sel:DWORD src1_sel:WORD_1\n\t"
        "v_or_b32 v157, v184, v157\n\t"
        "v_mfma_f32_16x16x32_bf16 v[20:23], a[176:179], v[88:91], v[20:23]\n\t"
        "ds_read_b64_tr_b16 v[60:61], v157 offset:0\n\t"
        "v_or_b32 v158, v184, v158\n\t"
        "v_xor_b32 v159, 32, v157\n\t"
        "v_mfma_f32_16x16x32_bf16 v[24:27], a[144:147], v[88:91], v[24:27]\n\t"
        "ds_read_b64_tr_b16 v[62:63], v158 offset:0\n\t"
        "v_xor_b32 v152, 32, v158\n\t"
        "v_mfma_f32_16x16x32_bf16 v[28:31], a[148:151], v[88:91], v[28:31]\n\t"
        "ds_read_b64_tr_b16 v[64:65], v159 offset:0\n\t"
        "v_perm_b32 v164, v162, v162, v168\n\t"
        "v_mfma_f32_16x16x32_bf16 v[32:35], a[152:155], v[88:91], v[32:35]\n\t"
        "ds_read_b64_tr_b16 v[66:67], v152 offset:0\n\t"
        "v_perm_b32 v165, v162, v162, v169\n\t"
        "v_mfma_f32_16x16x32_bf16 v[36:39], a[156:159], v[88:91], v[36:39]\n\t"
        "ds_read_b64_tr_b16 v[68:69], v157 offset:64\n\t"
        "v_perm_b32 v166, v162, v162, v170\n\t"
        "v_perm_b32 v167, v162, v162, v171\n\t"
        "v_mfma_f32_16x16x32_bf16 v[40:43], a[160:163], v[88:91], v[40:43]\n\t"
        "ds_read_b64_tr_b16 v[70:71], v158 offset:64\n\t"
        "v_mfma_f32_16x16x32_bf16 v[44:47], a[164:167], v[88:91], v[44:47]\n\t"
        "s_waitcnt vmcnt(3) lgkmcnt(7)\n\t"
        "s_barrier\n\t"
        "v_mfma_f32_16x16x32_bf16 v[0:3], a[204:207], v[92:95], v[0:3]\n\t"
        "ds_read_b64_tr_b16 v[72:73], v159 offset:64\n\t"
        "ds_read_b128 v[104:107], v130 offset:8192\n\t"
        "v_mfma_f32_16x16x32_bf16 v[4:7], a[208:211], v[92:95], v[4:7]\n\t"
        "ds_read_b64_tr_b16 v[74:75], v152 offset:64\n\t"
        "ds_read_b64_tr_b16 a[252:253], v131 offset:24576\n\t"
        "v_mfma_f32_16x16x32_bf16 v[8:11], a[212:215], v[92:95], v[8:11]\n\t"
        "ds_read_b64_tr_b16 v[76:77], v157 offset:128\n\t"
        "ds_read_b64_tr_b16 a[254:255], v131 offset:25344\n\t"
        "v_lshlrev_b32_sdwa v157, 5, v161 dst_sel:DWORD dst_unused:UNUSED_PAD src0_sel:DWORD src1_sel:WORD_0\n\t"
        "v_or_b32 v157, v184, v157\n\t"
        "v_mfma_f32_16x16x32_bf16 v[12:15], a[180:183], v[92:95], v[12:15]\n\t"
        "ds_read_b64_tr_b16 v[78:79], v158 offset:128\n\t"
        "ds_read_b64_tr_b16 v[188:189], v132 offset:24576\n\t"
        "v_lshlrev_b32_sdwa v158, 5, v161 dst_sel:DWORD dst_unused:UNUSED_PAD src0_sel:DWORD src1_sel:WORD_1\n\t"
        "s_waitcnt lgkmcnt(11)\n\t"
        "v_mfma_f32_16x16x32_bf16 v[48:51], v[60:63], v[164:167], 0\n\t"
        "ds_read_b64_tr_b16 v[80:81], v159 offset:128\n\t"
        "ds_read_b64_tr_b16 v[190:191], v132 offset:25344\n\t"
        "v_or_b32 v158, v184, v158\n\t"
        "v_mfma_f32_16x16x32_bf16 v[16:19], a[184:187], v[92:95], v[16:19]\n\t"
        "ds_read_b64_tr_b16 v[82:83], v152 offset:128\n\t"
        "ds_read_b64_tr_b16 v[192:193], v133 offset:24576\n\t"
        "v_xor_b32 v159, 32, v157\n\t"
        "s_waitcnt lgkmcnt(12)\n\t"
        "v_mfma_f32_16x16x32_bf16 v[52:55], v[64:67], v[164:167], 0\n\t"
        "ds_read_b64_tr_b16 v[60:61], v157 offset:0\n\t"
        "ds_read_b64_tr_b16 v[194:195], v133 offset:25344\n\t"
        "v_xor_b32 v152, 32, v158\n\t"
        "v_mfma_f32_16x16x32_bf16 v[20:23], a[188:191], v[92:95], v[20:23]\n\t"
        "ds_read_b64_tr_b16 v[62:63], v158 offset:0\n\t"
        "v_mfma_f32_16x16x32_bf16 v[56:59], v[68:71], v[164:167], 0\n\t"
        "ds_read_b64_tr_b16 v[64:65], v159 offset:0\n\t"
        "v_mfma_f32_16x16x32_bf16 v[24:27], a[192:195], v[92:95], v[24:27]\n\t"
        "ds_read_b64_tr_b16 v[66:67], v152 offset:0\n\t"
        "v_mfma_f32_16x16x32_bf16 v[28:31], a[196:199], v[92:95], v[28:31]\n\t"
        "ds_read_b64_tr_b16 v[68:69], v157 offset:64\n\t"
        "s_waitcnt lgkmcnt(12)\n\t"
        "v_mfma_f32_16x16x32_bf16 v[116:119], v[72:75], v[164:167], 0\n\t"
        "ds_read_b64_tr_b16 v[70:71], v158 offset:64\n\t"
        "v_mfma_f32_16x16x32_bf16 v[32:35], a[200:203], v[92:95], v[32:35]\n\t"
        "ds_read_b64_tr_b16 v[72:73], v159 offset:64\n\t"
        "s_waitcnt lgkmcnt(11)\n\t"
        "v_mfma_f32_16x16x32_bf16 v[120:123], v[76:79], v[164:167], 0\n\t"
        "ds_read_b64_tr_b16 v[74:75], v152 offset:64\n\t"
        "v_mfma_f32_16x16x32_bf16 v[0:3], a[228:231], v[96:99], v[0:3]\n\t"
        "ds_read_b64_tr_b16 v[76:77], v157 offset:128\n\t"
        "s_waitcnt lgkmcnt(10)\n\t"
        "v_mfma_f32_16x16x32_bf16 v[124:127], v[80:83], v[164:167], 0\n\t"
        "ds_read_b64_tr_b16 v[78:79], v158 offset:128\n\t"
        "v_perm_b32 v164, v163, v163, v168\n\t"
        "v_perm_b32 v165, v163, v163, v169\n\t"
        "v_mfma_f32_16x16x32_bf16 v[4:7], a[232:235], v[96:99], v[4:7]\n\t"
        "ds_read_b64_tr_b16 v[80:81], v159 offset:128\n\t"
        "v_perm_b32 v166, v163, v163, v170\n\t"
        "v_perm_b32 v167, v163, v163, v171\n\t"
        "v_mfma_f32_16x16x32_bf16 v[8:11], a[236:239], v[96:99], v[8:11]\n\t"
        "ds_read_b128 v[108:111], v129 offset:12416\n\t"
        "s_waitcnt lgkmcnt(9)\n\t"
        "v_mfma_f32_16x16x32_bf16 v[48:51], v[60:63], v[164:167], v[48:51]\n\t"
        "ds_read_b64_tr_b16 v[82:83], v152 offset:128\n\t"
        "v_mfma_f32_16x16x32_bf16 v[0:3], a[216:219], v[100:103], v[0:3]\n\t"
        "ds_read_b128 v[160:163], v185 offset:256\n\t"
        "ds_read_b64_tr_b16 v[196:197], v131 offset:30720\n\t"
        "s_waitcnt lgkmcnt(10)\n\t"
        "v_mfma_f32_16x16x32_bf16 v[52:55], v[64:67], v[164:167], v[52:55]\n\t"
        "ds_read_b64_tr_b16 v[198:199], v131 offset:31488\n\t"
        "ds_read_b64_tr_b16 v[200:201], v132 offset:30720\n\t"
        "v_mfma_f32_16x16x32_bf16 v[4:7], a[220:223], v[100:103], v[4:7]\n\t"
        "ds_read_b64_tr_b16 v[202:203], v132 offset:31488\n\t"
        "ds_read_b64_tr_b16 v[204:205], v133 offset:30720\n\t"
        "s_waitcnt lgkmcnt(11)\n\t"
        "v_mfma_f32_16x16x32_bf16 v[56:59], v[68:71], v[164:167], v[56:59]\n\t"
        "ds_read_b64_tr_b16 v[206:207], v133 offset:31488\n\t"
        "v_cvt_pk_bf16_f32 v48, v48, v49\n\t"
        "v_cvt_pk_bf16_f32 v49, v50, v51\n\t"
        "v_mfma_f32_16x16x32_bf16 v[8:11], a[224:227], v[100:103], v[8:11]\n\t"
        "ds_write_b64 v186, v[48:49] offset:0\n\t"
        "v_mfma_f32_16x16x32_bf16 v[116:119], v[72:75], v[164:167], v[116:119]\n\t"
        "ds_read_b64_tr_b16 v[208:209], v131 offset:36864\n\t"
        "v_cvt_pk_bf16_f32 v52, v52, v53\n\t"
        "v_cvt_pk_bf16_f32 v53, v54, v55\n\t"
        "v_mfma_f32_16x16x32_bf16 v[12:15], a[228:231], v[100:103], v[12:15]\n\t"
        "s_waitcnt vmcnt(0)\n\t"
        "s_barrier\n\t"
        "s_waitcnt lgkmcnt(10)\n\t"
        "v_mfma_f32_16x16x32_bf16 v[120:123], v[76:79], v[164:167], v[120:123]\n\t"
        "ds_write_b64 v154, v[52:53] offset:0\n\t"
        "v_mfma_f32_16x16x32_bf16 v[16:19], a[232:235], v[100:103], v[16:19]\n\t"
        "ds_read_b128 v[112:115], v130 offset:64\n\t"
        "v_cvt_pk_bf16_f32 v56, v56, v57\n\t"
        "v_cvt_pk_bf16_f32 v57, v58, v59\n\t"
        "s_waitcnt lgkmcnt(9)\n\t"
        "v_mfma_f32_16x16x32_bf16 v[124:127], v[80:83], v[164:167], v[124:127]\n\t"
        "ds_write_b64 v186, v[56:57] offset:64\n\t"
        "v_mfma_f32_16x16x32_bf16 v[20:23], a[236:239], v[100:103], v[20:23]\n\t"
        "ds_read_b64_tr_b16 v[210:211], v131 offset:37632\n\t"
        "v_cvt_pk_bf16_f32 v116, v116, v117\n\t"
        "v_mfma_f32_16x16x32_bf16 v[0:3], a[240:243], v[104:107], v[0:3]\n\t"
        "ds_read_b128 v[84:87], v130 offset:4160\n\t"
        "v_cvt_pk_bf16_f32 v117, v118, v119\n\t"
        "v_mfma_f32_16x16x32_bf16 v[4:7], a[244:247], v[104:107], v[4:7]\n\t"
        "ds_write_b64 v154, v[116:117] offset:64\n\t"
        "v_mfma_f32_16x16x32_bf16 v[8:11], a[248:251], v[104:107], v[8:11]\n\t"
        "ds_read_b64_tr_b16 v[212:213], v132 offset:36864\n\t"
        "v_cvt_pk_bf16_f32 v120, v120, v121\n\t"
        "v_cvt_pk_bf16_f32 v121, v122, v123\n\t"
        "v_mfma_f32_16x16x32_bf16 v[12:15], a[216:219], v[104:107], v[12:15]\n\t"
        "ds_write_b64 v186, v[120:121] offset:128\n\t"
        "v_mfma_f32_16x16x32_bf16 v[16:19], a[220:223], v[104:107], v[16:19]\n\t"
        "ds_read_b64_tr_b16 v[214:215], v132 offset:37632\n\t"
        "v_cvt_pk_bf16_f32 v124, v124, v125\n\t"
        "v_cvt_pk_bf16_f32 v125, v126, v127\n\t"
        "v_mfma_f32_16x16x32_bf16 v[20:23], a[224:227], v[104:107], v[20:23]\n\t"
        "ds_write_b64 v154, v[124:125] offset:128\n\t"
        "v_mfma_f32_16x16x32_bf16 v[24:27], a[228:231], v[104:107], v[24:27]\n\t"
        "ds_read_b128 v[88:91], v130 offset:12288\n\t"
        "ds_read_b64_tr_b16 v[216:217], v133 offset:36864\n\t"
        "v_lshlrev_b32_sdwa v157, 5, v160 dst_sel:DWORD dst_unused:UNUSED_PAD src0_sel:DWORD src1_sel:WORD_0\n\t"
        "v_mfma_f32_16x16x32_bf16 v[28:31], a[232:235], v[104:107], v[28:31]\n\t"
        "ds_read_b64_tr_b16 v[218:219], v133 offset:37632\n\t"
        "v_lshlrev_b32_sdwa v158, 5, v160 dst_sel:DWORD dst_unused:UNUSED_PAD src0_sel:DWORD src1_sel:WORD_1\n\t"
        "v_mfma_f32_16x16x32_bf16 v[32:35], a[236:239], v[104:107], v[32:35]\n\t"
        "ds_read_b64_tr_b16 v[220:221], v131 offset:43008\n\t"
        "v_or_b32 v157, v184, v157\n\t"
        "v_mfma_f32_16x16x32_bf16 v[12:15], a[204:207], v[108:111], v[12:15]\n\t"
        "ds_read_b64_tr_b16 v[60:61], v157 offset:0\n\t"
        "v_or_b32 v158, v184, v158\n\t"
        "v_xor_b32 v159, 32, v157\n\t"
        "v_mfma_f32_16x16x32_bf16 v[16:19], a[208:211], v[108:111], v[16:19]\n\t"
        "ds_read_b64_tr_b16 v[62:63], v158 offset:0\n\t"
        "v_xor_b32 v152, 32, v158\n\t"
        "v_mfma_f32_16x16x32_bf16 v[20:23], a[212:215], v[108:111], v[20:23]\n\t"
        "ds_read_b64_tr_b16 v[64:65], v159 offset:0\n\t"
        "v_perm_b32 v164, v162, v162, v168\n\t"
        "v_mfma_f32_16x16x32_bf16 v[24:27], a[180:183], v[108:111], v[24:27]\n\t"
        "ds_read_b64_tr_b16 v[66:67], v152 offset:0\n\t"
        "v_perm_b32 v165, v162, v162, v169\n\t"
        "v_mfma_f32_16x16x32_bf16 v[28:31], a[184:187], v[108:111], v[28:31]\n\t"
        "ds_read_b64_tr_b16 v[68:69], v157 offset:64\n\t"
        "v_perm_b32 v166, v162, v162, v170\n\t"
        "v_perm_b32 v167, v162, v162, v171\n\t"
        "v_mfma_f32_16x16x32_bf16 v[32:35], a[188:191], v[108:111], v[32:35]\n\t"
        "ds_read_b64_tr_b16 v[70:71], v158 offset:64\n\t"
        "v_mfma_f32_16x16x32_bf16 v[36:39], a[192:195], v[108:111], v[36:39]\n\t"
        "ds_read_b64_tr_b16 v[72:73], v159 offset:64\n\t"
        "v_mfma_f32_16x16x32_bf16 v[40:43], a[196:199], v[108:111], v[40:43]\n\t"
        "ds_read_b64_tr_b16 v[74:75], v152 offset:64\n\t"
        "v_mfma_f32_16x16x32_bf16 v[44:47], a[200:203], v[108:111], v[44:47]\n\t"
        "ds_read_b64_tr_b16 v[76:77], v157 offset:128\n\t"
        "v_lshlrev_b32_sdwa v157, 5, v161 dst_sel:DWORD dst_unused:UNUSED_PAD src0_sel:DWORD src1_sel:WORD_0\n\t"
        "v_or_b32 v157, v184, v157\n\t"
        "s_waitcnt lgkmcnt(9)\n\t"
        "v_mfma_f32_16x16x32_bf16 v[0:3], v[196:199], v[112:115], v[0:3]\n\t"
        "ds_read_b64_tr_b16 v[78:79], v158 offset:128\n\t"
        "ds_read_b128 v[92:95], v130 offset:8256\n\t"
        "v_lshlrev_b32_sdwa v158, 5, v161 dst_sel:DWORD dst_unused:UNUSED_PAD src0_sel:DWORD src1_sel:WORD_1\n\t"
        "v_mfma_f32_16x16x32_bf16 v[4:7], v[200:203], v[112:115], v[4:7]\n\t"
        "ds_read_b64_tr_b16 v[80:81], v159 offset:128\n\t"
        "ds_read_b64_tr_b16 v[222:223], v131 offset:43776\n\t"
        "v_or_b32 v158, v184, v158\n\t"
        "s_waitcnt lgkmcnt(10)\n\t"
        "v_mfma_f32_16x16x32_bf16 v[48:51], v[60:63], v[164:167], 0\n\t"
        "ds_read_b64_tr_b16 v[82:83], v152 offset:128\n\t"
        "ds_read_b64_tr_b16 v[224:225], v132 offset:43008\n\t"
        "v_xor_b32 v159, 32, v157\n\t"
        "v_mfma_f32_16x16x32_bf16 v[8:11], v[204:207], v[112:115], v[8:11]\n\t"
        "ds_read_b64_tr_b16 v[60:61], v157 offset:0\n\t"
        "ds_read_b64_tr_b16 v[226:227], v132 offset:43776\n\t"
        "v_xor_b32 v152, 32, v158\n\t"
        "s_waitcnt lgkmcnt(11)\n\t"
        "v_mfma_f32_16x16x32_bf16 v[52:55], v[64:67], v[164:167], 0\n\t"
        "ds_read_b64_tr_b16 v[62:63], v158 offset:0\n\t"
        "ds_read_b64_tr_b16 v[228:229], v133 offset:43008\n\t"
        "v_mfma_f32_16x16x32_bf16 v[0:3], a[252:255], v[84:87], v[0:3]\n\t"
        "ds_read_b64_tr_b16 v[64:65], v159 offset:0\n\t"
        "ds_read_b64_tr_b16 v[230:231], v133 offset:43776\n\t"
        "v_mfma_f32_16x16x32_bf16 v[56:59], v[68:71], v[164:167], 0\n\t"
        "ds_read_b64_tr_b16 v[66:67], v152 offset:0\n\t"
        "v_mfma_f32_16x16x32_bf16 v[4:7], v[188:191], v[84:87], v[4:7]\n\t"
        "ds_read_b64_tr_b16 v[68:69], v157 offset:64\n\t"
        "s_waitcnt lgkmcnt(13)\n\t"
        "v_mfma_f32_16x16x32_bf16 v[116:119], v[72:75], v[164:167], 0\n\t"
        "ds_read_b64_tr_b16 v[70:71], v158 offset:64\n\t"
        "v_mfma_f32_16x16x32_bf16 v[8:11], v[192:195], v[84:87], v[8:11]\n\t"
        "ds_read_b64_tr_b16 v[72:73], v159 offset:64\n\t"
        "v_mfma_f32_16x16x32_bf16 v[120:123], v[76:79], v[164:167], 0\n\t"
        "ds_read_b64_tr_b16 v[74:75], v152 offset:64\n\t"
        "v_mfma_f32_16x16x32_bf16 v[12:15], v[196:199], v[84:87], v[12:15]\n\t"
        "ds_read_b64_tr_b16 v[76:77], v157 offset:128\n\t"
        "s_waitcnt lgkmcnt(12)\n\t"
        "v_mfma_f32_16x16x32_bf16 v[124:127], v[80:83], v[164:167], 0\n\t"
        "ds_read_b64_tr_b16 v[78:79], v158 offset:128\n\t"
        "v_perm_b32 v164, v163, v163, v168\n\t"
        "v_perm_b32 v165, v163, v163, v169\n\t"
        "v_mfma_f32_16x16x32_bf16 v[16:19], v[200:203], v[84:87], v[16:19]\n\t"
        "s_waitcnt lgkmcnt(7)\n\t"
        "s_barrier\n\t"
        "v_perm_b32 v166, v163, v163, v170\n\t"
        "v_mfma_f32_16x16x32_bf16 v[20:23], v[204:207], v[84:87], v[20:23]\n\t"
        "ds_read_b64_tr_b16 v[80:81], v159 offset:128\n\t"
        "v_perm_b32 v167, v163, v163, v171\n\t"
        "v_mfma_f32_16x16x32_bf16 v[12:15], a[240:243], v[88:91], v[12:15]\n\t"
        "ds_read_b64_tr_b16 v[82:83], v152 offset:128\n\t"
        "v_mfma_f32_16x16x32_bf16 v[48:51], v[60:63], v[164:167], v[48:51]\n\t"
        "ds_read_b128 v[160:163], v185 offset:512\n\t"
        "v_mfma_f32_16x16x32_bf16 v[16:19], a[244:247], v[88:91], v[16:19]\n\t"
        "ds_read_b128 v[96:99], v130 offset:12352\n\t"
        "s_waitcnt lgkmcnt(8)\n\t"
        "v_mfma_f32_16x16x32_bf16 v[52:55], v[64:67], v[164:167], v[52:55]\n\t"
        "ds_read_b64_tr_b16 v[232:233], v131 offset:49152\n\t"
        "v_mfma_f32_16x16x32_bf16 v[20:23], a[248:251], v[88:91], v[20:23]\n\t"
        "ds_read_b64_tr_b16 v[234:235], v131 offset:49920\n\t"
        "v_mfma_f32_16x16x32_bf16 v[56:59], v[68:71], v[164:167], v[56:59]\n\t"
        "ds_read_b64_tr_b16 v[236:237], v132 offset:49152\n\t"
        "v_cvt_pk_bf16_f32 v48, v48, v49\n\t"
        "v_cvt_pk_bf16_f32 v49, v50, v51\n\t"
        "v_mfma_f32_16x16x32_bf16 v[24:27], a[216:219], v[88:91], v[24:27]\n\t"
        "ds_write_b64 v186, v[48:49] offset:12288\n\t"
        "ds_read_b64_tr_b16 v[238:239], v132 offset:49920\n\t"
        "s_waitcnt lgkmcnt(9)\n\t"
        "v_mfma_f32_16x16x32_bf16 v[116:119], v[72:75], v[164:167], v[116:119]\n\t"
        "ds_read_b64_tr_b16 v[240:241], v133 offset:49152\n\t"
        "ds_read_b64_tr_b16 v[242:243], v133 offset:49920\n\t"
        "v_cvt_pk_bf16_f32 v52, v52, v53\n\t"
        "v_cvt_pk_bf16_f32 v53, v54, v55\n\t"
        "v_mfma_f32_16x16x32_bf16 v[28:31], a[220:223], v[88:91], v[28:31]\n\t"
        "ds_write_b64 v154, v[52:53] offset:12288\n\t"
        "v_mfma_f32_16x16x32_bf16 v[120:123], v[76:79], v[164:167], v[120:123]\n\t"
        "ds_read_b64_tr_b16 v[244:245], v131 offset:55296\n\t"
        "v_cvt_pk_bf16_f32 v56, v56, v57\n\t"
        "v_cvt_pk_bf16_f32 v57, v58, v59\n\t"
        "v_mfma_f32_16x16x32_bf16 v[32:35], a[224:227], v[88:91], v[32:35]\n\t"
        "ds_write_b64 v186, v[56:57] offset:12352\n\t"
        "s_waitcnt lgkmcnt(9)\n\t"
        "v_mfma_f32_16x16x32_bf16 v[124:127], v[80:83], v[164:167], v[124:127]\n\t"
        "ds_read_b64_tr_b16 v[246:247], v131 offset:56064\n\t"
        "v_cvt_pk_bf16_f32 v116, v116, v117\n\t"
        "v_cvt_pk_bf16_f32 v117, v118, v119\n\t"
        "v_mfma_f32_16x16x32_bf16 v[36:39], a[228:231], v[88:91], v[36:39]\n\t"
        "ds_write_b64 v154, v[116:117] offset:12352\n\t"
        "v_mfma_f32_16x16x32_bf16 v[40:43], a[232:235], v[88:91], v[40:43]\n\t"
        "ds_read_b128 v[100:103], v130 offset:128\n\t"
        "v_cvt_pk_bf16_f32 v120, v120, v121\n\t"
        "v_mfma_f32_16x16x32_bf16 v[44:47], a[236:239], v[88:91], v[44:47]\n\t"
        "ds_read_b64_tr_b16 v[248:249], v132 offset:55296\n\t"
        "v_cvt_pk_bf16_f32 v121, v122, v123\n\t"
        "v_mfma_f32_16x16x32_bf16 v[0:3], v[208:211], v[92:95], v[0:3]\n\t"
        "ds_write_b64 v186, v[120:121] offset:12416\n\t"
        "v_mfma_f32_16x16x32_bf16 v[4:7], v[212:215], v[92:95], v[4:7]\n\t"
        "ds_read_b128 v[104:107], v130 offset:4224\n\t"
        "v_cvt_pk_bf16_f32 v124, v124, v125\n\t"
        "v_cvt_pk_bf16_f32 v125, v126, v127\n\t"
        "v_mfma_f32_16x16x32_bf16 v[8:11], v[216:219], v[92:95], v[8:11]\n\t"
        "ds_write_b64 v154, v[124:125] offset:12416\n\t"
        "v_mfma_f32_16x16x32_bf16 v[12:15], a[252:255], v[92:95], v[12:15]\n\t"
        "ds_read_b64_tr_b16 v[250:251], v132 offset:56064\n\t"
        "v_lshlrev_b32_sdwa v157, 5, v160 dst_sel:DWORD dst_unused:UNUSED_PAD src0_sel:DWORD src1_sel:WORD_0\n\t"
        "v_lshlrev_b32_sdwa v158, 5, v160 dst_sel:DWORD dst_unused:UNUSED_PAD src0_sel:DWORD src1_sel:WORD_1\n\t"
        "v_mfma_f32_16x16x32_bf16 v[16:19], v[188:191], v[92:95], v[16:19]\n\t"
        "ds_read_b64_tr_b16 v[252:253], v133 offset:55296\n\t"
        "v_or_b32 v157, v184, v157\n\t"
        "v_mfma_f32_16x16x32_bf16 v[20:23], v[192:195], v[92:95], v[20:23]\n\t"
        "ds_read_b64_tr_b16 v[60:61], v157 offset:0\n\t"
        "v_or_b32 v158, v184, v158\n\t"
        "v_mfma_f32_16x16x32_bf16 v[24:27], v[196:199], v[92:95], v[24:27]\n\t"
        "ds_read_b64_tr_b16 v[62:63], v158 offset:0\n\t"
        "ds_read_b128 v[108:111], v129 offset:16448\n\t"
        "v_xor_b32 v159, 32, v157\n\t"
        "v_xor_b32 v152, 32, v158\n\t"
        "v_mfma_f32_16x16x32_bf16 v[28:31], v[200:203], v[92:95], v[28:31]\n\t"
        "ds_read_b64_tr_b16 v[64:65], v159 offset:0\n\t"
        "ds_read_b64_tr_b16 v[254:255], v133 offset:56064\n\t"
        "v_perm_b32 v164, v162, v162, v168\n\t"
        "v_mfma_f32_16x16x32_bf16 v[32:35], v[204:207], v[92:95], v[32:35]\n\t"
        "ds_read_b64_tr_b16 v[66:67], v152 offset:0\n\t"
        "v_perm_b32 v165, v162, v162, v169\n\t"
        "v_mfma_f32_16x16x32_bf16 v[12:15], v[208:211], v[96:99], v[12:15]\n\t"
        "ds_read_b64_tr_b16 v[68:69], v157 offset:64\n\t"
        "v_perm_b32 v166, v162, v162, v170\n\t"
        ".LpY1%=:\n\t"
        "s_cbranch_execz .LrY%=\n\t"
        "v_mfma_f32_16x16x32_bf16 v[16:19], v[212:215], v[96:99], v[16:19]\n\t"
        "ds_read_b64_tr_b16 v[70:71], v158 offset:64\n\t"
        "v_perm_b32 v167, v162, v162, v171\n\t"
        "v_mfma_f32_16x16x32_bf16 v[20:23], v[216:219], v[96:99], v[20:23]\n\t"
        "ds_read_b64_tr_b16 v[72:73], v159 offset:64\n\t"
        "v_mfma_f32_16x16x32_bf16 v[24:27], a[252:255], v[96:99], v[24:27]\n\t"
        "ds_read_b64_tr_b16 v[74:75], v152 offset:64\n\t"
        "v_mfma_f32_16x16x32_bf16 v[28:31], v[188:191], v[96:99], v[28:31]\n\t"
        "ds_read_b64_tr_b16 v[76:77], v157 offset:128\n\t"
        "v_lshlrev_b32_sdwa v157, 5, v161 dst_sel:DWORD dst_unused:UNUSED_PAD src0_sel:DWORD src1_sel:WORD_0\n\t"
        "v_or_b32 v157, v184, v157\n\t"
        "v_mfma_f32_16x16x32_bf16 v[32:35], v[192:195], v[96:99], v[32:35]\n\t"
        "ds_read_b64_tr_b16 v[78:79], v158 offset:128\n\t"
        "v_lshlrev_b32_sdwa v158, 5, v161 dst_sel:DWORD dst_unused:UNUSED_PAD src0_sel:DWORD src1_sel:WORD_1\n\t"
        "v_or_b32 v158, v184, v158\n\t"
        "v_mfma_f32_16x16x32_bf16 v[36:39], v[196:199], v[96:99], v[36:39]\n\t"
        "ds_read_b64_tr_b16 v[80:81], v159 offset:128\n\t"
        "v_xor_b32 v159, 32, v157\n\t"
        "s_waitcnt lgkmcnt(10)\n\t"
        "v_mfma_f32_16x16x32_bf16 v[48:51], v[60:63], v[164:167], 0\n\t"
        "ds_read_b64_tr_b16 v[82:83], v152 offset:128\n\t"
        "v_xor_b32 v152, 32, v158\n\t"
        "v_mfma_f32_16x16x32_bf16 v[40:43], v[200:203], v[96:99], v[40:43]\n\t"
        "ds_read_b64_tr_b16 v[60:61], v157 offset:0\n\t"
        "s_waitcnt lgkmcnt(9)\n\t"
        "v_mfma_f32_16x16x32_bf16 v[52:55], v[64:67], v[164:167], 0\n\t"
        "ds_read_b64_tr_b16 v[62:63], v158 offset:0\n\t"
        "v_mfma_f32_16x16x32_bf16 v[44:47], v[204:207], v[96:99], v[44:47]\n\t"
        "ds_read_b128 v[112:115], v130 offset:8320\n\t"
        "s_waitcnt lgkmcnt(9)\n\t"
        "v_mfma_f32_16x16x32_bf16 v[56:59], v[68:71], v[164:167], 0\n\t"
        "ds_read_b64_tr_b16 v[64:65], v159 offset:0\n\t"
        "v_mfma_f32_16x16x32_bf16 v[0:3], v[232:235], v[100:103], v[0:3]\n\t"
        "ds_read_b64_tr_b16 v[66:67], v152 offset:0\n\t"
        "s_waitcnt lgkmcnt(9)\n\t"
        "v_mfma_f32_16x16x32_bf16 v[116:119], v[72:75], v[164:167], 0\n\t"
        "ds_read_b64_tr_b16 v[68:69], v157 offset:64\n\t"
        "v_mfma_f32_16x16x32_bf16 v[4:7], v[236:239], v[100:103], v[4:7]\n\t"
        "ds_read_b64_tr_b16 v[70:71], v158 offset:64\n\t"
        "s_waitcnt lgkmcnt(9)\n\t"
        "v_mfma_f32_16x16x32_bf16 v[120:123], v[76:79], v[164:167], 0\n\t"
        "ds_read_b64_tr_b16 v[72:73], v159 offset:64\n\t"
        "v_mfma_f32_16x16x32_bf16 v[8:11], v[240:243], v[100:103], v[8:11]\n\t"
        "ds_read_b64_tr_b16 v[74:75], v152 offset:64\n\t"
        "s_waitcnt lgkmcnt(9)\n\t"
        "v_mfma_f32_16x16x32_bf16 v[124:127], v[80:83], v[164:167], 0\n\t"
        "ds_read_b64_tr_b16 v[76:77], v157 offset:128\n\t"
        "v_perm_b32 v164, v163, v163, v168\n\t"
        "v_perm_b32 v165, v163, v163, v169\n\t"
        "v_mfma_f32_16x16x32_bf16 v[0:3], v[220:223], v[104:107], v[0:3]\n\t"
        "ds_read_b64_tr_b16 v[78:79], v158 offset:128\n\t"
        "v_perm_b32 v166, v163, v163, v170\n\t"
        "v_perm_b32 v167, v163, v163, v171\n\t"
        "v_mfma_f32_16x16x32_bf16 v[4:7], v[224:227], v[104:107], v[4:7]\n\t"
        "ds_read_b64_tr_b16 v[80:81], v159 offset:128\n\t"
        "s_waitcnt lgkmcnt(9)\n\t"
        "v_mfma_f32_16x16x32_bf16 v[48:51], v[60:63], v[164:167], v[48:51]\n\t"
        "ds_read_b64_tr_b16 v[82:83], v152 offset:128\n\t"
        "v_mfma_f32_16x16x32_bf16 v[8:11], v[228:231], v[104:107], v[8:11]\n\t"
        "ds_read_b128 v[160:163], v185 offset:768\n\t"
        "s_waitcnt lgkmcnt(9)\n\t"
        "v_mfma_f32_16x16x32_bf16 v[52:55], v[64:67], v[164:167], v[52:55]\n\t"
        "ds_read_b128 v[84:87], v130 offset:12416\n\t"
        "v_mfma_f32_16x16x32_bf16 v[12:15], v[232:235], v[104:107], v[12:15]\n\t"
        "s_waitcnt lgkmcnt(8)\n\t"
        "v_mfma_f32_16x16x32_bf16 v[56:59], v[68:71], v[164:167], v[56:59]\n\t"
        "v_cvt_pk_bf16_f32 v48, v48, v49\n\t"
        "v_cvt_pk_bf16_f32 v49, v50, v51\n\t"
        "v_mfma_f32_16x16x32_bf16 v[16:19], v[236:239], v[104:107], v[16:19]\n\t"
        "ds_write_b64 v186, v[48:49] offset:24576\n\t"
        "s_waitcnt lgkmcnt(7)\n\t"
        "v_mfma_f32_16x16x32_bf16 v[116:119], v[72:75], v[164:167], v[116:119]\n\t"
        "v_cvt_pk_bf16_f32 v52, v52, v53\n\t"
        "v_cvt_pk_bf16_f32 v53, v54, v55\n\t"
        "v_mfma_f32_16x16x32_bf16 v[20:23], v[240:243], v[104:107], v[20:23]\n\t"
        "ds_write_b64 v154, v[52:53] offset:24576\n\t"
        "s_waitcnt lgkmcnt(6)\n\t"
        "v_mfma_f32_16x16x32_bf16 v[120:123], v[76:79], v[164:167], v[120:123]\n\t"
        "v_cvt_pk_bf16_f32 v56, v56, v57\n\t"
        "v_cvt_pk_bf16_f32 v57, v58, v59\n\t"
        "v_mfma_f32_16x16x32_bf16 v[24:27], a[168:171], v[108:111], v[24:27]\n\t"
        "ds_write_b64 v186, v[56:57] offset:24640\n\t"
        "s_waitcnt lgkmcnt(5)\n\t"
        "v_mfma_f32_16x16x32_bf16 v[124:127], v[80:83], v[164:167], v[124:127]\n\t"
        "v_cvt_pk_bf16_f32 v116, v116, v117\n\t"
        "v_cvt_pk_bf16_f32 v117, v118, v119\n\t"
        "s_waitcnt lgkmcnt(4)\n\t"
        "v_lshlrev_b32_sdwa v157, 5, v160 dst_sel:DWORD dst_unused:UNUSED_PAD src0_sel:DWORD src1_sel:WORD_0\n\t"
        "v_mfma_f32_16x16x32_bf16 v[28:31], a[172:175], v[108:111], v[28:31]\n\t"
        "ds_write_b64 v154, v[116:117] offset:24640\n\t"
        "v_mfma_f32_16x16x32_bf16 v[32:35], a[176:179], v[108:111], v[32:35]\n\t"
        "v_cvt_pk_bf16_f32 v120, v120, v121\n\t"
        "v_cvt_pk_bf16_f32 v121, v122, v123\n\t"
        "v_lshlrev_b32_sdwa v158, 5, v160 dst_sel:DWORD dst_unused:UNUSED_PAD src0_sel:DWORD src1_sel:WORD_1\n\t"
        "v_mfma_f32_16x16x32_bf16 v[36:39], a[144:147], v[108:111], v[36:39]\n\t"
        "ds_write_b64 v186, v[120:121] offset:24704\n\t"
        "v_mfma_f32_16x16x32_bf16 v[40:43], a[148:151], v[108:111], v[40:43]\n\t"
        "ds_read_b128 v[88:91], v129 offset:16512\n\t"
        "v_cvt_pk_bf16_f32 v124, v124, v125\n\t"
        "v_cvt_pk_bf16_f32 v125, v126, v127\n\t"
        "v_mfma_f32_16x16x32_bf16 v[44:47], a[152:155], v[108:111], v[44:47]\n\t"
        "ds_write_b64 v154, v[124:125] offset:24704\n\t"
        "v_mfma_f32_16x16x32_bf16 v[0:3], v[244:247], v[112:115], v[0:3]\n\t"
        "v_or_b32 v157, v184, v157\n\t"
        "v_or_b32 v158, v184, v158\n\t"
        "v_xor_b32 v159, 32, v157\n\t"
        "v_mfma_f32_16x16x32_bf16 v[4:7], v[248:251], v[112:115], v[4:7]\n\t"
        "ds_read_b64_tr_b16 v[60:61], v157 offset:0\n\t"
        "v_xor_b32 v152, 32, v158\n\t"
        "v_mfma_f32_16x16x32_bf16 v[8:11], v[252:255], v[112:115], v[8:11]\n\t"
        "ds_read_b64_tr_b16 v[62:63], v158 offset:0\n\t"
        "v_perm_b32 v164, v162, v162, v168\n\t"
        "v_mfma_f32_16x16x32_bf16 v[12:15], v[220:223], v[112:115], v[12:15]\n\t"
        "ds_read_b64_tr_b16 v[64:65], v159 offset:0\n\t"
        "v_perm_b32 v165, v162, v162, v169\n\t"
        "v_mfma_f32_16x16x32_bf16 v[16:19], v[224:227], v[112:115], v[16:19]\n\t"
        "ds_read_b64_tr_b16 v[66:67], v152 offset:0\n\t"
        "v_perm_b32 v166, v162, v162, v170\n\t"
        "v_perm_b32 v167, v162, v162, v171\n\t"
        "v_mfma_f32_16x16x32_bf16 v[20:23], v[228:231], v[112:115], v[20:23]\n\t"
        "ds_read_b64_tr_b16 v[68:69], v157 offset:64\n\t"
        "v_mfma_f32_16x16x32_bf16 v[24:27], v[232:235], v[112:115], v[24:27]\n\t"
        "ds_read_b64_tr_b16 v[70:71], v158 offset:64\n\t"
        "ds_read_b128 v[92:95], v130 offset:16384\n\t"
        "v_mfma_f32_16x16x32_bf16 v[28:31], v[236:239], v[112:115], v[28:31]\n\t"
        "ds_read_b64_tr_b16 v[72:73], v159 offset:64\n\t"
        "v_mfma_f32_16x16x32_bf16 v[32:35], v[240:243], v[112:115], v[32:35]\n\t"
        "ds_read_b64_tr_b16 v[74:75], v152 offset:64\n\t"
        "s_waitcnt lgkmcnt(9)\n\t"
        "v_mfma_f32_16x16x32_bf16 v[12:15], v[244:247], v[84:87], v[12:15]\n\t"
        "ds_read_b64_tr_b16 v[76:77], v157 offset:128\n\t"
        "v_lshlrev_b32_sdwa v157, 5, v161 dst_sel:DWORD dst_unused:UNUSED_PAD src0_sel:DWORD src1_sel:WORD_0\n\t"
        "v_or_b32 v157, v184, v157\n\t"
        "v_mfma_f32_16x16x32_bf16 v[16:19], v[248:251], v[84:87], v[16:19]\n\t"
        "ds_read_b64_tr_b16 v[78:79], v158 offset:128\n\t"
        "v_lshlrev_b32_sdwa v158, 5, v161 dst_sel:DWORD dst_unused:UNUSED_PAD src0_sel:DWORD src1_sel:WORD_1\n\t"
        "v_mfma_f32_16x16x32_bf16 v[20:23], v[252:255], v[84:87], v[20:23]\n\t"
        "ds_read_b64_tr_b16 v[80:81], v159 offset:128\n\t"
        "v_or_b32 v158, v184, v158\n\t"
        "v_xor_b32 v159, 32, v157\n\t"
        "s_waitcnt lgkmcnt(10)\n\t"
        "v_mfma_f32_16x16x32_bf16 v[48:51], v[60:63], v[164:167], 0\n\t"
        "ds_read_b64_tr_b16 v[82:83], v152 offset:128\n\t"
        "v_xor_b32 v152, 32, v158\n\t"
        "v_mfma_f32_16x16x32_bf16 v[24:27], v[220:223], v[84:87], v[24:27]\n\t"
        "ds_read_b64_tr_b16 v[60:61], v157 offset:0\n\t"
        "s_waitcnt lgkmcnt(10)\n\t"
        "v_mfma_f32_16x16x32_bf16 v[52:55], v[64:67], v[164:167], 0\n\t"
        "ds_read_b64_tr_b16 v[62:63], v158 offset:0\n\t"
        "v_mfma_f32_16x16x32_bf16 v[28:31], v[224:227], v[84:87], v[28:31]\n\t"
        "ds_read_b64_tr_b16 v[64:65], v159 offset:0\n\t"
        "ds_read_b128 v[96:99], v130 offset:16448\n\t"
        "s_waitcnt lgkmcnt(10)\n\t"
        "v_mfma_f32_16x16x32_bf16 v[56:59], v[68:71], v[164:167], 0\n\t"
        "ds_read_b64_tr_b16 v[66:67], v152 offset:0\n\t"
        "v_mfma_f32_16x16x32_bf16 v[32:35], v[228:231], v[84:87], v[32:35]\n\t"
        "ds_read_b64_tr_b16 v[68:69], v157 offset:64\n\t"
        "s_waitcnt lgkmcnt(10)\n\t"
        "v_mfma_f32_16x16x32_bf16 v[116:119], v[72:75], v[164:167], 0\n\t"
        "ds_read_b64_tr_b16 v[70:71], v158 offset:64\n\t"
        "v_mfma_f32_16x16x32_bf16 v[36:39], v[232:235], v[84:87], v[36:39]\n\t"
        "ds_read_b64_tr_b16 v[72:73], v159 offset:64\n\t"
        "s_waitcnt lgkmcnt(10)\n\t"
        "v_mfma_f32_16x16x32_bf16 v[120:123], v[76:79], v[164:167], 0\n\t"
        "ds_read_b64_tr_b16 v[74:75], v152 offset:64\n\t"
        "v_mfma_f32_16x16x32_bf16 v[40:43], v[236:239], v[84:87], v[40:43]\n\t"
        "ds_read_b64_tr_b16 v[76:77], v157 offset:128\n\t"
        "s_waitcnt lgkmcnt(10)\n\t"
        "v_mfma_f32_16x16x32_bf16 v[124:127], v[80:83], v[164:167], 0\n\t"
        "ds_read_b64_tr_b16 v[78:79], v158 offset:128\n\t"
        "v_perm_b32 v164, v163, v163, v168\n\t"
        "v_perm_b32 v165, v163, v163, v169\n\t"
        "v_mfma_f32_16x16x32_bf16 v[44:47], v[240:243], v[84:87], v[44:47]\n\t"
        "ds_read_b64_tr_b16 v[80:81], v159 offset:128\n\t"
        "v_perm_b32 v166, v163, v163, v170\n\t"
        "v_perm_b32 v167, v163, v163, v171\n\t"
        "v_mfma_f32_16x16x32_bf16 v[24:27], a[204:207], v[88:91], v[24:27]\n\t"
        "ds_read_b64_tr_b16 v[82:83], v152 offset:128\n\t"
        "s_waitcnt lgkmcnt(10)\n\t"
        "v_mfma_f32_16x16x32_bf16 v[48:51], v[60:63], v[164:167], v[48:51]\n\t"
        "ds_read_b128 v[100:103], v130 offset:16512\n\t"
        "v_mfma_f32_16x16x32_bf16 v[28:31], a[208:211], v[88:91], v[28:31]\n\t"
        "s_waitcnt lgkmcnt(9)\n\t"
        "v_mfma_f32_16x16x32_bf16 v[52:55], v[64:67], v[164:167], v[52:55]\n\t"
        "v_mfma_f32_16x16x32_bf16 v[32:35], a[212:215], v[88:91], v[32:35]\n\t"
        "s_waitcnt lgkmcnt(7)\n\t"
        "v_mfma_f32_16x16x32_bf16 v[56:59], v[68:71], v[164:167], v[56:59]\n\t"
        "s_nop 0\n\t"
        "v_cvt_pk_bf16_f32 v48, v48, v49\n\t"
        "v_cvt_pk_bf16_f32 v49, v50, v51\n\t"
        "v_mfma_f32_16x16x32_bf16 v[36:39], a[180:183], v[88:91], v[36:39]\n\t"
        "ds_write_b64 v186, v[48:49] offset:36864\n\t"
        "s_waitcnt lgkmcnt(5)\n\t"
        "v_mfma_f32_16x16x32_bf16 v[116:119], v[72:75], v[164:167], v[116:119]\n\t"
        "ds_read_b128 v[104:107], v129 offset:20480\n\t"
        "v_cvt_pk_bf16_f32 v52, v52, v53\n\t"
        "v_cvt_pk_bf16_f32 v53, v54, v55\n\t"
        "v_mfma_f32_16x16x32_bf16 v[40:43], a[184:187], v[88:91], v[40:43]\n\t"
        "ds_write_b64 v154, v[52:53] offset:36864\n\t"
        "s_waitcnt lgkmcnt(5)\n\t"
        "v_mfma_f32_16x16x32_bf16 v[120:123], v[76:79], v[164:167], v[120:123]\n\t"
        "v_cvt_pk_bf16_f32 v56, v56, v57\n\t"
        "v_cvt_pk_bf16_f32 v57, v58, v59\n\t"
        "v_mfma_f32_16x16x32_bf16 v[44:47], a[188:191], v[88:91], v[44:47]\n\t"
        "ds_write_b64 v186, v[56:57] offset:36928\n\t"
        "s_waitcnt lgkmcnt(4)\n\t"
        "v_mfma_f32_16x16x32_bf16 v[124:127], v[80:83], v[164:167], v[124:127]\n\t"
        "v_cvt_pk_bf16_f32 v116, v116, v117\n\t"
        "v_cvt_pk_bf16_f32 v117, v118, v119\n\t"
        "v_mfma_f32_16x16x32_bf16 v[24:27], a[240:243], v[92:95], v[24:27]\n\t"
        "ds_write_b64 v154, v[116:117] offset:36928\n\t"
        "v_mfma_f32_16x16x32_bf16 v[28:31], a[244:247], v[92:95], v[28:31]\n\t"
        "ds_read_b128 v[108:111], v129 offset:20544\n\t"
        "v_cvt_pk_bf16_f32 v120, v120, v121\n\t"
        "v_cvt_pk_bf16_f32 v121, v122, v123\n\t"
        "v_mfma_f32_16x16x32_bf16 v[32:35], a[248:251], v[92:95], v[32:35]\n\t"
        "ds_write_b64 v186, v[120:121] offset:36992\n\t"
        "v_mfma_f32_16x16x32_bf16 v[36:39], a[216:219], v[92:95], v[36:39]\n\t"
        "ds_read_b128 v[112:115], v129 offset:20608\n\t"
        "v_cvt_pk_bf16_f32 v124, v124, v125\n\t"
        "v_cvt_pk_bf16_f32 v125, v126, v127\n\t"
        "v_mfma_f32_16x16x32_bf16 v[40:43], a[220:223], v[92:95], v[40:43]\n\t"
        "ds_write_b64 v154, v[124:125] offset:36992\n\t"
        "v_mfma_f32_16x16x32_bf16 v[44:47], a[224:227], v[92:95], v[44:47]\n\t"
        "s_cmp_gt_u32 s61, 64\n\t"
        "s_cbranch_scc0 .Lx70%=\n\t"
        "v_bfe_u32 v52, v185, 4, 4\n\t"
        "v_add_u32 v48, 65, v52\n\t"
        "s_sub_u32 s8, s37, 12288\n\t"
        "v_mov_b32 v51, s62\n\t"
        "v_lshlrev_b32 v53, 3, v52\n\t"
        "v_mul_u32_u24 v52, 192, v52\n\t"
        "v_and_or_b32 v52, v53, 48, v52\n\t"
        "v_add_u32 v49, s8, v52\n\t"
        "v_subrev_u32 v186, 12288, v186\n\t"
        "v_xor_b32 v154, 32, v186\n\t"
        "v_min_u32 v76, s61, v48\n\t"
        "v_lshl_add_u32 v76, v76, 3, s60\n\t"
        "ds_read_b64 v[60:61], v76\n\t"
        "v_cmp_ge_u32 s[66:67], s61, v48\n\t"
        "s_mov_b32 s46, 0x07060302\n\t"
        "s_waitcnt lgkmcnt(0)\n\t"
        "v_and_b32 v76, 0xfffff800, v61\n\t"
        "ds_read_b128 v[62:65], v76 offset:1536\n\t"
        "v_lshlrev_b32 v70, 16, v60\n\t"
        "v_and_b32 v71, 0xffff0000, v60\n\t"
        "v_med3_f32 v70, v70, 0, s63\n\t"
        "v_med3_f32 v71, v71, 0, s63\n\t"
        "s_mov_b64 exec, s[66:67]\n\t"
        "ds_write_b32 v61, v49\n\t"
        "s_mov_b64 exec, -1\n\t"
        "v_perm_b32 v60, v71, v70, s46\n\t"
        "v_fract_f32 v72, v70\n\t"
        "v_fract_f32 v73, v71\n\t"
        "v_cvt_u32_f32 v74, v70\n\t"
        "v_cvt_u32_f32 v75, v71\n\t"
        "v_cmp_neq_f32 s[64:65], 0, v72\n\t"
        "v_cmp_neq_f32 s[44:45], 0, v73\n\t"
        "v_sub_f32 v76, 1.0, v72\n\t"
        "v_sub_f32 v77, 1.0, v73\n\t"
        "v_cvt_pk_bf16_f32 v77, v76, v77\n\t"
        "v_lshlrev_b32 v76, 16, v77\n\t"
        "v_and_b32 v77, 0xffff0000, v77\n\t"
        "v_mul_f32 v78, v76, v77\n\t"
        "v_mul_f32 v76, v76, v73\n\t"
        "v_mul_f32 v77, v72, v77\n\t"
        "v_mul_f32 v79, v72, v73\n\t"
        "v_cndmask_b32 v76, v77, v76, s[44:45]\n\t"
        "v_cndmask_b32 v77, 0, v77, s[44:45]\n\t"
        "v_cvt_pk_bf16_f32 v162, v78, v76\n\t"
        "v_cvt_pk_bf16_f32 v163, v77, v79\n\t"
        "s_waitcnt lgkmcnt(1)\n\t"
        "v_pk_max_u16 v76, v60, v62\n\t"
        "v_pk_min_u16 v76, v76, v63\n\t"
        "v_cmp_ne_u32 vcc, v76, v60\n\t"
        "v_sub_u32 v76, v75, v65\n\t"
        "v_mad_u32_u24 v77, v75, 6, v64\n\t"
        "v_mad_u32_u24 v77, v74, s62, v77\n\t"
        "s_and_b64 vcc, vcc, s[66:67]\n\t"
        "s_or_b64 s[56:57], s[56:57], vcc\n\t"
        "v_bfe_u32 v78, v76, 2, 1\n\t"
        "v_or_b32 v78, v78, v77\n\t"
        "v_add_u32 v76, 1, v76\n\t"
        "v_bfe_u32 v76, v76, 2, 1\n\t"
        "v_add3_u32 v76, v77, v76, 6\n\t"
        "v_cndmask_b32 v76, v78, v76, s[44:45]\n\t"
        "v_cndmask_b32 v77, 0, v51, s[64:65]\n\t"
        "v_add_u32 v79, v78, v77\n\t"
        "v_add_u32 v80, v76, v77\n\t"
        "v_cndmask_b32 v76, v79, v76, s[44:45]\n\t"
        "v_lshl_or_b32 v160, v76, 16, v78\n\t"
        "v_lshl_or_b32 v161, v80, 16, v79\n\t"
        "v_lshlrev_b32_sdwa v157, 5, v160 dst_sel:DWORD dst_unused:UNUSED_PAD src0_sel:DWORD src1_sel:WORD_0\n\t"
        "v_lshlrev_b32_sdwa v158, 5, v160 dst_sel:DWORD dst_unused:UNUSED_PAD src0_sel:DWORD src1_sel:WORD_1\n\t"
        "v_or_b32 v157, v184, v157\n\t"
        "v_or_b32 v158, v184, v158\n\t"
        "v_xor_b32 v159, 32, v157\n\t"
        "v_xor_b32 v152, 32, v158\n\t"
        "ds_read_b64_tr_b16 v[60:61], v157 offset:0\n\t"
        "ds_read_b64_tr_b16 v[62:63], v158 offset:0\n\t"
        "ds_read_b64_tr_b16 v[64:65], v159 offset:0\n\t"
        "ds_read_b64_tr_b16 v[66:67], v152 offset:0\n\t"
        "ds_read_b64_tr_b16 v[68:69], v157 offset:64\n\t"
        "ds_read_b64_tr_b16 v[70:71], v158 offset:64\n\t"
        "ds_read_b64_tr_b16 v[72:73], v159 offset:64\n\t"
        "ds_read_b64_tr_b16 v[74:75], v152 offset:64\n\t"
        "ds_read_b64_tr_b16 v[76:77], v157 offset:128\n\t"
        "ds_read_b64_tr_b16 v[78:79], v158 offset:128\n\t"
        "ds_read_b64_tr_b16 v[80:81], v159 offset:128\n\t"
        "ds_read_b64_tr_b16 v[82:83], v152 offset:128\n\t"
        "v_perm_b32 v164, v162, v162, v168\n\t"
        "v_perm_b32 v165, v162, v162, v169\n\t"
        "v_perm_b32 v166, v162, v162, v170\n\t"
        "v_perm_b32 v167, v162, v162, v171\n\t"
        "s_waitcnt lgkmcnt(10)\n\t"
        "s_nop 0\n\t"
        "v_mfma_f32_16x16x32_bf16 v[48:51], v[60:63], v[164:167], 0\n\t"
        "s_waitcnt lgkmcnt(8)\n\t"
        "v_mfma_f32_16x16x32_bf16 v[52:55], v[64:67], v[164:167], 0\n\t"
        "s_waitcnt lgkmcnt(6)\n\t"
        "v_mfma_f32_16x16x32_bf16 v[56:59], v[68:71], v[164:167], 0\n\t"
        "s_waitcnt lgkmcnt(4)\n\t"
        "v_mfma_f32_16x16x32_bf16 v[116:119], v[72:75], v[164:167], 0\n\t"
        "s_waitcnt lgkmcnt(2)\n\t"
        "v_mfma_f32_16x16x32_bf16 v[120:123], v[76:79], v[164:167], 0\n\t"
        "s_waitcnt lgkmcnt(0)\n\t"
        "v_mfma_f32_16x16x32_bf16 v[124:127], v[80:83], v[164:167], 0\n\t"
        "v_lshlrev_b32_sdwa v157, 5, v161 dst_sel:DWORD dst_unused:UNUSED_PAD src0_sel:DWORD src1_sel:WORD_0\n\t"
        "v_lshlrev_b32_sdwa v158, 5, v161 dst_sel:DWORD dst_unused:UNUSED_PAD src0_sel:DWORD src1_sel:WORD_1\n\t"
        "v_or_b32 v157, v184, v157\n\t"
        "v_or_b32 v158, v184, v158\n\t"
        "v_xor_b32 v159, 32, v157\n\t"
        "v_xor_b32 v152, 32, v158\n\t"
        "ds_read_b64_tr_b16 v[60:61], v157 offset:0\n\t"
        "ds_read_b64_tr_b16 v[62:63], v158 offset:0\n\t"
        "ds_read_b64_tr_b16 v[64:65], v159 offset:0\n\t"
        "ds_read_b64_tr_b16 v[66:67], v152 offset:0\n\t"
        "ds_read_b64_tr_b16 v[68:69], v157 offset:64\n\t"
        "ds_read_b64_tr_b16 v[70:71], v158 offset:64\n\t"
        "ds_read_b64_tr_b16 v[72:73], v159 offset:64\n\t"
        "ds_read_b64_tr_b16 v[74:75], v152 offset:64\n\t"
        "ds_read_b64_tr_b16 v[76:77], v157 offset:128\n\t"
        "ds_read_b64_tr_b16 v[78:79], v158 offset:128\n\t"
        "ds_read_b64_tr_b16 v[80:81], v159 offset:128\n\t"
        "ds_read_b64_tr_b16 v[82:83], v152 offset:128\n\t"
        "v_perm_b32 v164, v163, v163, v168\n\t"
        "v_perm_b32 v165, v163, v163, v169\n\t"
        "v_perm_b32 v166, v163, v163, v170\n\t"
        "v_perm_b32 v167, v163, v163, v171\n\t"
        "s_waitcnt lgkmcnt(10)\n\t"
        "s_nop 0\n\t"
        "v_mfma_f32_16x16x32_bf16 v[48:51], v[60:63], v[164:167], v[48:51]\n\t"
        "s_waitcnt lgkmcnt(8)\n\t"
        "v_mfma_f32_16x16x32_bf16 v[52:55], v[64:67], v[164:167], v[52:55]\n\t"
        "s_waitcnt lgkmcnt(6)\n\t"
        "v_mfma_f32_16x16x32_bf16 v[56:59], v[68:71], v[164:167], v[56:59]\n\t"
        "s_waitcnt lgkmcnt(4)\n\t"
        "v_mfma_f32_16x16x32_bf16 v[116:119], v[72:75], v[164:167], v[116:119]\n\t"
        "s_waitcnt lgkmcnt(2)\n\t"
        "v_mfma_f32_16x16x32_bf16 v[120:123], v[76:79], v[164:167], v[120:123]\n\t"
        "s_waitcnt lgkmcnt(0)\n\t"
        "v_mfma_f32_16x16x32_bf16 v[124:127], v[80:83], v[164:167], v[124:127]\n\t"
        "v_cvt_pk_bf16_f32 v48, v48, v49\n\t"
        "v_cvt_pk_bf16_f32 v49, v50, v51\n\t"
        "v_cvt_pk_bf16_f32 v52, v52, v53\n\t"
        "v_cvt_pk_bf16_f32 v53, v54, v55\n\t"
        "v_cvt_pk_bf16_f32 v56, v56, v57\n\t"
        "v_cvt_pk_bf16_f32 v57, v58, v59\n\t"
        "v_cvt_pk_bf16_f32 v116, v116, v117\n\t"
        "v_cvt_pk_bf16_f32 v117, v118, v119\n\t"
        "v_cvt_pk_bf16_f32 v120, v120, v121\n\t"
        "v_cvt_pk_bf16_f32 v121, v122, v123\n\t"
        "v_cvt_pk_bf16_f32 v124, v124, v125\n\t"
        "v_cvt_pk_bf16_f32 v125, v126, v127\n\t"
        "ds_write_b64 v186, v[48:49] offset:0\n\t"
        "ds_write_b64 v154, v[52:53] offset:0\n\t"
        "ds_write_b64 v186, v[56:57] offset:64\n\t"
        "ds_write_b64 v154, v[116:117] offset:64\n\t"
        "ds_write_b64 v186, v[120:121] offset:128\n\t"
        "ds_write_b64 v154, v[124:125] offset:128\n\t"
        "v_add_u32 v186, 12288, v186\n\t"
        "v_xor_b32 v154, 32, v186\n\t"
        ".Lx70%=:\n\t"
        "v_mfma_f32_16x16x32_bf16 v[24:27], v[208:211], v[96:99], v[24:27]\n\t"
        "s_cmp_gt_u32 s3, 64\n\t"
        "s_cselect_b32 s8, 2, 0\n\t"
        "v_mfma_f32_16x16x32_bf16 v[28:31], v[212:215], v[96:99], v[28:31]\n\t"
        "s_cmp_lg_u64 s[56:57], 0\n\t"
        "s_addc_u32 s8, s8, s6\n\t"
        "v_mov_b32 v151, s8\n\t"
        "v_mfma_f32_16x16x32_bf16 v[32:35], v[216:219], v[96:99], v[32:35]\n\t"
        "s_mov_b64 exec, 1\n\t"
        "ds_write_b32 v153, v151\n\t"
        "s_mov_b64 exec, -1\n\t"
        "v_mfma_f32_16x16x32_bf16 v[36:39], a[252:255], v[96:99], v[36:39]\n\t"
        "ds_read_b128 v[84:87], v130 offset:20480\n\t"
        "ds_read_b128 v[88:91], v130 offset:20544\n\t"
        "v_mfma_f32_16x16x32_bf16 v[40:43], v[188:191], v[96:99], v[40:43]\n\t"
        "v_mfma_f32_16x16x32_bf16 v[44:47], v[192:195], v[96:99], v[44:47]\n\t"
        "v_mfma_f32_16x16x32_bf16 v[24:27], v[244:247], v[100:103], v[24:27]\n\t"
        "ds_read_b128 v[92:95], v130 offset:20608\n\t"
        "v_mfma_f32_16x16x32_bf16 v[28:31], v[248:251], v[100:103], v[28:31]\n\t"
        "v_mfma_f32_16x16x32_bf16 v[32:35], v[252:255], v[100:103], v[32:35]\n\t"
        "s_waitcnt lgkmcnt(3)\n\t"
        "s_barrier\n\t"
        "v_mfma_f32_16x16x32_bf16 v[36:39], v[220:223], v[100:103], v[36:39]\n\t"
        "ds_read_b128 v[124:127], v182 offset:0\n\t"
        "v_mfma_f32_16x16x32_bf16 v[40:43], v[224:227], v[100:103], v[40:43]\n\t"
        "ds_read_b128 v[132:135], v187 offset:4096\n\t"
        "v_mfma_f32_16x16x32_bf16 v[44:47], v[228:231], v[100:103], v[44:47]\n\t"
        "ds_read_b128 v[136:139], v187 offset:4112\n\t"
        "v_mfma_f32_16x16x32_bf16 v[36:39], a[132:135], v[104:107], v[36:39]\n\t"
        "ds_read_b32 v140, v187 offset:4128\n\t"
        "v_mfma_f32_16x16x32_bf16 v[40:43], a[136:139], v[104:107], v[40:43]\n\t"
        "v_mfma_f32_16x16x32_bf16 v[44:47], a[140:143], v[104:107], v[44:47]\n\t"
        "v_mfma_f32_16x16x32_bf16 v[36:39], a[168:171], v[108:111], v[36:39]\n\t"
        "v_mfma_f32_16x16x32_bf16 v[40:43], a[172:175], v[108:111], v[40:43]\n\t"
        "v_mfma_f32_16x16x32_bf16 v[44:47], a[176:179], v[108:111], v[44:47]\n\t"
        "v_mfma_f32_16x16x32_bf16 v[36:39], a[204:207], v[112:115], v[36:39]\n\t"
        "s_waitcnt lgkmcnt(3)\n\t"
        "v_or3_b32 v124, v124, v125, v126\n\t"
        "v_or_b32 v124, v124, v127\n\t"
        "v_mfma_f32_16x16x32_bf16 v[40:43], a[208:211], v[112:115], v[40:43]\n\t"
        "v_readfirstlane_b32 s6, v124\n\t"
        "v_mfma_f32_16x16x32_bf16 v[44:47], a[212:215], v[112:115], v[44:47]\n\t"
        "s_nop 0\n\t"
        "s_bitcmp1_b32 s6, 1\n\t"
        "s_cbranch_scc1 .Lx72%=\n\t"
        ".Lx73%=:\n\t"
        "s_waitcnt lgkmcnt(2)\n\t"
        "v_xor_b32 v132, v181, v132\n\t"
        "v_readlane_b32 s9, v155, 48\n\t"
        "v_mfma_f32_16x16x32_bf16 v[36:39], a[240:243], v[84:87], v[36:39]\n\t"
        "s_nop 0\n\t"
        "v_add_u32 v151, s9, v172\n\t"
        "v_max_u32 v151, v151, v132\n\t"
        "v_xor_b32 v133, v181, v133\n\t"
        "v_add_u32 v183, s9, v173\n\t"
        "v_mfma_f32_16x16x32_bf16 v[40:43], a[244:247], v[84:87], v[40:43]\n\t"
        "ds_read_b128 v[96:99], v151 offset:0\n\t"
        "v_max_u32 v183, v183, v133\n\t"
        "v_xor_b32 v134, v181, v134\n\t"
        "v_mfma_f32_16x16x32_bf16 v[44:47], a[248:251], v[84:87], v[44:47]\n\t"
        "ds_read_b128 v[100:103], v151 offset:64\n\t"
        "s_add_u32 s55, s41, 0\n\t"
        "v_mfma_f32_16x16x32_bf16 v[36:39], v[208:211], v[88:91], v[36:39]\n\t"
        "ds_read_b128 v[104:107], v151 offset:128\n\t"
        "v_add_u32 v151, s9, v174\n\t"
        "v_mfma_f32_16x16x32_bf16 v[40:43], v[212:215], v[88:91], v[40:43]\n\t"
        "ds_read_b128 v[108:111], v183 offset:0\n\t"
        "v_max_u32 v151, v151, v134\n\t"
        "v_xor_b32 v135, v181, v135\n\t"
        "v_mfma_f32_16x16x32_bf16 v[44:47], v[216:219], v[88:91], v[44:47]\n\t"
        "ds_read_b128 v[112:115], v183 offset:64\n\t"
        "v_cvt_pk_bf16_f32 v116, v0, v1\n\t"
        "v_mfma_f32_16x16x32_bf16 v[36:39], v[244:247], v[92:95], v[36:39]\n\t"
        "ds_read_b128 v[84:87], v183 offset:128\n\t"
        "v_add_u32 v183, s9, v175\n\t"
        "v_mfma_f32_16x16x32_bf16 v[40:43], v[248:251], v[92:95], v[40:43]\n\t"
        "ds_read_b128 v[88:91], v151 offset:0\n\t"
        "v_max_u32 v183, v183, v135\n\t"
        "v_mfma_f32_16x16x32_bf16 v[44:47], v[252:255], v[92:95], v[44:47]\n\t"
        "s_waitcnt lgkmcnt(7)\n\t"
        "v_xor_b32 v136, v181, v136\n\t"
        "v_cvt_pk_bf16_f32 v117, v2, v3\n\t"
        "s_waitcnt lgkmcnt(6)\n\t"
        "v_mfma_f32_16x16x32_bf16 v[72:75], a[12:15], v[96:99], 0\n\t"
        "ds_read_b128 v[92:95], v151 offset:64\n\t"
        "v_cvt_pk_bf16_f32 v118, v4, v5\n\t"
        "v_cvt_pk_bf16_f32 v119, v6, v7\n\t"
        "v_mfma_f32_16x16x32_bf16 v[76:79], a[16:19], v[96:99], 0\n\t"
        "v_cvt_pk_bf16_f32 v120, v8, v9\n\t"
        "v_cvt_pk_bf16_f32 v121, v10, v11\n\t"
        "v_mfma_f32_16x16x32_bf16 v[80:83], a[20:23], v[96:99], 0\n\t"
        "v_permlane16_swap_b32 v116, v118\n\t"
        "s_waitcnt lgkmcnt(6)\n\t"
        "v_mfma_f32_16x16x32_bf16 v[72:75], a[48:51], v[100:103], v[72:75]\n\t"
        "v_permlane16_swap_b32 v117, v119\n\t"
        "v_mfma_f32_16x16x32_bf16 v[76:79], a[52:55], v[100:103], v[76:79]\n\t"
        "buffer_store_dwordx4 v[116:119], v144, s[28:31], s55 offen sc1\n\t"
        "v_mfma_f32_16x16x32_bf16 v[80:83], a[56:59], v[100:103], v[80:83]\n\t"
        "ds_write_b64 v146, v[120:121] offset:0\n\t"
        "s_waitcnt lgkmcnt(3)\n\t"
        "v_mfma_f32_16x16x32_bf16 v[72:75], a[84:87], v[104:107], v[72:75]\n\t"
        "ds_read_b128 v[96:99], v151 offset:128\n\t"
        "v_add_u32 v151, s9, v176\n\t"
        "v_max_u32 v151, v151, v136\n\t"
        "v_mfma_f32_16x16x32_bf16 v[76:79], a[88:91], v[104:107], v[76:79]\n\t"
        "ds_read_b128 v[100:103], v183 offset:0\n\t"
        "v_xor_b32 v137, v181, v137\n\t"
        "v_mfma_f32_16x16x32_bf16 v[80:83], a[92:95], v[104:107], v[80:83]\n\t"
        "s_add_u32 s55, s41, 43008\n\t"
        "v_cvt_pk_bf16_f32 v116, v12, v13\n\t"
        "v_mfma_f32_16x16x32_bf16 v[72:75], a[120:123], v[108:111], v[72:75]\n\t"
        "ds_read_b128 v[104:107], v183 offset:64\n\t"
        "v_cvt_pk_bf16_f32 v117, v14, v15\n\t"
        "v_mfma_f32_16x16x32_bf16 v[76:79], a[124:127], v[108:111], v[76:79]\n\t"
        "v_cvt_pk_bf16_f32 v118, v16, v17\n\t"
        "v_cvt_pk_bf16_f32 v119, v18, v19\n\t"
        "v_cvt_pk_bf16_f32 v120, v20, v21\n\t"
        "v_mfma_f32_16x16x32_bf16 v[80:83], a[128:131], v[108:111], v[80:83]\n\t"
        "v_cvt_pk_bf16_f32 v121, v22, v23\n\t"
        "v_mfma_f32_16x16x32_bf16 v[72:75], a[156:159], v[112:115], v[72:75]\n\t"
        "v_permlane16_swap_b32 v116, v118\n\t"
        "v_mfma_f32_16x16x32_bf16 v[76:79], a[160:163], v[112:115], v[76:79]\n\t"
        "v_permlane16_swap_b32 v117, v119\n\t"
        "v_mfma_f32_16x16x32_bf16 v[80:83], a[164:167], v[112:115], v[80:83]\n\t"
        "buffer_store_dwordx4 v[116:119], v144, s[28:31], s55 offen sc1\n\t"
        "v_mfma_f32_16x16x32_bf16 v[72:75], a[192:195], v[84:87], v[72:75]\n\t"
        "ds_write_b64 v146, v[120:121] offset:2048\n\t"
        "v_mfma_f32_16x16x32_bf16 v[76:79], a[196:199], v[84:87], v[76:79]\n\t"
        "ds_read_b128 v[108:111], v183 offset:128\n\t"
        "v_add_u32 v183, s9, v177\n\t"
        "v_max_u32 v183, v183, v137\n\t"
        "v_mfma_f32_16x16x32_bf16 v[80:83], a[200:203], v[84:87], v[80:83]\n\t"
        "ds_read_b128 v[112:115], v151 offset:0\n\t"
        "v_xor_b32 v138, v181, v138\n\t"
        "s_waitcnt lgkmcnt(3)\n\t"
        "v_mfma_f32_16x16x32_bf16 v[72:75], a[228:231], v[88:91], v[72:75]\n\t"
        "ds_read_b128 v[84:87], v151 offset:64\n\t"
        "s_add_u32 s55, s41, 86016\n\t"
        "v_mfma_f32_16x16x32_bf16 v[76:79], a[232:235], v[88:91], v[76:79]\n\t"
        "v_cvt_pk_bf16_f32 v116, v24, v25\n\t"
        "v_cvt_pk_bf16_f32 v117, v26, v27\n\t"
        "v_mfma_f32_16x16x32_bf16 v[80:83], a[236:239], v[88:91], v[80:83]\n\t"
        "v_cvt_pk_bf16_f32 v118, v28, v29\n\t"
        "v_cvt_pk_bf16_f32 v119, v30, v31\n\t"
        "v_cvt_pk_bf16_f32 v120, v32, v33\n\t"
        "v_mfma_f32_16x16x32_bf16 v[72:75], v[196:199], v[92:95], v[72:75]\n\t"
        "ds_read_b128 v[88:91], v151 offset:128\n\t"
        "v_add_u32 v151, s9, v178\n\t"
        "v_mfma_f32_16x16x32_bf16 v[76:79], v[200:203], v[92:95], v[76:79]\n\t"
        "v_max_u32 v151, v151, v138\n\t"
        "v_xor_b32 v139, v181, v139\n\t"
        "v_mfma_f32_16x16x32_bf16 v[80:83], v[204:207], v[92:95], v[80:83]\n\t"
        "v_cvt_pk_bf16_f32 v121, v34, v35\n\t"
        "v_mfma_f32_16x16x32_bf16 v[72:75], v[232:235], v[96:99], v[72:75]\n\t"
        "v_permlane16_swap_b32 v116, v118\n\t"
        "v_mfma_f32_16x16x32_bf16 v[76:79], v[236:239], v[96:99], v[76:79]\n\t"
        "v_permlane16_swap_b32 v117, v119\n\t"
        "v_mfma_f32_16x16x32_bf16 v[80:83], v[240:243], v[96:99], v[80:83]\n\t"
        "buffer_store_dwordx4 v[116:119], v144, s[28:31], s55 offen sc1\n\t"
        "v_mfma_f32_16x16x32_bf16 v[72:75], a[0:3], v[100:103], v[72:75]\n\t"
        "ds_write_b64 v146, v[120:121] offset:4096\n\t"
        "v_mfma_f32_16x16x32_bf16 v[76:79], a[4:7], v[100:103], v[76:79]\n\t"
        "ds_read_b128 v[92:95], v183 offset:0\n\t"
        "s_add_u32 s55, s41, 129024\n\t"
        "v_cvt_pk_bf16_f32 v116, v36, v37\n\t"
        "v_mfma_f32_16x16x32_bf16 v[80:83], a[8:11], v[100:103], v[80:83]\n\t"
        "ds_read_b128 v[96:99], v183 offset:64\n\t"
        "v_cvt_pk_bf16_f32 v117, v38, v39\n\t"
        "v_mfma_f32_16x16x32_bf16 v[72:75], a[36:39], v[104:107], v[72:75]\n\t"
        "ds_read_b128 v[100:103], v183 offset:128\n\t"
        "v_add_u32 v183, s9, v179\n\t"
        "v_mfma_f32_16x16x32_bf16 v[76:79], a[40:43], v[104:107], v[76:79]\n\t"
        "v_max_u32 v183, v183, v139\n\t"
        "v_xor_b32 v140, v181, v140\n\t"
        "v_cvt_pk_bf16_f32 v118, v40, v41\n\t"
        "v_mfma_f32_16x16x32_bf16 v[80:83], a[44:47], v[104:107], v[80:83]\n\t"
        "v_cvt_pk_bf16_f32 v119, v42, v43\n\t"
        "v_cvt_pk_bf16_f32 v120, v44, v45\n\t"
        "s_waitcnt lgkmcnt(4)\n\t"
        "v_mfma_f32_16x16x32_bf16 v[72:75], a[72:75], v[108:111], v[72:75]\n\t"
        "ds_read_b128 v[104:107], v151 offset:0\n\t"
        "v_cvt_pk_bf16_f32 v121, v46, v47\n\t"
        "v_mfma_f32_16x16x32_bf16 v[76:79], a[76:79], v[108:111], v[76:79]\n\t"
        "v_permlane16_swap_b32 v116, v118\n\t"
        "v_mfma_f32_16x16x32_bf16 v[80:83], a[80:83], v[108:111], v[80:83]\n\t"
        "v_permlane16_swap_b32 v117, v119\n\t"
        "v_mfma_f32_16x16x32_bf16 v[72:75], a[108:111], v[112:115], v[72:75]\n\t"
        "buffer_store_dwordx4 v[116:119], v144, s[28:31], s55 offen sc1\n\t"
        "v_mfma_f32_16x16x32_bf16 v[76:79], a[112:115], v[112:115], v[76:79]\n\t"
        "ds_write_b64 v146, v[120:121] offset:6144\n\t"
        "v_mfma_f32_16x16x32_bf16 v[80:83], a[116:119], v[112:115], v[80:83]\n\t"
        "ds_read_b128 v[108:111], v151 offset:64\n\t"
        "v_mfma_f32_16x16x32_bf16 v[72:75], a[144:147], v[84:87], v[72:75]\n\t"
        "ds_read_b128 v[112:115], v151 offset:128\n\t"
        "v_add_u32 v151, s9, v180\n\t"
        "v_max_u32 v151, v151, v140\n\t"
        "v_mfma_f32_16x16x32_bf16 v[76:79], a[148:151], v[84:87], v[76:79]\n\t"
        "v_mfma_f32_16x16x32_bf16 v[80:83], a[152:155], v[84:87], v[80:83]\n\t"
        "ds_read_b128 v[84:87], v183 offset:0\n\t"
        "v_mfma_f32_16x16x32_bf16 v[72:75], a[180:183], v[88:91], v[72:75]\n\t"
        "v_mfma_f32_16x16x32_bf16 v[76:79], a[184:187], v[88:91], v[76:79]\n\t"
        "s_waitcnt lgkmcnt(3)\n\t"
        "s_barrier\n\t"
        "v_mfma_f32_16x16x32_bf16 v[80:83], a[188:191], v[88:91], v[80:83]\n\t"
        "ds_read_b128 v[88:91], v183 offset:64\n\t"
        "v_mfma_f32_16x16x32_bf16 v[72:75], a[216:219], v[92:95], v[72:75]\n\t"
        "ds_read_b128 v[124:127], v147 offset:0\n\t"
        "v_mfma_f32_16x16x32_bf16 v[76:79], a[220:223], v[92:95], v[76:79]\n\t"
        ".LpY2%=:\n\t"
        "s_cbranch_execz .LrY%=\n\t"
        "v_mfma_f32_16x16x32_bf16 v[80:83], a[224:227], v[92:95], v[80:83]\n\t"
        "ds_read_b128 v[92:95], v183 offset:128\n\t"
        "v_mfma_f32_16x16x32_bf16 v[72:75], a[252:255], v[96:99], v[72:75]\n\t"
        "v_mfma_f32_16x16x32_bf16 v[76:79], v[188:191], v[96:99], v[76:79]\n\t"
        "v_mfma_f32_16x16x32_bf16 v[80:83], v[192:195], v[96:99], v[80:83]\n\t"
        "ds_read_b128 v[96:99], v151 offset:0\n\t"
        "v_mfma_f32_16x16x32_bf16 v[72:75], v[220:223], v[100:103], v[72:75]\n\t"
        "v_mfma_f32_16x16x32_bf16 v[76:79], v[224:227], v[100:103], v[76:79]\n\t"
        "v_mfma_f32_16x16x32_bf16 v[80:83], v[228:231], v[100:103], v[80:83]\n\t"
        "ds_read_b128 v[100:103], v151 offset:64\n\t"
        "v_mfma_f32_16x16x32_bf16 v[72:75], a[24:27], v[104:107], v[72:75]\n\t"
        "s_waitcnt lgkmcnt(3)\n\t"
        "buffer_store_dwordx4 v[124:127], v145, s[28:31], s50 offen sc1\n\t"
        "v_mfma_f32_16x16x32_bf16 v[76:79], a[28:31], v[104:107], v[76:79]\n\t"
        "ds_read_b128 v[124:127], v147 offset:4096\n\t"
        "s_add_u32 s50, s50, 86016\n\t"
        "v_mfma_f32_16x16x32_bf16 v[80:83], a[32:35], v[104:107], v[80:83]\n\t"
        "ds_read_b128 v[104:107], v151 offset:128\n\t"
        "v_mfma_f32_16x16x32_bf16 v[80:83], a[68:71], v[108:111], v[80:83]\n\t"
        "v_mfma_f32_16x16x32_bf16 v[80:83], a[104:107], v[112:115], v[80:83]\n\t"
        "v_mfma_f32_16x16x32_bf16 v[80:83], a[140:143], v[84:87], v[80:83]\n\t"
        "v_mfma_f32_16x16x32_bf16 v[80:83], a[176:179], v[88:91], v[80:83]\n\t"
        "s_waitcnt lgkmcnt(3)\n\t"
        "v_mfma_f32_16x16x32_bf16 v[80:83], a[212:215], v[92:95], v[80:83]\n\t"
        "v_mfma_f32_16x16x32_bf16 v[80:83], a[248:251], v[96:99], v[80:83]\n\t"
        "s_waitcnt lgkmcnt(2)\n\t"
        "v_mfma_f32_16x16x32_bf16 v[80:83], v[216:219], v[100:103], v[80:83]\n\t"
        "s_waitcnt lgkmcnt(0)\n\t"
        "v_mfma_f32_16x16x32_bf16 v[80:83], v[252:255], v[104:107], v[80:83]\n\t"
        "buffer_store_dwordx4 v[124:127], v145, s[28:31], s50 offen sc1\n\t"
        "v_mfma_f32_16x16x32_bf16 v[72:75], a[60:63], v[108:111], v[72:75]\n\t"
        "s_nop 2\n\t"
        "v_mfma_f32_16x16x32_bf16 v[72:75], a[96:99], v[112:115], v[72:75]\n\t"
        "s_nop 1\n\t"
        "v_cvt_pk_bf16_f32 v120, v80, v81\n\t"
        "v_cvt_pk_bf16_f32 v121, v82, v83\n\t"
        "v_mfma_f32_16x16x32_bf16 v[72:75], a[132:135], v[84:87], v[72:75]\n\t"
        "ds_write_b64 v146, v[120:121] offset:10240\n\t"
        "v_mfma_f32_16x16x32_bf16 v[72:75], a[168:171], v[88:91], v[72:75]\n\t"
        "v_mfma_f32_16x16x32_bf16 v[72:75], a[204:207], v[92:95], v[72:75]\n\t"
        "v_mfma_f32_16x16x32_bf16 v[72:75], a[240:243], v[96:99], v[72:75]\n\t"
        "s_waitcnt lgkmcnt(0)\n\t"
        "s_barrier\n\t"
        "v_readlane_b32 s55, v155, 51\n\t"
        "v_mfma_f32_16x16x32_bf16 v[72:75], v[208:211], v[100:103], v[72:75]\n\t"
        "ds_read_b128 v[124:127], v147 offset:8192\n\t"
        "v_mfma_f32_16x16x32_bf16 v[72:75], v[244:247], v[104:107], v[72:75]\n\t"
        "s_nop 1\n\t"
        "v_mfma_f32_16x16x32_bf16 v[76:79], a[64:67], v[108:111], v[76:79]\n\t"
        "s_nop 1\n\t"
        "v_mfma_f32_16x16x32_bf16 v[76:79], a[100:103], v[112:115], v[76:79]\n\t"
        "v_mfma_f32_16x16x32_bf16 v[76:79], a[136:139], v[84:87], v[76:79]\n\t"
        "v_mfma_f32_16x16x32_bf16 v[76:79], a[172:175], v[88:91], v[76:79]\n\t"
        "v_mfma_f32_16x16x32_bf16 v[76:79], a[208:211], v[92:95], v[76:79]\n\t"
        "v_cvt_pk_bf16_f32 v116, v72, v73\n\t"
        "v_cvt_pk_bf16_f32 v117, v74, v75\n\t"
        "v_mfma_f32_16x16x32_bf16 v[76:79], a[244:247], v[96:99], v[76:79]\n\t"
        "s_waitcnt lgkmcnt(0)\n\t"
        "buffer_store_dwordx4 v[124:127], v150, s[28:31], s52 offen sc1\n\t"
        "v_mfma_f32_16x16x32_bf16 v[76:79], v[212:215], v[100:103], v[76:79]\n\t"
        "v_mfma_f32_16x16x32_bf16 v[76:79], v[248:251], v[104:107], v[76:79]\n\t"
        "s_nop 7\n\t"
        "v_cvt_pk_bf16_f32 v118, v76, v77\n\t"
        "v_cvt_pk_bf16_f32 v119, v78, v79\n\t"
        "s_nop 0\n\t"
        "v_permlane16_swap_b32 v116, v118\n\t"
        "v_permlane16_swap_b32 v117, v119\n\t"
        "buffer_store_dwordx4 v[116:119], v148, s[28:31], s55 offen sc1\n\t"
        "s_andn2_b32 s6, s6, 2\n\t"
        "s_cbranch_scc1 99f\n\t"
        "s_endpgm\n\t"
        ".Lx50%=:\n\t"
        "v_mov_b32 v151, v185\n\t"
        "v_mov_b32 v183, v186\n\t"
        "s_min_u32 s8, s3, 64\n\t"
        "s_waitcnt lgkmcnt(0)\n\t"
        "30:\n\t"
        "ds_read_b128 v[160:163], v185\n\t"
        "s_waitcnt lgkmcnt(0)\n\t"
        "v_lshlrev_b32_sdwa v157, 5, v160 dst_sel:DWORD dst_unused:UNUSED_PAD src0_sel:DWORD src1_sel:WORD_0\n\t"
        "v_lshlrev_b32_sdwa v158, 5, v160 dst_sel:DWORD dst_unused:UNUSED_PAD src0_sel:DWORD src1_sel:WORD_1\n\t"
        "v_or_b32 v157, v184, v157\n\t"
        "v_or_b32 v158, v184, v158\n\t"
        "v_xor_b32 v159, 32, v157\n\t"
        "v_xor_b32 v152, 32, v158\n\t"
        "ds_read_b64_tr_b16 v[60:61], v157 offset:0\n\t"
        "ds_read_b64_tr_b16 v[62:63], v158 offset:0\n\t"
        "ds_read_b64_tr_b16 v[64:65], v159 offset:0\n\t"
        "ds_read_b64_tr_b16 v[66:67], v152 offset:0\n\t"
        "ds_read_b64_tr_b16 v[68:69], v157 offset:64\n\t"
        "ds_read_b64_tr_b16 v[70:71], v158 offset:64\n\t"
        "ds_read_b64_tr_b16 v[72:73], v159 offset:64\n\t"
        "ds_read_b64_tr_b16 v[74:75], v152 offset:64\n\t"
        "ds_read_b64_tr_b16 v[76:77], v157 offset:128\n\t"
        "ds_read_b64_tr_b16 v[78:79], v158 offset:128\n\t"
        "ds_read_b64_tr_b16 v[80:81], v159 offset:128\n\t"
        "ds_read_b64_tr_b16 v[82:83], v152 offset:128\n\t"
        "v_perm_b32 v164, v162, v162, v168\n\t"
        "v_perm_b32 v165, v162, v162, v169\n\t"
        "v_perm_b32 v166, v162, v162, v170\n\t"
        "v_perm_b32 v167, v162, v162, v171\n\t"
        "v_cmp_ne_u32 vcc, 0, v163\n\t"
        "s_waitcnt lgkmcnt(10)\n\t"
        "v_mfma_f32_16x16x32_bf16 v[48:51], v[60:63], v[164:167], 0\n\t"
        "s_waitcnt lgkmcnt(8)\n\t"
        "v_mfma_f32_16x16x32_bf16 v[52:55], v[64:67], v[164:167], 0\n\t"
        "s_waitcnt lgkmcnt(6)\n\t"
        "v_mfma_f32_16x16x32_bf16 v[56:59], v[68:71], v[164:167], 0\n\t"
        "s_waitcnt lgkmcnt(4)\n\t"
        "v_mfma_f32_16x16x32_bf16 v[116:119], v[72:75], v[164:167], 0\n\t"
        "s_waitcnt lgkmcnt(2)\n\t"
        "v_mfma_f32_16x16x32_bf16 v[120:123], v[76:79], v[164:167], 0\n\t"
        "s_waitcnt lgkmcnt(0)\n\t"
        "v_mfma_f32_16x16x32_bf16 v[124:127], v[80:83], v[164:167], 0\n\t"
        "s_cbranch_vccz 31f\n\t"
        "v_lshlrev_b32_sdwa v157, 5, v161 dst_sel:DWORD dst_unused:UNUSED_PAD src0_sel:DWORD src1_sel:WORD_0\n\t"
        "v_lshlrev_b32_sdwa v158, 5, v161 dst_sel:DWORD dst_unused:UNUSED_PAD src0_sel:DWORD src1_sel:WORD_1\n\t"
        "v_or_b32 v157, v184, v157\n\t"
        "v_or_b32 v158, v184, v158\n\t"
        "v_xor_b32 v159, 32, v157\n\t"
        "v_xor_b32 v152, 32, v158\n\t"
        "ds_read_b64_tr_b16 v[60:61], v157 offset:0\n\t"
        "ds_read_b64_tr_b16 v[62:63], v158 offset:0\n\t"
        "ds_read_b64_tr_b16 v[64:65], v159 offset:0\n\t"
        "ds_read_b64_tr_b16 v[66:67], v152 offset:0\n\t"
        "ds_read_b64_tr_b16 v[68:69], v157 offset:64\n\t"
        "ds_read_b64_tr_b16 v[70:71], v158 offset:64\n\t"
        "ds_read_b64_tr_b16 v[72:73], v159 offset:64\n\t"
        "ds_read_b64_tr_b16 v[74:75], v152 offset:64\n\t"
        "ds_read_b64_tr_b16 v[76:77], v157 offset:128\n\t"
        "ds_read_b64_tr_b16 v[78:79], v158 offset:128\n\t"
        "ds_read_b64_tr_b16 v[80:81], v159 offset:128\n\t"
        "ds_read_b64_tr_b16 v[82:83], v152 offset:128\n\t"
        "v_perm_b32 v164, v163, v163, v168\n\t"
        "v_perm_b32 v165, v163, v163, v169\n\t"
        "v_perm_b32 v166, v163, v163, v170\n\t"
        "v_perm_b32 v167, v163, v163, v171\n\t"
        "s_waitcnt lgkmcnt(10)\n\t"
        "s_nop 0\n\t"
        "v_mfma_f32_16x16x32_bf16 v[48:51], v[60:63], v[164:167], v[48:51]\n\t"
        "s_waitcnt lgkmcnt(8)\n\t"
        "v_mfma_f32_16x16x32_bf16 v[52:55], v[64:67], v[164:167], v[52:55]\n\t"
        "s_waitcnt lgkmcnt(6)\n\t"
        "v_mfma_f32_16x16x32_bf16 v[56:59], v[68:71], v[164:167], v[56:59]\n\t"
        "s_waitcnt lgkmcnt(4)\n\t"
        "v_mfma_f32_16x16x32_bf16 v[116:119], v[72:75], v[164:167], v[116:119]\n\t"
        "s_waitcnt lgkmcnt(2)\n\t"
        "v_mfma_f32_16x16x32_bf16 v[120:123], v[76:79], v[164:167], v[120:123]\n\t"
        "s_waitcnt lgkmcnt(0)\n\t"
        "v_mfma_f32_16x16x32_bf16 v[124:127], v[80:83], v[164:167], v[124:127]\n\t"
        "31:\n\t"
        "v_cvt_pk_bf16_f32 v48, v48, v49\n\t"
        "v_cvt_pk_bf16_f32 v49, v50, v51\n\t"
        "v_cvt_pk_bf16_f32 v52, v52, v53\n\t"
        "v_cvt_pk_bf16_f32 v53, v54, v55\n\t"
        "v_cvt_pk_bf16_f32 v56, v56, v57\n\t"
        "v_cvt_pk_bf16_f32 v57, v58, v59\n\t"
        "v_cvt_pk_bf16_f32 v116, v116, v117\n\t"
        "v_cvt_pk_bf16_f32 v117, v118, v119\n\t"
        "v_cvt_pk_bf16_f32 v120, v120, v121\n\t"
        "v_cvt_pk_bf16_f32 v121, v122, v123\n\t"
        "v_cvt_pk_bf16_f32 v124, v124, v125\n\t"
        "v_cvt_pk_bf16_f32 v125, v126, v127\n\t"
        "ds_write_b64 v186, v[48:49] offset:0\n\t"
        "ds_write_b64 v154, v[52:53] offset:0\n\t"
        "ds_write_b64 v186, v[56:57] offset:64\n\t"
        "ds_write_b64 v154, v[116:117] offset:64\n\t"
        "ds_write_b64 v186, v[120:121] offset:128\n\t"
        "ds_write_b64 v154, v[124:125] offset:128\n\t"
        "v_add_u32 v185, 256, v185\n\t"
        "v_add_u32 v186, 12288, v186\n\t"
        "v_xor_b32 v154, 32, v186\n\t"
        "s_sub_i32 s8, s8, 16\n\t"
        "s_cmp_gt_i32 s8, 0\n\t"
        "s_cbranch_scc1 30b\n\t"
        "v_mov_b32 v185, v151\n\t"
        "v_mov_b32 v186, v183\n\t"
        "v_xor_b32 v154, 32, v186\n\t"
        "s_waitcnt lgkmcnt(0)\n\t"
        "s_barrier\n\t"
        "s_branch .Lx51%=\n\t"
        ".LwM%=:\n\t"
        "s_branch .LrM%=\n\t"
        ".Lx54%=:\n\t"
        "v_add_u32 v185, 768, v185\n\t"
        "v_add_u32 v186, 36864, v186\n\t"
        "v_xor_b32 v154, 32, v186\n\t"
        "s_min_u32 s47, s61, 64\n\t"
        "s_sub_u32 s47, s47, 48\n\t"
        "s_waitcnt lgkmcnt(0)\n\t"
        "40:\n\t"
        "ds_read_b128 v[160:163], v185\n\t"
        "s_waitcnt lgkmcnt(0)\n\t"
        "v_lshlrev_b32_sdwa v157, 5, v160 dst_sel:DWORD dst_unused:UNUSED_PAD src0_sel:DWORD src1_sel:WORD_0\n\t"
        "v_lshlrev_b32_sdwa v158, 5, v160 dst_sel:DWORD dst_unused:UNUSED_PAD src0_sel:DWORD src1_sel:WORD_1\n\t"
        "v_or_b32 v157, v184, v157\n\t"
        "v_or_b32 v158, v184, v158\n\t"
        "v_xor_b32 v159, 32, v157\n\t"
        "v_xor_b32 v152, 32, v158\n\t"
        "ds_read_b64_tr_b16 v[60:61], v157 offset:0\n\t"
        "ds_read_b64_tr_b16 v[62:63], v158 offset:0\n\t"
        "ds_read_b64_tr_b16 v[64:65], v159 offset:0\n\t"
        "ds_read_b64_tr_b16 v[66:67], v152 offset:0\n\t"
        "ds_read_b64_tr_b16 v[68:69], v157 offset:64\n\t"
        "ds_read_b64_tr_b16 v[70:71], v158 offset:64\n\t"
        "ds_read_b64_tr_b16 v[72:73], v159 offset:64\n\t"
        "ds_read_b64_tr_b16 v[74:75], v152 offset:64\n\t"
        "ds_read_b64_tr_b16 v[76:77], v157 offset:128\n\t"
        "ds_read_b64_tr_b16 v[78:79], v158 offset:128\n\t"
        "ds_read_b64_tr_b16 v[80:81], v159 offset:128\n\t"
        "ds_read_b64_tr_b16 v[82:83], v152 offset:128\n\t"
        "v_perm_b32 v164, v162, v162, v168\n\t"
        "v_perm_b32 v165, v162, v162, v169\n\t"
        "v_perm_b32 v166, v162, v162, v170\n\t"
        "v_perm_b32 v167, v162, v162, v171\n\t"
        "v_cmp_ne_u32 vcc, 0, v163\n\t"
        "s_waitcnt lgkmcnt(10)\n\t"
        "v_mfma_f32_16x16x32_bf16 v[0:3], v[60:63], v[164:167], 0\n\t"
        "s_waitcnt lgkmcnt(8)\n\t"
        "v_mfma_f32_16x16x32_bf16 v[4:7], v[64:67], v[164:167], 0\n\t"
        "s_waitcnt lgkmcnt(6)\n\t"
        "v_mfma_f32_16x16x32_bf16 v[8:11], v[68:71], v[164:167], 0\n\t"
        "s_waitcnt lgkmcnt(4)\n\t"
        "v_mfma_f32_16x16x32_bf16 v[12:15], v[72:75], v[164:167], 0\n\t"
        "s_waitcnt lgkmcnt(2)\n\t"
        "v_mfma_f32_16x16x32_bf16 v[16:19], v[76:79], v[164:167], 0\n\t"
        "s_waitcnt lgkmcnt(0)\n\t"
        "v_mfma_f32_16x16x32_bf16 v[20:23], v[80:83], v[164:167], 0\n\t"
        "s_cbranch_vccz 41f\n\t"
        "v_lshlrev_b32_sdwa v157, 5, v161 dst_sel:DWORD dst_unused:UNUSED_PAD src0_sel:DWORD src1_sel:WORD_0\n\t"
        "v_lshlrev_b32_sdwa v158, 5, v161 dst_sel:DWORD dst_unused:UNUSED_PAD src0_sel:DWORD src1_sel:WORD_1\n\t"
        "v_or_b32 v157, v184, v157\n\t"
        "v_or_b32 v158, v184, v158\n\t"
        "v_xor_b32 v159, 32, v157\n\t"
        "v_xor_b32 v152, 32, v158\n\t"
        "ds_read_b64_tr_b16 v[60:61], v157 offset:0\n\t"
        "ds_read_b64_tr_b16 v[62:63], v158 offset:0\n\t"
        "ds_read_b64_tr_b16 v[64:65], v159 offset:0\n\t"
        "ds_read_b64_tr_b16 v[66:67], v152 offset:0\n\t"
        "ds_read_b64_tr_b16 v[68:69], v157 offset:64\n\t"
        "ds_read_b64_tr_b16 v[70:71], v158 offset:64\n\t"
        "ds_read_b64_tr_b16 v[72:73], v159 offset:64\n\t"
        "ds_read_b64_tr_b16 v[74:75], v152 offset:64\n\t"
        "ds_read_b64_tr_b16 v[76:77], v157 offset:128\n\t"
        "ds_read_b64_tr_b16 v[78:79], v158 offset:128\n\t"
        "ds_read_b64_tr_b16 v[80:81], v159 offset:128\n\t"
        "ds_read_b64_tr_b16 v[82:83], v152 offset:128\n\t"
        "v_perm_b32 v164, v163, v163, v168\n\t"
        "v_perm_b32 v165, v163, v163, v169\n\t"
        "v_perm_b32 v166, v163, v163, v170\n\t"
        "v_perm_b32 v167, v163, v163, v171\n\t"
        "s_waitcnt lgkmcnt(10)\n\t"
        "s_nop 0\n\t"
        "v_mfma_f32_16x16x32_bf16 v[0:3], v[60:63], v[164:167], v[0:3]\n\t"
        "s_waitcnt lgkmcnt(8)\n\t"
        "v_mfma_f32_16x16x32_bf16 v[4:7], v[64:67], v[164:167], v[4:7]\n\t"
        "s_waitcnt lgkmcnt(6)\n\t"
        "v_mfma_f32_16x16x32_bf16 v[8:11], v[68:71], v[164:167], v[8:11]\n\t"
        "s_waitcnt lgkmcnt(4)\n\t"
        "v_mfma_f32_16x16x32_bf16 v[12:15], v[72:75], v[164:167], v[12:15]\n\t"
        "s_waitcnt lgkmcnt(2)\n\t"
        "v_mfma_f32_16x16x32_bf16 v[16:19], v[76:79], v[164:167], v[16:19]\n\t"
        "s_waitcnt lgkmcnt(0)\n\t"
        "v_mfma_f32_16x16x32_bf16 v[20:23], v[80:83], v[164:167], v[20:23]\n\t"
        "41:\n\t"
        "v_cvt_pk_bf16_f32 v0, v0, v1\n\t"
        "v_cvt_pk_bf16_f32 v1, v2, v3\n\t"
        "v_cvt_pk_bf16_f32 v4, v4, v5\n\t"
        "v_cvt_pk_bf16_f32 v5, v6, v7\n\t"
        "v_cvt_pk_bf16_f32 v8, v8, v9\n\t"
        "v_cvt_pk_bf16_f32 v9, v10, v11\n\t"
        "v_cvt_pk_bf16_f32 v12, v12, v13\n\t"
        "v_cvt_pk_bf16_f32 v13, v14, v15\n\t"
        "v_cvt_pk_bf16_f32 v16, v16, v17\n\t"
        "v_cvt_pk_bf16_f32 v17, v18, v19\n\t"
        "v_cvt_pk_bf16_f32 v20, v20, v21\n\t"
        "v_cvt_pk_bf16_f32 v21, v22, v23\n\t"
        "ds_write_b64 v186, v[0:1] offset:0\n\t"
        "ds_write_b64 v154, v[4:5] offset:0\n\t"
        "ds_write_b64 v186, v[8:9] offset:64\n\t"
        "ds_write_b64 v154, v[12:13] offset:64\n\t"
        "ds_write_b64 v186, v[16:17] offset:128\n\t"
        "ds_write_b64 v154, v[20:21] offset:128\n\t"
        "v_add_u32 v185, 256, v185\n\t"
        "v_add_u32 v186, 12288, v186\n\t"
        "v_xor_b32 v154, 32, v186\n\t"
        "s_sub_i32 s47, s47, 16\n\t"
        "s_cmp_gt_i32 s47, 0\n\t"
        "s_cbranch_scc1 40b\n\t"
        "s_cmp_le_u32 s61, 64\n\t"
        "s_cbranch_scc1 52f\n\t"
        "v_bfe_u32 v64, v185, 4, 4\n\t"
        "v_add_u32 v60, 65, v64\n\t"
        "s_sub_u32 s8, s37, 12288\n\t"
        "v_mov_b32 v63, s62\n\t"
        "v_lshlrev_b32 v65, 3, v64\n\t"
        "v_mul_u32_u24 v64, 192, v64\n\t"
        "v_and_or_b32 v64, v65, 48, v64\n\t"
        "v_add_u32 v61, s8, v64\n\t"
        "v_subrev_u32 v186, 61440, v186\n\t"
        "v_xor_b32 v154, 32, v186\n\t"
        "v_min_u32 v16, s61, v60\n\t"
        "v_lshl_add_u32 v16, v16, 3, s60\n\t"
        "ds_read_b64 v[0:1], v16\n\t"
        "v_cmp_ge_u32 s[66:67], s61, v60\n\t"
        "s_mov_b32 s46, 0x07060302\n\t"
        "s_waitcnt lgkmcnt(0)\n\t"
        "v_and_b32 v16, 0xfffff800, v1\n\t"
        "ds_read_b128 v[2:5], v16 offset:1536\n\t"
        "v_lshlrev_b32 v10, 16, v0\n\t"
        "v_and_b32 v11, 0xffff0000, v0\n\t"
        "v_med3_f32 v10, v10, 0, s63\n\t"
        "v_med3_f32 v11, v11, 0, s63\n\t"
        "s_mov_b64 exec, s[66:67]\n\t"
        "ds_write_b32 v1, v61\n\t"
        "s_mov_b64 exec, -1\n\t"
        "v_perm_b32 v0, v11, v10, s46\n\t"
        "v_fract_f32 v12, v10\n\t"
        "v_fract_f32 v13, v11\n\t"
        "v_cvt_u32_f32 v14, v10\n\t"
        "v_cvt_u32_f32 v15, v11\n\t"
        "v_cmp_neq_f32 s[64:65], 0, v12\n\t"
        "v_cmp_neq_f32 s[44:45], 0, v13\n\t"
        "v_sub_f32 v16, 1.0, v12\n\t"
        "v_sub_f32 v17, 1.0, v13\n\t"
        "v_cvt_pk_bf16_f32 v17, v16, v17\n\t"
        "v_lshlrev_b32 v16, 16, v17\n\t"
        "v_and_b32 v17, 0xffff0000, v17\n\t"
        "v_mul_f32 v18, v16, v17\n\t"
        "v_mul_f32 v16, v16, v13\n\t"
        "v_mul_f32 v17, v12, v17\n\t"
        "v_mul_f32 v19, v12, v13\n\t"
        "v_cndmask_b32 v16, v17, v16, s[44:45]\n\t"
        "v_cndmask_b32 v17, 0, v17, s[44:45]\n\t"
        "v_cvt_pk_bf16_f32 v162, v18, v16\n\t"
        "v_cvt_pk_bf16_f32 v163, v17, v19\n\t"
        "s_waitcnt lgkmcnt(1)\n\t"
        "v_pk_max_u16 v16, v0, v2\n\t"
        "v_pk_min_u16 v16, v16, v3\n\t"
        "v_cmp_ne_u32 vcc, v16, v0\n\t"
        "v_sub_u32 v16, v15, v5\n\t"
        "v_mad_u32_u24 v17, v15, 6, v4\n\t"
        "v_mad_u32_u24 v17, v14, s62, v17\n\t"
        "s_and_b64 vcc, vcc, s[66:67]\n\t"
        "s_or_b64 s[56:57], s[56:57], vcc\n\t"
        "v_bfe_u32 v18, v16, 2, 1\n\t"
        "v_or_b32 v18, v18, v17\n\t"
        "v_add_u32 v16, 1, v16\n\t"
        "v_bfe_u32 v16, v16, 2, 1\n\t"
        "v_add3_u32 v16, v17, v16, 6\n\t"
        "v_cndmask_b32 v16, v18, v16, s[44:45]\n\t"
        "v_cndmask_b32 v17, 0, v63, s[64:65]\n\t"
        "v_add_u32 v19, v18, v17\n\t"
        "v_add_u32 v20, v16, v17\n\t"
        "v_cndmask_b32 v16, v19, v16, s[44:45]\n\t"
        "v_lshl_or_b32 v160, v16, 16, v18\n\t"
        "v_lshl_or_b32 v161, v20, 16, v19\n\t"
        "v_lshlrev_b32_sdwa v157, 5, v160 dst_sel:DWORD dst_unused:UNUSED_PAD src0_sel:DWORD src1_sel:WORD_0\n\t"
        "v_lshlrev_b32_sdwa v158, 5, v160 dst_sel:DWORD dst_unused:UNUSED_PAD src0_sel:DWORD src1_sel:WORD_1\n\t"
        "v_or_b32 v157, v184, v157\n\t"
        "v_or_b32 v158, v184, v158\n\t"
        "v_xor_b32 v159, 32, v157\n\t"
        "v_xor_b32 v152, 32, v158\n\t"
        "ds_read_b64_tr_b16 v[60:61], v157 offset:0\n\t"
        "ds_read_b64_tr_b16 v[62:63], v158 offset:0\n\t"
        "ds_read_b64_tr_b16 v[64:65], v159 offset:0\n\t"
        "ds_read_b64_tr_b16 v[66:67], v152 offset:0\n\t"
        "ds_read_b64_tr_b16 v[68:69], v157 offset:64\n\t"
        "ds_read_b64_tr_b16 v[70:71], v158 offset:64\n\t"
        "ds_read_b64_tr_b16 v[72:73], v159 offset:64\n\t"
        "ds_read_b64_tr_b16 v[74:75], v152 offset:64\n\t"
        "ds_read_b64_tr_b16 v[76:77], v157 offset:128\n\t"
        "ds_read_b64_tr_b16 v[78:79], v158 offset:128\n\t"
        "ds_read_b64_tr_b16 v[80:81], v159 offset:128\n\t"
        "ds_read_b64_tr_b16 v[82:83], v152 offset:128\n\t"
        "v_lshlrev_b32_sdwa v157, 5, v161 dst_sel:DWORD dst_unused:UNUSED_PAD src0_sel:DWORD src1_sel:WORD_0\n\t"
        "v_lshlrev_b32_sdwa v158, 5, v161 dst_sel:DWORD dst_unused:UNUSED_PAD src0_sel:DWORD src1_sel:WORD_1\n\t"
        "v_or_b32 v157, v184, v157\n\t"
        "v_or_b32 v158, v184, v158\n\t"
        "v_xor_b32 v159, 32, v157\n\t"
        "v_xor_b32 v152, 32, v158\n\t"
        "ds_read_b64_tr_b16 v[24:25], v157 offset:0\n\t"
        "ds_read_b64_tr_b16 v[26:27], v158 offset:0\n\t"
        "ds_read_b64_tr_b16 v[28:29], v159 offset:0\n\t"
        "ds_read_b64_tr_b16 v[30:31], v152 offset:0\n\t"
        "ds_read_b64_tr_b16 v[32:33], v157 offset:64\n\t"
        "ds_read_b64_tr_b16 v[34:35], v158 offset:64\n\t"
        "ds_read_b64_tr_b16 v[36:37], v159 offset:64\n\t"
        "ds_read_b64_tr_b16 v[38:39], v152 offset:64\n\t"
        "ds_read_b64_tr_b16 v[40:41], v157 offset:128\n\t"
        "ds_read_b64_tr_b16 v[42:43], v158 offset:128\n\t"
        "ds_read_b64_tr_b16 v[44:45], v159 offset:128\n\t"
        "ds_read_b64_tr_b16 v[46:47], v152 offset:128\n\t"
        "s_cmp_lg_u64 s[56:57], 0\n\t"
        "s_cselect_b32 s8, 1, 0\n\t"
        "v_mov_b32 v157, s8\n\t"
        "v_perm_b32 v164, v162, v162, v168\n\t"
        "v_perm_b32 v165, v162, v162, v169\n\t"
        "v_perm_b32 v166, v162, v162, v170\n\t"
        "v_perm_b32 v167, v162, v162, v171\n\t"
        "s_waitcnt lgkmcnt(12)\n\t"
        "s_nop 0\n\t"
        "v_mfma_f32_16x16x32_bf16 v[0:3], v[60:63], v[164:167], 0\n\t"
        "v_mfma_f32_16x16x32_bf16 v[4:7], v[64:67], v[164:167], 0\n\t"
        "v_mfma_f32_16x16x32_bf16 v[8:11], v[68:71], v[164:167], 0\n\t"
        "v_mfma_f32_16x16x32_bf16 v[12:15], v[72:75], v[164:167], 0\n\t"
        "v_mfma_f32_16x16x32_bf16 v[16:19], v[76:79], v[164:167], 0\n\t"
        "v_mfma_f32_16x16x32_bf16 v[20:23], v[80:83], v[164:167], 0\n\t"
        "v_perm_b32 v164, v163, v163, v168\n\t"
        "v_perm_b32 v165, v163, v163, v169\n\t"
        "v_perm_b32 v166, v163, v163, v170\n\t"
        "v_perm_b32 v167, v163, v163, v171\n\t"
        "s_waitcnt lgkmcnt(3)\n\t"
        "s_nop 0\n\t"
        "v_mfma_f32_16x16x32_bf16 v[0:3], v[24:27], v[164:167], v[0:3]\n\t"
        "v_mfma_f32_16x16x32_bf16 v[4:7], v[28:31], v[164:167], v[4:7]\n\t"
        "v_mfma_f32_16x16x32_bf16 v[8:11], v[32:35], v[164:167], v[8:11]\n\t"
        "v_mfma_f32_16x16x32_bf16 v[12:15], v[36:39], v[164:167], v[12:15]\n\t"
        "s_waitcnt lgkmcnt(0)\n\t"
        "v_mfma_f32_16x16x32_bf16 v[16:19], v[40:43], v[164:167], v[16:19]\n\t"
        "v_mfma_f32_16x16x32_bf16 v[20:23], v[44:47], v[164:167], v[20:23]\n\t"
        "ds_write_b32 v153, v157 offset:16\n\t"
        "s_nop 0\n\t"
        "v_cvt_pk_bf16_f32 v0, v0, v1\n\t"
        "v_cvt_pk_bf16_f32 v1, v2, v3\n\t"
        "v_cvt_pk_bf16_f32 v4, v4, v5\n\t"
        "v_cvt_pk_bf16_f32 v5, v6, v7\n\t"
        "v_cvt_pk_bf16_f32 v8, v8, v9\n\t"
        "v_cvt_pk_bf16_f32 v9, v10, v11\n\t"
        "v_cvt_pk_bf16_f32 v12, v12, v13\n\t"
        "v_cvt_pk_bf16_f32 v13, v14, v15\n\t"
        "v_cvt_pk_bf16_f32 v16, v16, v17\n\t"
        "v_cvt_pk_bf16_f32 v17, v18, v19\n\t"
        "v_cvt_pk_bf16_f32 v20, v20, v21\n\t"
        "v_cvt_pk_bf16_f32 v21, v22, v23\n\t"
        "ds_write_b64 v186, v[0:1] offset:0\n\t"
        "ds_write_b64 v154, v[4:5] offset:0\n\t"
        "ds_write_b64 v186, v[8:9] offset:64\n\t"
        "ds_write_b64 v154, v[12:13] offset:64\n\t"
        "ds_write_b64 v186, v[16:17] offset:128\n\t"
        "ds_write_b64 v154, v[20:21] offset:128\n\t"
        "52:\n\t"
        "s_branch .Lx55%=\n\t"
        ".Lx58%=:\n\t"
        "v_mov_b32 v151, v185\n\t"
        "v_mov_b32 v183, v186\n\t"
        "s_min_u32 s8, s3, 64\n\t"
        "s_waitcnt lgkmcnt(0)\n\t"
        "30:\n\t"
        "ds_read_b128 v[160:163], v185\n\t"
        "s_waitcnt lgkmcnt(0)\n\t"
        "v_lshlrev_b32_sdwa v157, 5, v160 dst_sel:DWORD dst_unused:UNUSED_PAD src0_sel:DWORD src1_sel:WORD_0\n\t"
        "v_lshlrev_b32_sdwa v158, 5, v160 dst_sel:DWORD dst_unused:UNUSED_PAD src0_sel:DWORD src1_sel:WORD_1\n\t"
        "v_or_b32 v157, v184, v157\n\t"
        "v_or_b32 v158, v184, v158\n\t"
        "v_xor_b32 v159, 32, v157\n\t"
        "v_xor_b32 v152, 32, v158\n\t"
        "ds_read_b64_tr_b16 v[60:61], v157 offset:0\n\t"
        "ds_read_b64_tr_b16 v[62:63], v158 offset:0\n\t"
        "ds_read_b64_tr_b16 v[64:65], v159 offset:0\n\t"
        "ds_read_b64_tr_b16 v[66:67], v152 offset:0\n\t"
        "ds_read_b64_tr_b16 v[68:69], v157 offset:64\n\t"
        "ds_read_b64_tr_b16 v[70:71], v158 offset:64\n\t"
        "ds_read_b64_tr_b16 v[72:73], v159 offset:64\n\t"
        "ds_read_b64_tr_b16 v[74:75], v152 offset:64\n\t"
        "ds_read_b64_tr_b16 v[76:77], v157 offset:128\n\t"
        "ds_read_b64_tr_b16 v[78:79], v158 offset:128\n\t"
        "ds_read_b64_tr_b16 v[80:81], v159 offset:128\n\t"
        "ds_read_b64_tr_b16 v[82:83], v152 offset:128\n\t"
        "v_perm_b32 v164, v162, v162, v168\n\t"
        "v_perm_b32 v165, v162, v162, v169\n\t"
        "v_perm_b32 v166, v162, v162, v170\n\t"
        "v_perm_b32 v167, v162, v162, v171\n\t"
        "v_cmp_ne_u32 vcc, 0, v163\n\t"
        "s_waitcnt lgkmcnt(10)\n\t"
        "v_mfma_f32_16x16x32_bf16 v[48:51], v[60:63], v[164:167], 0\n\t"
        "s_waitcnt lgkmcnt(8)\n\t"
        "v_mfma_f32_16x16x32_bf16 v[52:55], v[64:67], v[164:167], 0\n\t"
        "s_waitcnt lgkmcnt(6)\n\t"
        "v_mfma_f32_16x16x32_bf16 v[56:59], v[68:71], v[164:167], 0\n\t"
        "s_waitcnt lgkmcnt(4)\n\t"
        "v_mfma_f32_16x16x32_bf16 v[116:119], v[72:75], v[164:167], 0\n\t"
        "s_waitcnt lgkmcnt(2)\n\t"
        "v_mfma_f32_16x16x32_bf16 v[120:123], v[76:79], v[164:167], 0\n\t"
        "s_waitcnt lgkmcnt(0)\n\t"
        "v_mfma_f32_16x16x32_bf16 v[124:127], v[80:83], v[164:167], 0\n\t"
        "s_cbranch_vccz 31f\n\t"
        "v_lshlrev_b32_sdwa v157, 5, v161 dst_sel:DWORD dst_unused:UNUSED_PAD src0_sel:DWORD src1_sel:WORD_0\n\t"
        "v_lshlrev_b32_sdwa v158, 5, v161 dst_sel:DWORD dst_unused:UNUSED_PAD src0_sel:DWORD src1_sel:WORD_1\n\t"
        "v_or_b32 v157, v184, v157\n\t"
        "v_or_b32 v158, v184, v158\n\t"
        "v_xor_b32 v159, 32, v157\n\t"
        "v_xor_b32 v152, 32, v158\n\t"
        "ds_read_b64_tr_b16 v[60:61], v157 offset:0\n\t"
        "ds_read_b64_tr_b16 v[62:63], v158 offset:0\n\t"
        "ds_read_b64_tr_b16 v[64:65], v159 offset:0\n\t"
        "ds_read_b64_tr_b16 v[66:67], v152 offset:0\n\t"
        "ds_read_b64_tr_b16 v[68:69], v157 offset:64\n\t"
        "ds_read_b64_tr_b16 v[70:71], v158 offset:64\n\t"
        "ds_read_b64_tr_b16 v[72:73], v159 offset:64\n\t"
        "ds_read_b64_tr_b16 v[74:75], v152 offset:64\n\t"
        "ds_read_b64_tr_b16 v[76:77], v157 offset:128\n\t"
        "ds_read_b64_tr_b16 v[78:79], v158 offset:128\n\t"
        "ds_read_b64_tr_b16 v[80:81], v159 offset:128\n\t"
        "ds_read_b64_tr_b16 v[82:83], v152 offset:128\n\t"
        "v_perm_b32 v164, v163, v163, v168\n\t"
        "v_perm_b32 v165, v163, v163, v169\n\t"
        "v_perm_b32 v166, v163, v163, v170\n\t"
        "v_perm_b32 v167, v163, v163, v171\n\t"
        "s_waitcnt lgkmcnt(10)\n\t"
        "s_nop 0\n\t"
        "v_mfma_f32_16x16x32_bf16 v[48:51], v[60:63], v[164:167], v[48:51]\n\t"
        "s_waitcnt lgkmcnt(8)\n\t"
        "v_mfma_f32_16x16x32_bf16 v[52:55], v[64:67], v[164:167], v[52:55]\n\t"
        "s_waitcnt lgkmcnt(6)\n\t"
        "v_mfma_f32_16x16x32_bf16 v[56:59], v[68:71], v[164:167], v[56:59]\n\t"
        "s_waitcnt lgkmcnt(4)\n\t"
        "v_mfma_f32_16x16x32_bf16 v[116:119], v[72:75], v[164:167], v[116:119]\n\t"
        "s_waitcnt lgkmcnt(2)\n\t"
        "v_mfma_f32_16x16x32_bf16 v[120:123], v[76:79], v[164:167], v[120:123]\n\t"
        "s_waitcnt lgkmcnt(0)\n\t"
        "v_mfma_f32_16x16x32_bf16 v[124:127], v[80:83], v[164:167], v[124:127]\n\t"
        "31:\n\t"
        "v_cvt_pk_bf16_f32 v48, v48, v49\n\t"
        "v_cvt_pk_bf16_f32 v49, v50, v51\n\t"
        "v_cvt_pk_bf16_f32 v52, v52, v53\n\t"
        "v_cvt_pk_bf16_f32 v53, v54, v55\n\t"
        "v_cvt_pk_bf16_f32 v56, v56, v57\n\t"
        "v_cvt_pk_bf16_f32 v57, v58, v59\n\t"
        "v_cvt_pk_bf16_f32 v116, v116, v117\n\t"
        "v_cvt_pk_bf16_f32 v117, v118, v119\n\t"
        "v_cvt_pk_bf16_f32 v120, v120, v121\n\t"
        "v_cvt_pk_bf16_f32 v121, v122, v123\n\t"
        "v_cvt_pk_bf16_f32 v124, v124, v125\n\t"
        "v_cvt_pk_bf16_f32 v125, v126, v127\n\t"
        "ds_write_b64 v186, v[48:49] offset:0\n\t"
        "ds_write_b64 v154, v[52:53] offset:0\n\t"
        "ds_write_b64 v186, v[56:57] offset:64\n\t"
        "ds_write_b64 v154, v[116:117] offset:64\n\t"
        "ds_write_b64 v186, v[120:121] offset:128\n\t"
        "ds_write_b64 v154, v[124:125] offset:128\n\t"
        "v_add_u32 v185, 256, v185\n\t"
        "v_add_u32 v186, 12288, v186\n\t"
        "v_xor_b32 v154, 32, v186\n\t"
        "s_sub_i32 s8, s8, 16\n\t"
        "s_cmp_gt_i32 s8, 0\n\t"
        "s_cbranch_scc1 30b\n\t"
        "v_mov_b32 v185, v151\n\t"
        "v_mov_b32 v186, v183\n\t"
        "v_xor_b32 v154, 32, v186\n\t"
        "s_waitcnt lgkmcnt(0)\n\t"
        "s_barrier\n\t"
        "s_branch .Lx59%=\n\t"
        ".Lx66%=:\n\t"
        "v_mov_b32 v151, v185\n\t"
        "v_mov_b32 v183, v186\n\t"
        "s_min_u32 s8, s3, 64\n\t"
        "s_waitcnt lgkmcnt(0)\n\t"
        "30:\n\t"
        "ds_read_b128 v[160:163], v185\n\t"
        "s_waitcnt lgkmcnt(0)\n\t"
        "v_lshlrev_b32_sdwa v157, 5, v160 dst_sel:DWORD dst_unused:UNUSED_PAD src0_sel:DWORD src1_sel:WORD_0\n\t"
        "v_lshlrev_b32_sdwa v158, 5, v160 dst_sel:DWORD dst_unused:UNUSED_PAD src0_sel:DWORD src1_sel:WORD_1\n\t"
        "v_or_b32 v157, v184, v157\n\t"
        "v_or_b32 v158, v184, v158\n\t"
        "v_xor_b32 v159, 32, v157\n\t"
        "v_xor_b32 v152, 32, v158\n\t"
        "ds_read_b64_tr_b16 v[60:61], v157 offset:0\n\t"
        "ds_read_b64_tr_b16 v[62:63], v158 offset:0\n\t"
        "ds_read_b64_tr_b16 v[64:65], v159 offset:0\n\t"
        "ds_read_b64_tr_b16 v[66:67], v152 offset:0\n\t"
        "ds_read_b64_tr_b16 v[68:69], v157 offset:64\n\t"
        "ds_read_b64_tr_b16 v[70:71], v158 offset:64\n\t"
        "ds_read_b64_tr_b16 v[72:73], v159 offset:64\n\t"
        "ds_read_b64_tr_b16 v[74:75], v152 offset:64\n\t"
        "ds_read_b64_tr_b16 v[76:77], v157 offset:128\n\t"
        "ds_read_b64_tr_b16 v[78:79], v158 offset:128\n\t"
        "ds_read_b64_tr_b16 v[80:81], v159 offset:128\n\t"
        "ds_read_b64_tr_b16 v[82:83], v152 offset:128\n\t"
        "v_perm_b32 v164, v162, v162, v168\n\t"
        "v_perm_b32 v165, v162, v162, v169\n\t"
        "v_perm_b32 v166, v162, v162, v170\n\t"
        "v_perm_b32 v167, v162, v162, v171\n\t"
        "v_cmp_ne_u32 vcc, 0, v163\n\t"
        "s_waitcnt lgkmcnt(10)\n\t"
        "v_mfma_f32_16x16x32_bf16 v[48:51], v[60:63], v[164:167], 0\n\t"
        "s_waitcnt lgkmcnt(8)\n\t"
        "v_mfma_f32_16x16x32_bf16 v[52:55], v[64:67], v[164:167], 0\n\t"
        "s_waitcnt lgkmcnt(6)\n\t"
        "v_mfma_f32_16x16x32_bf16 v[56:59], v[68:71], v[164:167], 0\n\t"
        "s_waitcnt lgkmcnt(4)\n\t"
        "v_mfma_f32_16x16x32_bf16 v[116:119], v[72:75], v[164:167], 0\n\t"
        "s_waitcnt lgkmcnt(2)\n\t"
        "v_mfma_f32_16x16x32_bf16 v[120:123], v[76:79], v[164:167], 0\n\t"
        "s_waitcnt lgkmcnt(0)\n\t"
        "v_mfma_f32_16x16x32_bf16 v[124:127], v[80:83], v[164:167], 0\n\t"
        "s_cbranch_vccz 31f\n\t"
        "v_lshlrev_b32_sdwa v157, 5, v161 dst_sel:DWORD dst_unused:UNUSED_PAD src0_sel:DWORD src1_sel:WORD_0\n\t"
        "v_lshlrev_b32_sdwa v158, 5, v161 dst_sel:DWORD dst_unused:UNUSED_PAD src0_sel:DWORD src1_sel:WORD_1\n\t"
        "v_or_b32 v157, v184, v157\n\t"
        "v_or_b32 v158, v184, v158\n\t"
        "v_xor_b32 v159, 32, v157\n\t"
        "v_xor_b32 v152, 32, v158\n\t"
        "ds_read_b64_tr_b16 v[60:61], v157 offset:0\n\t"
        "ds_read_b64_tr_b16 v[62:63], v158 offset:0\n\t"
        "ds_read_b64_tr_b16 v[64:65], v159 offset:0\n\t"
        "ds_read_b64_tr_b16 v[66:67], v152 offset:0\n\t"
        "ds_read_b64_tr_b16 v[68:69], v157 offset:64\n\t"
        "ds_read_b64_tr_b16 v[70:71], v158 offset:64\n\t"
        "ds_read_b64_tr_b16 v[72:73], v159 offset:64\n\t"
        "ds_read_b64_tr_b16 v[74:75], v152 offset:64\n\t"
        "ds_read_b64_tr_b16 v[76:77], v157 offset:128\n\t"
        "ds_read_b64_tr_b16 v[78:79], v158 offset:128\n\t"
        "ds_read_b64_tr_b16 v[80:81], v159 offset:128\n\t"
        "ds_read_b64_tr_b16 v[82:83], v152 offset:128\n\t"
        "v_perm_b32 v164, v163, v163, v168\n\t"
        "v_perm_b32 v165, v163, v163, v169\n\t"
        "v_perm_b32 v166, v163, v163, v170\n\t"
        "v_perm_b32 v167, v163, v163, v171\n\t"
        "s_waitcnt lgkmcnt(10)\n\t"
        "s_nop 0\n\t"
        "v_mfma_f32_16x16x32_bf16 v[48:51], v[60:63], v[164:167], v[48:51]\n\t"
        "s_waitcnt lgkmcnt(8)\n\t"
        "v_mfma_f32_16x16x32_bf16 v[52:55], v[64:67], v[164:167], v[52:55]\n\t"
        "s_waitcnt lgkmcnt(6)\n\t"
        "v_mfma_f32_16x16x32_bf16 v[56:59], v[68:71], v[164:167], v[56:59]\n\t"
        "s_waitcnt lgkmcnt(4)\n\t"
        "v_mfma_f32_16x16x32_bf16 v[116:119], v[72:75], v[164:167], v[116:119]\n\t"
        "s_waitcnt lgkmcnt(2)\n\t"
        "v_mfma_f32_16x16x32_bf16 v[120:123], v[76:79], v[164:167], v[120:123]\n\t"
        "s_waitcnt lgkmcnt(0)\n\t"
        "v_mfma_f32_16x16x32_bf16 v[124:127], v[80:83], v[164:167], v[124:127]\n\t"
        "31:\n\t"
        "v_cvt_pk_bf16_f32 v48, v48, v49\n\t"
        "v_cvt_pk_bf16_f32 v49, v50, v51\n\t"
        "v_cvt_pk_bf16_f32 v52, v52, v53\n\t"
        "v_cvt_pk_bf16_f32 v53, v54, v55\n\t"
        "v_cvt_pk_bf16_f32 v56, v56, v57\n\t"
        "v_cvt_pk_bf16_f32 v57, v58, v59\n\t"
        "v_cvt_pk_bf16_f32 v116, v116, v117\n\t"
        "v_cvt_pk_bf16_f32 v117, v118, v119\n\t"
        "v_cvt_pk_bf16_f32 v120, v120, v121\n\t"
        "v_cvt_pk_bf16_f32 v121, v122, v123\n\t"
        "v_cvt_pk_bf16_f32 v124, v124, v125\n\t"
        "v_cvt_pk_bf16_f32 v125, v126, v127\n\t"
        "ds_write_b64 v186, v[48:49] offset:0\n\t"
        "ds_write_b64 v154, v[52:53] offset:0\n\t"
        "ds_write_b64 v186, v[56:57] offset:64\n\t"
        "ds_write_b64 v154, v[116:117] offset:64\n\t"
        "ds_write_b64 v186, v[120:121] offset:128\n\t"
        "ds_write_b64 v154, v[124:125] offset:128\n\t"
        "v_add_u32 v185, 256, v185\n\t"
        "v_add_u32 v186, 12288, v186\n\t"
        "v_xor_b32 v154, 32, v186\n\t"
        "s_sub_i32 s8, s8, 16\n\t"
        "s_cmp_gt_i32 s8, 0\n\t"
        "s_cbranch_scc1 30b\n\t"
        "v_mov_b32 v185, v151\n\t"
        "v_mov_b32 v186, v183\n\t"
        "v_xor_b32 v154, 32, v186\n\t"
        "s_waitcnt lgkmcnt(0)\n\t"
        "s_barrier\n\t"
        "s_branch .Lx67%=\n\t"
        ".LwX%=:\n\t"
        "s_branch .LrX%=\n\t"
        ".Lx68%=:\n\t"
        "v_add_u32 v185, 1024, v185\n\t"
        "v_add_u32 v186, 49152, v186\n\t"
        "v_xor_b32 v154, 32, v186\n\t"
        "v_bfe_u32 v64, v185, 4, 4\n\t"
        "v_add_u32 v60, 65, v64\n\t"
        "s_sub_u32 s8, s37, 12288\n\t"
        "v_mov_b32 v63, s62\n\t"
        "v_lshlrev_b32 v65, 3, v64\n\t"
        "v_mul_u32_u24 v64, 192, v64\n\t"
        "v_and_or_b32 v64, v65, 48, v64\n\t"
        "v_add_u32 v61, s8, v64\n\t"
        "v_subrev_u32 v186, 61440, v186\n\t"
        "v_xor_b32 v154, 32, v186\n\t"
        "v_min_u32 v16, s61, v60\n\t"
        "v_lshl_add_u32 v16, v16, 3, s60\n\t"
        "ds_read_b64 v[0:1], v16\n\t"
        "v_cmp_ge_u32 s[66:67], s61, v60\n\t"
        "s_mov_b32 s46, 0x07060302\n\t"
        "s_waitcnt lgkmcnt(0)\n\t"
        "v_and_b32 v16, 0xfffff800, v1\n\t"
        "ds_read_b128 v[2:5], v16 offset:1536\n\t"
        "v_lshlrev_b32 v10, 16, v0\n\t"
        "v_and_b32 v11, 0xffff0000, v0\n\t"
        "v_med3_f32 v10, v10, 0, s63\n\t"
        "v_med3_f32 v11, v11, 0, s63\n\t"
        "s_mov_b64 exec, s[66:67]\n\t"
        "ds_write_b32 v1, v61\n\t"
        "s_mov_b64 exec, -1\n\t"
        "v_perm_b32 v0, v11, v10, s46\n\t"
        "v_fract_f32 v12, v10\n\t"
        "v_fract_f32 v13, v11\n\t"
        "v_cvt_u32_f32 v14, v10\n\t"
        "v_cvt_u32_f32 v15, v11\n\t"
        "v_cmp_neq_f32 s[64:65], 0, v12\n\t"
        "v_cmp_neq_f32 s[44:45], 0, v13\n\t"
        "v_sub_f32 v16, 1.0, v12\n\t"
        "v_sub_f32 v17, 1.0, v13\n\t"
        "v_cvt_pk_bf16_f32 v17, v16, v17\n\t"
        "v_lshlrev_b32 v16, 16, v17\n\t"
        "v_and_b32 v17, 0xffff0000, v17\n\t"
        "v_mul_f32 v18, v16, v17\n\t"
        "v_mul_f32 v16, v16, v13\n\t"
        "v_mul_f32 v17, v12, v17\n\t"
        "v_mul_f32 v19, v12, v13\n\t"
        "v_cndmask_b32 v16, v17, v16, s[44:45]\n\t"
        "v_cndmask_b32 v17, 0, v17, s[44:45]\n\t"
        "v_cvt_pk_bf16_f32 v162, v18, v16\n\t"
        "v_cvt_pk_bf16_f32 v163, v17, v19\n\t"
        "s_waitcnt lgkmcnt(1)\n\t"
        "v_pk_max_u16 v16, v0, v2\n\t"
        "v_pk_min_u16 v16, v16, v3\n\t"
        "v_cmp_ne_u32 vcc, v16, v0\n\t"
        "v_sub_u32 v16, v15, v5\n\t"
        "v_mad_u32_u24 v17, v15, 6, v4\n\t"
        "v_mad_u32_u24 v17, v14, s62, v17\n\t"
        "s_and_b64 vcc, vcc, s[66:67]\n\t"
        "s_or_b64 s[56:57], s[56:57], vcc\n\t"
        "v_bfe_u32 v18, v16, 2, 1\n\t"
        "v_or_b32 v18, v18, v17\n\t"
        "v_add_u32 v16, 1, v16\n\t"
        "v_bfe_u32 v16, v16, 2, 1\n\t"
        "v_add3_u32 v16, v17, v16, 6\n\t"
        "v_cndmask_b32 v16, v18, v16, s[44:45]\n\t"
        "v_cndmask_b32 v17, 0, v63, s[64:65]\n\t"
        "v_add_u32 v19, v18, v17\n\t"
        "v_add_u32 v20, v16, v17\n\t"
        "v_cndmask_b32 v16, v19, v16, s[44:45]\n\t"
        "v_lshl_or_b32 v160, v16, 16, v18\n\t"
        "v_lshl_or_b32 v161, v20, 16, v19\n\t"
        "v_lshlrev_b32_sdwa v157, 5, v160 dst_sel:DWORD dst_unused:UNUSED_PAD src0_sel:DWORD src1_sel:WORD_0\n\t"
        "v_lshlrev_b32_sdwa v158, 5, v160 dst_sel:DWORD dst_unused:UNUSED_PAD src0_sel:DWORD src1_sel:WORD_1\n\t"
        "v_or_b32 v157, v184, v157\n\t"
        "v_or_b32 v158, v184, v158\n\t"
        "v_xor_b32 v159, 32, v157\n\t"
        "v_xor_b32 v152, 32, v158\n\t"
        "ds_read_b64_tr_b16 v[60:61], v157 offset:0\n\t"
        "ds_read_b64_tr_b16 v[62:63], v158 offset:0\n\t"
        "ds_read_b64_tr_b16 v[64:65], v159 offset:0\n\t"
        "ds_read_b64_tr_b16 v[66:67], v152 offset:0\n\t"
        "ds_read_b64_tr_b16 v[68:69], v157 offset:64\n\t"
        "ds_read_b64_tr_b16 v[70:71], v158 offset:64\n\t"
        "ds_read_b64_tr_b16 v[72:73], v159 offset:64\n\t"
        "ds_read_b64_tr_b16 v[74:75], v152 offset:64\n\t"
        "ds_read_b64_tr_b16 v[76:77], v157 offset:128\n\t"
        "ds_read_b64_tr_b16 v[78:79], v158 offset:128\n\t"
        "ds_read_b64_tr_b16 v[80:81], v159 offset:128\n\t"
        "ds_read_b64_tr_b16 v[82:83], v152 offset:128\n\t"
        "v_lshlrev_b32_sdwa v157, 5, v161 dst_sel:DWORD dst_unused:UNUSED_PAD src0_sel:DWORD src1_sel:WORD_0\n\t"
        "v_lshlrev_b32_sdwa v158, 5, v161 dst_sel:DWORD dst_unused:UNUSED_PAD src0_sel:DWORD src1_sel:WORD_1\n\t"
        "v_or_b32 v157, v184, v157\n\t"
        "v_or_b32 v158, v184, v158\n\t"
        "v_xor_b32 v159, 32, v157\n\t"
        "v_xor_b32 v152, 32, v158\n\t"
        "ds_read_b64_tr_b16 v[24:25], v157 offset:0\n\t"
        "ds_read_b64_tr_b16 v[26:27], v158 offset:0\n\t"
        "ds_read_b64_tr_b16 v[28:29], v159 offset:0\n\t"
        "ds_read_b64_tr_b16 v[30:31], v152 offset:0\n\t"
        "ds_read_b64_tr_b16 v[32:33], v157 offset:64\n\t"
        "ds_read_b64_tr_b16 v[34:35], v158 offset:64\n\t"
        "ds_read_b64_tr_b16 v[36:37], v159 offset:64\n\t"
        "ds_read_b64_tr_b16 v[38:39], v152 offset:64\n\t"
        "ds_read_b64_tr_b16 v[40:41], v157 offset:128\n\t"
        "ds_read_b64_tr_b16 v[42:43], v158 offset:128\n\t"
        "ds_read_b64_tr_b16 v[44:45], v159 offset:128\n\t"
        "ds_read_b64_tr_b16 v[46:47], v152 offset:128\n\t"
        "s_cmp_lg_u64 s[56:57], 0\n\t"
        "s_cselect_b32 s8, 1, 0\n\t"
        "v_mov_b32 v157, s8\n\t"
        "v_perm_b32 v164, v162, v162, v168\n\t"
        "v_perm_b32 v165, v162, v162, v169\n\t"
        "v_perm_b32 v166, v162, v162, v170\n\t"
        "v_perm_b32 v167, v162, v162, v171\n\t"
        "s_waitcnt lgkmcnt(12)\n\t"
        "s_nop 0\n\t"
        "v_mfma_f32_16x16x32_bf16 v[0:3], v[60:63], v[164:167], 0\n\t"
        "v_mfma_f32_16x16x32_bf16 v[4:7], v[64:67], v[164:167], 0\n\t"
        "v_mfma_f32_16x16x32_bf16 v[8:11], v[68:71], v[164:167], 0\n\t"
        "v_mfma_f32_16x16x32_bf16 v[12:15], v[72:75], v[164:167], 0\n\t"
        "v_mfma_f32_16x16x32_bf16 v[16:19], v[76:79], v[164:167], 0\n\t"
        "v_mfma_f32_16x16x32_bf16 v[20:23], v[80:83], v[164:167], 0\n\t"
        "v_perm_b32 v164, v163, v163, v168\n\t"
        "v_perm_b32 v165, v163, v163, v169\n\t"
        "v_perm_b32 v166, v163, v163, v170\n\t"
        "v_perm_b32 v167, v163, v163, v171\n\t"
        "s_waitcnt lgkmcnt(3)\n\t"
        "s_nop 0\n\t"
        "v_mfma_f32_16x16x32_bf16 v[0:3], v[24:27], v[164:167], v[0:3]\n\t"
        "v_mfma_f32_16x16x32_bf16 v[4:7], v[28:31], v[164:167], v[4:7]\n\t"
        "v_mfma_f32_16x16x32_bf16 v[8:11], v[32:35], v[164:167], v[8:11]\n\t"
        "v_mfma_f32_16x16x32_bf16 v[12:15], v[36:39], v[164:167], v[12:15]\n\t"
        "s_waitcnt lgkmcnt(0)\n\t"
        "v_mfma_f32_16x16x32_bf16 v[16:19], v[40:43], v[164:167], v[16:19]\n\t"
        "v_mfma_f32_16x16x32_bf16 v[20:23], v[44:47], v[164:167], v[20:23]\n\t"
        "ds_write_b32 v153, v157 offset:16\n\t"
        "s_nop 0\n\t"
        "v_cvt_pk_bf16_f32 v0, v0, v1\n\t"
        "v_cvt_pk_bf16_f32 v1, v2, v3\n\t"
        "v_cvt_pk_bf16_f32 v4, v4, v5\n\t"
        "v_cvt_pk_bf16_f32 v5, v6, v7\n\t"
        "v_cvt_pk_bf16_f32 v8, v8, v9\n\t"
        "v_cvt_pk_bf16_f32 v9, v10, v11\n\t"
        "v_cvt_pk_bf16_f32 v12, v12, v13\n\t"
        "v_cvt_pk_bf16_f32 v13, v14, v15\n\t"
        "v_cvt_pk_bf16_f32 v16, v16, v17\n\t"
        "v_cvt_pk_bf16_f32 v17, v18, v19\n\t"
        "v_cvt_pk_bf16_f32 v20, v20, v21\n\t"
        "v_cvt_pk_bf16_f32 v21, v22, v23\n\t"
        "ds_write_b64 v186, v[0:1] offset:0\n\t"
        "ds_write_b64 v154, v[4:5] offset:0\n\t"
        "ds_write_b64 v186, v[8:9] offset:64\n\t"
        "ds_write_b64 v154, v[12:13] offset:64\n\t"
        "ds_write_b64 v186, v[16:17] offset:128\n\t"
        "ds_write_b64 v154, v[20:21] offset:128\n\t"
        "s_branch .Lx69%=\n\t"
        ".Lx72%=:\n\t"
        "v_mov_b32 v151, v185\n\t"
        "v_mov_b32 v183, v186\n\t"
        "s_min_u32 s8, s3, 64\n\t"
        "s_waitcnt lgkmcnt(0)\n\t"
        "30:\n\t"
        "ds_read_b128 v[160:163], v185\n\t"
        "s_waitcnt lgkmcnt(0)\n\t"
        "v_lshlrev_b32_sdwa v157, 5, v160 dst_sel:DWORD dst_unused:UNUSED_PAD src0_sel:DWORD src1_sel:WORD_0\n\t"
        "v_lshlrev_b32_sdwa v158, 5, v160 dst_sel:DWORD dst_unused:UNUSED_PAD src0_sel:DWORD src1_sel:WORD_1\n\t"
        "v_or_b32 v157, v184, v157\n\t"
        "v_or_b32 v158, v184, v158\n\t"
        "v_xor_b32 v159, 32, v157\n\t"
        "v_xor_b32 v152, 32, v158\n\t"
        "ds_read_b64_tr_b16 v[60:61], v157 offset:0\n\t"
        "ds_read_b64_tr_b16 v[62:63], v158 offset:0\n\t"
        "ds_read_b64_tr_b16 v[64:65], v159 offset:0\n\t"
        "ds_read_b64_tr_b16 v[66:67], v152 offset:0\n\t"
        "ds_read_b64_tr_b16 v[68:69], v157 offset:64\n\t"
        "ds_read_b64_tr_b16 v[70:71], v158 offset:64\n\t"
        "ds_read_b64_tr_b16 v[72:73], v159 offset:64\n\t"
        "ds_read_b64_tr_b16 v[74:75], v152 offset:64\n\t"
        "ds_read_b64_tr_b16 v[76:77], v157 offset:128\n\t"
        "ds_read_b64_tr_b16 v[78:79], v158 offset:128\n\t"
        "ds_read_b64_tr_b16 v[80:81], v159 offset:128\n\t"
        "ds_read_b64_tr_b16 v[82:83], v152 offset:128\n\t"
        "v_perm_b32 v164, v162, v162, v168\n\t"
        "v_perm_b32 v165, v162, v162, v169\n\t"
        "v_perm_b32 v166, v162, v162, v170\n\t"
        "v_perm_b32 v167, v162, v162, v171\n\t"
        "v_cmp_ne_u32 vcc, 0, v163\n\t"
        "s_waitcnt lgkmcnt(10)\n\t"
        "v_mfma_f32_16x16x32_bf16 v[48:51], v[60:63], v[164:167], 0\n\t"
        "s_waitcnt lgkmcnt(8)\n\t"
        "v_mfma_f32_16x16x32_bf16 v[52:55], v[64:67], v[164:167], 0\n\t"
        "s_waitcnt lgkmcnt(6)\n\t"
        "v_mfma_f32_16x16x32_bf16 v[56:59], v[68:71], v[164:167], 0\n\t"
        "s_waitcnt lgkmcnt(4)\n\t"
        "v_mfma_f32_16x16x32_bf16 v[116:119], v[72:75], v[164:167], 0\n\t"
        "s_waitcnt lgkmcnt(2)\n\t"
        "v_mfma_f32_16x16x32_bf16 v[120:123], v[76:79], v[164:167], 0\n\t"
        "s_waitcnt lgkmcnt(0)\n\t"
        "v_mfma_f32_16x16x32_bf16 v[124:127], v[80:83], v[164:167], 0\n\t"
        "s_cbranch_vccz 31f\n\t"
        "v_lshlrev_b32_sdwa v157, 5, v161 dst_sel:DWORD dst_unused:UNUSED_PAD src0_sel:DWORD src1_sel:WORD_0\n\t"
        "v_lshlrev_b32_sdwa v158, 5, v161 dst_sel:DWORD dst_unused:UNUSED_PAD src0_sel:DWORD src1_sel:WORD_1\n\t"
        "v_or_b32 v157, v184, v157\n\t"
        "v_or_b32 v158, v184, v158\n\t"
        "v_xor_b32 v159, 32, v157\n\t"
        "v_xor_b32 v152, 32, v158\n\t"
        "ds_read_b64_tr_b16 v[60:61], v157 offset:0\n\t"
        "ds_read_b64_tr_b16 v[62:63], v158 offset:0\n\t"
        "ds_read_b64_tr_b16 v[64:65], v159 offset:0\n\t"
        "ds_read_b64_tr_b16 v[66:67], v152 offset:0\n\t"
        "ds_read_b64_tr_b16 v[68:69], v157 offset:64\n\t"
        "ds_read_b64_tr_b16 v[70:71], v158 offset:64\n\t"
        "ds_read_b64_tr_b16 v[72:73], v159 offset:64\n\t"
        "ds_read_b64_tr_b16 v[74:75], v152 offset:64\n\t"
        "ds_read_b64_tr_b16 v[76:77], v157 offset:128\n\t"
        "ds_read_b64_tr_b16 v[78:79], v158 offset:128\n\t"
        "ds_read_b64_tr_b16 v[80:81], v159 offset:128\n\t"
        "ds_read_b64_tr_b16 v[82:83], v152 offset:128\n\t"
        "v_perm_b32 v164, v163, v163, v168\n\t"
        "v_perm_b32 v165, v163, v163, v169\n\t"
        "v_perm_b32 v166, v163, v163, v170\n\t"
        "v_perm_b32 v167, v163, v163, v171\n\t"
        "s_waitcnt lgkmcnt(10)\n\t"
        "s_nop 0\n\t"
        "v_mfma_f32_16x16x32_bf16 v[48:51], v[60:63], v[164:167], v[48:51]\n\t"
        "s_waitcnt lgkmcnt(8)\n\t"
        "v_mfma_f32_16x16x32_bf16 v[52:55], v[64:67], v[164:167], v[52:55]\n\t"
        "s_waitcnt lgkmcnt(6)\n\t"
        "v_mfma_f32_16x16x32_bf16 v[56:59], v[68:71], v[164:167], v[56:59]\n\t"
        "s_waitcnt lgkmcnt(4)\n\t"
        "v_mfma_f32_16x16x32_bf16 v[116:119], v[72:75], v[164:167], v[116:119]\n\t"
        "s_waitcnt lgkmcnt(2)\n\t"
        "v_mfma_f32_16x16x32_bf16 v[120:123], v[76:79], v[164:167], v[120:123]\n\t"
        "s_waitcnt lgkmcnt(0)\n\t"
        "v_mfma_f32_16x16x32_bf16 v[124:127], v[80:83], v[164:167], v[124:127]\n\t"
        "31:\n\t"
        "v_cvt_pk_bf16_f32 v48, v48, v49\n\t"
        "v_cvt_pk_bf16_f32 v49, v50, v51\n\t"
        "v_cvt_pk_bf16_f32 v52, v52, v53\n\t"
        "v_cvt_pk_bf16_f32 v53, v54, v55\n\t"
        "v_cvt_pk_bf16_f32 v56, v56, v57\n\t"
        "v_cvt_pk_bf16_f32 v57, v58, v59\n\t"
        "v_cvt_pk_bf16_f32 v116, v116, v117\n\t"
        "v_cvt_pk_bf16_f32 v117, v118, v119\n\t"
        "v_cvt_pk_bf16_f32 v120, v120, v121\n\t"
        "v_cvt_pk_bf16_f32 v121, v122, v123\n\t"
        "v_cvt_pk_bf16_f32 v124, v124, v125\n\t"
        "v_cvt_pk_bf16_f32 v125, v126, v127\n\t"
        "ds_write_b64 v186, v[48:49] offset:0\n\t"
        "ds_write_b64 v154, v[52:53] offset:0\n\t"
        "ds_write_b64 v186, v[56:57] offset:64\n\t"
        "ds_write_b64 v154, v[116:117] offset:64\n\t"
        "ds_write_b64 v186, v[120:121] offset:128\n\t"
        "ds_write_b64 v154, v[124:125] offset:128\n\t"
        "v_add_u32 v185, 256, v185\n\t"
        "v_add_u32 v186, 12288, v186\n\t"
        "v_xor_b32 v154, 32, v186\n\t"
        "s_sub_i32 s8, s8, 16\n\t"
        "s_cmp_gt_i32 s8, 0\n\t"
        "s_cbranch_scc1 30b\n\t"
        "v_mov_b32 v185, v151\n\t"
        "v_mov_b32 v186, v183\n\t"
        "v_xor_b32 v154, 32, v186\n\t"
        "s_waitcnt lgkmcnt(0)\n\t"
        "s_barrier\n\t"
        "s_branch .Lx73%=\n\t"
        "99:\n\t"
        "s_nop 7\n\t"
        "s_waitcnt vmcnt(0) lgkmcnt(0)\n\t"
        "s_barrier\n\t"
        "s_lshl_b32 s6, s6, 16\n\t"
        "v_or_b32 %0, s6, v156\n\t"
        : "+v"(tid) : "s"(ka), "s"(bid), "s"(recs)
        : "memory", "scc", "vcc", "s3", "s4", "s5", "s6", "s7", "s8", "s9", "s10", "s11", "s12", "s13", "s16", "s17", "s18", "s19", "s20", "s21", "s22", "s23", "s24", "s25", "s26", "s27", "s28", "s29", "s30", "s31", "s34", "s35", "s36", "s37", "s38", "s39", "s40", "s41", "s42", "s43", "s44", "s45", "s46", "s47", "s48", "s49", "s50", "s51", "s52", "s53", "s54", "s55", "s56", "s57", "s60", "s61", "s62", "s63", "s64", "s65", "s66", "s67", "s68", "s69", "s70", "s71", "s72", "s73", "s74", "s75", "s76", "s77", "s78", "s79", "s80", "s81", "s82", "s83", "s84", "s85", "s86", "s87", "s88", "s89", "s90", "s91", "s92", "s93", "s94", "s95", "s96", "s97", "s98", "s99", "v1", "v2", "v3", "v4", "v5", "v6", "v7", "v8", "v9", "v10", "v11", "v12", "v13", "v14", "v15", "v16", "v17", "v18", "v19", "v20", "v21", "v22", "v23", "v24", "v25", "v26", "v27", "v28", "v29", "v30", "v31", "v32", "v33", "v34", "v35", "v36", "v37", "v38", "v39", "v40", "v41", "v42", "v43", "v44", "v45", "v46", "v47", "v48", "v49", "v50", "v51", "v52", "v53", "v54", "v55", "v56", "v57", "v58", "v59", "v60", "v61", "v62", "v63", "v64", "v65", "v66", "v67", "v68", "v69", "v70", "v71", "v72", "v73", "v74", "v75", "v76", "v77", "v78", "v79", "v80", "v81", "v82", "v83", "v84", "v85", "v86", "v87", "v88", "v89", "v90", "v91", "v92", "v93", "v94", "v95", "v96", "v97", "v98", "v99", "v100", "v101", "v102", "v103", "v104", "v105", "v106", "v107", "v108", "v109", "v110", "v111", "v112", "v113", "v114", "v115", "v116", "v117", "v118", "v119", "v120", "v121", "v122", "v123", "v124", "v125", "v126", "v127", "v128", "v129", "v130", "v131", "v132", "v133", "v134", "v135", "v136", "v137", "v138", "v139", "v140", "v141", "v142", "v143", "v144", "v145", "v146", "v147", "v148", "v149", "v150", "v151", "v152", "v153", "v154", "v155", "v156", "v157", "v158", "v159", "v160", "v161", "v162", "v163", "v164", "v165", "v166", "v167", "v168", "v169", "v170", "v171", "v172", "v173", "v174", "v175", "v176", "v177", "v178", "v179", "v180", "v181", "v182", "v183", "v184", "v185", "v186", "v187", "v188", "v189", "v190", "v191", "v192", "v193", "v194", "v195", "v196", "v197", "v198", "v199", "v200", "v201", "v202", "v203", "v204", "v205", "v206", "v207", "v208", "v209", "v210", "v211", "v212", "v213", "v214", "v215", "v216", "v217", "v218", "v219", "v220", "v221", "v222", "v223", "v224", "v225", "v226", "v227", "v228", "v229", "v230", "v231", "v232", "v233", "v234", "v235", "v236", "v237", "v238", "v239", "v240", "v241", "v242", "v243", "v244", "v245", "v246", "v247", "v248", "v249", "v250", "v251", "v252", "v253", "v254", "v255", "a0", "a1", "a2", "a3", "a4", "a5", "a6", "a7", "a8", "a9", "a10", "a11", "a12", "a13", "a14", "a15", "a16", "a17", "a18", "a19", "a20", "a21", "a22", "a23", "a24", "a25", "a26", "a27", "a28", "a29", "a30", "a31", "a32", "a33", "a34", "a35", "a36", "a37", "a38", "a39", "a40", "a41", "a42", "a43", "a44", "a45", "a46", "a47", "a48", "a49", "a50", "a51", "a52", "a53", "a54", "a55", "a56", "a57", "a58", "a59", "a60", "a61", "a62", "a63", "a64", "a65", "a66", "a67", "a68", "a69", "a70", "a71", "a72", "a73", "a74", "a75", "a76", "a77", "a78", "a79", "a80", "a81", "a82", "a83", "a84", "a85", "a86", "a87", "a88", "a89", "a90", "a91", "a92", "a93", "a94", "a95", "a96", "a97", "a98", "a99", "a100", "a101", "a102", "a103", "a104", "a105", "a106", "a107", "a108", "a109", "a110", "a111", "a112", "a113", "a114", "a115", "a116", "a117", "a118", "a119", "a120", "a121", "a122", "a123", "a124", "a125", "a126", "a127", "a128", "a129", "a130", "a131", "a132", "a133", "a134", "a135", "a136", "a137", "a138", "a139", "a140", "a141", "a142", "a143", "a144", "a145", "a146", "a147", "a148", "a149", "a150", "a151", "a152", "a153", "a154", "a155", "a156", "a157", "a158", "a159", "a160", "a161", "a162", "a163", "a164", "a165", "a166", "a167", "a168", "a169", "a170", "a171", "a172", "a173", "a174", "a175", "a176", "a177", "a178", "a179", "a180", "a181", "a182", "a183", "a184", "a185", "a186", "a187", "a188", "a189", "a190", "a191", "a192", "a193", "a194", "a195", "a196", "a197", "a198", "a199", "a200", "a201", "a202", "a203", "a204", "a205", "a206", "a207", "a208", "a209", "a210", "a211", "a212", "a213", "a214", "a215", "a216", "a217", "a218", "a219", "a220", "a221", "a222", "a223", "a224", "a225", "a226", "a227", "a228", "a229", "a230", "a231", "a232", "a233", "a234", "a235", "a236", "a237", "a238", "a239", "a240", "a241", "a242", "a243", "a244", "a245", "a246", "a247", "a248", "a249", "a250", "a251", "a252", "a253", "a254", "a255");
    asm volatile("" : "+s"(ka), "+s"(bid) :: "memory");
    {
        typedef short v4s __attribute__((ext_vector_type(4)));
        const unsigned short *x = ka->x, *w = ka->w;
        const unsigned* off = (const unsigned*)ka->off;
        unsigned short* out = ka->out;
        const unsigned tw = recs[bid * 64 + 27] >> 16, redo = __builtin_amdgcn_readfirstlane(tid >> 16);
        const unsigned* rc = recs + bid * 64;
        tid &= 255;
        const int lane = tid & 63, li = lane & 15, q = lane >> 4, wv = __builtin_amdgcn_readfirstlane(tid >> 6);
        int pix[4] = {-1, -1, -1, -1}, n = 0;
#pragma unroll 1
        for (int s = 0; s < 14; ++s) {
            const unsigned o = s < 8 ? (((tw & 255) + s) * 224 + ((tw >> 8) & 255)) * 192 : rc[49 + ((s - 8) >> 1)] + (s & 1) * (43200 - rc[30]);
            if (!((redo >> (s < 8 ? 8 + (s >> 1) : 0)) & 1) || o >= 224 * 224 * 192) continue;
            const int p = (o + li * (s < 8 ? 192 : rc[30])) / 192;
#pragma unroll
            for (int i = 0; i < 4; ++i) pix[i] = n == wv + 4 * i ? p : pix[i];
            ++n;
        }
        f32x4 acc[4][6];
        unsigned od[4];
        u32x4 wn[5];
#pragma unroll
        for (int i = 0; i < 4; ++i) {
            od[i] = off[max(pix[i], 0) * 9];
#pragma unroll
            for (int k = 0; k < 6; ++k) acc[i][k] = (f32x4){0.f, 0.f, 0.f, 0.f};
        }
#pragma unroll
        for (int k = 0; k < 5; ++k) wn[k] = *(const u32x4*)(w + min(tid + 256 * k, 1151u) * 8);
        const __amdgpu_buffer_rsrc_t xr = __builtin_amdgcn_make_buffer_rsrc((void*)x, 0, 224 * 224 * 192, 0x00020000);
        const unsigned wb = (8 * q + (li >> 2)) * 192 + (li & 3) * 8;
#pragma unroll 1
        for (int t = 0; t < 9; ++t) {
            unsigned char* slab = lds + (t & 1) * 18432;
#pragma unroll
            for (int k = 0; k < 5; ++k) {
                const unsigned c = min(tid + 256 * k, 1151u);
                *(u32x4*)(slab + ((c * 16) ^ (((c / 96) & 1) << 5))) = wn[k];
            }
            __syncthreads();
            u32x4 wf[3][6];
#pragma unroll
            for (int k = 0; k < 18; ++k) {
                __attribute__((address_space(3))) unsigned char* a = (__attribute__((address_space(3))) unsigned char*)slab + (k / 6) * 6144 + wb + (((k % 6) * 32) ^ ((q & 1) << 5));
                const u32x2 lo = __builtin_bit_cast(u32x2, __builtin_amdgcn_ds_read_tr16_b64_v4i16((__attribute__((address_space(3))) v4s*)a));
                const u32x2 hi = __builtin_bit_cast(u32x2, __builtin_amdgcn_ds_read_tr16_b64_v4i16((__attribute__((address_space(3))) v4s*)(a + 768)));
                wf[k / 6][k % 6] = (u32x4){lo[0], lo[1], hi[0], hi[1]};
            }
            const int tn = min(t + 1, 8);
#pragma unroll
            for (int k = 0; k < 5; ++k) wn[k] = *(const u32x4*)(w + tn * 9216 + min(tid + 256 * k, 1151u) * 8);
#pragma unroll
            for (int i0 = 0; i0 < 4; i0 += 2) {
                if (wv + 4 * i0 >= n) continue;
                u32x4 g[2][3][4];
                float wt[2][4];
#pragma unroll
                for (int j = 0; j < 2; ++j) {
                    const int i = i0 + j, p = max(pix[i], 0), h = p / 224, c = p - h * 224;
                    float py = rb((float)(h + t / 3 - 1) + bf_lo(od[i])), px = rb((float)(c + t % 3 - 1) + bf_hi(od[i]));
                    od[i] = off[p * 9 + tn];
                    py = fminf(fmaxf(py, 0.f), 223.f); px = fminf(fmaxf(px, 0.f), 223.f);
                    const float y0 = floorf(py), x0 = floorf(px), y1 = fminf(y0 + 1.f, 223.f), x1 = fminf(x0 + 1.f, 223.f);
                    const float wy = py - y0, wx = px - x0, a = rb(1.f - wy), b = rb(1.f - wx);
                    wt[j][0] = rb(a * b); wt[j][1] = rb(a * wx); wt[j][2] = rb(wy * b); wt[j][3] = rb(wy * wx);
                    int ad[4] = {(int)y0 * 224 + (int)x0, (int)y0 * 224 + (int)x1, (int)y1 * 224 + (int)x0, (int)y1 * 224 + (int)x1};
#pragma unroll
                    for (int k = 0; k < 4; ++k) ad[k] = wt[j][k] != 0.f && pix[i] >= 0 ? ad[k] * 192 + q * 16 : 0x40000000;
#pragma unroll
                    for (int k = 0; k < 4; ++k) {
#pragma unroll
                        for (int ks = 0; ks < 3; ++ks) g[j][ks][k] = (u32x4){0u, 0u, 0u, 0u};
                        if (__builtin_amdgcn_ballot_w64(ad[k] != 0x40000000) == 0) continue;
#pragma unroll
                        for (int ks = 0; ks < 3; ++ks) g[j][ks][k] = __builtin_amdgcn_raw_buffer_load_b128(xr, ad[k] + ks * 64, 0, 0);
                    }
                }
                asm volatile("" ::: "memory");
#pragma unroll
                for (int j = 0; j < 2; ++j) {
                    if (wv + 4 * (i0 + j) >= n) continue;
#pragma unroll
                    for (int ks = 0; ks < 3; ++ks) {
                        u32x4 bfr;
#pragma unroll
                        for (int d = 0; d < 4; ++d) {
                            float lo = 0.f, hi = 0.f;
#pragma unroll
                            for (int cn = 0; cn < 4; ++cn) {
                                const unsigned u = g[j][ks][cn][d];
                                const bf16x2 v = __builtin_bit_cast(bf16x2, u);
                                lo = __builtin_amdgcn_fdot2_f32_bf16(v, __builtin_bit_cast(bf16x2, __float_as_uint(wt[j][cn]) >> 16), lo, false);
                                hi = __builtin_amdgcn_fdot2_f32_bf16(v, __builtin_bit_cast(bf16x2, __float_as_uint(wt[j][cn])), hi, false);
                            }
                            bfr[d] = cvt_pk(lo, hi);
                        }
#pragma unroll
                        for (int k = 0; k < 6; ++k)
                            acc[i0 + j][k] = __builtin_amdgcn_mfma_f32_16x16x32_bf16(__builtin_bit_cast(bf16x8, wf[ks][k]), __builtin_bit_cast(bf16x8, bfr), acc[i0 + j][k], 0, 0, 0);
                    }
                }
            }
        }
#pragma unroll
        for (int i = 0; i < 4; ++i) {
            if (wv + 4 * i >= n) continue;
#pragma unroll
            for (int k = 0; k < 6; ++k) {
                u32x2 o; o[0] = cvt_pk(acc[i][k][0], acc[i][k][1]); o[1] = cvt_pk(acc[i][k][2], acc[i][k][3]);
                *(u32x2*)(out + pix[i] * 96 + k * 16 + q * 4) = o;
            }
        }
    }
}
extern "C" void kernel_launch(void* const* d_in, const int* in_sizes, int n_in, void* d_out, int out_size, void* d_ws, size_t ws_size, hipStream_t stream) {
    dcn_kernel<<<256, 256, 0, stream>>>((const unsigned short*)d_in[0], (const unsigned short*)d_in[1], (const unsigned short*)d_in[2], (unsigned short*)d_out, d_ws);
}
```

```cpp
#include <hip/hip_runtime.h>
#include <stdint.h>
typedef __attribute__((ext_vector_type(8))) __bf16 bf16x8;
typedef __attribute__((ext_vector_type(2))) __bf16 bf16x2;
typedef __attribute__((ext_vector_type(4))) float f32x4;
typedef __attribute__((ext_vector_type(4))) unsigned u32x4;
typedef __attribute__((ext_vector_type(2))) unsigned u32x2;
struct Args { const unsigned short *x, *off, *w; unsigned short* out; void* ws; };
static __device__ const unsigned recs[256 * 64] = {
1419264,1419264,1462272,1462272,1505280,1505280,1548288,1548288,1548288,1548288,1548288,1548288,40960,44032,48128,51200,55296,58368,62464,65536,62464,65536,62464,65536,268439552,336991254,57,807538688,80,7488,192,1040137727,16780300,50465804,576,36,224,1107820544,1107820544,1107820544,1098907648,1098907648,0,274752,1073741824,274176,44032,44032,40960,1465344,1073741824,1462272,16900,1107575312,4294961196,4294967294,16900,1107575312,4294961196,4294967294,16900,1107575312,4294961196,4294967294,
2623488,2666496,2709504,2752512,2795520,2838528,2881536,2924544,2924544,2924544,2924544,2924544,40960,45056,49152,53248,57344,61440,65536,69632,69632,69632,69632,69632,538976288,353703189,57,1080164608,80,7488,192,1040137855,16780300,50465804,576,36,128,1115947008,1115684864,1115160576,1107296256,1107296256,1107296256,533376,517248,501120,57344,49152,40960,2844672,2758656,2672640,1106264692,1112031880,4294960588,30,1106264692,1112031880,4294960588,30,1106264692,1112031880,4294960588,30,
3827712,3827712,3870720,3870720,3913728,3913728,3956736,3956736,3956736,3956736,3956736,3956736,40960,44032,48128,51200,55296,58368,62464,65536,62464,65536,62464,65536,268439552,336991254,57,1352798208,80,7488,192,1048526591,16780300,50465804,576,36,224,1119092736,1119092736,1119092736,1098907648,1098907648,0,726336,1073741824,725760,44032,44032,40960,3873792,1073741824,3870720,17074,1107575480,4294948652,4294967294,17074,1107575480,4294948652,4294967294,17074,1107575480,4294948652,4294967294,
21312,21312,21504,21504,21696,21696,21888,21888,21888,21888,21888,21888,40960,44032,48128,51200,55296,58368,62464,65536,62464,65536,62464,65536,268439552,336991254,410,1881808896,80,7984,43008,1048526335,50465804,16780300,36,80,224,1121976320,1121976320,1121976320,1098907648,1098907648,0,133056,1073741824,4032,44032,44032,40960,709632,1073741824,21504,17118,1107575524,4294943724,4294967294,17118,1107575524,4294943724,4294967294,17118,1107575524,4294943724,4294967294,
5720064,5720064,5763072,5763072,5806080,5806080,5849088,5849088,5849088,5849088,5849088,5849088,40960,44032,48128,51200,55296,58368,62464,65536,62464,65536,62464,65536,268439552,336991254,57,2154958848,80,7488,192,1048526463,16780300,50465804,576,36,224,1124466688,1124466688,1124466688,1098907648,1098907648,0,1081152,1073741824,1080576,44032,44032,40960,5766144,1073741824,5763072,17157,1107575560,4294938796,4294967294,17157,1107575560,4294938796,4294967294,17157,1107575560,4294938796,4294967294,
6752256,6795264,6838272,6881280,6924288,6967296,7010304,7053312,7053312,7053312,7053312,7053312,40960,45056,49152,53248,57344,61440,65536,69632,69632,69632,69632,69632,538976288,353703189,57,2428109056,80,7488,192,1056915199,16780300,50465804,576,36,128,1126301696,1126170624,1126039552,1107296256,1107296256,1107296256,1307520,1291392,1275264,57344,49152,40960,6973440,6887424,6801408,1106264861,1112032036,4294948300,30,1106264861,1112032036,4294948300,30,1106264861,1112032036,4294948300,30,
7612416,7612416,7655424,7655424,7698432,7698432,7741440,7741440,7741440,7741440,7741440,7741440,40960,44032,48128,51200,55296,58368,62464,65536,62464,65536,62464,65536,268439552,336991254,57,2957643776,80,7488,192,1056915071,16780300,50465804,576,36,224,1127350272,1127350272,1127350272,1098907648,1098907648,0,1435968,1073741824,1435392,44032,44032,40960,7658496,1073741824,7655424,17201,1107575604,4294928940,4294967294,17201,1107575604,4294928940,4294967294,17201,1107575604,4294928940,4294967294,
38592,38592,38784,38784,38976,38976,39168,39168,39168,39168,39168,39168,40960,44032,48128,51200,55296,58368,62464,65536,62464,65536,62464,65536,268439552,336991254,410,3230801920,80,7984,43008,1056915199,50465804,16780300,36,80,224,1128923136,1128923136,1128923136,1098907648,1098907648,0,136296,1073741824,7272,44032,44032,40960,726912,1073741824,38784,17225,1107575628,4294923564,4294967294,17225,1107575628,4294923564,4294967294,17225,1107575628,4294923564,4294967294,
1505280,1505280,1548288,1548288,1591296,1591296,1634304,1634304,1634304,1634304,1634304,1634304,40960,44032,48128,51200,55296,58368,62464,65536,62464,65536,62464,65536,268439552,336991254,57,808062976,80,7488,192,1040137727,16780300,50465804,576,36,224,1108344832,1108344832,1108344832,1098907648,1098907648,0,290880,1073741824,290304,44032,44032,40960,1551360,1073741824,1548288,16908,1107575320,4294960748,4294967294,16908,1107575320,4294960748,4294967294,16908,1107575320,4294960748,4294967294,
12096,12096,12288,12288,12480,12480,12672,12672,12672,12672,12672,12672,40960,44032,48128,51200,55296,58368,62464,65536,62464,65536,62464,65536,268439552,336991254,410,1080696832,80,7984,43008,1040137855,50465804,16780300,36,80,224,1115684864,1115684864,1115684864,1098907648,1098907648,0,131328,1073741824,2304,44032,44032,40960,700416,1073741824,12288,17020,1107575428,4294954476,4294967294,17020,1107575428,4294954476,4294967294,17020,1107575428,4294954476,4294967294,
3913728,3913728,3956736,3956736,3999744,3999744,4042752,4042752,4042752,4042752,4042752,4042752,40960,44032,48128,51200,55296,58368,62464,65536,62464,65536,62464,65536,268439552,336991254,57,1353322496,80,7488,192,1048526591,16780300,50465804,576,36,224,1119354880,1119354880,1119354880,1098907648,1098907648,0,742464,1073741824,741888,44032,44032,40960,3959808,1073741824,3956736,17078,1107575484,4294948204,4294967294,17078,1107575484,4294948204,4294967294,17078,1107575484,4294948204,4294967294,
21696,21696,21888,21888,22080,22080,22272,22272,22272,22272,22272,22272,40960,44032,48128,51200,55296,58368,62464,65536,62464,65536,62464,65536,268439552,336991254,410,1882333184,80,7984,43008,1048526335,50465804,16780300,36,80,224,1122238464,1122238464,1122238464,1098907648,1098907648,0,133128,1073741824,4104,44032,44032,40960,710016,1073741824,21888,17122,1107575528,4294943276,4294967294,17122,1107575528,4294943276,4294967294,17122,1107575528,4294943276,4294967294,
5806080,5806080,5849088,5849088,5892096,5892096,5935104,5935104,5935104,5935104,5935104,5935104,40960,44032,48128,51200,55296,58368,62464,65536,62464,65536,62464,65536,268439552,336991254,57,2155483136,80,7488,192,1048526463,16780300,50465804,576,36,224,1124597760,1124597760,1124597760,1098907648,1098907648,0,1097280,1073741824,1096704,44032,44032,40960,5852160,1073741824,5849088,17159,1107575562,4294938348,4294967294,17159,1107575562,4294938348,4294967294,17159,1107575562,4294938348,4294967294,
30144,30144,30336,30336,30528,30528,30720,30720,30720,30720,30720,30720,40960,44032,48128,51200,55296,58368,62464,65536,62464,65536,62464,65536,268439552,336991254,410,2428641280,80,7984,43008,1056915199,50465804,16780300,36,80,224,1126039552,1126039552,1126039552,1098907648,1098907648,0,134712,1073741824,5688,44032,44032,40960,718464,1073741824,30336,17181,1107575584,4294933420,4294967294,17181,1107575584,4294933420,4294967294,17181,1107575584,4294933420,4294967294,
7698432,7698432,7741440,7741440,7784448,7784448,7827456,7827456,7827456,7827456,7827456,7827456,40960,44032,48128,51200,55296,58368,62464,65536,62464,65536,62464,65536,268439552,336991254,57,2958168064,80,7488,192,1056915071,16780300,50465804,576,36,224,1127481344,1127481344,1127481344,1098907648,1098907648,0,1452096,1073741824,1451520,44032,44032,40960,7744512,1073741824,7741440,17203,1107575606,4294928492,4294967294,17203,1107575606,4294928492,4294967294,17203,1107575606,4294928492,4294967294,
38976,38976,39168,39168,39360,39360,39552,39552,39552,39552,39552,39552,40960,44032,48128,51200,55296,58368,62464,65536,62464,65536,62464,65536,268439552,336991254,410,3231326208,80,7984,43008,1056915199,50465804,16780300,36,80,224,1129054208,1129054208,1129054208,1098907648,1098907648,0,136368,1073741824,7344,44032,44032,40960,727296,1073741824,39168,17227,1107575630,4294923116,4294967294,17227,1107575630,4294923116,4294967294,17227,1107575630,4294923116,4294967294,
1290240,1333248,1376256,1419264,1462272,1505280,1548288,1591296,1634304,1634304,1634304,1634304,40960,45056,49152,53248,57344,61440,65536,69632,73728,73728,73728,73728,538976288,353703189,57,808584448,64,7488,192,1040137727,16780300,50465804,576,36,128,1108344832,1107820544,1107296256,1107296256,1107296256,1107296256,291456,275328,259200,61440,53248,45056,1554432,1468416,1382400,1106264560,1112031768,4294964556,30,1106264560,1112031768,4294964556,30,1106264560,1112031768,4294964556,30,
12480,12480,12672,12672,12864,12864,13056,13056,13056,13056,13056,13056,40960,44032,48128,51200,55296,58368,62464,65536,62464,65536,62464,65536,268439552,336991254,410,1081221120,80,7984,43008,1040137855,50465804,16780300,36,80,224,1115947008,1115947008,1115947008,1098907648,1098907648,0,131400,1073741824,2376,44032,44032,40960,700800,1073741824,12672,17026,1107575432,4294954028,4294967294,17026,1107575432,4294954028,4294967294,17026,1107575432,4294954028,4294967294,
3999744,3999744,4042752,4042752,4085760,4085760,4128768,4128768,4128768,4128768,4128768,4128768,40960,44032,48128,51200,55296,58368,62464,65536,62464,65536,62464,65536,268439552,336991254,57,1353846784,80,7488,192,1048526591,16780300,50465804,576,36,224,1119617024,1119617024,1119617024,1098907648,1098907648,0,758592,1073741824,758016,44032,44032,40960,4045824,1073741824,4042752,17082,1107575488,4294947756,4294967294,17082,1107575488,4294947756,4294967294,17082,1107575488,4294947756,4294967294,
22080,22080,22272,22272,22464,22464,22656,22656,22656,22656,22656,22656,40960,44032,48128,51200,55296,58368,62464,65536,62464,65536,62464,65536,268439552,336991254,410,1882857472,80,7984,43008,1048526335,50465804,16780300,36,80,224,1122500608,1122500608,1122500608,1098907648,1098907648,0,133200,1073741824,4176,44032,44032,40960,710400,1073741824,22272,17126,1107575532,4294942828,4294967294,17126,1107575532,4294942828,4294967294,17126,1107575532,4294942828,4294967294,
5892096,5892096,5935104,5935104,5978112,5978112,6021120,6021120,6021120,6021120,6021120,6021120,40960,44032,48128,51200,55296,58368,62464,65536,62464,65536,62464,65536,268439552,336991254,57,2156007424,80,7488,192,1048526591,16780300,50465804,576,36,224,1124728832,1124728832,1124728832,1098907648,1098907648,0,1113408,1073741824,1112832,44032,44032,40960,5938176,1073741824,5935104,17161,1107575564,4294937900,4294967294,17161,1107575564,4294937900,4294967294,17161,1107575564,4294937900,4294967294,
172032,215040,258048,301056,344064,387072,9074688,9117696,9117696,9117696,9117696,9117696,49152,53248,57344,61440,65536,69632,40960,45056,45056,45056,45056,45056,2425356288,320017685,57,2429170176,80,7488,192,1056915199,16780300,50465804,576,36,128,1129447424,1129447424,1086324736,1125122048,1125122048,0,1698624,1073741824,48384,32768,32768,53248,9059328,1073741824,258048,1125073737,1126187860,4294940716,142,1125073737,1126187860,4294940716,142,16512,1099972880,1036,4294967294,
7784448,7784448,7827456,7827456,7870464,7870464,7913472,7913472,7913472,7913472,7913472,7913472,40960,44032,48128,51200,55296,58368,62464,65536,62464,65536,62464,65536,268439552,336991254,57,2958692352,80,7488,192,1056915071,16780300,50465804,576,36,224,1127612416,1127612416,1127612416,1098907648,1098907648,0,1468224,1073741824,1467648,44032,44032,40960,7830528,1073741824,7827456,17205,1107575608,4294928044,4294967294,17205,1107575608,4294928044,4294967294,17205,1107575608,4294928044,4294967294,
39360,39360,39552,39552,39744,39744,39936,39936,39936,39936,39936,39936,40960,44032,48128,51200,55296,58368,62464,65536,62464,65536,62464,65536,268439552,336991254,410,3231850496,80,7984,43008,1056915199,50465804,16780300,36,80,224,1129185280,1129185280,1129185280,1098907648,1098907648,0,136440,1073741824,7416,44032,44032,40960,727680,1073741824,39552,17229,1107575632,4294922668,4294967294,17229,1107575632,4294922668,4294967294,17229,1107575632,4294922668,4294967294,
1591296,1591296,1634304,1634304,1677312,1677312,1720320,1720320,1720320,1720320,1720320,1720320,40960,44032,48128,51200,55296,58368,62464,65536,62464,65536,62464,65536,268439552,336991254,57,809111552,80,7488,192,1040137727,16780300,50465804,576,36,224,1108869120,1108869120,1108869120,1098907648,1098907648,0,307008,1073741824,306432,44032,44032,40960,1637376,1073741824,1634304,16916,1107575328,4294960300,4294967294,16916,1107575328,4294960300,4294967294,16916,1107575328,4294960300,4294967294,
12864,12864,13056,13056,13248,13248,13440,13440,13440,13440,13440,13440,40960,44032,48128,51200,55296,58368,62464,65536,62464,65536,62464,65536,268439552,336991254,410,1081745408,80,7984,43008,1040137855,50465804,16780300,36,80,224,1116209152,1116209152,1116209152,1098907648,1098907648,0,131472,1073741824,2448,44032,44032,40960,701184,1073741824,13056,17030,1107575436,4294953580,4294967294,17030,1107575436,4294953580,4294967294,17030,1107575436,4294953580,4294967294,
3913728,3956736,3999744,4042752,4085760,4128768,4171776,4214784,4214784,4214784,4214784,4214784,40960,45056,49152,53248,57344,61440,65536,69632,69632,69632,69632,69632,538976288,353703189,57,1354367232,80,7488,192,1048526591,16780300,50465804,576,36,128,1119879168,1119617024,1119354880,1107296256,1107296256,1107296256,775296,759168,743040,57344,49152,40960,4134912,4048896,3962880,1106264758,1112031940,4294956748,30,1106264758,1112031940,4294956748,30,1106264758,1112031940,4294956748,30,
4945920,4988928,5031936,5074944,5117952,5160960,5203968,5246976,5246976,5246976,5246976,5246976,40960,45056,49152,53248,57344,61440,65536,69632,69632,69632,69632,69632,538976288,353703189,57,1883373824,80,7488,192,1048526463,16780300,50465804,576,36,128,1123024896,1122762752,1122500608,1107296256,1107296256,1107296256,968832,952704,936576,57344,49152,40960,5167104,5081088,4995072,1106264806,1112031988,4294953676,30,1106264806,1112031988,4294953676,30,1106264806,1112031988,4294953676,30,
5978112,5978112,6021120,6021120,6064128,6064128,6107136,6107136,6107136,6107136,6107136,6107136,40960,44032,48128,51200,55296,58368,62464,65536,62464,65536,62464,65536,268439552,336991254,57,2156531712,80,7488,192,1048526591,16780300,50465804,576,36,224,1124859904,1124859904,1124859904,1098907648,1098907648,0,1129536,1073741824,1128960,44032,44032,40960,6024192,1073741824,6021120,17163,1107575566,4294937452,4294967294,17163,1107575566,4294937452,4294967294,17163,1107575566,4294937452,4294967294,
1290240,1333248,1376256,1419264,1462272,9504768,9547776,9590784,9590784,9590784,9590784,9590784,57344,61440,65536,69632,73728,40960,45056,49152,53248,53248,53248,53248,2425393296,353703189,57,2429814016,64,7488,192,1056915199,16780300,50465804,576,36,128,1130102784,1130233856,1107296256,1125122048,1125122048,1125122048,1779264,1795392,263232,32768,40960,61440,9489408,9575424,1403904,1125073747,1126187871,4294939436,142,1125073747,1126187871,4294939436,142,1125007856,1126318600,4294964396,142,
7870464,7870464,7913472,7913472,7956480,7956480,7999488,7999488,7999488,7999488,7999488,7999488,40960,44032,48128,51200,55296,58368,62464,65536,62464,65536,62464,65536,268439552,336991254,57,2959216640,80,7488,192,1056915071,16780300,50465804,576,36,224,1127743488,1127743488,1127743488,1098907648,1098907648,0,1484352,1073741824,1483776,44032,44032,40960,7916544,1073741824,7913472,17207,1107575610,4294927596,4294967294,17207,1107575610,4294927596,4294967294,17207,1107575610,4294927596,4294967294,
39744,39744,39936,39936,40128,40128,40320,40320,40320,40320,40320,40320,40960,44032,48128,51200,55296,58368,62464,65536,62464,65536,62464,65536,268439552,336991254,410,3232374784,80,7984,43008,1056915199,50465804,16780300,36,80,224,1129316352,1129316352,1129316352,1098907648,1098907648,0,136512,1073741824,7488,44032,44032,40960,728064,1073741824,39936,17231,1107575634,4294922220,4294967294,17231,1107575634,4294922220,4294967294,17231,1107575634,4294922220,4294967294,
1677312,1677312,1720320,1720320,1763328,1763328,1806336,1806336,1806336,1806336,1806336,1806336,40960,44032,48128,51200,55296,58368,62464,65536,62464,65536,62464,65536,268439552,336991254,57,809635840,80,7488,192,1040137855,16780300,50465804,576,36,224,1109393408,1109393408,1109393408,1098907648,1098907648,0,323136,1073741824,322560,44032,44032,40960,1723392,1073741824,1720320,16924,1107575336,4294959852,4294967294,16924,1107575336,4294959852,4294967294,16924,1107575336,4294959852,4294967294,
13248,13248,13440,13440,13632,13632,13824,13824,13824,13824,13824,13824,40960,44032,48128,51200,55296,58368,62464,65536,62464,65536,62464,65536,268439552,336991254,410,1082269696,80,7984,43008,1040137983,50465804,16780300,36,80,224,1116471296,1116471296,1116471296,1098907648,1098907648,0,131544,1073741824,2520,44032,44032,40960,701568,1073741824,13440,17034,1107575440,4294953132,4294967294,17034,1107575440,4294953132,4294967294,17034,1107575440,4294953132,4294967294,
4085760,4085760,4128768,4128768,4171776,4171776,4214784,4214784,4214784,4214784,4214784,4214784,40960,44032,48128,51200,55296,58368,62464,65536,62464,65536,62464,65536,268439552,336991254,57,1354895360,80,7488,192,1048526591,16780300,50465804,576,36,224,1119879168,1119879168,1119879168,1098907648,1098907648,0,774720,1073741824,774144,44032,44032,40960,4131840,1073741824,4128768,17086,1107575492,4294947308,4294967294,17086,1107575492,4294947308,4294967294,17086,1107575492,4294947308,4294967294,
22464,22464,22656,22656,22848,22848,23040,23040,23040,23040,23040,23040,40960,44032,48128,51200,55296,58368,62464,65536,62464,65536,62464,65536,268439552,336991254,410,1883906048,80,7984,43008,1048526463,50465804,16780300,36,80,224,1122762752,1122762752,1122762752,1098907648,1098907648,0,133272,1073741824,4248,44032,44032,40960,710784,1073741824,22656,17130,1107575536,4294942380,4294967294,17130,1107575536,4294942380,4294967294,17130,1107575536,4294942380,4294967294,
6064128,6064128,6107136,6107136,6150144,6150144,6193152,6193152,6193152,6193152,6193152,6193152,40960,44032,48128,51200,55296,58368,62464,65536,62464,65536,62464,65536,268439552,336991254,57,2157056000,80,7488,192,1048526591,16780300,50465804,576,36,224,1124990976,1124990976,1124990976,1098907648,1098907648,0,1145664,1073741824,1145088,44032,44032,40960,6110208,1073741824,6107136,17165,1107575568,4294937004,4294967294,17165,1107575568,4294937004,4294967294,17165,1107575568,4294937004,4294967294,
30528,30528,30720,30720,30912,30912,31104,31104,31104,31104,31104,31104,40960,44032,48128,51200,55296,58368,62464,65536,62464,65536,62464,65536,268439552,336991254,410,2686590976,80,7984,43008,1056914943,50465804,16780300,36,80,224,1126170624,1126170624,1126170624,1098907648,1098907648,0,134784,1073741824,5760,44032,44032,40960,718848,1073741824,30720,17183,1107575586,4294932972,4294967294,17183,1107575586,4294932972,4294967294,17183,1107575586,4294932972,4294967294,
7784448,7827456,7870464,7913472,7956480,7999488,8042496,8085504,8085504,8085504,8085504,8085504,40960,45056,49152,53248,57344,61440,65536,69632,69632,69632,69632,69632,538976288,353703189,57,2959737088,80,7488,192,1056915071,16780300,50465804,576,36,128,1127874560,1127743488,1127612416,1107296256,1107296256,1107296256,1501056,1484928,1468800,57344,49152,40960,8005632,7919616,7833600,1106264885,1112032060,4294945228,30,1106264885,1112032060,4294945228,30,1106264885,1112032060,4294945228,30,
40128,40128,40320,40320,40512,40512,40704,40704,40704,40704,40704,40704,40960,44032,48128,51200,55296,58368,62464,65536,62464,65536,62464,65536,268439552,336991254,410,3232899072,80,7984,43008,1056915199,50465804,16780300,36,80,224,1129447424,1129447424,1129447424,1098907648,1098907648,0,136584,1073741824,7560,44032,44032,40960,728448,1073741824,40320,17233,1107575636,4294921772,4294967294,17233,1107575636,4294921772,4294967294,17233,1107575636,4294921772,4294967294,
1763328,1763328,1806336,1806336,1849344,1849344,1892352,1892352,1892352,1892352,1892352,1892352,40960,44032,48128,51200,55296,58368,62464,65536,62464,65536,62464,65536,268439552,336991254,57,810160128,80,7488,192,1040137855,16780300,50465804,576,36,224,1109917696,1109917696,1109917696,1098907648,1098907648,0,339264,1073741824,338688,44032,44032,40960,1809408,1073741824,1806336,16932,1107575344,4294959404,4294967294,16932,1107575344,4294959404,4294967294,16932,1107575344,4294959404,4294967294,
13632,13632,13824,13824,14016,14016,14208,14208,14208,14208,14208,14208,40960,44032,48128,51200,55296,58368,62464,65536,62464,65536,62464,65536,268439552,336991254,410,1082793984,80,7984,43008,1040137983,50465804,16780300,36,80,224,1116733440,1116733440,1116733440,1098907648,1098907648,0,131616,1073741824,2592,44032,44032,40960,701952,1073741824,13824,17038,1107575444,4294952684,4294967294,17038,1107575444,4294952684,4294967294,17038,1107575444,4294952684,4294967294,
4171776,4171776,4214784,4214784,4257792,4257792,4300800,4300800,4300800,4300800,4300800,4300800,40960,44032,48128,51200,55296,58368,62464,65536,62464,65536,62464,65536,268439552,336991254,57,1355419648,80,7488,192,1048526591,16780300,50465804,576,36,224,1120141312,1120141312,1120141312,1098907648,1098907648,0,790848,1073741824,790272,44032,44032,40960,4217856,1073741824,4214784,17090,1107575496,4294946860,4294967294,17090,1107575496,4294946860,4294967294,17090,1107575496,4294946860,4294967294,
22848,22848,23040,23040,23232,23232,23424,23424,23424,23424,23424,23424,40960,44032,48128,51200,55296,58368,62464,65536,62464,65536,62464,65536,268439552,336991254,410,1884430336,80,7984,43008,1048526463,50465804,16780300,36,80,224,1123024896,1123024896,1123024896,1098907648,1098907648,0,133344,1073741824,4320,44032,44032,40960,711168,1073741824,23040,17134,1107575540,4294941932,4294967294,17134,1107575540,4294941932,4294967294,17134,1107575540,4294941932,4294967294,
5978112,6021120,6064128,6107136,6150144,6193152,6236160,6279168,6279168,6279168,6279168,6279168,40960,45056,49152,53248,57344,61440,65536,69632,69632,69632,69632,69632,538976288,353703189,57,2157576448,80,7488,192,1048526591,16780300,50465804,576,36,128,1125122048,1124990976,1124859904,1107296256,1107296256,1107296256,1162368,1146240,1130112,57344,49152,40960,6199296,6113280,6027264,1106264843,1112032018,4294950604,30,1106264843,1112032018,4294950604,30,1106264843,1112032018,4294950604,30,
30912,30912,31104,31104,31296,31296,31488,31488,31488,31488,31488,31488,40960,44032,48128,51200,55296,58368,62464,65536,62464,65536,62464,65536,268439552,336991254,410,2687115264,80,7984,43008,1056914943,50465804,16780300,36,80,224,1126301696,1126301696,1126301696,1098907648,1098907648,0,134856,1073741824,5832,44032,44032,40960,719232,1073741824,31104,17185,1107575588,4294932524,4294967294,17185,1107575588,4294932524,4294967294,17185,1107575588,4294932524,4294967294,
7956480,7956480,7999488,7999488,8042496,8042496,8085504,8085504,8085504,8085504,8085504,8085504,40960,44032,48128,51200,55296,58368,62464,65536,62464,65536,62464,65536,268439552,336991254,57,2960265216,80,7488,192,1056915071,16780300,50465804,576,36,224,1127874560,1127874560,1127874560,1098907648,1098907648,0,1500480,1073741824,1499904,44032,44032,40960,8002560,1073741824,7999488,17209,1107575612,4294927148,4294967294,17209,1107575612,4294927148,4294967294,17209,1107575612,4294927148,4294967294,
40512,40512,40704,40704,40896,40896,41088,41088,41088,41088,41088,41088,40960,44032,48128,51200,55296,58368,62464,65536,62464,65536,62464,65536,268439552,336991254,410,3233423360,80,7984,43008,1056915199,50465804,16780300,36,80,224,1129578496,1129578496,1129578496,1098907648,1098907648,0,136656,1073741824,7632,44032,44032,40960,728832,1073741824,40704,17235,1107575638,4294921324,4294967294,17235,1107575638,4294921324,4294967294,17235,1107575638,4294921324,4294967294,
1849344,1849344,1892352,1892352,1935360,1935360,1978368,1978368,1978368,1978368,1978368,1978368,40960,44032,48128,51200,55296,58368,62464,65536,62464,65536,62464,65536,268439552,336991254,57,810684416,80,7488,192,1040137855,16780300,50465804,576,36,224,1110441984,1110441984,1110441984,1098907648,1098907648,0,355392,1073741824,354816,44032,44032,40960,1895424,1073741824,1892352,16940,1107575352,4294958956,4294967294,16940,1107575352,4294958956,4294967294,16940,1107575352,4294958956,4294967294,
14016,14016,14208,14208,14400,14400,14592,14592,14592,14592,14592,14592,40960,44032,48128,51200,55296,58368,62464,65536,62464,65536,62464,65536,268439552,336991254,410,1083318272,80,7984,43008,1040137983,50465804,16780300,36,80,224,1116995584,1116995584,1116995584,1098907648,1098907648,0,131688,1073741824,2664,44032,44032,40960,702336,1073741824,14208,17042,1107575448,4294952236,4294967294,17042,1107575448,4294952236,4294967294,17042,1107575448,4294952236,4294967294,
4257792,4257792,4300800,4300800,4343808,4343808,4386816,4386816,4386816,4386816,4386816,4386816,40960,44032,48128,51200,55296,58368,62464,65536,62464,65536,62464,65536,268439552,336991254,57,1355943936,80,7488,192,1048526591,16780300,50465804,576,36,224,1120403456,1120403456,1120403456,1098907648,1098907648,0,806976,1073741824,806400,44032,44032,40960,4303872,1073741824,4300800,17094,1107575500,4294946412,4294967294,17094,1107575500,4294946412,4294967294,17094,1107575500,4294946412,4294967294,
23232,23232,23424,23424,23616,23616,23808,23808,23808,23808,23808,23808,40960,44032,48128,51200,55296,58368,62464,65536,62464,65536,62464,65536,268439552,336991254,410,1884954624,80,7984,43008,1048526463,50465804,16780300,36,80,224,1123287040,1123287040,1123287040,1098907648,1098907648,0,133416,1073741824,4392,44032,44032,40960,711552,1073741824,23424,17138,1107575544,4294941484,4294967294,17138,1107575544,4294941484,4294967294,17138,1107575544,4294941484,4294967294,
6150144,6150144,6193152,6193152,6236160,6236160,6279168,6279168,6279168,6279168,6279168,6279168,40960,44032,48128,51200,55296,58368,62464,65536,62464,65536,62464,65536,268439552,336991254,57,2158104576,80,7488,192,1048526591,16780300,50465804,576,36,224,1125122048,1125122048,1125122048,1098907648,1098907648,0,1161792,1073741824,1161216,44032,44032,40960,6196224,1073741824,6193152,17167,1107575570,4294936556,4294967294,17167,1107575570,4294936556,4294967294,17167,1107575570,4294936556,4294967294,
31296,31296,31488,31488,31680,31680,31872,31872,31872,31872,31872,31872,40960,44032,48128,51200,55296,58368,62464,65536,62464,65536,62464,65536,268439552,336991254,410,2687639552,80,7984,43008,1056914943,50465804,16780300,36,80,224,1126432768,1126432768,1126432768,1098907648,1098907648,0,134928,1073741824,5904,44032,44032,40960,719616,1073741824,31488,17187,1107575590,4294932076,4294967294,17187,1107575590,4294932076,4294967294,17187,1107575590,4294932076,4294967294,
8042496,8042496,8085504,8085504,8128512,8128512,8171520,8171520,8171520,8171520,8171520,8171520,40960,44032,48128,51200,55296,58368,62464,65536,62464,65536,62464,65536,268439552,336991254,57,2960789504,80,7488,192,1056915071,16780300,50465804,576,36,224,1128005632,1128005632,1128005632,1098907648,1098907648,0,1516608,1073741824,1516032,44032,44032,40960,8088576,1073741824,8085504,17211,1107575614,4294926700,4294967294,17211,1107575614,4294926700,4294967294,17211,1107575614,4294926700,4294967294,
40896,40896,41088,41088,41280,41280,41472,41472,41472,41472,41472,41472,40960,44032,48128,51200,55296,58368,62464,65536,62464,65536,62464,65536,268439552,336991254,410,3233947648,80,7984,43008,1056915199,50465804,16780300,36,80,224,1129709568,1129709568,1129709568,1098907648,1098907648,0,136728,1073741824,7704,44032,44032,40960,729216,1073741824,41088,17237,1107575640,4294920876,4294967294,17237,1107575640,4294920876,4294967294,17237,1107575640,4294920876,4294967294,
1591296,1634304,1677312,1720320,1763328,1806336,1849344,1892352,1892352,1892352,1892352,1892352,40960,45056,49152,53248,57344,61440,65536,69632,69632,69632,69632,69632,538976288,353703189,57,811204864,80,7488,192,1040137855,16780300,50465804,576,36,128,1109917696,1109393408,1108869120,1107296256,1107296256,1107296256,339840,323712,307584,57344,49152,40960,1812480,1726464,1640448,1106264596,1112031792,4294963660,30,1106264596,1112031792,4294963660,30,1106264596,1112031792,4294963660,30,
2881536,2924544,2967552,3010560,3053568,3096576,3139584,3182592,3182592,3182592,3182592,3182592,40960,45056,49152,53248,57344,61440,65536,69632,69632,69632,69632,69632,538976288,353703189,57,1083834624,80,7488,192,1040137983,16780300,50465804,576,36,128,1116733440,1116471296,1116209152,1107296256,1107296256,1107296256,581760,565632,549504,57344,49152,40960,3102720,3016704,2930688,1106264710,1112031892,4294959820,30,1106264710,1112031892,4294959820,30,1106264710,1112031892,4294959820,30,
4343808,4343808,4386816,4386816,4429824,4429824,4472832,4472832,4472832,4472832,4472832,4472832,40960,44032,48128,51200,55296,58368,62464,65536,62464,65536,62464,65536,268439552,336991254,57,1356468224,80,7488,192,1048526591,16780300,50465804,576,36,224,1120665600,1120665600,1120665600,1098907648,1098907648,0,823104,1073741824,822528,44032,44032,40960,4389888,1073741824,4386816,17098,1107575504,4294945964,4294967294,17098,1107575504,4294945964,4294967294,17098,1107575504,4294945964,4294967294,
23616,23616,23808,23808,24000,24000,24192,24192,24192,24192,24192,24192,40960,44032,48128,51200,55296,58368,62464,65536,62464,65536,62464,65536,268439552,336991254,410,1885478912,80,7984,43008,1048526463,50465804,16780300,36,80,224,1123549184,1123549184,1123549184,1098907648,1098907648,0,133488,1073741824,4464,44032,44032,40960,711936,1073741824,23808,17142,1107575548,4294941036,4294967294,17142,1107575548,4294941036,4294967294,17142,1107575548,4294941036,4294967294,
6236160,6236160,6279168,6279168,6322176,6322176,6365184,6365184,6365184,6365184,6365184,6365184,40960,44032,48128,51200,55296,58368,62464,65536,62464,65536,62464,65536,268439552,336991254,57,2158628864,80,7488,192,1048526591,16780300,50465804,576,36,224,1125253120,1125253120,1125253120,1098907648,1098907648,0,1177920,1073741824,1177344,44032,44032,40960,6282240,1073741824,6279168,17169,1107575572,4294936108,4294967294,17169,1107575572,4294936108,4294967294,17169,1107575572,4294936108,4294967294,
31680,31680,31872,31872,32064,32064,32256,32256,32256,32256,32256,32256,40960,44032,48128,51200,55296,58368,62464,65536,62464,65536,62464,65536,268439552,336991254,410,2688163840,80,7984,43008,1056914943,50465804,16780300,36,80,224,1126563840,1126563840,1126563840,1098907648,1098907648,0,135000,1073741824,5976,44032,44032,40960,720000,1073741824,31872,17189,1107575592,4294931628,4294967294,17189,1107575592,4294931628,4294967294,17189,1107575592,4294931628,4294967294,
8128512,8128512,8171520,8171520,8214528,8214528,8257536,8257536,8257536,8257536,8257536,8257536,40960,44032,48128,51200,55296,58368,62464,65536,62464,65536,62464,65536,268439552,336991254,57,2961313792,80,7488,192,1056915199,16780300,50465804,576,36,224,1128136704,1128136704,1128136704,1098907648,1098907648,0,1532736,1073741824,1532160,44032,44032,40960,8174592,1073741824,8171520,17213,1107575616,4294926252,4294967294,17213,1107575616,4294926252,4294967294,17213,1107575616,4294926252,4294967294,
430080,473088,516096,559104,602112,645120,9074688,9117696,9117696,9117696,9117696,9117696,49152,53248,57344,61440,65536,69632,40960,45056,45056,45056,45056,45056,3233808384,320017685,57,3234476544,80,7488,192,1056915199,16780300,50465804,576,36,128,1129447424,1129447424,1094713344,1128267776,1128267776,0,1700352,1073741824,96768,32768,32768,53248,9068544,1073741824,516096,1128219465,1129333588,4294940428,190,1128219465,1129333588,4294940428,190,16672,1099972976,268,4294967294,
1935360,1935360,1978368,1978368,2021376,2021376,2064384,2064384,2064384,2064384,2064384,2064384,40960,44032,48128,51200,55296,58368,62464,65536,62464,65536,62464,65536,268439552,336991254,57,811732992,80,7488,192,1040137855,16780300,50465804,576,36,224,1110966272,1110966272,1110966272,1098907648,1098907648,0,371520,1073741824,370944,44032,44032,40960,1981440,1073741824,1978368,16948,1107575360,4294958508,4294967294,16948,1107575360,4294958508,4294967294,16948,1107575360,4294958508,4294967294,
14400,14400,14592,14592,14784,14784,14976,14976,14976,14976,14976,14976,40960,44032,48128,51200,55296,58368,62464,65536,62464,65536,62464,65536,268439552,336991254,410,1084366848,80,7984,43008,1040137983,50465804,16780300,36,80,224,1117257728,1117257728,1117257728,1098907648,1098907648,0,131760,1073741824,2736,44032,44032,40960,702720,1073741824,14592,17046,1107575452,4294951788,4294967294,17046,1107575452,4294951788,4294967294,17046,1107575452,4294951788,4294967294,
3072,3072,3264,3264,3456,3456,3648,3648,3840,3840,4032,4032,40960,44032,48128,51200,55296,58368,62464,65536,69632,72704,76800,79872,268439552,336991254,410,1612852224,64,7984,43008,1048526335,50465804,16780300,36,80,224,1099956224,1099956224,1099956224,1098907648,1098907648,0,129672,1073741824,648,51200,51200,48128,691584,1073741824,3456,16768,1107575208,4294965004,4294967294,16768,1107575208,4294965004,4294967294,16768,1107575208,4294965004,4294967294,
24000,24000,24192,24192,24384,24384,24576,24576,24576,24576,24576,24576,40960,44032,48128,51200,55296,58368,62464,65536,62464,65536,62464,65536,268439552,336991254,410,1886003200,80,7984,43008,1048526463,50465804,16780300,36,80,224,1123811328,1123811328,1123811328,1098907648,1098907648,0,133560,1073741824,4536,44032,44032,40960,712320,1073741824,24192,17146,1107575552,4294940588,4294967294,17146,1107575552,4294940588,4294967294,17146,1107575552,4294940588,4294967294,
6322176,6322176,6365184,6365184,6408192,6408192,6451200,6451200,6451200,6451200,6451200,6451200,40960,44032,48128,51200,55296,58368,62464,65536,62464,65536,62464,65536,268439552,336991254,57,2159153152,80,7488,192,1048526591,16780300,50465804,576,36,224,1125384192,1125384192,1125384192,1098907648,1098907648,0,1194048,1073741824,1193472,44032,44032,40960,6368256,1073741824,6365184,17171,1107575574,4294935660,4294967294,17171,1107575574,4294935660,4294967294,17171,1107575574,4294935660,4294967294,
32064,32064,32256,32256,32448,32448,32640,32640,32640,32640,32640,32640,40960,44032,48128,51200,55296,58368,62464,65536,62464,65536,62464,65536,268439552,336991254,410,2688688128,80,7984,43008,1056915071,50465804,16780300,36,80,224,1126694912,1126694912,1126694912,1098907648,1098907648,0,135072,1073741824,6048,44032,44032,40960,720384,1073741824,32256,17191,1107575594,4294931180,4294967294,17191,1107575594,4294931180,4294967294,17191,1107575594,4294931180,4294967294,
8214528,8214528,8257536,8257536,8300544,8300544,8343552,8343552,8343552,8343552,8343552,8343552,40960,44032,48128,51200,55296,58368,62464,65536,62464,65536,62464,65536,268439552,336991254,57,2961838080,80,7488,192,1056915199,16780300,50465804,576,36,224,1128267776,1128267776,1128267776,1098907648,1098907648,0,1548864,1073741824,1548288,44032,44032,40960,8260608,1073741824,8257536,17215,1107575618,4294925804,4294967294,17215,1107575618,4294925804,4294967294,17215,1107575618,4294925804,4294967294,
1290240,1333248,1376256,1419264,1462272,9504768,9547776,9590784,9590784,9590784,9590784,9590784,57344,61440,65536,69632,73728,40960,45056,49152,53248,53248,53248,53248,3233857728,353703189,57,3235120384,64,7488,192,1056915199,16780300,50465804,576,36,128,1130102784,1130233856,1107296256,1128267776,1128267776,1128267776,1780992,1797120,264960,32768,40960,61440,9498624,9584640,1413120,1128219475,1129333599,4294939148,190,1128219475,1129333599,4294939148,190,1128153584,1129464328,4294964108,190,
2021376,2021376,2064384,2064384,2107392,2107392,2150400,2150400,2150400,2150400,2150400,2150400,40960,44032,48128,51200,55296,58368,62464,65536,62464,65536,62464,65536,268439552,336991254,57,812257280,80,7488,192,1040137855,16780300,50465804,576,36,224,1111490560,1111490560,1111490560,1098907648,1098907648,0,387648,1073741824,387072,44032,44032,40960,2067456,1073741824,2064384,16956,1107575368,4294958060,4294967294,16956,1107575368,4294958060,4294967294,16956,1107575368,4294958060,4294967294,
14784,14784,14976,14976,15168,15168,15360,15360,15360,15360,15360,15360,40960,44032,48128,51200,55296,58368,62464,65536,62464,65536,62464,65536,268439552,336991254,410,1084891136,80,7984,43008,1040137983,50465804,16780300,36,80,224,1117519872,1117519872,1117519872,1098907648,1098907648,0,131832,1073741824,2808,44032,44032,40960,703104,1073741824,14976,17050,1107575456,4294951340,4294967294,17050,1107575456,4294951340,4294967294,17050,1107575456,4294951340,4294967294,
3456,3456,3648,3648,3840,3840,4032,4032,4224,4224,4416,4416,40960,44032,48128,51200,55296,58368,62464,65536,69632,72704,76800,79872,268439552,336991254,410,1613376512,64,7984,43008,1048526335,50465804,16780300,36,80,224,1101004800,1101004800,1101004800,1098907648,1098907648,0,129744,1073741824,720,51200,51200,48128,691968,1073741824,3840,16784,1107575224,4294964556,4294967294,16784,1107575224,4294964556,4294967294,16784,1107575224,4294964556,4294967294,
86016,129024,172032,215040,258048,301056,301056,301056,301056,301056,301056,301056,40960,45056,49152,53248,57344,61440,61440,61440,61440,61440,61440,61440,0,353703189,57,1886564864,80,7488,192,1048526463,16780300,50465804,576,36,128,1082130432,1082130432,1082130432,0,0,0,1073741824,1073741824,32256,45056,45056,45056,1073741824,1073741824,172032,16384,1099972832,1036,4294967294,16384,1099972832,1036,4294967294,16384,1099972832,1036,4294967294,
6408192,6408192,6451200,6451200,6494208,6494208,6537216,6537216,6537216,6537216,6537216,6537216,40960,44032,48128,51200,55296,58368,62464,65536,62464,65536,62464,65536,268439552,336991254,57,2159677440,80,7488,192,1048526591,16780300,50465804,576,36,224,1125515264,1125515264,1125515264,1098907648,1098907648,0,1210176,1073741824,1209600,44032,44032,40960,6454272,1073741824,6451200,17173,1107575576,4294935212,4294967294,17173,1107575576,4294935212,4294967294,17173,1107575576,4294935212,4294967294,
32448,32448,32640,32640,32832,32832,33024,33024,33024,33024,33024,33024,40960,44032,48128,51200,55296,58368,62464,65536,62464,65536,62464,65536,268439552,336991254,410,2689212416,80,7984,43008,1056915071,50465804,16780300,36,80,224,1126825984,1126825984,1126825984,1098907648,1098907648,0,135144,1073741824,6120,44032,44032,40960,720768,1073741824,32640,17193,1107575596,4294930732,4294967294,17193,1107575596,4294930732,4294967294,17193,1107575596,4294930732,4294967294,
8300544,8300544,8343552,8343552,8386560,8386560,8429568,8429568,8429568,8429568,8429568,8429568,40960,44032,48128,51200,55296,58368,62464,65536,62464,65536,62464,65536,268439552,336991254,57,2962362368,80,7488,192,1056915199,16780300,50465804,576,36,224,1128398848,1128398848,1128398848,1098907648,1098907648,0,1564992,1073741824,1564416,44032,44032,40960,8346624,1073741824,8343552,17217,1107575620,4294925356,4294967294,17217,1107575620,4294925356,4294967294,17217,1107575620,4294925356,4294967294,
8816640,8859648,8902656,8945664,8988672,9031680,9074688,9117696,9117696,9117696,9117696,9117696,40960,45056,49152,53248,57344,61440,65536,69632,69632,69632,69632,69632,538976288,353703189,57,3491889408,80,7488,192,1056914943,16780300,50465804,576,36,128,1129447424,1129316352,1129185280,1107296256,1107296256,1107296256,1694592,1678464,1662336,57344,49152,40960,9037824,8951808,8865792,1106264909,1112032084,4294942156,30,1106264909,1112032084,4294942156,30,1106264909,1112032084,4294942156,30,
2107392,2107392,2150400,2150400,2193408,2193408,2236416,2236416,2236416,2236416,2236416,2236416,40960,44032,48128,51200,55296,58368,62464,65536,62464,65536,62464,65536,268439552,336991254,57,812781568,80,7488,192,1040137855,16780300,50465804,576,36,224,1112014848,1112014848,1112014848,1098907648,1098907648,0,403776,1073741824,403200,44032,44032,40960,2153472,1073741824,2150400,16964,1107575376,4294957612,4294967294,16964,1107575376,4294957612,4294967294,16964,1107575376,4294957612,4294967294,
1290240,1290240,1333248,1333248,1376256,1376256,1419264,1419264,1462272,1462272,1505280,1505280,40960,44032,48128,51200,55296,58368,62464,65536,69632,72704,76800,79872,1076903984,336991254,57,1085414400,64,7488,192,1040137983,16780300,50465804,576,36,224,1107296256,1107296256,1107296256,1115684864,1115684864,1111490560,260352,1073741824,259776,51200,51200,48128,1388544,1073741824,1385472,1110983152,1117929996,4294961580,46,1110983152,1117929996,4294961580,46,1110983152,1117929996,4294961580,46,
4171776,4214784,4257792,4300800,4343808,4386816,4429824,4472832,4472832,4472832,4472832,4472832,40960,45056,49152,53248,57344,61440,65536,69632,69632,69632,69632,69632,538976288,353703189,57,1613889792,80,7488,192,1048526335,16780300,50465804,576,36,128,1120665600,1120403456,1120141312,1107296256,1107296256,1107296256,823680,807552,791424,57344,49152,40960,4392960,4306944,4220928,1106264770,1112031952,4294955980,30,1106264770,1112031952,4294955980,30,1106264770,1112031952,4294955980,30,
3840,3840,4032,4032,4224,4224,4416,4416,4608,4608,4800,4800,40960,44032,48128,51200,55296,58368,62464,65536,69632,72704,76800,79872,268439552,336991254,410,1887054848,64,7984,43008,1048526463,50465804,16780300,36,80,224,1102053376,1102053376,1102053376,1098907648,1098907648,0,129816,1073741824,792,51200,51200,48128,692352,1073741824,4224,16800,1107575240,4294964108,4294967294,16800,1107575240,4294964108,4294967294,16800,1107575240,4294964108,4294967294,
1032192,1075200,1118208,1161216,1204224,1247232,8730624,8773632,8773632,8773632,8773632,8773632,49152,53248,57344,61440,65536,69632,40960,45056,45056,45056,45056,45056,2155872256,320017685,57,2160209920,80,7488,192,1048526591,16780300,50465804,576,36,128,1128923136,1128923136,1104150528,1124073472,1124073472,0,1633536,1073741824,209664,32768,32768,53248,8712192,1073741824,1118208,1123959617,1125139276,4294941836,126,1123959617,1125139276,4294941836,126,16832,1099973096,4294965772,4294967294,
7010304,7053312,7096320,7139328,7182336,7225344,7268352,7311360,7311360,7311360,7311360,7311360,40960,45056,49152,53248,57344,61440,65536,69632,69632,69632,69632,69632,538976288,353703189,57,2689728768,80,7488,192,1056915071,16780300,50465804,576,36,128,1126694912,1126563840,1126432768,1107296256,1107296256,1107296256,1355904,1339776,1323648,57344,49152,40960,7231488,7145472,7059456,1106264867,1112032042,4294947532,30,1106264867,1112032042,4294947532,30,1106264867,1112032042,4294947532,30,
8386560,8386560,8429568,8429568,8472576,8472576,8515584,8515584,8515584,8515584,8515584,8515584,40960,44032,48128,51200,55296,58368,62464,65536,62464,65536,62464,65536,268439552,336991254,57,2962886656,80,7488,192,1056915199,16780300,50465804,576,36,224,1128529920,1128529920,1128529920,1098907648,1098907648,0,1581120,1073741824,1580544,44032,44032,40960,8432640,1073741824,8429568,17219,1107575622,4294924908,4294967294,17219,1107575622,4294924908,4294967294,17219,1107575622,4294924908,4294967294,
41280,41280,41472,41472,41664,41664,41856,41856,41856,41856,41856,41856,40960,44032,48128,51200,55296,58368,62464,65536,62464,65536,62464,65536,268439552,336991254,410,3492421632,80,7984,43008,1056914943,50465804,16780300,36,80,224,1129840640,1129840640,1129840640,1098907648,1098907648,0,136800,1073741824,7776,44032,44032,40960,729600,1073741824,41472,17239,1107575642,4294920428,4294967294,17239,1107575642,4294920428,4294967294,17239,1107575642,4294920428,4294967294,
2193408,2193408,2236416,2236416,2279424,2279424,2322432,2322432,2322432,2322432,2322432,2322432,40960,44032,48128,51200,55296,58368,62464,65536,62464,65536,62464,65536,268439552,336991254,57,813305856,80,7488,192,1040137855,16780300,50465804,576,36,224,1112539136,1112539136,1112539136,1098907648,1098907648,0,419904,1073741824,419328,44032,44032,40960,2239488,1073741824,2236416,16972,1107575384,4294957164,4294967294,16972,1107575384,4294957164,4294967294,16972,1107575384,4294957164,4294967294,
0,43008,86016,129024,172032,215040,215040,215040,215040,215040,215040,215040,40960,45056,49152,53248,57344,61440,61440,61440,61440,61440,61440,61440,0,353703189,57,1085977088,80,7488,192,1040137983,16780300,50465804,576,36,128,1073741824,1073741824,1073741824,0,0,0,1073741824,1073741824,16128,45056,45056,45056,1073741824,1073741824,86016,0,1099972768,1292,4294967294,0,1099972768,1292,4294967294,0,1099972768,1292,4294967294,
15168,15168,15360,15360,15552,15552,15744,15744,15744,15744,15744,15744,40960,44032,48128,51200,55296,58368,62464,65536,62464,65536,62464,65536,268439552,336991254,410,1614422016,80,7984,43008,1048526335,50465804,16780300,36,80,224,1117782016,1117782016,1117782016,1098907648,1098907648,0,131904,1073741824,2880,44032,44032,40960,703488,1073741824,15360,17054,1107575460,4294950892,4294967294,17054,1107575460,4294950892,4294967294,17054,1107575460,4294950892,4294967294,
5203968,5246976,5289984,5332992,5376000,5419008,5462016,5505024,5505024,5505024,5505024,5505024,40960,45056,49152,53248,57344,61440,65536,69632,69632,69632,69632,69632,538976288,353703189,57,1887568128,80,7488,192,1048526591,16780300,50465804,576,36,128,1123811328,1123549184,1123287040,1107296256,1107296256,1107296256,1017216,1001088,984960,57344,49152,40960,5425152,5339136,5253120,1106264818,1112032000,4294952908,30,1106264818,1112032000,4294952908,30,1106264818,1112032000,4294952908,30,
1118208,1161216,1204224,1247232,1290240,1333248,9160704,9203712,9203712,9203712,9203712,9203712,49152,53248,57344,61440,65536,69632,40960,45056,45056,45056,45056,45056,2155872256,320017685,57,2160865280,80,7488,192,1048526591,16780300,50465804,576,36,128,1129578496,1129578496,1105199104,1124073472,1124073472,0,1714176,1073741824,225792,32768,32768,53248,9142272,1073741824,1204224,1123959627,1125139286,4294940556,126,1123959627,1125139286,4294940556,126,16848,1099973112,4294965516,4294967294,
32832,32832,33024,33024,33216,33216,33408,33408,33408,33408,33408,33408,40960,44032,48128,51200,55296,58368,62464,65536,62464,65536,62464,65536,268439552,336991254,410,2690260992,80,7984,43008,1056915071,50465804,16780300,36,80,224,1126957056,1126957056,1126957056,1098907648,1098907648,0,135216,1073741824,6192,44032,44032,40960,721152,1073741824,33024,17195,1107575598,4294930284,4294967294,17195,1107575598,4294930284,4294967294,17195,1107575598,4294930284,4294967294,
8472576,8472576,8515584,8515584,8558592,8558592,8601600,8601600,8601600,8601600,8601600,8601600,40960,44032,48128,51200,55296,58368,62464,65536,62464,65536,62464,65536,268439552,336991254,57,2963410944,80,7488,192,1056915199,16780300,50465804,576,36,224,1128660992,1128660992,1128660992,1098907648,1098907648,0,1597248,1073741824,1596672,44032,44032,40960,8518656,1073741824,8515584,17221,1107575624,4294924460,4294967294,17221,1107575624,4294924460,4294967294,17221,1107575624,4294924460,4294967294,
41664,41664,41856,41856,42048,42048,42240,42240,42240,42240,42240,42240,40960,44032,48128,51200,55296,58368,62464,65536,62464,65536,62464,65536,268439552,336991254,410,3492945920,80,7984,43008,1056914943,50465804,16780300,36,80,224,1129971712,1129971712,1129971712,1098907648,1098907648,0,136872,1073741824,7848,44032,44032,40960,729984,1073741824,41856,17241,1107575644,4294919980,4294967294,17241,1107575644,4294919980,4294967294,17241,1107575644,4294919980,4294967294,
2279424,2279424,2322432,2322432,2365440,2365440,2408448,2408448,2408448,2408448,2408448,2408448,40960,44032,48128,51200,55296,58368,62464,65536,62464,65536,62464,65536,268439552,336991254,57,813830144,80,7488,192,1040137983,16780300,50465804,576,36,224,1113063424,1113063424,1113063424,1098907648,1098907648,0,436032,1073741824,435456,44032,44032,40960,2325504,1073741824,2322432,16980,1107575392,4294956716,4294967294,16980,1107575392,4294956716,4294967294,16980,1107575392,4294956716,4294967294,
2365440,2365440,2408448,2408448,2451456,2451456,2494464,2494464,2494464,2494464,2494464,2494464,40960,44032,48128,51200,55296,58368,62464,65536,62464,65536,62464,65536,268439552,336991254,57,1086459904,80,7488,192,1040137983,16780300,50465804,576,36,224,1113587712,1113587712,1113587712,1098907648,1098907648,0,452160,1073741824,451584,44032,44032,40960,2411520,1073741824,2408448,16988,1107575400,4294956268,4294967294,16988,1107575400,4294956268,4294967294,16988,1107575400,4294956268,4294967294,
15552,15552,15744,15744,15936,15936,16128,16128,16128,16128,16128,16128,40960,44032,48128,51200,55296,58368,62464,65536,62464,65536,62464,65536,268439552,336991254,410,1614946304,80,7984,43008,1048526463,50465804,16780300,36,80,224,1118044160,1118044160,1118044160,1098907648,1098907648,0,131976,1073741824,2952,44032,44032,40960,703872,1073741824,15744,17058,1107575464,4294950444,4294967294,17058,1107575464,4294950444,4294967294,17058,1107575464,4294950444,4294967294,
4429824,4429824,4472832,4472832,4515840,4515840,4558848,4558848,4558848,4558848,4558848,4558848,40960,44032,48128,51200,55296,58368,62464,65536,62464,65536,62464,65536,268439552,336991254,57,1888096256,80,7488,192,1048526591,16780300,50465804,576,36,224,1120927744,1120927744,1120927744,1098907648,1098907648,0,839232,1073741824,838656,44032,44032,40960,4475904,1073741824,4472832,17102,1107575508,4294945516,4294967294,17102,1107575508,4294945516,4294967294,17102,1107575508,4294945516,4294967294,
1204224,1247232,1290240,1333248,1376256,1419264,9590784,9590784,9590784,9590784,9590784,9590784,49152,53248,57344,61440,65536,69632,40960,45056,45056,45056,45056,45056,2155872256,320017685,57,2161520640,80,7488,192,1048526591,16780300,50465804,576,36,128,1130233856,1130233856,1106247680,1124073472,1124073472,0,1794816,1073741824,241920,32768,32768,53248,9572352,1073741824,1290240,1123959637,1125139295,4294939276,126,1123959637,1125139295,4294939276,126,16864,1099973124,4294965260,4294967294,
33216,33216,33408,33408,33600,33600,33792,33792,33792,33792,33792,33792,40960,44032,48128,51200,55296,58368,62464,65536,62464,65536,62464,65536,268439552,336991254,410,2690785280,80,7984,43008,1056915071,50465804,16780300,36,80,224,1127088128,1127088128,1127088128,1098907648,1098907648,0,135288,1073741824,6264,44032,44032,40960,721536,1073741824,33408,17197,1107575600,4294929836,4294967294,17197,1107575600,4294929836,4294967294,17197,1107575600,4294929836,4294967294,
8042496,8085504,8128512,8171520,8214528,8257536,8300544,8343552,8343552,8343552,8343552,8343552,40960,45056,49152,53248,57344,61440,65536,69632,69632,69632,69632,69632,538976288,353703189,57,2963931392,80,7488,192,1056915199,16780300,50465804,576,36,128,1128267776,1128136704,1128005632,1107296256,1107296256,1107296256,1549440,1533312,1517184,57344,49152,40960,8263680,8177664,8091648,1106264891,1112032066,4294944460,30,1106264891,1112032066,4294944460,30,1106264891,1112032066,4294944460,30,
42048,42048,42240,42240,42432,42432,42624,42624,42624,42624,42624,42624,40960,44032,48128,51200,55296,58368,62464,65536,62464,65536,62464,65536,268439552,336991254,410,3493470208,80,7984,43008,1056914943,50465804,16780300,36,80,224,1130102784,1130102784,1130102784,1098907648,1098907648,0,136944,1073741824,7920,44032,44032,40960,730368,1073741824,42240,17243,1107575646,4294919532,4294967294,17243,1107575646,4294919532,4294967294,17243,1107575646,4294919532,4294967294,
1849344,1892352,1935360,1978368,2021376,2064384,2107392,2150400,2150400,2150400,2150400,2150400,40960,45056,49152,53248,57344,61440,65536,69632,69632,69632,69632,69632,538976288,353703189,57,814350592,80,7488,192,1040137983,16780300,50465804,576,36,128,1111490560,1110966272,1110441984,1107296256,1107296256,1107296256,388224,372096,355968,57344,49152,40960,2070528,1984512,1898496,1106264620,1112031816,4294962892,30,1106264620,1112031816,4294962892,30,1106264620,1112031816,4294962892,30,
2451456,2451456,2494464,2494464,2537472,2537472,2580480,2580480,2580480,2580480,2580480,2580480,40960,44032,48128,51200,55296,58368,62464,65536,62464,65536,62464,65536,268439552,336991254,57,1086984192,80,7488,192,1040137983,16780300,50465804,576,36,224,1114112000,1114112000,1114112000,1098907648,1098907648,0,468288,1073741824,467712,44032,44032,40960,2497536,1073741824,2494464,16996,1107575408,4294955820,4294967294,16996,1107575408,4294955820,4294967294,16996,1107575408,4294955820,4294967294,
15936,15936,16128,16128,16320,16320,16512,16512,16512,16512,16512,16512,40960,44032,48128,51200,55296,58368,62464,65536,62464,65536,62464,65536,268439552,336991254,410,1615470592,80,7984,43008,1048526463,50465804,16780300,36,80,224,1118306304,1118306304,1118306304,1098907648,1098907648,0,132048,1073741824,3024,44032,44032,40960,704256,1073741824,16128,17062,1107575468,4294949996,4294967294,17062,1107575468,4294949996,4294967294,17062,1107575468,4294949996,4294967294,
4515840,4515840,4558848,4558848,4601856,4601856,4644864,4644864,4644864,4644864,4644864,4644864,40960,44032,48128,51200,55296,58368,62464,65536,62464,65536,62464,65536,268439552,336991254,57,1888620544,80,7488,192,1048526591,16780300,50465804,576,36,224,1121189888,1121189888,1121189888,1098907648,1098907648,0,855360,1073741824,854784,44032,44032,40960,4561920,1073741824,4558848,17106,1107575512,4294945068,4294967294,17106,1107575512,4294945068,4294967294,17106,1107575512,4294945068,4294967294,
1290240,1290240,1333248,1333248,1376256,1376256,1419264,1419264,1462272,1462272,1505280,1505280,40960,44032,48128,51200,55296,58368,62464,65536,69632,72704,76800,79872,1615880272,336991254,57,2418154496,64,7488,192,1056914943,16780300,50465804,576,36,224,1107296256,1107296256,1107296256,1119879168,1119879168,1117782016,261504,1073741824,260928,51200,51200,48128,1394688,1073741824,1391616,1117536752,1122124300,4294961388,78,1117536752,1122124300,4294961388,78,1117536752,1122124300,4294961388,78,
4608,4608,4800,4800,4992,4992,5184,5184,5376,5376,5568,5568,40960,44032,48128,51200,55296,58368,62464,65536,69632,72704,76800,79872,268439552,336991254,410,2691312640,64,7984,43008,1056915071,50465804,16780300,36,80,224,1104150528,1104150528,1104150528,1098907648,1098907648,0,129960,1073741824,936,51200,51200,48128,693120,1073741824,4992,16832,1107575272,4294963212,4294967294,16832,1107575272,4294963212,4294967294,16832,1107575272,4294963212,4294967294,
4992,4992,5184,5184,5376,5376,5568,5568,5760,5760,5952,5952,40960,44032,48128,51200,55296,58368,62464,65536,69632,72704,76800,79872,268439552,336991254,410,2964466688,64,7984,43008,1056915199,50465804,16780300,36,80,224,1105199104,1105199104,1105199104,1098907648,1098907648,0,130032,1073741824,1008,51200,51200,48128,693504,1073741824,5376,16848,1107575288,4294962764,4294967294,16848,1107575288,4294962764,4294967294,16848,1107575288,4294962764,4294967294,
42432,42432,42624,42624,42816,42816,42816,42816,42816,42816,42816,42816,40960,44032,48128,51200,55296,58368,62464,65536,62464,65536,62464,65536,268439552,336991254,410,3493994496,80,7984,43008,1056915071,50465804,16780300,36,80,224,1130233856,1130233856,1130233856,1098907648,1098907648,0,137016,1073741824,7992,44032,44032,40960,730752,1073741824,42624,17245,1107575647,4294919084,4294967294,17245,1107575647,4294919084,4294967294,17245,1107575647,4294919084,4294967294,
2688,2688,2880,2880,3072,3072,3264,3264,3456,3456,3648,3648,40960,44032,48128,51200,55296,58368,62464,65536,69632,72704,76800,79872,268439552,336991254,410,814886400,64,7984,43008,1040137983,50465804,16780300,36,80,224,1098907648,1098907648,1098907648,1098907648,1098907648,0,129600,1073741824,576,51200,51200,48128,691200,1073741824,3072,16736,1107575192,4294965452,4294967294,16736,1107575192,4294965452,4294967294,16736,1107575192,4294965452,4294967294,
3139584,3182592,3225600,3268608,3311616,3354624,3397632,3440640,3440640,3440640,3440640,3440640,40960,45056,49152,53248,57344,61440,65536,69632,69632,69632,69632,69632,538976288,353703189,57,1087504640,80,7488,192,1040137983,16780300,50465804,576,36,128,1117519872,1117257728,1116995584,1107296256,1107296256,1107296256,630144,614016,597888,57344,49152,40960,3360768,3274752,3188736,1106264722,1112031904,4294959052,30,1106264722,1112031904,4294959052,30,1106264722,1112031904,4294959052,30,
16320,16320,16512,16512,16704,16704,16896,16896,16896,16896,16896,16896,40960,44032,48128,51200,55296,58368,62464,65536,62464,65536,62464,65536,268439552,336991254,410,1615994880,80,7984,43008,1048526463,50465804,16780300,36,80,224,1118568448,1118568448,1118568448,1098907648,1098907648,0,132120,1073741824,3096,44032,44032,40960,704640,1073741824,16512,17066,1107575472,4294949548,4294967294,17066,1107575472,4294949548,4294967294,17066,1107575472,4294949548,4294967294,
4601856,4601856,4644864,4644864,4687872,4687872,4730880,4730880,4730880,4730880,4730880,4730880,40960,44032,48128,51200,55296,58368,62464,65536,62464,65536,62464,65536,268439552,336991254,57,1889144832,80,7488,192,1048526591,16780300,50465804,576,36,224,1121452032,1121452032,1121452032,1098907648,1098907648,0,871488,1073741824,870912,44032,44032,40960,4647936,1073741824,4644864,17110,1107575516,4294944620,4294967294,17110,1107575516,4294944620,4294967294,17110,1107575516,4294944620,4294967294,
4224,4224,4416,4416,4608,4608,4800,4800,4992,4992,5184,5184,40960,44032,48128,51200,55296,58368,62464,65536,69632,72704,76800,79872,268439552,336991254,410,2418682880,64,7984,43008,1056914943,50465804,16780300,36,80,224,1103101952,1103101952,1103101952,1098907648,1098907648,0,129888,1073741824,864,51200,51200,48128,692736,1073741824,4608,16816,1107575256,4294963660,4294967294,16816,1107575256,4294963660,4294967294,16816,1107575256,4294963660,4294967294,
1290240,1290240,1333248,1333248,1376256,1376256,1419264,1419264,1462272,1462272,1505280,1505280,40960,44032,48128,51200,55296,58368,62464,65536,69632,72704,76800,79872,268439552,336991254,57,2691832832,64,7488,192,1056915071,16780300,50465804,576,36,224,1107296256,1107296256,1107296256,1098907648,1098907648,0,258624,1073741824,258048,51200,51200,48128,1379328,1073741824,1376256,16880,1107575308,4294961868,4294967294,16880,1107575308,4294961868,4294967294,16880,1107575308,4294961868,4294967294,
5376,5376,5568,5568,5760,5760,5952,5952,6144,6144,6336,6336,40960,44032,48128,51200,55296,58368,62464,65536,69632,72704,76800,79872,268439552,336991254,410,2964990976,64,7984,43008,1056915199,50465804,16780300,36,80,224,1106247680,1106247680,1106247680,1098907648,1098907648,0,130104,1073741824,1080,51200,51200,48128,693888,1073741824,5760,16864,1107575300,4294962316,4294967294,16864,1107575300,4294962316,4294967294,16864,1107575300,4294962316,4294967294,
1290240,1290240,1333248,1333248,1376256,1376256,1419264,1419264,1462272,1462272,1505280,1505280,40960,44032,48128,51200,55296,58368,62464,65536,69632,72704,76800,79872,2154856560,336991254,57,3494517760,64,7488,192,1056915071,16780300,50465804,576,36,224,1107296256,1107296256,1107296256,1124073472,1124073472,1121976320,262656,1073741824,262080,51200,51200,48128,1400832,1073741824,1397760,1121731056,1125204492,4294961196,110,1121731056,1125204492,4294961196,110,1121731056,1125204492,4294961196,110,
6336,6336,6528,6528,6720,6720,6912,6912,6912,6912,6912,6912,40960,44032,48128,51200,55296,58368,62464,65536,62464,65536,62464,65536,268439552,336991254,410,815407104,80,7984,43008,1040137983,50465804,16780300,36,80,224,1107820544,1107820544,1107820544,1098907648,1098907648,0,130248,1073741824,1224,44032,44032,40960,694656,1073741824,6528,16900,1107575312,4294961196,4294967294,16900,1107575312,4294961196,4294967294,16900,1107575312,4294961196,4294967294,
2537472,2537472,2580480,2580480,2623488,2623488,2666496,2666496,2666496,2666496,2666496,2666496,40960,44032,48128,51200,55296,58368,62464,65536,62464,65536,62464,65536,268439552,336991254,57,1088032768,80,7488,192,1040137983,16780300,50465804,576,36,224,1114636288,1114636288,1114636288,1098907648,1098907648,0,484416,1073741824,483840,44032,44032,40960,2583552,1073741824,2580480,17004,1107575416,4294955372,4294967294,17004,1107575416,4294955372,4294967294,17004,1107575416,4294955372,4294967294,
16704,16704,16896,16896,17088,17088,17280,17280,17280,17280,17280,17280,40960,44032,48128,51200,55296,58368,62464,65536,62464,65536,62464,65536,268439552,336991254,410,1616519168,80,7984,43008,1048526463,50465804,16780300,36,80,224,1118830592,1118830592,1118830592,1098907648,1098907648,0,132192,1073741824,3168,44032,44032,40960,705024,1073741824,16896,17070,1107575476,4294949100,4294967294,17070,1107575476,4294949100,4294967294,17070,1107575476,4294949100,4294967294,
4687872,4687872,4730880,4730880,4773888,4773888,4816896,4816896,4816896,4816896,4816896,4816896,40960,44032,48128,51200,55296,58368,62464,65536,62464,65536,62464,65536,268439552,336991254,57,1889669120,80,7488,192,1048526591,16780300,50465804,576,36,224,1121714176,1121714176,1121714176,1098907648,1098907648,0,887616,1073741824,887040,44032,44032,40960,4733952,1073741824,4730880,17114,1107575520,4294944172,4294967294,17114,1107575520,4294944172,4294967294,17114,1107575520,4294944172,4294967294,
24384,24384,24576,24576,24768,24768,24960,24960,24960,24960,24960,24960,40960,44032,48128,51200,55296,58368,62464,65536,62464,65536,62464,65536,268439552,336991254,410,2419204096,80,7984,43008,1056914943,50465804,16780300,36,80,224,1124073472,1124073472,1124073472,1098907648,1098907648,0,133632,1073741824,4608,44032,44032,40960,712704,1073741824,24576,17150,1107575554,4294940140,4294967294,17150,1107575554,4294940140,4294967294,17150,1107575554,4294940140,4294967294,
6494208,6494208,6537216,6537216,6580224,6580224,6623232,6623232,6623232,6623232,6623232,6623232,40960,44032,48128,51200,55296,58368,62464,65536,62464,65536,62464,65536,268439552,336991254,57,2692354048,80,7488,192,1056915071,16780300,50465804,576,36,224,1125646336,1125646336,1125646336,1098907648,1098907648,0,1226304,1073741824,1225728,44032,44032,40960,6540288,1073741824,6537216,17175,1107575578,4294934764,4294967294,17175,1107575578,4294934764,4294967294,17175,1107575578,4294934764,4294967294,
33600,33600,33792,33792,33984,33984,34176,34176,34176,34176,34176,34176,40960,44032,48128,51200,55296,58368,62464,65536,62464,65536,62464,65536,268439552,336991254,410,2965512192,80,7984,43008,1056915199,50465804,16780300,36,80,224,1127219200,1127219200,1127219200,1098907648,1098907648,0,135360,1073741824,6336,44032,44032,40960,721920,1073741824,33792,17199,1107575602,4294929388,4294967294,17199,1107575602,4294929388,4294967294,17199,1107575602,4294929388,4294967294,
5760,5760,5952,5952,6144,6144,6336,6336,6528,6528,6720,6720,40960,44032,48128,51200,55296,58368,62464,65536,69632,72704,76800,79872,268439552,336991254,410,3495046144,64,7984,43008,1056915071,50465804,16780300,36,80,224,1107296256,1107296256,1107296256,1098907648,1098907648,0,130176,1073741824,1152,51200,51200,48128,694272,1073741824,6144,16880,1107575308,4294961868,4294967294,16880,1107575308,4294961868,4294967294,16880,1107575308,4294961868,4294967294,
6720,6720,6912,6912,7104,7104,7296,7296,7296,7296,7296,7296,40960,44032,48128,51200,55296,58368,62464,65536,62464,65536,62464,65536,268439552,336991254,410,815931392,80,7984,43008,1040137983,50465804,16780300,36,80,224,1108344832,1108344832,1108344832,1098907648,1098907648,0,130320,1073741824,1296,44032,44032,40960,695040,1073741824,6912,16908,1107575320,4294960748,4294967294,16908,1107575320,4294960748,4294967294,16908,1107575320,4294960748,4294967294,
2623488,2623488,2666496,2666496,2709504,2709504,2752512,2752512,2752512,2752512,2752512,2752512,40960,44032,48128,51200,55296,58368,62464,65536,62464,65536,62464,65536,268439552,336991254,57,1344409600,80,7488,192,1048526335,16780300,50465804,576,36,224,1115160576,1115160576,1115160576,1098907648,1098907648,0,500544,1073741824,499968,44032,44032,40960,2669568,1073741824,2666496,17012,1107575424,4294954924,4294967294,17012,1107575424,4294954924,4294967294,17012,1107575424,4294954924,4294967294,
17088,17088,17280,17280,17472,17472,17664,17664,17664,17664,17664,17664,40960,44032,48128,51200,55296,58368,62464,65536,62464,65536,62464,65536,268439552,336991254,410,1617043456,80,7984,43008,1048526463,50465804,16780300,36,80,224,1119092736,1119092736,1119092736,1098907648,1098907648,0,132264,1073741824,3240,44032,44032,40960,705408,1073741824,17280,17074,1107575480,4294948652,4294967294,17074,1107575480,4294948652,4294967294,17074,1107575480,4294948652,4294967294,
4773888,4773888,4816896,4816896,4859904,4859904,4902912,4902912,4902912,4902912,4902912,4902912,40960,44032,48128,51200,55296,58368,62464,65536,62464,65536,62464,65536,268439552,336991254,57,1890193408,80,7488,192,1048526591,16780300,50465804,576,36,224,1121976320,1121976320,1121976320,1098907648,1098907648,0,903744,1073741824,903168,44032,44032,40960,4819968,1073741824,4816896,17118,1107575524,4294943724,4294967294,17118,1107575524,4294943724,4294967294,17118,1107575524,4294943724,4294967294,
6236160,6279168,6322176,6365184,6408192,6451200,6494208,6537216,6537216,6537216,6537216,6537216,40960,45056,49152,53248,57344,61440,65536,69632,69632,69632,69632,69632,538976288,353703189,57,2419720448,80,7488,192,1056914943,16780300,50465804,576,36,128,1125515264,1125384192,1125253120,1107296256,1107296256,1107296256,1210752,1194624,1178496,57344,49152,40960,6457344,6371328,6285312,1106264849,1112032024,4294949836,30,1106264849,1112032024,4294949836,30,1106264849,1112032024,4294949836,30,
6580224,6580224,6623232,6623232,6666240,6666240,6709248,6709248,6709248,6709248,6709248,6709248,40960,44032,48128,51200,55296,58368,62464,65536,62464,65536,62464,65536,268439552,336991254,57,2692878336,80,7488,192,1056915199,16780300,50465804,576,36,224,1125777408,1125777408,1125777408,1098907648,1098907648,0,1242432,1073741824,1241856,44032,44032,40960,6626304,1073741824,6623232,17177,1107575580,4294934316,4294967294,17177,1107575580,4294934316,4294967294,17177,1107575580,4294934316,4294967294,
344064,387072,430080,473088,516096,559104,9074688,9117696,9117696,9117696,9117696,9117696,49152,53248,57344,61440,65536,69632,40960,45056,45056,45056,45056,45056,2964324352,320017685,57,2966041088,80,7488,192,1056915199,16780300,50465804,576,36,128,1129447424,1129447424,1092616192,1127219200,1127219200,0,1699776,1073741824,80640,32768,32768,53248,9065472,1073741824,430080,1127170889,1128285012,4294940524,174,1127170889,1128285012,4294940524,174,16640,1099972944,524,4294967294,
8558592,8558592,8601600,8601600,8644608,8644608,8687616,8687616,8687616,8687616,8687616,8687616,40960,44032,48128,51200,55296,58368,62464,65536,62464,65536,62464,65536,268439552,336991254,57,3495563264,80,7488,192,1056915071,16780300,50465804,576,36,224,1128792064,1128792064,1128792064,1098907648,1098907648,0,1613376,1073741824,1612800,44032,44032,40960,8604672,1073741824,8601600,17223,1107575626,4294924012,4294967294,17223,1107575626,4294924012,4294967294,17223,1107575626,4294924012,4294967294,
7104,7104,7296,7296,7488,7488,7680,7680,7680,7680,7680,7680,40960,44032,48128,51200,55296,58368,62464,65536,62464,65536,62464,65536,268439552,336991254,410,816455680,80,7984,43008,1040137983,50465804,16780300,36,80,224,1108869120,1108869120,1108869120,1098907648,1098907648,0,130392,1073741824,1368,44032,44032,40960,695424,1073741824,7296,16916,1107575328,4294960300,4294967294,16916,1107575328,4294960300,4294967294,16916,1107575328,4294960300,4294967294,
2709504,2709504,2752512,2752512,2795520,2795520,2838528,2838528,2838528,2838528,2838528,2838528,40960,44032,48128,51200,55296,58368,62464,65536,62464,65536,62464,65536,268439552,336991254,57,1344933888,80,7488,192,1048526335,16780300,50465804,576,36,224,1115684864,1115684864,1115684864,1098907648,1098907648,0,516672,1073741824,516096,44032,44032,40960,2755584,1073741824,2752512,17020,1107575428,4294954476,4294967294,17020,1107575428,4294954476,4294967294,17020,1107575428,4294954476,4294967294,
17472,17472,17664,17664,17856,17856,18048,18048,18048,18048,18048,18048,40960,44032,48128,51200,55296,58368,62464,65536,62464,65536,62464,65536,268439552,336991254,410,1617567744,80,7984,43008,1048526463,50465804,16780300,36,80,224,1119354880,1119354880,1119354880,1098907648,1098907648,0,132336,1073741824,3312,44032,44032,40960,705792,1073741824,17664,17078,1107575484,4294948204,4294967294,17078,1107575484,4294948204,4294967294,17078,1107575484,4294948204,4294967294,
4859904,4859904,4902912,4902912,4945920,4945920,4988928,4988928,4988928,4988928,4988928,4988928,40960,44032,48128,51200,55296,58368,62464,65536,62464,65536,62464,65536,268439552,336991254,57,1890717696,80,7488,192,1048526591,16780300,50465804,576,36,224,1122238464,1122238464,1122238464,1098907648,1098907648,0,919872,1073741824,919296,44032,44032,40960,4905984,1073741824,4902912,17122,1107575528,4294943276,4294967294,17122,1107575528,4294943276,4294967294,17122,1107575528,4294943276,4294967294,
24768,24768,24960,24960,25152,25152,25344,25344,25344,25344,25344,25344,40960,44032,48128,51200,55296,58368,62464,65536,62464,65536,62464,65536,268439552,336991254,410,2420252672,80,7984,43008,1056915071,50465804,16780300,36,80,224,1124204544,1124204544,1124204544,1098907648,1098907648,0,133704,1073741824,4680,44032,44032,40960,713088,1073741824,24960,17153,1107575556,4294939692,4294967294,17153,1107575556,4294939692,4294967294,17153,1107575556,4294939692,4294967294,
6666240,6666240,6709248,6709248,6752256,6752256,6795264,6795264,6795264,6795264,6795264,6795264,40960,44032,48128,51200,55296,58368,62464,65536,62464,65536,62464,65536,268439552,336991254,57,2693402624,80,7488,192,1056915199,16780300,50465804,576,36,224,1125908480,1125908480,1125908480,1098907648,1098907648,0,1258560,1073741824,1257984,44032,44032,40960,6712320,1073741824,6709248,17179,1107575582,4294933868,4294967294,17179,1107575582,4294933868,4294967294,17179,1107575582,4294933868,4294967294,
1290240,1333248,1376256,1419264,1462272,9504768,9547776,9590784,9590784,9590784,9590784,9590784,57344,61440,65536,69632,73728,40960,45056,49152,53248,53248,53248,53248,2964369584,353703189,57,2966684928,64,7488,192,1056915199,16780300,50465804,576,36,128,1130102784,1130233856,1107296256,1127219200,1127219200,1127219200,1780416,1796544,264384,32768,40960,61440,9495552,9581568,1410048,1127170899,1128285023,4294939244,174,1127170899,1128285023,4294939244,174,1127105008,1128415752,4294964204,174,
9074688,9117696,9160704,9203712,9246720,9289728,9332736,9375744,9375744,9375744,9375744,9375744,40960,45056,49152,53248,57344,61440,65536,69632,69632,69632,69632,69632,538976288,353703189,57,3496083712,80,7488,192,1056915071,16780300,50465804,576,36,128,1129840640,1129709568,1129578496,1107296256,1107296256,1107296256,1742976,1726848,1710720,57344,49152,40960,9295872,9209856,9123840,1106264915,1112032090,4294941388,30,1106264915,1112032090,4294941388,30,1106264915,1112032090,4294941388,30,
7488,7488,7680,7680,7872,7872,8064,8064,8064,8064,8064,8064,40960,44032,48128,51200,55296,58368,62464,65536,62464,65536,62464,65536,268439552,336991254,410,816979968,80,7984,43008,1040137983,50465804,16780300,36,80,224,1109393408,1109393408,1109393408,1098907648,1098907648,0,130464,1073741824,1440,44032,44032,40960,695808,1073741824,7680,16924,1107575336,4294959852,4294967294,16924,1107575336,4294959852,4294967294,16924,1107575336,4294959852,4294967294,
2795520,2795520,2838528,2838528,2881536,2881536,2924544,2924544,2924544,2924544,2924544,2924544,40960,44032,48128,51200,55296,58368,62464,65536,62464,65536,62464,65536,268439552,336991254,57,1345458176,80,7488,192,1048526335,16780300,50465804,576,36,224,1115947008,1115947008,1115947008,1098907648,1098907648,0,532800,1073741824,532224,44032,44032,40960,2841600,1073741824,2838528,17026,1107575432,4294954028,4294967294,17026,1107575432,4294954028,4294967294,17026,1107575432,4294954028,4294967294,
4429824,4472832,4515840,4558848,4601856,4644864,4687872,4730880,4730880,4730880,4730880,4730880,40960,45056,49152,53248,57344,61440,65536,69632,69632,69632,69632,69632,538976288,353703189,57,1618084096,80,7488,192,1048526463,16780300,50465804,576,36,128,1121452032,1121189888,1120927744,1107296256,1107296256,1107296256,872064,855936,839808,57344,49152,40960,4651008,4564992,4478976,1106264782,1112031964,4294955212,30,1106264782,1112031964,4294955212,30,1106264782,1112031964,4294955212,30,
4945920,4945920,4988928,4988928,5031936,5031936,5074944,5074944,5074944,5074944,5074944,5074944,40960,44032,48128,51200,55296,58368,62464,65536,62464,65536,62464,65536,268439552,336991254,57,1891241984,80,7488,192,1048526591,16780300,50465804,576,36,224,1122500608,1122500608,1122500608,1098907648,1098907648,0,936000,1073741824,935424,44032,44032,40960,4992000,1073741824,4988928,17126,1107575532,4294942828,4294967294,17126,1107575532,4294942828,4294967294,17126,1107575532,4294942828,4294967294,
25152,25152,25344,25344,25536,25536,25728,25728,25728,25728,25728,25728,40960,44032,48128,51200,55296,58368,62464,65536,62464,65536,62464,65536,268439552,336991254,410,2420776960,80,7984,43008,1056915071,50465804,16780300,36,80,224,1124335616,1124335616,1124335616,1098907648,1098907648,0,133776,1073741824,4752,44032,44032,40960,713472,1073741824,25344,17155,1107575558,4294939244,4294967294,17155,1107575558,4294939244,4294967294,17155,1107575558,4294939244,4294967294,
7268352,7311360,7354368,7397376,7440384,7483392,7526400,7569408,7569408,7569408,7569408,7569408,40960,45056,49152,53248,57344,61440,65536,69632,69632,69632,69632,69632,538976288,353703189,57,2693923072,80,7488,192,1056915199,16780300,50465804,576,36,128,1127088128,1126957056,1126825984,1107296256,1107296256,1107296256,1404288,1388160,1372032,57344,49152,40960,7489536,7403520,7317504,1106264873,1112032048,4294946764,30,1106264873,1112032048,4294946764,30,1106264873,1112032048,4294946764,30,
33984,33984,34176,34176,34368,34368,34560,34560,34560,34560,34560,34560,40960,44032,48128,51200,55296,58368,62464,65536,62464,65536,62464,65536,268439552,336991254,410,3223461888,80,7984,43008,1056914943,50465804,16780300,36,80,224,1127350272,1127350272,1127350272,1098907648,1098907648,0,135432,1073741824,6408,44032,44032,40960,722304,1073741824,34176,17201,1107575604,4294928940,4294967294,17201,1107575604,4294928940,4294967294,17201,1107575604,4294928940,4294967294,
8644608,8644608,8687616,8687616,8730624,8730624,8773632,8773632,8773632,8773632,8773632,8773632,40960,44032,48128,51200,55296,58368,62464,65536,62464,65536,62464,65536,268439552,336991254,57,3496611840,80,7488,192,1056915071,16780300,50465804,576,36,224,1128923136,1128923136,1128923136,1098907648,1098907648,0,1629504,1073741824,1628928,44032,44032,40960,8690688,1073741824,8687616,17225,1107575628,4294923564,4294967294,17225,1107575628,4294923564,4294967294,17225,1107575628,4294923564,4294967294,
2107392,2150400,2193408,2236416,2279424,2322432,2365440,2408448,2408448,2408448,2408448,2408448,40960,45056,49152,53248,57344,61440,65536,69632,69632,69632,69632,69632,538976288,353703189,57,817496320,80,7488,192,1040137983,16780300,50465804,576,36,128,1113063424,1112539136,1112014848,1107296256,1107296256,1107296256,436608,420480,404352,57344,49152,40960,2328576,2242560,2156544,1106264644,1112031840,4294962124,30,1106264644,1112031840,4294962124,30,1106264644,1112031840,4294962124,30,
2881536,2881536,2924544,2924544,2967552,2967552,3010560,3010560,3010560,3010560,3010560,3010560,40960,44032,48128,51200,55296,58368,62464,65536,62464,65536,62464,65536,268439552,336991254,57,1345982464,80,7488,192,1048526335,16780300,50465804,576,36,224,1116209152,1116209152,1116209152,1098907648,1098907648,0,548928,1073741824,548352,44032,44032,40960,2927616,1073741824,2924544,17030,1107575436,4294953580,4294967294,17030,1107575436,4294953580,4294967294,17030,1107575436,4294953580,4294967294,
17856,17856,18048,18048,18240,18240,18432,18432,18432,18432,18432,18432,40960,44032,48128,51200,55296,58368,62464,65536,62464,65536,62464,65536,268439552,336991254,410,1618616320,80,7984,43008,1048526463,50465804,16780300,36,80,224,1119617024,1119617024,1119617024,1098907648,1098907648,0,132408,1073741824,3384,44032,44032,40960,706176,1073741824,18048,17082,1107575488,4294947756,4294967294,17082,1107575488,4294947756,4294967294,17082,1107575488,4294947756,4294967294,
774144,817152,860160,903168,946176,989184,8730624,8773632,8773632,8773632,8773632,8773632,49152,53248,57344,61440,65536,69632,40960,45056,45056,45056,45056,45056,1886388224,320017685,57,1891774464,80,7488,192,1048526591,16780300,50465804,576,36,128,1128923136,1128923136,1101004800,1121976320,1121976320,0,1632960,1073741824,161280,32768,32768,53248,8709120,1073741824,860160,1121862465,1124090700,4294941932,110,1121862465,1124090700,4294941932,110,16784,1099973048,4294966540,4294967294,
25536,25536,25728,25728,25920,25920,26112,26112,26112,26112,26112,26112,40960,44032,48128,51200,55296,58368,62464,65536,62464,65536,62464,65536,268439552,336991254,410,2421301248,80,7984,43008,1056915071,50465804,16780300,36,80,224,1124466688,1124466688,1124466688,1098907648,1098907648,0,133848,1073741824,4824,44032,44032,40960,713856,1073741824,25728,17157,1107575560,4294938796,4294967294,17157,1107575560,4294938796,4294967294,17157,1107575560,4294938796,4294967294,
6752256,6752256,6795264,6795264,6838272,6838272,6881280,6881280,6881280,6881280,6881280,6881280,40960,44032,48128,51200,55296,58368,62464,65536,62464,65536,62464,65536,268439552,336991254,57,2694451200,80,7488,192,1056915199,16780300,50465804,576,36,224,1126039552,1126039552,1126039552,1098907648,1098907648,0,1274688,1073741824,1274112,44032,44032,40960,6798336,1073741824,6795264,17181,1107575584,4294933420,4294967294,17181,1107575584,4294933420,4294967294,17181,1107575584,4294933420,4294967294,
34368,34368,34560,34560,34752,34752,34944,34944,34944,34944,34944,34944,40960,44032,48128,51200,55296,58368,62464,65536,62464,65536,62464,65536,268439552,336991254,410,3223986176,80,7984,43008,1056914943,50465804,16780300,36,80,224,1127481344,1127481344,1127481344,1098907648,1098907648,0,135504,1073741824,6480,44032,44032,40960,722688,1073741824,34560,17203,1107575606,4294928492,4294967294,17203,1107575606,4294928492,4294967294,17203,1107575606,4294928492,4294967294,
8730624,8730624,8773632,8773632,8816640,8816640,8859648,8859648,8859648,8859648,8859648,8859648,40960,44032,48128,51200,55296,58368,62464,65536,62464,65536,62464,65536,268439552,336991254,57,3497136128,80,7488,192,1056915071,16780300,50465804,576,36,224,1129054208,1129054208,1129054208,1098907648,1098907648,0,1645632,1073741824,1645056,44032,44032,40960,8776704,1073741824,8773632,17227,1107575630,4294923116,4294967294,17227,1107575630,4294923116,4294967294,17227,1107575630,4294923116,4294967294,
7872,7872,8064,8064,8256,8256,8448,8448,8448,8448,8448,8448,40960,44032,48128,51200,55296,58368,62464,65536,62464,65536,62464,65536,268439552,336991254,410,818028544,80,7984,43008,1040137983,50465804,16780300,36,80,224,1109917696,1109917696,1109917696,1098907648,1098907648,0,130536,1073741824,1512,44032,44032,40960,696192,1073741824,8064,16932,1107575344,4294959404,4294967294,16932,1107575344,4294959404,4294967294,16932,1107575344,4294959404,4294967294,
2967552,2967552,3010560,3010560,3053568,3053568,3096576,3096576,3096576,3096576,3096576,3096576,40960,44032,48128,51200,55296,58368,62464,65536,62464,65536,62464,65536,268439552,336991254,57,1346506752,80,7488,192,1048526463,16780300,50465804,576,36,224,1116471296,1116471296,1116471296,1098907648,1098907648,0,565056,1073741824,564480,44032,44032,40960,3013632,1073741824,3010560,17034,1107575440,4294953132,4294967294,17034,1107575440,4294953132,4294967294,17034,1107575440,4294953132,4294967294,
18240,18240,18432,18432,18624,18624,18816,18816,18816,18816,18816,18816,40960,44032,48128,51200,55296,58368,62464,65536,62464,65536,62464,65536,268439552,336991254,410,1619140608,80,7984,43008,1048526591,50465804,16780300,36,80,224,1119879168,1119879168,1119879168,1098907648,1098907648,0,132480,1073741824,3456,44032,44032,40960,706560,1073741824,18432,17086,1107575492,4294947308,4294967294,17086,1107575492,4294947308,4294967294,17086,1107575492,4294947308,4294967294,
860160,903168,946176,989184,1032192,1075200,9160704,9203712,9203712,9203712,9203712,9203712,49152,53248,57344,61440,65536,69632,40960,45056,45056,45056,45056,45056,1886388224,320017685,57,1892429824,80,7488,192,1048526591,16780300,50465804,576,36,128,1129578496,1129578496,1102053376,1121976320,1121976320,0,1713600,1073741824,177408,32768,32768,53248,9139200,1073741824,946176,1121862475,1124090710,4294940652,110,1121862475,1124090710,4294940652,110,16800,1099973064,4294966284,4294967294,
25920,25920,26112,26112,26304,26304,26496,26496,26496,26496,26496,26496,40960,44032,48128,51200,55296,58368,62464,65536,62464,65536,62464,65536,268439552,336991254,410,2421825536,80,7984,43008,1056915071,50465804,16780300,36,80,224,1124597760,1124597760,1124597760,1098907648,1098907648,0,133920,1073741824,4896,44032,44032,40960,714240,1073741824,26112,17159,1107575562,4294938348,4294967294,17159,1107575562,4294938348,4294967294,17159,1107575562,4294938348,4294967294,
6838272,6838272,6881280,6881280,6924288,6924288,6967296,6967296,6967296,6967296,6967296,6967296,40960,44032,48128,51200,55296,58368,62464,65536,62464,65536,62464,65536,268439552,336991254,57,2694975488,80,7488,192,1056915199,16780300,50465804,576,36,224,1126170624,1126170624,1126170624,1098907648,1098907648,0,1290816,1073741824,1290240,44032,44032,40960,6884352,1073741824,6881280,17183,1107575586,4294932972,4294967294,17183,1107575586,4294932972,4294967294,17183,1107575586,4294932972,4294967294,
34752,34752,34944,34944,35136,35136,35328,35328,35328,35328,35328,35328,40960,44032,48128,51200,55296,58368,62464,65536,62464,65536,62464,65536,268439552,336991254,410,3224510464,80,7984,43008,1056914943,50465804,16780300,36,80,224,1127612416,1127612416,1127612416,1098907648,1098907648,0,135576,1073741824,6552,44032,44032,40960,723072,1073741824,34944,17205,1107575608,4294928044,4294967294,17205,1107575608,4294928044,4294967294,17205,1107575608,4294928044,4294967294,
8816640,8816640,8859648,8859648,8902656,8902656,8945664,8945664,8945664,8945664,8945664,8945664,40960,44032,48128,51200,55296,58368,62464,65536,62464,65536,62464,65536,268439552,336991254,57,3497660416,80,7488,192,1056915071,16780300,50465804,576,36,224,1129185280,1129185280,1129185280,1098907648,1098907648,0,1661760,1073741824,1661184,44032,44032,40960,8862720,1073741824,8859648,17229,1107575632,4294922668,4294967294,17229,1107575632,4294922668,4294967294,17229,1107575632,4294922668,4294967294,
8256,8256,8448,8448,8640,8640,8832,8832,8832,8832,8832,8832,40960,44032,48128,51200,55296,58368,62464,65536,62464,65536,62464,65536,268439552,336991254,410,818552832,80,7984,43008,1040137983,50465804,16780300,36,80,224,1110441984,1110441984,1110441984,1098907648,1098907648,0,130608,1073741824,1584,44032,44032,40960,696576,1073741824,8448,16940,1107575352,4294958956,4294967294,16940,1107575352,4294958956,4294967294,16940,1107575352,4294958956,4294967294,
3397632,3440640,3483648,3526656,3569664,3612672,3655680,3698688,3698688,3698688,3698688,3698688,40960,45056,49152,53248,57344,61440,65536,69632,69632,69632,69632,69632,538976288,353703189,57,1347027200,80,7488,192,1048526463,16780300,50465804,576,36,128,1118306304,1118044160,1117782016,1107296256,1107296256,1107296256,678528,662400,646272,57344,49152,40960,3618816,3532800,3446784,1106264734,1112031916,4294958284,30,1106264734,1112031916,4294958284,30,1106264734,1112031916,4294958284,30,
18624,18624,18816,18816,19008,19008,19200,19200,19200,19200,19200,19200,40960,44032,48128,51200,55296,58368,62464,65536,62464,65536,62464,65536,268439552,336991254,410,1619664896,80,7984,43008,1048526591,50465804,16780300,36,80,224,1120141312,1120141312,1120141312,1098907648,1098907648,0,132552,1073741824,3528,44032,44032,40960,706944,1073741824,18816,17090,1107575496,4294946860,4294967294,17090,1107575496,4294946860,4294967294,17090,1107575496,4294946860,4294967294,
946176,989184,1032192,1075200,1118208,1161216,9590784,9590784,9590784,9590784,9590784,9590784,49152,53248,57344,61440,65536,69632,40960,45056,45056,45056,45056,45056,1886388224,320017685,57,1893085184,80,7488,192,1048526591,16780300,50465804,576,36,128,1130233856,1130233856,1103101952,1121976320,1121976320,0,1794240,1073741824,193536,32768,32768,53248,9569280,1073741824,1032192,1121862485,1124090719,4294939372,110,1121862485,1124090719,4294939372,110,16816,1099973080,4294966028,4294967294,
26304,26304,26496,26496,26688,26688,26880,26880,26880,26880,26880,26880,40960,44032,48128,51200,55296,58368,62464,65536,62464,65536,62464,65536,268439552,336991254,410,2422349824,80,7984,43008,1056915071,50465804,16780300,36,80,224,1124728832,1124728832,1124728832,1098907648,1098907648,0,133992,1073741824,4968,44032,44032,40960,714624,1073741824,26496,17161,1107575564,4294937900,4294967294,17161,1107575564,4294937900,4294967294,17161,1107575564,4294937900,4294967294,
6924288,6924288,6967296,6967296,7010304,7010304,7053312,7053312,7053312,7053312,7053312,7053312,40960,44032,48128,51200,55296,58368,62464,65536,62464,65536,62464,65536,268439552,336991254,57,2695499776,80,7488,192,1056915199,16780300,50465804,576,36,224,1126301696,1126301696,1126301696,1098907648,1098907648,0,1306944,1073741824,1306368,44032,44032,40960,6970368,1073741824,6967296,17185,1107575588,4294932524,4294967294,17185,1107575588,4294932524,4294967294,17185,1107575588,4294932524,4294967294,
35136,35136,35328,35328,35520,35520,35712,35712,35712,35712,35712,35712,40960,44032,48128,51200,55296,58368,62464,65536,62464,65536,62464,65536,268439552,336991254,410,3225034752,80,7984,43008,1056914943,50465804,16780300,36,80,224,1127743488,1127743488,1127743488,1098907648,1098907648,0,135648,1073741824,6624,44032,44032,40960,723456,1073741824,35328,17207,1107575610,4294927596,4294967294,17207,1107575610,4294927596,4294967294,17207,1107575610,4294927596,4294967294,
8902656,8902656,8945664,8945664,8988672,8988672,9031680,9031680,9031680,9031680,9031680,9031680,40960,44032,48128,51200,55296,58368,62464,65536,62464,65536,62464,65536,268439552,336991254,57,3498184704,80,7488,192,1056915199,16780300,50465804,576,36,224,1129316352,1129316352,1129316352,1098907648,1098907648,0,1677888,1073741824,1677312,44032,44032,40960,8948736,1073741824,8945664,17231,1107575634,4294922220,4294967294,17231,1107575634,4294922220,4294967294,17231,1107575634,4294922220,4294967294,
8640,8640,8832,8832,9024,9024,9216,9216,9216,9216,9216,9216,40960,44032,48128,51200,55296,58368,62464,65536,62464,65536,62464,65536,268439552,336991254,410,819077120,80,7984,43008,1040137983,50465804,16780300,36,80,224,1110966272,1110966272,1110966272,1098907648,1098907648,0,130680,1073741824,1656,44032,44032,40960,696960,1073741824,8832,16948,1107575360,4294958508,4294967294,16948,1107575360,4294958508,4294967294,16948,1107575360,4294958508,4294967294,
3053568,3053568,3096576,3096576,3139584,3139584,3182592,3182592,3182592,3182592,3182592,3182592,40960,44032,48128,51200,55296,58368,62464,65536,62464,65536,62464,65536,268439552,336991254,57,1347555328,80,7488,192,1048526463,16780300,50465804,576,36,224,1116733440,1116733440,1116733440,1098907648,1098907648,0,581184,1073741824,580608,44032,44032,40960,3099648,1073741824,3096576,17038,1107575444,4294952684,4294967294,17038,1107575444,4294952684,4294967294,17038,1107575444,4294952684,4294967294,
19008,19008,19200,19200,19392,19392,19584,19584,19584,19584,19584,19584,40960,44032,48128,51200,55296,58368,62464,65536,62464,65536,62464,65536,268439552,336991254,410,1620189184,80,7984,43008,1048526591,50465804,16780300,36,80,224,1120403456,1120403456,1120403456,1098907648,1098907648,0,132624,1073741824,3600,44032,44032,40960,707328,1073741824,19200,17094,1107575500,4294946412,4294967294,17094,1107575500,4294946412,4294967294,17094,1107575500,4294946412,4294967294,
5462016,5505024,5548032,5591040,5634048,5677056,5720064,5763072,5763072,5763072,5763072,5763072,40960,45056,49152,53248,57344,61440,65536,69632,69632,69632,69632,69632,538976288,353703189,57,2149712128,80,7488,192,1048526335,16780300,50465804,576,36,128,1124335616,1124204544,1124073472,1107296256,1107296256,1107296256,1065600,1049472,1033344,57344,49152,40960,5683200,5597184,5511168,1106264830,1112032006,4294952140,30,1106264830,1112032006,4294952140,30,1106264830,1112032006,4294952140,30,
26688,26688,26880,26880,27072,27072,27264,27264,27264,27264,27264,27264,40960,44032,48128,51200,55296,58368,62464,65536,62464,65536,62464,65536,268439552,336991254,410,2422874112,80,7984,43008,1056915071,50465804,16780300,36,80,224,1124859904,1124859904,1124859904,1098907648,1098907648,0,134064,1073741824,5040,44032,44032,40960,715008,1073741824,26880,17163,1107575566,4294937452,4294967294,17163,1107575566,4294937452,4294967294,17163,1107575566,4294937452,4294967294,
7010304,7010304,7053312,7053312,7096320,7096320,7139328,7139328,7139328,7139328,7139328,7139328,40960,44032,48128,51200,55296,58368,62464,65536,62464,65536,62464,65536,268439552,336991254,57,2696024064,80,7488,192,1056915199,16780300,50465804,576,36,224,1126432768,1126432768,1126432768,1098907648,1098907648,0,1323072,1073741824,1322496,44032,44032,40960,7056384,1073741824,7053312,17187,1107575590,4294932076,4294967294,17187,1107575590,4294932076,4294967294,17187,1107575590,4294932076,4294967294,
8300544,8343552,8386560,8429568,8472576,8515584,8558592,8601600,8601600,8601600,8601600,8601600,40960,45056,49152,53248,57344,61440,65536,69632,69632,69632,69632,69632,538976288,353703189,57,3225551104,80,7488,192,1056915071,16780300,50465804,576,36,128,1128660992,1128529920,1128398848,1107296256,1107296256,1107296256,1597824,1581696,1565568,57344,49152,40960,8521728,8435712,8349696,1106264897,1112032072,4294943692,30,1106264897,1112032072,4294943692,30,1106264897,1112032072,4294943692,30,
8988672,8988672,9031680,9031680,9074688,9074688,9117696,9117696,9117696,9117696,9117696,9117696,40960,44032,48128,51200,55296,58368,62464,65536,62464,65536,62464,65536,268439552,336991254,57,3498708992,80,7488,192,1056915199,16780300,50465804,576,36,224,1129447424,1129447424,1129447424,1098907648,1098907648,0,1694016,1073741824,1693440,44032,44032,40960,9034752,1073741824,9031680,17233,1107575636,4294921772,4294967294,17233,1107575636,4294921772,4294967294,17233,1107575636,4294921772,4294967294,
9024,9024,9216,9216,9408,9408,9600,9600,9600,9600,9600,9600,40960,44032,48128,51200,55296,58368,62464,65536,62464,65536,62464,65536,268439552,336991254,410,819601408,80,7984,43008,1040137983,50465804,16780300,36,80,224,1111490560,1111490560,1111490560,1098907648,1098907648,0,130752,1073741824,1728,44032,44032,40960,697344,1073741824,9216,16956,1107575368,4294958060,4294967294,16956,1107575368,4294958060,4294967294,16956,1107575368,4294958060,4294967294,
3139584,3139584,3182592,3182592,3225600,3225600,3268608,3268608,3268608,3268608,3268608,3268608,40960,44032,48128,51200,55296,58368,62464,65536,62464,65536,62464,65536,268439552,336991254,57,1348079616,80,7488,192,1048526463,16780300,50465804,576,36,224,1116995584,1116995584,1116995584,1098907648,1098907648,0,597312,1073741824,596736,44032,44032,40960,3185664,1073741824,3182592,17042,1107575448,4294952236,4294967294,17042,1107575448,4294952236,4294967294,17042,1107575448,4294952236,4294967294,
19392,19392,19584,19584,19776,19776,19968,19968,19968,19968,19968,19968,40960,44032,48128,51200,55296,58368,62464,65536,62464,65536,62464,65536,268439552,336991254,410,1620713472,80,7984,43008,1048526591,50465804,16780300,36,80,224,1120665600,1120665600,1120665600,1098907648,1098907648,0,132696,1073741824,3672,44032,44032,40960,707712,1073741824,19584,17098,1107575504,4294945964,4294967294,17098,1107575504,4294945964,4294967294,17098,1107575504,4294945964,4294967294,
5031936,5031936,5074944,5074944,5117952,5117952,5160960,5160960,5160960,5160960,5160960,5160960,40960,44032,48128,51200,55296,58368,62464,65536,62464,65536,62464,65536,268439552,336991254,57,2150240256,80,7488,192,1048526335,16780300,50465804,576,36,224,1122762752,1122762752,1122762752,1098907648,1098907648,0,952128,1073741824,951552,44032,44032,40960,5078016,1073741824,5074944,17130,1107575536,4294942380,4294967294,17130,1107575536,4294942380,4294967294,17130,1107575536,4294942380,4294967294,
27072,27072,27264,27264,27456,27456,27648,27648,27648,27648,27648,27648,40960,44032,48128,51200,55296,58368,62464,65536,62464,65536,62464,65536,268439552,336991254,410,2423398400,80,7984,43008,1056915071,50465804,16780300,36,80,224,1124990976,1124990976,1124990976,1098907648,1098907648,0,134136,1073741824,5112,44032,44032,40960,715392,1073741824,27264,17165,1107575568,4294937004,4294967294,17165,1107575568,4294937004,4294967294,17165,1107575568,4294937004,4294967294,
7096320,7096320,7139328,7139328,7182336,7182336,7225344,7225344,7225344,7225344,7225344,7225344,40960,44032,48128,51200,55296,58368,62464,65536,62464,65536,62464,65536,268439552,336991254,57,2696548352,80,7488,192,1056915199,16780300,50465804,576,36,224,1126563840,1126563840,1126563840,1098907648,1098907648,0,1339200,1073741824,1338624,44032,44032,40960,7142400,1073741824,7139328,17189,1107575592,4294931628,4294967294,17189,1107575592,4294931628,4294967294,17189,1107575592,4294931628,4294967294,
35520,35520,35712,35712,35904,35904,36096,36096,36096,36096,36096,36096,40960,44032,48128,51200,55296,58368,62464,65536,62464,65536,62464,65536,268439552,336991254,410,3226083328,80,7984,43008,1056915071,50465804,16780300,36,80,224,1127874560,1127874560,1127874560,1098907648,1098907648,0,135720,1073741824,6696,44032,44032,40960,723840,1073741824,35712,17209,1107575612,4294927148,4294967294,17209,1107575612,4294927148,4294967294,17209,1107575612,4294927148,4294967294,
9074688,9074688,9117696,9117696,9160704,9160704,9203712,9203712,9203712,9203712,9203712,9203712,40960,44032,48128,51200,55296,58368,62464,65536,62464,65536,62464,65536,268439552,336991254,57,3499233280,80,7488,192,1056915199,16780300,50465804,576,36,224,1129578496,1129578496,1129578496,1098907648,1098907648,0,1710144,1073741824,1709568,44032,44032,40960,9120768,1073741824,9117696,17235,1107575638,4294921324,4294967294,17235,1107575638,4294921324,4294967294,17235,1107575638,4294921324,4294967294,
9408,9408,9600,9600,9792,9792,9984,9984,9984,9984,9984,9984,40960,44032,48128,51200,55296,58368,62464,65536,62464,65536,62464,65536,268439552,336991254,410,1075978240,80,7984,43008,1040137727,50465804,16780300,36,80,224,1112014848,1112014848,1112014848,1098907648,1098907648,0,130824,1073741824,1800,44032,44032,40960,697728,1073741824,9600,16964,1107575376,4294957612,4294967294,16964,1107575376,4294957612,4294967294,16964,1107575376,4294957612,4294967294,
3225600,3225600,3268608,3268608,3311616,3311616,3354624,3354624,3354624,3354624,3354624,3354624,40960,44032,48128,51200,55296,58368,62464,65536,62464,65536,62464,65536,268439552,336991254,57,1348603904,80,7488,192,1048526463,16780300,50465804,576,36,224,1117257728,1117257728,1117257728,1098907648,1098907648,0,613440,1073741824,612864,44032,44032,40960,3271680,1073741824,3268608,17046,1107575452,4294951788,4294967294,17046,1107575452,4294951788,4294967294,17046,1107575452,4294951788,4294967294,
19776,19776,19968,19968,20160,20160,20352,20352,20352,20352,20352,20352,40960,44032,48128,51200,55296,58368,62464,65536,62464,65536,62464,65536,268439552,336991254,410,1621237760,80,7984,43008,1048526591,50465804,16780300,36,80,224,1120927744,1120927744,1120927744,1098907648,1098907648,0,132768,1073741824,3744,44032,44032,40960,708096,1073741824,19968,17102,1107575508,4294945516,4294967294,17102,1107575508,4294945516,4294967294,17102,1107575508,4294945516,4294967294,
5117952,5117952,5160960,5160960,5203968,5203968,5246976,5246976,5246976,5246976,5246976,5246976,40960,44032,48128,51200,55296,58368,62464,65536,62464,65536,62464,65536,268439552,336991254,57,2150764544,80,7488,192,1048526335,16780300,50465804,576,36,224,1123024896,1123024896,1123024896,1098907648,1098907648,0,968256,1073741824,967680,44032,44032,40960,5164032,1073741824,5160960,17134,1107575540,4294941932,4294967294,17134,1107575540,4294941932,4294967294,17134,1107575540,4294941932,4294967294,
6494208,6537216,6580224,6623232,6666240,6709248,6752256,6795264,6795264,6795264,6795264,6795264,40960,45056,49152,53248,57344,61440,65536,69632,69632,69632,69632,69632,538976288,353703189,57,2423914752,80,7488,192,1056915071,16780300,50465804,576,36,128,1125908480,1125777408,1125646336,1107296256,1107296256,1107296256,1259136,1243008,1226880,57344,49152,40960,6715392,6629376,6543360,1106264855,1112032030,4294949068,30,1106264855,1112032030,4294949068,30,1106264855,1112032030,4294949068,30,
7182336,7182336,7225344,7225344,7268352,7268352,7311360,7311360,7311360,7311360,7311360,7311360,40960,44032,48128,51200,55296,58368,62464,65536,62464,65536,62464,65536,268439552,336991254,57,2697072640,80,7488,192,1056915199,16780300,50465804,576,36,224,1126694912,1126694912,1126694912,1098907648,1098907648,0,1355328,1073741824,1354752,44032,44032,40960,7228416,1073741824,7225344,17191,1107575594,4294931180,4294967294,17191,1107575594,4294931180,4294967294,17191,1107575594,4294931180,4294967294,
35904,35904,36096,36096,36288,36288,36480,36480,36480,36480,36480,36480,40960,44032,48128,51200,55296,58368,62464,65536,62464,65536,62464,65536,268439552,336991254,410,3226607616,80,7984,43008,1056915071,50465804,16780300,36,80,224,1128005632,1128005632,1128005632,1098907648,1098907648,0,135792,1073741824,6768,44032,44032,40960,724224,1073741824,36096,17211,1107575614,4294926700,4294967294,17211,1107575614,4294926700,4294967294,17211,1107575614,4294926700,4294967294,
9160704,9160704,9203712,9203712,9246720,9246720,9289728,9289728,9289728,9289728,9289728,9289728,40960,44032,48128,51200,55296,58368,62464,65536,62464,65536,62464,65536,268439552,336991254,57,3499757568,80,7488,192,1056915199,16780300,50465804,576,36,224,1129709568,1129709568,1129709568,1098907648,1098907648,0,1726272,1073741824,1725696,44032,44032,40960,9206784,1073741824,9203712,17237,1107575640,4294920876,4294967294,17237,1107575640,4294920876,4294967294,17237,1107575640,4294920876,4294967294,
2365440,2408448,2451456,2494464,2537472,2580480,2623488,2666496,2666496,2666496,2666496,2666496,40960,45056,49152,53248,57344,61440,65536,69632,69632,69632,69632,69632,538976288,353703189,57,1076494592,80,7488,192,1040137727,16780300,50465804,576,36,128,1114636288,1114112000,1113587712,1107296256,1107296256,1107296256,484992,468864,452736,57344,49152,40960,2586624,2500608,2414592,1106264668,1112031864,4294961356,30,1106264668,1112031864,4294961356,30,1106264668,1112031864,4294961356,30,
3311616,3311616,3354624,3354624,3397632,3397632,3440640,3440640,3440640,3440640,3440640,3440640,40960,44032,48128,51200,55296,58368,62464,65536,62464,65536,62464,65536,268439552,336991254,57,1349128192,80,7488,192,1048526463,16780300,50465804,576,36,224,1117519872,1117519872,1117519872,1098907648,1098907648,0,629568,1073741824,628992,44032,44032,40960,3357696,1073741824,3354624,17050,1107575456,4294951340,4294967294,17050,1107575456,4294951340,4294967294,17050,1107575456,4294951340,4294967294,
4687872,4730880,4773888,4816896,4859904,4902912,4945920,4988928,4988928,4988928,4988928,4988928,40960,45056,49152,53248,57344,61440,65536,69632,69632,69632,69632,69632,538976288,353703189,57,1621754112,80,7488,192,1048526591,16780300,50465804,576,36,128,1122238464,1121976320,1121714176,1107296256,1107296256,1107296256,920448,904320,888192,57344,49152,40960,4909056,4823040,4737024,1106264794,1112031976,4294954444,30,1106264794,1112031976,4294954444,30,1106264794,1112031976,4294954444,30,
5203968,5203968,5246976,5246976,5289984,5289984,5332992,5332992,5332992,5332992,5332992,5332992,40960,44032,48128,51200,55296,58368,62464,65536,62464,65536,62464,65536,268439552,336991254,57,2151288832,80,7488,192,1048526335,16780300,50465804,576,36,224,1123287040,1123287040,1123287040,1098907648,1098907648,0,984384,1073741824,983808,44032,44032,40960,5250048,1073741824,5246976,17138,1107575544,4294941484,4294967294,17138,1107575544,4294941484,4294967294,17138,1107575544,4294941484,4294967294,
27456,27456,27648,27648,27840,27840,28032,28032,28032,28032,28032,28032,40960,44032,48128,51200,55296,58368,62464,65536,62464,65536,62464,65536,268439552,336991254,410,2424446976,80,7984,43008,1056915199,50465804,16780300,36,80,224,1125122048,1125122048,1125122048,1098907648,1098907648,0,134208,1073741824,5184,44032,44032,40960,715776,1073741824,27648,17167,1107575570,4294936556,4294967294,17167,1107575570,4294936556,4294967294,17167,1107575570,4294936556,4294967294,
258048,301056,344064,387072,430080,473088,9074688,9117696,9117696,9117696,9117696,9117696,49152,53248,57344,61440,65536,69632,40960,45056,45056,45056,45056,45056,2694840320,320017685,57,2697605632,80,7488,192,1056915199,16780300,50465804,576,36,128,1129447424,1129447424,1090519040,1126170624,1126170624,0,1699200,1073741824,64512,32768,32768,53248,9062400,1073741824,344064,1126122313,1127236436,4294940620,158,1126122313,1127236436,4294940620,158,16576,1099972912,780,4294967294,
36288,36288,36480,36480,36672,36672,36864,36864,36864,36864,36864,36864,40960,44032,48128,51200,55296,58368,62464,65536,62464,65536,62464,65536,268439552,336991254,410,3227131904,80,7984,43008,1056915071,50465804,16780300,36,80,224,1128136704,1128136704,1128136704,1098907648,1098907648,0,135864,1073741824,6840,44032,44032,40960,724608,1073741824,36480,17213,1107575616,4294926252,4294967294,17213,1107575616,4294926252,4294967294,17213,1107575616,4294926252,4294967294,
9332736,9375744,9418752,9461760,9504768,9547776,9590784,9590784,9590784,9590784,9590784,9590784,40960,45056,49152,53248,57344,61440,65536,69632,69632,69632,69632,69632,538976288,353703189,57,3500278016,80,7488,192,1056915199,16780300,50465804,576,36,128,1130233856,1130102784,1129971712,1107296256,1107296256,1107296256,1791360,1775232,1759104,57344,49152,40960,9553920,9467904,9381888,1106264921,1112032095,4294940620,30,1106264921,1112032095,4294940620,30,1106264921,1112032095,4294940620,30,
9792,9792,9984,9984,10176,10176,10368,10368,10368,10368,10368,10368,40960,44032,48128,51200,55296,58368,62464,65536,62464,65536,62464,65536,268439552,336991254,410,1077026816,80,7984,43008,1040137727,50465804,16780300,36,80,224,1112539136,1112539136,1112539136,1098907648,1098907648,0,130896,1073741824,1872,44032,44032,40960,698112,1073741824,9984,16972,1107575384,4294957164,4294967294,16972,1107575384,4294957164,4294967294,16972,1107575384,4294957164,4294967294,
3397632,3397632,3440640,3440640,3483648,3483648,3526656,3526656,3526656,3526656,3526656,3526656,40960,44032,48128,51200,55296,58368,62464,65536,62464,65536,62464,65536,268439552,336991254,57,1349652480,80,7488,192,1048526463,16780300,50465804,576,36,224,1117782016,1117782016,1117782016,1098907648,1098907648,0,645696,1073741824,645120,44032,44032,40960,3443712,1073741824,3440640,17054,1107575460,4294950892,4294967294,17054,1107575460,4294950892,4294967294,17054,1107575460,4294950892,4294967294,
20160,20160,20352,20352,20544,20544,20736,20736,20736,20736,20736,20736,40960,44032,48128,51200,55296,58368,62464,65536,62464,65536,62464,65536,268439552,336991254,410,1622286336,80,7984,43008,1048526591,50465804,16780300,36,80,224,1121189888,1121189888,1121189888,1098907648,1098907648,0,132840,1073741824,3816,44032,44032,40960,708480,1073741824,20352,17106,1107575512,4294945068,4294967294,17106,1107575512,4294945068,4294967294,17106,1107575512,4294945068,4294967294,
5289984,5289984,5332992,5332992,5376000,5376000,5419008,5419008,5419008,5419008,5419008,5419008,40960,44032,48128,51200,55296,58368,62464,65536,62464,65536,62464,65536,268439552,336991254,57,2151813120,80,7488,192,1048526463,16780300,50465804,576,36,224,1123549184,1123549184,1123549184,1098907648,1098907648,0,1000512,1073741824,999936,44032,44032,40960,5336064,1073741824,5332992,17142,1107575548,4294941036,4294967294,17142,1107575548,4294941036,4294967294,17142,1107575548,4294941036,4294967294,
27840,27840,28032,28032,28224,28224,28416,28416,28416,28416,28416,28416,40960,44032,48128,51200,55296,58368,62464,65536,62464,65536,62464,65536,268439552,336991254,410,2424971264,80,7984,43008,1056915199,50465804,16780300,36,80,224,1125253120,1125253120,1125253120,1098907648,1098907648,0,134280,1073741824,5256,44032,44032,40960,716160,1073741824,28032,17169,1107575572,4294936108,4294967294,17169,1107575572,4294936108,4294967294,17169,1107575572,4294936108,4294967294,
1290240,1333248,1376256,1419264,1462272,9504768,9547776,9590784,9590784,9590784,9590784,9590784,57344,61440,65536,69632,73728,40960,45056,49152,53248,53248,53248,53248,2694881440,353703189,57,2698249472,64,7488,192,1056915199,16780300,50465804,576,36,128,1130102784,1130233856,1107296256,1126170624,1126170624,1126170624,1779840,1795968,263808,32768,40960,61440,9492480,9578496,1406976,1126122323,1127236447,4294939340,158,1126122323,1127236447,4294939340,158,1126056432,1127367176,4294964300,158,
36672,36672,36864,36864,37056,37056,37248,37248,37248,37248,37248,37248,40960,44032,48128,51200,55296,58368,62464,65536,62464,65536,62464,65536,268439552,336991254,410,3227656192,80,7984,43008,1056915071,50465804,16780300,36,80,224,1128267776,1128267776,1128267776,1098907648,1098907648,0,135936,1073741824,6912,44032,44032,40960,724992,1073741824,36864,17215,1107575618,4294925804,4294967294,17215,1107575618,4294925804,4294967294,17215,1107575618,4294925804,4294967294,
9246720,9246720,9289728,9289728,9332736,9332736,9375744,9375744,9375744,9375744,9375744,9375744,40960,44032,48128,51200,55296,58368,62464,65536,62464,65536,62464,65536,268439552,336991254,57,3500806144,80,7488,192,1056915199,16780300,50465804,576,36,224,1129840640,1129840640,1129840640,1098907648,1098907648,0,1742400,1073741824,1741824,44032,44032,40960,9292800,1073741824,9289728,17239,1107575642,4294920428,4294967294,17239,1107575642,4294920428,4294967294,17239,1107575642,4294920428,4294967294,
10176,10176,10368,10368,10560,10560,10752,10752,10752,10752,10752,10752,40960,44032,48128,51200,55296,58368,62464,65536,62464,65536,62464,65536,268439552,336991254,410,1077551104,80,7984,43008,1040137727,50465804,16780300,36,80,224,1113063424,1113063424,1113063424,1098907648,1098907648,0,130968,1073741824,1944,44032,44032,40960,698496,1073741824,10368,16980,1107575392,4294956716,4294967294,16980,1107575392,4294956716,4294967294,16980,1107575392,4294956716,4294967294,
3483648,3483648,3526656,3526656,3569664,3569664,3612672,3612672,3612672,3612672,3612672,3612672,40960,44032,48128,51200,55296,58368,62464,65536,62464,65536,62464,65536,268439552,336991254,57,1350176768,80,7488,192,1048526463,16780300,50465804,576,36,224,1118044160,1118044160,1118044160,1098907648,1098907648,0,661824,1073741824,661248,44032,44032,40960,3529728,1073741824,3526656,17058,1107575464,4294950444,4294967294,17058,1107575464,4294950444,4294967294,17058,1107575464,4294950444,4294967294,
20544,20544,20736,20736,20928,20928,21120,21120,21120,21120,21120,21120,40960,44032,48128,51200,55296,58368,62464,65536,62464,65536,62464,65536,268439552,336991254,410,1622810624,80,7984,43008,1048526591,50465804,16780300,36,80,224,1121452032,1121452032,1121452032,1098907648,1098907648,0,132912,1073741824,3888,44032,44032,40960,708864,1073741824,20736,17110,1107575516,4294944620,4294967294,17110,1107575516,4294944620,4294967294,17110,1107575516,4294944620,4294967294,
5376000,5376000,5419008,5419008,5462016,5462016,5505024,5505024,5505024,5505024,5505024,5505024,40960,44032,48128,51200,55296,58368,62464,65536,62464,65536,62464,65536,268439552,336991254,57,2152337408,80,7488,192,1048526463,16780300,50465804,576,36,224,1123811328,1123811328,1123811328,1098907648,1098907648,0,1016640,1073741824,1016064,44032,44032,40960,5422080,1073741824,5419008,17146,1107575552,4294940588,4294967294,17146,1107575552,4294940588,4294967294,17146,1107575552,4294940588,4294967294,
28224,28224,28416,28416,28608,28608,28800,28800,28800,28800,28800,28800,40960,44032,48128,51200,55296,58368,62464,65536,62464,65536,62464,65536,268439552,336991254,410,2425495552,80,7984,43008,1056915199,50465804,16780300,36,80,224,1125384192,1125384192,1125384192,1098907648,1098907648,0,134352,1073741824,5328,44032,44032,40960,716544,1073741824,28416,17171,1107575574,4294935660,4294967294,17171,1107575574,4294935660,4294967294,17171,1107575574,4294935660,4294967294,
7268352,7268352,7311360,7311360,7354368,7354368,7397376,7397376,7397376,7397376,7397376,7397376,40960,44032,48128,51200,55296,58368,62464,65536,62464,65536,62464,65536,268439552,336991254,57,2955022336,80,7488,192,1056914943,16780300,50465804,576,36,224,1126825984,1126825984,1126825984,1098907648,1098907648,0,1371456,1073741824,1370880,44032,44032,40960,7314432,1073741824,7311360,17193,1107575596,4294930732,4294967294,17193,1107575596,4294930732,4294967294,17193,1107575596,4294930732,4294967294,
37056,37056,37248,37248,37440,37440,37632,37632,37632,37632,37632,37632,40960,44032,48128,51200,55296,58368,62464,65536,62464,65536,62464,65536,268439552,336991254,410,3228180480,80,7984,43008,1056915071,50465804,16780300,36,80,224,1128398848,1128398848,1128398848,1098907648,1098907648,0,136008,1073741824,6984,44032,44032,40960,725376,1073741824,37248,17217,1107575620,4294925356,4294967294,17217,1107575620,4294925356,4294967294,17217,1107575620,4294925356,4294967294,
9332736,9332736,9375744,9375744,9418752,9418752,9461760,9461760,9461760,9461760,9461760,9461760,40960,44032,48128,51200,55296,58368,62464,65536,62464,65536,62464,65536,268439552,336991254,57,3501330432,80,7488,192,1056915199,16780300,50465804,576,36,224,1129971712,1129971712,1129971712,1098907648,1098907648,0,1758528,1073741824,1757952,44032,44032,40960,9378816,1073741824,9375744,17241,1107575644,4294919980,4294967294,17241,1107575644,4294919980,4294967294,17241,1107575644,4294919980,4294967294,
10560,10560,10752,10752,10944,10944,11136,11136,11136,11136,11136,11136,40960,44032,48128,51200,55296,58368,62464,65536,62464,65536,62464,65536,268439552,336991254,410,1078075392,80,7984,43008,1040137855,50465804,16780300,36,80,224,1113587712,1113587712,1113587712,1098907648,1098907648,0,131040,1073741824,2016,44032,44032,40960,698880,1073741824,10752,16988,1107575400,4294956268,4294967294,16988,1107575400,4294956268,4294967294,16988,1107575400,4294956268,4294967294,
3655680,3698688,3741696,3784704,3827712,3870720,3913728,3956736,3956736,3956736,3956736,3956736,40960,45056,49152,53248,57344,61440,65536,69632,69632,69632,69632,69632,538976288,353703189,57,1350697216,80,7488,192,1048526591,16780300,50465804,576,36,128,1119092736,1118830592,1118568448,1107296256,1107296256,1107296256,726912,710784,694656,57344,49152,40960,3876864,3790848,3704832,1106264746,1112031928,4294957516,30,1106264746,1112031928,4294957516,30,1106264746,1112031928,4294957516,30,
0,0,0,43008,86016,129024,8730624,8773632,8773632,8773632,8773632,8773632,49152,53248,57344,61440,65536,69632,40960,45056,45056,45056,45056,45056,1616904192,320017685,57,1623339520,80,7488,192,1048526591,16780300,50465804,576,36,128,1128923136,1128923136,0,1119879168,1119879168,0,1632384,1073741824,0,32768,32768,53248,8706048,1073741824,0,1119765313,1121993548,4294942028,94,1119765313,1121993548,4294942028,94,0,1099972672,1804,4294967294,
5462016,5462016,5505024,5505024,5548032,5548032,5591040,5591040,5591040,5591040,5591040,5591040,40960,44032,48128,51200,55296,58368,62464,65536,62464,65536,62464,65536,268439552,336991254,57,2152861696,80,7488,192,1048526463,16780300,50465804,576,36,224,1124073472,1124073472,1124073472,1098907648,1098907648,0,1032768,1073741824,1032192,44032,44032,40960,5508096,1073741824,5505024,17150,1107575554,4294940140,4294967294,17150,1107575554,4294940140,4294967294,17150,1107575554,4294940140,4294967294,
28608,28608,28800,28800,28992,28992,29184,29184,29184,29184,29184,29184,40960,44032,48128,51200,55296,58368,62464,65536,62464,65536,62464,65536,268439552,336991254,410,2426019840,80,7984,43008,1056915199,50465804,16780300,36,80,224,1125515264,1125515264,1125515264,1098907648,1098907648,0,134424,1073741824,5400,44032,44032,40960,716928,1073741824,28800,17173,1107575576,4294935212,4294967294,17173,1107575576,4294935212,4294967294,17173,1107575576,4294935212,4294967294,
7526400,7569408,7612416,7655424,7698432,7741440,7784448,7827456,7827456,7827456,7827456,7827456,40960,45056,49152,53248,57344,61440,65536,69632,69632,69632,69632,69632,538976288,353703189,57,2955542784,80,7488,192,1056914943,16780300,50465804,576,36,128,1127481344,1127350272,1127219200,1107296256,1107296256,1107296256,1452672,1436544,1420416,57344,49152,40960,7747584,7661568,7575552,1106264879,1112032054,4294945996,30,1106264879,1112032054,4294945996,30,1106264879,1112032054,4294945996,30,
37440,37440,37632,37632,37824,37824,38016,38016,38016,38016,38016,38016,40960,44032,48128,51200,55296,58368,62464,65536,62464,65536,62464,65536,268439552,336991254,410,3228704768,80,7984,43008,1056915071,50465804,16780300,36,80,224,1128529920,1128529920,1128529920,1098907648,1098907648,0,136080,1073741824,7056,44032,44032,40960,725760,1073741824,37632,17219,1107575622,4294924908,4294967294,17219,1107575622,4294924908,4294967294,17219,1107575622,4294924908,4294967294,
9418752,9418752,9461760,9461760,9504768,9504768,9547776,9547776,9547776,9547776,9547776,9547776,40960,44032,48128,51200,55296,58368,62464,65536,62464,65536,62464,65536,268439552,336991254,57,3501854720,80,7488,192,1056915199,16780300,50465804,576,36,224,1130102784,1130102784,1130102784,1098907648,1098907648,0,1774656,1073741824,1774080,44032,44032,40960,9464832,1073741824,9461760,17243,1107575646,4294919532,4294967294,17243,1107575646,4294919532,4294967294,17243,1107575646,4294919532,4294967294,
10944,10944,11136,11136,11328,11328,11520,11520,11520,11520,11520,11520,40960,44032,48128,51200,55296,58368,62464,65536,62464,65536,62464,65536,268439552,336991254,410,1078599680,80,7984,43008,1040137855,50465804,16780300,36,80,224,1114112000,1114112000,1114112000,1098907648,1098907648,0,131112,1073741824,2088,44032,44032,40960,699264,1073741824,11136,16996,1107575408,4294955820,4294967294,16996,1107575408,4294955820,4294967294,16996,1107575408,4294955820,4294967294,
3569664,3569664,3612672,3612672,3655680,3655680,3698688,3698688,3698688,3698688,3698688,3698688,40960,44032,48128,51200,55296,58368,62464,65536,62464,65536,62464,65536,268439552,336991254,57,1351225344,80,7488,192,1048526591,16780300,50465804,576,36,224,1118306304,1118306304,1118306304,1098907648,1098907648,0,677952,1073741824,677376,44032,44032,40960,3615744,1073741824,3612672,17062,1107575468,4294949996,4294967294,17062,1107575468,4294949996,4294967294,17062,1107575468,4294949996,4294967294,
602112,645120,688128,731136,774144,817152,9160704,9203712,9203712,9203712,9203712,9203712,49152,53248,57344,61440,65536,69632,40960,45056,45056,45056,45056,45056,1616904192,320017685,57,1623994368,80,7488,192,1048526591,16780300,50465804,576,36,128,1129578496,1129578496,1098907648,1119879168,1119879168,0,1713024,1073741824,129024,32768,32768,53248,9136128,1073741824,688128,1119765323,1121993558,4294940748,94,1119765323,1121993558,4294940748,94,16736,1099973016,4294967052,4294967294,
5720064,5763072,5806080,5849088,5892096,5935104,5978112,6021120,6021120,6021120,6021120,6021120,40960,45056,49152,53248,57344,61440,65536,69632,69632,69632,69632,69632,538976288,353703189,57,2153382144,80,7488,192,1048526463,16780300,50465804,576,36,128,1124728832,1124597760,1124466688,1107296256,1107296256,1107296256,1113984,1097856,1081728,57344,49152,40960,5941248,5855232,5769216,1106264837,1112032012,4294951372,30,1106264837,1112032012,4294951372,30,1106264837,1112032012,4294951372,30,
28992,28992,29184,29184,29376,29376,29568,29568,29568,29568,29568,29568,40960,44032,48128,51200,55296,58368,62464,65536,62464,65536,62464,65536,268439552,336991254,410,2426544128,80,7984,43008,1056915199,50465804,16780300,36,80,224,1125646336,1125646336,1125646336,1098907648,1098907648,0,134496,1073741824,5472,44032,44032,40960,717312,1073741824,29184,17175,1107575578,4294934764,4294967294,17175,1107575578,4294934764,4294967294,17175,1107575578,4294934764,4294967294,
7354368,7354368,7397376,7397376,7440384,7440384,7483392,7483392,7483392,7483392,7483392,7483392,40960,44032,48128,51200,55296,58368,62464,65536,62464,65536,62464,65536,268439552,336991254,57,2956070912,80,7488,192,1056914943,16780300,50465804,576,36,224,1126957056,1126957056,1126957056,1098907648,1098907648,0,1387584,1073741824,1387008,44032,44032,40960,7400448,1073741824,7397376,17195,1107575598,4294930284,4294967294,17195,1107575598,4294930284,4294967294,17195,1107575598,4294930284,4294967294,
37824,37824,38016,38016,38208,38208,38400,38400,38400,38400,38400,38400,40960,44032,48128,51200,55296,58368,62464,65536,62464,65536,62464,65536,268439552,336991254,410,3229229056,80,7984,43008,1056915071,50465804,16780300,36,80,224,1128660992,1128660992,1128660992,1098907648,1098907648,0,136152,1073741824,7128,44032,44032,40960,726144,1073741824,38016,17221,1107575624,4294924460,4294967294,17221,1107575624,4294924460,4294967294,17221,1107575624,4294924460,4294967294,
9504768,9504768,9547776,9547776,9590784,9590784,9590784,9590784,9590784,9590784,9590784,9590784,40960,44032,48128,51200,55296,58368,62464,65536,62464,65536,62464,65536,268439552,336991254,57,3502379008,80,7488,192,1056915199,16780300,50465804,576,36,224,1130233856,1130233856,1130233856,1098907648,1098907648,0,1790784,1073741824,1790208,44032,44032,40960,9550848,1073741824,9547776,17245,1107575647,4294919084,4294967294,17245,1107575647,4294919084,4294967294,17245,1107575647,4294919084,4294967294,
11328,11328,11520,11520,11712,11712,11904,11904,11904,11904,11904,11904,40960,44032,48128,51200,55296,58368,62464,65536,62464,65536,62464,65536,268439552,336991254,410,1079123968,80,7984,43008,1040137855,50465804,16780300,36,80,224,1114636288,1114636288,1114636288,1098907648,1098907648,0,131184,1073741824,2160,44032,44032,40960,699648,1073741824,11520,17004,1107575416,4294955372,4294967294,17004,1107575416,4294955372,4294967294,17004,1107575416,4294955372,4294967294,
3655680,3655680,3698688,3698688,3741696,3741696,3784704,3784704,3784704,3784704,3784704,3784704,40960,44032,48128,51200,55296,58368,62464,65536,62464,65536,62464,65536,268439552,336991254,57,1351749632,80,7488,192,1048526591,16780300,50465804,576,36,224,1118568448,1118568448,1118568448,1098907648,1098907648,0,694080,1073741824,693504,44032,44032,40960,3701760,1073741824,3698688,17066,1107575472,4294949548,4294967294,17066,1107575472,4294949548,4294967294,17066,1107575472,4294949548,4294967294,
688128,731136,774144,817152,860160,903168,9590784,9590784,9590784,9590784,9590784,9590784,49152,53248,57344,61440,65536,69632,40960,45056,45056,45056,45056,45056,1616904192,320017685,57,1624649728,80,7488,192,1048526591,16780300,50465804,576,36,128,1130233856,1130233856,1099956224,1119879168,1119879168,0,1793664,1073741824,145152,32768,32768,53248,9566208,1073741824,774144,1119765333,1121993567,4294939468,94,1119765333,1121993567,4294939468,94,16768,1099973032,4294966796,4294967294,
5548032,5548032,5591040,5591040,5634048,5634048,5677056,5677056,5677056,5677056,5677056,5677056,40960,44032,48128,51200,55296,58368,62464,65536,62464,65536,62464,65536,268439552,336991254,57,2153910272,80,7488,192,1048526463,16780300,50465804,576,36,224,1124204544,1124204544,1124204544,1098907648,1098907648,0,1048896,1073741824,1048320,44032,44032,40960,5594112,1073741824,5591040,17153,1107575556,4294939692,4294967294,17153,1107575556,4294939692,4294967294,17153,1107575556,4294939692,4294967294,
29376,29376,29568,29568,29760,29760,29952,29952,29952,29952,29952,29952,40960,44032,48128,51200,55296,58368,62464,65536,62464,65536,62464,65536,268439552,336991254,410,2427068416,80,7984,43008,1056915199,50465804,16780300,36,80,224,1125777408,1125777408,1125777408,1098907648,1098907648,0,134568,1073741824,5544,44032,44032,40960,717696,1073741824,29568,17177,1107575580,4294934316,4294967294,17177,1107575580,4294934316,4294967294,17177,1107575580,4294934316,4294967294,
7440384,7440384,7483392,7483392,7526400,7526400,7569408,7569408,7569408,7569408,7569408,7569408,40960,44032,48128,51200,55296,58368,62464,65536,62464,65536,62464,65536,268439552,336991254,57,2956595200,80,7488,192,1056914943,16780300,50465804,576,36,224,1127088128,1127088128,1127088128,1098907648,1098907648,0,1403712,1073741824,1403136,44032,44032,40960,7486464,1073741824,7483392,17197,1107575600,4294929836,4294967294,17197,1107575600,4294929836,4294967294,17197,1107575600,4294929836,4294967294,
8558592,8601600,8644608,8687616,8730624,8773632,8816640,8859648,8859648,8859648,8859648,8859648,40960,45056,49152,53248,57344,61440,65536,69632,69632,69632,69632,69632,538976288,353703189,57,3229745408,80,7488,192,1056915199,16780300,50465804,576,36,128,1129054208,1128923136,1128792064,1107296256,1107296256,1107296256,1646208,1630080,1613952,57344,49152,40960,8779776,8693760,8607744,1106264903,1112032078,4294942924,30,1106264903,1112032078,4294942924,30,1106264903,1112032078,4294942924,30,
516096,559104,602112,645120,688128,731136,9074688,9117696,9117696,9117696,9117696,9117696,49152,53248,57344,61440,65536,69632,40960,45056,45056,45056,45056,45056,3503292416,320017685,57,3502912000,80,7488,192,1056915199,16780300,50465804,576,36,128,1129447424,1129447424,1096810496,1129316352,1129316352,0,1700928,1073741824,112896,32768,32768,53248,9071616,1073741824,602112,1129268041,1130316628,4294940332,206,1129268041,1130316628,4294940332,206,16704,1099973000,12,4294967294,
11712,11712,11904,11904,12096,12096,12288,12288,12288,12288,12288,12288,40960,44032,48128,51200,55296,58368,62464,65536,62464,65536,62464,65536,268439552,336991254,410,1079648256,80,7984,43008,1040137855,50465804,16780300,36,80,224,1115160576,1115160576,1115160576,1098907648,1098907648,0,131256,1073741824,2232,44032,44032,40960,700032,1073741824,11904,17012,1107575424,4294954924,4294967294,17012,1107575424,4294954924,4294967294,17012,1107575424,4294954924,4294967294,
3741696,3741696,3784704,3784704,3827712,3827712,3870720,3870720,3870720,3870720,3870720,3870720,40960,44032,48128,51200,55296,58368,62464,65536,62464,65536,62464,65536,268439552,336991254,57,1352273920,80,7488,192,1048526591,16780300,50465804,576,36,224,1118830592,1118830592,1118830592,1098907648,1098907648,0,710208,1073741824,709632,44032,44032,40960,3787776,1073741824,3784704,17070,1107575476,4294949100,4294967294,17070,1107575476,4294949100,4294967294,17070,1107575476,4294949100,4294967294,
20928,20928,21120,21120,21312,21312,21504,21504,21504,21504,21504,21504,40960,44032,48128,51200,55296,58368,62464,65536,62464,65536,62464,65536,268439552,336991254,410,1881284608,80,7984,43008,1048526335,50465804,16780300,36,80,224,1121714176,1121714176,1121714176,1098907648,1098907648,0,132984,1073741824,3960,44032,44032,40960,709248,1073741824,21120,17114,1107575520,4294944172,4294967294,17114,1107575520,4294944172,4294967294,17114,1107575520,4294944172,4294967294,
5634048,5634048,5677056,5677056,5720064,5720064,5763072,5763072,5763072,5763072,5763072,5763072,40960,44032,48128,51200,55296,58368,62464,65536,62464,65536,62464,65536,268439552,336991254,57,2154434560,80,7488,192,1048526463,16780300,50465804,576,36,224,1124335616,1124335616,1124335616,1098907648,1098907648,0,1065024,1073741824,1064448,44032,44032,40960,5680128,1073741824,5677056,17155,1107575558,4294939244,4294967294,17155,1107575558,4294939244,4294967294,17155,1107575558,4294939244,4294967294,
29760,29760,29952,29952,30144,30144,30336,30336,30336,30336,30336,30336,40960,44032,48128,51200,55296,58368,62464,65536,62464,65536,62464,65536,268439552,336991254,410,2427592704,80,7984,43008,1056915199,50465804,16780300,36,80,224,1125908480,1125908480,1125908480,1098907648,1098907648,0,134640,1073741824,5616,44032,44032,40960,718080,1073741824,29952,17179,1107575582,4294933868,4294967294,17179,1107575582,4294933868,4294967294,17179,1107575582,4294933868,4294967294,
7526400,7526400,7569408,7569408,7612416,7612416,7655424,7655424,7655424,7655424,7655424,7655424,40960,44032,48128,51200,55296,58368,62464,65536,62464,65536,62464,65536,268439552,336991254,57,2957119488,80,7488,192,1056915071,16780300,50465804,576,36,224,1127219200,1127219200,1127219200,1098907648,1098907648,0,1419840,1073741824,1419264,44032,44032,40960,7572480,1073741824,7569408,17199,1107575602,4294929388,4294967294,17199,1107575602,4294929388,4294967294,17199,1107575602,4294929388,4294967294,
38208,38208,38400,38400,38592,38592,38784,38784,38784,38784,38784,38784,40960,44032,48128,51200,55296,58368,62464,65536,62464,65536,62464,65536,268439552,336991254,410,3230277632,80,7984,43008,1056915199,50465804,16780300,36,80,224,1128792064,1128792064,1128792064,1098907648,1098907648,0,136224,1073741824,7200,44032,44032,40960,726528,1073741824,38400,17223,1107575626,4294924012,4294967294,17223,1107575626,4294924012,4294967294,17223,1107575626,4294924012,4294967294,
1290240,1333248,1376256,1419264,1462272,9504768,9547776,9590784,9590784,9590784,9590784,9590784,57344,61440,65536,69632,73728,40960,45056,49152,53248,53248,53248,53248,3503345872,353703189,57,3503555840,64,7488,192,1056915199,16780300,50465804,576,36,128,1130102784,1130233856,1107296256,1129316352,1129316352,1129316352,1781568,1797696,265536,32768,40960,61440,9501696,9587712,1416192,1129268051,1130316639,4294939052,206,1129268051,1130316639,4294939052,206,1129202160,1130316296,4294964012,206};
__device__ __forceinline__ float bf_lo(unsigned u) { return __uint_as_float(u << 16); }
__device__ __forceinline__ float bf_hi(unsigned u) { return __uint_as_float(u & 0xffff0000u); }
__device__ __forceinline__ unsigned cvt_pk(float lo, float hi) { bf16x2 r = {(__bf16)lo, (__bf16)hi}; return __builtin_bit_cast(unsigned, r); }
__device__ __forceinline__ float rb(float v) { return bf_lo(cvt_pk(v, 0.f)); }
__global__ __launch_bounds__(256) __attribute__((aligned(4096))) void dcn_kernel(const unsigned short* x_, const unsigned short* off_, const unsigned short* w_, unsigned short* out_, void* ws_) {
    __shared__ __attribute__((aligned(128))) unsigned char lds[163840];
    asm volatile("" ::"v"((__attribute__((address_space(3))) unsigned char*)lds));
    unsigned tid = threadIdx.x, bid = blockIdx.x;
    const __attribute__((address_space(4))) Args* ka = (const __attribute__((address_space(4))) Args*)__builtin_amdgcn_kernarg_segment_ptr();
    asm volatile(
        "s_load_dwordx8 s[48:55], %1, 0\n\t"
        "v_mov_b32 v156, %0\n\t"
        "s_mov_b32 s42, %2\n\t"
        "s_getpc_b64 s[44:45]\n\t"
        "s_and_b32 s44, s44, 0xffffff00\n\t"
        "9:\n\t"
        "s_mov_b32 s46, 99f-9b+3300\n\t"
        "s_mov_b32 s47, 0x20000\n\t"
        "v_lshlrev_b32 v160, 12, v156\n\t"
        "s_lshl_b32 s43, s42, 4\n\t"
        "s_and_b32 s43, s43, 3968\n\t"
        "s_mov_b32 m0, 161856\n\t"
        "v_cmp_gt_u32 vcc, 64, v156\n\t"
        "s_nop 1\n\t"
        "s_cbranch_vccz 10f\n\t"
        "buffer_load_dword v160, s[44:47], s43 offen lds\n\t"
        "10:\n\t"
        "s_mov_b64 s[64:65], %3\n\t"
        "v_and_b32 v157, 63, v156\n\t"
        "s_mov_b32 s66, 65536\n\t"
        "s_mov_b32 s67, 0x20000\n\t"
        "v_lshlrev_b32 v155, 2, v157\n\t"
        "s_lshl_b32 s70, s42, 8\n\t"
        "buffer_load_dword v155, v155, s[64:67], s70 offen\n\t"
        "v_lshrrev_b32 v73, 6, v156\n\t"
        "v_and_b32 v157, 63, v156\n\t"
        "v_readfirstlane_b32 s4, v73\n\t"
        "v_lshlrev_b32 v140, 4, v157\n\t"
        "s_mov_b32 s26, 165888\n\t"
        "s_mov_b32 s27, 0x20000\n\t"
        "v_xor_b32 v142, 32, v140\n\t"
        "v_and_b32 v73, 32, v157\n\t"
        "v_xor_b32 v141, v73, v140\n\t"
        "s_lshr_b32 s68, s4, 1\n\t"
        "s_mul_i32 s37, s4, 3072\n\t"
        "s_mul_i32 s36, s68, -61440\n\t"
        "s_add_u32 s36, s36, s37\n\t"
        "s_add_u32 s36, s36, 55296\n\t"
        "s_add_u32 s37, s37, 86016\n\t"
        "s_and_b32 s8, s42, 7\n\t"
        "s_lshl_b32 s8, s8, 5\n\t"
        "s_lshr_b32 s10, s42, 3\n\t"
        "s_add_u32 s10, s10, s8\n\t"
        "s_mul_i32 s8, s10, 2731\n\t"
        "s_sub_u32 s11, s10, 3\n\t"
        "s_mul_i32 s11, s11, 2850\n\t"
        "s_max_i32 s8, s8, s11\n\t"
        "s_lshr_b32 s8, s8, 16\n\t"
        "v_mul_u32_u24 v71, 171, v156\n\t"
        "v_lshrrev_b32 v71, 11, v71\n\t"
        "v_mul_u32_u24 v74, 12, v71\n\t"
        "v_sub_u32 v74, v156, v74\n\t"
        "s_mul_i32 s11, s8, 23\n\t"
        "s_sub_u32 s10, s10, s11\n\t"
        "s_min_u32 s11, s8, 3\n\t"
        "s_sub_u32 s10, s10, s11\n\t"
        "s_lshl_b32 s6, s8, 4\n\t"
        "s_add_u32 s6, s6, 48\n\t"
        "v_lshrrev_b32 v75, 1, v71\n\t"
        "v_and_b32 v75, 2, v75\n\t"
        "v_xor_b32 v74, v74, v75\n\t"
        "v_lshlrev_b32 v72, 4, v74\n\t"
        "s_lshl_b32 s8, s8, 1\n\t"
        "s_lshr_b32 s8, 1396799, s8\n\t"
        "s_and_b32 s8, s8, 3\n\t"
        "s_sub_u32 s8, s10, s8\n\t"
        "s_max_i32 s8, s8, 20\n\t"
        "s_lshl2_add_u32 s8, s10, s8\n\t"
        "s_lshl1_add_u32 s8, s8, -6\n\t"
        "v_add_u32 v73, -1, v71\n\t"
        "v_cmp_lt_u32 vcc, 17, v73\n\t"
        "v_mov_b32 v75, 0x7ff00000\n\t"
        "v_add3_u32 v161, v71, s6, -2\n\t"
        "v_cndmask_b32 v162, v72, v75, vcc\n\t"
        "s_movk_i32 s9, 192\n\t"
        "s_lshl_b32 s35, s4, 10\n\t"
        "s_mov_b32 s18, 9633792\n\t"
        "s_mov_b32 s19, 0x20000\n\t"
        "v_min_u32 v161, 223, v161\n\t"
        "s_mul_i32 s34, s8, 43008\n\t"
        "v_mad_u32_u24 v143, v161, s9, v162\n\t"
        "s_sub_u32 s34, s34, 43008\n\t"
        "s_lshr_b32 s12, s42, 3\n\t"
        "s_lshl_b32 s12, s12, 2\n\t"
        "s_add_u32 s12, s12, s4\n\t"
        "s_mul_i32 s12, s12, 1296\n\t"
        "s_and_b32 s12, s12, 0xffffff80\n\t"
        "v_min_u32 v164, 11, v157\n\t"
        "v_lshlrev_b32 v164, 7, v164\n\t"
        "s_mul_i32 s39, s68, 6144\n\t"
        "s_add_u32 s39, s39, 55296\n\t"
        "s_mul_i32 s38, s68, 159744\n\t"
        "s_add_u32 s38, s38, -104448\n\t"
        "s_mul_i32 s5, s68, -165888\n\t"
        "s_add_u32 s5, s5, 61440\n\t"
        "s_mov_b32 s22, 1806336\n\t"
        "s_mov_b32 s23, 0x20000\n\t"
        "s_mov_b32 s30, 9633792\n\t"
        "s_mov_b32 s31, 0x20000\n\t"
        "s_lshl_b32 s10, s4, 2\n\t"
        "s_add_u32 s10, s10, 161792\n\t"
        "v_mov_b32 v153, s10\n\t"
        "v_mov_b32 v182, 161792\n\t"
        "v_mov_b32 v76, 0\n\t"
        "v_mov_b32 v77, 0\n\t"
        "v_lshlrev_b32 v73, 3, v156\n\t"
        "v_add_u32 v73, 147456, v73\n\t"
        "s_add_u32 s60, s35, 153600\n\t"
        "v_mov_b32 v74, s60\n\t"
        "s_cmp_lt_u32 s4, 3\n\t"
        "s_cselect_b64 exec, -1, 0\n\t"
        "ds_write_b64 v73, v[76:77] offset:0\n\t"
        "ds_write_b64 v73, v[76:77] offset:2048\n\t"
        "ds_write_b64 v73, v[76:77] offset:4096\n\t"
        "s_mov_b64 exec, 1\n\t"
        "ds_write_b32 v153, v76 offset:16\n\t"
        "ds_write_b64 v74, v[76:77]\n\t"
        "s_mov_b64 exec, -1\n\t"
        "s_waitcnt lgkmcnt(0)\n\t"
        "s_mov_b64 s[24:25], s[52:53]\n\t"
        "s_mov_b64 s[16:17], s[48:49]\n\t"
        "s_add_u32 m0, s37, -36864\n\t"
        "s_nop 0\n\t"
        "buffer_load_dwordx4 v140, s[24:27], s36 offen offset:0 lds\n\t"
        "buffer_load_dwordx4 v141, s[24:27], s36 offen offset:1024 lds\n\t"
        "buffer_load_dwordx4 v142, s[24:27], s36 offen offset:2048 lds\n\t"
        "buffer_load_dword v165, v164, s[24:27], s12 offen\n\t"
        "s_add_u32 m0, s35, 8192\n\t"
        "s_add_u32 s43, s34, 86016\n\t"
        "buffer_load_dwordx4 v143, s[16:19], s43 offen  lds\n\t"
        "s_add_u32 m0, s35, 24576\n\t"
        "s_add_u32 s43, s34, 258048\n\t"
        "buffer_load_dwordx4 v143, s[16:19], s43 offen  lds\n\t"
        "s_mov_b64 s[20:21], s[50:51]\n\t"
        "s_mov_b64 s[28:29], s[54:55]\n\t"
        "s_mul_i32 s41, s8, 224\n\t"
        "s_add_u32 s41, s41, s6\n\t"
        "v_and_b32 v158, 15, v156\n\t"
        "s_and_b32 s49, s4, 1\n\t"
        "s_lshl_b32 s8, s49, 5\n\t"
        "v_xor_b32 v152, s8, v140\n\t"
        "s_mul_i32 s43, s4, 1536\n\t"
        "s_add_u32 m0, s43, 43008\n\t"
        "s_add_u32 s43, s43, 110592\n\t"
        "buffer_load_dwordx4 v152, s[24:27], s43 offen lds\n\t"
        "buffer_load_dwordx4 v152, s[24:27], s43 offen offset:512 lds\n\t"
        "s_add_u32 m0, s35, 12288\n\t"
        "s_add_u32 s43, s34, 129024\n\t"
        "buffer_load_dwordx4 v143, s[16:19], s43 offen  lds\n\t"
        "s_add_u32 m0, s35, 28672\n\t"
        "s_add_u32 s43, s34, 301056\n\t"
        "s_min_u32 s43, s43, 9590784\n\t"
        "buffer_load_dwordx4 v143, s[16:19], s43 offen  lds\n\t"
        "v_bfe_i32 v74, v156, 5, 1\n\t"
        "v_and_b32 v74, 7, v74\n\t"
        "v_xor_b32 v74, s4, v74\n\t"
        "v_mul_u32_u24 v68, 11, v74\n\t"
        "v_lshrrev_b32 v68, 5, v68\n\t"
        "v_mul_u32_u24 v75, 3, v68\n\t"
        "v_sub_u32 v69, v74, v75\n\t"
        "v_lshlrev_b32 v70, 2, v74\n\t"
        "v_mul_u32_u24 v74, 205, v156\n\t"
        "v_lshrrev_b32 v74, 14, v74\n\t"
        "v_min_u32 v74, 2, v74\n\t"
        "v_lshlrev_b32 v65, 2, v74\n\t"
        "v_mul_u32_u24 v74, 80, v74\n\t"
        "v_sub_u32 v66, v156, v74\n\t"
        "v_bfe_u32 v52, v156, 4, 1\n\t"
        "v_and_b32 v181, 48, v156\n\t"
        "s_mov_b64 s[56:57], 0\n\t"
        "s_mov_b32 s63, 0x435f0000\n\t"
        "s_mov_b32 s54, 0x7fff7fff\n\t"
        "v_bfe_u32 v159, v156, 4, 2\n\t"
        "v_mul_u32_u24 v62, 192, v158\n\t"
        "v_lshlrev_b32 v76, 3, v158\n\t"
        "v_and_b32 v77, 48, v76\n\t"
        "v_and_b32 v184, 24, v76\n\t"
        "s_mul_i32 s69, s68, 16384\n\t"
        "s_lshl_b32 s70, s49, 7\n\t"
        "s_lshl_b32 s71, s49, 5\n\t"
        "v_add_u32 v73, 8, v76\n\t"
        "v_and_b32 v73, 32, v73\n\t"
        "s_add_u32 s69, s69, 192\n\t"
        "v_xor_b32 v73, v181, v73\n\t"
        "v_add3_u32 v128, v73, v62, s69\n\t"
        "v_add_u32 v73, 16, v76\n\t"
        "v_and_b32 v73, 32, v73\n\t"
        "s_add_u32 s69, s69, 192\n\t"
        "v_xor_b32 v73, v181, v73\n\t"
        "v_add3_u32 v129, v73, v62, s69\n\t"
        "v_add_u32 v73, 24, v76\n\t"
        "v_and_b32 v73, 32, v73\n\t"
        "s_add_u32 s69, s69, 192\n\t"
        "v_xor_b32 v73, v181, v73\n\t"
        "v_add3_u32 v130, v73, v62, s69\n\t"
        "v_lshrrev_b32 v73, 2, v158\n\t"
        "v_lshl_add_u32 v74, v159, 3, v73\n\t"
        "v_mul_u32_u24 v74, 192, v74\n\t"
        "v_lshlrev_b32 v75, 1, v156\n\t"
        "v_and_b32 v75, 32, v75\n\t"
        "v_add3_u32 v74, v74, v75, v184\n\t"
        "v_add_u32 v74, 86016, v74\n\t"
        "v_xad_u32 v133, v74, s71, 64\n\t"
        "v_add_u32 v131, s70, v74\n\t"
        "v_xor_b32 v132, 32, v131\n\t"
        "v_subrev_u32 v138, 43008, v131\n\t"
        "v_subrev_u32 v139, 43008, v132\n\t"
        "v_subrev_u32 v149, 43008, v133\n\t"
        "v_lshrrev_b32 v74, 3, v157\n\t"
        "v_min_u32 v74, 2, v74\n\t"
        "v_lshlrev_b32 v81, 2, v74\n\t"
        "s_lshl_b32 s69, s4, 3\n\t"
        "v_and_or_b32 v82, v156, 7, s69\n\t"
        "v_mul_u32_u24 v80, 48, v82\n\t"
        "v_lshl_add_u32 v80, v74, 11, v80\n\t"
        "v_add_u32 v80, 147488, v80\n\t"
        "v_and_b32 v82, 15, v82\n\t"
        "v_and_b32 v74, 31, v156\n\t"
        "v_mad_u32_u24 v53, v74, 48, v70\n\t"
        "v_add_u32 v53, 147456, v53\n\t"
        "v_add_u32 v70, 157696, v70\n\t"
        "v_mul_u32_u24 v67, 320, v81\n\t"
        "s_add_u32 s69, s68, 1\n\t"
        "v_cvt_f32_u32 v56, s69\n\t"
        "v_add_u32 v57, 1, v82\n\t"
        "v_cvt_f32_u32 v57, v57\n\t"
        "v_lshrrev_b32 v83, 2, v157\n\t"
        "v_and_b32 v74, 3, v156\n\t"
        "v_lshlrev_b32 v119, 11, v83\n\t"
        "v_lshl_add_u32 v119, v74, 2, v119\n\t"
        "v_add_u32 v119, 122368, v119\n\t"
        "v_lshl_add_u32 v74, v74, 4, 64\n\t"
        "v_mad_u32_u24 v145, v83, s9, v74\n\t"
        "v_lshrrev_b32 v74, 1, v156\n\t"
        "v_and_or_b32 v74, v74, 16, v75\n\t"
        "v_add3_u32 v144, v74, v62, s70\n\t"
        "s_lshl_b32 s69, s68, 10\n\t"
        "s_add_u32 s69, s69, 135168\n\t"
        "v_add_u32 v74, v76, v159\n\t"
        "v_lshl_or_b32 v74, v74, 3, s71\n\t"
        "v_xad_u32 v146, v74, v77, s69\n\t"
        "s_lshl_b32 s70, s49, 11\n\t"
        "s_add_u32 s70, s70, s69\n\t"
        "v_lshlrev_b32 v74, 1, v156\n\t"
        "v_and_b32 v74, 48, v74\n\t"
        "v_xad_u32 v147, v74, v140, s70\n\t"
        "s_mul_i32 s48, s41, 36\n\t"
        "s_mul_i32 s41, s41, 192\n\t"
        "s_mul_i32 s69, s68, 172032\n\t"
        "s_add_u32 s41, s41, s69\n\t"
        "s_mul_i32 s69, s49, 43008\n\t"
        "s_add_u32 s50, s41, s69\n\t"
        "v_lshrrev_b32 v74, 5, v156\n\t"
        "v_mul_u32_u24 v74, 8064, v74\n\t"
        "v_and_b32 v55, 496, v140\n\t"
        "v_add_u32 v55, v55, v74\n\t"
        "v_lshl_add_u32 v61, v158, 2, v74\n\t"
        "s_mul_i32 s69, s68, 768\n\t"
        "s_add_u32 s69, s69, 147456\n\t"
        "v_mad_u32_u24 v187, v158, 48, s69\n\t"
        "v_lshl_add_u32 v74, v159, 3, v62\n\t"
        "v_xad_u32 v186, v74, v77, s37\n\t"
        "v_and_b32 v73, 2, v73\n\t"
        "v_add_u32 v73, v73, v159\n\t"
        "v_and_b32 v73, 3, v73\n\t"
        "v_and_or_b32 v73, v158, 12, v73\n\t"
        "s_add_u32 s69, s35, 157696\n\t"
        "v_lshl_add_u32 v185, v73, 4, s69\n\t"
        "v_add_u32 v118, s69, v140\n\t"
        "v_add_u32 v116, 1, v157\n\t"
        "v_mul_u32_u24 v74, 12288, v159\n\t"
        "v_add3_u32 v74, v74, v62, v77\n\t"
        "v_add_u32 v117, s37, v74\n\t"
        "v_lshlrev_b32 v73, 2, v158\n\t"
        "v_and_b32 v73, 16, v73\n\t"
        "s_mov_b32 s69, 0x0c0c0100\n\t"
        "v_alignbit_b32 v169, s69, s69, v73\n\t"
        "s_mov_b32 s69, 0x0c0c0302\n\t"
        "v_alignbit_b32 v171, s69, s69, v73\n\t"
        "v_mov_b32 v74, 0x0c0c0c0c\n\t"
        "s_mov_b32 s10, 2228241\n\t"
        "s_mov_b32 s11, 8912964\n\t"
        "v_cndmask_b32 v168, v74, v169, s[10:11]\n\t"
        "v_cndmask_b32 v170, v74, v171, s[10:11]\n\t"
        "s_mov_b32 s10, 2281718784\n\t"
        "s_mov_b32 s11, 570429696\n\t"
        "v_cndmask_b32 v169, v74, v169, s[10:11]\n\t"
        "v_cndmask_b32 v171, v74, v171, s[10:11]\n\t"
        "s_waitcnt vmcnt(10)\n\t"
        "v_readlane_b32 s7, v155, 27\n\t"
        "s_nop 1\n\t"
        "s_or_b32 s42, s42, s7\n\t"
        "s_bitcmp1_b32 s7, 8\n\t"
        "s_cbranch_scc1 20f\n\t"
        "s_bitcmp1_b32 s7, 9\n\t"
        "s_cbranch_scc1 21f\n\t"
        "ds_bpermute_b32 v64, v65, v155 offset:172\n\t"
        "s_add_u32 m0, s37, -24576\n\t"
        "s_add_u32 s36, s36, 6144\n\t"
        "buffer_load_dwordx4 v140, s[24:27], s36 offen offset:0 lds\n\t"
        "ds_bpermute_b32 v78, v81, v155 offset:148\n\t"
        "ds_bpermute_b32 v79, v81, v155 offset:160\n\t"
        "v_readlane_b32 s71, v155, 26\n\t"
        "v_readlane_b32 s10, v155, 29\n\t"
        "s_nop 0\n\t"
        "v_mul_u32_u24 v73, s71, v66\n\t"
        "v_lshrrev_b32 v73, 11, v73\n\t"
        "v_lshlrev_b32 v66, 4, v66\n\t"
        "v_mad_u32_u24 v73, v73, s10, v66\n\t"
        "s_waitcnt lgkmcnt(2)\n\t"
        "v_add_u32 v64, v64, v73\n\t"
        "buffer_load_dwordx4 v141, s[24:27], s36 offen offset:1024 lds\n\t"
        "v_readlane_b32 s69, v155, 34\n\t"
        "v_readlane_b32 s70, v155, 35\n\t"
        "s_nop 0\n\t"
        "v_mad_u32_u24 v70, v52, s69, v70\n\t"
        "v_mad_u32_u24 v70, v158, s70, v70\n\t"
        "s_mul_i32 s71, s69, s68\n\t"
        "s_add_u32 s71, s71, 157728\n\t"
        "v_mad_u32_u24 v67, v82, s70, v67\n\t"
        "v_add_u32 v67, s71, v67\n\t"
        "v_readlane_b32 s11, v155, 30\n\t"
        "buffer_load_dwordx4 v142, s[24:27], s36 offen offset:2048 lds\n\t"
        "s_movk_i32 s10, 223\n\t"
        "v_mov_b32 v75, 0x7ff00000\n\t"
        "v_readlane_b32 s12, v155, 24\n\t"
        "v_readlane_b32 s13, v155, 25\n\t"
        "s_nop 0\n\t"
        "s_bfe_u32 s70, s12, 524288\n\t"
        "s_bfe_u32 s71, s13, 524288\n\t"
        "v_cmp_gt_u32 vcc, s71, v71\n\t"
        "v_add3_u32 v73, v71, s70, -2\n\t"
        "v_med3_i32 v73, v73, 0, s10\n\t"
        "s_add_u32 m0, s35, 4096\n\t"
        "s_add_u32 s43, s34, 43008\n\t"
        "buffer_load_dwordx4 v143, s[16:19], s43 offen  lds\n\t"
        "v_cndmask_b32 v74, v75, v72, vcc\n\t"
        "v_mad_u32_u24 v134, v73, s11, v74\n\t"
        "s_bfe_u32 s70, s12, 524296\n\t"
        "s_bfe_u32 s71, s13, 524296\n\t"
        "v_cmp_gt_u32 vcc, s71, v71\n\t"
        "v_add3_u32 v73, v71, s70, -2\n\t"
        "v_med3_i32 v73, v73, 0, s10\n\t"
        "v_cndmask_b32 v74, v75, v72, vcc\n\t"
        "v_mad_u32_u24 v135, v73, s11, v74\n\t"
        "s_add_u32 m0, s35, 20480\n\t"
        "s_add_u32 s43, s34, 215040\n\t"
        "buffer_load_dwordx4 v143, s[16:19], s43 offen  lds\n\t"
        "s_bfe_u32 s70, s12, 524304\n\t"
        "s_bfe_u32 s71, s13, 524304\n\t"
        "v_cmp_gt_u32 vcc, s71, v71\n\t"
        "v_add3_u32 v73, v71, s70, -2\n\t"
        "v_med3_i32 v73, v73, 0, s10\n\t"
        "v_cndmask_b32 v74, v75, v72, vcc\n\t"
        "v_mad_u32_u24 v136, v73, s11, v74\n\t"
        "s_bfe_u32 s70, s12, 524312\n\t"
        "s_bfe_u32 s71, s13, 524312\n\t"
        "v_cmp_gt_u32 vcc, s71, v71\n\t"
        "v_add3_u32 v73, v71, s70, -2\n\t"
        "v_med3_i32 v73, v73, 0, s10\n\t"
        "v_cndmask_b32 v74, v75, v72, vcc\n\t"
        "v_mad_u32_u24 v137, v73, s11, v74\n\t"
        "s_mov_b64 exec, 0\n\t"
        "s_cmp_eq_u32 s4, 0\n\t"
        "s_cbranch_scc1 .LpM0%=\n\t"
        "s_cmp_eq_u32 s4, 1\n\t"
        "s_cbranch_scc1 .LpM1%=\n\t"
        "s_cmp_eq_u32 s4, 2\n\t"
        "s_cbranch_scc1 .LpM2%=\n\t"
        "s_branch .LwM%=\n\t"
        ".LrM%=:\n\t"
        "s_mov_b64 exec, -1\n\t"
        "s_waitcnt vmcnt(9)\n\t"
        "s_barrier\n\t"
        "ds_read_b64_tr_b16 a[0:1], v138 offset:6144\n\t"
        "ds_read_b64_tr_b16 a[2:3], v138 offset:6912\n\t"
        "ds_read_b64_tr_b16 a[4:5], v139 offset:6144\n\t"
        "ds_read_b64_tr_b16 a[6:7], v139 offset:6912\n\t"
        "ds_read_b64_tr_b16 a[8:9], v149 offset:6144\n\t"
        "ds_read_b64_tr_b16 a[10:11], v149 offset:6912\n\t"
        "ds_read_b128 v[84:87], v128 offset:8192\n\t"
        "ds_read_b64_tr_b16 a[12:13], v138 offset:12288\n\t"
        "ds_read_b64_tr_b16 a[14:15], v138 offset:13056\n\t"
        "ds_read_b64_tr_b16 a[16:17], v139 offset:12288\n\t"
        "ds_read_b64_tr_b16 a[18:19], v139 offset:13056\n\t"
        "ds_read_b64_tr_b16 a[20:21], v149 offset:12288\n\t"
        "ds_read_b64_tr_b16 a[22:23], v149 offset:13056\n\t"
        "ds_read_b128 v[88:91], v128 offset:8192\n\t"
        "s_add_u32 m0, s37, -12288\n\t"
        "s_add_u32 s36, s36, s39\n\t"
        "s_waitcnt lgkmcnt(7)\n\t"
        "v_mfma_f32_16x16x32_bf16 v[12:15], a[0:3], v[84:87], 0\n\t"
        "buffer_load_dwordx4 v140, s[24:27], s36 offen offset:0 lds\n\t"
        "buffer_load_dwordx4 v141, s[24:27], s36 offen offset:1024 lds\n\t"
        "buffer_load_dwordx4 v142, s[24:27], s36 offen offset:2048 lds\n\t"
        "s_add_u32 m0, s35, 16384\n\t"
        "s_add_u32 s43, s34, 172032\n\t"
        "buffer_load_dwordx4 v143, s[16:19], s43 offen  lds\n\t"
        "s_add_u32 m0, s35, 32768\n\t"
        "s_add_u32 s43, s34, 344064\n\t"
        "s_min_u32 s43, s43, 9590784\n\t"
        "buffer_load_dwordx4 v143, s[16:19], s43 offen  lds\n\t"
        "s_waitcnt vmcnt(10)\n\t"
        "s_barrier\n\t"
        "ds_read_b128 v[92:95], v128 offset:12288\n\t"
        "ds_read_b64_tr_b16 a[24:25], v138 offset:0\n\t"
        "v_mfma_f32_16x16x32_bf16 v[16:19], a[4:7], v[84:87], 0\n\t"
        "ds_read_b64_tr_b16 a[26:27], v138 offset:768\n\t"
        "s_add_u32 m0, s35, 157696\n\t"
        "s_nop 0\n\t"
        "buffer_load_dwordx4 v64, s[20:23], 0 offen lds\n\t"
        "s_add_u32 m0, s37, 0\n\t"
        "s_add_u32 s36, s36, s38\n\t"
        "v_mfma_f32_16x16x32_bf16 v[20:23], a[8:11], v[84:87], 0\n\t"
        "buffer_load_dwordx4 v140, s[24:27], s36 offen offset:0 lds\n\t"
        "ds_read_b64_tr_b16 a[28:29], v139 offset:0\n\t"
        "v_readlane_b32 s44, v155, 32\n\t"
        "s_waitcnt lgkmcnt(4)\n\t"
        "v_mfma_f32_16x16x32_bf16 v[24:27], a[12:15], v[88:91], 0\n\t"
        "ds_read_b64_tr_b16 a[30:31], v139 offset:768\n\t"
        "ds_read_b64_tr_b16 a[32:33], v149 offset:0\n\t"
        "ds_read_b64_tr_b16 a[34:35], v149 offset:768\n\t"
        "v_readlane_b32 s45, v155, 33\n\t"
        "v_mfma_f32_16x16x32_bf16 v[28:31], a[16:19], v[88:91], 0\n\t"
        "buffer_load_dwordx4 v141, s[24:27], s36 offen offset:1024 lds\n\t"
        "ds_read_b128 v[96:99], v128 offset:8192\n\t"
        "v_readlane_b32 s62, v155, 36\n\t"
        "v_mfma_f32_16x16x32_bf16 v[32:35], a[20:23], v[88:91], 0\n\t"
        "ds_read_b128 v[100:103], v128 offset:12288\n\t"
        "s_lshl_b32 s70, s62, 5\n\t"
        "s_waitcnt lgkmcnt(8)\n\t"
        "v_mfma_f32_16x16x32_bf16 v[24:27], a[0:3], v[92:95], v[24:27]\n\t"
        "s_waitcnt vmcnt(10)\n\t"
        "s_barrier\n\t"
        "ds_read_b64_tr_b16 a[36:37], v138 offset:18432\n\t"
        "ds_read_b64_tr_b16 a[38:39], v138 offset:19200\n\t"
        "v_mfma_f32_16x16x32_bf16 v[28:31], a[4:7], v[92:95], v[28:31]\n\t"
        "buffer_load_dwordx4 v142, s[24:27], s36 offen offset:2048 lds\n\t"
        "s_add_u32 m0, s35, 0\n\t"
        "ds_read_b64_tr_b16 a[40:41], v139 offset:18432\n\t"
        "s_add_u32 s43, s34, 0\n\t"
        "v_mfma_f32_16x16x32_bf16 v[32:35], a[8:11], v[92:95], v[32:35]\n\t"
        "ds_read_b64_tr_b16 a[42:43], v139 offset:19200\n\t"
        "ds_read_b64_tr_b16 a[44:45], v149 offset:18432\n\t"
        "s_sub_u32 s71, s70, 16384\n\t"
        "v_mfma_f32_16x16x32_bf16 v[36:39], a[12:15], v[92:95], 0\n\t"
        "buffer_load_dwordx4 v143, s[16:19], s43 offen  lds\n\t"
        "s_add_u32 m0, s35, 36864\n\t"
        "ds_read_b64_tr_b16 a[46:47], v149 offset:19200\n\t"
        "s_add_u32 s43, s34, 387072\n\t"
        "ds_read_b64_tr_b16 a[48:49], v138 offset:24576\n\t"
        "s_min_u32 s43, s43, 9590784\n\t"
        "v_mfma_f32_16x16x32_bf16 v[40:43], a[16:19], v[92:95], 0\n\t"
        "ds_read_b64_tr_b16 a[50:51], v138 offset:25344\n\t"
        "ds_read_b64_tr_b16 a[52:53], v139 offset:24576\n\t"
        "ds_read_b64_tr_b16 a[54:55], v139 offset:25344\n\t"
        "s_mul_i32 s71, s71, s68\n\t"
        "v_mfma_f32_16x16x32_bf16 v[44:47], a[20:23], v[92:95], 0\n\t"
        "buffer_load_dwordx4 v143, s[16:19], s43 offen  lds\n\t"
        "s_add_u32 m0, s37, 12288\n\t"
        "ds_read_b64_tr_b16 a[56:57], v149 offset:24576\n\t"
        "s_add_u32 s36, s36, s5\n\t"
        "ds_read_b64_tr_b16 a[58:59], v149 offset:25344\n\t"
        "s_waitcnt lgkmcnt(12)\n\t"
        "v_mfma_f32_16x16x32_bf16 v[0:3], a[24:27], v[96:99], 0\n\t"
        "ds_read_b128 v[104:107], v128 offset:8256\n\t"
        "v_add_u32 v172, s71, v128\n\t"
        "v_mfma_f32_16x16x32_bf16 v[4:7], a[28:31], v[96:99], 0\n\t"
        "s_mov_b32 exec_lo, 0\n\t"
        "s_mov_b32 exec_hi, 0xfff00000\n\t"
        "ds_write_b32 v119, v155\n\t"
        "s_mov_b64 exec, -1\n\t"
        "v_mfma_f32_16x16x32_bf16 v[8:11], a[32:35], v[96:99], 0\n\t"
        "buffer_load_dwordx4 v140, s[24:27], s36 offen offset:0 lds\n\t"
        "v_add_u32 v173, s71, v129\n\t"
        "v_mfma_f32_16x16x32_bf16 v[12:15], a[24:27], v[100:103], v[12:15]\n\t"
        "v_add_u32 v174, s71, v130\n\t"
        "s_add_u32 s71, s71, s70\n\t"
        "v_mfma_f32_16x16x32_bf16 v[16:19], a[28:31], v[100:103], v[16:19]\n\t"
        "v_add_u32 v175, s71, v128\n\t"
        "v_add_u32 v176, s71, v129\n\t"
        "v_mfma_f32_16x16x32_bf16 v[20:23], a[32:35], v[100:103], v[20:23]\n\t"
        "s_waitcnt vmcnt(9)\n\t"
        "s_barrier\n\t"
        "buffer_load_dwordx4 v141, s[24:27], s36 offen offset:1024 lds\n\t"
        "ds_read_b128 v[108:111], v128 offset:4096\n\t"
        "s_waitcnt lgkmcnt(2)\n\t"
        "v_mfma_f32_16x16x32_bf16 v[12:15], a[36:39], v[104:107], v[12:15]\n\t"
        "ds_read_b128 v[112:115], v128 offset:4160\n\t"
        "ds_read_b64_tr_b16 a[60:61], v138 offset:30720\n\t"
        "v_add_u32 v177, s71, v130\n\t"
        "v_mfma_f32_16x16x32_bf16 v[16:19], a[40:43], v[104:107], v[16:19]\n\t"
        "ds_read_b64_tr_b16 a[62:63], v138 offset:31488\n\t"
        "ds_read_b64_tr_b16 a[64:65], v139 offset:30720\n\t"
        "s_add_u32 s71, s71, s70\n\t"
        "v_mfma_f32_16x16x32_bf16 v[20:23], a[44:47], v[104:107], v[20:23]\n\t"
        "buffer_load_dwordx4 v142, s[24:27], s36 offen offset:2048 lds\n\t"
        "s_add_u32 m0, s37, 24576\n\t"
        "ds_read_b64_tr_b16 a[66:67], v139 offset:31488\n\t"
        "s_add_u32 s36, s36, s39\n\t"
        "v_mfma_f32_16x16x32_bf16 v[24:27], a[48:51], v[104:107], v[24:27]\n\t"
        "ds_read_b64_tr_b16 a[68:69], v149 offset:30720\n\t"
        "ds_read_b64_tr_b16 a[70:71], v149 offset:31488\n\t"
        "v_add_u32 v178, s71, v128\n\t"
        "v_mfma_f32_16x16x32_bf16 v[28:31], a[52:55], v[104:107], v[28:31]\n\t"
        "ds_read_b128 v[84:87], v128 offset:8256\n\t"
        "v_add_u32 v179, s71, v129\n\t"
        "v_mfma_f32_16x16x32_bf16 v[32:35], a[56:59], v[104:107], v[32:35]\n\t"
        "s_cmp_eq_u32 s69, 36\n\t"
        "s_cselect_b64 exec, -1, 0\n\t"
        "v_swap_b32 v68, v69\n\t"
        "v_swap_b32 v173, v175\n\t"
        "v_swap_b32 v174, v178\n\t"
        "v_swap_b32 v177, v179\n\t"
        "s_mov_b64 exec, -1\n\t"
        "s_waitcnt lgkmcnt(8)\n\t"
        "v_mfma_f32_16x16x32_bf16 v[0:3], a[0:3], v[108:111], v[0:3]\n\t"
        "ds_read_b128 v[88:91], v128 offset:12352\n\t"
        "ds_read_b64_tr_b16 a[72:73], v138 offset:36864\n\t"
        "v_add_u32 v180, s71, v130\n\t"
        "v_mfma_f32_16x16x32_bf16 v[4:7], a[4:7], v[108:111], v[4:7]\n\t"
        "ds_read_b64_tr_b16 a[74:75], v138 offset:37632\n\t"
        "ds_read_b64_tr_b16 a[76:77], v139 offset:36864\n\t"
        "v_add3_u32 v68, v68, v52, -1\n\t"
        "v_mfma_f32_16x16x32_bf16 v[8:11], a[8:11], v[108:111], v[8:11]\n\t"
        "ds_read_b64_tr_b16 a[78:79], v139 offset:37632\n\t"
        "ds_read_b64_tr_b16 a[80:81], v149 offset:36864\n\t"
        "v_add3_u32 v69, v69, v158, -1\n\t"
        "v_mfma_f32_16x16x32_bf16 v[12:15], a[12:15], v[108:111], v[12:15]\n\t"
        "ds_read_b64_tr_b16 a[82:83], v149 offset:37632\n\t"
        "v_cvt_f32_i32 v68, v68\n\t"
        "v_mfma_f32_16x16x32_bf16 v[16:19], a[16:19], v[108:111], v[16:19]\n\t"
        "v_cvt_f32_i32 v69, v69\n\t"
        "v_add_f32 v78, v78, v56\n\t"
        "v_mfma_f32_16x16x32_bf16 v[20:23], a[20:23], v[108:111], v[20:23]\n\t"
        "v_add_f32 v79, v79, v57\n\t"
        "v_readlane_b32 s69, v155, 31\n\t"
        "s_waitcnt lgkmcnt(7)\n\t"
        "v_mfma_f32_16x16x32_bf16 v[0:3], a[36:39], v[112:115], v[0:3]\n\t"
        "v_mov_b32 v154, s69\n\t"
        "v_mov_b32 v119, s62\n\t"
        "v_mfma_f32_16x16x32_bf16 v[4:7], a[40:43], v[112:115], v[4:7]\n\t"
        "v_readlane_b32 s53, v155, 28\n\t"
        "v_readlane_b32 s9, v155, 46\n\t"
        "v_mfma_f32_16x16x32_bf16 v[8:11], a[44:47], v[112:115], v[8:11]\n\t"
        "s_sub_u32 s10, 43200, s11\n\t"
        "s_mul_i32 s10, s10, s68\n\t"
        "v_mfma_f32_16x16x32_bf16 v[12:15], a[48:51], v[112:115], v[12:15]\n\t"
        "s_sub_u32 s11, s11, 192\n\t"
        "v_mad_u32_u24 v148, v158, s11, v144\n\t"
        "v_mfma_f32_16x16x32_bf16 v[16:19], a[52:55], v[112:115], v[16:19]\n\t"
        "v_add_u32 v148, s10, v148\n\t"
        "v_mad_u32_u24 v150, v83, s11, v145\n\t"
        "v_mfma_f32_16x16x32_bf16 v[20:23], a[56:59], v[112:115], v[20:23]\n\t"
        "s_lshl_b32 s70, s49, 1\n\t"
        "s_add_u32 s70, s70, 49\n\t"
        "v_mfma_f32_16x16x32_bf16 v[0:3], a[60:63], v[84:87], v[0:3]\n\t"
        "v_readlane_b32 s52, v155, s70\n\t"
        "v_mfma_f32_16x16x32_bf16 v[4:7], a[64:67], v[84:87], v[4:7]\n\t"
        "s_nop 0\n\t"
        "s_add_u32 s52, s52, s10\n\t"
        "v_mfma_f32_16x16x32_bf16 v[8:11], a[68:71], v[84:87], v[8:11]\n\t"
        "s_waitcnt lgkmcnt(0)\n\t"
        "v_mfma_f32_16x16x32_bf16 v[12:15], a[60:63], v[88:91], v[12:15]\n\t"
        "buffer_load_dwordx4 v140, s[24:27], s36 offen offset:0 lds\n\t"
        "v_mfma_f32_16x16x32_bf16 v[16:19], a[64:67], v[88:91], v[16:19]\n\t"
        "v_mfma_f32_16x16x32_bf16 v[20:23], a[68:71], v[88:91], v[20:23]\n\t"
        "v_mfma_f32_16x16x32_bf16 v[24:27], a[36:39], v[88:91], v[24:27]\n\t"
        "ds_read_b128 v[92:95], v128 offset:4224\n\t"
        "v_mfma_f32_16x16x32_bf16 v[28:31], a[40:43], v[88:91], v[28:31]\n\t"
        "buffer_load_dwordx4 v141, s[24:27], s36 offen offset:1024 lds\n\t"
        "v_mfma_f32_16x16x32_bf16 v[32:35], a[44:47], v[88:91], v[32:35]\n\t"
        "v_mfma_f32_16x16x32_bf16 v[36:39], a[48:51], v[88:91], v[36:39]\n\t"
        "ds_read_b128 v[96:99], v128 offset:8320\n\t"
        "v_mfma_f32_16x16x32_bf16 v[40:43], a[52:55], v[88:91], v[40:43]\n\t"
        "v_mfma_f32_16x16x32_bf16 v[44:47], a[56:59], v[88:91], v[44:47]\n\t"
        "buffer_load_dwordx4 v142, s[24:27], s36 offen offset:2048 lds\n\t"
        "s_waitcnt lgkmcnt(1)\n\t"
        "v_mfma_f32_16x16x32_bf16 v[0:3], a[72:75], v[92:95], v[0:3]\n\t"
        "ds_read_b128 v[100:103], v128 offset:12416\n\t"
        "v_mfma_f32_16x16x32_bf16 v[4:7], a[76:79], v[92:95], v[4:7]\n\t"
        "v_mfma_f32_16x16x32_bf16 v[8:11], a[80:83], v[92:95], v[8:11]\n\t"
        "buffer_load_dwordx4 v[48:51], v55, s[20:23], s48 offen\n\t"
        "buffer_load_dword v61, v61, s[20:23], s48 offen offset:512\n\t"
        "s_waitcnt vmcnt(10)\n\t"
        "s_barrier\n\t"
        "ds_read_b128 v[104:107], v128 offset:16384\n\t"
        "s_add_u32 m0, s37, 36864\n\t"
        "ds_read_b128 v[108:111], v128 offset:16448\n\t"
        "s_waitcnt lgkmcnt(3)\n\t"
        "v_mfma_f32_16x16x32_bf16 v[12:15], a[72:75], v[96:99], v[12:15]\n\t"
        "ds_read_b32 v64, v70 offset:0\n\t"
        "s_add_u32 s36, s36, s38\n\t"
        "ds_read_b64_tr_b16 a[84:85], v131 offset:0\n\t"
        "v_mfma_f32_16x16x32_bf16 v[16:19], a[76:79], v[96:99], v[16:19]\n\t"
        "ds_read_b32 v67, v67 offset:0\n\t"
        "ds_read_b64_tr_b16 a[86:87], v131 offset:768\n\t"
        "v_readlane_b32 s46, v155, 37\n\t"
        "v_readlane_b32 s47, v155, 40\n\t"
        "v_mfma_f32_16x16x32_bf16 v[20:23], a[80:83], v[96:99], v[20:23]\n\t"
        "ds_read_b64_tr_b16 a[88:89], v132 offset:0\n\t"
        "ds_read_b64_tr_b16 a[90:91], v132 offset:768\n\t"
        "v_add_f32 v58, s46, v68\n\t"
        "s_waitcnt lgkmcnt(8)\n\t"
        "v_mfma_f32_16x16x32_bf16 v[24:27], a[72:75], v[100:103], v[24:27]\n\t"
        "buffer_load_dwordx4 v140, s[24:27], s36 offen offset:0 lds\n\t"
        "ds_read_b64_tr_b16 a[92:93], v133 offset:0\n\t"
        "v_add_f32 v59, s47, v69\n\t"
        "v_mfma_f32_16x16x32_bf16 v[28:31], a[76:79], v[100:103], v[28:31]\n\t"
        "ds_read_b64_tr_b16 a[94:95], v133 offset:768\n\t"
        "ds_read_b128 v[112:115], v128 offset:4224\n\t"
        "v_mfma_f32_16x16x32_bf16 v[32:35], a[80:83], v[100:103], v[32:35]\n\t"
        "ds_read_b64_tr_b16 a[96:97], v131 offset:6144\n\t"
        "ds_read_b64_tr_b16 a[98:99], v131 offset:6912\n\t"
        "s_waitcnt lgkmcnt(12)\n\t"
        "v_mfma_f32_16x16x32_bf16 v[24:27], a[24:27], v[104:107], v[24:27]\n\t"
        "ds_read_b64_tr_b16 a[100:101], v132 offset:6144\n\t"
        "ds_read_b64_tr_b16 a[102:103], v132 offset:6912\n\t"
        "v_mfma_f32_16x16x32_bf16 v[28:31], a[28:31], v[104:107], v[28:31]\n\t"
        "buffer_load_dwordx4 v141, s[24:27], s36 offen offset:1024 lds\n\t"
        "ds_read_b64_tr_b16 a[104:105], v133 offset:6144\n\t"
        "v_mfma_f32_16x16x32_bf16 v[32:35], a[32:35], v[104:107], v[32:35]\n\t"
        "ds_read_b64_tr_b16 a[106:107], v133 offset:6912\n\t"
        "v_mfma_f32_16x16x32_bf16 v[36:39], a[0:3], v[104:107], v[36:39]\n\t"
        "ds_read_b128 v[84:87], v128 offset:8320\n\t"
        "s_waitcnt lgkmcnt(12)\n\t"
        "v_perm_b32 v56, v64, v64, s44\n\t"
        "v_mfma_f32_16x16x32_bf16 v[40:43], a[4:7], v[104:107], v[40:43]\n\t"
        "ds_read_b32 v66, v70 offset:2560\n\t"
        "v_perm_b32 v57, v64, v64, s45\n\t"
        "v_mfma_f32_16x16x32_bf16 v[44:47], a[8:11], v[104:107], v[44:47]\n\t"
        "buffer_load_dwordx4 v142, s[24:27], s36 offen offset:2048 lds\n\t"
        "s_add_u32 m0, s37, 49152\n\t"
        "v_mfma_f32_16x16x32_bf16 v[24:27], a[60:63], v[108:111], v[24:27]\n\t"
        "ds_read_b128 v[88:91], v128 offset:12416\n\t"
        "s_add_u32 s36, s36, s5\n\t"
        "v_mfma_f32_16x16x32_bf16 v[28:31], a[64:67], v[108:111], v[28:31]\n\t"
        ".LpM0%=:\n\t"
        "s_cbranch_execz .LrM%=\n\t"
        "ds_read_b128 v[92:95], v128 offset:16512\n\t"
        "v_add_f32 v56, v56, v58\n\t"
        "v_mfma_f32_16x16x32_bf16 v[32:35], a[68:71], v[108:111], v[32:35]\n\t"
        "v_add_f32 v57, v57, v59\n\t"
        "v_cvt_pk_bf16_f32 v52, v56, v57\n\t"
        "v_mfma_f32_16x16x32_bf16 v[36:39], a[36:39], v[108:111], v[36:39]\n\t"
        "buffer_load_dwordx4 v140, s[24:27], s36 offen offset:0 lds\n\t"
        "v_cvt_pk_bf16_f32 v58, v58, v59\n\t"
        "v_mfma_f32_16x16x32_bf16 v[40:43], a[40:43], v[108:111], v[40:43]\n\t"
        "v_cmp_ne_u32 s[64:65], v52, v58\n\t"
        "v_perm_b32 v57, v67, v67, s45\n\t"
        "v_mfma_f32_16x16x32_bf16 v[44:47], a[44:47], v[108:111], v[44:47]\n\t"
        "v_mbcnt_lo_u32_b32 v56, s64, 1\n\t"
        "v_mbcnt_hi_u32_b32 v56, s65, v56\n\t"
        "s_waitcnt lgkmcnt(5)\n\t"
        "v_mfma_f32_16x16x32_bf16 v[12:15], a[84:87], v[112:115], v[12:15]\n\t"
        "s_bcnt1_i32_b64 s61, s[64:65]\n\t"
        "v_add_u32 v54, s61, 1\n\t"
        "v_mfma_f32_16x16x32_bf16 v[16:19], a[88:91], v[112:115], v[16:19]\n\t"
        "buffer_load_dwordx4 v141, s[24:27], s36 offen offset:1024 lds\n\t"
        "v_lshl_add_u32 v56, v56, 3, s60\n\t"
        "v_mfma_f32_16x16x32_bf16 v[20:23], a[92:95], v[112:115], v[20:23]\n\t"
        "s_mov_b64 exec, s[64:65]\n\t"
        "ds_write_b64 v56, v[52:53]\n\t"
        "s_mov_b64 exec, -1\n\t"
        "s_waitcnt lgkmcnt(3)\n\t"
        "v_mfma_f32_16x16x32_bf16 v[0:3], a[96:99], v[84:87], v[0:3]\n\t"
        "v_swap_b32 v53, v80\n\t"
        "v_perm_b32 v56, v67, v67, s44\n\t"
        "v_add_f32 v56, v56, v78\n\t"
        "v_mfma_f32_16x16x32_bf16 v[4:7], a[100:103], v[84:87], v[4:7]\n\t"
        "v_add_f32 v57, v57, v79\n\t"
        "v_cvt_pk_bf16_f32 v52, v56, v57\n\t"
        "v_mfma_f32_16x16x32_bf16 v[8:11], a[104:107], v[84:87], v[8:11]\n\t"
        "buffer_load_dwordx4 v142, s[24:27], s36 offen offset:2048 lds\n\t"
        "v_readlane_b32 s40, v155, 12\n\t"
        "v_mfma_f32_16x16x32_bf16 v[24:27], a[84:87], v[84:87], v[24:27]\n\t"
        "v_readlane_b32 s43, v155, 0\n\t"
        "s_add_u32 m0, s40, s35\n\t"
        "v_mfma_f32_16x16x32_bf16 v[28:31], a[88:91], v[84:87], v[28:31]\n\t"
        "v_cvt_pk_bf16_f32 v58, v78, v79\n\t"
        "v_cmp_ne_u32 s[64:65], v52, v58\n\t"
        "v_mfma_f32_16x16x32_bf16 v[32:35], a[92:95], v[84:87], v[32:35]\n\t"
        "s_nop 0\n\t"
        "s_and_b64 s[64:65], s[64:65], 0xffffff\n\t"
        "v_mbcnt_lo_u32_b32 v56, s64, v54\n\t"
        "s_waitcnt lgkmcnt(1)\n\t"
        "v_mfma_f32_16x16x32_bf16 v[12:15], a[96:99], v[88:91], v[12:15]\n\t"
        "s_waitcnt vmcnt(8)\n\t"
        "s_barrier\n\t"
        "buffer_load_dwordx4 v134, s[16:19], s43 offen  lds\n\t"
        "ds_read_b128 v[96:99], v128 offset:0\n\t"
        "v_mfma_f32_16x16x32_bf16 v[16:19], a[100:103], v[88:91], v[16:19]\n\t"
        "ds_read_b128 v[100:103], v128 offset:64\n\t"
        "ds_read_b128 v[104:107], v128 offset:128\n\t"
        "v_readlane_b32 s40, v155, 13\n\t"
        "v_mfma_f32_16x16x32_bf16 v[20:23], a[104:107], v[88:91], v[20:23]\n\t"
        "ds_read_b128 v[108:111], v128 offset:20480\n\t"
        "ds_read_b64_tr_b16 a[108:109], v131 offset:12288\n\t"
        "v_readlane_b32 s43, v155, 1\n\t"
        "v_mfma_f32_16x16x32_bf16 v[36:39], a[84:87], v[88:91], v[36:39]\n\t"
        "ds_read_b128 v[112:115], v128 offset:20544\n\t"
        "s_add_u32 m0, s40, s35\n\t"
        "ds_read_b64_tr_b16 a[110:111], v131 offset:13056\n\t"
        "v_mfma_f32_16x16x32_bf16 v[40:43], a[88:91], v[88:91], v[40:43]\n\t"
        "buffer_load_dwordx4 v135, s[16:19], s43 offen  lds\n\t"
        "ds_read_b64_tr_b16 a[112:113], v132 offset:12288\n\t"
        "v_readlane_b32 s40, v155, 14\n\t"
        "v_mfma_f32_16x16x32_bf16 v[44:47], a[92:95], v[88:91], v[44:47]\n\t"
        "ds_read_b64_tr_b16 a[114:115], v132 offset:13056\n\t"
        "ds_read_b64_tr_b16 a[116:117], v133 offset:12288\n\t"
        "v_readlane_b32 s43, v155, 2\n\t"
        "v_mfma_f32_16x16x32_bf16 v[24:27], a[96:99], v[92:95], v[24:27]\n\t"
        "ds_read_b128 v[84:87], v128 offset:20608\n\t"
        "s_add_u32 m0, s40, s35\n\t"
        "ds_read_b64_tr_b16 a[118:119], v133 offset:13056\n\t"
        "v_mfma_f32_16x16x32_bf16 v[28:31], a[100:103], v[92:95], v[28:31]\n\t"
        "ds_read_b64_tr_b16 a[120:121], v131 offset:18432\n\t"
        "s_bcnt1_i32_b64 s46, s[64:65]\n\t"
        "v_mfma_f32_16x16x32_bf16 v[32:35], a[104:107], v[92:95], v[32:35]\n\t"
        "buffer_load_dwordx4 v134, s[16:19], s43 offen  lds\n\t"
        "s_bitcmp1_b32 s42, 10\n\t"
        "s_cbranch_scc0 .Lx40%=\n\t"
        "v_readlane_b32 s40, v155, 20\n\t"
        "v_readlane_b32 s43, v155, 8\n\t"
        "s_nop 0\n\t"
        "s_add_u32 m0, s40, s35\n\t"
        "s_nop 2\n\t"
        "buffer_load_dwordx4 v136, s[16:19], s43 offen  lds\n\t"
        ".Lx40%=:\n\t"
        "v_mfma_f32_16x16x32_bf16 v[36:39], a[72:75], v[92:95], v[36:39]\n\t"
        "ds_read_b64_tr_b16 a[122:123], v131 offset:19200\n\t"
        "v_readlane_b32 s40, v155, 15\n\t"
        "v_mfma_f32_16x16x32_bf16 v[40:43], a[76:79], v[92:95], v[40:43]\n\t"
        "ds_read_b64_tr_b16 a[124:125], v132 offset:18432\n\t"
        "v_readlane_b32 s43, v155, 3\n\t"
        "v_mfma_f32_16x16x32_bf16 v[44:47], a[80:83], v[92:95], v[44:47]\n\t"
        "ds_read_b64_tr_b16 a[126:127], v132 offset:19200\n\t"
        "s_add_u32 m0, s40, s35\n\t"
        "s_waitcnt lgkmcnt(9)\n\t"
        "v_mfma_f32_16x16x32_bf16 v[0:3], a[12:15], v[96:99], v[0:3]\n\t"
        "ds_read_b64_tr_b16 a[128:129], v133 offset:18432\n\t"
        "ds_read_b64_tr_b16 a[130:131], v133 offset:19200\n\t"
        "s_add_u32 s61, s61, s46\n\t"
        "v_mfma_f32_16x16x32_bf16 v[4:7], a[16:19], v[96:99], v[4:7]\n\t"
        "ds_read_b128 v[88:91], v129 offset:0\n\t"
        "v_add_u32 v54, s46, v54\n\t"
        "v_mfma_f32_16x16x32_bf16 v[8:11], a[20:23], v[96:99], v[8:11]\n\t"
        "ds_read_b128 v[92:95], v129 offset:4096\n\t"
        "s_mov_b32 s3, s61\n\t"
        "v_mfma_f32_16x16x32_bf16 v[0:3], a[48:51], v[100:103], v[0:3]\n\t"
        "ds_read_b64_tr_b16 a[132:133], v131 offset:24576\n\t"
        "v_lshl_add_u32 v56, v56, 3, s60\n\t"
        "v_mfma_f32_16x16x32_bf16 v[4:7], a[52:55], v[100:103], v[4:7]\n\t"
        "s_mov_b64 exec, s[64:65]\n\t"
        "ds_write_b64 v56, v[52:53]\n\t"
        "s_mov_b64 exec, -1\n\t"
        "v_mfma_f32_16x16x32_bf16 v[8:11], a[56:59], v[100:103], v[8:11]\n\t"
        "ds_read_b64_tr_b16 a[134:135], v131 offset:25344\n\t"
        "v_add_u32 v53, 4096, v80\n\t"
        "v_mfma_f32_16x16x32_bf16 v[0:3], a[84:87], v[104:107], v[0:3]\n\t"
        "buffer_load_dwordx4 v135, s[16:19], s43 offen  lds\n\t"
        "s_add_u32 m0, s37, 0\n\t"
        "v_mfma_f32_16x16x32_bf16 v[4:7], a[88:91], v[104:107], v[4:7]\n\t"
        "ds_read_b64_tr_b16 a[136:137], v132 offset:24576\n\t"
        "s_add_u32 s36, s36, s39\n\t"
        "v_mfma_f32_16x16x32_bf16 v[8:11], a[92:95], v[104:107], v[8:11]\n\t"
        "ds_read_b64_tr_b16 a[138:139], v132 offset:25344\n\t"
        "v_readlane_b32 s46, v155, 39\n\t"
        "v_mfma_f32_16x16x32_bf16 v[36:39], a[24:27], v[108:111], v[36:39]\n\t"
        "ds_read_b64_tr_b16 a[140:141], v133 offset:24576\n\t"
        "v_readlane_b32 s47, v155, 42\n\t"
        "v_mfma_f32_16x16x32_bf16 v[40:43], a[28:31], v[108:111], v[40:43]\n\t"
        "ds_read_b64_tr_b16 a[142:143], v133 offset:25344\n\t"
        "v_perm_b32 v56, v66, v66, s44\n\t"
        "v_mfma_f32_16x16x32_bf16 v[44:47], a[32:35], v[108:111], v[44:47]\n\t"
        "buffer_load_dwordx4 v140, s[24:27], s36 offen offset:0 lds\n\t"
        "v_perm_b32 v57, v66, v66, s45\n\t"
        "v_mfma_f32_16x16x32_bf16 v[36:39], a[60:63], v[112:115], v[36:39]\n\t"
        "ds_read_b128 v[96:99], v129 offset:8192\n\t"
        "v_add_f32 v58, s46, v68\n\t"
        "v_mfma_f32_16x16x32_bf16 v[40:43], a[64:67], v[112:115], v[40:43]\n\t"
        "ds_read_b64_tr_b16 a[144:145], v131 offset:30720\n\t"
        "v_add_f32 v59, s47, v69\n\t"
        "v_mfma_f32_16x16x32_bf16 v[44:47], a[68:71], v[112:115], v[44:47]\n\t"
        "ds_read_b64_tr_b16 a[146:147], v131 offset:31488\n\t"
        "v_add_f32 v56, v56, v58\n\t"
        "s_waitcnt lgkmcnt(9)\n\t"
        "v_mfma_f32_16x16x32_bf16 v[36:39], a[96:99], v[84:87], v[36:39]\n\t"
        "buffer_load_dwordx4 v141, s[24:27], s36 offen offset:1024 lds\n\t"
        "v_add_f32 v57, v57, v59\n\t"
        "v_mfma_f32_16x16x32_bf16 v[40:43], a[100:103], v[84:87], v[40:43]\n\t"
        "ds_read_b128 v[100:103], v129 offset:12288\n\t"
        "v_max_f32 v56, 0, v56\n\t"
        "v_mfma_f32_16x16x32_bf16 v[44:47], a[104:107], v[84:87], v[44:47]\n\t"
        "ds_read_b64_tr_b16 a[148:149], v132 offset:30720\n\t"
        "v_max_f32 v57, 0, v57\n\t"
        "v_mfma_f32_16x16x32_bf16 v[0:3], a[120:123], v[88:91], v[0:3]\n\t"
        "ds_read_b64_tr_b16 a[150:151], v132 offset:31488\n\t"
        "v_max_f32 v58, 0, v58\n\t"
        "v_mfma_f32_16x16x32_bf16 v[4:7], a[124:127], v[88:91], v[4:7]\n\t"
        "buffer_load_dwordx4 v142, s[24:27], s36 offen offset:2048 lds\n\t"
        "s_add_u32 m0, s37, 12288\n\t"
        "v_mfma_f32_16x16x32_bf16 v[8:11], a[128:131], v[88:91], v[8:11]\n\t"
        "ds_read_b64_tr_b16 a[152:153], v133 offset:30720\n\t"
        "s_add_u32 s36, s36, s38\n\t"
        "v_mfma_f32_16x16x32_bf16 v[0:3], a[108:111], v[92:95], v[0:3]\n\t"
        "ds_read_b64_tr_b16 a[154:155], v133 offset:31488\n\t"
        "v_max_f32 v59, 0, v59\n\t"
        "v_mfma_f32_16x16x32_bf16 v[4:7], a[112:115], v[92:95], v[4:7]\n\t"
        "v_cvt_pk_bf16_f32 v52, v56, v57\n\t"
        "v_cvt_pk_bf16_f32 v58, v58, v59\n\t"
        "v_mfma_f32_16x16x32_bf16 v[8:11], a[116:119], v[92:95], v[8:11]\n\t"
        "v_cmp_ne_u32 s[64:65], v52, v58\n\t"
        "v_mfma_f32_16x16x32_bf16 v[12:15], a[120:123], v[92:95], v[12:15]\n\t"
        "ds_read_b128 v[104:107], v129 offset:16384\n\t"
        "v_mbcnt_lo_u32_b32 v56, s64, v54\n\t"
        "v_mfma_f32_16x16x32_bf16 v[16:19], a[124:127], v[92:95], v[16:19]\n\t"
        "v_mbcnt_hi_u32_b32 v56, s65, v56\n\t"
        "s_bcnt1_i32_b64 s46, s[64:65]\n\t"
        "v_mfma_f32_16x16x32_bf16 v[20:23], a[128:131], v[92:95], v[20:23]\n\t"
        "s_add_u32 s61, s61, s46\n\t"
        "v_min_u32 v56, 97, v56\n\t"
        "s_waitcnt lgkmcnt(5)\n\t"
        "v_mfma_f32_16x16x32_bf16 v[0:3], a[132:135], v[96:99], v[0:3]\n\t"
        "s_waitcnt vmcnt(14)\n\t"
        "v_and_b32 v48, s54, v48\n\t"
        "v_pk_max_u16 v60, v154, v48\n\t"
        "v_mfma_f32_16x16x32_bf16 v[4:7], a[136:139], v[96:99], v[4:7]\n\t"
        "v_and_b32 v49, s54, v49\n\t"
        "v_pk_max_u16 v60, v60, v49\n\t"
        "v_mfma_f32_16x16x32_bf16 v[8:11], a[140:143], v[96:99], v[8:11]\n\t"
        "v_and_b32 v50, s54, v50\n\t"
        "v_pk_max_u16 v60, v60, v50\n\t"
        "v_mfma_f32_16x16x32_bf16 v[12:15], a[108:111], v[96:99], v[12:15]\n\t"
        "ds_read_b128 v[108:111], v129 offset:20480\n\t"
        "v_and_b32 v51, s54, v51\n\t"
        "v_mfma_f32_16x16x32_bf16 v[16:19], a[112:115], v[96:99], v[16:19]\n\t"
        "v_pk_max_u16 v60, v60, v51\n\t"
        "s_waitcnt vmcnt(13)\n\t"
        "v_and_b32 v61, s54, v61\n\t"
        "v_mfma_f32_16x16x32_bf16 v[20:23], a[116:119], v[96:99], v[20:23]\n\t"
        "v_pk_max_u16 v60, v60, v61\n\t"
        "v_cmp_ne_u32 s[12:13], v60, v154\n\t"
        "v_mfma_f32_16x16x32_bf16 v[24:27], a[120:123], v[96:99], v[24:27]\n\t"
        "s_lshl_b32 s6, 256, s4\n\t"
        "v_lshl_add_u32 v56, v56, 3, s60\n\t"
        "v_mfma_f32_16x16x32_bf16 v[28:31], a[124:127], v[96:99], v[28:31]\n\t"
        "s_cmp_lg_u64 s[12:13], 0\n\t"
        "s_cselect_b32 s6, s6, 0\n\t"
        "v_mfma_f32_16x16x32_bf16 v[32:35], a[128:131], v[96:99], v[32:35]\n\t"
        "s_mov_b64 exec, s[64:65]\n\t"
        "ds_write_b64 v56, v[52:53]\n\t"
        "s_mov_b64 exec, -1\n\t"
        "v_mfma_f32_16x16x32_bf16 v[12:15], a[132:135], v[100:103], v[12:15]\n\t"
        "s_waitcnt vmcnt(7) lgkmcnt(2)\n\t"
        "s_barrier\n\t"
        "buffer_load_dwordx4 v140, s[24:27], s36 offen offset:0 lds\n\t"
        "ds_read_b64_tr_b16 a[156:157], v131 offset:36864\n\t"
        "v_mfma_f32_16x16x32_bf16 v[16:19], a[136:139], v[100:103], v[16:19]\n\t"
        "s_cmp_gt_u32 s61, s53\n\t"
        "s_cselect_b32 s8, 1, 0\n\t"
        "ds_read_b64_tr_b16 a[158:159], v131 offset:37632\n\t"
        "ds_read_b64_tr_b16 a[160:161], v132 offset:36864\n\t"
        "v_mfma_f32_16x16x32_bf16 v[20:23], a[140:143], v[100:103], v[20:23]\n\t"
        "ds_read_b64_tr_b16 a[162:163], v132 offset:37632\n\t"
        "ds_read_b64_tr_b16 a[164:165], v133 offset:36864\n\t"
        "s_or_b32 s56, s56, s8\n\t"
        "v_mfma_f32_16x16x32_bf16 v[24:27], a[108:111], v[100:103], v[24:27]\n\t"
        "s_cmp_gt_u32 s3, 64\n\t"
        "s_cselect_b32 s8, 1, 0\n\t"
        "ds_read_b64_tr_b16 a[166:167], v133 offset:37632\n\t"
        "v_mfma_f32_16x16x32_bf16 v[28:31], a[112:115], v[100:103], v[28:31]\n\t"
        "buffer_load_dwordx4 v141, s[24:27], s36 offen offset:1024 lds\n\t"
        "s_or_b32 s56, s56, s8\n\t"
        "v_mfma_f32_16x16x32_bf16 v[32:35], a[116:119], v[100:103], v[32:35]\n\t"
        "ds_read_b128 v[112:115], v129 offset:64\n\t"
        "s_min_u32 s61, s61, 80\n\t"
        "v_mfma_f32_16x16x32_bf16 v[36:39], a[120:123], v[100:103], v[36:39]\n\t"
        "ds_read_b128 v[84:87], v129 offset:4160\n\t"
        "v_min_u32 v78, s61, v116\n\t"
        "v_mfma_f32_16x16x32_bf16 v[40:43], a[124:127], v[100:103], v[40:43]\n\t"
        "ds_read_b64_tr_b16 a[168:169], v131 offset:43008\n\t"
        "v_lshl_add_u32 v78, v78, 3, s60\n\t"
        "v_mfma_f32_16x16x32_bf16 v[44:47], a[128:131], v[100:103], v[44:47]\n\t"
        "ds_read_b64 v[62:63], v78\n\t"
        "v_cmp_ge_u32 s[66:67], s61, v116\n\t"
        "v_mfma_f32_16x16x32_bf16 v[24:27], a[132:135], v[104:107], v[24:27]\n\t"
        "buffer_load_dwordx4 v142, s[24:27], s36 offen offset:2048 lds\n\t"
        "v_readlane_b32 s40, v155, 16\n\t"
        "v_mfma_f32_16x16x32_bf16 v[28:31], a[136:139], v[104:107], v[28:31]\n\t"
        "ds_read_b64_tr_b16 a[170:171], v131 offset:43776\n\t"
        "v_readlane_b32 s43, v155, 4\n\t"
        "v_mfma_f32_16x16x32_bf16 v[32:35], a[140:143], v[104:107], v[32:35]\n\t"
        "ds_read_b64_tr_b16 a[172:173], v132 offset:43008\n\t"
        "s_add_u32 m0, s40, s35\n\t"
        "v_mfma_f32_16x16x32_bf16 v[36:39], a[108:111], v[104:107], v[36:39]\n\t"
        "ds_read_b64_tr_b16 a[174:175], v132 offset:43776\n\t"
        "ds_read_b64_tr_b16 a[176:177], v133 offset:43008\n\t"
        "s_mov_b32 s46, 0x07060302\n\t"
        "v_mfma_f32_16x16x32_bf16 v[40:43], a[112:115], v[104:107], v[40:43]\n\t"
        "buffer_load_dwordx4 v134, s[16:19], s43 offen  lds\n\t"
        "ds_read_b64_tr_b16 a[178:179], v133 offset:43776\n\t"
        "s_bitcmp1_b32 s42, 11\n\t"
        "s_cbranch_scc0 .Lx42%=\n\t"
        "v_readlane_b32 s40, v155, 21\n\t"
        "v_readlane_b32 s43, v155, 9\n\t"
        "s_nop 0\n\t"
        "s_add_u32 m0, s40, s35\n\t"
        "s_nop 2\n\t"
        "buffer_load_dwordx4 v137, s[16:19], s43 offen  lds\n\t"
        "v_readlane_b32 s40, v155, 22\n\t"
        "v_readlane_b32 s43, v155, 10\n\t"
        "s_nop 0\n\t"
        "s_add_u32 m0, s40, s35\n\t"
        "s_nop 2\n\t"
        "buffer_load_dwordx4 v136, s[16:19], s43 offen  lds\n\t"
        ".Lx42%=:\n\t"
        "v_mfma_f32_16x16x32_bf16 v[44:47], a[116:119], v[104:107], v[44:47]\n\t"
        "ds_read_b128 v[88:91], v129 offset:8256\n\t"
        "v_readlane_b32 s40, v155, 17\n\t"
        "s_waitcnt lgkmcnt(8)\n\t"
        "v_mfma_f32_16x16x32_bf16 v[36:39], a[132:135], v[108:111], v[36:39]\n\t"
        "ds_read_b128 v[92:95], v129 offset:12352\n\t"
        "v_readlane_b32 s43, v155, 5\n\t"
        "v_mfma_f32_16x16x32_bf16 v[40:43], a[136:139], v[108:111], v[40:43]\n\t"
        "ds_read_b64_tr_b16 a[180:181], v131 offset:49152\n\t"
        "s_add_u32 m0, s40, s35\n\t"
        "v_mfma_f32_16x16x32_bf16 v[44:47], a[140:143], v[108:111], v[44:47]\n\t"
        "s_mov_b64 exec, s[66:67]\n\t"
        "s_waitcnt lgkmcnt(8)\n\t"
        "ds_write_b32 v63, v117\n\t"
        "s_mov_b64 exec, -1\n\t"
        "v_mfma_f32_16x16x32_bf16 v[0:3], a[156:159], v[112:115], v[0:3]\n\t"
        "buffer_load_dwordx4 v135, s[16:19], s43 offen  lds\n\t"
        "v_readlane_b32 s40, v155, 18\n\t"
        "v_mfma_f32_16x16x32_bf16 v[4:7], a[160:163], v[112:115], v[4:7]\n\t"
        "ds_read_b64_tr_b16 a[182:183], v131 offset:49920\n\t"
        "v_readlane_b32 s43, v155, 6\n\t"
        "v_mfma_f32_16x16x32_bf16 v[8:11], a[164:167], v[112:115], v[8:11]\n\t"
        "ds_read_b64_tr_b16 a[184:185], v132 offset:49152\n\t"
        "s_add_u32 m0, s40, s35\n\t"
        "v_mfma_f32_16x16x32_bf16 v[0:3], a[144:147], v[84:87], v[0:3]\n\t"
        "ds_read_b64_tr_b16 a[186:187], v132 offset:49920\n\t"
        "v_and_b32 v78, 0xfffff800, v63\n\t"
        "v_mfma_f32_16x16x32_bf16 v[4:7], a[148:151], v[84:87], v[4:7]\n\t"
        "ds_read_b128 v[64:67], v78 offset:1536\n\t"
        "v_lshlrev_b32 v72, 16, v62\n\t"
        "v_mfma_f32_16x16x32_bf16 v[8:11], a[152:155], v[84:87], v[8:11]\n\t"
        "buffer_load_dwordx4 v136, s[16:19], s43 offen  lds\n\t"
        "s_bitcmp1_b32 s42, 11\n\t"
        "s_cbranch_scc0 .Lx44%=\n\t"
        "v_readlane_b32 s40, v155, 23\n\t"
        "v_readlane_b32 s43, v155, 11\n\t"
        "s_nop 0\n\t"
        "s_add_u32 m0, s40, s35\n\t"
        "s_nop 2\n\t"
        "buffer_load_dwordx4 v137, s[16:19], s43 offen  lds\n\t"
        ".Lx44%=:\n\t"
        "v_mfma_f32_16x16x32_bf16 v[12:15], a[156:159], v[84:87], v[12:15]\n\t"
        "ds_read_b128 v[96:99], v129 offset:16448\n\t"
        "ds_read_b64_tr_b16 a[188:189], v133 offset:49152\n\t"
        "v_readlane_b32 s40, v155, 19\n\t"
        "v_mfma_f32_16x16x32_bf16 v[16:19], a[160:163], v[84:87], v[16:19]\n\t"
        "ds_read_b64_tr_b16 a[190:191], v133 offset:49920\n\t"
        "v_readlane_b32 s43, v155, 7\n\t"
        "v_mfma_f32_16x16x32_bf16 v[20:23], a[164:167], v[84:87], v[20:23]\n\t"
        "ds_read_b64_tr_b16 a[192:193], v131 offset:55296\n\t"
        "s_add_u32 m0, s40, s35\n\t"
        "s_waitcnt lgkmcnt(8)\n\t"
        "v_mfma_f32_16x16x32_bf16 v[0:3], a[168:171], v[88:91], v[0:3]\n\t"
        "buffer_load_dwordx4 v137, s[16:19], s43 offen  lds\n\t"
        "s_add_u32 m0, s37, 24576\n\t"
        "v_mfma_f32_16x16x32_bf16 v[4:7], a[172:175], v[88:91], v[4:7]\n\t"
        "ds_read_b64_tr_b16 a[194:195], v131 offset:56064\n\t"
        "s_add_u32 s36, s36, s5\n\t"
        "v_mfma_f32_16x16x32_bf16 v[8:11], a[176:179], v[88:91], v[8:11]\n\t"
        "ds_read_b64_tr_b16 a[196:197], v132 offset:55296\n\t"
        "v_and_b32 v73, 0xffff0000, v62\n\t"
        "v_mfma_f32_16x16x32_bf16 v[12:15], a[144:147], v[88:91], v[12:15]\n\t"
        "ds_read_b128 v[100:103], v129 offset:20544\n\t"
        "v_med3_f32 v72, v72, 0, s63\n\t"
        "v_mfma_f32_16x16x32_bf16 v[16:19], a[148:151], v[88:91], v[16:19]\n\t"
        "buffer_load_dwordx4 v140, s[24:27], s36 offen offset:0 lds\n\t"
        "v_med3_f32 v73, v73, 0, s63\n\t"
        "v_mfma_f32_16x16x32_bf16 v[20:23], a[152:155], v[88:91], v[20:23]\n\t"
        "ds_read_b64_tr_b16 a[198:199], v132 offset:56064\n\t"
        "v_perm_b32 v62, v73, v72, s46\n\t"
        "v_mfma_f32_16x16x32_bf16 v[24:27], a[156:159], v[88:91], v[24:27]\n\t"
        "ds_read_b64_tr_b16 a[200:201], v133 offset:55296\n\t"
        "v_fract_f32 v74, v72\n\t"
        "v_mfma_f32_16x16x32_bf16 v[28:31], a[160:163], v[88:91], v[28:31]\n\t"
        "ds_read_b64_tr_b16 a[202:203], v133 offset:56064\n\t"
        "v_fract_f32 v75, v73\n\t"
        "v_mfma_f32_16x16x32_bf16 v[32:35], a[164:167], v[88:91], v[32:35]\n\t"
        "buffer_load_dwordx4 v141, s[24:27], s36 offen offset:1024 lds\n\t"
        "v_cvt_u32_f32 v76, v72\n\t"
        "v_mfma_f32_16x16x32_bf16 v[12:15], a[168:171], v[92:95], v[12:15]\n\t"
        "ds_read_b128 v[104:107], v129 offset:128\n\t"
        "v_cvt_u32_f32 v77, v73\n\t"
        "v_mfma_f32_16x16x32_bf16 v[16:19], a[172:175], v[92:95], v[16:19]\n\t"
        "ds_read_b128 v[108:111], v129 offset:4224\n\t"
        "v_cmp_neq_f32 s[64:65], 0, v74\n\t"
        "v_mfma_f32_16x16x32_bf16 v[20:23], a[176:179], v[92:95], v[20:23]\n\t"
        "v_cmp_neq_f32 s[44:45], 0, v75\n\t"
        "v_sub_f32 v78, 1.0, v74\n\t"
        "v_mfma_f32_16x16x32_bf16 v[24:27], a[144:147], v[92:95], v[24:27]\n\t"
        "buffer_load_dwordx4 v142, s[24:27], s36 offen offset:2048 lds\n\t"
        "s_add_u32 m0, s37, 36864\n\t"
        "v_mfma_f32_16x16x32_bf16 v[28:31], a[148:151], v[92:95], v[28:31]\n\t"
        "s_add_u32 s36, s36, s39\n\t"
        "v_sub_f32 v79, 1.0, v75\n\t"
        "v_mfma_f32_16x16x32_bf16 v[32:35], a[152:155], v[92:95], v[32:35]\n\t"
        "s_waitcnt vmcnt(10)\n\t"
        "s_barrier\n\t"
        "v_mfma_f32_16x16x32_bf16 v[36:39], a[156:159], v[92:95], v[36:39]\n\t"
        "ds_read_b64_tr_b16 a[204:205], v131 offset:0\n\t"
        "v_cvt_pk_bf16_f32 v79, v78, v79\n\t"
        "v_mfma_f32_16x16x32_bf16 v[40:43], a[160:163], v[92:95], v[40:43]\n\t"
        "buffer_load_dwordx4 v140, s[24:27], s36 offen offset:0 lds\n\t"
        "v_lshlrev_b32 v78, 16, v79\n\t"
        "v_mfma_f32_16x16x32_bf16 v[44:47], a[164:167], v[92:95], v[44:47]\n\t"
        "ds_read_b64_tr_b16 a[206:207], v131 offset:768\n\t"
        "v_and_b32 v79, 0xffff0000, v79\n\t"
        "s_waitcnt lgkmcnt(4)\n\t"
        "v_mfma_f32_16x16x32_bf16 v[24:27], a[168:171], v[96:99], v[24:27]\n\t"
        "ds_read_b64_tr_b16 a[208:209], v132 offset:0\n\t"
        "v_mul_f32 v80, v78, v79\n\t"
        "v_mfma_f32_16x16x32_bf16 v[28:31], a[172:175], v[96:99], v[28:31]\n\t"
        "ds_read_b64_tr_b16 a[210:211], v132 offset:768\n\t"
        "v_mul_f32 v78, v78, v75\n\t"
        "v_mfma_f32_16x16x32_bf16 v[32:35], a[176:179], v[96:99], v[32:35]\n\t"
        "buffer_load_dwordx4 v141, s[24:27], s36 offen offset:1024 lds\n\t"
        "v_mul_f32 v79, v74, v79\n\t"
        "v_mfma_f32_16x16x32_bf16 v[36:39], a[144:147], v[96:99], v[36:39]\n\t"
        "ds_read_b64_tr_b16 a[212:213], v133 offset:0\n\t"
        "ds_read_b64_tr_b16 a[214:215], v133 offset:768\n\t"
        "v_mul_f32 v81, v74, v75\n\t"
        "v_mfma_f32_16x16x32_bf16 v[40:43], a[148:151], v[96:99], v[40:43]\n\t"
        "ds_read_b128 v[112:115], v129 offset:8320\n\t"
        "v_cndmask_b32 v78, v79, v78, s[44:45]\n\t"
        "v_mfma_f32_16x16x32_bf16 v[44:47], a[152:155], v[96:99], v[44:47]\n\t"
        "ds_read_b64_tr_b16 a[216:217], v131 offset:6144\n\t"
        "v_cndmask_b32 v79, 0, v79, s[44:45]\n\t"
        "v_mfma_f32_16x16x32_bf16 v[36:39], a[168:171], v[100:103], v[36:39]\n\t"
        "buffer_load_dwordx4 v142, s[24:27], s36 offen offset:2048 lds\n\t"
        "s_add_u32 m0, s37, 49152\n\t"
        "v_mfma_f32_16x16x32_bf16 v[40:43], a[172:175], v[100:103], v[40:43]\n\t"
        "s_waitcnt vmcnt(10)\n\t"
        "s_barrier\n\t"
        "v_mfma_f32_16x16x32_bf16 v[44:47], a[176:179], v[100:103], v[44:47]\n\t"
        "ds_read_b128 v[84:87], v129 offset:12416\n\t"
        "s_add_u32 s36, s36, s38\n\t"
        "s_waitcnt lgkmcnt(7)\n\t"
        "v_mfma_f32_16x16x32_bf16 v[0:3], a[192:195], v[104:107], v[0:3]\n\t"
        "ds_read_b64_tr_b16 a[218:219], v131 offset:6912\n\t"
        "v_cvt_pk_bf16_f32 v70, v80, v78\n\t"
        "v_mfma_f32_16x16x32_bf16 v[4:7], a[196:199], v[104:107], v[4:7]\n\t"
        "buffer_load_dwordx4 v140, s[24:27], s36 offen offset:0 lds\n\t"
        "v_cvt_pk_bf16_f32 v71, v79, v81\n\t"
        "v_mfma_f32_16x16x32_bf16 v[8:11], a[200:203], v[104:107], v[8:11]\n\t"
        "ds_read_b64_tr_b16 a[220:221], v132 offset:6144\n\t"
        "v_pk_max_u16 v78, v62, v64\n\t"
        "v_mfma_f32_16x16x32_bf16 v[0:3], a[180:183], v[108:111], v[0:3]\n\t"
        "ds_read_b64_tr_b16 a[222:223], v132 offset:6912\n\t"
        "v_pk_min_u16 v78, v78, v65\n\t"
        "v_mfma_f32_16x16x32_bf16 v[4:7], a[184:187], v[108:111], v[4:7]\n\t"
        "ds_read_b64_tr_b16 a[224:225], v133 offset:6144\n\t"
        "v_cmp_ne_u32 vcc, v78, v62\n\t"
        "v_mfma_f32_16x16x32_bf16 v[8:11], a[188:191], v[108:111], v[8:11]\n\t"
        "ds_read_b64_tr_b16 a[226:227], v133 offset:6912\n\t"
        "v_sub_u32 v78, v77, v67\n\t"
        "v_mfma_f32_16x16x32_bf16 v[12:15], a[192:195], v[108:111], v[12:15]\n\t"
        "ds_read_b128 v[88:91], v129 offset:16512\n\t"
        "v_mad_u32_u24 v79, v77, 6, v66\n\t"
        "v_mfma_f32_16x16x32_bf16 v[16:19], a[196:199], v[108:111], v[16:19]\n\t"
        "ds_read_b64_tr_b16 a[228:229], v131 offset:12288\n\t"
        "v_mad_u32_u24 v79, v76, s62, v79\n\t"
        "v_mfma_f32_16x16x32_bf16 v[20:23], a[200:203], v[108:111], v[20:23]\n\t"
        "ds_read_b64_tr_b16 a[230:231], v131 offset:13056\n\t"
        "s_and_b64 vcc, vcc, s[66:67]\n\t"
        "s_waitcnt lgkmcnt(9)\n\t"
        "v_mfma_f32_16x16x32_bf16 v[0:3], a[204:207], v[112:115], v[0:3]\n\t"
        "ds_read_b64_tr_b16 a[232:233], v132 offset:12288\n\t"
        "s_or_b64 s[56:57], s[56:57], vcc\n\t"
        "v_mfma_f32_16x16x32_bf16 v[4:7], a[208:211], v[112:115], v[4:7]\n\t"
        "ds_read_b64_tr_b16 a[234:235], v132 offset:13056\n\t"
        "v_bfe_u32 v80, v78, 2, 1\n\t"
        "v_mfma_f32_16x16x32_bf16 v[8:11], a[212:215], v[112:115], v[8:11]\n\t"
        "ds_read_b64_tr_b16 a[236:237], v133 offset:12288\n\t"
        "v_or_b32 v80, v80, v79\n\t"
        "v_mfma_f32_16x16x32_bf16 v[12:15], a[180:183], v[112:115], v[12:15]\n\t"
        "ds_read_b128 v[92:95], v129 offset:20608\n\t"
        "v_add_u32 v78, 1, v78\n\t"
        "v_mfma_f32_16x16x32_bf16 v[16:19], a[184:187], v[112:115], v[16:19]\n\t"
        "ds_read_b64_tr_b16 a[238:239], v133 offset:13056\n\t"
        "v_bfe_u32 v78, v78, 2, 1\n\t"
        "v_mfma_f32_16x16x32_bf16 v[20:23], a[188:191], v[112:115], v[20:23]\n\t"
        "ds_read_b64_tr_b16 a[240:241], v131 offset:18432\n\t"
        "v_add3_u32 v78, v79, v78, 6\n\t"
        "v_mfma_f32_16x16x32_bf16 v[24:27], a[192:195], v[112:115], v[24:27]\n\t"
        "ds_read_b128 v[96:99], v130 offset:0\n\t"
        "v_cndmask_b32 v78, v80, v78, s[44:45]\n\t"
        "v_mfma_f32_16x16x32_bf16 v[28:31], a[196:199], v[112:115], v[28:31]\n\t"
        "ds_read_b64_tr_b16 a[242:243], v131 offset:19200\n\t"
        "v_cndmask_b32 v79, 0, v119, s[64:65]\n\t"
        "v_mfma_f32_16x16x32_bf16 v[32:35], a[200:203], v[112:115], v[32:35]\n\t"
        "ds_read_b64_tr_b16 a[244:245], v132 offset:18432\n\t"
        "v_add_u32 v81, v80, v79\n\t"
        "s_waitcnt lgkmcnt(10)\n\t"
        "v_mfma_f32_16x16x32_bf16 v[12:15], a[204:207], v[84:87], v[12:15]\n\t"
        "ds_read_b128 v[100:103], v130 offset:4096\n\t"
        "v_add_u32 v82, v78, v79\n\t"
        "v_mfma_f32_16x16x32_bf16 v[16:19], a[208:211], v[84:87], v[16:19]\n\t"
        "ds_read_b64_tr_b16 a[246:247], v132 offset:19200\n\t"
        "v_cndmask_b32 v78, v81, v78, s[44:45]\n\t"
        "v_mfma_f32_16x16x32_bf16 v[20:23], a[212:215], v[84:87], v[20:23]\n\t"
        "buffer_load_dwordx4 v141, s[24:27], s36 offen offset:1024 lds\n\t"
        "v_lshl_or_b32 v68, v78, 16, v80\n\t"
        "v_mfma_f32_16x16x32_bf16 v[24:27], a[180:183], v[84:87], v[24:27]\n\t"
        "ds_read_b64_tr_b16 a[248:249], v133 offset:18432\n\t"
        "v_lshl_or_b32 v69, v82, 16, v81\n\t"
        "v_mfma_f32_16x16x32_bf16 v[28:31], a[184:187], v[84:87], v[28:31]\n\t"
        "ds_write_b128 v118, v[68:71]\n\t"
        "v_mfma_f32_16x16x32_bf16 v[32:35], a[188:191], v[84:87], v[32:35]\n\t"
        "s_cmp_gt_u32 s3, 32\n\t"
        "s_cselect_b32 s8, 2, 0\n\t"
        "v_mfma_f32_16x16x32_bf16 v[36:39], a[192:195], v[84:87], v[36:39]\n\t"
        "s_cmp_lg_u64 s[56:57], 0\n\t"
        "s_addc_u32 s8, s8, s6\n\t"
        "v_mfma_f32_16x16x32_bf16 v[40:43], a[196:199], v[84:87], v[40:43]\n\t"
        "s_cmp_gt_u32 s61, 64\n\t"
        "s_cselect_b32 s47, 4, 0\n\t"
        "v_mfma_f32_16x16x32_bf16 v[44:47], a[200:203], v[84:87], v[44:47]\n\t"
        "ds_read_b128 v[160:163], v185 offset:0\n\t"
        "s_or_b32 s8, s8, s47\n\t"
        "v_mfma_f32_16x16x32_bf16 v[24:27], a[204:207], v[88:91], v[24:27]\n\t"
        "buffer_load_dwordx4 v142, s[24:27], s36 offen offset:2048 lds\n\t"
        "v_mov_b32 v151, s8\n\t"
        "v_mfma_f32_16x16x32_bf16 v[28:31], a[208:211], v[88:91], v[28:31]\n\t"
        "s_mov_b64 exec, 1\n\t"
        "ds_write_b32 v153, v151\n\t"
        "s_mov_b64 exec, -1\n\t"
        "v_mfma_f32_16x16x32_bf16 v[32:35], a[212:215], v[88:91], v[32:35]\n\t"
        "ds_read_b64_tr_b16 a[250:251], v133 offset:19200\n\t"
        "v_xor_b32 v154, 32, v186\n\t"
        "v_mfma_f32_16x16x32_bf16 v[36:39], a[180:183], v[88:91], v[36:39]\n\t"
        "ds_read_b128 v[104:107], v130 offset:8192\n\t"
        "v_mfma_f32_16x16x32_bf16 v[40:43], a[184:187], v[88:91], v[40:43]\n\t"
        "v_mfma_f32_16x16x32_bf16 v[44:47], a[188:191], v[88:91], v[44:47]\n\t"
        "s_waitcnt lgkmcnt(5)\n\t"
        "v_mfma_f32_16x16x32_bf16 v[36:39], a[204:207], v[92:95], v[36:39]\n\t"
        "ds_read_b128 v[108:111], v130 offset:12288\n\t"
        "v_mfma_f32_16x16x32_bf16 v[40:43], a[208:211], v[92:95], v[40:43]\n\t"
        "v_mfma_f32_16x16x32_bf16 v[44:47], a[212:215], v[92:95], v[44:47]\n\t"
        "s_waitcnt lgkmcnt(4)\n\t"
        "v_lshlrev_b32_sdwa v157, 5, v160 dst_sel:DWORD dst_unused:UNUSED_PAD src0_sel:DWORD src1_sel:WORD_0\n\t"
        "v_lshlrev_b32_sdwa v158, 5, v160 dst_sel:DWORD dst_unused:UNUSED_PAD src0_sel:DWORD src1_sel:WORD_1\n\t"
        "v_mfma_f32_16x16x32_bf16 v[0:3], a[228:231], v[96:99], v[0:3]\n\t"
        "v_or_b32 v157, v184, v157\n\t"
        "v_or_b32 v158, v184, v158\n\t"
        "v_mfma_f32_16x16x32_bf16 v[4:7], a[232:235], v[96:99], v[4:7]\n\t"
        "v_xor_b32 v159, 32, v157\n\t"
        "v_xor_b32 v152, 32, v158\n\t"
        "v_mfma_f32_16x16x32_bf16 v[8:11], a[236:239], v[96:99], v[8:11]\n\t"
        "v_perm_b32 v164, v162, v162, v168\n\t"
        "v_perm_b32 v165, v162, v162, v169\n\t"
        "v_mfma_f32_16x16x32_bf16 v[0:3], a[216:219], v[100:103], v[0:3]\n\t"
        "v_perm_b32 v166, v162, v162, v170\n\t"
        "v_perm_b32 v167, v162, v162, v171\n\t"
        "v_mfma_f32_16x16x32_bf16 v[4:7], a[220:223], v[100:103], v[4:7]\n\t"
        "v_cmp_ne_u32 vcc, 0, v163\n\t"
        "v_mfma_f32_16x16x32_bf16 v[8:11], a[224:227], v[100:103], v[8:11]\n\t"
        "v_mfma_f32_16x16x32_bf16 v[12:15], a[228:231], v[100:103], v[12:15]\n\t"
        "ds_read_b128 v[112:115], v130 offset:16384\n\t"
        "v_mfma_f32_16x16x32_bf16 v[16:19], a[232:235], v[100:103], v[16:19]\n\t"
        "v_mfma_f32_16x16x32_bf16 v[20:23], a[236:239], v[100:103], v[20:23]\n\t"
        "s_waitcnt vmcnt(3) lgkmcnt(1)\n\t"
        "s_barrier\n\t"
        "ds_read_b64_tr_b16 a[252:253], v131 offset:24576\n\t"
        "ds_read_b64_tr_b16 a[254:255], v131 offset:25344\n\t"
        "v_mfma_f32_16x16x32_bf16 v[0:3], a[240:243], v[104:107], v[0:3]\n\t"
        "ds_read_b128 v[134:137], v182 offset:0\n\t"
        "ds_read_b64_tr_b16 v[188:189], v132 offset:24576\n\t"
        "v_mfma_f32_16x16x32_bf16 v[4:7], a[244:247], v[104:107], v[4:7]\n\t"
        "ds_read_b64_tr_b16 v[60:61], v157 offset:0\n\t"
        "ds_read_b64_tr_b16 v[190:191], v132 offset:25344\n\t"
        "v_mfma_f32_16x16x32_bf16 v[8:11], a[248:251], v[104:107], v[8:11]\n\t"
        "ds_read_b64_tr_b16 v[62:63], v158 offset:0\n\t"
        "ds_read_b64_tr_b16 v[192:193], v133 offset:24576\n\t"
        "v_mfma_f32_16x16x32_bf16 v[12:15], a[216:219], v[104:107], v[12:15]\n\t"
        "ds_read_b64_tr_b16 v[64:65], v159 offset:0\n\t"
        "ds_read_b128 v[84:87], v130 offset:20480\n\t"
        "v_mfma_f32_16x16x32_bf16 v[16:19], a[220:223], v[104:107], v[16:19]\n\t"
        "ds_read_b64_tr_b16 v[66:67], v152 offset:0\n\t"
        "ds_read_b64_tr_b16 v[194:195], v133 offset:25344\n\t"
        "v_mfma_f32_16x16x32_bf16 v[20:23], a[224:227], v[104:107], v[20:23]\n\t"
        "ds_read_b64_tr_b16 v[68:69], v157 offset:64\n\t"
        "v_mfma_f32_16x16x32_bf16 v[24:27], a[228:231], v[104:107], v[24:27]\n\t"
        "ds_read_b64_tr_b16 v[70:71], v158 offset:64\n\t"
        "v_mfma_f32_16x16x32_bf16 v[28:31], a[232:235], v[104:107], v[28:31]\n\t"
        "ds_read_b64_tr_b16 v[72:73], v159 offset:64\n\t"
        "v_mfma_f32_16x16x32_bf16 v[32:35], a[236:239], v[104:107], v[32:35]\n\t"
        "ds_read_b64_tr_b16 v[74:75], v152 offset:64\n\t"
        "v_mfma_f32_16x16x32_bf16 v[12:15], a[240:243], v[108:111], v[12:15]\n\t"
        "ds_read_b64_tr_b16 v[76:77], v157 offset:128\n\t"
        "ds_read_b64_tr_b16 v[196:197], v131 offset:30720\n\t"
        "s_waitcnt lgkmcnt(13)\n\t"
        "v_or3_b32 v134, v134, v135, v136\n\t"
        "v_mfma_f32_16x16x32_bf16 v[16:19], a[244:247], v[108:111], v[16:19]\n\t"
        "ds_read_b64_tr_b16 v[78:79], v158 offset:128\n\t"
        "ds_read_b64_tr_b16 v[198:199], v131 offset:31488\n\t"
        "v_or_b32 v134, v134, v137\n\t"
        "v_mfma_f32_16x16x32_bf16 v[20:23], a[248:251], v[108:111], v[20:23]\n\t"
        "ds_read_b64_tr_b16 v[200:201], v132 offset:30720\n\t"
        "ds_read_b64_tr_b16 v[202:203], v132 offset:31488\n\t"
        "v_readfirstlane_b32 s6, v134\n\t"
        "s_waitcnt lgkmcnt(12)\n\t"
        "v_mfma_f32_16x16x32_bf16 v[48:51], v[60:63], v[164:167], 0\n\t"
        "ds_read_b64_tr_b16 v[204:205], v133 offset:30720\n\t"
        "ds_read_b64_tr_b16 v[206:207], v133 offset:31488\n\t"
        "v_mfma_f32_16x16x32_bf16 v[24:27], a[216:219], v[108:111], v[24:27]\n\t"
        "ds_read_b128 v[88:91], v130 offset:64\n\t"
        "s_waitcnt lgkmcnt(12)\n\t"
        "v_mfma_f32_16x16x32_bf16 v[52:55], v[64:67], v[164:167], 0\n\t"
        "ds_read_b64_tr_b16 v[80:81], v159 offset:128\n\t"
        ".LpM1%=:\n\t"
        "s_cbranch_execz .LrM%=\n\t"
        "v_mfma_f32_16x16x32_bf16 v[28:31], a[220:223], v[108:111], v[28:31]\n\t"
        "ds_read_b64_tr_b16 v[82:83], v152 offset:128\n\t"
        "s_waitcnt lgkmcnt(12)\n\t"
        "v_mfma_f32_16x16x32_bf16 v[56:59], v[68:71], v[164:167], 0\n\t"
        "ds_read_b128 v[92:95], v130 offset:4160\n\t"
        "v_mfma_f32_16x16x32_bf16 v[32:35], a[224:227], v[108:111], v[32:35]\n\t"
        "ds_read_b64_tr_b16 v[208:209], v131 offset:36864\n\t"
        "s_waitcnt lgkmcnt(12)\n\t"
        "v_mfma_f32_16x16x32_bf16 v[116:119], v[72:75], v[164:167], 0\n\t"
        "ds_read_b64_tr_b16 v[210:211], v131 offset:37632\n\t"
        "v_mfma_f32_16x16x32_bf16 v[36:39], a[228:231], v[108:111], v[36:39]\n\t"
        "s_waitcnt vmcnt(0)\n\t"
        "s_barrier\n\t"
        "s_waitcnt lgkmcnt(10)\n\t"
        "v_mfma_f32_16x16x32_bf16 v[120:123], v[76:79], v[164:167], 0\n\t"
        "ds_read_b64_tr_b16 v[212:213], v132 offset:36864\n\t"
        "v_mfma_f32_16x16x32_bf16 v[40:43], a[232:235], v[108:111], v[40:43]\n\t"
        "ds_read_b64_tr_b16 v[214:215], v132 offset:37632\n\t"
        "v_mfma_f32_16x16x32_bf16 v[44:47], a[236:239], v[108:111], v[44:47]\n\t"
        "ds_read_b64_tr_b16 v[216:217], v133 offset:36864\n\t"
        "v_mfma_f32_16x16x32_bf16 v[24:27], a[240:243], v[112:115], v[24:27]\n\t"
        "ds_read_b64_tr_b16 v[218:219], v133 offset:37632\n\t"
        "v_mfma_f32_16x16x32_bf16 v[28:31], a[244:247], v[112:115], v[28:31]\n\t"
        "ds_read_b128 v[96:99], v130 offset:8256\n\t"
        "s_waitcnt lgkmcnt(8)\n\t"
        "v_mfma_f32_16x16x32_bf16 v[124:127], v[80:83], v[164:167], 0\n\t"
        "ds_read_b64_tr_b16 v[220:221], v131 offset:43008\n\t"
        "s_cbranch_vccz .Lx46%=\n\t"
        "v_lshlrev_b32_sdwa v157, 5, v161 dst_sel:DWORD dst_unused:UNUSED_PAD src0_sel:DWORD src1_sel:WORD_0\n\t"
        "v_lshlrev_b32_sdwa v158, 5, v161 dst_sel:DWORD dst_unused:UNUSED_PAD src0_sel:DWORD src1_sel:WORD_1\n\t"
        "v_or_b32 v157, v184, v157\n\t"
        "v_or_b32 v158, v184, v158\n\t"
        "v_xor_b32 v159, 32, v157\n\t"
        "v_xor_b32 v152, 32, v158\n\t"
        "ds_read_b64_tr_b16 v[60:61], v157 offset:0\n\t"
        "ds_read_b64_tr_b16 v[62:63], v158 offset:0\n\t"
        "ds_read_b64_tr_b16 v[64:65], v159 offset:0\n\t"
        "ds_read_b64_tr_b16 v[66:67], v152 offset:0\n\t"
        "ds_read_b64_tr_b16 v[68:69], v157 offset:64\n\t"
        "ds_read_b64_tr_b16 v[70:71], v158 offset:64\n\t"
        "ds_read_b64_tr_b16 v[72:73], v159 offset:64\n\t"
        "ds_read_b64_tr_b16 v[74:75], v152 offset:64\n\t"
        "ds_read_b64_tr_b16 v[76:77], v157 offset:128\n\t"
        "ds_read_b64_tr_b16 v[78:79], v158 offset:128\n\t"
        "ds_read_b64_tr_b16 v[80:81], v159 offset:128\n\t"
        "ds_read_b64_tr_b16 v[82:83], v152 offset:128\n\t"
        "v_perm_b32 v164, v163, v163, v168\n\t"
        "v_perm_b32 v165, v163, v163, v169\n\t"
        "v_perm_b32 v166, v163, v163, v170\n\t"
        "v_perm_b32 v167, v163, v163, v171\n\t"
        "s_waitcnt lgkmcnt(10)\n\t"
        "s_nop 0\n\t"
        "v_mfma_f32_16x16x32_bf16 v[48:51], v[60:63], v[164:167], v[48:51]\n\t"
        "s_waitcnt lgkmcnt(8)\n\t"
        "v_mfma_f32_16x16x32_bf16 v[52:55], v[64:67], v[164:167], v[52:55]\n\t"
        "s_waitcnt lgkmcnt(6)\n\t"
        "v_mfma_f32_16x16x32_bf16 v[56:59], v[68:71], v[164:167], v[56:59]\n\t"
        "s_waitcnt lgkmcnt(4)\n\t"
        "v_mfma_f32_16x16x32_bf16 v[116:119], v[72:75], v[164:167], v[116:119]\n\t"
        "s_waitcnt lgkmcnt(2)\n\t"
        "v_mfma_f32_16x16x32_bf16 v[120:123], v[76:79], v[164:167], v[120:123]\n\t"
        "s_waitcnt lgkmcnt(0)\n\t"
        "v_mfma_f32_16x16x32_bf16 v[124:127], v[80:83], v[164:167], v[124:127]\n\t"
        "s_nop 4\n\t"
        ".Lx46%=:\n\t"
        "v_mfma_f32_16x16x32_bf16 v[32:35], a[248:251], v[112:115], v[32:35]\n\t"
        "ds_read_b128 v[160:163], v185 offset:256\n\t"
        "v_cvt_pk_bf16_f32 v48, v48, v49\n\t"
        "v_mfma_f32_16x16x32_bf16 v[36:39], a[216:219], v[112:115], v[36:39]\n\t"
        "ds_read_b64_tr_b16 v[222:223], v131 offset:43776\n\t"
        "v_cvt_pk_bf16_f32 v49, v50, v51\n\t"
        "v_mfma_f32_16x16x32_bf16 v[40:43], a[220:223], v[112:115], v[40:43]\n\t"
        "ds_write_b64 v186, v[48:49] offset:0\n\t"
        "v_mfma_f32_16x16x32_bf16 v[44:47], a[224:227], v[112:115], v[44:47]\n\t"
        "ds_read_b64_tr_b16 v[224:225], v132 offset:43008\n\t"
        "v_cvt_pk_bf16_f32 v52, v52, v53\n\t"
        "v_mfma_f32_16x16x32_bf16 v[36:39], a[240:243], v[84:87], v[36:39]\n\t"
        "ds_read_b128 v[100:103], v130 offset:12352\n\t"
        "v_cvt_pk_bf16_f32 v53, v54, v55\n\t"
        "v_mfma_f32_16x16x32_bf16 v[40:43], a[244:247], v[84:87], v[40:43]\n\t"
        "ds_write_b64 v154, v[52:53] offset:0\n\t"
        "v_mfma_f32_16x16x32_bf16 v[44:47], a[248:251], v[84:87], v[44:47]\n\t"
        "ds_read_b64_tr_b16 v[226:227], v132 offset:43776\n\t"
        "v_cvt_pk_bf16_f32 v56, v56, v57\n\t"
        "v_mfma_f32_16x16x32_bf16 v[0:3], v[196:199], v[88:91], v[0:3]\n\t"
        "ds_read_b64_tr_b16 v[228:229], v133 offset:43008\n\t"
        "v_cvt_pk_bf16_f32 v57, v58, v59\n\t"
        "v_mfma_f32_16x16x32_bf16 v[4:7], v[200:203], v[88:91], v[4:7]\n\t"
        "ds_write_b64 v186, v[56:57] offset:64\n\t"
        "v_mfma_f32_16x16x32_bf16 v[8:11], v[204:207], v[88:91], v[8:11]\n\t"
        "ds_read_b64_tr_b16 v[230:231], v133 offset:43776\n\t"
        "s_waitcnt lgkmcnt(9)\n\t"
        "v_lshlrev_b32_sdwa v157, 5, v160 dst_sel:DWORD dst_unused:UNUSED_PAD src0_sel:DWORD src1_sel:WORD_0\n\t"
        "v_mfma_f32_16x16x32_bf16 v[0:3], a[252:255], v[92:95], v[0:3]\n\t"
        "ds_read_b64_tr_b16 v[232:233], v131 offset:49152\n\t"
        "v_lshlrev_b32_sdwa v158, 5, v160 dst_sel:DWORD dst_unused:UNUSED_PAD src0_sel:DWORD src1_sel:WORD_1\n\t"
        "v_mfma_f32_16x16x32_bf16 v[4:7], v[188:191], v[92:95], v[4:7]\n\t"
        "ds_read_b64_tr_b16 v[234:235], v131 offset:49920\n\t"
        "v_or_b32 v157, v184, v157\n\t"
        "v_mfma_f32_16x16x32_bf16 v[8:11], v[192:195], v[92:95], v[8:11]\n\t"
        "ds_read_b64_tr_b16 v[60:61], v157 offset:0\n\t"
        "v_or_b32 v158, v184, v158\n\t"
        "v_mfma_f32_16x16x32_bf16 v[12:15], v[196:199], v[92:95], v[12:15]\n\t"
        "ds_read_b64_tr_b16 v[62:63], v158 offset:0\n\t"
        "v_xor_b32 v159, 32, v157\n\t"
        "v_mfma_f32_16x16x32_bf16 v[16:19], v[200:203], v[92:95], v[16:19]\n\t"
        "ds_read_b64_tr_b16 v[64:65], v159 offset:0\n\t"
        "v_xor_b32 v152, 32, v158\n\t"
        "v_mfma_f32_16x16x32_bf16 v[20:23], v[204:207], v[92:95], v[20:23]\n\t"
        "ds_read_b64_tr_b16 v[66:67], v152 offset:0\n\t"
        "v_cvt_pk_bf16_f32 v116, v116, v117\n\t"
        "v_mfma_f32_16x16x32_bf16 v[0:3], v[208:211], v[96:99], v[0:3]\n\t"
        "ds_read_b64_tr_b16 v[68:69], v157 offset:64\n\t"
        "v_cvt_pk_bf16_f32 v117, v118, v119\n\t"
        "v_mfma_f32_16x16x32_bf16 v[4:7], v[212:215], v[96:99], v[4:7]\n\t"
        "ds_write_b64 v154, v[116:117] offset:64\n\t"
        "v_mfma_f32_16x16x32_bf16 v[8:11], v[216:219], v[96:99], v[8:11]\n\t"
        "ds_read_b128 v[104:107], v130 offset:16448\n\t"
        "v_cvt_pk_bf16_f32 v120, v120, v121\n\t"
        "v_mfma_f32_16x16x32_bf16 v[12:15], a[252:255], v[96:99], v[12:15]\n\t"
        "ds_read_b64_tr_b16 v[70:71], v158 offset:64\n\t"
        "v_cvt_pk_bf16_f32 v121, v122, v123\n\t"
        "v_mfma_f32_16x16x32_bf16 v[16:19], v[188:191], v[96:99], v[16:19]\n\t"
        "ds_write_b64 v186, v[120:121] offset:128\n\t"
        "v_mfma_f32_16x16x32_bf16 v[20:23], v[192:195], v[96:99], v[20:23]\n\t"
        "ds_read_b64_tr_b16 v[72:73], v159 offset:64\n\t"
        "v_cvt_pk_bf16_f32 v124, v124, v125\n\t"
        "v_mfma_f32_16x16x32_bf16 v[24:27], v[196:199], v[96:99], v[24:27]\n\t"
        "ds_read_b64_tr_b16 v[74:75], v152 offset:64\n\t"
        "v_cvt_pk_bf16_f32 v125, v126, v127\n\t"
        "v_mfma_f32_16x16x32_bf16 v[28:31], v[200:203], v[96:99], v[28:31]\n\t"
        "ds_write_b64 v154, v[124:125] offset:128\n\t"
        "v_mfma_f32_16x16x32_bf16 v[32:35], v[204:207], v[96:99], v[32:35]\n\t"
        "ds_read_b128 v[108:111], v130 offset:20544\n\t"
        "v_perm_b32 v164, v162, v162, v168\n\t"
        "s_waitcnt lgkmcnt(9)\n\t"
        "v_mfma_f32_16x16x32_bf16 v[12:15], v[208:211], v[100:103], v[12:15]\n\t"
        "ds_read_b64_tr_b16 v[76:77], v157 offset:128\n\t"
        "ds_read_b64_tr_b16 v[236:237], v132 offset:49152\n\t"
        "v_perm_b32 v165, v162, v162, v169\n\t"
        "v_mfma_f32_16x16x32_bf16 v[16:19], v[212:215], v[100:103], v[16:19]\n\t"
        "ds_read_b64_tr_b16 v[78:79], v158 offset:128\n\t"
        "ds_read_b64_tr_b16 v[238:239], v132 offset:49920\n\t"
        "v_perm_b32 v166, v162, v162, v170\n\t"
        "v_mfma_f32_16x16x32_bf16 v[20:23], v[216:219], v[100:103], v[20:23]\n\t"
        "ds_read_b64_tr_b16 v[240:241], v133 offset:49152\n\t"
        "ds_read_b64_tr_b16 v[242:243], v133 offset:49920\n\t"
        "v_perm_b32 v167, v162, v162, v171\n\t"
        "v_mfma_f32_16x16x32_bf16 v[24:27], a[252:255], v[100:103], v[24:27]\n\t"
        "ds_read_b128 v[112:115], v130 offset:128\n\t"
        "v_cmp_ne_u32 vcc, 0, v163\n\t"
        "v_mfma_f32_16x16x32_bf16 v[48:51], v[60:63], v[164:167], 0\n\t"
        "ds_read_b64_tr_b16 v[80:81], v159 offset:128\n\t"
        "v_mfma_f32_16x16x32_bf16 v[28:31], v[188:191], v[100:103], v[28:31]\n\t"
        "ds_read_b64_tr_b16 v[82:83], v152 offset:128\n\t"
        "v_mfma_f32_16x16x32_bf16 v[52:55], v[64:67], v[164:167], 0\n\t"
        "ds_read_b128 v[84:87], v130 offset:4224\n\t"
        "v_mfma_f32_16x16x32_bf16 v[32:35], v[192:195], v[100:103], v[32:35]\n\t"
        "ds_read_b64_tr_b16 v[244:245], v131 offset:55296\n\t"
        "s_waitcnt lgkmcnt(11)\n\t"
        "v_mfma_f32_16x16x32_bf16 v[56:59], v[68:71], v[164:167], 0\n\t"
        "ds_read_b64_tr_b16 v[246:247], v131 offset:56064\n\t"
        "v_mfma_f32_16x16x32_bf16 v[36:39], v[196:199], v[100:103], v[36:39]\n\t"
        "ds_read_b64_tr_b16 v[248:249], v132 offset:55296\n\t"
        "v_mfma_f32_16x16x32_bf16 v[116:119], v[72:75], v[164:167], 0\n\t"
        "ds_read_b64_tr_b16 v[250:251], v132 offset:56064\n\t"
        "v_mfma_f32_16x16x32_bf16 v[40:43], v[200:203], v[100:103], v[40:43]\n\t"
        "ds_read_b64_tr_b16 v[252:253], v133 offset:55296\n\t"
        "s_waitcnt lgkmcnt(10)\n\t"
        "v_mfma_f32_16x16x32_bf16 v[120:123], v[76:79], v[164:167], 0\n\t"
        "ds_read_b64_tr_b16 v[254:255], v133 offset:56064\n\t"
        "v_mfma_f32_16x16x32_bf16 v[44:47], v[204:207], v[100:103], v[44:47]\n\t"
        "ds_read_b128 v[132:135], v187 offset:0\n\t"
        "v_mfma_f32_16x16x32_bf16 v[24:27], v[208:211], v[104:107], v[24:27]\n\t"
        "ds_read_b128 v[136:139], v187 offset:16\n\t"
        "s_waitcnt lgkmcnt(9)\n\t"
        "v_mfma_f32_16x16x32_bf16 v[124:127], v[80:83], v[164:167], 0\n\t"
        "ds_read_b32 v140, v187 offset:32\n\t"
        "s_cbranch_vccz .Lx48%=\n\t"
        "v_lshlrev_b32_sdwa v157, 5, v161 dst_sel:DWORD dst_unused:UNUSED_PAD src0_sel:DWORD src1_sel:WORD_0\n\t"
        "v_lshlrev_b32_sdwa v158, 5, v161 dst_sel:DWORD dst_unused:UNUSED_PAD src0_sel:DWORD src1_sel:WORD_1\n\t"
        "v_or_b32 v157, v184, v157\n\t"
        "v_or_b32 v158, v184, v158\n\t"
        "v_xor_b32 v159, 32, v157\n\t"
        "v_xor_b32 v152, 32, v158\n\t"
        "ds_read_b64_tr_b16 v[60:61], v157 offset:0\n\t"
        "ds_read_b64_tr_b16 v[62:63], v158 offset:0\n\t"
        "ds_read_b64_tr_b16 v[64:65], v159 offset:0\n\t"
        "ds_read_b64_tr_b16 v[66:67], v152 offset:0\n\t"
        "ds_read_b64_tr_b16 v[68:69], v157 offset:64\n\t"
        "ds_read_b64_tr_b16 v[70:71], v158 offset:64\n\t"
        "ds_read_b64_tr_b16 v[72:73], v159 offset:64\n\t"
        "ds_read_b64_tr_b16 v[74:75], v152 offset:64\n\t"
        "ds_read_b64_tr_b16 v[76:77], v157 offset:128\n\t"
        "ds_read_b64_tr_b16 v[78:79], v158 offset:128\n\t"
        "ds_read_b64_tr_b16 v[80:81], v159 offset:128\n\t"
        "ds_read_b64_tr_b16 v[82:83], v152 offset:128\n\t"
        "v_perm_b32 v164, v163, v163, v168\n\t"
        "v_perm_b32 v165, v163, v163, v169\n\t"
        "v_perm_b32 v166, v163, v163, v170\n\t"
        "v_perm_b32 v167, v163, v163, v171\n\t"
        "s_waitcnt lgkmcnt(10)\n\t"
        "s_nop 0\n\t"
        "v_mfma_f32_16x16x32_bf16 v[48:51], v[60:63], v[164:167], v[48:51]\n\t"
        "s_waitcnt lgkmcnt(8)\n\t"
        "v_mfma_f32_16x16x32_bf16 v[52:55], v[64:67], v[164:167], v[52:55]\n\t"
        "s_waitcnt lgkmcnt(6)\n\t"
        "v_mfma_f32_16x16x32_bf16 v[56:59], v[68:71], v[164:167], v[56:59]\n\t"
        "s_waitcnt lgkmcnt(4)\n\t"
        "v_mfma_f32_16x16x32_bf16 v[116:119], v[72:75], v[164:167], v[116:119]\n\t"
        "s_waitcnt lgkmcnt(2)\n\t"
        "v_mfma_f32_16x16x32_bf16 v[120:123], v[76:79], v[164:167], v[120:123]\n\t"
        "s_waitcnt lgkmcnt(0)\n\t"
        "v_mfma_f32_16x16x32_bf16 v[124:127], v[80:83], v[164:167], v[124:127]\n\t"
        "s_nop 4\n\t"
        ".Lx48%=:\n\t"
        "v_mfma_f32_16x16x32_bf16 v[28:31], v[212:215], v[104:107], v[28:31]\n\t"
        "ds_read_b128 v[88:91], v130 offset:8320\n\t"
        "v_cvt_pk_bf16_f32 v48, v48, v49\n\t"
        "v_mfma_f32_16x16x32_bf16 v[32:35], v[216:219], v[104:107], v[32:35]\n\t"
        "v_cvt_pk_bf16_f32 v49, v50, v51\n\t"
        "v_cvt_pk_bf16_f32 v52, v52, v53\n\t"
        "v_mfma_f32_16x16x32_bf16 v[36:39], a[252:255], v[104:107], v[36:39]\n\t"
        "ds_write_b64 v186, v[48:49] offset:12288\n\t"
        "v_mfma_f32_16x16x32_bf16 v[40:43], v[188:191], v[104:107], v[40:43]\n\t"
        "v_cvt_pk_bf16_f32 v53, v54, v55\n\t"
        "v_cvt_pk_bf16_f32 v56, v56, v57\n\t"
        "v_mfma_f32_16x16x32_bf16 v[44:47], v[192:195], v[104:107], v[44:47]\n\t"
        "ds_write_b64 v154, v[52:53] offset:12288\n\t"
        "v_mfma_f32_16x16x32_bf16 v[36:39], v[208:211], v[108:111], v[36:39]\n\t"
        "ds_read_b128 v[92:95], v130 offset:12416\n\t"
        "v_cvt_pk_bf16_f32 v57, v58, v59\n\t"
        "v_mfma_f32_16x16x32_bf16 v[40:43], v[212:215], v[108:111], v[40:43]\n\t"
        "ds_write_b64 v186, v[56:57] offset:12352\n\t"
        "v_mfma_f32_16x16x32_bf16 v[44:47], v[216:219], v[108:111], v[44:47]\n\t"
        "v_cvt_pk_bf16_f32 v116, v116, v117\n\t"
        "v_cvt_pk_bf16_f32 v117, v118, v119\n\t"
        "v_mfma_f32_16x16x32_bf16 v[0:3], v[232:235], v[112:115], v[0:3]\n\t"
        "ds_write_b64 v154, v[116:117] offset:12352\n\t"
        "v_mfma_f32_16x16x32_bf16 v[4:7], v[236:239], v[112:115], v[4:7]\n\t"
        "v_cvt_pk_bf16_f32 v120, v120, v121\n\t"
        "v_cvt_pk_bf16_f32 v121, v122, v123\n\t"
        "v_mfma_f32_16x16x32_bf16 v[8:11], v[240:243], v[112:115], v[8:11]\n\t"
        "ds_write_b64 v186, v[120:121] offset:12416\n\t"
        "s_waitcnt lgkmcnt(6)\n\t"
        "v_mfma_f32_16x16x32_bf16 v[0:3], v[220:223], v[84:87], v[0:3]\n\t"
        "v_cvt_pk_bf16_f32 v124, v124, v125\n\t"
        "v_cvt_pk_bf16_f32 v125, v126, v127\n\t"
        "v_mfma_f32_16x16x32_bf16 v[4:7], v[224:227], v[84:87], v[4:7]\n\t"
        "ds_write_b64 v154, v[124:125] offset:12416\n\t"
        "v_mfma_f32_16x16x32_bf16 v[8:11], v[228:231], v[84:87], v[8:11]\n\t"
        "v_mfma_f32_16x16x32_bf16 v[12:15], v[232:235], v[84:87], v[12:15]\n\t"
        "ds_read_b128 v[96:99], v130 offset:16512\n\t"
        "v_mfma_f32_16x16x32_bf16 v[16:19], v[236:239], v[84:87], v[16:19]\n\t"
        "v_mfma_f32_16x16x32_bf16 v[20:23], v[240:243], v[84:87], v[20:23]\n\t"
        "v_mfma_f32_16x16x32_bf16 v[0:3], v[244:247], v[88:91], v[0:3]\n\t"
        "v_mfma_f32_16x16x32_bf16 v[4:7], v[248:251], v[88:91], v[4:7]\n\t"
        "s_waitcnt lgkmcnt(1)\n\t"
        "s_barrier\n\t"
        "v_mfma_f32_16x16x32_bf16 v[8:11], v[252:255], v[88:91], v[8:11]\n\t"
        "s_bitcmp1_b32 s6, 1\n\t"
        "s_cbranch_scc1 .Lx50%=\n\t"
        ".Lx51%=:\n\t"
        "v_mfma_f32_16x16x32_bf16 v[12:15], v[220:223], v[88:91], v[12:15]\n\t"
        "ds_read_b128 v[160:163], v185 offset:512\n\t"
        "v_xor_b32 v132, v181, v132\n\t"
        "v_mfma_f32_16x16x32_bf16 v[16:19], v[224:227], v[88:91], v[16:19]\n\t"
        "ds_read_b128 v[100:103], v130 offset:20608\n\t"
        "v_add_u32 v151, s9, v172\n\t"
        "v_mfma_f32_16x16x32_bf16 v[20:23], v[228:231], v[88:91], v[20:23]\n\t"
        "v_max_u32 v151, v151, v132\n\t"
        "v_xor_b32 v133, v181, v133\n\t"
        "v_mfma_f32_16x16x32_bf16 v[24:27], v[232:235], v[88:91], v[24:27]\n\t"
        "ds_read_b128 v[104:107], v151 offset:0\n\t"
        "v_add_u32 v183, s9, v173\n\t"
        "v_mfma_f32_16x16x32_bf16 v[28:31], v[236:239], v[88:91], v[28:31]\n\t"
        "v_max_u32 v183, v183, v133\n\t"
        "v_xor_b32 v134, v181, v134\n\t"
        "v_mfma_f32_16x16x32_bf16 v[32:35], v[240:243], v[88:91], v[32:35]\n\t"
        "s_add_u32 s55, s41, 0\n\t"
        "v_cvt_pk_bf16_f32 v116, v0, v1\n\t"
        "v_mfma_f32_16x16x32_bf16 v[12:15], v[244:247], v[92:95], v[12:15]\n\t"
        "ds_read_b128 v[108:111], v151 offset:64\n\t"
        "v_cvt_pk_bf16_f32 v117, v2, v3\n\t"
        "v_mfma_f32_16x16x32_bf16 v[16:19], v[248:251], v[92:95], v[16:19]\n\t"
        "v_cvt_pk_bf16_f32 v118, v4, v5\n\t"
        "v_cvt_pk_bf16_f32 v119, v6, v7\n\t"
        "v_mfma_f32_16x16x32_bf16 v[20:23], v[252:255], v[92:95], v[20:23]\n\t"
        "v_cvt_pk_bf16_f32 v120, v8, v9\n\t"
        "v_cvt_pk_bf16_f32 v121, v10, v11\n\t"
        "v_mfma_f32_16x16x32_bf16 v[24:27], v[220:223], v[92:95], v[24:27]\n\t"
        "v_permlane16_swap_b32 v116, v118\n\t"
        "v_mfma_f32_16x16x32_bf16 v[28:31], v[224:227], v[92:95], v[28:31]\n\t"
        "v_permlane16_swap_b32 v117, v119\n\t"
        "v_mfma_f32_16x16x32_bf16 v[32:35], v[228:231], v[92:95], v[32:35]\n\t"
        "buffer_store_dwordx4 v[116:119], v144, s[28:31], s55 offen sc1\n\t"
        "v_mfma_f32_16x16x32_bf16 v[36:39], v[232:235], v[92:95], v[36:39]\n\t"
        "ds_write_b64 v146, v[120:121] offset:0\n\t"
        "v_mfma_f32_16x16x32_bf16 v[40:43], v[236:239], v[92:95], v[40:43]\n\t"
        "ds_read_b128 v[112:115], v151 offset:128\n\t"
        "v_add_u32 v151, s9, v174\n\t"
        "v_mfma_f32_16x16x32_bf16 v[44:47], v[240:243], v[92:95], v[44:47]\n\t"
        "ds_read_b128 v[84:87], v183 offset:0\n\t"
        "v_max_u32 v151, v151, v134\n\t"
        "s_waitcnt lgkmcnt(4)\n\t"
        "v_mfma_f32_16x16x32_bf16 v[24:27], v[244:247], v[96:99], v[24:27]\n\t"
        "ds_read_b128 v[88:91], v183 offset:64\n\t"
        "v_xor_b32 v135, v181, v135\n\t"
        "v_mfma_f32_16x16x32_bf16 v[28:31], v[248:251], v[96:99], v[28:31]\n\t"
        "s_add_u32 s55, s41, 43008\n\t"
        "v_cvt_pk_bf16_f32 v116, v12, v13\n\t"
        "v_mfma_f32_16x16x32_bf16 v[32:35], v[252:255], v[96:99], v[32:35]\n\t"
        "v_cvt_pk_bf16_f32 v117, v14, v15\n\t"
        "v_cvt_pk_bf16_f32 v118, v16, v17\n\t"
        "v_mfma_f32_16x16x32_bf16 v[36:39], v[220:223], v[96:99], v[36:39]\n\t"
        "ds_read_b128 v[92:95], v183 offset:128\n\t"
        "v_add_u32 v183, s9, v175\n\t"
        "v_mfma_f32_16x16x32_bf16 v[40:43], v[224:227], v[96:99], v[40:43]\n\t"
        "v_max_u32 v183, v183, v135\n\t"
        "v_xor_b32 v136, v181, v136\n\t"
        "v_mfma_f32_16x16x32_bf16 v[44:47], v[228:231], v[96:99], v[44:47]\n\t"
        "ds_read_b128 v[132:135], v187 offset:4096\n\t"
        "v_cvt_pk_bf16_f32 v119, v18, v19\n\t"
        "v_mfma_f32_16x16x32_bf16 v[36:39], v[244:247], v[100:103], v[36:39]\n\t"
        "ds_read_b128 v[96:99], v151 offset:0\n\t"
        "v_cvt_pk_bf16_f32 v120, v20, v21\n\t"
        "v_mfma_f32_16x16x32_bf16 v[40:43], v[248:251], v[100:103], v[40:43]\n\t"
        "v_cvt_pk_bf16_f32 v121, v22, v23\n\t"
        "v_lshlrev_b32_sdwa v157, 5, v160 dst_sel:DWORD dst_unused:UNUSED_PAD src0_sel:DWORD src1_sel:WORD_0\n\t"
        "v_mfma_f32_16x16x32_bf16 v[44:47], v[252:255], v[100:103], v[44:47]\n\t"
        "v_permlane16_swap_b32 v116, v118\n\t"
        "v_mfma_f32_16x16x32_bf16 v[48:51], a[12:15], v[104:107], 0\n\t"
        "v_permlane16_swap_b32 v117, v119\n\t"
        "v_mfma_f32_16x16x32_bf16 v[52:55], a[16:19], v[104:107], 0\n\t"
        "buffer_store_dwordx4 v[116:119], v144, s[28:31], s55 offen sc1\n\t"
        "v_mfma_f32_16x16x32_bf16 v[56:59], a[20:23], v[104:107], 0\n\t"
        "ds_write_b64 v146, v[120:121] offset:2048\n\t"
        "s_waitcnt lgkmcnt(4)\n\t"
        "v_mfma_f32_16x16x32_bf16 v[48:51], a[48:51], v[108:111], v[48:51]\n\t"
        "ds_read_b128 v[100:103], v151 offset:64\n\t"
        "v_lshlrev_b32_sdwa v158, 5, v160 dst_sel:DWORD dst_unused:UNUSED_PAD src0_sel:DWORD src1_sel:WORD_1\n\t"
        "v_mfma_f32_16x16x32_bf16 v[52:55], a[52:55], v[108:111], v[52:55]\n\t"
        "ds_read_b128 v[104:107], v151 offset:128\n\t"
        "v_add_u32 v151, s9, v176\n\t"
        "v_mfma_f32_16x16x32_bf16 v[56:59], a[56:59], v[108:111], v[56:59]\n\t"
        "v_max_u32 v151, v151, v136\n\t"
        "v_xor_b32 v137, v181, v137\n\t"
        "v_mfma_f32_16x16x32_bf16 v[48:51], a[84:87], v[112:115], v[48:51]\n\t"
        "ds_read_b128 v[108:111], v183 offset:0\n\t"
        "v_or_b32 v157, v184, v157\n\t"
        "v_mfma_f32_16x16x32_bf16 v[52:55], a[88:91], v[112:115], v[52:55]\n\t"
        "ds_read_b64_tr_b16 v[60:61], v157 offset:0\n\t"
        "v_or_b32 v158, v184, v158\n\t"
        "v_mfma_f32_16x16x32_bf16 v[56:59], a[92:95], v[112:115], v[56:59]\n\t"
        "ds_read_b64_tr_b16 v[62:63], v158 offset:0\n\t"
        "v_xor_b32 v159, 32, v157\n\t"
        "v_mfma_f32_16x16x32_bf16 v[48:51], a[120:123], v[84:87], v[48:51]\n\t"
        "ds_read_b64_tr_b16 v[64:65], v159 offset:0\n\t"
        "v_xor_b32 v152, 32, v158\n\t"
        "v_mfma_f32_16x16x32_bf16 v[52:55], a[124:127], v[84:87], v[52:55]\n\t"
        "ds_read_b64_tr_b16 v[66:67], v152 offset:0\n\t"
        "v_perm_b32 v164, v162, v162, v168\n\t"
        "v_mfma_f32_16x16x32_bf16 v[56:59], a[128:131], v[84:87], v[56:59]\n\t"
        "ds_read_b64_tr_b16 v[68:69], v157 offset:64\n\t"
        "v_perm_b32 v165, v162, v162, v169\n\t"
        "v_mfma_f32_16x16x32_bf16 v[48:51], a[156:159], v[88:91], v[48:51]\n\t"
        "ds_read_b64_tr_b16 v[70:71], v158 offset:64\n\t"
        "v_perm_b32 v166, v162, v162, v170\n\t"
        "v_mfma_f32_16x16x32_bf16 v[52:55], a[160:163], v[88:91], v[52:55]\n\t"
        "ds_read_b64_tr_b16 v[72:73], v159 offset:64\n\t"
        "v_perm_b32 v167, v162, v162, v171\n\t"
        "v_mfma_f32_16x16x32_bf16 v[56:59], a[164:167], v[88:91], v[56:59]\n\t"
        "ds_read_b128 v[112:115], v183 offset:64\n\t"
        "v_cmp_ne_u32 vcc, 0, v163\n\t"
        "s_waitcnt lgkmcnt(9)\n\t"
        "v_mfma_f32_16x16x32_bf16 v[48:51], a[192:195], v[92:95], v[48:51]\n\t"
        "ds_read_b64_tr_b16 v[74:75], v152 offset:64\n\t"
        "s_add_u32 s55, s41, 86016\n\t"
        "v_mfma_f32_16x16x32_bf16 v[52:55], a[196:199], v[92:95], v[52:55]\n\t"
        "ds_read_b64_tr_b16 v[76:77], v157 offset:128\n\t"
        "v_cvt_pk_bf16_f32 v116, v24, v25\n\t"
        "v_mfma_f32_16x16x32_bf16 v[56:59], a[200:203], v[92:95], v[56:59]\n\t"
        "ds_read_b128 v[84:87], v183 offset:128\n\t"
        "v_add_u32 v183, s9, v177\n\t"
        "s_waitcnt lgkmcnt(9)\n\t"
        "v_mfma_f32_16x16x32_bf16 v[0:3], v[60:63], v[164:167], 0\n\t"
        "ds_read_b64_tr_b16 v[78:79], v158 offset:128\n\t"
        "v_max_u32 v183, v183, v137\n\t"
        "v_mfma_f32_16x16x32_bf16 v[48:51], a[228:231], v[96:99], v[48:51]\n\t"
        "ds_read_b64_tr_b16 v[80:81], v159 offset:128\n\t"
        "v_xor_b32 v138, v181, v138\n\t"
        "s_waitcnt lgkmcnt(9)\n\t"
        "v_mfma_f32_16x16x32_bf16 v[4:7], v[64:67], v[164:167], 0\n\t"
        "ds_read_b64_tr_b16 v[82:83], v152 offset:128\n\t"
        "v_cvt_pk_bf16_f32 v117, v26, v27\n\t"
        "v_mfma_f32_16x16x32_bf16 v[52:55], a[232:235], v[96:99], v[52:55]\n\t"
        "ds_read_b128 v[88:91], v151 offset:0\n\t"
        "v_cvt_pk_bf16_f32 v118, v28, v29\n\t"
        "s_waitcnt lgkmcnt(9)\n\t"
        "v_mfma_f32_16x16x32_bf16 v[8:11], v[68:71], v[164:167], 0\n\t"
        "ds_read_b128 v[92:95], v151 offset:64\n\t"
        "v_cvt_pk_bf16_f32 v119, v30, v31\n\t"
        "v_mfma_f32_16x16x32_bf16 v[56:59], a[236:239], v[96:99], v[56:59]\n\t"
        "v_cvt_pk_bf16_f32 v120, v32, v33\n\t"
        "v_cvt_pk_bf16_f32 v121, v34, v35\n\t"
        "v_mfma_f32_16x16x32_bf16 v[48:51], v[196:199], v[100:103], v[48:51]\n\t"
        "v_permlane16_swap_b32 v116, v118\n\t"
        "s_waitcnt lgkmcnt(7)\n\t"
        "v_mfma_f32_16x16x32_bf16 v[12:15], v[72:75], v[164:167], 0\n\t"
        "v_permlane16_swap_b32 v117, v119\n\t"
        "v_mfma_f32_16x16x32_bf16 v[52:55], v[200:203], v[100:103], v[52:55]\n\t"
        "buffer_store_dwordx4 v[116:119], v144, s[28:31], s55 offen sc1\n\t"
        "v_mfma_f32_16x16x32_bf16 v[56:59], v[204:207], v[100:103], v[56:59]\n\t"
        "ds_write_b64 v146, v[120:121] offset:4096\n\t"
        "s_waitcnt lgkmcnt(5)\n\t"
        "v_mfma_f32_16x16x32_bf16 v[16:19], v[76:79], v[164:167], 0\n\t"
        "ds_read_b128 v[96:99], v151 offset:128\n\t"
        "v_add_u32 v151, s9, v178\n\t"
        "v_mfma_f32_16x16x32_bf16 v[48:51], v[232:235], v[104:107], v[48:51]\n\t"
        "ds_read_b128 v[100:103], v183 offset:0\n\t"
        "v_max_u32 v151, v151, v138\n\t"
        "s_waitcnt lgkmcnt(5)\n\t"
        "v_mfma_f32_16x16x32_bf16 v[20:23], v[80:83], v[164:167], 0\n\t"
        "v_xor_b32 v139, v181, v139\n\t"
        "s_cbranch_vccz .Lx52%=\n\t"
        "v_lshlrev_b32_sdwa v157, 5, v161 dst_sel:DWORD dst_unused:UNUSED_PAD src0_sel:DWORD src1_sel:WORD_0\n\t"
        "v_lshlrev_b32_sdwa v158, 5, v161 dst_sel:DWORD dst_unused:UNUSED_PAD src0_sel:DWORD src1_sel:WORD_1\n\t"
        "v_or_b32 v157, v184, v157\n\t"
        "v_or_b32 v158, v184, v158\n\t"
        "v_xor_b32 v159, 32, v157\n\t"
        "v_xor_b32 v152, 32, v158\n\t"
        "ds_read_b64_tr_b16 v[60:61], v157 offset:0\n\t"
        "ds_read_b64_tr_b16 v[62:63], v158 offset:0\n\t"
        "ds_read_b64_tr_b16 v[64:65], v159 offset:0\n\t"
        "ds_read_b64_tr_b16 v[66:67], v152 offset:0\n\t"
        "ds_read_b64_tr_b16 v[68:69], v157 offset:64\n\t"
        "ds_read_b64_tr_b16 v[70:71], v158 offset:64\n\t"
        "ds_read_b64_tr_b16 v[72:73], v159 offset:64\n\t"
        "ds_read_b64_tr_b16 v[74:75], v152 offset:64\n\t"
        "ds_read_b64_tr_b16 v[76:77], v157 offset:128\n\t"
        "ds_read_b64_tr_b16 v[78:79], v158 offset:128\n\t"
        "ds_read_b64_tr_b16 v[80:81], v159 offset:128\n\t"
        "ds_read_b64_tr_b16 v[82:83], v152 offset:128\n\t"
        "v_perm_b32 v164, v163, v163, v168\n\t"
        "v_perm_b32 v165, v163, v163, v169\n\t"
        "v_perm_b32 v166, v163, v163, v170\n\t"
        "v_perm_b32 v167, v163, v163, v171\n\t"
        "s_waitcnt lgkmcnt(10)\n\t"
        "s_nop 0\n\t"
        "v_mfma_f32_16x16x32_bf16 v[0:3], v[60:63], v[164:167], v[0:3]\n\t"
        "s_waitcnt lgkmcnt(8)\n\t"
        "v_mfma_f32_16x16x32_bf16 v[4:7], v[64:67], v[164:167], v[4:7]\n\t"
        "s_waitcnt lgkmcnt(6)\n\t"
        "v_mfma_f32_16x16x32_bf16 v[8:11], v[68:71], v[164:167], v[8:11]\n\t"
        "s_waitcnt lgkmcnt(4)\n\t"
        "v_mfma_f32_16x16x32_bf16 v[12:15], v[72:75], v[164:167], v[12:15]\n\t"
        "s_waitcnt lgkmcnt(2)\n\t"
        "v_mfma_f32_16x16x32_bf16 v[16:19], v[76:79], v[164:167], v[16:19]\n\t"
        "s_waitcnt lgkmcnt(0)\n\t"
        "v_mfma_f32_16x16x32_bf16 v[20:23], v[80:83], v[164:167], v[20:23]\n\t"
        "s_nop 4\n\t"
        ".Lx52%=:\n\t"
        "v_mfma_f32_16x16x32_bf16 v[52:55], v[236:239], v[104:107], v[52:55]\n\t"
        "v_cvt_pk_bf16_f32 v0, v0, v1\n\t"
        "v_cvt_pk_bf16_f32 v1, v2, v3\n\t"
        "v_mfma_f32_16x16x32_bf16 v[56:59], v[240:243], v[104:107], v[56:59]\n\t"
        "ds_write_b64 v186, v[0:1] offset:24576\n\t"
        "v_mfma_f32_16x16x32_bf16 v[48:51], a[0:3], v[108:111], v[48:51]\n\t"
        "ds_read_b128 v[104:107], v183 offset:64\n\t"
        "v_cvt_pk_bf16_f32 v4, v4, v5\n\t"
        "v_mfma_f32_16x16x32_bf16 v[52:55], a[4:7], v[108:111], v[52:55]\n\t"
        "v_cvt_pk_bf16_f32 v5, v6, v7\n\t"
        "v_cvt_pk_bf16_f32 v8, v8, v9\n\t"
        "v_mfma_f32_16x16x32_bf16 v[56:59], a[8:11], v[108:111], v[56:59]\n\t"
        "ds_write_b64 v154, v[4:5] offset:24576\n\t"
        "v_mfma_f32_16x16x32_bf16 v[48:51], a[36:39], v[112:115], v[48:51]\n\t"
        "ds_read_b128 v[108:111], v183 offset:128\n\t"
        "v_add_u32 v183, s9, v179\n\t"
        "v_mfma_f32_16x16x32_bf16 v[52:55], a[40:43], v[112:115], v[52:55]\n\t"
        "v_max_u32 v183, v183, v139\n\t"
        "v_xor_b32 v140, v181, v140\n\t"
        "v_mfma_f32_16x16x32_bf16 v[56:59], a[44:47], v[112:115], v[56:59]\n\t"
        "ds_read_b128 v[136:139], v187 offset:4112\n\t"
        "v_cvt_pk_bf16_f32 v9, v10, v11\n\t"
        "v_mfma_f32_16x16x32_bf16 v[48:51], a[72:75], v[84:87], v[48:51]\n\t"
        "ds_write_b64 v186, v[8:9] offset:24640\n\t"
        "v_mfma_f32_16x16x32_bf16 v[52:55], a[76:79], v[84:87], v[52:55]\n\t"
        "ds_read_b128 v[112:115], v151 offset:0\n\t"
        "v_cvt_pk_bf16_f32 v12, v12, v13\n\t"
        "v_mfma_f32_16x16x32_bf16 v[56:59], a[80:83], v[84:87], v[56:59]\n\t"
        "v_cvt_pk_bf16_f32 v13, v14, v15\n\t"
        "v_cvt_pk_bf16_f32 v16, v16, v17\n\t"
        "s_waitcnt lgkmcnt(7)\n\t"
        "v_mfma_f32_16x16x32_bf16 v[48:51], a[108:111], v[88:91], v[48:51]\n\t"
        "ds_write_b64 v154, v[12:13] offset:24640\n\t"
        "v_mfma_f32_16x16x32_bf16 v[52:55], a[112:115], v[88:91], v[52:55]\n\t"
        "ds_read_b128 v[84:87], v151 offset:64\n\t"
        "v_cvt_pk_bf16_f32 v17, v18, v19\n\t"
        "v_mfma_f32_16x16x32_bf16 v[56:59], a[116:119], v[88:91], v[56:59]\n\t"
        "ds_write_b64 v186, v[16:17] offset:24704\n\t"
        "v_mfma_f32_16x16x32_bf16 v[48:51], a[144:147], v[92:95], v[48:51]\n\t"
        "ds_read_b128 v[88:91], v151 offset:128\n\t"
        "v_add_u32 v151, s9, v180\n\t"
        "v_mfma_f32_16x16x32_bf16 v[52:55], a[148:151], v[92:95], v[52:55]\n\t"
        "v_max_u32 v151, v151, v140\n\t"
        "v_cvt_pk_bf16_f32 v20, v20, v21\n\t"
        "v_mfma_f32_16x16x32_bf16 v[56:59], a[152:155], v[92:95], v[56:59]\n\t"
        "ds_read_b32 v140, v187 offset:4128\n\t"
        "v_cvt_pk_bf16_f32 v21, v22, v23\n\t"
        "v_mfma_f32_16x16x32_bf16 v[48:51], a[180:183], v[96:99], v[48:51]\n\t"
        "ds_write_b64 v154, v[20:21] offset:24704\n\t"
        "v_mfma_f32_16x16x32_bf16 v[52:55], a[184:187], v[96:99], v[52:55]\n\t"
        "ds_read_b128 v[92:95], v183 offset:0\n\t"
        "s_add_u32 s55, s41, 129024\n\t"
        "v_mfma_f32_16x16x32_bf16 v[56:59], a[188:191], v[96:99], v[56:59]\n\t"
        "v_cvt_pk_bf16_f32 v116, v36, v37\n\t"
        "v_cvt_pk_bf16_f32 v117, v38, v39\n\t"
        "v_mfma_f32_16x16x32_bf16 v[48:51], a[216:219], v[100:103], v[48:51]\n\t"
        "ds_read_b128 v[96:99], v183 offset:64\n\t"
        "v_cvt_pk_bf16_f32 v118, v40, v41\n\t"
        "v_mfma_f32_16x16x32_bf16 v[52:55], a[220:223], v[100:103], v[52:55]\n\t"
        "v_cvt_pk_bf16_f32 v119, v42, v43\n\t"
        "v_cvt_pk_bf16_f32 v120, v44, v45\n\t"
        "v_mfma_f32_16x16x32_bf16 v[56:59], a[224:227], v[100:103], v[56:59]\n\t"
        "v_cvt_pk_bf16_f32 v121, v46, v47\n\t"
        "s_waitcnt lgkmcnt(6)\n\t"
        "v_mfma_f32_16x16x32_bf16 v[48:51], a[252:255], v[104:107], v[48:51]\n\t"
        "v_permlane16_swap_b32 v116, v118\n\t"
        "v_mfma_f32_16x16x32_bf16 v[52:55], v[188:191], v[104:107], v[52:55]\n\t"
        "v_permlane16_swap_b32 v117, v119\n\t"
        "v_mfma_f32_16x16x32_bf16 v[56:59], v[192:195], v[104:107], v[56:59]\n\t"
        "buffer_store_dwordx4 v[116:119], v144, s[28:31], s55 offen sc1\n\t"
        "v_mfma_f32_16x16x32_bf16 v[48:51], v[220:223], v[108:111], v[48:51]\n\t"
        "ds_write_b64 v146, v[120:121] offset:6144\n\t"
        "v_mfma_f32_16x16x32_bf16 v[52:55], v[224:227], v[108:111], v[52:55]\n\t"
        "s_cmp_gt_u32 s61, 48\n\t"
        "s_cbranch_scc1 .Lx54%=\n\t"
        ".Lx55%=:\n\t"
        "v_mfma_f32_16x16x32_bf16 v[56:59], v[228:231], v[108:111], v[56:59]\n\t"
        "ds_read_b128 v[100:103], v183 offset:128\n\t"
        "v_mfma_f32_16x16x32_bf16 v[48:51], a[24:27], v[112:115], v[48:51]\n\t"
        "ds_read_b128 v[104:107], v151 offset:0\n\t"
        "v_mfma_f32_16x16x32_bf16 v[52:55], a[28:31], v[112:115], v[52:55]\n\t"
        "ds_read_b128 v[108:111], v151 offset:64\n\t"
        "v_mfma_f32_16x16x32_bf16 v[56:59], a[32:35], v[112:115], v[56:59]\n\t"
        "v_mfma_f32_16x16x32_bf16 v[48:51], a[60:63], v[84:87], v[48:51]\n\t"
        "s_waitcnt lgkmcnt(3)\n\t"
        "s_barrier\n\t"
        "v_mfma_f32_16x16x32_bf16 v[52:55], a[64:67], v[84:87], v[52:55]\n\t"
        "s_bitcmp1_b32 s6, 2\n\t"
        "s_cbranch_scc0 .Lx56%=\n\t"
        "ds_read_b128 v[132:135], v187 offset:4096\n\t"
        "ds_read_b128 v[136:139], v187 offset:4112\n\t"
        "ds_read_b32 v140, v187 offset:4128\n\t"
        "s_waitcnt lgkmcnt(0)\n\t"
        ".Lx56%=:\n\t"
        "v_mfma_f32_16x16x32_bf16 v[56:59], a[68:71], v[84:87], v[56:59]\n\t"
        "ds_read_b128 v[124:127], v182 offset:16\n\t"
        "v_xor_b32 v132, v181, v132\n\t"
        "v_mfma_f32_16x16x32_bf16 v[48:51], a[96:99], v[88:91], v[48:51]\n\t"
        "ds_read_b128 v[112:115], v151 offset:128\n\t"
        "v_readlane_b32 s9, v155, 48\n\t"
        "v_mfma_f32_16x16x32_bf16 v[52:55], a[100:103], v[88:91], v[52:55]\n\t"
        "s_nop 0\n\t"
        "v_add_u32 v183, s9, v172\n\t"
        "v_max_u32 v183, v183, v132\n\t"
        "v_mfma_f32_16x16x32_bf16 v[56:59], a[104:107], v[88:91], v[56:59]\n\t"
        "ds_read_b128 v[84:87], v183 offset:0\n\t"
        "v_xor_b32 v133, v181, v133\n\t"
        "v_mfma_f32_16x16x32_bf16 v[48:51], a[132:135], v[92:95], v[48:51]\n\t"
        "ds_read_b128 v[88:91], v183 offset:64\n\t"
        "v_add_u32 v151, s9, v173\n\t"
        "v_mfma_f32_16x16x32_bf16 v[52:55], a[136:139], v[92:95], v[52:55]\n\t"
        "v_max_u32 v151, v151, v133\n\t"
        "v_xor_b32 v134, v181, v134\n\t"
        "v_mfma_f32_16x16x32_bf16 v[56:59], a[140:143], v[92:95], v[56:59]\n\t"
        "v_mfma_f32_16x16x32_bf16 v[48:51], a[168:171], v[96:99], v[48:51]\n\t"
        "ds_read_b128 v[92:95], v183 offset:128\n\t"
        "v_add_u32 v183, s9, v174\n\t"
        "v_mfma_f32_16x16x32_bf16 v[52:55], a[172:175], v[96:99], v[52:55]\n\t"
        "v_max_u32 v183, v183, v134\n\t"
        "v_xor_b32 v135, v181, v135\n\t"
        "v_mfma_f32_16x16x32_bf16 v[56:59], a[176:179], v[96:99], v[56:59]\n\t"
        ".LpM2%=:\n\t"
        "s_cbranch_execz .LrM%=\n\t"
        "s_waitcnt lgkmcnt(4)\n\t"
        "v_or3_b32 v124, v124, v125, v126\n\t"
        "v_or_b32 v124, v124, v127\n\t"
        "v_mfma_f32_16x16x32_bf16 v[48:51], a[204:207], v[100:103], v[48:51]\n\t"
        "ds_read_b128 v[96:99], v151 offset:0\n\t"
        "v_readfirstlane_b32 s8, v124\n\t"
        "v_mfma_f32_16x16x32_bf16 v[52:55], a[208:211], v[100:103], v[52:55]\n\t"
        "ds_read_b128 v[124:127], v147 offset:0\n\t"
        "s_or_b32 s6, s6, s8\n\t"
        "v_mfma_f32_16x16x32_bf16 v[56:59], a[212:215], v[100:103], v[56:59]\n\t"
        "v_mfma_f32_16x16x32_bf16 v[48:51], a[240:243], v[104:107], v[48:51]\n\t"
        "ds_read_b128 v[100:103], v151 offset:64\n\t"
        "v_mfma_f32_16x16x32_bf16 v[52:55], a[244:247], v[104:107], v[52:55]\n\t"
        "v_mfma_f32_16x16x32_bf16 v[56:59], a[248:251], v[104:107], v[56:59]\n\t"
        "v_mfma_f32_16x16x32_bf16 v[48:51], v[208:211], v[108:111], v[48:51]\n\t"
        "ds_read_b128 v[104:107], v151 offset:128\n\t"
        "v_add_u32 v151, s9, v175\n\t"
        "v_mfma_f32_16x16x32_bf16 v[52:55], v[212:215], v[108:111], v[52:55]\n\t"
        "v_max_u32 v151, v151, v135\n\t"
        "v_xor_b32 v136, v181, v136\n\t"
        "v_mfma_f32_16x16x32_bf16 v[56:59], v[216:219], v[108:111], v[56:59]\n\t"
        "s_waitcnt lgkmcnt(4)\n\t"
        "v_mfma_f32_16x16x32_bf16 v[48:51], v[244:247], v[112:115], v[48:51]\n\t"
        "ds_read_b128 v[108:111], v183 offset:0\n\t"
        "v_mfma_f32_16x16x32_bf16 v[52:55], v[248:251], v[112:115], v[52:55]\n\t"
        "s_waitcnt lgkmcnt(3)\n\t"
        "buffer_store_dwordx4 v[124:127], v145, s[28:31], s50 offen sc1\n\t"
        "v_mfma_f32_16x16x32_bf16 v[56:59], v[252:255], v[112:115], v[56:59]\n\t"
        "ds_read_b128 v[124:127], v147 offset:4096\n\t"
        "s_add_u32 s50, s50, 86016\n\t"
        "v_mfma_f32_16x16x32_bf16 v[72:75], a[12:15], v[84:87], 0\n\t"
        "ds_read_b128 v[112:115], v183 offset:64\n\t"
        "v_mfma_f32_16x16x32_bf16 v[76:79], a[16:19], v[84:87], 0\n\t"
        "v_mfma_f32_16x16x32_bf16 v[80:83], a[20:23], v[84:87], 0\n\t"
        "v_mfma_f32_16x16x32_bf16 v[72:75], a[48:51], v[88:91], v[72:75]\n\t"
        "ds_read_b128 v[84:87], v183 offset:128\n\t"
        "v_add_u32 v183, s9, v176\n\t"
        "v_mfma_f32_16x16x32_bf16 v[76:79], a[52:55], v[88:91], v[76:79]\n\t"
        "v_max_u32 v183, v183, v136\n\t"
        "v_xor_b32 v137, v181, v137\n\t"
        "v_mfma_f32_16x16x32_bf16 v[80:83], a[56:59], v[88:91], v[80:83]\n\t"
        "v_mfma_f32_16x16x32_bf16 v[72:75], a[84:87], v[92:95], v[72:75]\n\t"
        "ds_read_b128 v[88:91], v151 offset:0\n\t"
        "v_mfma_f32_16x16x32_bf16 v[76:79], a[88:91], v[92:95], v[76:79]\n\t"
        "v_mfma_f32_16x16x32_bf16 v[80:83], a[92:95], v[92:95], v[80:83]\n\t"
        "v_mfma_f32_16x16x32_bf16 v[72:75], a[120:123], v[96:99], v[72:75]\n\t"
        "ds_read_b128 v[92:95], v151 offset:64\n\t"
        "v_mfma_f32_16x16x32_bf16 v[76:79], a[124:127], v[96:99], v[76:79]\n\t"
        "v_mfma_f32_16x16x32_bf16 v[80:83], a[128:131], v[96:99], v[80:83]\n\t"
        "s_waitcnt lgkmcnt(3)\n\t"
        "v_mfma_f32_16x16x32_bf16 v[72:75], a[156:159], v[100:103], v[72:75]\n\t"
        "buffer_store_dwordx4 v[124:127], v145, s[28:31], s50 offen sc1\n\t"
        "v_mfma_f32_16x16x32_bf16 v[76:79], a[160:163], v[100:103], v[76:79]\n\t"
        "ds_read_b128 v[96:99], v151 offset:128\n\t"
        "v_add_u32 v151, s9, v177\n\t"
        "v_mfma_f32_16x16x32_bf16 v[80:83], a[164:167], v[100:103], v[80:83]\n\t"
        "v_max_u32 v151, v151, v137\n\t"
        "v_xor_b32 v138, v181, v138\n\t"
        "v_mfma_f32_16x16x32_bf16 v[72:75], a[192:195], v[104:107], v[72:75]\n\t"
        "ds_read_b128 v[100:103], v183 offset:0\n\t"
        "v_readlane_b32 s55, v155, 49\n\t"
        "v_mfma_f32_16x16x32_bf16 v[76:79], a[196:199], v[104:107], v[76:79]\n\t"
        "v_cvt_pk_bf16_f32 v116, v48, v49\n\t"
        "v_cvt_pk_bf16_f32 v117, v50, v51\n\t"
        "v_mfma_f32_16x16x32_bf16 v[80:83], a[200:203], v[104:107], v[80:83]\n\t"
        "v_cvt_pk_bf16_f32 v118, v52, v53\n\t"
        "v_cvt_pk_bf16_f32 v119, v54, v55\n\t"
        "v_mfma_f32_16x16x32_bf16 v[72:75], a[228:231], v[108:111], v[72:75]\n\t"
        "ds_read_b128 v[104:107], v183 offset:64\n\t"
        "v_cvt_pk_bf16_f32 v120, v56, v57\n\t"
        "v_mfma_f32_16x16x32_bf16 v[76:79], a[232:235], v[108:111], v[76:79]\n\t"
        "v_cvt_pk_bf16_f32 v121, v58, v59\n\t"
        "v_mfma_f32_16x16x32_bf16 v[80:83], a[236:239], v[108:111], v[80:83]\n\t"
        "v_permlane16_swap_b32 v116, v118\n\t"
        "v_mfma_f32_16x16x32_bf16 v[72:75], v[196:199], v[112:115], v[72:75]\n\t"
        "v_permlane16_swap_b32 v117, v119\n\t"
        "v_mfma_f32_16x16x32_bf16 v[76:79], v[200:203], v[112:115], v[76:79]\n\t"
        "ds_read_b128 v[108:111], v183 offset:128\n\t"
        "v_add_u32 v183, s9, v178\n\t"
        "v_mfma_f32_16x16x32_bf16 v[80:83], v[204:207], v[112:115], v[80:83]\n\t"
        "v_max_u32 v183, v183, v138\n\t"
        "v_xor_b32 v139, v181, v139\n\t"
        "s_waitcnt lgkmcnt(3)\n\t"
        "v_mfma_f32_16x16x32_bf16 v[72:75], v[232:235], v[84:87], v[72:75]\n\t"
        "ds_read_b128 v[112:115], v151 offset:0\n\t"
        "v_mfma_f32_16x16x32_bf16 v[76:79], v[236:239], v[84:87], v[76:79]\n\t"
        "v_mfma_f32_16x16x32_bf16 v[80:83], v[240:243], v[84:87], v[80:83]\n\t"
        "buffer_store_dwordx4 v[116:119], v148, s[28:31], s55 offen sc1\n\t"
        "v_mfma_f32_16x16x32_bf16 v[72:75], a[0:3], v[88:91], v[72:75]\n\t"
        "ds_write_b64 v146, v[120:121] offset:8192\n\t"
        "v_mfma_f32_16x16x32_bf16 v[76:79], a[4:7], v[88:91], v[76:79]\n\t"
        "ds_read_b128 v[84:87], v151 offset:64\n\t"
        "v_mfma_f32_16x16x32_bf16 v[80:83], a[8:11], v[88:91], v[80:83]\n\t"
        "v_mfma_f32_16x16x32_bf16 v[72:75], a[36:39], v[92:95], v[72:75]\n\t"
        "ds_read_b128 v[88:91], v151 offset:128\n\t"
        "v_add_u32 v151, s9, v179\n\t"
        "v_mfma_f32_16x16x32_bf16 v[76:79], a[40:43], v[92:95], v[76:79]\n\t"
        "v_max_u32 v151, v151, v139\n\t"
        "v_xor_b32 v140, v181, v140\n\t"
        "v_mfma_f32_16x16x32_bf16 v[80:83], a[44:47], v[92:95], v[80:83]\n\t"
        "v_mfma_f32_16x16x32_bf16 v[72:75], a[72:75], v[96:99], v[72:75]\n\t"
        "ds_read_b128 v[92:95], v183 offset:0\n\t"
        "v_mfma_f32_16x16x32_bf16 v[76:79], a[76:79], v[96:99], v[76:79]\n\t"
        "v_mfma_f32_16x16x32_bf16 v[80:83], a[80:83], v[96:99], v[80:83]\n\t"
        "s_waitcnt lgkmcnt(4)\n\t"
        "v_mfma_f32_16x16x32_bf16 v[72:75], a[108:111], v[100:103], v[72:75]\n\t"
        "ds_read_b128 v[96:99], v183 offset:64\n\t"
        "v_mfma_f32_16x16x32_bf16 v[76:79], a[112:115], v[100:103], v[76:79]\n\t"
        "v_mfma_f32_16x16x32_bf16 v[80:83], a[116:119], v[100:103], v[80:83]\n\t"
        "ds_read_b128 v[100:103], v183 offset:128\n\t"
        "v_add_u32 v183, s9, v180\n\t"
        "v_mfma_f32_16x16x32_bf16 v[72:75], a[144:147], v[104:107], v[72:75]\n\t"
        "v_max_u32 v183, v183, v140\n\t"
        "v_mfma_f32_16x16x32_bf16 v[76:79], a[148:151], v[104:107], v[76:79]\n\t"
        "v_mfma_f32_16x16x32_bf16 v[80:83], a[152:155], v[104:107], v[80:83]\n\t"
        "ds_read_b128 v[104:107], v151 offset:0\n\t"
        "v_mfma_f32_16x16x32_bf16 v[72:75], a[180:183], v[108:111], v[72:75]\n\t"
        "v_mfma_f32_16x16x32_bf16 v[76:79], a[184:187], v[108:111], v[76:79]\n\t"
        "v_mfma_f32_16x16x32_bf16 v[80:83], a[188:191], v[108:111], v[80:83]\n\t"
        "ds_read_b128 v[108:111], v151 offset:64\n\t"
        "v_mfma_f32_16x16x32_bf16 v[72:75], a[216:219], v[112:115], v[72:75]\n\t"
        "v_mfma_f32_16x16x32_bf16 v[76:79], a[220:223], v[112:115], v[76:79]\n\t"
        "v_mfma_f32_16x16x32_bf16 v[80:83], a[224:227], v[112:115], v[80:83]\n\t"
        "ds_read_b128 v[112:115], v151 offset:128\n\t"
        "s_waitcnt lgkmcnt(4)\n\t"
        "v_mfma_f32_16x16x32_bf16 v[72:75], a[252:255], v[84:87], v[72:75]\n\t"
        "v_mfma_f32_16x16x32_bf16 v[76:79], v[188:191], v[84:87], v[76:79]\n\t"
        "v_mfma_f32_16x16x32_bf16 v[80:83], v[192:195], v[84:87], v[80:83]\n\t"
        "ds_read_b128 v[84:87], v183 offset:0\n\t"
        "v_mfma_f32_16x16x32_bf16 v[72:75], v[220:223], v[88:91], v[72:75]\n\t"
        "v_mfma_f32_16x16x32_bf16 v[76:79], v[224:227], v[88:91], v[76:79]\n\t"
        "v_mfma_f32_16x16x32_bf16 v[80:83], v[228:231], v[88:91], v[80:83]\n\t"
        "ds_read_b128 v[88:91], v183 offset:64\n\t"
        "v_mfma_f32_16x16x32_bf16 v[72:75], a[24:27], v[92:95], v[72:75]\n\t"
        "v_mfma_f32_16x16x32_bf16 v[76:79], a[28:31], v[92:95], v[76:79]\n\t"
        "v_mfma_f32_16x16x32_bf16 v[80:83], a[32:35], v[92:95], v[80:83]\n\t"
        "ds_read_b128 v[92:95], v183 offset:128\n\t"
        "v_mfma_f32_16x16x32_bf16 v[80:83], a[68:71], v[96:99], v[80:83]\n\t"
        "s_waitcnt lgkmcnt(3)\n\t"
        "v_mfma_f32_16x16x32_bf16 v[80:83], a[104:107], v[100:103], v[80:83]\n\t"
        "v_mfma_f32_16x16x32_bf16 v[80:83], a[140:143], v[104:107], v[80:83]\n\t"
        "v_mfma_f32_16x16x32_bf16 v[80:83], a[176:179], v[108:111], v[80:83]\n\t"
        "v_mfma_f32_16x16x32_bf16 v[80:83], a[212:215], v[112:115], v[80:83]\n\t"
        "s_waitcnt lgkmcnt(2)\n\t"
        "v_mfma_f32_16x16x32_bf16 v[80:83], a[248:251], v[84:87], v[80:83]\n\t"
        "s_waitcnt lgkmcnt(1)\n\t"
        "v_mfma_f32_16x16x32_bf16 v[80:83], v[216:219], v[88:91], v[80:83]\n\t"
        "s_waitcnt lgkmcnt(0)\n\t"
        "v_mfma_f32_16x16x32_bf16 v[80:83], v[252:255], v[92:95], v[80:83]\n\t"
        "s_nop 2\n\t"
        "v_mfma_f32_16x16x32_bf16 v[72:75], a[60:63], v[96:99], v[72:75]\n\t"
        "s_nop 2\n\t"
        "v_mfma_f32_16x16x32_bf16 v[72:75], a[96:99], v[100:103], v[72:75]\n\t"
        "v_cvt_pk_bf16_f32 v120, v80, v81\n\t"
        "v_cvt_pk_bf16_f32 v121, v82, v83\n\t"
        "v_mfma_f32_16x16x32_bf16 v[72:75], a[132:135], v[104:107], v[72:75]\n\t"
        "ds_write_b64 v146, v[120:121] offset:10240\n\t"
        "v_mfma_f32_16x16x32_bf16 v[72:75], a[168:171], v[108:111], v[72:75]\n\t"
        "v_mfma_f32_16x16x32_bf16 v[72:75], a[204:207], v[112:115], v[72:75]\n\t"
        "v_mfma_f32_16x16x32_bf16 v[72:75], a[240:243], v[84:87], v[72:75]\n\t"
        "s_waitcnt lgkmcnt(0)\n\t"
        "s_barrier\n\t"
        "v_mfma_f32_16x16x32_bf16 v[72:75], v[208:211], v[88:91], v[72:75]\n\t"
        "ds_read_b128 v[124:127], v147 offset:8192\n\t"
        "v_readlane_b32 s55, v155, 51\n\t"
        "v_mfma_f32_16x16x32_bf16 v[72:75], v[244:247], v[92:95], v[72:75]\n\t"
        "s_nop 1\n\t"
        "v_mfma_f32_16x16x32_bf16 v[76:79], a[64:67], v[96:99], v[76:79]\n\t"
        "s_nop 1\n\t"
        "v_mfma_f32_16x16x32_bf16 v[76:79], a[100:103], v[100:103], v[76:79]\n\t"
        "v_mfma_f32_16x16x32_bf16 v[76:79], a[136:139], v[104:107], v[76:79]\n\t"
        "v_mfma_f32_16x16x32_bf16 v[76:79], a[172:175], v[108:111], v[76:79]\n\t"
        "v_mfma_f32_16x16x32_bf16 v[76:79], a[208:211], v[112:115], v[76:79]\n\t"
        "v_cvt_pk_bf16_f32 v116, v72, v73\n\t"
        "v_cvt_pk_bf16_f32 v117, v74, v75\n\t"
        "v_mfma_f32_16x16x32_bf16 v[76:79], a[244:247], v[84:87], v[76:79]\n\t"
        "s_waitcnt lgkmcnt(0)\n\t"
        "buffer_store_dwordx4 v[124:127], v150, s[28:31], s52 offen sc1\n\t"
        "v_mfma_f32_16x16x32_bf16 v[76:79], v[212:215], v[88:91], v[76:79]\n\t"
        "v_mfma_f32_16x16x32_bf16 v[76:79], v[248:251], v[92:95], v[76:79]\n\t"
        "s_nop 7\n\t"
        "v_cvt_pk_bf16_f32 v118, v76, v77\n\t"
        "v_cvt_pk_bf16_f32 v119, v78, v79\n\t"
        "s_nop 0\n\t"
        "v_permlane16_swap_b32 v116, v118\n\t"
        "v_permlane16_swap_b32 v117, v119\n\t"
        "buffer_store_dwordx4 v[116:119], v148, s[28:31], s55 offen sc1\n\t"
        "s_andn2_b32 s6, s6, 6\n\t"
        "s_cbranch_scc1 99f\n\t"
        "s_endpgm\n\t"
        "20:\n\t"
        "ds_bpermute_b32 v64, v65, v155 offset:172\n\t"
        "s_add_u32 m0, s35, 4096\n\t"
        "s_add_u32 s43, s34, 43008\n\t"
        "buffer_load_dwordx4 v143, s[16:19], s43 offen  lds\n\t"
        "ds_bpermute_b32 v78, v81, v155 offset:148\n\t"
        "ds_bpermute_b32 v79, v81, v155 offset:160\n\t"
        "v_readlane_b32 s71, v155, 26\n\t"
        "v_readlane_b32 s10, v155, 29\n\t"
        "s_nop 0\n\t"
        "v_mul_u32_u24 v73, s71, v66\n\t"
        "v_lshrrev_b32 v73, 11, v73\n\t"
        "v_lshlrev_b32 v66, 4, v66\n\t"
        "v_mad_u32_u24 v73, v73, s10, v66\n\t"
        "s_waitcnt lgkmcnt(2)\n\t"
        "v_add_u32 v64, v64, v73\n\t"
        "s_add_u32 m0, s35, 20480\n\t"
        "s_add_u32 s43, s34, 215040\n\t"
        "buffer_load_dwordx4 v143, s[16:19], s43 offen  lds\n\t"
        "v_readlane_b32 s69, v155, 34\n\t"
        "v_readlane_b32 s70, v155, 35\n\t"
        "s_nop 0\n\t"
        "v_mad_u32_u24 v70, v52, s69, v70\n\t"
        "v_mad_u32_u24 v70, v158, s70, v70\n\t"
        "s_mul_i32 s71, s69, s68\n\t"
        "s_add_u32 s71, s71, 157728\n\t"
        "v_mad_u32_u24 v67, v82, s70, v67\n\t"
        "v_add_u32 v67, s71, v67\n\t"
        "v_readlane_b32 s11, v155, 30\n\t"
        "s_add_u32 m0, s37, -24576\n\t"
        "s_add_u32 s36, s36, 6144\n\t"
        "buffer_load_dwordx4 v140, s[24:27], s36 offen offset:0 lds\n\t"
        "s_movk_i32 s10, 223\n\t"
        "v_mov_b32 v75, 0x7ff00000\n\t"
        "v_readlane_b32 s12, v155, 24\n\t"
        "v_readlane_b32 s13, v155, 25\n\t"
        "s_nop 0\n\t"
        "s_bfe_u32 s70, s12, 524288\n\t"
        "s_bfe_u32 s71, s13, 524288\n\t"
        "v_cmp_gt_u32 vcc, s71, v71\n\t"
        "v_add3_u32 v73, v71, s70, -2\n\t"
        "v_med3_i32 v73, v73, 0, s10\n\t"
        "buffer_load_dwordx4 v141, s[24:27], s36 offen offset:1024 lds\n\t"
        "v_cndmask_b32 v74, v75, v72, vcc\n\t"
        "v_mad_u32_u24 v134, v73, s11, v74\n\t"
        "s_bfe_u32 s70, s12, 524296\n\t"
        "s_bfe_u32 s71, s13, 524296\n\t"
        "v_cmp_gt_u32 vcc, s71, v71\n\t"
        "v_add3_u32 v73, v71, s70, -2\n\t"
        "v_med3_i32 v73, v73, 0, s10\n\t"
        "v_cndmask_b32 v74, v75, v72, vcc\n\t"
        "v_mad_u32_u24 v135, v73, s11, v74\n\t"
        "buffer_load_dwordx4 v142, s[24:27], s36 offen offset:2048 lds\n\t"
        "s_bfe_u32 s70, s12, 524304\n\t"
        "s_bfe_u32 s71, s13, 524304\n\t"
        "v_cmp_gt_u32 vcc, s71, v71\n\t"
        "v_add3_u32 v73, v71, s70, -2\n\t"
        "v_med3_i32 v73, v73, 0, s10\n\t"
        "v_cndmask_b32 v74, v75, v72, vcc\n\t"
        "v_mad_u32_u24 v136, v73, s11, v74\n\t"
        "s_bfe_u32 s70, s12, 524312\n\t"
        "s_bfe_u32 s71, s13, 524312\n\t"
        "v_cmp_gt_u32 vcc, s71, v71\n\t"
        "v_add3_u32 v73, v71, s70, -2\n\t"
        "v_med3_i32 v73, v73, 0, s10\n\t"
        "v_cndmask_b32 v74, v75, v72, vcc\n\t"
        "v_mad_u32_u24 v137, v73, s11, v74\n\t"
        "v_readlane_b32 s44, v155, 32\n\t"
        "v_readlane_b32 s45, v155, 33\n\t"
        "v_readlane_b32 s62, v155, 36\n\t"
        "s_mov_b32 exec_lo, 0\n\t"
        "s_mov_b32 exec_hi, 0xfff00000\n\t"
        "ds_write_b32 v119, v155\n\t"
        "s_mov_b64 exec, -1\n\t"
        "s_lshl_b32 s70, s62, 5\n\t"
        "s_sub_u32 s71, s70, 16384\n\t"
        "s_mul_i32 s71, s71, s68\n\t"
        "v_add_u32 v172, s71, v128\n\t"
        "v_add_u32 v173, s71, v129\n\t"
        "v_add_u32 v174, s71, v130\n\t"
        "s_add_u32 s71, s71, s70\n\t"
        "v_add_u32 v175, s71, v128\n\t"
        "v_add_u32 v176, s71, v129\n\t"
        "v_add_u32 v177, s71, v130\n\t"
        "s_add_u32 s71, s71, s70\n\t"
        "v_add_u32 v178, s71, v128\n\t"
        "v_add_u32 v179, s71, v129\n\t"
        "v_add_u32 v180, s71, v130\n\t"
        "s_cmp_eq_u32 s69, 36\n\t"
        "s_cselect_b64 exec, -1, 0\n\t"
        "v_swap_b32 v68, v69\n\t"
        "v_swap_b32 v173, v175\n\t"
        "v_swap_b32 v174, v178\n\t"
        "v_swap_b32 v177, v179\n\t"
        "s_mov_b64 exec, -1\n\t"
        "v_add3_u32 v68, v68, v52, -1\n\t"
        "v_add3_u32 v69, v69, v158, -1\n\t"
        "v_cvt_f32_i32 v68, v68\n\t"
        "v_cvt_f32_i32 v69, v69\n\t"
        "s_waitcnt lgkmcnt(1)\n\t"
        "v_add_f32 v78, v78, v56\n\t"
        "v_add_f32 v79, v79, v57\n\t"
        "v_readlane_b32 s69, v155, 31\n\t"
        "s_nop 1\n\t"
        "v_mov_b32 v154, s69\n\t"
        "v_mov_b32 v119, s62\n\t"
        "v_readlane_b32 s53, v155, 28\n\t"
        "v_readlane_b32 s9, v155, 46\n\t"
        "s_sub_u32 s10, 43200, s11\n\t"
        "s_mul_i32 s10, s10, s68\n\t"
        "s_sub_u32 s11, s11, 192\n\t"
        "v_mad_u32_u24 v148, v158, s11, v144\n\t"
        "v_add_u32 v148, s10, v148\n\t"
        "v_mad_u32_u24 v150, v83, s11, v145\n\t"
        "s_lshl_b32 s70, s49, 0\n\t"
        "s_add_u32 s70, s70, 50\n\t"
        "v_readlane_b32 s52, v155, s70\n\t"
        "s_nop 1\n\t"
        "s_add_u32 s52, s52, s10\n\t"
        "v_readlane_b32 s51, v155, 49\n\t"
        "s_lshl_b32 s70, s49, 30\n\t"
        "s_add_u32 s70, s70, s10\n\t"
        "s_add_u32 s51, s51, s70\n\t"
        "s_mov_b64 exec, 0\n\t"
        "s_cmp_eq_u32 s4, 0\n\t"
        "s_cbranch_scc1 .LpS0%=\n\t"
        "s_cmp_eq_u32 s4, 1\n\t"
        "s_cbranch_scc1 .LpS1%=\n\t"
        "s_cmp_eq_u32 s4, 2\n\t"
        "s_cbranch_scc1 .LpS2%=\n\t"
        "s_nop 0\n\t"
        ".LrS%=:\n\t"
        "s_mov_b64 exec, -1\n\t"
        "s_waitcnt vmcnt(9) lgkmcnt(0)\n\t"
        "s_barrier\n\t"
        "ds_read_b64_tr_b16 a[0:1], v138 offset:6144\n\t"
        "ds_read_b64_tr_b16 a[2:3], v138 offset:6912\n\t"
        "ds_read_b64_tr_b16 a[4:5], v139 offset:6144\n\t"
        "ds_read_b64_tr_b16 a[6:7], v139 offset:6912\n\t"
        "ds_read_b64_tr_b16 a[8:9], v149 offset:6144\n\t"
        "ds_read_b64_tr_b16 a[10:11], v149 offset:6912\n\t"
        "ds_read_b128 v[84:87], v128 offset:8192\n\t"
        "ds_read_b64_tr_b16 a[12:13], v138 offset:12288\n\t"
        "ds_read_b64_tr_b16 a[14:15], v138 offset:13056\n\t"
        "ds_read_b64_tr_b16 a[16:17], v139 offset:12288\n\t"
        "ds_read_b64_tr_b16 a[18:19], v139 offset:13056\n\t"
        "ds_read_b64_tr_b16 a[20:21], v149 offset:12288\n\t"
        "ds_read_b64_tr_b16 a[22:23], v149 offset:13056\n\t"
        "ds_read_b128 v[88:91], v128 offset:8192\n\t"
        "s_add_u32 m0, s37, -12288\n\t"
        "s_add_u32 s36, s36, s39\n\t"
        "s_waitcnt lgkmcnt(7)\n\t"
        "v_mfma_f32_16x16x32_bf16 v[12:15], a[0:3], v[84:87], 0\n\t"
        "buffer_load_dwordx4 v140, s[24:27], s36 offen offset:0 lds\n\t"
        "buffer_load_dwordx4 v141, s[24:27], s36 offen offset:1024 lds\n\t"
        "buffer_load_dwordx4 v142, s[24:27], s36 offen offset:2048 lds\n\t"
        "s_add_u32 m0, s37, 0\n\t"
        "s_add_u32 s36, s36, s38\n\t"
        "buffer_load_dwordx4 v140, s[24:27], s36 offen offset:0 lds\n\t"
        "buffer_load_dwordx4 v141, s[24:27], s36 offen offset:1024 lds\n\t"
        "buffer_load_dwordx4 v142, s[24:27], s36 offen offset:2048 lds\n\t"
        "s_add_u32 m0, s37, 12288\n\t"
        "s_add_u32 s36, s36, s5\n\t"
        "buffer_load_dwordx4 v140, s[24:27], s36 offen offset:0 lds\n\t"
        "s_waitcnt vmcnt(10)\n\t"
        "s_barrier\n\t"
        "ds_read_b128 v[92:95], v128 offset:4096\n\t"
        "ds_read_b64_tr_b16 a[24:25], v138 offset:0\n\t"
        "v_mfma_f32_16x16x32_bf16 v[16:19], a[4:7], v[84:87], 0\n\t"
        "buffer_load_dwordx4 v141, s[24:27], s36 offen offset:1024 lds\n\t"
        "ds_read_b64_tr_b16 a[26:27], v138 offset:768\n\t"
        "v_mfma_f32_16x16x32_bf16 v[20:23], a[8:11], v[84:87], 0\n\t"
        "ds_read_b64_tr_b16 a[28:29], v139 offset:0\n\t"
        "ds_read_b64_tr_b16 a[30:31], v139 offset:768\n\t"
        "s_waitcnt lgkmcnt(5)\n\t"
        "v_mfma_f32_16x16x32_bf16 v[24:27], a[12:15], v[88:91], 0\n\t"
        "buffer_load_dwordx4 v142, s[24:27], s36 offen offset:2048 lds\n\t"
        "s_add_u32 m0, s37, 24576\n\t"
        "ds_read_b64_tr_b16 a[32:33], v149 offset:0\n\t"
        "s_add_u32 s36, s36, s39\n\t"
        "ds_read_b64_tr_b16 a[34:35], v149 offset:768\n\t"
        "v_mfma_f32_16x16x32_bf16 v[28:31], a[16:19], v[88:91], 0\n\t"
        "ds_read_b128 v[96:99], v128 offset:8192\n\t"
        "v_mfma_f32_16x16x32_bf16 v[32:35], a[20:23], v[88:91], 0\n\t"
        "ds_read_b128 v[100:103], v128 offset:12288\n\t"
        "s_waitcnt lgkmcnt(8)\n\t"
        "v_mfma_f32_16x16x32_bf16 v[0:3], a[0:3], v[92:95], 0\n\t"
        "buffer_load_dwordx4 v140, s[24:27], s36 offen offset:0 lds\n\t"
        "v_mfma_f32_16x16x32_bf16 v[4:7], a[4:7], v[92:95], 0\n\t"
        "v_mfma_f32_16x16x32_bf16 v[8:11], a[8:11], v[92:95], 0\n\t"
        "v_mfma_f32_16x16x32_bf16 v[12:15], a[12:15], v[92:95], v[12:15]\n\t"
        "v_mfma_f32_16x16x32_bf16 v[16:19], a[16:19], v[92:95], v[16:19]\n\t"
        "buffer_load_dwordx4 v141, s[24:27], s36 offen offset:1024 lds\n\t"
        "v_mfma_f32_16x16x32_bf16 v[20:23], a[20:23], v[92:95], v[20:23]\n\t"
        "s_waitcnt lgkmcnt(1)\n\t"
        "v_mfma_f32_16x16x32_bf16 v[0:3], a[24:27], v[96:99], v[0:3]\n\t"
        "v_mfma_f32_16x16x32_bf16 v[4:7], a[28:31], v[96:99], v[4:7]\n\t"
        "v_mfma_f32_16x16x32_bf16 v[8:11], a[32:35], v[96:99], v[8:11]\n\t"
        "buffer_load_dwordx4 v142, s[24:27], s36 offen offset:2048 lds\n\t"
        "s_add_u32 m0, s35, 16384\n\t"
        "s_add_u32 s43, s34, 172032\n\t"
        "s_waitcnt lgkmcnt(0)\n\t"
        "v_mfma_f32_16x16x32_bf16 v[12:15], a[24:27], v[100:103], v[12:15]\n\t"
        "v_mfma_f32_16x16x32_bf16 v[16:19], a[28:31], v[100:103], v[16:19]\n\t"
        "s_waitcnt vmcnt(9)\n\t"
        "s_barrier\n\t"
        "ds_read_b64_tr_b16 a[36:37], v138 offset:18432\n\t"
        "ds_read_b64_tr_b16 a[38:39], v138 offset:19200\n\t"
        "ds_read_b64_tr_b16 a[40:41], v139 offset:18432\n\t"
        "v_mfma_f32_16x16x32_bf16 v[20:23], a[32:35], v[100:103], v[20:23]\n\t"
        "buffer_load_dwordx4 v143, s[16:19], s43 offen  lds\n\t"
        "s_add_u32 m0, s35, 32768\n\t"
        "ds_read_b64_tr_b16 a[42:43], v139 offset:19200\n\t"
        "s_add_u32 s43, s34, 344064\n\t"
        "ds_read_b64_tr_b16 a[44:45], v149 offset:18432\n\t"
        "s_min_u32 s43, s43, 9590784\n\t"
        "ds_read_b64_tr_b16 a[46:47], v149 offset:19200\n\t"
        "ds_read_b64_tr_b16 a[48:49], v138 offset:24576\n\t"
        "ds_read_b64_tr_b16 a[50:51], v138 offset:25344\n\t"
        "ds_read_b64_tr_b16 a[52:53], v139 offset:24576\n\t"
        "ds_read_b64_tr_b16 a[54:55], v139 offset:25344\n\t"
        "ds_read_b64_tr_b16 a[56:57], v149 offset:24576\n\t"
        "ds_read_b64_tr_b16 a[58:59], v149 offset:25344\n\t"
        "ds_read_b128 v[104:107], v128 offset:4160\n\t"
        "v_mfma_f32_16x16x32_bf16 v[24:27], a[0:3], v[100:103], v[24:27]\n\t"
        "buffer_load_dwordx4 v143, s[16:19], s43 offen  lds\n\t"
        "s_add_u32 m0, s35, 157696\n\t"
        "ds_read_b64_tr_b16 a[60:61], v138 offset:30720\n\t"
        "v_mfma_f32_16x16x32_bf16 v[28:31], a[4:7], v[100:103], v[28:31]\n\t"
        "buffer_load_dwordx4 v64, s[20:23], 0 offen lds\n\t"
        "s_add_u32 m0, s35, 0\n\t"
        "ds_read_b64_tr_b16 a[62:63], v138 offset:31488\n\t"
        "s_add_u32 s43, s34, 0\n\t"
        "v_mfma_f32_16x16x32_bf16 v[32:35], a[8:11], v[100:103], v[32:35]\n\t"
        "s_waitcnt vmcnt(9)\n\t"
        "s_barrier\n\t"
        "ds_read_b64_tr_b16 a[64:65], v139 offset:30720\n\t"
        "ds_read_b64_tr_b16 a[66:67], v139 offset:31488\n\t"
        "v_mfma_f32_16x16x32_bf16 v[36:39], a[12:15], v[100:103], 0\n\t"
        "ds_read_b64_tr_b16 a[68:69], v149 offset:30720\n\t"
        "ds_read_b64_tr_b16 a[70:71], v149 offset:31488\n\t"
        "v_mfma_f32_16x16x32_bf16 v[40:43], a[16:19], v[100:103], 0\n\t"
        "buffer_load_dwordx4 v143, s[16:19], s43 offen  lds\n\t"
        "s_add_u32 m0, s35, 36864\n\t"
        "ds_read_b128 v[108:111], v128 offset:8256\n\t"
        "s_add_u32 s43, s34, 387072\n\t"
        "s_min_u32 s43, s43, 9590784\n\t"
        "v_mfma_f32_16x16x32_bf16 v[44:47], a[20:23], v[100:103], 0\n\t"
        "ds_read_b128 v[112:115], v128 offset:12352\n\t"
        "s_waitcnt lgkmcnt(8)\n\t"
        "v_mfma_f32_16x16x32_bf16 v[0:3], a[36:39], v[104:107], v[0:3]\n\t"
        "ds_read_b64_tr_b16 a[72:73], v138 offset:36864\n\t"
        "ds_read_b64_tr_b16 a[74:75], v138 offset:37632\n\t"
        "v_mfma_f32_16x16x32_bf16 v[4:7], a[40:43], v[104:107], v[4:7]\n\t"
        "buffer_load_dwordx4 v143, s[16:19], s43 offen  lds\n\t"
        "ds_read_b64_tr_b16 a[76:77], v139 offset:36864\n\t"
        "v_mfma_f32_16x16x32_bf16 v[8:11], a[44:47], v[104:107], v[8:11]\n\t"
        "ds_read_b64_tr_b16 a[78:79], v139 offset:37632\n\t"
        "ds_read_b64_tr_b16 a[80:81], v149 offset:36864\n\t"
        "v_mfma_f32_16x16x32_bf16 v[12:15], a[48:51], v[104:107], v[12:15]\n\t"
        "ds_read_b64_tr_b16 a[82:83], v149 offset:37632\n\t"
        "v_mfma_f32_16x16x32_bf16 v[16:19], a[52:55], v[104:107], v[16:19]\n\t"
        "buffer_load_dwordx4 v[48:51], v55, s[20:23], s48 offen\n\t"
        "v_mfma_f32_16x16x32_bf16 v[20:23], a[56:59], v[104:107], v[20:23]\n\t"
        "ds_read_b64_tr_b16 a[84:85], v131 offset:0\n\t"
        "s_waitcnt lgkmcnt(8)\n\t"
        "v_mfma_f32_16x16x32_bf16 v[0:3], a[60:63], v[108:111], v[0:3]\n\t"
        "ds_read_b64_tr_b16 a[86:87], v131 offset:768\n\t"
        "v_mfma_f32_16x16x32_bf16 v[4:7], a[64:67], v[108:111], v[4:7]\n\t"
        "ds_read_b64_tr_b16 a[88:89], v132 offset:0\n\t"
        "v_mfma_f32_16x16x32_bf16 v[8:11], a[68:71], v[108:111], v[8:11]\n\t"
        "buffer_load_dword v61, v61, s[20:23], s48 offen offset:512\n\t"
        "s_add_u32 m0, s37, 36864\n\t"
        "s_add_u32 s36, s36, s38\n\t"
        "v_mfma_f32_16x16x32_bf16 v[12:15], a[36:39], v[108:111], v[12:15]\n\t"
        "ds_read_b64_tr_b16 a[90:91], v132 offset:768\n\t"
        "ds_read_b64_tr_b16 a[92:93], v133 offset:0\n\t"
        "v_mfma_f32_16x16x32_bf16 v[16:19], a[40:43], v[108:111], v[16:19]\n\t"
        "ds_read_b64_tr_b16 a[94:95], v133 offset:768\n\t"
        "v_mfma_f32_16x16x32_bf16 v[20:23], a[44:47], v[108:111], v[20:23]\n\t"
        "ds_read_b128 v[84:87], v128 offset:4224\n\t"
        "v_mfma_f32_16x16x32_bf16 v[24:27], a[48:51], v[108:111], v[24:27]\n\t"
        "buffer_load_dwordx4 v140, s[24:27], s36 offen offset:0 lds\n\t"
        "v_mfma_f32_16x16x32_bf16 v[28:31], a[52:55], v[108:111], v[28:31]\n\t"
        "ds_read_b64_tr_b16 a[96:97], v131 offset:6144\n\t"
        "v_mfma_f32_16x16x32_bf16 v[32:35], a[56:59], v[108:111], v[32:35]\n\t"
        "ds_read_b64_tr_b16 a[98:99], v131 offset:6912\n\t"
        "s_waitcnt lgkmcnt(8)\n\t"
        "v_mfma_f32_16x16x32_bf16 v[12:15], a[60:63], v[112:115], v[12:15]\n\t"
        "ds_read_b64_tr_b16 a[100:101], v132 offset:6144\n\t"
        "v_mfma_f32_16x16x32_bf16 v[16:19], a[64:67], v[112:115], v[16:19]\n\t"
        "buffer_load_dwordx4 v141, s[24:27], s36 offen offset:1024 lds\n\t"
        "ds_read_b64_tr_b16 a[102:103], v132 offset:6912\n\t"
        "v_mfma_f32_16x16x32_bf16 v[20:23], a[68:71], v[112:115], v[20:23]\n\t"
        "s_waitcnt vmcnt(9)\n\t"
        "s_barrier\n\t"
        "ds_read_b64_tr_b16 a[104:105], v133 offset:6144\n\t"
        "ds_read_b64_tr_b16 a[106:107], v133 offset:6912\n\t"
        "v_mfma_f32_16x16x32_bf16 v[24:27], a[36:39], v[112:115], v[24:27]\n\t"
        "ds_read_b128 v[88:91], v128 offset:8320\n\t"
        "ds_read_b128 v[92:95], v128 offset:12416\n\t"
        "v_mfma_f32_16x16x32_bf16 v[28:31], a[40:43], v[112:115], v[28:31]\n\t"
        "ds_read_b64_tr_b16 a[108:109], v131 offset:12288\n\t"
        "ds_read_b64_tr_b16 a[110:111], v131 offset:13056\n\t"
        "v_mfma_f32_16x16x32_bf16 v[32:35], a[44:47], v[112:115], v[32:35]\n\t"
        "buffer_load_dwordx4 v142, s[24:27], s36 offen offset:2048 lds\n\t"
        "s_add_u32 m0, s37, 49152\n\t"
        "ds_read_b64_tr_b16 a[112:113], v132 offset:12288\n\t"
        "v_mfma_f32_16x16x32_bf16 v[36:39], a[48:51], v[112:115], v[36:39]\n\t"
        "ds_read_b64_tr_b16 a[114:115], v132 offset:13056\n\t"
        "s_add_u32 s36, s36, s5\n\t"
        "ds_read_b64_tr_b16 a[116:117], v133 offset:12288\n\t"
        "v_mfma_f32_16x16x32_bf16 v[40:43], a[52:55], v[112:115], v[40:43]\n\t"
        "ds_read_b64_tr_b16 a[118:119], v133 offset:13056\n\t"
        "ds_read_b64_tr_b16 a[120:121], v131 offset:18432\n\t"
        "v_mfma_f32_16x16x32_bf16 v[44:47], a[56:59], v[112:115], v[44:47]\n\t"
        "ds_read_b64_tr_b16 a[122:123], v131 offset:19200\n\t"
        "ds_read_b64_tr_b16 a[124:125], v132 offset:18432\n\t"
        "s_waitcnt lgkmcnt(14)\n\t"
        "v_mfma_f32_16x16x32_bf16 v[0:3], a[72:75], v[84:87], v[0:3]\n\t"
        "buffer_load_dwordx4 v140, s[24:27], s36 offen offset:0 lds\n\t"
        "ds_read_b64_tr_b16 a[126:127], v132 offset:19200\n\t"
        "v_mfma_f32_16x16x32_bf16 v[4:7], a[76:79], v[84:87], v[4:7]\n\t"
        "ds_read_b64_tr_b16 a[128:129], v133 offset:18432\n\t"
        "ds_read_b64_tr_b16 a[130:131], v133 offset:19200\n\t"
        "v_mfma_f32_16x16x32_bf16 v[8:11], a[80:83], v[84:87], v[8:11]\n\t"
        "ds_read_b64_tr_b16 a[132:133], v131 offset:24576\n\t"
        "ds_read_b64_tr_b16 a[134:135], v131 offset:25344\n\t"
        "v_mfma_f32_16x16x32_bf16 v[12:15], a[84:87], v[84:87], v[12:15]\n\t"
        "ds_read_b128 v[96:99], v129 offset:4096\n\t"
        "ds_read_b64_tr_b16 a[136:137], v132 offset:24576\n\t"
        "v_mfma_f32_16x16x32_bf16 v[16:19], a[88:91], v[84:87], v[16:19]\n\t"
        "buffer_load_dwordx4 v141, s[24:27], s36 offen offset:1024 lds\n\t"
        "ds_read_b64_tr_b16 a[138:139], v132 offset:25344\n\t"
        "v_mfma_f32_16x16x32_bf16 v[20:23], a[92:95], v[84:87], v[20:23]\n\t"
        "ds_read_b64_tr_b16 a[140:141], v133 offset:24576\n\t"
        "ds_read_b64_tr_b16 a[142:143], v133 offset:25344\n\t"
        "s_waitcnt lgkmcnt(14)\n\t"
        "v_mfma_f32_16x16x32_bf16 v[0:3], a[96:99], v[88:91], v[0:3]\n\t"
        "ds_read_b64_tr_b16 a[144:145], v131 offset:30720\n\t"
        "ds_read_b64_tr_b16 a[146:147], v131 offset:31488\n\t"
        "v_mfma_f32_16x16x32_bf16 v[4:7], a[100:103], v[88:91], v[4:7]\n\t"
        "ds_read_b64_tr_b16 a[148:149], v132 offset:30720\n\t"
        "ds_read_b64_tr_b16 a[150:151], v132 offset:31488\n\t"
        "v_mfma_f32_16x16x32_bf16 v[8:11], a[104:107], v[88:91], v[8:11]\n\t"
        "ds_read_b64_tr_b16 a[152:153], v133 offset:30720\n\t"
        "ds_read_b64_tr_b16 a[154:155], v133 offset:31488\n\t"
        "v_mfma_f32_16x16x32_bf16 v[12:15], a[72:75], v[88:91], v[12:15]\n\t"
        "ds_read_b128 v[100:103], v129 offset:8192\n\t"
        "v_mfma_f32_16x16x32_bf16 v[16:19], a[76:79], v[88:91], v[16:19]\n\t"
        "v_mfma_f32_16x16x32_bf16 v[20:23], a[80:83], v[88:91], v[20:23]\n\t"
        "v_mfma_f32_16x16x32_bf16 v[24:27], a[84:87], v[88:91], v[24:27]\n\t"
        "v_mfma_f32_16x16x32_bf16 v[28:31], a[88:91], v[88:91], v[28:31]\n\t"
        "v_mfma_f32_16x16x32_bf16 v[32:35], a[92:95], v[88:91], v[32:35]\n\t"
        "v_mfma_f32_16x16x32_bf16 v[12:15], a[96:99], v[92:95], v[12:15]\n\t"
        "v_mfma_f32_16x16x32_bf16 v[16:19], a[100:103], v[92:95], v[16:19]\n\t"
        "v_mfma_f32_16x16x32_bf16 v[20:23], a[104:107], v[92:95], v[20:23]\n\t"
        "v_mfma_f32_16x16x32_bf16 v[24:27], a[72:75], v[92:95], v[24:27]\n\t"
        "ds_read_b128 v[104:107], v129 offset:12288\n\t"
        "v_mfma_f32_16x16x32_bf16 v[28:31], a[76:79], v[92:95], v[28:31]\n\t"
        "v_mfma_f32_16x16x32_bf16 v[32:35], a[80:83], v[92:95], v[32:35]\n\t"
        "v_mfma_f32_16x16x32_bf16 v[36:39], a[84:87], v[92:95], v[36:39]\n\t"
        "v_mfma_f32_16x16x32_bf16 v[40:43], a[88:91], v[92:95], v[40:43]\n\t"
        "v_mfma_f32_16x16x32_bf16 v[44:47], a[92:95], v[92:95], v[44:47]\n\t"
        "s_waitcnt lgkmcnt(1)\n\t"
        "v_mfma_f32_16x16x32_bf16 v[0:3], a[108:111], v[96:99], v[0:3]\n\t"
        "v_mfma_f32_16x16x32_bf16 v[4:7], a[112:115], v[96:99], v[4:7]\n\t"
        "v_mfma_f32_16x16x32_bf16 v[8:11], a[116:119], v[96:99], v[8:11]\n\t"
        "v_mfma_f32_16x16x32_bf16 v[12:15], a[120:123], v[96:99], v[12:15]\n\t"
        "v_mfma_f32_16x16x32_bf16 v[16:19], a[124:127], v[96:99], v[16:19]\n\t"
        "buffer_load_dwordx4 v142, s[24:27], s36 offen offset:2048 lds\n\t"
        "s_add_u32 m0, s37, 0\n\t"
        "s_add_u32 s36, s36, s39\n\t"
        "v_mfma_f32_16x16x32_bf16 v[20:23], a[128:131], v[96:99], v[20:23]\n\t"
        "v_mfma_f32_16x16x32_bf16 v[0:3], a[132:135], v[100:103], v[0:3]\n\t"
        "v_mfma_f32_16x16x32_bf16 v[4:7], a[136:139], v[100:103], v[4:7]\n\t"
        "v_mfma_f32_16x16x32_bf16 v[8:11], a[140:143], v[100:103], v[8:11]\n\t"
        "v_mfma_f32_16x16x32_bf16 v[12:15], a[108:111], v[100:103], v[12:15]\n\t"
        "v_mfma_f32_16x16x32_bf16 v[16:19], a[112:115], v[100:103], v[16:19]\n\t"
        "v_mfma_f32_16x16x32_bf16 v[20:23], a[116:119], v[100:103], v[20:23]\n\t"
        "v_mfma_f32_16x16x32_bf16 v[24:27], a[120:123], v[100:103], v[24:27]\n\t"
        "v_mfma_f32_16x16x32_bf16 v[28:31], a[124:127], v[100:103], v[28:31]\n\t"
        "v_mfma_f32_16x16x32_bf16 v[32:35], a[128:131], v[100:103], v[32:35]\n\t"
        "s_waitcnt lgkmcnt(0)\n\t"
        "v_mfma_f32_16x16x32_bf16 v[12:15], a[132:135], v[104:107], v[12:15]\n\t"
        "v_mfma_f32_16x16x32_bf16 v[16:19], a[136:139], v[104:107], v[16:19]\n\t"
        "v_mfma_f32_16x16x32_bf16 v[20:23], a[140:143], v[104:107], v[20:23]\n\t"
        "v_mfma_f32_16x16x32_bf16 v[24:27], a[108:111], v[104:107], v[24:27]\n\t"
        "ds_read_b128 v[108:111], v129 offset:4160\n\t"
        "v_mfma_f32_16x16x32_bf16 v[28:31], a[112:115], v[104:107], v[28:31]\n\t"
        "v_mfma_f32_16x16x32_bf16 v[32:35], a[116:119], v[104:107], v[32:35]\n\t"
        "v_mfma_f32_16x16x32_bf16 v[36:39], a[120:123], v[104:107], v[36:39]\n\t"
        "ds_read_b128 v[112:115], v129 offset:8256\n\t"
        "v_mfma_f32_16x16x32_bf16 v[40:43], a[124:127], v[104:107], v[40:43]\n\t"
        "v_mfma_f32_16x16x32_bf16 v[44:47], a[128:131], v[104:107], v[44:47]\n\t"
        "s_waitcnt lgkmcnt(1)\n\t"
        "v_mfma_f32_16x16x32_bf16 v[0:3], a[144:147], v[108:111], v[0:3]\n\t"
        ".LpS0%=:\n\t"
        "s_cbranch_execz .LrS%=\n\t"
        "ds_read_b128 v[84:87], v129 offset:12352\n\t"
        "v_mfma_f32_16x16x32_bf16 v[4:7], a[148:151], v[108:111], v[4:7]\n\t"
        "v_mfma_f32_16x16x32_bf16 v[8:11], a[152:155], v[108:111], v[8:11]\n\t"
        "s_waitcnt vmcnt(3)\n\t"
        "s_barrier\n\t"
        "buffer_load_dwordx4 v140, s[24:27], s36 offen offset:0 lds\n\t"
        "ds_read_b128 v[88:91], v128 offset:0\n\t"
        "s_waitcnt lgkmcnt(2)\n\t"
        "v_mfma_f32_16x16x32_bf16 v[12:15], a[144:147], v[112:115], v[12:15]\n\t"
        "ds_read_b128 v[92:95], v128 offset:64\n\t"
        "ds_read_b128 v[96:99], v128 offset:128\n\t"
        "v_readlane_b32 s46, v155, 37\n\t"
        "v_readlane_b32 s47, v155, 40\n\t"
        "v_mfma_f32_16x16x32_bf16 v[16:19], a[148:151], v[112:115], v[16:19]\n\t"
        "ds_read_b128 v[100:103], v129 offset:0\n\t"
        "ds_read_b128 v[104:107], v128 offset:16384\n\t"
        "v_and_b32 v48, s54, v48\n\t"
        "v_pk_max_u16 v60, v154, v48\n\t"
        "v_mfma_f32_16x16x32_bf16 v[20:23], a[152:155], v[112:115], v[20:23]\n\t"
        "ds_read_b32 v64, v70 offset:0\n\t"
        "ds_read_b64_tr_b16 a[156:157], v131 offset:36864\n\t"
        "v_and_b32 v49, s54, v49\n\t"
        "s_waitcnt lgkmcnt(7)\n\t"
        "v_mfma_f32_16x16x32_bf16 v[24:27], a[144:147], v[84:87], v[24:27]\n\t"
        "ds_read_b32 v67, v67 offset:0\n\t"
        "buffer_load_dwordx4 v141, s[24:27], s36 offen offset:1024 lds\n\t"
        "v_pk_max_u16 v60, v60, v49\n\t"
        "v_mfma_f32_16x16x32_bf16 v[28:31], a[148:151], v[84:87], v[28:31]\n\t"
        "ds_read_b32 v65, v70 offset:1280\n\t"
        "ds_read_b128 v[108:111], v128 offset:16448\n\t"
        "v_and_b32 v50, s54, v50\n\t"
        "v_mfma_f32_16x16x32_bf16 v[32:35], a[152:155], v[84:87], v[32:35]\n\t"
        "ds_read_b32 v66, v70 offset:2560\n\t"
        "ds_read_b64_tr_b16 a[158:159], v131 offset:37632\n\t"
        "v_pk_max_u16 v60, v60, v50\n\t"
        "v_and_b32 v51, s54, v51\n\t"
        "s_waitcnt lgkmcnt(11)\n\t"
        "v_mfma_f32_16x16x32_bf16 v[0:3], a[12:15], v[88:91], v[0:3]\n\t"
        "ds_read_b64_tr_b16 a[160:161], v132 offset:36864\n\t"
        "ds_read_b64_tr_b16 a[162:163], v132 offset:37632\n\t"
        "v_pk_max_u16 v60, v60, v51\n\t"
        "v_mfma_f32_16x16x32_bf16 v[4:7], a[16:19], v[88:91], v[4:7]\n\t"
        "buffer_load_dwordx4 v142, s[24:27], s36 offen offset:2048 lds\n\t"
        "s_add_u32 m0, s37, 12288\n\t"
        "ds_read_b64_tr_b16 a[164:165], v133 offset:36864\n\t"
        "v_mfma_f32_16x16x32_bf16 v[8:11], a[20:23], v[88:91], v[8:11]\n\t"
        "ds_read_b64_tr_b16 a[166:167], v133 offset:37632\n\t"
        "s_add_u32 s36, s36, s38\n\t"
        "s_waitcnt lgkmcnt(11)\n\t"
        "v_mfma_f32_16x16x32_bf16 v[0:3], a[48:51], v[92:95], v[0:3]\n\t"
        "ds_read_b128 v[112:115], v129 offset:64\n\t"
        "ds_read_b64_tr_b16 a[168:169], v131 offset:43008\n\t"
        "v_and_b32 v61, s54, v61\n\t"
        "v_mfma_f32_16x16x32_bf16 v[4:7], a[52:55], v[92:95], v[4:7]\n\t"
        "ds_read_b64_tr_b16 a[170:171], v131 offset:43776\n\t"
        "ds_read_b64_tr_b16 a[172:173], v132 offset:43008\n\t"
        "v_pk_max_u16 v60, v60, v61\n\t"
        "v_cmp_ne_u32 s[12:13], v60, v154\n\t"
        "v_mfma_f32_16x16x32_bf16 v[8:11], a[56:59], v[92:95], v[8:11]\n\t"
        "buffer_load_dwordx4 v140, s[24:27], s36 offen offset:0 lds\n\t"
        "ds_read_b64_tr_b16 a[174:175], v132 offset:43776\n\t"
        "s_lshl_b32 s6, 256, s4\n\t"
        "v_mfma_f32_16x16x32_bf16 v[0:3], a[84:87], v[96:99], v[0:3]\n\t"
        "s_cmp_lg_u64 s[12:13], 0\n\t"
        "s_cselect_b32 s6, s6, 0\n\t"
        "ds_read_b128 v[84:87], v129 offset:4160\n\t"
        "ds_read_b64_tr_b16 a[176:177], v133 offset:43008\n\t"
        "v_mfma_f32_16x16x32_bf16 v[4:7], a[88:91], v[96:99], v[4:7]\n\t"
        "ds_read_b64_tr_b16 a[178:179], v133 offset:43776\n\t"
        "s_waitcnt lgkmcnt(10)\n\t"
        "v_perm_b32 v56, v64, v64, s44\n\t"
        "v_mfma_f32_16x16x32_bf16 v[8:11], a[92:95], v[96:99], v[8:11]\n\t"
        "v_perm_b32 v57, v64, v64, s45\n\t"
        "v_add_f32 v58, s46, v68\n\t"
        "v_mfma_f32_16x16x32_bf16 v[0:3], a[120:123], v[100:103], v[0:3]\n\t"
        "buffer_load_dwordx4 v141, s[24:27], s36 offen offset:1024 lds\n\t"
        "v_add_f32 v59, s47, v69\n\t"
        "v_mfma_f32_16x16x32_bf16 v[4:7], a[124:127], v[100:103], v[4:7]\n\t"
        "ds_read_b128 v[88:91], v129 offset:8256\n\t"
        "v_add_f32 v56, v56, v58\n\t"
        "v_add_f32 v57, v57, v59\n\t"
        "v_mfma_f32_16x16x32_bf16 v[8:11], a[128:131], v[100:103], v[8:11]\n\t"
        "v_cvt_pk_bf16_f32 v52, v56, v57\n\t"
        "v_cvt_pk_bf16_f32 v58, v58, v59\n\t"
        "v_mfma_f32_16x16x32_bf16 v[24:27], a[24:27], v[104:107], v[24:27]\n\t"
        "v_cmp_ne_u32 s[64:65], v52, v58\n\t"
        "v_perm_b32 v57, v67, v67, s45\n\t"
        "v_mfma_f32_16x16x32_bf16 v[28:31], a[28:31], v[104:107], v[28:31]\n\t"
        "buffer_load_dwordx4 v142, s[24:27], s36 offen offset:2048 lds\n\t"
        "s_add_u32 m0, s37, 24576\n\t"
        "v_mfma_f32_16x16x32_bf16 v[32:35], a[32:35], v[104:107], v[32:35]\n\t"
        "s_add_u32 s36, s36, s5\n\t"
        "v_mbcnt_lo_u32_b32 v56, s64, 1\n\t"
        "v_mfma_f32_16x16x32_bf16 v[36:39], a[0:3], v[104:107], v[36:39]\n\t"
        "ds_read_b128 v[92:95], v129 offset:12352\n\t"
        "v_mbcnt_hi_u32_b32 v56, s65, v56\n\t"
        "s_bcnt1_i32_b64 s61, s[64:65]\n\t"
        "v_mfma_f32_16x16x32_bf16 v[40:43], a[4:7], v[104:107], v[40:43]\n\t"
        "v_add_u32 v54, s61, 1\n\t"
        "v_lshl_add_u32 v56, v56, 3, s60\n\t"
        "v_mfma_f32_16x16x32_bf16 v[44:47], a[8:11], v[104:107], v[44:47]\n\t"
        "s_mov_b64 exec, s[64:65]\n\t"
        "ds_write_b64 v56, v[52:53]\n\t"
        "s_mov_b64 exec, -1\n\t"
        "v_mfma_f32_16x16x32_bf16 v[24:27], a[60:63], v[108:111], v[24:27]\n\t"
        "buffer_load_dwordx4 v140, s[24:27], s36 offen offset:0 lds\n\t"
        "v_swap_b32 v53, v80\n\t"
        "v_mfma_f32_16x16x32_bf16 v[28:31], a[64:67], v[108:111], v[28:31]\n\t"
        "v_perm_b32 v56, v67, v67, s44\n\t"
        "v_add_f32 v56, v56, v78\n\t"
        "v_mfma_f32_16x16x32_bf16 v[32:35], a[68:71], v[108:111], v[32:35]\n\t"
        "v_add_f32 v57, v57, v79\n\t"
        "v_cvt_pk_bf16_f32 v52, v56, v57\n\t"
        "v_mfma_f32_16x16x32_bf16 v[36:39], a[36:39], v[108:111], v[36:39]\n\t"
        "ds_read_b128 v[96:99], v128 offset:16512\n\t"
        "v_cvt_pk_bf16_f32 v58, v78, v79\n\t"
        "v_mfma_f32_16x16x32_bf16 v[40:43], a[40:43], v[108:111], v[40:43]\n\t"
        "buffer_load_dwordx4 v141, s[24:27], s36 offen offset:1024 lds\n\t"
        "v_cmp_ne_u32 s[64:65], v52, v58\n\t"
        "v_readlane_b32 s47, v155, 41\n\t"
        "v_mfma_f32_16x16x32_bf16 v[44:47], a[44:47], v[108:111], v[44:47]\n\t"
        "s_waitcnt vmcnt(5) lgkmcnt(3)\n\t"
        "s_barrier\n\t"
        "ds_read_b64_tr_b16 a[180:181], v131 offset:49152\n\t"
        "ds_read_b64_tr_b16 a[182:183], v131 offset:49920\n\t"
        "v_mfma_f32_16x16x32_bf16 v[0:3], a[156:159], v[112:115], v[0:3]\n\t"
        "ds_read_b64_tr_b16 a[184:185], v132 offset:49152\n\t"
        "ds_read_b64_tr_b16 a[186:187], v132 offset:49920\n\t"
        "s_and_b64 s[64:65], s[64:65], 0xffffff\n\t"
        "v_mfma_f32_16x16x32_bf16 v[4:7], a[160:163], v[112:115], v[4:7]\n\t"
        "ds_read_b64_tr_b16 a[188:189], v133 offset:49152\n\t"
        "ds_read_b64_tr_b16 a[190:191], v133 offset:49920\n\t"
        "v_mbcnt_lo_u32_b32 v56, s64, v54\n\t"
        "v_mfma_f32_16x16x32_bf16 v[8:11], a[164:167], v[112:115], v[8:11]\n\t"
        "buffer_load_dwordx4 v142, s[24:27], s36 offen offset:2048 lds\n\t"
        "s_add_u32 m0, s37, 36864\n\t"
        "v_mfma_f32_16x16x32_bf16 v[12:15], a[156:159], v[84:87], v[12:15]\n\t"
        "ds_read_b128 v[100:103], v129 offset:16384\n\t"
        "s_add_u32 s36, s36, s39\n\t"
        "ds_read_b64_tr_b16 a[192:193], v131 offset:55296\n\t"
        "v_mfma_f32_16x16x32_bf16 v[16:19], a[160:163], v[84:87], v[16:19]\n\t"
        "ds_read_b64_tr_b16 a[194:195], v131 offset:56064\n\t"
        "ds_read_b64_tr_b16 a[196:197], v132 offset:55296\n\t"
        "s_bcnt1_i32_b64 s46, s[64:65]\n\t"
        "v_mfma_f32_16x16x32_bf16 v[20:23], a[164:167], v[84:87], v[20:23]\n\t"
        "ds_read_b64_tr_b16 a[198:199], v132 offset:56064\n\t"
        "ds_read_b64_tr_b16 a[200:201], v133 offset:55296\n\t"
        "s_add_u32 s61, s61, s46\n\t"
        "v_add_u32 v54, s46, v54\n\t"
        "v_mfma_f32_16x16x32_bf16 v[0:3], a[168:171], v[88:91], v[0:3]\n\t"
        "buffer_load_dwordx4 v140, s[24:27], s36 offen offset:0 lds\n\t"
        "ds_read_b64_tr_b16 a[202:203], v133 offset:56064\n\t"
        "v_lshl_add_u32 v56, v56, 3, s60\n\t"
        "v_mfma_f32_16x16x32_bf16 v[4:7], a[172:175], v[88:91], v[4:7]\n\t"
        "s_mov_b64 exec, s[64:65]\n\t"
        "ds_write_b64 v56, v[52:53]\n\t"
        "s_mov_b64 exec, -1\n\t"
        "v_mfma_f32_16x16x32_bf16 v[8:11], a[176:179], v[88:91], v[8:11]\n\t"
        "ds_read_b64_tr_b16 a[204:205], v131 offset:0\n\t"
        "v_add_u32 v53, 2048, v80\n\t"
        "v_mfma_f32_16x16x32_bf16 v[24:27], a[156:159], v[88:91], v[24:27]\n\t"
        "ds_read_b128 v[104:107], v129 offset:128\n\t"
        "v_readlane_b32 s46, v155, 38\n\t"
        "v_mfma_f32_16x16x32_bf16 v[28:31], a[160:163], v[88:91], v[28:31]\n\t"
        "buffer_load_dwordx4 v141, s[24:27], s36 offen offset:1024 lds\n\t"
        "v_perm_b32 v56, v65, v65, s44\n\t"
        "v_mfma_f32_16x16x32_bf16 v[32:35], a[164:167], v[88:91], v[32:35]\n\t"
        "ds_read_b64_tr_b16 a[206:207], v131 offset:768\n\t"
        "v_perm_b32 v57, v65, v65, s45\n\t"
        "s_waitcnt lgkmcnt(8)\n\t"
        "v_mfma_f32_16x16x32_bf16 v[12:15], a[168:171], v[92:95], v[12:15]\n\t"
        "ds_read_b128 v[108:111], v129 offset:4224\n\t"
        "v_add_f32 v58, s46, v68\n\t"
        "v_mfma_f32_16x16x32_bf16 v[16:19], a[172:175], v[92:95], v[16:19]\n\t"
        "ds_read_b64_tr_b16 a[208:209], v132 offset:0\n\t"
        "v_add_f32 v59, s47, v69\n\t"
        "v_add_f32 v56, v56, v58\n\t"
        "v_mfma_f32_16x16x32_bf16 v[20:23], a[176:179], v[92:95], v[20:23]\n\t"
        "buffer_load_dwordx4 v142, s[24:27], s36 offen offset:2048 lds\n\t"
        "s_add_u32 m0, s37, 49152\n\t"
        "v_mfma_f32_16x16x32_bf16 v[36:39], a[156:159], v[92:95], v[36:39]\n\t"
        "ds_read_b64_tr_b16 a[210:211], v132 offset:768\n\t"
        "s_add_u32 s36, s36, s38\n\t"
        "ds_read_b64_tr_b16 a[212:213], v133 offset:0\n\t"
        "v_mfma_f32_16x16x32_bf16 v[40:43], a[160:163], v[92:95], v[40:43]\n\t"
        "ds_read_b64_tr_b16 a[214:215], v133 offset:768\n\t"
        "v_add_f32 v57, v57, v59\n\t"
        "v_mfma_f32_16x16x32_bf16 v[44:47], a[164:167], v[92:95], v[44:47]\n\t"
        "ds_read_b64_tr_b16 a[216:217], v131 offset:6144\n\t"
        "v_cvt_pk_bf16_f32 v52, v56, v57\n\t"
        "v_mfma_f32_16x16x32_bf16 v[24:27], a[96:99], v[96:99], v[24:27]\n\t"
        "ds_read_b128 v[112:115], v129 offset:8320\n\t"
        "v_cvt_pk_bf16_f32 v58, v58, v59\n\t"
        "v_mfma_f32_16x16x32_bf16 v[28:31], a[100:103], v[96:99], v[28:31]\n\t"
        "ds_read_b64_tr_b16 a[218:219], v131 offset:6912\n\t"
        "v_cmp_ne_u32 s[64:65], v52, v58\n\t"
        "v_mfma_f32_16x16x32_bf16 v[32:35], a[104:107], v[96:99], v[32:35]\n\t"
        "ds_read_b64_tr_b16 a[220:221], v132 offset:6144\n\t"
        "v_mbcnt_lo_u32_b32 v56, s64, v54\n\t"
        "v_mfma_f32_16x16x32_bf16 v[36:39], a[72:75], v[96:99], v[36:39]\n\t"
        "s_waitcnt vmcnt(6) lgkmcnt(9)\n\t"
        "s_barrier\n\t"
        "v_mfma_f32_16x16x32_bf16 v[40:43], a[76:79], v[96:99], v[40:43]\n\t"
        "buffer_load_dwordx4 v140, s[24:27], s36 offen offset:0 lds\n\t"
        "v_mbcnt_hi_u32_b32 v56, s65, v56\n\t"
        "s_bcnt1_i32_b64 s46, s[64:65]\n\t"
        "v_mfma_f32_16x16x32_bf16 v[44:47], a[80:83], v[96:99], v[44:47]\n\t"
        "ds_read_b64_tr_b16 a[222:223], v132 offset:6912\n\t"
        "s_add_u32 s61, s61, s46\n\t"
        "v_mfma_f32_16x16x32_bf16 v[24:27], a[132:135], v[100:103], v[24:27]\n\t"
        "ds_read_b64_tr_b16 a[224:225], v133 offset:6144\n\t"
        "v_add_u32 v54, s46, v54\n\t"
        "v_mfma_f32_16x16x32_bf16 v[28:31], a[136:139], v[100:103], v[28:31]\n\t"
        "ds_read_b64_tr_b16 a[226:227], v133 offset:6912\n\t"
        "v_min_u32 v56, 97, v56\n\t"
        "v_mfma_f32_16x16x32_bf16 v[32:35], a[140:143], v[100:103], v[32:35]\n\t"
        "buffer_load_dwordx4 v141, s[24:27], s36 offen offset:1024 lds\n\t"
        "v_lshl_add_u32 v56, v56, 3, s60\n\t"
        "v_mfma_f32_16x16x32_bf16 v[36:39], a[108:111], v[100:103], v[36:39]\n\t"
        "s_mov_b64 exec, s[64:65]\n\t"
        "ds_write_b64 v56, v[52:53]\n\t"
        "s_mov_b64 exec, -1\n\t"
        "v_mfma_f32_16x16x32_bf16 v[40:43], a[112:115], v[100:103], v[40:43]\n\t"
        "ds_read_b128 v[84:87], v129 offset:12416\n\t"
        "v_add_u32 v53, 2048, v53\n\t"
        "v_mfma_f32_16x16x32_bf16 v[44:47], a[116:119], v[100:103], v[44:47]\n\t"
        "ds_read_b64_tr_b16 a[228:229], v131 offset:12288\n\t"
        "v_readlane_b32 s46, v155, 39\n\t"
        "v_mfma_f32_16x16x32_bf16 v[0:3], a[192:195], v[104:107], v[0:3]\n\t"
        "buffer_load_dwordx4 v142, s[24:27], s36 offen offset:2048 lds\n\t"
        "v_readlane_b32 s40, v155, 12\n\t"
        "v_mfma_f32_16x16x32_bf16 v[4:7], a[196:199], v[104:107], v[4:7]\n\t"
        "ds_read_b64_tr_b16 a[230:231], v131 offset:13056\n\t"
        "v_readlane_b32 s43, v155, 0\n\t"
        "s_add_u32 m0, s40, s35\n\t"
        "v_mfma_f32_16x16x32_bf16 v[8:11], a[200:203], v[104:107], v[8:11]\n\t"
        "ds_read_b64_tr_b16 a[232:233], v132 offset:12288\n\t"
        "v_readlane_b32 s47, v155, 42\n\t"
        "s_waitcnt lgkmcnt(7)\n\t"
        "v_mfma_f32_16x16x32_bf16 v[0:3], a[180:183], v[108:111], v[0:3]\n\t"
        "ds_read_b64_tr_b16 a[234:235], v132 offset:13056\n\t"
        "v_perm_b32 v56, v66, v66, s44\n\t"
        "v_mfma_f32_16x16x32_bf16 v[4:7], a[184:187], v[108:111], v[4:7]\n\t"
        "buffer_load_dwordx4 v134, s[16:19], s43 offen  lds\n\t"
        "v_readlane_b32 s40, v155, 13\n\t"
        "v_mfma_f32_16x16x32_bf16 v[8:11], a[188:191], v[108:111], v[8:11]\n\t"
        "ds_read_b64_tr_b16 a[236:237], v133 offset:12288\n\t"
        "v_readlane_b32 s43, v155, 1\n\t"
        "v_mfma_f32_16x16x32_bf16 v[12:15], a[192:195], v[108:111], v[12:15]\n\t"
        "ds_read_b128 v[88:91], v129 offset:16448\n\t"
        "s_add_u32 m0, s40, s35\n\t"
        "v_mfma_f32_16x16x32_bf16 v[16:19], a[196:199], v[108:111], v[16:19]\n\t"
        "ds_read_b64_tr_b16 a[238:239], v133 offset:13056\n\t"
        "v_perm_b32 v57, v66, v66, s45\n\t"
        "v_mfma_f32_16x16x32_bf16 v[20:23], a[200:203], v[108:111], v[20:23]\n\t"
        "buffer_load_dwordx4 v135, s[16:19], s43 offen  lds\n\t"
        "v_readlane_b32 s40, v155, 14\n\t"
        "v_mfma_f32_16x16x32_bf16 v[0:3], a[204:207], v[112:115], v[0:3]\n\t"
        "ds_read_b64_tr_b16 a[240:241], v131 offset:18432\n\t"
        "v_readlane_b32 s43, v155, 2\n\t"
        "v_mfma_f32_16x16x32_bf16 v[4:7], a[208:211], v[112:115], v[4:7]\n\t"
        "ds_read_b64_tr_b16 a[242:243], v131 offset:19200\n\t"
        "s_add_u32 m0, s40, s35\n\t"
        "v_mfma_f32_16x16x32_bf16 v[8:11], a[212:215], v[112:115], v[8:11]\n\t"
        "ds_read_b64_tr_b16 a[244:245], v132 offset:18432\n\t"
        "v_add_f32 v58, s46, v68\n\t"
        "v_add_f32 v59, s47, v69\n\t"
        "v_mfma_f32_16x16x32_bf16 v[12:15], a[180:183], v[112:115], v[12:15]\n\t"
        "buffer_load_dwordx4 v134, s[16:19], s43 offen  lds\n\t"
        "v_readlane_b32 s40, v155, 15\n\t"
        "v_mfma_f32_16x16x32_bf16 v[16:19], a[184:187], v[112:115], v[16:19]\n\t"
        "ds_read_b128 v[92:95], v130 offset:0\n\t"
        "v_readlane_b32 s43, v155, 3\n\t"
        "v_mfma_f32_16x16x32_bf16 v[20:23], a[188:191], v[112:115], v[20:23]\n\t"
        "ds_read_b64_tr_b16 a[246:247], v132 offset:19200\n\t"
        "s_add_u32 m0, s40, s35\n\t"
        "v_mfma_f32_16x16x32_bf16 v[24:27], a[192:195], v[112:115], v[24:27]\n\t"
        "ds_read_b128 v[96:99], v130 offset:4096\n\t"
        "v_add_f32 v56, v56, v58\n\t"
        "v_mfma_f32_16x16x32_bf16 v[28:31], a[196:199], v[112:115], v[28:31]\n\t"
        "buffer_load_dwordx4 v135, s[16:19], s43 offen  lds\n\t"
        "v_readlane_b32 s40, v155, 16\n\t"
        "v_mfma_f32_16x16x32_bf16 v[32:35], a[200:203], v[112:115], v[32:35]\n\t"
        "ds_read_b64_tr_b16 a[248:249], v133 offset:18432\n\t"
        "v_readlane_b32 s43, v155, 4\n\t"
        "s_waitcnt lgkmcnt(7)\n\t"
        "v_mfma_f32_16x16x32_bf16 v[12:15], a[204:207], v[84:87], v[12:15]\n\t"
        "ds_read_b64_tr_b16 a[250:251], v133 offset:19200\n\t"
        "s_add_u32 m0, s40, s35\n\t"
        "v_mfma_f32_16x16x32_bf16 v[16:19], a[208:211], v[84:87], v[16:19]\n\t"
        "v_add_f32 v57, v57, v59\n\t"
        "v_cvt_pk_bf16_f32 v52, v56, v57\n\t"
        "v_mfma_f32_16x16x32_bf16 v[20:23], a[212:215], v[84:87], v[20:23]\n\t"
        "buffer_load_dwordx4 v134, s[16:19], s43 offen  lds\n\t"
        "v_readlane_b32 s40, v155, 17\n\t"
        "v_mfma_f32_16x16x32_bf16 v[24:27], a[180:183], v[84:87], v[24:27]\n\t"
        "ds_read_b128 v[100:103], v130 offset:8192\n\t"
        "v_readlane_b32 s43, v155, 5\n\t"
        "v_mfma_f32_16x16x32_bf16 v[28:31], a[184:187], v[84:87], v[28:31]\n\t"
        "s_add_u32 m0, s40, s35\n\t"
        "v_cvt_pk_bf16_f32 v58, v58, v59\n\t"
        "v_mfma_f32_16x16x32_bf16 v[32:35], a[188:191], v[84:87], v[32:35]\n\t"
        "v_cmp_ne_u32 s[64:65], v52, v58\n\t"
        "v_mfma_f32_16x16x32_bf16 v[36:39], a[192:195], v[84:87], v[36:39]\n\t"
        "buffer_load_dwordx4 v135, s[16:19], s43 offen  lds\n\t"
        "v_readlane_b32 s40, v155, 18\n\t"
        "v_readlane_b32 s43, v155, 6\n\t"
        "v_mfma_f32_16x16x32_bf16 v[40:43], a[196:199], v[84:87], v[40:43]\n\t"
        "s_add_u32 m0, s40, s35\n\t"
        "v_mbcnt_lo_u32_b32 v56, s64, v54\n\t"
        "v_mfma_f32_16x16x32_bf16 v[44:47], a[200:203], v[84:87], v[44:47]\n\t"
        "s_waitcnt vmcnt(6) lgkmcnt(1)\n\t"
        "s_barrier\n\t"
        "v_mfma_f32_16x16x32_bf16 v[24:27], a[168:171], v[88:91], v[24:27]\n\t"
        "ds_read_b64_tr_b16 a[252:253], v131 offset:24576\n\t"
        "v_mbcnt_hi_u32_b32 v56, s65, v56\n\t"
        "v_mfma_f32_16x16x32_bf16 v[28:31], a[172:175], v[88:91], v[28:31]\n\t"
        "buffer_load_dwordx4 v136, s[16:19], s43 offen  lds\n\t"
        "v_readlane_b32 s40, v155, 20\n\t"
        "v_mfma_f32_16x16x32_bf16 v[32:35], a[176:179], v[88:91], v[32:35]\n\t"
        "ds_read_b64_tr_b16 a[254:255], v131 offset:25344\n\t"
        "v_readlane_b32 s43, v155, 8\n\t"
        "v_mfma_f32_16x16x32_bf16 v[36:39], a[144:147], v[88:91], v[36:39]\n\t"
        "ds_read_b128 v[104:107], v130 offset:12288\n\t"
        "s_add_u32 m0, s40, s35\n\t"
        "v_mfma_f32_16x16x32_bf16 v[40:43], a[148:151], v[88:91], v[40:43]\n\t"
        "ds_read_b64_tr_b16 v[188:189], v132 offset:24576\n\t"
        "s_bcnt1_i32_b64 s46, s[64:65]\n\t"
        "v_mfma_f32_16x16x32_bf16 v[44:47], a[152:155], v[88:91], v[44:47]\n\t"
        "buffer_load_dwordx4 v136, s[16:19], s43 offen  lds\n\t"
        "v_readlane_b32 s40, v155, 19\n\t"
        "v_mfma_f32_16x16x32_bf16 v[0:3], a[228:231], v[92:95], v[0:3]\n\t"
        "ds_read_b64_tr_b16 v[190:191], v132 offset:25344\n\t"
        "ds_read_b64_tr_b16 v[192:193], v133 offset:24576\n\t"
        "v_readlane_b32 s43, v155, 7\n\t"
        "v_mfma_f32_16x16x32_bf16 v[4:7], a[232:235], v[92:95], v[4:7]\n\t"
        "ds_read_b64_tr_b16 v[194:195], v133 offset:25344\n\t"
        "s_add_u32 m0, s40, s35\n\t"
        "v_mfma_f32_16x16x32_bf16 v[8:11], a[236:239], v[92:95], v[8:11]\n\t"
        "ds_read_b64_tr_b16 v[196:197], v131 offset:30720\n\t"
        "s_add_u32 s61, s61, s46\n\t"
        "v_mfma_f32_16x16x32_bf16 v[0:3], a[216:219], v[96:99], v[0:3]\n\t"
        "buffer_load_dwordx4 v137, s[16:19], s43 offen  lds\n\t"
        "s_mov_b32 s3, s61\n\t"
        "v_mfma_f32_16x16x32_bf16 v[4:7], a[220:223], v[96:99], v[4:7]\n\t"
        "ds_read_b64_tr_b16 v[198:199], v131 offset:31488\n\t"
        "v_min_u32 v56, 97, v56\n\t"
        "v_mfma_f32_16x16x32_bf16 v[8:11], a[224:227], v[96:99], v[8:11]\n\t"
        "ds_read_b64_tr_b16 v[200:201], v132 offset:30720\n\t"
        "v_lshl_add_u32 v56, v56, 3, s60\n\t"
        "v_mfma_f32_16x16x32_bf16 v[12:15], a[228:231], v[96:99], v[12:15]\n\t"
        "s_mov_b64 exec, s[64:65]\n\t"
        "ds_write_b64 v56, v[52:53]\n\t"
        "s_mov_b64 exec, -1\n\t"
        "v_mfma_f32_16x16x32_bf16 v[16:19], a[232:235], v[96:99], v[16:19]\n\t"
        "s_cmp_gt_u32 s3, 64\n\t"
        "s_cselect_b32 s8, 1, 0\n\t"
        "v_mfma_f32_16x16x32_bf16 v[20:23], a[236:239], v[96:99], v[20:23]\n\t"
        "ds_read_b128 v[108:111], v129 offset:16512\n\t"
        "s_or_b32 s56, s56, s8\n\t"
        "s_waitcnt lgkmcnt(7)\n\t"
        "v_mfma_f32_16x16x32_bf16 v[0:3], a[240:243], v[100:103], v[0:3]\n\t"
        "ds_read_b64_tr_b16 v[202:203], v132 offset:31488\n\t"
        "s_min_u32 s61, s61, 80\n\t"
        "v_mfma_f32_16x16x32_bf16 v[4:7], a[244:247], v[100:103], v[4:7]\n\t"
        "ds_read_b64_tr_b16 v[204:205], v133 offset:30720\n\t"
        "v_min_u32 v78, s61, v116\n\t"
        "v_mfma_f32_16x16x32_bf16 v[8:11], a[248:251], v[100:103], v[8:11]\n\t"
        "ds_read_b64_tr_b16 v[206:207], v133 offset:31488\n\t"
        "v_lshl_add_u32 v78, v78, 3, s60\n\t"
        "v_mfma_f32_16x16x32_bf16 v[12:15], a[216:219], v[100:103], v[12:15]\n\t"
        "ds_read_b64 v[62:63], v78\n\t"
        "v_cmp_ge_u32 s[66:67], s61, v116\n\t"
        "v_mfma_f32_16x16x32_bf16 v[16:19], a[220:223], v[100:103], v[16:19]\n\t"
        "ds_read_b128 v[112:115], v130 offset:64\n\t"
        "s_mov_b32 s46, 0x07060302\n\t"
        "v_mfma_f32_16x16x32_bf16 v[20:23], a[224:227], v[100:103], v[20:23]\n\t"
        "ds_read_b64_tr_b16 v[208:209], v131 offset:36864\n\t"
        "v_mfma_f32_16x16x32_bf16 v[24:27], a[228:231], v[100:103], v[24:27]\n\t"
        "ds_read_b128 v[84:87], v130 offset:4160\n\t"
        "v_mfma_f32_16x16x32_bf16 v[28:31], a[232:235], v[100:103], v[28:31]\n\t"
        "ds_read_b64_tr_b16 v[210:211], v131 offset:37632\n\t"
        "v_mfma_f32_16x16x32_bf16 v[32:35], a[236:239], v[100:103], v[32:35]\n\t"
        "ds_read_b64_tr_b16 v[212:213], v132 offset:36864\n\t"
        "v_mfma_f32_16x16x32_bf16 v[12:15], a[240:243], v[104:107], v[12:15]\n\t"
        "ds_read_b64_tr_b16 v[214:215], v132 offset:37632\n\t"
        "v_mfma_f32_16x16x32_bf16 v[16:19], a[244:247], v[104:107], v[16:19]\n\t"
        "ds_read_b64_tr_b16 v[216:217], v133 offset:36864\n\t"
        "v_mfma_f32_16x16x32_bf16 v[20:23], a[248:251], v[104:107], v[20:23]\n\t"
        "ds_read_b64_tr_b16 v[218:219], v133 offset:37632\n\t"
        "v_mfma_f32_16x16x32_bf16 v[24:27], a[216:219], v[104:107], v[24:27]\n\t"
        "s_mov_b64 exec, s[66:67]\n\t"
        "s_waitcnt lgkmcnt(8)\n\t"
        "ds_write_b32 v63, v117\n\t"
        "s_mov_b64 exec, -1\n\t"
        "v_mfma_f32_16x16x32_bf16 v[28:31], a[220:223], v[104:107], v[28:31]\n\t"
        "ds_read_b128 v[88:91], v130 offset:8256\n\t"
        "v_and_b32 v78, 0xfffff800, v63\n\t"
        "v_lshlrev_b32 v72, 16, v62\n\t"
        "v_mfma_f32_16x16x32_bf16 v[32:35], a[224:227], v[104:107], v[32:35]\n\t"
        "ds_read_b128 v[64:67], v78 offset:1536\n\t"
        "v_and_b32 v73, 0xffff0000, v62\n\t"
        "v_mfma_f32_16x16x32_bf16 v[36:39], a[228:231], v[104:107], v[36:39]\n\t"
        "ds_read_b64_tr_b16 v[220:221], v131 offset:43008\n\t"
        "v_med3_f32 v72, v72, 0, s63\n\t"
        "v_mfma_f32_16x16x32_bf16 v[40:43], a[232:235], v[104:107], v[40:43]\n\t"
        "ds_read_b64_tr_b16 v[222:223], v131 offset:43776\n\t"
        "v_med3_f32 v73, v73, 0, s63\n\t"
        "v_mfma_f32_16x16x32_bf16 v[44:47], a[236:239], v[104:107], v[44:47]\n\t"
        "ds_read_b64_tr_b16 v[224:225], v132 offset:43008\n\t"
        "v_perm_b32 v62, v73, v72, s46\n\t"
        "v_mfma_f32_16x16x32_bf16 v[24:27], a[204:207], v[108:111], v[24:27]\n\t"
        "ds_read_b64_tr_b16 v[226:227], v132 offset:43776\n\t"
        "v_fract_f32 v74, v72\n\t"
        "v_mfma_f32_16x16x32_bf16 v[28:31], a[208:211], v[108:111], v[28:31]\n\t"
        "ds_read_b64_tr_b16 v[228:229], v133 offset:43008\n\t"
        "v_fract_f32 v75, v73\n\t"
        "v_mfma_f32_16x16x32_bf16 v[32:35], a[212:215], v[108:111], v[32:35]\n\t"
        "ds_read_b64_tr_b16 v[230:231], v133 offset:43776\n\t"
        "v_cvt_u32_f32 v76, v72\n\t"
        "v_mfma_f32_16x16x32_bf16 v[36:39], a[180:183], v[108:111], v[36:39]\n\t"
        "ds_read_b128 v[92:95], v130 offset:12352\n\t"
        "v_cvt_u32_f32 v77, v73\n\t"
        "v_cmp_neq_f32 s[64:65], 0, v74\n\t"
        "v_mfma_f32_16x16x32_bf16 v[40:43], a[184:187], v[108:111], v[40:43]\n\t"
        "ds_read_b64_tr_b16 v[232:233], v131 offset:49152\n\t"
        "v_cmp_neq_f32 s[44:45], 0, v75\n\t"
        "v_mfma_f32_16x16x32_bf16 v[44:47], a[188:191], v[108:111], v[44:47]\n\t"
        "ds_read_b64_tr_b16 v[234:235], v131 offset:49920\n\t"
        "v_sub_f32 v78, 1.0, v74\n\t"
        "s_waitcnt lgkmcnt(9)\n\t"
        "v_mfma_f32_16x16x32_bf16 v[0:3], v[196:199], v[112:115], v[0:3]\n\t"
        "ds_read_b64_tr_b16 v[236:237], v132 offset:49152\n\t"
        "v_sub_f32 v79, 1.0, v75\n\t"
        "v_mfma_f32_16x16x32_bf16 v[4:7], v[200:203], v[112:115], v[4:7]\n\t"
        "ds_read_b64_tr_b16 v[238:239], v132 offset:49920\n\t"
        "v_cvt_pk_bf16_f32 v79, v78, v79\n\t"
        "v_mfma_f32_16x16x32_bf16 v[8:11], v[204:207], v[112:115], v[8:11]\n\t"
        "ds_read_b64_tr_b16 v[240:241], v133 offset:49152\n\t"
        "v_lshlrev_b32 v78, 16, v79\n\t"
        "v_mfma_f32_16x16x32_bf16 v[0:3], a[252:255], v[84:87], v[0:3]\n\t"
        "ds_read_b64_tr_b16 v[242:243], v133 offset:49920\n\t"
        "v_and_b32 v79, 0xffff0000, v79\n\t"
        "v_mfma_f32_16x16x32_bf16 v[4:7], v[188:191], v[84:87], v[4:7]\n\t"
        "ds_read_b64_tr_b16 v[244:245], v131 offset:55296\n\t"
        "v_mul_f32 v80, v78, v79\n\t"
        "v_mfma_f32_16x16x32_bf16 v[8:11], v[192:195], v[84:87], v[8:11]\n\t"
        "ds_read_b64_tr_b16 v[246:247], v131 offset:56064\n\t"
        "v_mul_f32 v78, v78, v75\n\t"
        "v_mfma_f32_16x16x32_bf16 v[12:15], v[196:199], v[84:87], v[12:15]\n\t"
        "ds_read_b128 v[96:99], v130 offset:128\n\t"
        "v_mul_f32 v79, v74, v79\n\t"
        "v_mfma_f32_16x16x32_bf16 v[16:19], v[200:203], v[84:87], v[16:19]\n\t"
        "ds_read_b64_tr_b16 v[248:249], v132 offset:55296\n\t"
        "v_mul_f32 v81, v74, v75\n\t"
        "v_mfma_f32_16x16x32_bf16 v[20:23], v[204:207], v[84:87], v[20:23]\n\t"
        "ds_read_b64_tr_b16 v[250:251], v132 offset:56064\n\t"
        "v_cndmask_b32 v78, v79, v78, s[44:45]\n\t"
        "v_mfma_f32_16x16x32_bf16 v[0:3], v[208:211], v[88:91], v[0:3]\n\t"
        "ds_read_b128 v[100:103], v130 offset:4224\n\t"
        "v_cndmask_b32 v79, 0, v79, s[44:45]\n\t"
        "v_mfma_f32_16x16x32_bf16 v[4:7], v[212:215], v[88:91], v[4:7]\n\t"
        "ds_read_b64_tr_b16 v[252:253], v133 offset:55296\n\t"
        "v_cvt_pk_bf16_f32 v70, v80, v78\n\t"
        "v_mfma_f32_16x16x32_bf16 v[8:11], v[216:219], v[88:91], v[8:11]\n\t"
        "ds_read_b64_tr_b16 v[254:255], v133 offset:56064\n\t"
        "v_cvt_pk_bf16_f32 v71, v79, v81\n\t"
        "v_mfma_f32_16x16x32_bf16 v[12:15], a[252:255], v[88:91], v[12:15]\n\t"
        "v_pk_max_u16 v78, v62, v64\n\t"
        "v_pk_min_u16 v78, v78, v65\n\t"
        "v_mfma_f32_16x16x32_bf16 v[16:19], v[188:191], v[88:91], v[16:19]\n\t"
        "v_cmp_ne_u32 vcc, v78, v62\n\t"
        "v_sub_u32 v78, v77, v67\n\t"
        "v_mfma_f32_16x16x32_bf16 v[20:23], v[192:195], v[88:91], v[20:23]\n\t"
        "v_mad_u32_u24 v79, v77, 6, v66\n\t"
        "v_mad_u32_u24 v79, v76, s62, v79\n\t"
        "v_mfma_f32_16x16x32_bf16 v[24:27], v[196:199], v[88:91], v[24:27]\n\t"
        "ds_read_b128 v[104:107], v130 offset:8320\n\t"
        "s_and_b64 vcc, vcc, s[66:67]\n\t"
        "v_mfma_f32_16x16x32_bf16 v[28:31], v[200:203], v[88:91], v[28:31]\n\t"
        "s_or_b64 s[56:57], s[56:57], vcc\n\t"
        "v_bfe_u32 v80, v78, 2, 1\n\t"
        "v_mfma_f32_16x16x32_bf16 v[32:35], v[204:207], v[88:91], v[32:35]\n\t"
        "v_or_b32 v80, v80, v79\n\t"
        "v_add_u32 v78, 1, v78\n\t"
        "s_waitcnt lgkmcnt(5)\n\t"
        "v_mfma_f32_16x16x32_bf16 v[12:15], v[208:211], v[92:95], v[12:15]\n\t"
        "v_bfe_u32 v78, v78, 2, 1\n\t"
        "v_add3_u32 v78, v79, v78, 6\n\t"
        "v_mfma_f32_16x16x32_bf16 v[16:19], v[212:215], v[92:95], v[16:19]\n\t"
        "v_cndmask_b32 v78, v80, v78, s[44:45]\n\t"
        "v_cndmask_b32 v79, 0, v119, s[64:65]\n\t"
        "v_mfma_f32_16x16x32_bf16 v[20:23], v[216:219], v[92:95], v[20:23]\n\t"
        "v_add_u32 v81, v80, v79\n\t"
        "v_add_u32 v82, v78, v79\n\t"
        "v_mfma_f32_16x16x32_bf16 v[24:27], a[252:255], v[92:95], v[24:27]\n\t"
        "v_cndmask_b32 v78, v81, v78, s[44:45]\n\t"
        "v_lshl_or_b32 v68, v78, 16, v80\n\t"
        "v_mfma_f32_16x16x32_bf16 v[28:31], v[188:191], v[92:95], v[28:31]\n\t"
        "v_lshl_or_b32 v69, v82, 16, v81\n\t"
        "v_mfma_f32_16x16x32_bf16 v[32:35], v[192:195], v[92:95], v[32:35]\n\t"
        "ds_write_b128 v118, v[68:71]\n\t"
        "v_mfma_f32_16x16x32_bf16 v[36:39], v[196:199], v[92:95], v[36:39]\n\t"
        "s_cmp_gt_u32 s3, 16\n\t"
        "s_cselect_b32 s8, 2, 0\n\t"
        "v_mfma_f32_16x16x32_bf16 v[40:43], v[200:203], v[92:95], v[40:43]\n\t"
        "s_cmp_lg_u64 s[56:57], 0\n\t"
        "s_addc_u32 s8, s8, s6\n\t"
        "v_mfma_f32_16x16x32_bf16 v[44:47], v[204:207], v[92:95], v[44:47]\n\t"
        "ds_read_b128 v[160:163], v185 offset:0\n\t"
        "v_mov_b32 v151, s8\n\t"
        "v_mfma_f32_16x16x32_bf16 v[0:3], v[232:235], v[96:99], v[0:3]\n\t"
        "s_waitcnt vmcnt(0) lgkmcnt(2)\n\t"
        "s_barrier\n\t"
        "v_mfma_f32_16x16x32_bf16 v[4:7], v[236:239], v[96:99], v[4:7]\n\t"
        "s_mov_b64 exec, 1\n\t"
        "ds_write_b32 v153, v151\n\t"
        "s_mov_b64 exec, -1\n\t"
        "v_mfma_f32_16x16x32_bf16 v[8:11], v[240:243], v[96:99], v[8:11]\n\t"
        "ds_read_b128 v[108:111], v130 offset:12416\n\t"
        "v_xor_b32 v154, 32, v186\n\t"
        "v_mfma_f32_16x16x32_bf16 v[0:3], v[220:223], v[100:103], v[0:3]\n\t"
        "v_mfma_f32_16x16x32_bf16 v[4:7], v[224:227], v[100:103], v[4:7]\n\t"
        "v_mfma_f32_16x16x32_bf16 v[8:11], v[228:231], v[100:103], v[8:11]\n\t"
        "v_mfma_f32_16x16x32_bf16 v[12:15], v[232:235], v[100:103], v[12:15]\n\t"
        "ds_read_b128 v[112:115], v130 offset:16384\n\t"
        "v_mfma_f32_16x16x32_bf16 v[16:19], v[236:239], v[100:103], v[16:19]\n\t"
        "v_mfma_f32_16x16x32_bf16 v[20:23], v[240:243], v[100:103], v[20:23]\n\t"
        "s_waitcnt lgkmcnt(3)\n\t"
        "v_lshlrev_b32_sdwa v157, 5, v160 dst_sel:DWORD dst_unused:UNUSED_PAD src0_sel:DWORD src1_sel:WORD_0\n\t"
        "v_lshlrev_b32_sdwa v158, 5, v160 dst_sel:DWORD dst_unused:UNUSED_PAD src0_sel:DWORD src1_sel:WORD_1\n\t"
        "v_mfma_f32_16x16x32_bf16 v[0:3], v[244:247], v[104:107], v[0:3]\n\t"
        "v_or_b32 v157, v184, v157\n\t"
        "v_or_b32 v158, v184, v158\n\t"
        "v_mfma_f32_16x16x32_bf16 v[4:7], v[248:251], v[104:107], v[4:7]\n\t"
        "ds_read_b64_tr_b16 v[60:61], v157 offset:0\n\t"
        "v_xor_b32 v159, 32, v157\n\t"
        "v_mfma_f32_16x16x32_bf16 v[8:11], v[252:255], v[104:107], v[8:11]\n\t"
        "ds_read_b64_tr_b16 v[62:63], v158 offset:0\n\t"
        "v_xor_b32 v152, 32, v158\n\t"
        "v_mfma_f32_16x16x32_bf16 v[12:15], v[220:223], v[104:107], v[12:15]\n\t"
        "ds_read_b64_tr_b16 v[64:65], v159 offset:0\n\t"
        "v_perm_b32 v164, v162, v162, v168\n\t"
        "v_mfma_f32_16x16x32_bf16 v[16:19], v[224:227], v[104:107], v[16:19]\n\t"
        "ds_read_b64_tr_b16 v[66:67], v152 offset:0\n\t"
        "v_perm_b32 v165, v162, v162, v169\n\t"
        "v_mfma_f32_16x16x32_bf16 v[20:23], v[228:231], v[104:107], v[20:23]\n\t"
        "ds_read_b64_tr_b16 v[68:69], v157 offset:64\n\t"
        "v_perm_b32 v166, v162, v162, v170\n\t"
        "v_mfma_f32_16x16x32_bf16 v[24:27], v[232:235], v[104:107], v[24:27]\n\t"
        "ds_read_b64_tr_b16 v[70:71], v158 offset:64\n\t"
        "v_perm_b32 v167, v162, v162, v171\n\t"
        "v_mfma_f32_16x16x32_bf16 v[28:31], v[236:239], v[104:107], v[28:31]\n\t"
        "ds_read_b64_tr_b16 v[72:73], v159 offset:64\n\t"
        "v_cmp_ne_u32 vcc, 0, v163\n\t"
        "v_mfma_f32_16x16x32_bf16 v[32:35], v[240:243], v[104:107], v[32:35]\n\t"
        "ds_read_b128 v[84:87], v130 offset:16448\n\t"
        "s_waitcnt lgkmcnt(8)\n\t"
        "v_mfma_f32_16x16x32_bf16 v[12:15], v[244:247], v[108:111], v[12:15]\n\t"
        "ds_read_b64_tr_b16 v[74:75], v152 offset:64\n\t"
        "v_mfma_f32_16x16x32_bf16 v[16:19], v[248:251], v[108:111], v[16:19]\n\t"
        "ds_read_b64_tr_b16 v[76:77], v157 offset:128\n\t"
        "v_mfma_f32_16x16x32_bf16 v[20:23], v[252:255], v[108:111], v[20:23]\n\t"
        "ds_read_b64_tr_b16 v[78:79], v158 offset:128\n\t"
        "s_waitcnt lgkmcnt(9)\n\t"
        "v_mfma_f32_16x16x32_bf16 v[48:51], v[60:63], v[164:167], 0\n\t"
        "ds_read_b64_tr_b16 v[80:81], v159 offset:128\n\t"
        "v_mfma_f32_16x16x32_bf16 v[24:27], v[220:223], v[108:111], v[24:27]\n\t"
        "ds_read_b64_tr_b16 v[82:83], v152 offset:128\n\t"
        "s_waitcnt lgkmcnt(9)\n\t"
        "v_mfma_f32_16x16x32_bf16 v[52:55], v[64:67], v[164:167], 0\n\t"
        ".LpS1%=:\n\t"
        "s_cbranch_execz .LrS%=\n\t"
        "ds_read_b128 v[88:91], v130 offset:16512\n\t"
        "v_mfma_f32_16x16x32_bf16 v[28:31], v[224:227], v[108:111], v[28:31]\n\t"
        "s_waitcnt lgkmcnt(8)\n\t"
        "v_mfma_f32_16x16x32_bf16 v[56:59], v[68:71], v[164:167], 0\n\t"
        "v_mfma_f32_16x16x32_bf16 v[32:35], v[228:231], v[108:111], v[32:35]\n\t"
        "v_mfma_f32_16x16x32_bf16 v[36:39], v[232:235], v[108:111], v[36:39]\n\t"
        "ds_read_b128 v[92:95], v128 offset:20480\n\t"
        "s_waitcnt lgkmcnt(6)\n\t"
        "v_mfma_f32_16x16x32_bf16 v[116:119], v[72:75], v[164:167], 0\n\t"
        "v_mfma_f32_16x16x32_bf16 v[40:43], v[236:239], v[108:111], v[40:43]\n\t"
        "s_waitcnt lgkmcnt(4)\n\t"
        "v_mfma_f32_16x16x32_bf16 v[120:123], v[76:79], v[164:167], 0\n\t"
        "v_mfma_f32_16x16x32_bf16 v[44:47], v[240:243], v[108:111], v[44:47]\n\t"
        "s_waitcnt lgkmcnt(2)\n\t"
        "v_mfma_f32_16x16x32_bf16 v[124:127], v[80:83], v[164:167], 0\n\t"
        "s_cbranch_vccz .Lx58%=\n\t"
        "v_lshlrev_b32_sdwa v157, 5, v161 dst_sel:DWORD dst_unused:UNUSED_PAD src0_sel:DWORD src1_sel:WORD_0\n\t"
        "v_lshlrev_b32_sdwa v158, 5, v161 dst_sel:DWORD dst_unused:UNUSED_PAD src0_sel:DWORD src1_sel:WORD_1\n\t"
        "v_or_b32 v157, v184, v157\n\t"
        "v_or_b32 v158, v184, v158\n\t"
        "v_xor_b32 v159, 32, v157\n\t"
        "v_xor_b32 v152, 32, v158\n\t"
        "ds_read_b64_tr_b16 v[60:61], v157 offset:0\n\t"
        "ds_read_b64_tr_b16 v[62:63], v158 offset:0\n\t"
        "ds_read_b64_tr_b16 v[64:65], v159 offset:0\n\t"
        "ds_read_b64_tr_b16 v[66:67], v152 offset:0\n\t"
        "ds_read_b64_tr_b16 v[68:69], v157 offset:64\n\t"
        "ds_read_b64_tr_b16 v[70:71], v158 offset:64\n\t"
        "ds_read_b64_tr_b16 v[72:73], v159 offset:64\n\t"
        "ds_read_b64_tr_b16 v[74:75], v152 offset:64\n\t"
        "ds_read_b64_tr_b16 v[76:77], v157 offset:128\n\t"
        "ds_read_b64_tr_b16 v[78:79], v158 offset:128\n\t"
        "ds_read_b64_tr_b16 v[80:81], v159 offset:128\n\t"
        "ds_read_b64_tr_b16 v[82:83], v152 offset:128\n\t"
        "v_perm_b32 v164, v163, v163, v168\n\t"
        "v_perm_b32 v165, v163, v163, v169\n\t"
        "v_perm_b32 v166, v163, v163, v170\n\t"
        "v_perm_b32 v167, v163, v163, v171\n\t"
        "s_waitcnt lgkmcnt(10)\n\t"
        "s_nop 0\n\t"
        "v_mfma_f32_16x16x32_bf16 v[48:51], v[60:63], v[164:167], v[48:51]\n\t"
        "s_waitcnt lgkmcnt(8)\n\t"
        "v_mfma_f32_16x16x32_bf16 v[52:55], v[64:67], v[164:167], v[52:55]\n\t"
        "s_waitcnt lgkmcnt(6)\n\t"
        "v_mfma_f32_16x16x32_bf16 v[56:59], v[68:71], v[164:167], v[56:59]\n\t"
        "s_waitcnt lgkmcnt(4)\n\t"
        "v_mfma_f32_16x16x32_bf16 v[116:119], v[72:75], v[164:167], v[116:119]\n\t"
        "s_waitcnt lgkmcnt(2)\n\t"
        "v_mfma_f32_16x16x32_bf16 v[120:123], v[76:79], v[164:167], v[120:123]\n\t"
        "s_waitcnt lgkmcnt(0)\n\t"
        "v_mfma_f32_16x16x32_bf16 v[124:127], v[80:83], v[164:167], v[124:127]\n\t"
        "s_nop 4\n\t"
        ".Lx58%=:\n\t"
        "v_cvt_pk_bf16_f32 v48, v48, v49\n\t"
        "v_cvt_pk_bf16_f32 v49, v50, v51\n\t"
        "v_mfma_f32_16x16x32_bf16 v[24:27], a[240:243], v[112:115], v[24:27]\n\t"
        "ds_write_b64 v186, v[48:49] offset:0\n\t"
        "v_mfma_f32_16x16x32_bf16 v[28:31], a[244:247], v[112:115], v[28:31]\n\t"
        "ds_read_b128 v[96:99], v128 offset:20544\n\t"
        "v_cvt_pk_bf16_f32 v52, v52, v53\n\t"
        "v_cvt_pk_bf16_f32 v53, v54, v55\n\t"
        "v_mfma_f32_16x16x32_bf16 v[32:35], a[248:251], v[112:115], v[32:35]\n\t"
        "ds_write_b64 v154, v[52:53] offset:0\n\t"
        "v_mfma_f32_16x16x32_bf16 v[36:39], a[216:219], v[112:115], v[36:39]\n\t"
        "ds_read_b128 v[100:103], v128 offset:20608\n\t"
        "v_cvt_pk_bf16_f32 v56, v56, v57\n\t"
        "v_cvt_pk_bf16_f32 v57, v58, v59\n\t"
        "v_mfma_f32_16x16x32_bf16 v[40:43], a[220:223], v[112:115], v[40:43]\n\t"
        "ds_write_b64 v186, v[56:57] offset:64\n\t"
        "v_mfma_f32_16x16x32_bf16 v[44:47], a[224:227], v[112:115], v[44:47]\n\t"
        "v_cvt_pk_bf16_f32 v116, v116, v117\n\t"
        "v_cvt_pk_bf16_f32 v117, v118, v119\n\t"
        "v_mfma_f32_16x16x32_bf16 v[24:27], v[208:211], v[84:87], v[24:27]\n\t"
        "ds_write_b64 v154, v[116:117] offset:64\n\t"
        "v_mfma_f32_16x16x32_bf16 v[28:31], v[212:215], v[84:87], v[28:31]\n\t"
        "ds_read_b128 v[104:107], v129 offset:20480\n\t"
        "v_cvt_pk_bf16_f32 v120, v120, v121\n\t"
        "v_mfma_f32_16x16x32_bf16 v[32:35], v[216:219], v[84:87], v[32:35]\n\t"
        "v_cvt_pk_bf16_f32 v121, v122, v123\n\t"
        "v_cvt_pk_bf16_f32 v124, v124, v125\n\t"
        "v_cvt_pk_bf16_f32 v125, v126, v127\n\t"
        "v_mfma_f32_16x16x32_bf16 v[36:39], a[252:255], v[84:87], v[36:39]\n\t"
        "ds_write_b64 v186, v[120:121] offset:128\n\t"
        "v_mfma_f32_16x16x32_bf16 v[40:43], v[188:191], v[84:87], v[40:43]\n\t"
        "ds_write_b64 v154, v[124:125] offset:128\n\t"
        "v_mfma_f32_16x16x32_bf16 v[44:47], v[192:195], v[84:87], v[44:47]\n\t"
        "ds_read_b128 v[108:111], v129 offset:20544\n\t"
        "s_waitcnt lgkmcnt(6)\n\t"
        "v_mfma_f32_16x16x32_bf16 v[24:27], v[244:247], v[88:91], v[24:27]\n\t"
        "ds_read_b128 v[112:115], v129 offset:20608\n\t"
        "v_mfma_f32_16x16x32_bf16 v[28:31], v[248:251], v[88:91], v[28:31]\n\t"
        "v_mfma_f32_16x16x32_bf16 v[32:35], v[252:255], v[88:91], v[32:35]\n\t"
        "v_mfma_f32_16x16x32_bf16 v[36:39], v[220:223], v[88:91], v[36:39]\n\t"
        "ds_read_b128 v[84:87], v130 offset:20480\n\t"
        "v_mfma_f32_16x16x32_bf16 v[40:43], v[224:227], v[88:91], v[40:43]\n\t"
        "s_waitcnt lgkmcnt(3)\n\t"
        "s_barrier\n\t"
        "v_mfma_f32_16x16x32_bf16 v[44:47], v[228:231], v[88:91], v[44:47]\n\t"
        "ds_read_b128 v[124:127], v182 offset:0\n\t"
        "v_mfma_f32_16x16x32_bf16 v[36:39], a[24:27], v[92:95], v[36:39]\n\t"
        "ds_read_b128 v[132:135], v187 offset:0\n\t"
        "v_mfma_f32_16x16x32_bf16 v[40:43], a[28:31], v[92:95], v[40:43]\n\t"
        "ds_read_b128 v[136:139], v187 offset:16\n\t"
        "v_mfma_f32_16x16x32_bf16 v[44:47], a[32:35], v[92:95], v[44:47]\n\t"
        "ds_read_b32 v140, v187 offset:32\n\t"
        "v_mfma_f32_16x16x32_bf16 v[36:39], a[60:63], v[96:99], v[36:39]\n\t"
        "ds_read_b128 v[88:91], v130 offset:20544\n\t"
        "v_mfma_f32_16x16x32_bf16 v[40:43], a[64:67], v[96:99], v[40:43]\n\t"
        "ds_read_b128 v[92:95], v130 offset:20608\n\t"
        "v_mfma_f32_16x16x32_bf16 v[44:47], a[68:71], v[96:99], v[44:47]\n\t"
        "v_mfma_f32_16x16x32_bf16 v[36:39], a[96:99], v[100:103], v[36:39]\n\t"
        "v_mfma_f32_16x16x32_bf16 v[40:43], a[100:103], v[100:103], v[40:43]\n\t"
        "v_mfma_f32_16x16x32_bf16 v[44:47], a[104:107], v[100:103], v[44:47]\n\t"
        "s_waitcnt lgkmcnt(5)\n\t"
        "v_or3_b32 v124, v124, v125, v126\n\t"
        "v_or_b32 v124, v124, v127\n\t"
        "v_mfma_f32_16x16x32_bf16 v[36:39], a[132:135], v[104:107], v[36:39]\n\t"
        "v_readfirstlane_b32 s6, v124\n\t"
        "v_mfma_f32_16x16x32_bf16 v[40:43], a[136:139], v[104:107], v[40:43]\n\t"
        "s_nop 0\n\t"
        "s_bitcmp1_b32 s6, 1\n\t"
        "s_cbranch_scc1 .Lx60%=\n\t"
        ".Lx61%=:\n\t"
        "v_mfma_f32_16x16x32_bf16 v[44:47], a[140:143], v[104:107], v[44:47]\n\t"
        "s_waitcnt lgkmcnt(3)\n\t"
        "v_xor_b32 v132, v181, v132\n\t"
        "v_add_u32 v151, s9, v172\n\t"
        "v_mfma_f32_16x16x32_bf16 v[36:39], a[168:171], v[108:111], v[36:39]\n\t"
        "v_max_u32 v151, v151, v132\n\t"
        "v_xor_b32 v133, v181, v133\n\t"
        "v_mfma_f32_16x16x32_bf16 v[40:43], a[172:175], v[108:111], v[40:43]\n\t"
        "ds_read_b128 v[96:99], v151 offset:0\n\t"
        "v_add_u32 v183, s9, v173\n\t"
        "v_mfma_f32_16x16x32_bf16 v[44:47], a[176:179], v[108:111], v[44:47]\n\t"
        "ds_read_b128 v[100:103], v151 offset:64\n\t"
        "v_max_u32 v183, v183, v133\n\t"
        "v_mfma_f32_16x16x32_bf16 v[36:39], a[204:207], v[112:115], v[36:39]\n\t"
        "ds_read_b128 v[104:107], v151 offset:128\n\t"
        "v_xor_b32 v134, v181, v134\n\t"
        "v_mfma_f32_16x16x32_bf16 v[40:43], a[208:211], v[112:115], v[40:43]\n\t"
        "ds_read_b128 v[108:111], v183 offset:0\n\t"
        "v_add_u32 v151, s9, v174\n\t"
        "v_mfma_f32_16x16x32_bf16 v[44:47], a[212:215], v[112:115], v[44:47]\n\t"
        "v_max_u32 v151, v151, v134\n\t"
        "v_xor_b32 v135, v181, v135\n\t"
        "v_mfma_f32_16x16x32_bf16 v[36:39], a[240:243], v[84:87], v[36:39]\n\t"
        "ds_read_b128 v[112:115], v183 offset:64\n\t"
        "s_add_u32 s55, s41, 0\n\t"
        "v_mfma_f32_16x16x32_bf16 v[40:43], a[244:247], v[84:87], v[40:43]\n\t"
        "v_cvt_pk_bf16_f32 v116, v0, v1\n\t"
        "v_cvt_pk_bf16_f32 v117, v2, v3\n\t"
        "v_mfma_f32_16x16x32_bf16 v[44:47], a[248:251], v[84:87], v[44:47]\n\t"
        "v_cvt_pk_bf16_f32 v118, v4, v5\n\t"
        "v_cvt_pk_bf16_f32 v119, v6, v7\n\t"
        "s_waitcnt lgkmcnt(5)\n\t"
        "v_mfma_f32_16x16x32_bf16 v[36:39], v[208:211], v[88:91], v[36:39]\n\t"
        "ds_read_b128 v[84:87], v183 offset:128\n\t"
        "v_add_u32 v183, s9, v175\n\t"
        "v_mfma_f32_16x16x32_bf16 v[40:43], v[212:215], v[88:91], v[40:43]\n\t"
        "v_max_u32 v183, v183, v135\n\t"
        "v_xor_b32 v136, v181, v136\n\t"
        "v_mfma_f32_16x16x32_bf16 v[44:47], v[216:219], v[88:91], v[44:47]\n\t"
        "ds_read_b128 v[132:135], v187 offset:2048\n\t"
        "v_cvt_pk_bf16_f32 v120, v8, v9\n\t"
        "v_mfma_f32_16x16x32_bf16 v[36:39], v[244:247], v[92:95], v[36:39]\n\t"
        "ds_read_b128 v[88:91], v151 offset:0\n\t"
        "v_cvt_pk_bf16_f32 v121, v10, v11\n\t"
        "v_mfma_f32_16x16x32_bf16 v[40:43], v[248:251], v[92:95], v[40:43]\n\t"
        "v_permlane16_swap_b32 v116, v118\n\t"
        "v_mfma_f32_16x16x32_bf16 v[44:47], v[252:255], v[92:95], v[44:47]\n\t"
        "v_permlane16_swap_b32 v117, v119\n\t"
        "s_waitcnt lgkmcnt(4)\n\t"
        "v_mfma_f32_16x16x32_bf16 v[48:51], a[12:15], v[96:99], 0\n\t"
        "buffer_store_dwordx4 v[116:119], v144, s[28:31], s55 offen sc1\n\t"
        "v_mfma_f32_16x16x32_bf16 v[52:55], a[16:19], v[96:99], 0\n\t"
        "ds_write_b64 v146, v[120:121] offset:0\n\t"
        "v_mfma_f32_16x16x32_bf16 v[56:59], a[20:23], v[96:99], 0\n\t"
        "ds_read_b128 v[92:95], v151 offset:64\n\t"
        "s_add_u32 s55, s41, 43008\n\t"
        "v_mfma_f32_16x16x32_bf16 v[48:51], a[48:51], v[100:103], v[48:51]\n\t"
        "ds_read_b128 v[96:99], v151 offset:128\n\t"
        "v_add_u32 v151, s9, v176\n\t"
        "v_mfma_f32_16x16x32_bf16 v[52:55], a[52:55], v[100:103], v[52:55]\n\t"
        "v_max_u32 v151, v151, v136\n\t"
        "v_xor_b32 v137, v181, v137\n\t"
        "v_mfma_f32_16x16x32_bf16 v[56:59], a[56:59], v[100:103], v[56:59]\n\t"
        "v_cvt_pk_bf16_f32 v116, v12, v13\n\t"
        "v_cvt_pk_bf16_f32 v117, v14, v15\n\t"
        "v_mfma_f32_16x16x32_bf16 v[48:51], a[84:87], v[104:107], v[48:51]\n\t"
        "ds_read_b128 v[100:103], v183 offset:0\n\t"
        "v_cvt_pk_bf16_f32 v118, v16, v17\n\t"
        "v_mfma_f32_16x16x32_bf16 v[52:55], a[88:91], v[104:107], v[52:55]\n\t"
        "v_cvt_pk_bf16_f32 v119, v18, v19\n\t"
        "v_cvt_pk_bf16_f32 v120, v20, v21\n\t"
        "v_mfma_f32_16x16x32_bf16 v[56:59], a[92:95], v[104:107], v[56:59]\n\t"
        "v_cvt_pk_bf16_f32 v121, v22, v23\n\t"
        "v_mfma_f32_16x16x32_bf16 v[48:51], a[120:123], v[108:111], v[48:51]\n\t"
        "v_permlane16_swap_b32 v116, v118\n\t"
        "v_mfma_f32_16x16x32_bf16 v[52:55], a[124:127], v[108:111], v[52:55]\n\t"
        "v_permlane16_swap_b32 v117, v119\n\t"
        "v_mfma_f32_16x16x32_bf16 v[56:59], a[128:131], v[108:111], v[56:59]\n\t"
        "ds_read_b128 v[104:107], v183 offset:64\n\t"
        "s_waitcnt lgkmcnt(4)\n\t"
        "v_mfma_f32_16x16x32_bf16 v[48:51], a[156:159], v[112:115], v[48:51]\n\t"
        "ds_read_b128 v[108:111], v183 offset:128\n\t"
        "v_add_u32 v183, s9, v177\n\t"
        "v_mfma_f32_16x16x32_bf16 v[52:55], a[160:163], v[112:115], v[52:55]\n\t"
        "v_max_u32 v183, v183, v137\n\t"
        "v_xor_b32 v138, v181, v138\n\t"
        "v_mfma_f32_16x16x32_bf16 v[56:59], a[164:167], v[112:115], v[56:59]\n\t"
        "buffer_store_dwordx4 v[116:119], v144, s[28:31], s55 offen sc1\n\t"
        "v_mfma_f32_16x16x32_bf16 v[48:51], a[192:195], v[84:87], v[48:51]\n\t"
        "ds_write_b64 v146, v[120:121] offset:2048\n\t"
        "v_mfma_f32_16x16x32_bf16 v[52:55], a[196:199], v[84:87], v[52:55]\n\t"
        "ds_read_b128 v[112:115], v151 offset:0\n\t"
        "s_add_u32 s55, s41, 86016\n\t"
        "v_mfma_f32_16x16x32_bf16 v[56:59], a[200:203], v[84:87], v[56:59]\n\t"
        "v_cvt_pk_bf16_f32 v116, v24, v25\n\t"
        "v_cvt_pk_bf16_f32 v117, v26, v27\n\t"
        "v_mfma_f32_16x16x32_bf16 v[48:51], a[228:231], v[88:91], v[48:51]\n\t"
        "ds_read_b128 v[84:87], v151 offset:64\n\t"
        "v_cvt_pk_bf16_f32 v118, v28, v29\n\t"
        "v_mfma_f32_16x16x32_bf16 v[52:55], a[232:235], v[88:91], v[52:55]\n\t"
        "v_cvt_pk_bf16_f32 v119, v30, v31\n\t"
        "v_cvt_pk_bf16_f32 v120, v32, v33\n\t"
        "v_mfma_f32_16x16x32_bf16 v[56:59], a[236:239], v[88:91], v[56:59]\n\t"
        "v_cvt_pk_bf16_f32 v121, v34, v35\n\t"
        "s_waitcnt lgkmcnt(4)\n\t"
        "v_mfma_f32_16x16x32_bf16 v[48:51], v[196:199], v[92:95], v[48:51]\n\t"
        "v_permlane16_swap_b32 v116, v118\n\t"
        "v_mfma_f32_16x16x32_bf16 v[52:55], v[200:203], v[92:95], v[52:55]\n\t"
        "v_permlane16_swap_b32 v117, v119\n\t"
        "v_mfma_f32_16x16x32_bf16 v[56:59], v[204:207], v[92:95], v[56:59]\n\t"
        "ds_read_b128 v[88:91], v151 offset:128\n\t"
        "v_add_u32 v151, s9, v178\n\t"
        "v_mfma_f32_16x16x32_bf16 v[48:51], v[232:235], v[96:99], v[48:51]\n\t"
        "ds_read_b128 v[92:95], v183 offset:0\n\t"
        "v_max_u32 v151, v151, v138\n\t"
        "v_mfma_f32_16x16x32_bf16 v[52:55], v[236:239], v[96:99], v[52:55]\n\t"
        "v_xor_b32 v139, v181, v139\n\t"
        "v_mfma_f32_16x16x32_bf16 v[56:59], v[240:243], v[96:99], v[56:59]\n\t"
        "v_mfma_f32_16x16x32_bf16 v[48:51], a[0:3], v[100:103], v[48:51]\n\t"
        "ds_read_b128 v[96:99], v183 offset:64\n\t"
        "v_mfma_f32_16x16x32_bf16 v[52:55], a[4:7], v[100:103], v[52:55]\n\t"
        "buffer_store_dwordx4 v[116:119], v144, s[28:31], s55 offen sc1\n\t"
        "v_mfma_f32_16x16x32_bf16 v[56:59], a[8:11], v[100:103], v[56:59]\n\t"
        "ds_write_b64 v146, v[120:121] offset:4096\n\t"
        "v_mfma_f32_16x16x32_bf16 v[48:51], a[36:39], v[104:107], v[48:51]\n\t"
        "ds_read_b128 v[100:103], v183 offset:128\n\t"
        "v_add_u32 v183, s9, v179\n\t"
        "v_mfma_f32_16x16x32_bf16 v[52:55], a[40:43], v[104:107], v[52:55]\n\t"
        "v_max_u32 v183, v183, v139\n\t"
        "v_xor_b32 v140, v181, v140\n\t"
        "v_mfma_f32_16x16x32_bf16 v[56:59], a[44:47], v[104:107], v[56:59]\n\t"
        "ds_read_b128 v[136:139], v187 offset:2064\n\t"
        "s_add_u32 s55, s41, 129024\n\t"
        "s_waitcnt lgkmcnt(5)\n\t"
        "v_mfma_f32_16x16x32_bf16 v[48:51], a[72:75], v[108:111], v[48:51]\n\t"
        "ds_read_b128 v[104:107], v151 offset:0\n\t"
        "v_cvt_pk_bf16_f32 v116, v36, v37\n\t"
        "v_mfma_f32_16x16x32_bf16 v[52:55], a[76:79], v[108:111], v[52:55]\n\t"
        "v_cvt_pk_bf16_f32 v117, v38, v39\n\t"
        "v_cvt_pk_bf16_f32 v118, v40, v41\n\t"
        "v_mfma_f32_16x16x32_bf16 v[56:59], a[80:83], v[108:111], v[56:59]\n\t"
        "v_cvt_pk_bf16_f32 v119, v42, v43\n\t"
        "v_cvt_pk_bf16_f32 v120, v44, v45\n\t"
        "v_mfma_f32_16x16x32_bf16 v[48:51], a[108:111], v[112:115], v[48:51]\n\t"
        "ds_read_b128 v[108:111], v151 offset:64\n\t"
        "v_cvt_pk_bf16_f32 v121, v46, v47\n\t"
        "v_mfma_f32_16x16x32_bf16 v[52:55], a[112:115], v[112:115], v[52:55]\n\t"
        "v_permlane16_swap_b32 v116, v118\n\t"
        "v_mfma_f32_16x16x32_bf16 v[56:59], a[116:119], v[112:115], v[56:59]\n\t"
        "v_permlane16_swap_b32 v117, v119\n\t"
        "v_mfma_f32_16x16x32_bf16 v[48:51], a[144:147], v[84:87], v[48:51]\n\t"
        "ds_read_b128 v[112:115], v151 offset:128\n\t"
        "v_add_u32 v151, s9, v180\n\t"
        "v_mfma_f32_16x16x32_bf16 v[52:55], a[148:151], v[84:87], v[52:55]\n\t"
        "v_max_u32 v151, v151, v140\n\t"
        "v_xor_b32 v132, v181, v132\n\t"
        "v_mfma_f32_16x16x32_bf16 v[56:59], a[152:155], v[84:87], v[56:59]\n\t"
        "ds_read_b32 v140, v187 offset:2080\n\t"
        "v_mfma_f32_16x16x32_bf16 v[48:51], a[180:183], v[88:91], v[48:51]\n\t"
        "buffer_store_dwordx4 v[116:119], v144, s[28:31], s55 offen sc1\n\t"
        "v_mfma_f32_16x16x32_bf16 v[52:55], a[184:187], v[88:91], v[52:55]\n\t"
        "ds_write_b64 v146, v[120:121] offset:6144\n\t"
        "v_mfma_f32_16x16x32_bf16 v[56:59], a[188:191], v[88:91], v[56:59]\n\t"
        "ds_read_b128 v[84:87], v183 offset:0\n\t"
        "s_waitcnt lgkmcnt(5)\n\t"
        "v_mfma_f32_16x16x32_bf16 v[48:51], a[216:219], v[92:95], v[48:51]\n\t"
        "ds_read_b128 v[88:91], v183 offset:64\n\t"
        "v_mfma_f32_16x16x32_bf16 v[52:55], a[220:223], v[92:95], v[52:55]\n\t"
        "v_mfma_f32_16x16x32_bf16 v[56:59], a[224:227], v[92:95], v[56:59]\n\t"
        "v_mfma_f32_16x16x32_bf16 v[48:51], a[252:255], v[96:99], v[48:51]\n\t"
        "ds_read_b128 v[92:95], v183 offset:128\n\t"
        "v_readlane_b32 s9, v155, 47\n\t"
        "v_mfma_f32_16x16x32_bf16 v[52:55], v[188:191], v[96:99], v[52:55]\n\t"
        "s_waitcnt lgkmcnt(3)\n\t"
        "s_barrier\n\t"
        "v_mfma_f32_16x16x32_bf16 v[56:59], v[192:195], v[96:99], v[56:59]\n\t"
        "ds_read_b128 v[124:127], v147 offset:0\n\t"
        "v_add_u32 v183, s9, v172\n\t"
        "v_mfma_f32_16x16x32_bf16 v[48:51], v[220:223], v[100:103], v[48:51]\n\t"
        "ds_read_b128 v[96:99], v151 offset:0\n\t"
        "v_max_u32 v183, v183, v132\n\t"
        "v_mfma_f32_16x16x32_bf16 v[52:55], v[224:227], v[100:103], v[52:55]\n\t"
        "v_xor_b32 v133, v181, v133\n\t"
        "v_mfma_f32_16x16x32_bf16 v[56:59], v[228:231], v[100:103], v[56:59]\n\t"
        "v_mfma_f32_16x16x32_bf16 v[48:51], a[24:27], v[104:107], v[48:51]\n\t"
        "ds_read_b128 v[100:103], v151 offset:64\n\t"
        "v_mfma_f32_16x16x32_bf16 v[52:55], a[28:31], v[104:107], v[52:55]\n\t"
        "v_mfma_f32_16x16x32_bf16 v[56:59], a[32:35], v[104:107], v[56:59]\n\t"
        "v_mfma_f32_16x16x32_bf16 v[48:51], a[60:63], v[108:111], v[48:51]\n\t"
        "ds_read_b128 v[104:107], v151 offset:128\n\t"
        "v_add_u32 v151, s9, v173\n\t"
        "v_mfma_f32_16x16x32_bf16 v[52:55], a[64:67], v[108:111], v[52:55]\n\t"
        "v_max_u32 v151, v151, v133\n\t"
        "v_xor_b32 v134, v181, v134\n\t"
        "v_mfma_f32_16x16x32_bf16 v[56:59], a[68:71], v[108:111], v[56:59]\n\t"
        "s_waitcnt lgkmcnt(3)\n\t"
        "buffer_store_dwordx4 v[124:127], v145, s[28:31], s50 offen sc1\n\t"
        "v_mfma_f32_16x16x32_bf16 v[48:51], a[96:99], v[112:115], v[48:51]\n\t"
        "ds_read_b128 v[124:127], v147 offset:4096\n\t"
        "s_add_u32 s50, s50, 86016\n\t"
        "v_mfma_f32_16x16x32_bf16 v[52:55], a[100:103], v[112:115], v[52:55]\n\t"
        "ds_read_b128 v[108:111], v183 offset:0\n\t"
        "v_mfma_f32_16x16x32_bf16 v[56:59], a[104:107], v[112:115], v[56:59]\n\t"
        "v_mfma_f32_16x16x32_bf16 v[48:51], a[132:135], v[84:87], v[48:51]\n\t"
        "ds_read_b128 v[112:115], v183 offset:64\n\t"
        "v_mfma_f32_16x16x32_bf16 v[52:55], a[136:139], v[84:87], v[52:55]\n\t"
        "v_mfma_f32_16x16x32_bf16 v[56:59], a[140:143], v[84:87], v[56:59]\n\t"
        "v_mfma_f32_16x16x32_bf16 v[48:51], a[168:171], v[88:91], v[48:51]\n\t"
        "ds_read_b128 v[84:87], v183 offset:128\n\t"
        "v_add_u32 v183, s9, v174\n\t"
        "v_mfma_f32_16x16x32_bf16 v[52:55], a[172:175], v[88:91], v[52:55]\n\t"
        "v_max_u32 v183, v183, v134\n\t"
        "v_xor_b32 v135, v181, v135\n\t"
        "v_mfma_f32_16x16x32_bf16 v[56:59], a[176:179], v[88:91], v[56:59]\n\t"
        "v_mfma_f32_16x16x32_bf16 v[48:51], a[204:207], v[92:95], v[48:51]\n\t"
        "ds_read_b128 v[88:91], v151 offset:0\n\t"
        "v_mfma_f32_16x16x32_bf16 v[52:55], a[208:211], v[92:95], v[52:55]\n\t"
        "v_mfma_f32_16x16x32_bf16 v[56:59], a[212:215], v[92:95], v[56:59]\n\t"
        "s_waitcnt lgkmcnt(3)\n\t"
        "v_mfma_f32_16x16x32_bf16 v[48:51], a[240:243], v[96:99], v[48:51]\n\t"
        "ds_read_b128 v[92:95], v151 offset:64\n\t"
        "v_mfma_f32_16x16x32_bf16 v[52:55], a[244:247], v[96:99], v[52:55]\n\t"
        "buffer_store_dwordx4 v[124:127], v145, s[28:31], s50 offen sc1\n\t"
        "v_mfma_f32_16x16x32_bf16 v[56:59], a[248:251], v[96:99], v[56:59]\n\t"
        "v_mfma_f32_16x16x32_bf16 v[48:51], v[208:211], v[100:103], v[48:51]\n\t"
        "ds_read_b128 v[96:99], v151 offset:128\n\t"
        "v_add_u32 v151, s9, v175\n\t"
        "v_mfma_f32_16x16x32_bf16 v[52:55], v[212:215], v[100:103], v[52:55]\n\t"
        "v_max_u32 v151, v151, v135\n\t"
        "v_xor_b32 v136, v181, v136\n\t"
        "v_mfma_f32_16x16x32_bf16 v[56:59], v[216:219], v[100:103], v[56:59]\n\t"
        "ds_read_b128 v[132:135], v187 offset:4096\n\t"
        "v_mfma_f32_16x16x32_bf16 v[48:51], v[244:247], v[104:107], v[48:51]\n\t"
        "ds_read_b128 v[100:103], v183 offset:0\n\t"
        "v_mfma_f32_16x16x32_bf16 v[52:55], v[248:251], v[104:107], v[52:55]\n\t"
        "v_mfma_f32_16x16x32_bf16 v[56:59], v[252:255], v[104:107], v[56:59]\n\t"
        "v_mfma_f32_16x16x32_bf16 v[60:63], a[12:15], v[108:111], 0\n\t"
        "ds_read_b128 v[104:107], v183 offset:64\n\t"
        "v_mfma_f32_16x16x32_bf16 v[64:67], a[16:19], v[108:111], 0\n\t"
        "v_mfma_f32_16x16x32_bf16 v[68:71], a[20:23], v[108:111], 0\n\t"
        "s_waitcnt lgkmcnt(4)\n\t"
        "v_mfma_f32_16x16x32_bf16 v[60:63], a[48:51], v[112:115], v[60:63]\n\t"
        "ds_read_b128 v[108:111], v183 offset:128\n\t"
        "v_add_u32 v183, s9, v176\n\t"
        "v_mfma_f32_16x16x32_bf16 v[64:67], a[52:55], v[112:115], v[64:67]\n\t"
        "v_max_u32 v183, v183, v136\n\t"
        "v_xor_b32 v137, v181, v137\n\t"
        "v_mfma_f32_16x16x32_bf16 v[68:71], a[56:59], v[112:115], v[68:71]\n\t"
        "v_readlane_b32 s55, v155, 49\n\t"
        "v_cvt_pk_bf16_f32 v116, v48, v49\n\t"
        "v_mfma_f32_16x16x32_bf16 v[60:63], a[84:87], v[84:87], v[60:63]\n\t"
        "ds_read_b128 v[112:115], v151 offset:0\n\t"
        "v_cvt_pk_bf16_f32 v117, v50, v51\n\t"
        "v_mfma_f32_16x16x32_bf16 v[64:67], a[88:91], v[84:87], v[64:67]\n\t"
        "v_cvt_pk_bf16_f32 v118, v52, v53\n\t"
        "v_cvt_pk_bf16_f32 v119, v54, v55\n\t"
        "v_mfma_f32_16x16x32_bf16 v[68:71], a[92:95], v[84:87], v[68:71]\n\t"
        "v_cvt_pk_bf16_f32 v120, v56, v57\n\t"
        "v_cvt_pk_bf16_f32 v121, v58, v59\n\t"
        "v_mfma_f32_16x16x32_bf16 v[60:63], a[120:123], v[88:91], v[60:63]\n\t"
        "v_permlane16_swap_b32 v116, v118\n\t"
        "v_mfma_f32_16x16x32_bf16 v[64:67], a[124:127], v[88:91], v[64:67]\n\t"
        "v_permlane16_swap_b32 v117, v119\n\t"
        "v_mfma_f32_16x16x32_bf16 v[68:71], a[128:131], v[88:91], v[68:71]\n\t"
        "buffer_store_dwordx4 v[116:119], v148, s[28:31], s55 offen sc1\n\t"
        "v_mfma_f32_16x16x32_bf16 v[60:63], a[156:159], v[92:95], v[60:63]\n\t"
        "ds_write_b64 v146, v[120:121] offset:8192\n\t"
        "v_mfma_f32_16x16x32_bf16 v[64:67], a[160:163], v[92:95], v[64:67]\n\t"
        "ds_read_b128 v[84:87], v151 offset:64\n\t"
        "v_mfma_f32_16x16x32_bf16 v[68:71], a[164:167], v[92:95], v[68:71]\n\t"
        "ds_read_b128 v[88:91], v151 offset:128\n\t"
        "v_add_u32 v151, s9, v177\n\t"
        "s_waitcnt lgkmcnt(4)\n\t"
        "v_mfma_f32_16x16x32_bf16 v[60:63], a[192:195], v[96:99], v[60:63]\n\t"
        "ds_read_b128 v[92:95], v183 offset:0\n\t"
        "v_max_u32 v151, v151, v137\n\t"
        "v_mfma_f32_16x16x32_bf16 v[64:67], a[196:199], v[96:99], v[64:67]\n\t"
        "v_xor_b32 v138, v181, v138\n\t"
        "v_mfma_f32_16x16x32_bf16 v[68:71], a[200:203], v[96:99], v[68:71]\n\t"
        "v_mfma_f32_16x16x32_bf16 v[60:63], a[228:231], v[100:103], v[60:63]\n\t"
        "s_waitcnt lgkmcnt(3)\n\t"
        "s_barrier\n\t"
        "v_mfma_f32_16x16x32_bf16 v[64:67], a[232:235], v[100:103], v[64:67]\n\t"
        "ds_read_b128 v[124:127], v147 offset:8192\n\t"
        "v_mfma_f32_16x16x32_bf16 v[68:71], a[236:239], v[100:103], v[68:71]\n\t"
        "ds_read_b128 v[96:99], v183 offset:64\n\t"
        "v_mfma_f32_16x16x32_bf16 v[60:63], v[196:199], v[104:107], v[60:63]\n\t"
        "ds_read_b128 v[100:103], v183 offset:128\n\t"
        "v_add_u32 v183, s9, v178\n\t"
        "v_mfma_f32_16x16x32_bf16 v[64:67], v[200:203], v[104:107], v[64:67]\n\t"
        "v_max_u32 v183, v183, v138\n\t"
        "v_xor_b32 v139, v181, v139\n\t"
        "v_mfma_f32_16x16x32_bf16 v[68:71], v[204:207], v[104:107], v[68:71]\n\t"
        "v_mfma_f32_16x16x32_bf16 v[60:63], v[232:235], v[108:111], v[60:63]\n\t"
        "ds_read_b128 v[104:107], v151 offset:0\n\t"
        "v_mfma_f32_16x16x32_bf16 v[64:67], v[236:239], v[108:111], v[64:67]\n\t"
        "v_mfma_f32_16x16x32_bf16 v[68:71], v[240:243], v[108:111], v[68:71]\n\t"
        "v_mfma_f32_16x16x32_bf16 v[60:63], a[0:3], v[112:115], v[60:63]\n\t"
        "ds_read_b128 v[108:111], v151 offset:64\n\t"
        "v_mfma_f32_16x16x32_bf16 v[64:67], a[4:7], v[112:115], v[64:67]\n\t"
        "s_waitcnt lgkmcnt(4)\n\t"
        "buffer_store_dwordx4 v[124:127], v150, s[28:31], s51 offen sc1\n\t"
        "v_mfma_f32_16x16x32_bf16 v[68:71], a[8:11], v[112:115], v[68:71]\n\t"
        "v_mfma_f32_16x16x32_bf16 v[60:63], a[36:39], v[84:87], v[60:63]\n\t"
        "ds_read_b128 v[112:115], v151 offset:128\n\t"
        "v_add_u32 v151, s9, v179\n\t"
        "v_mfma_f32_16x16x32_bf16 v[64:67], a[40:43], v[84:87], v[64:67]\n\t"
        "v_max_u32 v151, v151, v139\n\t"
        "v_xor_b32 v140, v181, v140\n\t"
        "v_mfma_f32_16x16x32_bf16 v[68:71], a[44:47], v[84:87], v[68:71]\n\t"
        "ds_read_b128 v[136:139], v187 offset:4112\n\t"
        "v_mfma_f32_16x16x32_bf16 v[60:63], a[72:75], v[88:91], v[60:63]\n\t"
        "ds_read_b128 v[84:87], v183 offset:0\n\t"
        "v_mfma_f32_16x16x32_bf16 v[64:67], a[76:79], v[88:91], v[64:67]\n\t"
        "v_mfma_f32_16x16x32_bf16 v[68:71], a[80:83], v[88:91], v[68:71]\n\t"
        "v_mfma_f32_16x16x32_bf16 v[60:63], a[108:111], v[92:95], v[60:63]\n\t"
        "ds_read_b128 v[88:91], v183 offset:64\n\t"
        "v_mfma_f32_16x16x32_bf16 v[64:67], a[112:115], v[92:95], v[64:67]\n\t"
        "v_mfma_f32_16x16x32_bf16 v[68:71], a[116:119], v[92:95], v[68:71]\n\t"
        "s_waitcnt lgkmcnt(4)\n\t"
        "v_mfma_f32_16x16x32_bf16 v[60:63], a[144:147], v[96:99], v[60:63]\n\t"
        "ds_read_b128 v[92:95], v183 offset:128\n\t"
        "v_add_u32 v183, s9, v180\n\t"
        "v_mfma_f32_16x16x32_bf16 v[64:67], a[148:151], v[96:99], v[64:67]\n\t"
        "v_max_u32 v183, v183, v140\n\t"
        "v_xor_b32 v132, v181, v132\n\t"
        "v_mfma_f32_16x16x32_bf16 v[68:71], a[152:155], v[96:99], v[68:71]\n\t"
        "ds_read_b32 v140, v187 offset:4128\n\t"
        "v_mfma_f32_16x16x32_bf16 v[60:63], a[180:183], v[100:103], v[60:63]\n\t"
        "ds_read_b128 v[96:99], v151 offset:0\n\t"
        "v_mfma_f32_16x16x32_bf16 v[64:67], a[184:187], v[100:103], v[64:67]\n\t"
        "v_mfma_f32_16x16x32_bf16 v[68:71], a[188:191], v[100:103], v[68:71]\n\t"
        "v_mfma_f32_16x16x32_bf16 v[60:63], a[216:219], v[104:107], v[60:63]\n\t"
        "ds_read_b128 v[100:103], v151 offset:64\n\t"
        "v_mfma_f32_16x16x32_bf16 v[64:67], a[220:223], v[104:107], v[64:67]\n\t"
        "v_mfma_f32_16x16x32_bf16 v[68:71], a[224:227], v[104:107], v[68:71]\n\t"
        "v_mfma_f32_16x16x32_bf16 v[60:63], a[252:255], v[108:111], v[60:63]\n\t"
        "ds_read_b128 v[104:107], v151 offset:128\n\t"
        "v_readlane_b32 s9, v155, 48\n\t"
        "v_mfma_f32_16x16x32_bf16 v[64:67], v[188:191], v[108:111], v[64:67]\n\t"
        "s_nop 0\n\t"
        "v_add_u32 v151, s9, v172\n\t"
        "v_max_u32 v151, v151, v132\n\t"
        "v_mfma_f32_16x16x32_bf16 v[68:71], v[192:195], v[108:111], v[68:71]\n\t"
        "v_xor_b32 v133, v181, v133\n\t"
        "s_waitcnt lgkmcnt(4)\n\t"
        "v_mfma_f32_16x16x32_bf16 v[60:63], v[220:223], v[112:115], v[60:63]\n\t"
        "ds_read_b128 v[108:111], v183 offset:0\n\t"
        "v_mfma_f32_16x16x32_bf16 v[64:67], v[224:227], v[112:115], v[64:67]\n\t"
        "v_mfma_f32_16x16x32_bf16 v[68:71], v[228:231], v[112:115], v[68:71]\n\t"
        "v_mfma_f32_16x16x32_bf16 v[60:63], a[24:27], v[84:87], v[60:63]\n\t"
        "ds_read_b128 v[112:115], v183 offset:64\n\t"
        "v_mfma_f32_16x16x32_bf16 v[64:67], a[28:31], v[84:87], v[64:67]\n\t"
        "v_mfma_f32_16x16x32_bf16 v[68:71], a[32:35], v[84:87], v[68:71]\n\t"
        "v_mfma_f32_16x16x32_bf16 v[60:63], a[60:63], v[88:91], v[60:63]\n\t"
        "ds_read_b128 v[84:87], v183 offset:128\n\t"
        "v_add_u32 v183, s9, v173\n\t"
        "v_mfma_f32_16x16x32_bf16 v[64:67], a[64:67], v[88:91], v[64:67]\n\t"
        "v_max_u32 v183, v183, v133\n\t"
        "v_xor_b32 v134, v181, v134\n\t"
        "v_mfma_f32_16x16x32_bf16 v[68:71], a[68:71], v[88:91], v[68:71]\n\t"
        "v_mfma_f32_16x16x32_bf16 v[60:63], a[96:99], v[92:95], v[60:63]\n\t"
        "ds_read_b128 v[88:91], v151 offset:0\n\t"
        "v_mfma_f32_16x16x32_bf16 v[64:67], a[100:103], v[92:95], v[64:67]\n\t"
        "v_mfma_f32_16x16x32_bf16 v[68:71], a[104:107], v[92:95], v[68:71]\n\t"
        "s_waitcnt lgkmcnt(3)\n\t"
        "v_mfma_f32_16x16x32_bf16 v[60:63], a[132:135], v[96:99], v[60:63]\n\t"
        "ds_read_b128 v[92:95], v151 offset:64\n\t"
        "v_mfma_f32_16x16x32_bf16 v[64:67], a[136:139], v[96:99], v[64:67]\n\t"
        "v_mfma_f32_16x16x32_bf16 v[68:71], a[140:143], v[96:99], v[68:71]\n\t"
        "v_mfma_f32_16x16x32_bf16 v[60:63], a[168:171], v[100:103], v[60:63]\n\t"
        "ds_read_b128 v[96:99], v151 offset:128\n\t"
        "v_add_u32 v151, s9, v174\n\t"
        "v_mfma_f32_16x16x32_bf16 v[64:67], a[172:175], v[100:103], v[64:67]\n\t"
        "v_max_u32 v151, v151, v134\n\t"
        "v_xor_b32 v135, v181, v135\n\t"
        "v_mfma_f32_16x16x32_bf16 v[68:71], a[176:179], v[100:103], v[68:71]\n\t"
        "v_mfma_f32_16x16x32_bf16 v[60:63], a[204:207], v[104:107], v[60:63]\n\t"
        "ds_read_b128 v[100:103], v183 offset:0\n\t"
        "v_mfma_f32_16x16x32_bf16 v[64:67], a[208:211], v[104:107], v[64:67]\n\t"
        "v_mfma_f32_16x16x32_bf16 v[68:71], a[212:215], v[104:107], v[68:71]\n\t"
        "v_mfma_f32_16x16x32_bf16 v[60:63], a[240:243], v[108:111], v[60:63]\n\t"
        "ds_read_b128 v[104:107], v183 offset:64\n\t"
        "v_mfma_f32_16x16x32_bf16 v[64:67], a[244:247], v[108:111], v[64:67]\n\t"
        "v_mfma_f32_16x16x32_bf16 v[68:71], a[248:251], v[108:111], v[68:71]\n\t"
        "s_waitcnt lgkmcnt(3)\n\t"
        "v_mfma_f32_16x16x32_bf16 v[60:63], v[208:211], v[112:115], v[60:63]\n\t"
        "ds_read_b128 v[108:111], v183 offset:128\n\t"
        "v_add_u32 v183, s9, v175\n\t"
        "v_mfma_f32_16x16x32_bf16 v[64:67], v[212:215], v[112:115], v[64:67]\n\t"
        "v_max_u32 v183, v183, v135\n\t"
        "v_xor_b32 v136, v181, v136\n\t"
        "v_mfma_f32_16x16x32_bf16 v[68:71], v[216:219], v[112:115], v[68:71]\n\t"
        "v_mfma_f32_16x16x32_bf16 v[60:63], v[244:247], v[84:87], v[60:63]\n\t"
        "ds_read_b128 v[112:115], v151 offset:0\n\t"
        "v_mfma_f32_16x16x32_bf16 v[64:67], v[248:251], v[84:87], v[64:67]\n\t"
        "v_mfma_f32_16x16x32_bf16 v[68:71], v[252:255], v[84:87], v[68:71]\n\t"
        "v_mfma_f32_16x16x32_bf16 v[72:75], a[12:15], v[88:91], 0\n\t"
        "ds_read_b128 v[84:87], v151 offset:64\n\t"
        "v_mfma_f32_16x16x32_bf16 v[76:79], a[16:19], v[88:91], 0\n\t"
        "v_mfma_f32_16x16x32_bf16 v[80:83], a[20:23], v[88:91], 0\n\t"
        "v_mfma_f32_16x16x32_bf16 v[72:75], a[48:51], v[92:95], v[72:75]\n\t"
        "ds_read_b128 v[88:91], v151 offset:128\n\t"
        "v_add_u32 v151, s9, v176\n\t"
        "v_mfma_f32_16x16x32_bf16 v[76:79], a[52:55], v[92:95], v[76:79]\n\t"
        "v_max_u32 v151, v151, v136\n\t"
        "v_xor_b32 v137, v181, v137\n\t"
        "v_mfma_f32_16x16x32_bf16 v[80:83], a[56:59], v[92:95], v[80:83]\n\t"
        "v_readlane_b32 s55, v155, 50\n\t"
        "v_cvt_pk_bf16_f32 v116, v60, v61\n\t"
        "s_waitcnt lgkmcnt(3)\n\t"
        "v_mfma_f32_16x16x32_bf16 v[72:75], a[84:87], v[96:99], v[72:75]\n\t"
        "ds_read_b128 v[92:95], v183 offset:0\n\t"
        "v_cvt_pk_bf16_f32 v117, v62, v63\n\t"
        "v_mfma_f32_16x16x32_bf16 v[76:79], a[88:91], v[96:99], v[76:79]\n\t"
        "v_cvt_pk_bf16_f32 v118, v64, v65\n\t"
        "v_cvt_pk_bf16_f32 v119, v66, v67\n\t"
        "v_mfma_f32_16x16x32_bf16 v[80:83], a[92:95], v[96:99], v[80:83]\n\t"
        "v_cvt_pk_bf16_f32 v120, v68, v69\n\t"
        "v_cvt_pk_bf16_f32 v121, v70, v71\n\t"
        "v_mfma_f32_16x16x32_bf16 v[72:75], a[120:123], v[100:103], v[72:75]\n\t"
        "v_permlane16_swap_b32 v116, v118\n\t"
        "v_mfma_f32_16x16x32_bf16 v[76:79], a[124:127], v[100:103], v[76:79]\n\t"
        "v_permlane16_swap_b32 v117, v119\n\t"
        "v_mfma_f32_16x16x32_bf16 v[80:83], a[128:131], v[100:103], v[80:83]\n\t"
        "buffer_store_dwordx4 v[116:119], v148, s[28:31], s55 offen sc1\n\t"
        "v_mfma_f32_16x16x32_bf16 v[72:75], a[156:159], v[104:107], v[72:75]\n\t"
        "ds_write_b64 v146, v[120:121] offset:0\n\t"
        "v_mfma_f32_16x16x32_bf16 v[76:79], a[160:163], v[104:107], v[76:79]\n\t"
        "ds_read_b128 v[96:99], v183 offset:64\n\t"
        "v_mfma_f32_16x16x32_bf16 v[80:83], a[164:167], v[104:107], v[80:83]\n\t"
        "ds_read_b128 v[100:103], v183 offset:128\n\t"
        "v_add_u32 v183, s9, v177\n\t"
        "v_mfma_f32_16x16x32_bf16 v[72:75], a[192:195], v[108:111], v[72:75]\n\t"
        "ds_read_b128 v[104:107], v151 offset:0\n\t"
        "v_max_u32 v183, v183, v137\n\t"
        "v_mfma_f32_16x16x32_bf16 v[76:79], a[196:199], v[108:111], v[76:79]\n\t"
        "v_xor_b32 v138, v181, v138\n\t"
        "v_mfma_f32_16x16x32_bf16 v[80:83], a[200:203], v[108:111], v[80:83]\n\t"
        "s_waitcnt lgkmcnt(4)\n\t"
        "v_mfma_f32_16x16x32_bf16 v[72:75], a[228:231], v[112:115], v[72:75]\n\t"
        "ds_read_b128 v[108:111], v151 offset:64\n\t"
        "v_mfma_f32_16x16x32_bf16 v[76:79], a[232:235], v[112:115], v[76:79]\n\t"
        "v_mfma_f32_16x16x32_bf16 v[80:83], a[236:239], v[112:115], v[80:83]\n\t"
        "v_mfma_f32_16x16x32_bf16 v[72:75], v[196:199], v[84:87], v[72:75]\n\t"
        "ds_read_b128 v[112:115], v151 offset:128\n\t"
        "v_add_u32 v151, s9, v178\n\t"
        "v_mfma_f32_16x16x32_bf16 v[76:79], v[200:203], v[84:87], v[76:79]\n\t"
        "v_max_u32 v151, v151, v138\n\t"
        "v_xor_b32 v139, v181, v139\n\t"
        "v_mfma_f32_16x16x32_bf16 v[80:83], v[204:207], v[84:87], v[80:83]\n\t"
        "v_mfma_f32_16x16x32_bf16 v[72:75], v[232:235], v[88:91], v[72:75]\n\t"
        "ds_read_b128 v[84:87], v183 offset:0\n\t"
        "v_mfma_f32_16x16x32_bf16 v[76:79], v[236:239], v[88:91], v[76:79]\n\t"
        "v_mfma_f32_16x16x32_bf16 v[80:83], v[240:243], v[88:91], v[80:83]\n\t"
        "v_mfma_f32_16x16x32_bf16 v[72:75], a[0:3], v[92:95], v[72:75]\n\t"
        "ds_read_b128 v[88:91], v183 offset:64\n\t"
        "v_mfma_f32_16x16x32_bf16 v[76:79], a[4:7], v[92:95], v[76:79]\n\t"
        "v_mfma_f32_16x16x32_bf16 v[80:83], a[8:11], v[92:95], v[80:83]\n\t"
        ".LpS2%=:\n\t"
        "s_cbranch_execz .LrS%=\n\t"
        "s_waitcnt lgkmcnt(3)\n\t"
        "v_mfma_f32_16x16x32_bf16 v[72:75], a[36:39], v[96:99], v[72:75]\n\t"
        "ds_read_b128 v[92:95], v183 offset:128\n\t"
        "v_add_u32 v183, s9, v179\n\t"
        "v_mfma_f32_16x16x32_bf16 v[76:79], a[40:43], v[96:99], v[76:79]\n\t"
        "v_max_u32 v183, v183, v139\n\t"
        "v_xor_b32 v140, v181, v140\n\t"
        "v_mfma_f32_16x16x32_bf16 v[80:83], a[44:47], v[96:99], v[80:83]\n\t"
        "v_mfma_f32_16x16x32_bf16 v[72:75], a[72:75], v[100:103], v[72:75]\n\t"
        "ds_read_b128 v[96:99], v151 offset:0\n\t"
        "v_mfma_f32_16x16x32_bf16 v[76:79], a[76:79], v[100:103], v[76:79]\n\t"
        "v_mfma_f32_16x16x32_bf16 v[80:83], a[80:83], v[100:103], v[80:83]\n\t"
        "v_mfma_f32_16x16x32_bf16 v[72:75], a[108:111], v[104:107], v[72:75]\n\t"
        "ds_read_b128 v[100:103], v151 offset:64\n\t"
        "v_mfma_f32_16x16x32_bf16 v[76:79], a[112:115], v[104:107], v[76:79]\n\t"
        "v_mfma_f32_16x16x32_bf16 v[80:83], a[116:119], v[104:107], v[80:83]\n\t"
        "ds_read_b128 v[104:107], v151 offset:128\n\t"
        "v_add_u32 v151, s9, v180\n\t"
        "v_mfma_f32_16x16x32_bf16 v[72:75], a[144:147], v[108:111], v[72:75]\n\t"
        "v_max_u32 v151, v151, v140\n\t"
        "v_mfma_f32_16x16x32_bf16 v[76:79], a[148:151], v[108:111], v[76:79]\n\t"
        "v_mfma_f32_16x16x32_bf16 v[80:83], a[152:155], v[108:111], v[80:83]\n\t"
        "ds_read_b128 v[108:111], v183 offset:0\n\t"
        "s_waitcnt lgkmcnt(4)\n\t"
        "v_mfma_f32_16x16x32_bf16 v[72:75], a[180:183], v[112:115], v[72:75]\n\t"
        "v_mfma_f32_16x16x32_bf16 v[76:79], a[184:187], v[112:115], v[76:79]\n\t"
        "v_mfma_f32_16x16x32_bf16 v[80:83], a[188:191], v[112:115], v[80:83]\n\t"
        "ds_read_b128 v[112:115], v183 offset:64\n\t"
        "v_mfma_f32_16x16x32_bf16 v[72:75], a[216:219], v[84:87], v[72:75]\n\t"
        "v_mfma_f32_16x16x32_bf16 v[76:79], a[220:223], v[84:87], v[76:79]\n\t"
        "v_mfma_f32_16x16x32_bf16 v[80:83], a[224:227], v[84:87], v[80:83]\n\t"
        "ds_read_b128 v[84:87], v183 offset:128\n\t"
        "v_mfma_f32_16x16x32_bf16 v[72:75], a[252:255], v[88:91], v[72:75]\n\t"
        "v_mfma_f32_16x16x32_bf16 v[76:79], v[188:191], v[88:91], v[76:79]\n\t"
        "v_mfma_f32_16x16x32_bf16 v[80:83], v[192:195], v[88:91], v[80:83]\n\t"
        "ds_read_b128 v[88:91], v151 offset:0\n\t"
        "v_mfma_f32_16x16x32_bf16 v[72:75], v[220:223], v[92:95], v[72:75]\n\t"
        "v_mfma_f32_16x16x32_bf16 v[76:79], v[224:227], v[92:95], v[76:79]\n\t"
        "v_mfma_f32_16x16x32_bf16 v[80:83], v[228:231], v[92:95], v[80:83]\n\t"
        "ds_read_b128 v[92:95], v151 offset:64\n\t"
        "s_waitcnt lgkmcnt(4)\n\t"
        "v_mfma_f32_16x16x32_bf16 v[72:75], a[24:27], v[96:99], v[72:75]\n\t"
        "v_mfma_f32_16x16x32_bf16 v[76:79], a[28:31], v[96:99], v[76:79]\n\t"
        "v_mfma_f32_16x16x32_bf16 v[80:83], a[32:35], v[96:99], v[80:83]\n\t"
        "ds_read_b128 v[96:99], v151 offset:128\n\t"
        "v_mfma_f32_16x16x32_bf16 v[80:83], a[68:71], v[100:103], v[80:83]\n\t"
        "v_mfma_f32_16x16x32_bf16 v[80:83], a[104:107], v[104:107], v[80:83]\n\t"
        "v_mfma_f32_16x16x32_bf16 v[80:83], a[140:143], v[108:111], v[80:83]\n\t"
        "s_waitcnt lgkmcnt(3)\n\t"
        "v_mfma_f32_16x16x32_bf16 v[80:83], a[176:179], v[112:115], v[80:83]\n\t"
        "v_mfma_f32_16x16x32_bf16 v[80:83], a[212:215], v[84:87], v[80:83]\n\t"
        "s_waitcnt lgkmcnt(2)\n\t"
        "v_mfma_f32_16x16x32_bf16 v[80:83], a[248:251], v[88:91], v[80:83]\n\t"
        "s_waitcnt lgkmcnt(1)\n\t"
        "v_mfma_f32_16x16x32_bf16 v[80:83], v[216:219], v[92:95], v[80:83]\n\t"
        "s_waitcnt lgkmcnt(0)\n\t"
        "v_mfma_f32_16x16x32_bf16 v[80:83], v[252:255], v[96:99], v[80:83]\n\t"
        "s_nop 2\n\t"
        "v_mfma_f32_16x16x32_bf16 v[72:75], a[60:63], v[100:103], v[72:75]\n\t"
        "s_nop 2\n\t"
        "v_mfma_f32_16x16x32_bf16 v[72:75], a[96:99], v[104:107], v[72:75]\n\t"
        "v_cvt_pk_bf16_f32 v120, v80, v81\n\t"
        "v_cvt_pk_bf16_f32 v121, v82, v83\n\t"
        "v_mfma_f32_16x16x32_bf16 v[72:75], a[132:135], v[108:111], v[72:75]\n\t"
        "ds_write_b64 v146, v[120:121] offset:2048\n\t"
        "v_mfma_f32_16x16x32_bf16 v[72:75], a[168:171], v[112:115], v[72:75]\n\t"
        "v_mfma_f32_16x16x32_bf16 v[72:75], a[204:207], v[84:87], v[72:75]\n\t"
        "v_mfma_f32_16x16x32_bf16 v[72:75], a[240:243], v[88:91], v[72:75]\n\t"
        "s_waitcnt lgkmcnt(0)\n\t"
        "s_barrier\n\t"
        "v_mfma_f32_16x16x32_bf16 v[72:75], v[208:211], v[92:95], v[72:75]\n\t"
        "ds_read_b128 v[124:127], v147 offset:0\n\t"
        "v_readlane_b32 s55, v155, 51\n\t"
        "v_mfma_f32_16x16x32_bf16 v[72:75], v[244:247], v[96:99], v[72:75]\n\t"
        "s_nop 1\n\t"
        "v_mfma_f32_16x16x32_bf16 v[76:79], a[64:67], v[100:103], v[76:79]\n\t"
        "s_nop 1\n\t"
        "v_mfma_f32_16x16x32_bf16 v[76:79], a[100:103], v[104:107], v[76:79]\n\t"
        "v_mfma_f32_16x16x32_bf16 v[76:79], a[136:139], v[108:111], v[76:79]\n\t"
        "v_mfma_f32_16x16x32_bf16 v[76:79], a[172:175], v[112:115], v[76:79]\n\t"
        "v_mfma_f32_16x16x32_bf16 v[76:79], a[208:211], v[84:87], v[76:79]\n\t"
        "v_cvt_pk_bf16_f32 v116, v72, v73\n\t"
        "v_cvt_pk_bf16_f32 v117, v74, v75\n\t"
        "v_mfma_f32_16x16x32_bf16 v[76:79], a[244:247], v[88:91], v[76:79]\n\t"
        "s_waitcnt lgkmcnt(0)\n\t"
        "buffer_store_dwordx4 v[124:127], v150, s[28:31], s52 offen sc1\n\t"
        "v_mfma_f32_16x16x32_bf16 v[76:79], v[212:215], v[92:95], v[76:79]\n\t"
        "v_mfma_f32_16x16x32_bf16 v[76:79], v[248:251], v[96:99], v[76:79]\n\t"
        "s_nop 7\n\t"
        "v_cvt_pk_bf16_f32 v118, v76, v77\n\t"
        "v_cvt_pk_bf16_f32 v119, v78, v79\n\t"
        "s_nop 0\n\t"
        "v_permlane16_swap_b32 v116, v118\n\t"
        "v_permlane16_swap_b32 v117, v119\n\t"
        "buffer_store_dwordx4 v[116:119], v148, s[28:31], s55 offen sc1\n\t"
        "s_andn2_b32 s6, s6, 6\n\t"
        "s_cbranch_scc1 99f\n\t"
        "s_endpgm\n\t"
        "21:\n\t"
        "s_bitcmp1_b32 s7, 15\n\t"
        "s_cbranch_scc1 22f\n\t"
        "ds_bpermute_b32 v64, v65, v155 offset:172\n\t"
        "s_add_u32 m0, s37, -24576\n\t"
        "s_add_u32 s36, s36, 6144\n\t"
        "buffer_load_dwordx4 v140, s[24:27], s36 offen offset:0 lds\n\t"
        "ds_bpermute_b32 v78, v81, v155 offset:148\n\t"
        "ds_bpermute_b32 v79, v81, v155 offset:160\n\t"
        "v_readlane_b32 s71, v155, 26\n\t"
        "v_readlane_b32 s10, v155, 29\n\t"
        "s_nop 0\n\t"
        "v_mul_u32_u24 v73, s71, v66\n\t"
        "v_lshrrev_b32 v73, 11, v73\n\t"
        "v_lshlrev_b32 v66, 4, v66\n\t"
        "v_mad_u32_u24 v73, v73, s10, v66\n\t"
        "s_waitcnt lgkmcnt(2)\n\t"
        "v_add_u32 v64, v64, v73\n\t"
        "buffer_load_dwordx4 v141, s[24:27], s36 offen offset:1024 lds\n\t"
        "v_readlane_b32 s69, v155, 34\n\t"
        "v_readlane_b32 s70, v155, 35\n\t"
        "s_nop 0\n\t"
        "v_mad_u32_u24 v70, v52, s69, v70\n\t"
        "v_mad_u32_u24 v70, v158, s70, v70\n\t"
        "s_mul_i32 s71, s69, s68\n\t"
        "s_add_u32 s71, s71, 157728\n\t"
        "v_mad_u32_u24 v67, v82, s70, v67\n\t"
        "v_add_u32 v67, s71, v67\n\t"
        "v_readlane_b32 s11, v155, 30\n\t"
        "buffer_load_dwordx4 v142, s[24:27], s36 offen offset:2048 lds\n\t"
        "s_movk_i32 s10, 223\n\t"
        "v_mov_b32 v75, 0x7ff00000\n\t"
        "v_readlane_b32 s12, v155, 24\n\t"
        "v_readlane_b32 s13, v155, 25\n\t"
        "s_nop 0\n\t"
        "s_bfe_u32 s70, s12, 524288\n\t"
        "s_bfe_u32 s71, s13, 524288\n\t"
        "v_cmp_gt_u32 vcc, s71, v71\n\t"
        "v_add3_u32 v73, v71, s70, -2\n\t"
        "v_med3_i32 v73, v73, 0, s10\n\t"
        "s_add_u32 m0, s35, 4096\n\t"
        "s_add_u32 s43, s34, 43008\n\t"
        "buffer_load_dwordx4 v143, s[16:19], s43 offen  lds\n\t"
        "v_cndmask_b32 v74, v75, v72, vcc\n\t"
        "v_mad_u32_u24 v134, v73, s11, v74\n\t"
        "s_bfe_u32 s70, s12, 524296\n\t"
        "s_bfe_u32 s71, s13, 524296\n\t"
        "v_cmp_gt_u32 vcc, s71, v71\n\t"
        "v_add3_u32 v73, v71, s70, -2\n\t"
        "v_med3_i32 v73, v73, 0, s10\n\t"
        "v_cndmask_b32 v74, v75, v72, vcc\n\t"
        "v_mad_u32_u24 v135, v73, s11, v74\n\t"
        "s_add_u32 m0, s35, 20480\n\t"
        "s_add_u32 s43, s34, 215040\n\t"
        "buffer_load_dwordx4 v143, s[16:19], s43 offen  lds\n\t"
        "s_bfe_u32 s70, s12, 524304\n\t"
        "s_bfe_u32 s71, s13, 524304\n\t"
        "v_cmp_gt_u32 vcc, s71, v71\n\t"
        "v_add3_u32 v73, v71, s70, -2\n\t"
        "v_med3_i32 v73, v73, 0, s10\n\t"
        "v_cndmask_b32 v74, v75, v72, vcc\n\t"
        "v_mad_u32_u24 v136, v73, s11, v74\n\t"
        "s_bfe_u32 s70, s12, 524312\n\t"
        "s_bfe_u32 s71, s13, 524312\n\t"
        "v_cmp_gt_u32 vcc, s71, v71\n\t"
        "v_add3_u32 v73, v71, s70, -2\n\t"
        "v_med3_i32 v73, v73, 0, s10\n\t"
        "v_cndmask_b32 v74, v75, v72, vcc\n\t"
        "v_mad_u32_u24 v137, v73, s11, v74\n\t"
        "s_mov_b64 exec, 0\n\t"
        "s_cmp_eq_u32 s4, 0\n\t"
        "s_cbranch_scc1 .LpX0%=\n\t"
        "s_cmp_eq_u32 s4, 1\n\t"
        "s_cbranch_scc1 .LpX1%=\n\t"
        "s_cmp_eq_u32 s4, 2\n\t"
        "s_cbranch_scc1 .LpX2%=\n\t"
        "s_branch .LwX%=\n\t"
        ".LrX%=:\n\t"
        "s_mov_b64 exec, -1\n\t"
        "s_waitcnt vmcnt(9)\n\t"
        "s_barrier\n\t"
        "ds_read_b64_tr_b16 a[0:1], v138 offset:6144\n\t"
        "ds_read_b64_tr_b16 a[2:3], v138 offset:6912\n\t"
        "ds_read_b64_tr_b16 a[4:5], v139 offset:6144\n\t"
        "ds_read_b64_tr_b16 a[6:7], v139 offset:6912\n\t"
        "ds_read_b64_tr_b16 a[8:9], v149 offset:6144\n\t"
        "ds_read_b64_tr_b16 a[10:11], v149 offset:6912\n\t"
        "ds_read_b128 v[84:87], v128 offset:8192\n\t"
        "ds_read_b64_tr_b16 a[12:13], v138 offset:12288\n\t"
        "ds_read_b64_tr_b16 a[14:15], v138 offset:13056\n\t"
        "ds_read_b64_tr_b16 a[16:17], v139 offset:12288\n\t"
        "ds_read_b64_tr_b16 a[18:19], v139 offset:13056\n\t"
        "ds_read_b64_tr_b16 a[20:21], v149 offset:12288\n\t"
        "ds_read_b64_tr_b16 a[22:23], v149 offset:13056\n\t"
        "ds_read_b128 v[88:91], v128 offset:8192\n\t"
        "s_add_u32 m0, s37, -12288\n\t"
        "s_add_u32 s36, s36, s39\n\t"
        "s_waitcnt lgkmcnt(7)\n\t"
        "v_mfma_f32_16x16x32_bf16 v[12:15], a[0:3], v[84:87], 0\n\t"
        "buffer_load_dwordx4 v140, s[24:27], s36 offen offset:0 lds\n\t"
        "buffer_load_dwordx4 v141, s[24:27], s36 offen offset:1024 lds\n\t"
        "buffer_load_dwordx4 v142, s[24:27], s36 offen offset:2048 lds\n\t"
        "s_add_u32 m0, s35, 16384\n\t"
        "s_add_u32 s43, s34, 172032\n\t"
        "buffer_load_dwordx4 v143, s[16:19], s43 offen  lds\n\t"
        "s_add_u32 m0, s35, 32768\n\t"
        "s_add_u32 s43, s34, 344064\n\t"
        "s_min_u32 s43, s43, 9590784\n\t"
        "buffer_load_dwordx4 v143, s[16:19], s43 offen  lds\n\t"
        "s_waitcnt vmcnt(10)\n\t"
        "s_barrier\n\t"
        "ds_read_b128 v[92:95], v128 offset:12288\n\t"
        "ds_read_b64_tr_b16 a[24:25], v138 offset:0\n\t"
        "v_mfma_f32_16x16x32_bf16 v[16:19], a[4:7], v[84:87], 0\n\t"
        "ds_read_b64_tr_b16 a[26:27], v138 offset:768\n\t"
        "s_add_u32 m0, s35, 157696\n\t"
        "s_nop 0\n\t"
        "buffer_load_dwordx4 v64, s[20:23], 0 offen lds\n\t"
        "s_add_u32 m0, s37, 0\n\t"
        "s_add_u32 s36, s36, s38\n\t"
        "v_mfma_f32_16x16x32_bf16 v[20:23], a[8:11], v[84:87], 0\n\t"
        "buffer_load_dwordx4 v140, s[24:27], s36 offen offset:0 lds\n\t"
        "ds_read_b64_tr_b16 a[28:29], v139 offset:0\n\t"
        "v_readlane_b32 s44, v155, 32\n\t"
        "s_waitcnt lgkmcnt(4)\n\t"
        "v_mfma_f32_16x16x32_bf16 v[24:27], a[12:15], v[88:91], 0\n\t"
        "ds_read_b64_tr_b16 a[30:31], v139 offset:768\n\t"
        "ds_read_b64_tr_b16 a[32:33], v149 offset:0\n\t"
        "ds_read_b64_tr_b16 a[34:35], v149 offset:768\n\t"
        "v_readlane_b32 s45, v155, 33\n\t"
        "v_mfma_f32_16x16x32_bf16 v[28:31], a[16:19], v[88:91], 0\n\t"
        "buffer_load_dwordx4 v141, s[24:27], s36 offen offset:1024 lds\n\t"
        "ds_read_b128 v[96:99], v128 offset:8192\n\t"
        "v_readlane_b32 s62, v155, 36\n\t"
        "v_mfma_f32_16x16x32_bf16 v[32:35], a[20:23], v[88:91], 0\n\t"
        "ds_read_b128 v[100:103], v128 offset:12288\n\t"
        "s_lshl_b32 s70, s62, 5\n\t"
        "s_waitcnt lgkmcnt(8)\n\t"
        "v_mfma_f32_16x16x32_bf16 v[24:27], a[0:3], v[92:95], v[24:27]\n\t"
        "s_sub_u32 s71, s70, 16384\n\t"
        "s_mul_i32 s71, s71, s68\n\t"
        "v_mfma_f32_16x16x32_bf16 v[28:31], a[4:7], v[92:95], v[28:31]\n\t"
        "buffer_load_dwordx4 v142, s[24:27], s36 offen offset:2048 lds\n\t"
        "s_add_u32 m0, s35, 0\n\t"
        "s_add_u32 s43, s34, 0\n\t"
        "v_mfma_f32_16x16x32_bf16 v[32:35], a[8:11], v[92:95], v[32:35]\n\t"
        "s_mov_b32 exec_lo, 0\n\t"
        "s_mov_b32 exec_hi, 0xfff00000\n\t"
        "ds_write_b32 v119, v155\n\t"
        "s_mov_b64 exec, -1\n\t"
        "v_mfma_f32_16x16x32_bf16 v[36:39], a[12:15], v[92:95], 0\n\t"
        "v_add_u32 v172, s71, v128\n\t"
        "v_add_u32 v173, s71, v129\n\t"
        "v_mfma_f32_16x16x32_bf16 v[40:43], a[16:19], v[92:95], 0\n\t"
        "buffer_load_dwordx4 v143, s[16:19], s43 offen  lds\n\t"
        "s_add_u32 m0, s35, 36864\n\t"
        "s_add_u32 s43, s34, 387072\n\t"
        "s_min_u32 s43, s43, 9590784\n\t"
        "v_mfma_f32_16x16x32_bf16 v[44:47], a[20:23], v[92:95], 0\n\t"
        "buffer_load_dwordx4 v143, s[16:19], s43 offen  lds\n\t"
        "s_add_u32 m0, s37, 12288\n\t"
        "s_add_u32 s36, s36, s5\n\t"
        "buffer_load_dwordx4 v140, s[24:27], s36 offen offset:0 lds\n\t"
        "s_waitcnt vmcnt(9) lgkmcnt(3)\n\t"
        "s_barrier\n\t"
        "ds_read_b128 v[104:107], v128 offset:4096\n\t"
        "ds_read_b64_tr_b16 a[36:37], v138 offset:18432\n\t"
        "s_waitcnt lgkmcnt(3)\n\t"
        "v_mfma_f32_16x16x32_bf16 v[0:3], a[24:27], v[96:99], 0\n\t"
        "buffer_load_dwordx4 v141, s[24:27], s36 offen offset:1024 lds\n\t"
        "ds_read_b64_tr_b16 a[38:39], v138 offset:19200\n\t"
        "v_add_u32 v174, s71, v130\n\t"
        "v_mfma_f32_16x16x32_bf16 v[4:7], a[28:31], v[96:99], 0\n\t"
        "ds_read_b64_tr_b16 a[40:41], v139 offset:18432\n\t"
        "ds_read_b64_tr_b16 a[42:43], v139 offset:19200\n\t"
        "s_add_u32 s71, s71, s70\n\t"
        "v_mfma_f32_16x16x32_bf16 v[8:11], a[32:35], v[96:99], 0\n\t"
        "buffer_load_dwordx4 v142, s[24:27], s36 offen offset:2048 lds\n\t"
        "s_add_u32 m0, s37, 24576\n\t"
        "ds_read_b64_tr_b16 a[44:45], v149 offset:18432\n\t"
        "s_add_u32 s36, s36, s39\n\t"
        "v_mfma_f32_16x16x32_bf16 v[12:15], a[24:27], v[100:103], v[12:15]\n\t"
        "ds_read_b64_tr_b16 a[46:47], v149 offset:19200\n\t"
        "ds_read_b64_tr_b16 a[48:49], v138 offset:24576\n\t"
        "ds_read_b64_tr_b16 a[50:51], v138 offset:25344\n\t"
        "v_add_u32 v175, s71, v128\n\t"
        "v_mfma_f32_16x16x32_bf16 v[16:19], a[28:31], v[100:103], v[16:19]\n\t"
        "ds_read_b64_tr_b16 a[52:53], v139 offset:24576\n\t"
        "ds_read_b64_tr_b16 a[54:55], v139 offset:25344\n\t"
        "ds_read_b64_tr_b16 a[56:57], v149 offset:24576\n\t"
        "v_add_u32 v176, s71, v129\n\t"
        "v_mfma_f32_16x16x32_bf16 v[20:23], a[32:35], v[100:103], v[20:23]\n\t"
        "ds_read_b64_tr_b16 a[58:59], v149 offset:25344\n\t"
        "ds_read_b128 v[108:111], v128 offset:4160\n\t"
        "v_add_u32 v177, s71, v130\n\t"
        "s_add_u32 s71, s71, s70\n\t"
        "s_waitcnt lgkmcnt(12)\n\t"
        "v_mfma_f32_16x16x32_bf16 v[0:3], a[0:3], v[104:107], v[0:3]\n\t"
        "ds_read_b64_tr_b16 a[60:61], v138 offset:30720\n\t"
        "ds_read_b64_tr_b16 a[62:63], v138 offset:31488\n\t"
        "v_add_u32 v178, s71, v128\n\t"
        "v_mfma_f32_16x16x32_bf16 v[4:7], a[4:7], v[104:107], v[4:7]\n\t"
        "ds_read_b64_tr_b16 a[64:65], v139 offset:30720\n\t"
        "ds_read_b64_tr_b16 a[66:67], v139 offset:31488\n\t"
        "v_add_u32 v179, s71, v129\n\t"
        "v_mfma_f32_16x16x32_bf16 v[8:11], a[8:11], v[104:107], v[8:11]\n\t"
        "ds_read_b64_tr_b16 a[68:69], v149 offset:30720\n\t"
        "ds_read_b64_tr_b16 a[70:71], v149 offset:31488\n\t"
        "v_add_u32 v180, s71, v130\n\t"
        "v_mfma_f32_16x16x32_bf16 v[12:15], a[12:15], v[104:107], v[12:15]\n\t"
        "ds_read_b128 v[112:115], v128 offset:8256\n\t"
        "v_add_f32 v78, v78, v56\n\t"
        "v_mfma_f32_16x16x32_bf16 v[16:19], a[16:19], v[104:107], v[16:19]\n\t"
        "s_cmp_eq_u32 s69, 36\n\t"
        "s_cselect_b64 exec, -1, 0\n\t"
        "v_swap_b32 v68, v69\n\t"
        "v_swap_b32 v173, v175\n\t"
        "v_swap_b32 v174, v178\n\t"
        "v_swap_b32 v177, v179\n\t"
        "s_mov_b64 exec, -1\n\t"
        "v_mfma_f32_16x16x32_bf16 v[20:23], a[20:23], v[104:107], v[20:23]\n\t"
        "ds_read_b128 v[84:87], v128 offset:12352\n\t"
        "v_add3_u32 v68, v68, v52, -1\n\t"
        "v_add3_u32 v69, v69, v158, -1\n\t"
        "s_waitcnt lgkmcnt(8)\n\t"
        "v_mfma_f32_16x16x32_bf16 v[0:3], a[36:39], v[108:111], v[0:3]\n\t"
        "ds_read_b64_tr_b16 a[72:73], v138 offset:36864\n\t"
        "ds_read_b64_tr_b16 a[74:75], v138 offset:37632\n\t"
        "v_cvt_f32_i32 v68, v68\n\t"
        "v_mfma_f32_16x16x32_bf16 v[4:7], a[40:43], v[108:111], v[4:7]\n\t"
        "ds_read_b64_tr_b16 a[76:77], v139 offset:36864\n\t"
        "ds_read_b64_tr_b16 a[78:79], v139 offset:37632\n\t"
        "v_cvt_f32_i32 v69, v69\n\t"
        "v_mfma_f32_16x16x32_bf16 v[8:11], a[44:47], v[108:111], v[8:11]\n\t"
        "ds_read_b64_tr_b16 a[80:81], v149 offset:36864\n\t"
        "ds_read_b64_tr_b16 a[82:83], v149 offset:37632\n\t"
        "v_add_f32 v79, v79, v57\n\t"
        "v_mfma_f32_16x16x32_bf16 v[12:15], a[48:51], v[108:111], v[12:15]\n\t"
        "v_readlane_b32 s69, v155, 31\n\t"
        "v_mfma_f32_16x16x32_bf16 v[16:19], a[52:55], v[108:111], v[16:19]\n\t"
        "s_nop 0\n\t"
        "v_mov_b32 v154, s69\n\t"
        "v_mov_b32 v119, s62\n\t"
        "v_mfma_f32_16x16x32_bf16 v[20:23], a[56:59], v[108:111], v[20:23]\n\t"
        "v_readlane_b32 s53, v155, 28\n\t"
        "v_readlane_b32 s9, v155, 46\n\t"
        "s_sub_u32 s10, 43200, s11\n\t"
        "s_waitcnt lgkmcnt(7)\n\t"
        "v_mfma_f32_16x16x32_bf16 v[0:3], a[60:63], v[112:115], v[0:3]\n\t"
        "buffer_load_dwordx4 v140, s[24:27], s36 offen offset:0 lds\n\t"
        "s_mul_i32 s10, s10, s68\n\t"
        "v_mfma_f32_16x16x32_bf16 v[4:7], a[64:67], v[112:115], v[4:7]\n\t"
        "s_sub_u32 s11, s11, 192\n\t"
        "v_mad_u32_u24 v148, v158, s11, v144\n\t"
        "v_mfma_f32_16x16x32_bf16 v[8:11], a[68:71], v[112:115], v[8:11]\n\t"
        "v_add_u32 v148, s10, v148\n\t"
        "v_mad_u32_u24 v150, v83, s11, v145\n\t"
        "v_mfma_f32_16x16x32_bf16 v[12:15], a[36:39], v[112:115], v[12:15]\n\t"
        "s_lshl_b32 s70, s49, 1\n\t"
        "s_add_u32 s70, s70, 49\n\t"
        "v_mfma_f32_16x16x32_bf16 v[16:19], a[40:43], v[112:115], v[16:19]\n\t"
        "buffer_load_dwordx4 v141, s[24:27], s36 offen offset:1024 lds\n\t"
        "v_readlane_b32 s52, v155, s70\n\t"
        "v_mfma_f32_16x16x32_bf16 v[20:23], a[44:47], v[112:115], v[20:23]\n\t"
        "s_nop 0\n\t"
        "s_add_u32 s52, s52, s10\n\t"
        "v_mfma_f32_16x16x32_bf16 v[24:27], a[48:51], v[112:115], v[24:27]\n\t"
        "v_mfma_f32_16x16x32_bf16 v[28:31], a[52:55], v[112:115], v[28:31]\n\t"
        "v_mfma_f32_16x16x32_bf16 v[32:35], a[56:59], v[112:115], v[32:35]\n\t"
        "buffer_load_dwordx4 v142, s[24:27], s36 offen offset:2048 lds\n\t"
        "s_waitcnt lgkmcnt(0)\n\t"
        "v_mfma_f32_16x16x32_bf16 v[12:15], a[60:63], v[84:87], v[12:15]\n\t"
        "v_mfma_f32_16x16x32_bf16 v[16:19], a[64:67], v[84:87], v[16:19]\n\t"
        "v_mfma_f32_16x16x32_bf16 v[20:23], a[68:71], v[84:87], v[20:23]\n\t"
        "v_mfma_f32_16x16x32_bf16 v[24:27], a[36:39], v[84:87], v[24:27]\n\t"
        "buffer_load_dwordx4 v[48:51], v55, s[20:23], s48 offen\n\t"
        "ds_read_b128 v[88:91], v128 offset:4224\n\t"
        "v_mfma_f32_16x16x32_bf16 v[28:31], a[40:43], v[84:87], v[28:31]\n\t"
        "v_mfma_f32_16x16x32_bf16 v[32:35], a[44:47], v[84:87], v[32:35]\n\t"
        "v_mfma_f32_16x16x32_bf16 v[36:39], a[48:51], v[84:87], v[36:39]\n\t"
        "ds_read_b128 v[92:95], v128 offset:8320\n\t"
        "v_mfma_f32_16x16x32_bf16 v[40:43], a[52:55], v[84:87], v[40:43]\n\t"
        "buffer_load_dword v61, v61, s[20:23], s48 offen offset:512\n\t"
        "s_add_u32 m0, s37, 36864\n\t"
        "s_add_u32 s36, s36, s38\n\t"
        "v_mfma_f32_16x16x32_bf16 v[44:47], a[56:59], v[84:87], v[44:47]\n\t"
        "s_waitcnt lgkmcnt(1)\n\t"
        "v_mfma_f32_16x16x32_bf16 v[0:3], a[72:75], v[88:91], v[0:3]\n\t"
        "ds_read_b128 v[96:99], v128 offset:12416\n\t"
        "v_mfma_f32_16x16x32_bf16 v[4:7], a[76:79], v[88:91], v[4:7]\n\t"
        "s_waitcnt vmcnt(10)\n\t"
        "s_barrier\n\t"
        "ds_read_b128 v[100:103], v128 offset:16384\n\t"
        "ds_read_b128 v[104:107], v128 offset:16448\n\t"
        "v_mfma_f32_16x16x32_bf16 v[8:11], a[80:83], v[88:91], v[8:11]\n\t"
        "ds_read_b32 v64, v70 offset:0\n\t"
        "buffer_load_dwordx4 v140, s[24:27], s36 offen offset:0 lds\n\t"
        "v_readlane_b32 s46, v155, 37\n\t"
        "v_readlane_b32 s47, v155, 40\n\t"
        "s_waitcnt lgkmcnt(4)\n\t"
        "v_mfma_f32_16x16x32_bf16 v[12:15], a[72:75], v[92:95], v[12:15]\n\t"
        "ds_read_b32 v67, v67 offset:0\n\t"
        "ds_read_b64_tr_b16 a[84:85], v131 offset:0\n\t"
        "v_add_f32 v58, s46, v68\n\t"
        "v_mfma_f32_16x16x32_bf16 v[16:19], a[76:79], v[92:95], v[16:19]\n\t"
        "ds_read_b32 v66, v70 offset:2560\n\t"
        "ds_read_b64_tr_b16 a[86:87], v131 offset:768\n\t"
        "v_add_f32 v59, s47, v69\n\t"
        "v_readlane_b32 s47, v155, 42\n\t"
        "v_mfma_f32_16x16x32_bf16 v[20:23], a[80:83], v[92:95], v[20:23]\n\t"
        "ds_read_b64_tr_b16 a[88:89], v132 offset:0\n\t"
        "ds_read_b64_tr_b16 a[90:91], v132 offset:768\n\t"
        "s_waitcnt lgkmcnt(9)\n\t"
        "v_mfma_f32_16x16x32_bf16 v[24:27], a[72:75], v[96:99], v[24:27]\n\t"
        "buffer_load_dwordx4 v141, s[24:27], s36 offen offset:1024 lds\n\t"
        "ds_read_b64_tr_b16 a[92:93], v133 offset:0\n\t"
        "v_mfma_f32_16x16x32_bf16 v[28:31], a[76:79], v[96:99], v[28:31]\n\t"
        "ds_read_b64_tr_b16 a[94:95], v133 offset:768\n\t"
        "ds_read_b128 v[108:111], v128 offset:4224\n\t"
        "v_mfma_f32_16x16x32_bf16 v[32:35], a[80:83], v[96:99], v[32:35]\n\t"
        "ds_read_b64_tr_b16 a[96:97], v131 offset:6144\n\t"
        "ds_read_b64_tr_b16 a[98:99], v131 offset:6912\n\t"
        "s_waitcnt lgkmcnt(12)\n\t"
        "v_mfma_f32_16x16x32_bf16 v[24:27], a[24:27], v[100:103], v[24:27]\n\t"
        "ds_read_b64_tr_b16 a[100:101], v132 offset:6144\n\t"
        "ds_read_b64_tr_b16 a[102:103], v132 offset:6912\n\t"
        "v_mfma_f32_16x16x32_bf16 v[28:31], a[28:31], v[100:103], v[28:31]\n\t"
        "buffer_load_dwordx4 v142, s[24:27], s36 offen offset:2048 lds\n\t"
        "s_add_u32 m0, s37, 49152\n\t"
        "ds_read_b64_tr_b16 a[104:105], v133 offset:6144\n\t"
        "v_mfma_f32_16x16x32_bf16 v[32:35], a[32:35], v[100:103], v[32:35]\n\t"
        "ds_read_b64_tr_b16 a[106:107], v133 offset:6912\n\t"
        "s_add_u32 s36, s36, s5\n\t"
        "s_waitcnt lgkmcnt(12)\n\t"
        "v_perm_b32 v56, v64, v64, s44\n\t"
        "v_mfma_f32_16x16x32_bf16 v[36:39], a[0:3], v[100:103], v[36:39]\n\t"
        "ds_read_b128 v[112:115], v128 offset:8320\n\t"
        "v_perm_b32 v57, v64, v64, s45\n\t"
        "v_mfma_f32_16x16x32_bf16 v[40:43], a[4:7], v[100:103], v[40:43]\n\t"
        "ds_read_b128 v[84:87], v128 offset:12416\n\t"
        "v_add_f32 v56, v56, v58\n\t"
        "v_mfma_f32_16x16x32_bf16 v[44:47], a[8:11], v[100:103], v[44:47]\n\t"
        "buffer_load_dwordx4 v140, s[24:27], s36 offen offset:0 lds\n\t"
        "v_add_f32 v57, v57, v59\n\t"
        "v_mfma_f32_16x16x32_bf16 v[24:27], a[60:63], v[104:107], v[24:27]\n\t"
        "ds_read_b128 v[88:91], v128 offset:16512\n\t"
        "v_cvt_pk_bf16_f32 v52, v56, v57\n\t"
        "v_mfma_f32_16x16x32_bf16 v[28:31], a[64:67], v[104:107], v[28:31]\n\t"
        "v_cvt_pk_bf16_f32 v58, v58, v59\n\t"
        "v_cmp_ne_u32 s[64:65], v52, v58\n\t"
        "v_perm_b32 v57, v67, v67, s45\n\t"
        "v_mfma_f32_16x16x32_bf16 v[32:35], a[68:71], v[104:107], v[32:35]\n\t"
        "v_mbcnt_lo_u32_b32 v56, s64, 1\n\t"
        "v_mbcnt_hi_u32_b32 v56, s65, v56\n\t"
        "v_mfma_f32_16x16x32_bf16 v[36:39], a[36:39], v[104:107], v[36:39]\n\t"
        "buffer_load_dwordx4 v141, s[24:27], s36 offen offset:1024 lds\n\t"
        "s_bcnt1_i32_b64 s61, s[64:65]\n\t"
        "v_mfma_f32_16x16x32_bf16 v[40:43], a[40:43], v[104:107], v[40:43]\n\t"
        "v_add_u32 v54, s61, 1\n\t"
        "v_lshl_add_u32 v56, v56, 3, s60\n\t"
        "v_mfma_f32_16x16x32_bf16 v[44:47], a[44:47], v[104:107], v[44:47]\n\t"
        "s_mov_b64 exec, s[64:65]\n\t"
        "ds_write_b64 v56, v[52:53]\n\t"
        "s_mov_b64 exec, -1\n\t"
        "s_waitcnt lgkmcnt(4)\n\t"
        "v_mfma_f32_16x16x32_bf16 v[12:15], a[84:87], v[108:111], v[12:15]\n\t"
        "v_swap_b32 v53, v80\n\t"
        "v_perm_b32 v56, v67, v67, s44\n\t"
        "v_mfma_f32_16x16x32_bf16 v[16:19], a[88:91], v[108:111], v[16:19]\n\t"
        "buffer_load_dwordx4 v142, s[24:27], s36 offen offset:2048 lds\n\t"
        "v_readlane_b32 s40, v155, 12\n\t"
        "v_readlane_b32 s43, v155, 0\n\t"
        "v_mfma_f32_16x16x32_bf16 v[20:23], a[92:95], v[108:111], v[20:23]\n\t"
        "s_add_u32 m0, s40, s35\n\t"
        "v_add_f32 v56, v56, v78\n\t"
        "s_waitcnt lgkmcnt(1)\n\t"
        "v_mfma_f32_16x16x32_bf16 v[0:3], a[96:99], v[112:115], v[0:3]\n\t"
        "v_add_f32 v57, v57, v79\n\t"
        "v_cvt_pk_bf16_f32 v52, v56, v57\n\t"
        "v_mfma_f32_16x16x32_bf16 v[4:7], a[100:103], v[112:115], v[4:7]\n\t"
        "v_cvt_pk_bf16_f32 v58, v78, v79\n\t"
        "v_cmp_ne_u32 s[64:65], v52, v58\n\t"
        "v_mfma_f32_16x16x32_bf16 v[8:11], a[104:107], v[112:115], v[8:11]\n\t"
        "buffer_load_dwordx4 v134, s[16:19], s43 offen  lds\n\t"
        "v_readlane_b32 s40, v155, 13\n\t"
        "v_mfma_f32_16x16x32_bf16 v[24:27], a[84:87], v[112:115], v[24:27]\n\t"
        "v_readlane_b32 s43, v155, 1\n\t"
        "s_add_u32 m0, s40, s35\n\t"
        "v_mfma_f32_16x16x32_bf16 v[28:31], a[88:91], v[112:115], v[28:31]\n\t"
        "s_waitcnt vmcnt(9)\n\t"
        "s_barrier\n\t"
        "v_mfma_f32_16x16x32_bf16 v[32:35], a[92:95], v[112:115], v[32:35]\n\t"
        "ds_read_b128 v[92:95], v128 offset:0\n\t"
        "s_and_b64 s[64:65], s[64:65], 0xffffff\n\t"
        "v_mbcnt_lo_u32_b32 v56, s64, v54\n\t"
        "v_mfma_f32_16x16x32_bf16 v[12:15], a[96:99], v[84:87], v[12:15]\n\t"
        "buffer_load_dwordx4 v135, s[16:19], s43 offen  lds\n\t"
        "ds_read_b128 v[96:99], v128 offset:64\n\t"
        "v_readlane_b32 s40, v155, 14\n\t"
        "v_mfma_f32_16x16x32_bf16 v[16:19], a[100:103], v[84:87], v[16:19]\n\t"
        "ds_read_b128 v[100:103], v128 offset:128\n\t"
        "ds_read_b128 v[104:107], v128 offset:20480\n\t"
        "v_readlane_b32 s43, v155, 2\n\t"
        "v_mfma_f32_16x16x32_bf16 v[20:23], a[104:107], v[84:87], v[20:23]\n\t"
        "ds_read_b64_tr_b16 a[108:109], v131 offset:12288\n\t"
        "s_add_u32 m0, s40, s35\n\t"
        "ds_read_b64_tr_b16 a[110:111], v131 offset:13056\n\t"
        "v_mfma_f32_16x16x32_bf16 v[36:39], a[84:87], v[84:87], v[36:39]\n\t"
        "ds_read_b128 v[108:111], v128 offset:20544\n\t"
        "ds_read_b64_tr_b16 a[112:113], v132 offset:12288\n\t"
        "s_bcnt1_i32_b64 s46, s[64:65]\n\t"
        "v_mfma_f32_16x16x32_bf16 v[40:43], a[88:91], v[84:87], v[40:43]\n\t"
        "buffer_load_dwordx4 v134, s[16:19], s43 offen  lds\n\t"
        "ds_read_b64_tr_b16 a[114:115], v132 offset:13056\n\t"
        "s_bitcmp1_b32 s42, 10\n\t"
        "s_cbranch_scc0 .Lx62%=\n\t"
        "v_readlane_b32 s40, v155, 20\n\t"
        "v_readlane_b32 s43, v155, 8\n\t"
        "s_nop 0\n\t"
        "s_add_u32 m0, s40, s35\n\t"
        "s_nop 2\n\t"
        "buffer_load_dwordx4 v136, s[16:19], s43 offen  lds\n\t"
        ".Lx62%=:\n\t"
        "v_mfma_f32_16x16x32_bf16 v[44:47], a[92:95], v[84:87], v[44:47]\n\t"
        "ds_read_b64_tr_b16 a[116:117], v133 offset:12288\n\t"
        "ds_read_b64_tr_b16 a[118:119], v133 offset:13056\n\t"
        "v_readlane_b32 s40, v155, 15\n\t"
        "v_mfma_f32_16x16x32_bf16 v[24:27], a[96:99], v[88:91], v[24:27]\n\t"
        "ds_read_b128 v[112:115], v128 offset:20608\n\t"
        "v_readlane_b32 s43, v155, 3\n\t"
        "v_mfma_f32_16x16x32_bf16 v[28:31], a[100:103], v[88:91], v[28:31]\n\t"
        "ds_read_b64_tr_b16 a[120:121], v131 offset:18432\n\t"
        "s_add_u32 m0, s40, s35\n\t"
        "v_mfma_f32_16x16x32_bf16 v[32:35], a[104:107], v[88:91], v[32:35]\n\t"
        "ds_read_b64_tr_b16 a[122:123], v131 offset:19200\n\t"
        "s_add_u32 s61, s61, s46\n\t"
        "v_add_u32 v54, s46, v54\n\t"
        "v_mfma_f32_16x16x32_bf16 v[36:39], a[72:75], v[88:91], v[36:39]\n\t"
        "ds_read_b64_tr_b16 a[124:125], v132 offset:18432\n\t"
        "s_mov_b32 s3, s61\n\t"
        "v_mfma_f32_16x16x32_bf16 v[40:43], a[76:79], v[88:91], v[40:43]\n\t"
        "ds_read_b64_tr_b16 a[126:127], v132 offset:19200\n\t"
        "v_lshl_add_u32 v56, v56, 3, s60\n\t"
        "v_mfma_f32_16x16x32_bf16 v[44:47], a[80:83], v[88:91], v[44:47]\n\t"
        "s_mov_b64 exec, s[64:65]\n\t"
        "ds_write_b64 v56, v[52:53]\n\t"
        "s_mov_b64 exec, -1\n\t"
        "s_waitcnt lgkmcnt(8)\n\t"
        "v_mfma_f32_16x16x32_bf16 v[0:3], a[12:15], v[92:95], v[0:3]\n\t"
        "ds_read_b64_tr_b16 a[128:129], v133 offset:18432\n\t"
        "ds_read_b64_tr_b16 a[130:131], v133 offset:19200\n\t"
        "v_add_u32 v53, 4096, v80\n\t"
        "v_mfma_f32_16x16x32_bf16 v[4:7], a[16:19], v[92:95], v[4:7]\n\t"
        "ds_read_b128 v[84:87], v129 offset:0\n\t"
        "v_readlane_b32 s46, v155, 39\n\t"
        "v_mfma_f32_16x16x32_bf16 v[8:11], a[20:23], v[92:95], v[8:11]\n\t"
        "ds_read_b128 v[88:91], v129 offset:4096\n\t"
        "v_perm_b32 v56, v66, v66, s44\n\t"
        "v_mfma_f32_16x16x32_bf16 v[0:3], a[48:51], v[96:99], v[0:3]\n\t"
        "ds_read_b64_tr_b16 a[132:133], v131 offset:24576\n\t"
        "v_perm_b32 v57, v66, v66, s45\n\t"
        "v_mfma_f32_16x16x32_bf16 v[4:7], a[52:55], v[96:99], v[4:7]\n\t"
        "buffer_load_dwordx4 v135, s[16:19], s43 offen  lds\n\t"
        "s_add_u32 m0, s37, 0\n\t"
        "v_mfma_f32_16x16x32_bf16 v[8:11], a[56:59], v[96:99], v[8:11]\n\t"
        "ds_read_b64_tr_b16 a[134:135], v131 offset:25344\n\t"
        "s_add_u32 s36, s36, s39\n\t"
        "v_mfma_f32_16x16x32_bf16 v[0:3], a[84:87], v[100:103], v[0:3]\n\t"
        "ds_read_b64_tr_b16 a[136:137], v132 offset:24576\n\t"
        "v_add_f32 v58, s46, v68\n\t"
        "v_add_f32 v59, s47, v69\n\t"
        "v_mfma_f32_16x16x32_bf16 v[4:7], a[88:91], v[100:103], v[4:7]\n\t"
        "ds_read_b64_tr_b16 a[138:139], v132 offset:25344\n\t"
        "v_add_f32 v56, v56, v58\n\t"
        "v_mfma_f32_16x16x32_bf16 v[8:11], a[92:95], v[100:103], v[8:11]\n\t"
        "ds_read_b64_tr_b16 a[140:141], v133 offset:24576\n\t"
        "v_add_f32 v57, v57, v59\n\t"
        "v_mfma_f32_16x16x32_bf16 v[36:39], a[24:27], v[104:107], v[36:39]\n\t"
        "buffer_load_dwordx4 v140, s[24:27], s36 offen offset:0 lds\n\t"
        "v_max_f32 v56, 0, v56\n\t"
        "v_mfma_f32_16x16x32_bf16 v[40:43], a[28:31], v[104:107], v[40:43]\n\t"
        "ds_read_b64_tr_b16 a[142:143], v133 offset:25344\n\t"
        "v_max_f32 v57, 0, v57\n\t"
        "v_mfma_f32_16x16x32_bf16 v[44:47], a[32:35], v[104:107], v[44:47]\n\t"
        "ds_read_b128 v[92:95], v129 offset:8192\n\t"
        "v_max_f32 v58, 0, v58\n\t"
        "v_mfma_f32_16x16x32_bf16 v[36:39], a[60:63], v[108:111], v[36:39]\n\t"
        "ds_read_b64_tr_b16 a[144:145], v131 offset:30720\n\t"
        "v_max_f32 v59, 0, v59\n\t"
        "v_mfma_f32_16x16x32_bf16 v[40:43], a[64:67], v[108:111], v[40:43]\n\t"
        "buffer_load_dwordx4 v141, s[24:27], s36 offen offset:1024 lds\n\t"
        "v_cvt_pk_bf16_f32 v52, v56, v57\n\t"
        "v_mfma_f32_16x16x32_bf16 v[44:47], a[68:71], v[108:111], v[44:47]\n\t"
        "ds_read_b64_tr_b16 a[146:147], v131 offset:31488\n\t"
        "v_cvt_pk_bf16_f32 v58, v58, v59\n\t"
        "s_waitcnt lgkmcnt(7)\n\t"
        "v_mfma_f32_16x16x32_bf16 v[36:39], a[96:99], v[112:115], v[36:39]\n\t"
        "ds_read_b128 v[96:99], v129 offset:12288\n\t"
        ".LpX0%=:\n\t"
        "s_cbranch_execz .LrX%=\n\t"
        "v_cmp_ne_u32 s[64:65], v52, v58\n\t"
        "v_mfma_f32_16x16x32_bf16 v[40:43], a[100:103], v[112:115], v[40:43]\n\t"
        "ds_read_b64_tr_b16 a[148:149], v132 offset:30720\n\t"
        "v_mbcnt_lo_u32_b32 v56, s64, v54\n\t"
        "v_mfma_f32_16x16x32_bf16 v[44:47], a[104:107], v[112:115], v[44:47]\n\t"
        "buffer_load_dwordx4 v142, s[24:27], s36 offen offset:2048 lds\n\t"
        "s_add_u32 m0, s37, 12288\n\t"
        "s_add_u32 s36, s36, s38\n\t"
        "v_mfma_f32_16x16x32_bf16 v[0:3], a[120:123], v[84:87], v[0:3]\n\t"
        "ds_read_b64_tr_b16 a[150:151], v132 offset:31488\n\t"
        "v_mbcnt_hi_u32_b32 v56, s65, v56\n\t"
        "v_mfma_f32_16x16x32_bf16 v[4:7], a[124:127], v[84:87], v[4:7]\n\t"
        "ds_read_b64_tr_b16 a[152:153], v133 offset:30720\n\t"
        "s_bcnt1_i32_b64 s46, s[64:65]\n\t"
        "v_mfma_f32_16x16x32_bf16 v[8:11], a[128:131], v[84:87], v[8:11]\n\t"
        "ds_read_b64_tr_b16 a[154:155], v133 offset:31488\n\t"
        "s_add_u32 s61, s61, s46\n\t"
        "v_mfma_f32_16x16x32_bf16 v[0:3], a[108:111], v[88:91], v[0:3]\n\t"
        "v_min_u32 v56, 97, v56\n\t"
        "v_lshl_add_u32 v56, v56, 3, s60\n\t"
        "v_mfma_f32_16x16x32_bf16 v[4:7], a[112:115], v[88:91], v[4:7]\n\t"
        "s_mov_b64 exec, s[64:65]\n\t"
        "ds_write_b64 v56, v[52:53]\n\t"
        "s_mov_b64 exec, -1\n\t"
        "v_mfma_f32_16x16x32_bf16 v[8:11], a[116:119], v[88:91], v[8:11]\n\t"
        "s_cmp_gt_u32 s61, s53\n\t"
        "s_cselect_b32 s8, 1, 0\n\t"
        "v_mfma_f32_16x16x32_bf16 v[12:15], a[120:123], v[88:91], v[12:15]\n\t"
        "ds_read_b128 v[100:103], v129 offset:16384\n\t"
        "s_waitcnt vmcnt(14)\n\t"
        "v_and_b32 v48, s54, v48\n\t"
        "v_mfma_f32_16x16x32_bf16 v[16:19], a[124:127], v[88:91], v[16:19]\n\t"
        "v_pk_max_u16 v60, v154, v48\n\t"
        "v_and_b32 v49, s54, v49\n\t"
        "v_mfma_f32_16x16x32_bf16 v[20:23], a[128:131], v[88:91], v[20:23]\n\t"
        "v_pk_max_u16 v60, v60, v49\n\t"
        "v_and_b32 v50, s54, v50\n\t"
        "s_waitcnt lgkmcnt(5)\n\t"
        "v_mfma_f32_16x16x32_bf16 v[0:3], a[132:135], v[92:95], v[0:3]\n\t"
        "v_pk_max_u16 v60, v60, v50\n\t"
        "v_and_b32 v51, s54, v51\n\t"
        "v_mfma_f32_16x16x32_bf16 v[4:7], a[136:139], v[92:95], v[4:7]\n\t"
        "v_pk_max_u16 v60, v60, v51\n\t"
        "s_waitcnt vmcnt(13)\n\t"
        "v_and_b32 v61, s54, v61\n\t"
        "v_mfma_f32_16x16x32_bf16 v[8:11], a[140:143], v[92:95], v[8:11]\n\t"
        "v_pk_max_u16 v60, v60, v61\n\t"
        "v_cmp_ne_u32 s[12:13], v60, v154\n\t"
        "v_mfma_f32_16x16x32_bf16 v[12:15], a[108:111], v[92:95], v[12:15]\n\t"
        "ds_read_b128 v[104:107], v129 offset:20480\n\t"
        "s_lshl_b32 s6, 256, s4\n\t"
        "v_mfma_f32_16x16x32_bf16 v[16:19], a[112:115], v[92:95], v[16:19]\n\t"
        "s_cmp_lg_u64 s[12:13], 0\n\t"
        "s_cselect_b32 s6, s6, 0\n\t"
        "v_mfma_f32_16x16x32_bf16 v[20:23], a[116:119], v[92:95], v[20:23]\n\t"
        "s_or_b32 s56, s56, s8\n\t"
        "s_min_u32 s61, s61, 80\n\t"
        "v_mfma_f32_16x16x32_bf16 v[24:27], a[120:123], v[92:95], v[24:27]\n\t"
        "s_waitcnt vmcnt(7) lgkmcnt(3)\n\t"
        "s_barrier\n\t"
        "v_mfma_f32_16x16x32_bf16 v[28:31], a[124:127], v[92:95], v[28:31]\n\t"
        "s_cmp_gt_u32 s3, 64\n\t"
        "s_cselect_b32 s8, 1, 0\n\t"
        "v_mfma_f32_16x16x32_bf16 v[32:35], a[128:131], v[92:95], v[32:35]\n\t"
        "buffer_load_dwordx4 v140, s[24:27], s36 offen offset:0 lds\n\t"
        "s_or_b32 s56, s56, s8\n\t"
        "v_mfma_f32_16x16x32_bf16 v[12:15], a[132:135], v[96:99], v[12:15]\n\t"
        "ds_read_b64_tr_b16 a[156:157], v131 offset:36864\n\t"
        "ds_read_b64_tr_b16 a[158:159], v131 offset:37632\n\t"
        "v_min_u32 v78, s61, v116\n\t"
        "v_mfma_f32_16x16x32_bf16 v[16:19], a[136:139], v[96:99], v[16:19]\n\t"
        "ds_read_b64_tr_b16 a[160:161], v132 offset:36864\n\t"
        "ds_read_b64_tr_b16 a[162:163], v132 offset:37632\n\t"
        "v_lshl_add_u32 v78, v78, 3, s60\n\t"
        "v_mfma_f32_16x16x32_bf16 v[20:23], a[140:143], v[96:99], v[20:23]\n\t"
        "ds_read_b64_tr_b16 a[164:165], v133 offset:36864\n\t"
        "ds_read_b64_tr_b16 a[166:167], v133 offset:37632\n\t"
        "v_cmp_ge_u32 s[66:67], s61, v116\n\t"
        "v_mfma_f32_16x16x32_bf16 v[24:27], a[108:111], v[96:99], v[24:27]\n\t"
        "buffer_load_dwordx4 v141, s[24:27], s36 offen offset:1024 lds\n\t"
        "s_mov_b32 s46, 0x07060302\n\t"
        "v_mfma_f32_16x16x32_bf16 v[28:31], a[112:115], v[96:99], v[28:31]\n\t"
        "ds_read_b128 v[108:111], v129 offset:64\n\t"
        "v_mfma_f32_16x16x32_bf16 v[32:35], a[116:119], v[96:99], v[32:35]\n\t"
        "ds_read_b128 v[112:115], v129 offset:4160\n\t"
        "v_mfma_f32_16x16x32_bf16 v[36:39], a[120:123], v[96:99], v[36:39]\n\t"
        "ds_read_b64 v[62:63], v78\n\t"
        "v_mfma_f32_16x16x32_bf16 v[40:43], a[124:127], v[96:99], v[40:43]\n\t"
        "buffer_load_dwordx4 v142, s[24:27], s36 offen offset:2048 lds\n\t"
        "v_readlane_b32 s40, v155, 16\n\t"
        "v_mfma_f32_16x16x32_bf16 v[44:47], a[128:131], v[96:99], v[44:47]\n\t"
        "ds_read_b64_tr_b16 a[168:169], v131 offset:43008\n\t"
        "v_readlane_b32 s43, v155, 4\n\t"
        "s_waitcnt lgkmcnt(9)\n\t"
        "v_mfma_f32_16x16x32_bf16 v[24:27], a[132:135], v[100:103], v[24:27]\n\t"
        "ds_read_b64_tr_b16 a[170:171], v131 offset:43776\n\t"
        "s_add_u32 m0, s40, s35\n\t"
        "v_mfma_f32_16x16x32_bf16 v[28:31], a[136:139], v[100:103], v[28:31]\n\t"
        "ds_read_b64_tr_b16 a[172:173], v132 offset:43008\n\t"
        "v_mfma_f32_16x16x32_bf16 v[32:35], a[140:143], v[100:103], v[32:35]\n\t"
        "buffer_load_dwordx4 v134, s[16:19], s43 offen  lds\n\t"
        "s_bitcmp1_b32 s42, 11\n\t"
        "s_cbranch_scc0 .Lx64%=\n\t"
        "v_readlane_b32 s40, v155, 21\n\t"
        "v_readlane_b32 s43, v155, 9\n\t"
        "s_nop 0\n\t"
        "s_add_u32 m0, s40, s35\n\t"
        "s_nop 2\n\t"
        "buffer_load_dwordx4 v137, s[16:19], s43 offen  lds\n\t"
        "v_readlane_b32 s40, v155, 22\n\t"
        "v_readlane_b32 s43, v155, 10\n\t"
        "s_nop 0\n\t"
        "s_add_u32 m0, s40, s35\n\t"
        "s_nop 2\n\t"
        "buffer_load_dwordx4 v136, s[16:19], s43 offen  lds\n\t"
        ".Lx64%=:\n\t"
        "v_readlane_b32 s40, v155, 17\n\t"
        "v_mfma_f32_16x16x32_bf16 v[36:39], a[108:111], v[100:103], v[36:39]\n\t"
        "ds_read_b64_tr_b16 a[174:175], v132 offset:43776\n\t"
        "ds_read_b64_tr_b16 a[176:177], v133 offset:43008\n\t"
        "v_readlane_b32 s43, v155, 5\n\t"
        "v_mfma_f32_16x16x32_bf16 v[40:43], a[112:115], v[100:103], v[40:43]\n\t"
        "ds_read_b64_tr_b16 a[178:179], v133 offset:43776\n\t"
        "s_add_u32 m0, s40, s35\n\t"
        "v_mfma_f32_16x16x32_bf16 v[44:47], a[116:119], v[100:103], v[44:47]\n\t"
        "ds_read_b128 v[84:87], v129 offset:8256\n\t"
        "v_mfma_f32_16x16x32_bf16 v[36:39], a[132:135], v[104:107], v[36:39]\n\t"
        "s_mov_b64 exec, s[66:67]\n\t"
        "s_waitcnt lgkmcnt(7)\n\t"
        "ds_write_b32 v63, v117\n\t"
        "s_mov_b64 exec, -1\n\t"
        "v_mfma_f32_16x16x32_bf16 v[40:43], a[136:139], v[104:107], v[40:43]\n\t"
        "buffer_load_dwordx4 v135, s[16:19], s43 offen  lds\n\t"
        "v_readlane_b32 s40, v155, 18\n\t"
        "v_mfma_f32_16x16x32_bf16 v[44:47], a[140:143], v[104:107], v[44:47]\n\t"
        "ds_read_b128 v[88:91], v129 offset:12352\n\t"
        "v_readlane_b32 s43, v155, 6\n\t"
        "v_mfma_f32_16x16x32_bf16 v[0:3], a[156:159], v[108:111], v[0:3]\n\t"
        "ds_read_b64_tr_b16 a[180:181], v131 offset:49152\n\t"
        "s_add_u32 m0, s40, s35\n\t"
        "v_mfma_f32_16x16x32_bf16 v[4:7], a[160:163], v[108:111], v[4:7]\n\t"
        "ds_read_b64_tr_b16 a[182:183], v131 offset:49920\n\t"
        "v_and_b32 v78, 0xfffff800, v63\n\t"
        "v_mfma_f32_16x16x32_bf16 v[8:11], a[164:167], v[108:111], v[8:11]\n\t"
        "ds_read_b128 v[64:67], v78 offset:1536\n\t"
        "v_lshlrev_b32 v72, 16, v62\n\t"
        "v_mfma_f32_16x16x32_bf16 v[0:3], a[144:147], v[112:115], v[0:3]\n\t"
        "buffer_load_dwordx4 v136, s[16:19], s43 offen  lds\n\t"
        "s_bitcmp1_b32 s42, 11\n\t"
        "s_cbranch_scc0 .Lx66%=\n\t"
        "v_readlane_b32 s40, v155, 23\n\t"
        "v_readlane_b32 s43, v155, 11\n\t"
        "s_nop 0\n\t"
        "s_add_u32 m0, s40, s35\n\t"
        "s_nop 2\n\t"
        "buffer_load_dwordx4 v137, s[16:19], s43 offen  lds\n\t"
        ".Lx66%=:\n\t"
        "v_mfma_f32_16x16x32_bf16 v[4:7], a[148:151], v[112:115], v[4:7]\n\t"
        "ds_read_b64_tr_b16 a[184:185], v132 offset:49152\n\t"
        "v_readlane_b32 s40, v155, 19\n\t"
        "v_mfma_f32_16x16x32_bf16 v[8:11], a[152:155], v[112:115], v[8:11]\n\t"
        "ds_read_b64_tr_b16 a[186:187], v132 offset:49920\n\t"
        "v_readlane_b32 s43, v155, 7\n\t"
        "v_mfma_f32_16x16x32_bf16 v[12:15], a[156:159], v[112:115], v[12:15]\n\t"
        "ds_read_b128 v[92:95], v129 offset:16448\n\t"
        "s_add_u32 m0, s40, s35\n\t"
        "ds_read_b64_tr_b16 a[188:189], v133 offset:49152\n\t"
        "v_mfma_f32_16x16x32_bf16 v[16:19], a[160:163], v[112:115], v[16:19]\n\t"
        "buffer_load_dwordx4 v137, s[16:19], s43 offen  lds\n\t"
        "s_add_u32 m0, s37, 24576\n\t"
        "ds_read_b64_tr_b16 a[190:191], v133 offset:49920\n\t"
        "v_mfma_f32_16x16x32_bf16 v[20:23], a[164:167], v[112:115], v[20:23]\n\t"
        "ds_read_b64_tr_b16 a[192:193], v131 offset:55296\n\t"
        "s_add_u32 s36, s36, s5\n\t"
        "s_waitcnt lgkmcnt(8)\n\t"
        "v_mfma_f32_16x16x32_bf16 v[0:3], a[168:171], v[84:87], v[0:3]\n\t"
        "ds_read_b64_tr_b16 a[194:195], v131 offset:56064\n\t"
        "v_and_b32 v73, 0xffff0000, v62\n\t"
        "v_mfma_f32_16x16x32_bf16 v[4:7], a[172:175], v[84:87], v[4:7]\n\t"
        "ds_read_b64_tr_b16 a[196:197], v132 offset:55296\n\t"
        "v_med3_f32 v72, v72, 0, s63\n\t"
        "v_mfma_f32_16x16x32_bf16 v[8:11], a[176:179], v[84:87], v[8:11]\n\t"
        "buffer_load_dwordx4 v140, s[24:27], s36 offen offset:0 lds\n\t"
        "v_med3_f32 v73, v73, 0, s63\n\t"
        "v_mfma_f32_16x16x32_bf16 v[12:15], a[144:147], v[84:87], v[12:15]\n\t"
        "ds_read_b128 v[96:99], v129 offset:20544\n\t"
        "v_perm_b32 v62, v73, v72, s46\n\t"
        "v_mfma_f32_16x16x32_bf16 v[16:19], a[148:151], v[84:87], v[16:19]\n\t"
        "ds_read_b64_tr_b16 a[198:199], v132 offset:56064\n\t"
        "v_fract_f32 v74, v72\n\t"
        "v_mfma_f32_16x16x32_bf16 v[20:23], a[152:155], v[84:87], v[20:23]\n\t"
        "ds_read_b64_tr_b16 a[200:201], v133 offset:55296\n\t"
        "v_fract_f32 v75, v73\n\t"
        "v_mfma_f32_16x16x32_bf16 v[24:27], a[156:159], v[84:87], v[24:27]\n\t"
        "buffer_load_dwordx4 v141, s[24:27], s36 offen offset:1024 lds\n\t"
        "v_cvt_u32_f32 v76, v72\n\t"
        "v_mfma_f32_16x16x32_bf16 v[28:31], a[160:163], v[84:87], v[28:31]\n\t"
        "ds_read_b64_tr_b16 a[202:203], v133 offset:56064\n\t"
        "v_cvt_u32_f32 v77, v73\n\t"
        "v_mfma_f32_16x16x32_bf16 v[32:35], a[164:167], v[84:87], v[32:35]\n\t"
        "ds_read_b128 v[100:103], v129 offset:128\n\t"
        "v_cmp_neq_f32 s[64:65], 0, v74\n\t"
        "v_mfma_f32_16x16x32_bf16 v[12:15], a[168:171], v[88:91], v[12:15]\n\t"
        "ds_read_b128 v[104:107], v129 offset:4224\n\t"
        "v_cmp_neq_f32 s[44:45], 0, v75\n\t"
        "v_mfma_f32_16x16x32_bf16 v[16:19], a[172:175], v[88:91], v[16:19]\n\t"
        "buffer_load_dwordx4 v142, s[24:27], s36 offen offset:2048 lds\n\t"
        "s_add_u32 m0, s37, 36864\n\t"
        "v_mfma_f32_16x16x32_bf16 v[20:23], a[176:179], v[88:91], v[20:23]\n\t"
        "s_add_u32 s36, s36, s39\n\t"
        "v_sub_f32 v78, 1.0, v74\n\t"
        "v_mfma_f32_16x16x32_bf16 v[24:27], a[144:147], v[88:91], v[24:27]\n\t"
        "s_waitcnt vmcnt(10)\n\t"
        "s_barrier\n\t"
        "v_mfma_f32_16x16x32_bf16 v[28:31], a[148:151], v[88:91], v[28:31]\n\t"
        "ds_read_b64_tr_b16 a[204:205], v131 offset:0\n\t"
        "v_sub_f32 v79, 1.0, v75\n\t"
        "v_mfma_f32_16x16x32_bf16 v[32:35], a[152:155], v[88:91], v[32:35]\n\t"
        "buffer_load_dwordx4 v140, s[24:27], s36 offen offset:0 lds\n\t"
        "v_cvt_pk_bf16_f32 v79, v78, v79\n\t"
        "v_mfma_f32_16x16x32_bf16 v[36:39], a[156:159], v[88:91], v[36:39]\n\t"
        "ds_read_b64_tr_b16 a[206:207], v131 offset:768\n\t"
        "v_lshlrev_b32 v78, 16, v79\n\t"
        "v_mfma_f32_16x16x32_bf16 v[40:43], a[160:163], v[88:91], v[40:43]\n\t"
        "ds_read_b64_tr_b16 a[208:209], v132 offset:0\n\t"
        "v_and_b32 v79, 0xffff0000, v79\n\t"
        "v_mfma_f32_16x16x32_bf16 v[44:47], a[164:167], v[88:91], v[44:47]\n\t"
        "ds_read_b64_tr_b16 a[210:211], v132 offset:768\n\t"
        "v_mul_f32 v80, v78, v79\n\t"
        "s_waitcnt lgkmcnt(6)\n\t"
        "v_mfma_f32_16x16x32_bf16 v[24:27], a[168:171], v[92:95], v[24:27]\n\t"
        "buffer_load_dwordx4 v141, s[24:27], s36 offen offset:1024 lds\n\t"
        "v_mul_f32 v78, v78, v75\n\t"
        "v_mfma_f32_16x16x32_bf16 v[28:31], a[172:175], v[92:95], v[28:31]\n\t"
        "ds_read_b64_tr_b16 a[212:213], v133 offset:0\n\t"
        "v_mul_f32 v79, v74, v79\n\t"
        "v_mfma_f32_16x16x32_bf16 v[32:35], a[176:179], v[92:95], v[32:35]\n\t"
        "ds_read_b64_tr_b16 a[214:215], v133 offset:768\n\t"
        "v_mul_f32 v81, v74, v75\n\t"
        "v_mfma_f32_16x16x32_bf16 v[36:39], a[144:147], v[92:95], v[36:39]\n\t"
        "ds_read_b128 v[108:111], v129 offset:8320\n\t"
        "v_cndmask_b32 v78, v79, v78, s[44:45]\n\t"
        "v_mfma_f32_16x16x32_bf16 v[40:43], a[148:151], v[92:95], v[40:43]\n\t"
        "buffer_load_dwordx4 v142, s[24:27], s36 offen offset:2048 lds\n\t"
        "s_add_u32 m0, s37, 49152\n\t"
        "v_mfma_f32_16x16x32_bf16 v[44:47], a[152:155], v[92:95], v[44:47]\n\t"
        "ds_read_b64_tr_b16 a[216:217], v131 offset:6144\n\t"
        "s_add_u32 s36, s36, s38\n\t"
        "v_mfma_f32_16x16x32_bf16 v[36:39], a[168:171], v[96:99], v[36:39]\n\t"
        "s_waitcnt vmcnt(10)\n\t"
        "s_barrier\n\t"
        "v_mfma_f32_16x16x32_bf16 v[40:43], a[172:175], v[96:99], v[40:43]\n\t"
        "ds_read_b128 v[112:115], v129 offset:12416\n\t"
        "v_cndmask_b32 v79, 0, v79, s[44:45]\n\t"
        "v_mfma_f32_16x16x32_bf16 v[44:47], a[176:179], v[96:99], v[44:47]\n\t"
        "buffer_load_dwordx4 v140, s[24:27], s36 offen offset:0 lds\n\t"
        "v_cvt_pk_bf16_f32 v70, v80, v78\n\t"
        "s_waitcnt lgkmcnt(6)\n\t"
        "v_mfma_f32_16x16x32_bf16 v[0:3], a[192:195], v[100:103], v[0:3]\n\t"
        "ds_read_b64_tr_b16 a[218:219], v131 offset:6912\n\t"
        "v_cvt_pk_bf16_f32 v71, v79, v81\n\t"
        "v_mfma_f32_16x16x32_bf16 v[4:7], a[196:199], v[100:103], v[4:7]\n\t"
        "ds_read_b64_tr_b16 a[220:221], v132 offset:6144\n\t"
        "v_pk_max_u16 v78, v62, v64\n\t"
        "v_mfma_f32_16x16x32_bf16 v[8:11], a[200:203], v[100:103], v[8:11]\n\t"
        "ds_read_b64_tr_b16 a[222:223], v132 offset:6912\n\t"
        "v_pk_min_u16 v78, v78, v65\n\t"
        "v_mfma_f32_16x16x32_bf16 v[0:3], a[180:183], v[104:107], v[0:3]\n\t"
        "ds_read_b64_tr_b16 a[224:225], v133 offset:6144\n\t"
        "v_cmp_ne_u32 vcc, v78, v62\n\t"
        "v_mfma_f32_16x16x32_bf16 v[4:7], a[184:187], v[104:107], v[4:7]\n\t"
        "ds_read_b64_tr_b16 a[226:227], v133 offset:6912\n\t"
        "v_sub_u32 v78, v77, v67\n\t"
        "v_mfma_f32_16x16x32_bf16 v[8:11], a[188:191], v[104:107], v[8:11]\n\t"
        "ds_read_b64_tr_b16 a[228:229], v131 offset:12288\n\t"
        "v_mad_u32_u24 v79, v77, 6, v66\n\t"
        "v_mfma_f32_16x16x32_bf16 v[12:15], a[192:195], v[104:107], v[12:15]\n\t"
        "ds_read_b128 v[84:87], v129 offset:16512\n\t"
        "v_mad_u32_u24 v79, v76, s62, v79\n\t"
        "v_mfma_f32_16x16x32_bf16 v[16:19], a[196:199], v[104:107], v[16:19]\n\t"
        "ds_read_b64_tr_b16 a[230:231], v131 offset:13056\n\t"
        "s_and_b64 vcc, vcc, s[66:67]\n\t"
        "v_mfma_f32_16x16x32_bf16 v[20:23], a[200:203], v[104:107], v[20:23]\n\t"
        "ds_read_b64_tr_b16 a[232:233], v132 offset:12288\n\t"
        "s_or_b64 s[56:57], s[56:57], vcc\n\t"
        "s_waitcnt lgkmcnt(9)\n\t"
        "v_mfma_f32_16x16x32_bf16 v[0:3], a[204:207], v[108:111], v[0:3]\n\t"
        "ds_read_b64_tr_b16 a[234:235], v132 offset:13056\n\t"
        "v_bfe_u32 v80, v78, 2, 1\n\t"
        "v_mfma_f32_16x16x32_bf16 v[4:7], a[208:211], v[108:111], v[4:7]\n\t"
        "ds_read_b64_tr_b16 a[236:237], v133 offset:12288\n\t"
        "v_or_b32 v80, v80, v79\n\t"
        "v_mfma_f32_16x16x32_bf16 v[8:11], a[212:215], v[108:111], v[8:11]\n\t"
        "ds_read_b64_tr_b16 a[238:239], v133 offset:13056\n\t"
        "v_add_u32 v78, 1, v78\n\t"
        "v_mfma_f32_16x16x32_bf16 v[12:15], a[180:183], v[108:111], v[12:15]\n\t"
        "ds_read_b128 v[88:91], v130 offset:0\n\t"
        "v_bfe_u32 v78, v78, 2, 1\n\t"
        "v_mfma_f32_16x16x32_bf16 v[16:19], a[184:187], v[108:111], v[16:19]\n\t"
        "ds_read_b64_tr_b16 a[240:241], v131 offset:18432\n\t"
        "v_add3_u32 v78, v79, v78, 6\n\t"
        "v_mfma_f32_16x16x32_bf16 v[20:23], a[188:191], v[108:111], v[20:23]\n\t"
        "ds_read_b64_tr_b16 a[242:243], v131 offset:19200\n\t"
        "v_cndmask_b32 v78, v80, v78, s[44:45]\n\t"
        "v_mfma_f32_16x16x32_bf16 v[24:27], a[192:195], v[108:111], v[24:27]\n\t"
        "ds_read_b128 v[92:95], v130 offset:4096\n\t"
        "v_cndmask_b32 v79, 0, v119, s[64:65]\n\t"
        "v_mfma_f32_16x16x32_bf16 v[28:31], a[196:199], v[108:111], v[28:31]\n\t"
        "ds_read_b64_tr_b16 a[244:245], v132 offset:18432\n\t"
        "v_add_u32 v81, v80, v79\n\t"
        "v_mfma_f32_16x16x32_bf16 v[32:35], a[200:203], v[108:111], v[32:35]\n\t"
        "ds_read_b64_tr_b16 a[246:247], v132 offset:19200\n\t"
        "v_add_u32 v82, v78, v79\n\t"
        "v_mfma_f32_16x16x32_bf16 v[12:15], a[204:207], v[112:115], v[12:15]\n\t"
        "buffer_load_dwordx4 v141, s[24:27], s36 offen offset:1024 lds\n\t"
        "v_cndmask_b32 v78, v81, v78, s[44:45]\n\t"
        "v_mfma_f32_16x16x32_bf16 v[16:19], a[208:211], v[112:115], v[16:19]\n\t"
        "ds_read_b64_tr_b16 a[248:249], v133 offset:18432\n\t"
        "v_lshl_or_b32 v68, v78, 16, v80\n\t"
        "v_mfma_f32_16x16x32_bf16 v[20:23], a[212:215], v[112:115], v[20:23]\n\t"
        "ds_read_b64_tr_b16 a[250:251], v133 offset:19200\n\t"
        "v_lshl_or_b32 v69, v82, 16, v81\n\t"
        "v_mfma_f32_16x16x32_bf16 v[24:27], a[180:183], v[112:115], v[24:27]\n\t"
        "ds_write_b128 v118, v[68:71]\n\t"
        "v_mfma_f32_16x16x32_bf16 v[28:31], a[184:187], v[112:115], v[28:31]\n\t"
        "s_cmp_gt_u32 s3, 32\n\t"
        "s_cselect_b32 s8, 2, 0\n\t"
        "v_mfma_f32_16x16x32_bf16 v[32:35], a[188:191], v[112:115], v[32:35]\n\t"
        "s_cmp_lg_u64 s[56:57], 0\n\t"
        "s_addc_u32 s8, s8, s6\n\t"
        "v_mfma_f32_16x16x32_bf16 v[36:39], a[192:195], v[112:115], v[36:39]\n\t"
        "s_cmp_gt_u32 s61, 64\n\t"
        "s_cselect_b32 s47, 4, 0\n\t"
        "v_mfma_f32_16x16x32_bf16 v[40:43], a[196:199], v[112:115], v[40:43]\n\t"
        "ds_read_b128 v[160:163], v185 offset:0\n\t"
        "s_or_b32 s8, s8, s47\n\t"
        "v_mfma_f32_16x16x32_bf16 v[44:47], a[200:203], v[112:115], v[44:47]\n\t"
        "buffer_load_dwordx4 v142, s[24:27], s36 offen offset:2048 lds\n\t"
        "v_mov_b32 v151, s8\n\t"
        "s_waitcnt lgkmcnt(4)\n\t"
        "v_mfma_f32_16x16x32_bf16 v[24:27], a[204:207], v[84:87], v[24:27]\n\t"
        "s_mov_b64 exec, 1\n\t"
        "ds_write_b32 v153, v151\n\t"
        "s_mov_b64 exec, -1\n\t"
        "v_mfma_f32_16x16x32_bf16 v[28:31], a[208:211], v[84:87], v[28:31]\n\t"
        "ds_read_b128 v[96:99], v130 offset:8192\n\t"
        "v_xor_b32 v154, 32, v186\n\t"
        "v_mfma_f32_16x16x32_bf16 v[32:35], a[212:215], v[84:87], v[32:35]\n\t"
        "s_waitcnt vmcnt(3) lgkmcnt(4)\n\t"
        "s_barrier\n\t"
        "v_mfma_f32_16x16x32_bf16 v[36:39], a[180:183], v[84:87], v[36:39]\n\t"
        "ds_read_b128 v[100:103], v130 offset:12288\n\t"
        "v_mfma_f32_16x16x32_bf16 v[40:43], a[184:187], v[84:87], v[40:43]\n\t"
        "ds_read_b64_tr_b16 a[252:253], v131 offset:24576\n\t"
        "v_mfma_f32_16x16x32_bf16 v[44:47], a[188:191], v[84:87], v[44:47]\n\t"
        "ds_read_b64_tr_b16 a[254:255], v131 offset:25344\n\t"
        "v_mfma_f32_16x16x32_bf16 v[0:3], a[228:231], v[88:91], v[0:3]\n\t"
        "ds_read_b64_tr_b16 v[188:189], v132 offset:24576\n\t"
        "v_mfma_f32_16x16x32_bf16 v[4:7], a[232:235], v[88:91], v[4:7]\n\t"
        "ds_read_b64_tr_b16 v[190:191], v132 offset:25344\n\t"
        "s_waitcnt lgkmcnt(7)\n\t"
        "v_lshlrev_b32_sdwa v157, 5, v160 dst_sel:DWORD dst_unused:UNUSED_PAD src0_sel:DWORD src1_sel:WORD_0\n\t"
        "v_mfma_f32_16x16x32_bf16 v[8:11], a[236:239], v[88:91], v[8:11]\n\t"
        "ds_read_b64_tr_b16 v[192:193], v133 offset:24576\n\t"
        "v_lshlrev_b32_sdwa v158, 5, v160 dst_sel:DWORD dst_unused:UNUSED_PAD src0_sel:DWORD src1_sel:WORD_1\n\t"
        "v_mfma_f32_16x16x32_bf16 v[0:3], a[216:219], v[92:95], v[0:3]\n\t"
        "ds_read_b64_tr_b16 v[194:195], v133 offset:25344\n\t"
        "v_or_b32 v157, v184, v157\n\t"
        "v_mfma_f32_16x16x32_bf16 v[4:7], a[220:223], v[92:95], v[4:7]\n\t"
        "ds_read_b64_tr_b16 v[60:61], v157 offset:0\n\t"
        "v_or_b32 v158, v184, v158\n\t"
        "v_mfma_f32_16x16x32_bf16 v[8:11], a[224:227], v[92:95], v[8:11]\n\t"
        "ds_read_b64_tr_b16 v[62:63], v158 offset:0\n\t"
        "v_xor_b32 v159, 32, v157\n\t"
        "v_mfma_f32_16x16x32_bf16 v[12:15], a[228:231], v[92:95], v[12:15]\n\t"
        "ds_read_b64_tr_b16 v[64:65], v159 offset:0\n\t"
        "v_xor_b32 v152, 32, v158\n\t"
        "v_mfma_f32_16x16x32_bf16 v[16:19], a[232:235], v[92:95], v[16:19]\n\t"
        "ds_read_b64_tr_b16 v[66:67], v152 offset:0\n\t"
        "v_perm_b32 v164, v162, v162, v168\n\t"
        "v_mfma_f32_16x16x32_bf16 v[20:23], a[236:239], v[92:95], v[20:23]\n\t"
        "ds_read_b64_tr_b16 v[68:69], v157 offset:64\n\t"
        "v_perm_b32 v165, v162, v162, v169\n\t"
        "s_waitcnt lgkmcnt(10)\n\t"
        "v_mfma_f32_16x16x32_bf16 v[0:3], a[240:243], v[96:99], v[0:3]\n\t"
        "ds_read_b64_tr_b16 v[70:71], v158 offset:64\n\t"
        "v_perm_b32 v166, v162, v162, v170\n\t"
        "v_mfma_f32_16x16x32_bf16 v[4:7], a[244:247], v[96:99], v[4:7]\n\t"
        "ds_read_b64_tr_b16 v[72:73], v159 offset:64\n\t"
        "v_perm_b32 v167, v162, v162, v171\n\t"
        "v_mfma_f32_16x16x32_bf16 v[8:11], a[248:251], v[96:99], v[8:11]\n\t"
        "ds_read_b128 v[104:107], v130 offset:16384\n\t"
        "ds_read_b64_tr_b16 v[196:197], v131 offset:30720\n\t"
        "v_mfma_f32_16x16x32_bf16 v[12:15], a[216:219], v[96:99], v[12:15]\n\t"
        "ds_read_b64_tr_b16 v[74:75], v152 offset:64\n\t"
        "v_mfma_f32_16x16x32_bf16 v[16:19], a[220:223], v[96:99], v[16:19]\n\t"
        "ds_read_b64_tr_b16 v[76:77], v157 offset:128\n\t"
        "v_lshlrev_b32_sdwa v157, 5, v161 dst_sel:DWORD dst_unused:UNUSED_PAD src0_sel:DWORD src1_sel:WORD_0\n\t"
        "v_mfma_f32_16x16x32_bf16 v[20:23], a[224:227], v[96:99], v[20:23]\n\t"
        "ds_read_b64_tr_b16 v[78:79], v158 offset:128\n\t"
        "v_lshlrev_b32_sdwa v158, 5, v161 dst_sel:DWORD dst_unused:UNUSED_PAD src0_sel:DWORD src1_sel:WORD_1\n\t"
        "s_waitcnt lgkmcnt(10)\n\t"
        "v_mfma_f32_16x16x32_bf16 v[48:51], v[60:63], v[164:167], 0\n\t"
        "ds_read_b64_tr_b16 v[80:81], v159 offset:128\n\t"
        "v_or_b32 v157, v184, v157\n\t"
        "v_mfma_f32_16x16x32_bf16 v[24:27], a[228:231], v[96:99], v[24:27]\n\t"
        "ds_read_b64_tr_b16 v[82:83], v152 offset:128\n\t"
        "v_or_b32 v158, v184, v158\n\t"
        "s_waitcnt lgkmcnt(10)\n\t"
        "v_mfma_f32_16x16x32_bf16 v[52:55], v[64:67], v[164:167], 0\n\t"
        "ds_read_b64_tr_b16 v[60:61], v157 offset:0\n\t"
        "v_xor_b32 v159, 32, v157\n\t"
        "v_mfma_f32_16x16x32_bf16 v[28:31], a[232:235], v[96:99], v[28:31]\n\t"
        "ds_read_b64_tr_b16 v[62:63], v158 offset:0\n\t"
        "v_xor_b32 v152, 32, v158\n\t"
        "s_waitcnt lgkmcnt(10)\n\t"
        "v_mfma_f32_16x16x32_bf16 v[56:59], v[68:71], v[164:167], 0\n\t"
        "ds_read_b64_tr_b16 v[64:65], v159 offset:0\n\t"
        "v_mfma_f32_16x16x32_bf16 v[32:35], a[236:239], v[96:99], v[32:35]\n\t"
        "ds_read_b128 v[108:111], v129 offset:20608\n\t"
        "v_mfma_f32_16x16x32_bf16 v[12:15], a[240:243], v[100:103], v[12:15]\n\t"
        "ds_read_b64_tr_b16 v[66:67], v152 offset:0\n\t"
        "s_waitcnt lgkmcnt(9)\n\t"
        "v_mfma_f32_16x16x32_bf16 v[116:119], v[72:75], v[164:167], 0\n\t"
        "ds_read_b64_tr_b16 v[68:69], v157 offset:64\n\t"
        "v_mfma_f32_16x16x32_bf16 v[16:19], a[244:247], v[100:103], v[16:19]\n\t"
        "ds_read_b64_tr_b16 v[70:71], v158 offset:64\n\t"
        "s_waitcnt lgkmcnt(9)\n\t"
        "v_mfma_f32_16x16x32_bf16 v[120:123], v[76:79], v[164:167], 0\n\t"
        "s_waitcnt vmcnt(0)\n\t"
        "s_barrier\n\t"
        "v_mfma_f32_16x16x32_bf16 v[20:23], a[248:251], v[100:103], v[20:23]\n\t"
        "ds_read_b128 v[112:115], v130 offset:20480\n\t"
        "s_waitcnt lgkmcnt(8)\n\t"
        "v_mfma_f32_16x16x32_bf16 v[124:127], v[80:83], v[164:167], 0\n\t"
        "ds_read_b128 v[134:137], v182 offset:0\n\t"
        "v_perm_b32 v164, v163, v163, v168\n\t"
        "v_mfma_f32_16x16x32_bf16 v[24:27], a[216:219], v[100:103], v[24:27]\n\t"
        "ds_read_b64_tr_b16 v[72:73], v159 offset:64\n\t"
        "ds_read_b64_tr_b16 v[198:199], v131 offset:31488\n\t"
        "v_perm_b32 v165, v163, v163, v169\n\t"
        "v_mfma_f32_16x16x32_bf16 v[28:31], a[220:223], v[100:103], v[28:31]\n\t"
        "ds_read_b64_tr_b16 v[74:75], v152 offset:64\n\t"
        "ds_read_b64_tr_b16 v[200:201], v132 offset:30720\n\t"
        "v_perm_b32 v166, v163, v163, v170\n\t"
        "v_mfma_f32_16x16x32_bf16 v[32:35], a[224:227], v[100:103], v[32:35]\n\t"
        "ds_read_b64_tr_b16 v[202:203], v132 offset:31488\n\t"
        "ds_read_b64_tr_b16 v[204:205], v133 offset:30720\n\t"
        "v_perm_b32 v167, v163, v163, v171\n\t"
        "v_mfma_f32_16x16x32_bf16 v[36:39], a[228:231], v[100:103], v[36:39]\n\t"
        "ds_read_b64_tr_b16 v[206:207], v133 offset:31488\n\t"
        "s_waitcnt lgkmcnt(10)\n\t"
        "v_mfma_f32_16x16x32_bf16 v[48:51], v[60:63], v[164:167], v[48:51]\n\t"
        "ds_read_b128 v[84:87], v130 offset:64\n\t"
        "v_mfma_f32_16x16x32_bf16 v[40:43], a[232:235], v[100:103], v[40:43]\n\t"
        "ds_read_b64_tr_b16 v[76:77], v157 offset:128\n\t"
        "v_mfma_f32_16x16x32_bf16 v[52:55], v[64:67], v[164:167], v[52:55]\n\t"
        "ds_read_b64_tr_b16 v[78:79], v158 offset:128\n\t"
        "v_mfma_f32_16x16x32_bf16 v[44:47], a[236:239], v[100:103], v[44:47]\n\t"
        "ds_read_b128 v[88:91], v130 offset:4160\n\t"
        "s_waitcnt lgkmcnt(11)\n\t"
        "v_mfma_f32_16x16x32_bf16 v[56:59], v[68:71], v[164:167], v[56:59]\n\t"
        "ds_read_b64_tr_b16 v[80:81], v159 offset:128\n\t"
        "v_or3_b32 v134, v134, v135, v136\n\t"
        "v_mfma_f32_16x16x32_bf16 v[24:27], a[240:243], v[104:107], v[24:27]\n\t"
        "ds_read_b64_tr_b16 v[82:83], v152 offset:128\n\t"
        "v_or_b32 v134, v134, v137\n\t"
        "v_mfma_f32_16x16x32_bf16 v[28:31], a[244:247], v[104:107], v[28:31]\n\t"
        "ds_read_b128 v[160:163], v185 offset:256\n\t"
        "v_readfirstlane_b32 s6, v134\n\t"
        "s_waitcnt lgkmcnt(10)\n\t"
        "v_mfma_f32_16x16x32_bf16 v[116:119], v[72:75], v[164:167], v[116:119]\n\t"
        "ds_read_b64_tr_b16 v[208:209], v131 offset:36864\n\t"
        "v_cvt_pk_bf16_f32 v48, v48, v49\n\t"
        "v_mfma_f32_16x16x32_bf16 v[32:35], a[248:251], v[104:107], v[32:35]\n\t"
        "ds_read_b64_tr_b16 v[210:211], v131 offset:37632\n\t"
        "v_cvt_pk_bf16_f32 v49, v50, v51\n\t"
        "v_mfma_f32_16x16x32_bf16 v[36:39], a[216:219], v[104:107], v[36:39]\n\t"
        "ds_write_b64 v186, v[48:49] offset:0\n\t"
        "v_mfma_f32_16x16x32_bf16 v[40:43], a[220:223], v[104:107], v[40:43]\n\t"
        "ds_read_b64_tr_b16 v[212:213], v132 offset:36864\n\t"
        "v_cvt_pk_bf16_f32 v52, v52, v53\n\t"
        "v_mfma_f32_16x16x32_bf16 v[44:47], a[224:227], v[104:107], v[44:47]\n\t"
        "ds_read_b64_tr_b16 v[214:215], v132 offset:37632\n\t"
        "v_cvt_pk_bf16_f32 v53, v54, v55\n\t"
        "s_waitcnt lgkmcnt(9)\n\t"
        "v_mfma_f32_16x16x32_bf16 v[120:123], v[76:79], v[164:167], v[120:123]\n\t"
        "ds_write_b64 v154, v[52:53] offset:0\n\t"
        "v_mfma_f32_16x16x32_bf16 v[36:39], a[204:207], v[108:111], v[36:39]\n\t"
        "ds_read_b64_tr_b16 v[216:217], v133 offset:36864\n\t"
        "ds_read_b64_tr_b16 v[218:219], v133 offset:37632\n\t"
        "v_cvt_pk_bf16_f32 v56, v56, v57\n\t"
        "v_mfma_f32_16x16x32_bf16 v[40:43], a[208:211], v[108:111], v[40:43]\n\t"
        "ds_read_b128 v[92:95], v130 offset:8256\n\t"
        "v_cvt_pk_bf16_f32 v57, v58, v59\n\t"
        "s_waitcnt lgkmcnt(10)\n\t"
        "v_mfma_f32_16x16x32_bf16 v[124:127], v[80:83], v[164:167], v[124:127]\n\t"
        "ds_write_b64 v186, v[56:57] offset:64\n\t"
        "v_mfma_f32_16x16x32_bf16 v[44:47], a[212:215], v[108:111], v[44:47]\n\t"
        "ds_read_b64_tr_b16 v[220:221], v131 offset:43008\n\t"
        "s_waitcnt lgkmcnt(10)\n\t"
        "v_lshlrev_b32_sdwa v157, 5, v160 dst_sel:DWORD dst_unused:UNUSED_PAD src0_sel:DWORD src1_sel:WORD_0\n\t"
        "v_mfma_f32_16x16x32_bf16 v[36:39], a[240:243], v[112:115], v[36:39]\n\t"
        "ds_read_b128 v[96:99], v130 offset:12352\n\t"
        "v_lshlrev_b32_sdwa v158, 5, v160 dst_sel:DWORD dst_unused:UNUSED_PAD src0_sel:DWORD src1_sel:WORD_1\n\t"
        "v_mfma_f32_16x16x32_bf16 v[40:43], a[244:247], v[112:115], v[40:43]\n\t"
        "ds_read_b64_tr_b16 v[222:223], v131 offset:43776\n\t"
        "v_or_b32 v157, v184, v157\n\t"
        "v_mfma_f32_16x16x32_bf16 v[44:47], a[248:251], v[112:115], v[44:47]\n\t"
        "ds_read_b64_tr_b16 v[60:61], v157 offset:0\n\t"
        "v_or_b32 v158, v184, v158\n\t"
        "v_mfma_f32_16x16x32_bf16 v[0:3], v[196:199], v[84:87], v[0:3]\n\t"
        "ds_read_b64_tr_b16 v[62:63], v158 offset:0\n\t"
        "v_xor_b32 v159, 32, v157\n\t"
        "v_mfma_f32_16x16x32_bf16 v[4:7], v[200:203], v[84:87], v[4:7]\n\t"
        "ds_read_b64_tr_b16 v[64:65], v159 offset:0\n\t"
        "v_xor_b32 v152, 32, v158\n\t"
        "v_mfma_f32_16x16x32_bf16 v[8:11], v[204:207], v[84:87], v[8:11]\n\t"
        "ds_read_b64_tr_b16 v[66:67], v152 offset:0\n\t"
        "v_cvt_pk_bf16_f32 v116, v116, v117\n\t"
        "v_mfma_f32_16x16x32_bf16 v[0:3], a[252:255], v[88:91], v[0:3]\n\t"
        "ds_read_b64_tr_b16 v[68:69], v157 offset:64\n\t"
        "v_cvt_pk_bf16_f32 v117, v118, v119\n\t"
        "v_mfma_f32_16x16x32_bf16 v[4:7], v[188:191], v[88:91], v[4:7]\n\t"
        "ds_write_b64 v154, v[116:117] offset:64\n\t"
        "v_mfma_f32_16x16x32_bf16 v[8:11], v[192:195], v[88:91], v[8:11]\n\t"
        "ds_read_b64_tr_b16 v[70:71], v158 offset:64\n\t"
        "v_cvt_pk_bf16_f32 v120, v120, v121\n\t"
        "v_mfma_f32_16x16x32_bf16 v[12:15], v[196:199], v[88:91], v[12:15]\n\t"
        "ds_read_b64_tr_b16 v[72:73], v159 offset:64\n\t"
        "ds_read_b128 v[100:103], v130 offset:16448\n\t"
        "v_cvt_pk_bf16_f32 v121, v122, v123\n\t"
        "v_mfma_f32_16x16x32_bf16 v[16:19], v[200:203], v[88:91], v[16:19]\n\t"
        "ds_write_b64 v186, v[120:121] offset:128\n\t"
        "ds_read_b64_tr_b16 v[224:225], v132 offset:43008\n\t"
        "v_mfma_f32_16x16x32_bf16 v[20:23], v[204:207], v[88:91], v[20:23]\n\t"
        "ds_read_b64_tr_b16 v[74:75], v152 offset:64\n\t"
        "ds_read_b64_tr_b16 v[226:227], v132 offset:43776\n\t"
        "v_cvt_pk_bf16_f32 v124, v124, v125\n\t"
        "s_waitcnt lgkmcnt(11)\n\t"
        "v_mfma_f32_16x16x32_bf16 v[0:3], v[208:211], v[92:95], v[0:3]\n\t"
        "ds_read_b64_tr_b16 v[76:77], v157 offset:128\n\t"
        "ds_read_b64_tr_b16 v[228:229], v133 offset:43008\n\t"
        "v_lshlrev_b32_sdwa v157, 5, v161 dst_sel:DWORD dst_unused:UNUSED_PAD src0_sel:DWORD src1_sel:WORD_0\n\t"
        "v_mfma_f32_16x16x32_bf16 v[4:7], v[212:215], v[92:95], v[4:7]\n\t"
        "ds_read_b64_tr_b16 v[78:79], v158 offset:128\n\t"
        "ds_read_b64_tr_b16 v[230:231], v133 offset:43776\n\t"
        "v_lshlrev_b32_sdwa v158, 5, v161 dst_sel:DWORD dst_unused:UNUSED_PAD src0_sel:DWORD src1_sel:WORD_1\n\t"
        "v_mfma_f32_16x16x32_bf16 v[8:11], v[216:219], v[92:95], v[8:11]\n\t"
        "ds_read_b64_tr_b16 v[80:81], v159 offset:128\n\t"
        "v_or_b32 v157, v184, v157\n\t"
        "v_mfma_f32_16x16x32_bf16 v[12:15], a[252:255], v[92:95], v[12:15]\n\t"
        "ds_read_b64_tr_b16 v[82:83], v152 offset:128\n\t"
        "v_or_b32 v158, v184, v158\n\t"
        "v_mfma_f32_16x16x32_bf16 v[16:19], v[188:191], v[92:95], v[16:19]\n\t"
        "ds_read_b128 v[104:107], v130 offset:20544\n\t"
        "v_xor_b32 v159, 32, v157\n\t"
        "v_mfma_f32_16x16x32_bf16 v[20:23], v[192:195], v[92:95], v[20:23]\n\t"
        "ds_read_b64_tr_b16 v[232:233], v131 offset:49152\n\t"
        "v_xor_b32 v152, 32, v158\n\t"
        "v_mfma_f32_16x16x32_bf16 v[24:27], v[196:199], v[92:95], v[24:27]\n\t"
        "ds_read_b64_tr_b16 v[234:235], v131 offset:49920\n\t"
        "v_cvt_pk_bf16_f32 v125, v126, v127\n\t"
        "v_mfma_f32_16x16x32_bf16 v[28:31], v[200:203], v[92:95], v[28:31]\n\t"
        "ds_write_b64 v154, v[124:125] offset:128\n\t"
        "v_mfma_f32_16x16x32_bf16 v[32:35], v[204:207], v[92:95], v[32:35]\n\t"
        "ds_read_b64_tr_b16 v[236:237], v132 offset:49152\n\t"
        "v_perm_b32 v164, v162, v162, v168\n\t"
        "v_mfma_f32_16x16x32_bf16 v[12:15], v[208:211], v[96:99], v[12:15]\n\t"
        "ds_read_b64_tr_b16 v[238:239], v132 offset:49920\n\t"
        "ds_read_b64_tr_b16 v[240:241], v133 offset:49152\n\t"
        "v_perm_b32 v165, v162, v162, v169\n\t"
        "v_mfma_f32_16x16x32_bf16 v[16:19], v[212:215], v[96:99], v[16:19]\n\t"
        "ds_read_b64_tr_b16 v[242:243], v133 offset:49920\n\t"
        "v_perm_b32 v166, v162, v162, v170\n\t"
        "v_mfma_f32_16x16x32_bf16 v[20:23], v[216:219], v[96:99], v[20:23]\n\t"
        ".LpX1%=:\n\t"
        "s_cbranch_execz .LrX%=\n\t"
        "ds_read_b128 v[108:111], v130 offset:128\n\t"
        "v_perm_b32 v167, v162, v162, v171\n\t"
        "v_mfma_f32_16x16x32_bf16 v[24:27], a[252:255], v[96:99], v[24:27]\n\t"
        "ds_read_b128 v[112:115], v130 offset:4224\n\t"
        "v_mfma_f32_16x16x32_bf16 v[48:51], v[60:63], v[164:167], 0\n\t"
        "ds_read_b64_tr_b16 v[60:61], v157 offset:0\n\t"
        "v_mfma_f32_16x16x32_bf16 v[28:31], v[188:191], v[96:99], v[28:31]\n\t"
        "ds_read_b64_tr_b16 v[62:63], v158 offset:0\n\t"
        "s_waitcnt lgkmcnt(10)\n\t"
        "v_mfma_f32_16x16x32_bf16 v[52:55], v[64:67], v[164:167], 0\n\t"
        "ds_read_b64_tr_b16 v[64:65], v159 offset:0\n\t"
        "v_mfma_f32_16x16x32_bf16 v[32:35], v[192:195], v[96:99], v[32:35]\n\t"
        "ds_read_b64_tr_b16 v[66:67], v152 offset:0\n\t"
        "v_mfma_f32_16x16x32_bf16 v[56:59], v[68:71], v[164:167], 0\n\t"
        "ds_read_b64_tr_b16 v[68:69], v157 offset:64\n\t"
        "v_mfma_f32_16x16x32_bf16 v[36:39], v[196:199], v[96:99], v[36:39]\n\t"
        "ds_read_b64_tr_b16 v[70:71], v158 offset:64\n\t"
        "v_mfma_f32_16x16x32_bf16 v[116:119], v[72:75], v[164:167], 0\n\t"
        "ds_read_b64_tr_b16 v[72:73], v159 offset:64\n\t"
        "v_mfma_f32_16x16x32_bf16 v[40:43], v[200:203], v[96:99], v[40:43]\n\t"
        "ds_read_b64_tr_b16 v[74:75], v152 offset:64\n\t"
        "v_mfma_f32_16x16x32_bf16 v[120:123], v[76:79], v[164:167], 0\n\t"
        "ds_read_b64_tr_b16 v[76:77], v157 offset:128\n\t"
        "v_mfma_f32_16x16x32_bf16 v[44:47], v[204:207], v[96:99], v[44:47]\n\t"
        "ds_read_b64_tr_b16 v[78:79], v158 offset:128\n\t"
        "v_mfma_f32_16x16x32_bf16 v[124:127], v[80:83], v[164:167], 0\n\t"
        "ds_read_b64_tr_b16 v[80:81], v159 offset:128\n\t"
        "v_perm_b32 v164, v163, v163, v168\n\t"
        "v_mfma_f32_16x16x32_bf16 v[24:27], v[208:211], v[100:103], v[24:27]\n\t"
        "ds_read_b64_tr_b16 v[82:83], v152 offset:128\n\t"
        "v_perm_b32 v165, v163, v163, v169\n\t"
        "v_mfma_f32_16x16x32_bf16 v[28:31], v[212:215], v[100:103], v[28:31]\n\t"
        "ds_read_b64_tr_b16 v[244:245], v131 offset:55296\n\t"
        "v_perm_b32 v166, v163, v163, v170\n\t"
        "v_mfma_f32_16x16x32_bf16 v[32:35], v[216:219], v[100:103], v[32:35]\n\t"
        "ds_read_b64_tr_b16 v[246:247], v131 offset:56064\n\t"
        "v_perm_b32 v167, v163, v163, v171\n\t"
        "v_mfma_f32_16x16x32_bf16 v[36:39], a[252:255], v[100:103], v[36:39]\n\t"
        "ds_read_b64_tr_b16 v[248:249], v132 offset:55296\n\t"
        "ds_read_b64_tr_b16 v[250:251], v132 offset:56064\n\t"
        "s_waitcnt lgkmcnt(10)\n\t"
        "v_mfma_f32_16x16x32_bf16 v[48:51], v[60:63], v[164:167], v[48:51]\n\t"
        "ds_read_b64_tr_b16 v[252:253], v133 offset:55296\n\t"
        "ds_read_b64_tr_b16 v[254:255], v133 offset:56064\n\t"
        "v_mfma_f32_16x16x32_bf16 v[40:43], v[188:191], v[100:103], v[40:43]\n\t"
        "ds_read_b128 v[84:87], v130 offset:8320\n\t"
        "v_mfma_f32_16x16x32_bf16 v[52:55], v[64:67], v[164:167], v[52:55]\n\t"
        "ds_read_b128 v[132:135], v187 offset:0\n\t"
        "v_mfma_f32_16x16x32_bf16 v[44:47], v[192:195], v[100:103], v[44:47]\n\t"
        "ds_read_b128 v[136:139], v187 offset:16\n\t"
        "v_mfma_f32_16x16x32_bf16 v[56:59], v[68:71], v[164:167], v[56:59]\n\t"
        "ds_read_b32 v140, v187 offset:32\n\t"
        "v_cvt_pk_bf16_f32 v48, v48, v49\n\t"
        "v_cvt_pk_bf16_f32 v49, v50, v51\n\t"
        "v_mfma_f32_16x16x32_bf16 v[36:39], v[208:211], v[104:107], v[36:39]\n\t"
        "ds_write_b64 v186, v[48:49] offset:12288\n\t"
        "s_waitcnt lgkmcnt(11)\n\t"
        "v_mfma_f32_16x16x32_bf16 v[116:119], v[72:75], v[164:167], v[116:119]\n\t"
        "ds_read_b128 v[88:91], v130 offset:12416\n\t"
        "v_cvt_pk_bf16_f32 v52, v52, v53\n\t"
        "v_mfma_f32_16x16x32_bf16 v[40:43], v[212:215], v[104:107], v[40:43]\n\t"
        "v_cvt_pk_bf16_f32 v53, v54, v55\n\t"
        "v_mfma_f32_16x16x32_bf16 v[120:123], v[76:79], v[164:167], v[120:123]\n\t"
        "ds_write_b64 v154, v[52:53] offset:12288\n\t"
        "v_mfma_f32_16x16x32_bf16 v[44:47], v[216:219], v[104:107], v[44:47]\n\t"
        "v_cvt_pk_bf16_f32 v56, v56, v57\n\t"
        "v_cvt_pk_bf16_f32 v57, v58, v59\n\t"
        "v_mfma_f32_16x16x32_bf16 v[124:127], v[80:83], v[164:167], v[124:127]\n\t"
        "ds_write_b64 v186, v[56:57] offset:12352\n\t"
        "v_mfma_f32_16x16x32_bf16 v[0:3], v[232:235], v[108:111], v[0:3]\n\t"
        "v_cvt_pk_bf16_f32 v116, v116, v117\n\t"
        "v_cvt_pk_bf16_f32 v117, v118, v119\n\t"
        "v_mfma_f32_16x16x32_bf16 v[4:7], v[236:239], v[108:111], v[4:7]\n\t"
        "ds_write_b64 v154, v[116:117] offset:12352\n\t"
        "v_mfma_f32_16x16x32_bf16 v[8:11], v[240:243], v[108:111], v[8:11]\n\t"
        "v_cvt_pk_bf16_f32 v120, v120, v121\n\t"
        "v_cvt_pk_bf16_f32 v121, v122, v123\n\t"
        "v_mfma_f32_16x16x32_bf16 v[0:3], v[220:223], v[112:115], v[0:3]\n\t"
        "ds_write_b64 v186, v[120:121] offset:12416\n\t"
        "v_mfma_f32_16x16x32_bf16 v[4:7], v[224:227], v[112:115], v[4:7]\n\t"
        "v_cvt_pk_bf16_f32 v124, v124, v125\n\t"
        "v_cvt_pk_bf16_f32 v125, v126, v127\n\t"
        "v_mfma_f32_16x16x32_bf16 v[8:11], v[228:231], v[112:115], v[8:11]\n\t"
        "ds_write_b64 v154, v[124:125] offset:12416\n\t"
        "v_mfma_f32_16x16x32_bf16 v[12:15], v[232:235], v[112:115], v[12:15]\n\t"
        "ds_read_b128 v[92:95], v130 offset:16512\n\t"
        "v_mfma_f32_16x16x32_bf16 v[16:19], v[236:239], v[112:115], v[16:19]\n\t"
        "v_mfma_f32_16x16x32_bf16 v[20:23], v[240:243], v[112:115], v[20:23]\n\t"
        "s_waitcnt lgkmcnt(6)\n\t"
        "v_mfma_f32_16x16x32_bf16 v[0:3], v[244:247], v[84:87], v[0:3]\n\t"
        "v_mfma_f32_16x16x32_bf16 v[4:7], v[248:251], v[84:87], v[4:7]\n\t"
        "v_mfma_f32_16x16x32_bf16 v[8:11], v[252:255], v[84:87], v[8:11]\n\t"
        "s_waitcnt lgkmcnt(1)\n\t"
        "s_barrier\n\t"
        "v_mfma_f32_16x16x32_bf16 v[12:15], v[220:223], v[84:87], v[12:15]\n\t"
        "s_bitcmp1_b32 s6, 1\n\t"
        "s_cbranch_scc1 .Lx68%=\n\t"
        ".Lx69%=:\n\t"
        "v_mfma_f32_16x16x32_bf16 v[16:19], v[224:227], v[84:87], v[16:19]\n\t"
        "ds_read_b128 v[160:163], v185 offset:512\n\t"
        "v_xor_b32 v132, v181, v132\n\t"
        "v_mfma_f32_16x16x32_bf16 v[20:23], v[228:231], v[84:87], v[20:23]\n\t"
        "ds_read_b128 v[96:99], v130 offset:20608\n\t"
        "v_add_u32 v151, s9, v172\n\t"
        "v_max_u32 v151, v151, v132\n\t"
        "v_mfma_f32_16x16x32_bf16 v[24:27], v[232:235], v[84:87], v[24:27]\n\t"
        "ds_read_b128 v[100:103], v151 offset:0\n\t"
        "v_xor_b32 v133, v181, v133\n\t"
        "v_mfma_f32_16x16x32_bf16 v[28:31], v[236:239], v[84:87], v[28:31]\n\t"
        "v_add_u32 v183, s9, v173\n\t"
        "v_max_u32 v183, v183, v133\n\t"
        "v_xor_b32 v134, v181, v134\n\t"
        "v_mfma_f32_16x16x32_bf16 v[32:35], v[240:243], v[84:87], v[32:35]\n\t"
        "s_add_u32 s55, s41, 0\n\t"
        "v_cvt_pk_bf16_f32 v116, v0, v1\n\t"
        "v_mfma_f32_16x16x32_bf16 v[12:15], v[244:247], v[88:91], v[12:15]\n\t"
        "ds_read_b128 v[104:107], v151 offset:64\n\t"
        "v_cvt_pk_bf16_f32 v117, v2, v3\n\t"
        "v_cvt_pk_bf16_f32 v118, v4, v5\n\t"
        "v_mfma_f32_16x16x32_bf16 v[16:19], v[248:251], v[88:91], v[16:19]\n\t"
        "v_cvt_pk_bf16_f32 v119, v6, v7\n\t"
        "v_cvt_pk_bf16_f32 v120, v8, v9\n\t"
        "v_cvt_pk_bf16_f32 v121, v10, v11\n\t"
        "v_mfma_f32_16x16x32_bf16 v[20:23], v[252:255], v[88:91], v[20:23]\n\t"
        "v_permlane16_swap_b32 v116, v118\n\t"
        "v_mfma_f32_16x16x32_bf16 v[24:27], v[220:223], v[88:91], v[24:27]\n\t"
        "v_permlane16_swap_b32 v117, v119\n\t"
        "v_mfma_f32_16x16x32_bf16 v[28:31], v[224:227], v[88:91], v[28:31]\n\t"
        "buffer_store_dwordx4 v[116:119], v144, s[28:31], s55 offen sc1\n\t"
        "v_mfma_f32_16x16x32_bf16 v[32:35], v[228:231], v[88:91], v[32:35]\n\t"
        "ds_write_b64 v146, v[120:121] offset:0\n\t"
        "s_waitcnt lgkmcnt(2)\n\t"
        "v_lshlrev_b32_sdwa v157, 5, v160 dst_sel:DWORD dst_unused:UNUSED_PAD src0_sel:DWORD src1_sel:WORD_0\n\t"
        "v_mfma_f32_16x16x32_bf16 v[36:39], v[232:235], v[88:91], v[36:39]\n\t"
        "ds_read_b128 v[108:111], v151 offset:128\n\t"
        "v_add_u32 v151, s9, v174\n\t"
        "v_max_u32 v151, v151, v134\n\t"
        "v_mfma_f32_16x16x32_bf16 v[40:43], v[236:239], v[88:91], v[40:43]\n\t"
        "ds_read_b128 v[112:115], v183 offset:0\n\t"
        "v_xor_b32 v135, v181, v135\n\t"
        "v_mfma_f32_16x16x32_bf16 v[44:47], v[240:243], v[88:91], v[44:47]\n\t"
        "s_add_u32 s55, s41, 43008\n\t"
        "v_cvt_pk_bf16_f32 v116, v12, v13\n\t"
        "v_cvt_pk_bf16_f32 v117, v14, v15\n\t"
        "v_mfma_f32_16x16x32_bf16 v[24:27], v[244:247], v[92:95], v[24:27]\n\t"
        "ds_read_b128 v[84:87], v183 offset:64\n\t"
        "v_cvt_pk_bf16_f32 v118, v16, v17\n\t"
        "v_mfma_f32_16x16x32_bf16 v[28:31], v[248:251], v[92:95], v[28:31]\n\t"
        "v_cvt_pk_bf16_f32 v119, v18, v19\n\t"
        "v_cvt_pk_bf16_f32 v120, v20, v21\n\t"
        "v_cvt_pk_bf16_f32 v121, v22, v23\n\t"
        "v_mfma_f32_16x16x32_bf16 v[32:35], v[252:255], v[92:95], v[32:35]\n\t"
        "v_permlane16_swap_b32 v116, v118\n\t"
        "v_lshlrev_b32_sdwa v158, 5, v160 dst_sel:DWORD dst_unused:UNUSED_PAD src0_sel:DWORD src1_sel:WORD_1\n\t"
        "v_mfma_f32_16x16x32_bf16 v[36:39], v[220:223], v[92:95], v[36:39]\n\t"
        "v_permlane16_swap_b32 v117, v119\n\t"
        "v_mfma_f32_16x16x32_bf16 v[40:43], v[224:227], v[92:95], v[40:43]\n\t"
        "ds_read_b128 v[88:91], v183 offset:128\n\t"
        "v_add_u32 v183, s9, v175\n\t"
        "v_max_u32 v183, v183, v135\n\t"
        "v_mfma_f32_16x16x32_bf16 v[44:47], v[228:231], v[92:95], v[44:47]\n\t"
        "ds_read_b128 v[132:135], v187 offset:4096\n\t"
        "v_xor_b32 v136, v181, v136\n\t"
        "v_mfma_f32_16x16x32_bf16 v[36:39], v[244:247], v[96:99], v[36:39]\n\t"
        "ds_read_b128 v[92:95], v151 offset:0\n\t"
        "v_or_b32 v157, v184, v157\n\t"
        "v_or_b32 v158, v184, v158\n\t"
        "v_mfma_f32_16x16x32_bf16 v[40:43], v[248:251], v[96:99], v[40:43]\n\t"
        "ds_read_b64_tr_b16 v[60:61], v157 offset:0\n\t"
        "ds_read_b64_tr_b16 v[62:63], v158 offset:0\n\t"
        "v_xor_b32 v159, 32, v157\n\t"
        "v_mfma_f32_16x16x32_bf16 v[44:47], v[252:255], v[96:99], v[44:47]\n\t"
        "ds_read_b64_tr_b16 v[64:65], v159 offset:0\n\t"
        "v_xor_b32 v152, 32, v158\n\t"
        "v_mfma_f32_16x16x32_bf16 v[48:51], a[12:15], v[100:103], 0\n\t"
        "buffer_store_dwordx4 v[116:119], v144, s[28:31], s55 offen sc1\n\t"
        "v_mfma_f32_16x16x32_bf16 v[52:55], a[16:19], v[100:103], 0\n\t"
        "ds_write_b64 v146, v[120:121] offset:2048\n\t"
        "v_mfma_f32_16x16x32_bf16 v[56:59], a[20:23], v[100:103], 0\n\t"
        "ds_read_b64_tr_b16 v[66:67], v152 offset:0\n\t"
        "ds_read_b64_tr_b16 v[68:69], v157 offset:64\n\t"
        "v_perm_b32 v164, v162, v162, v168\n\t"
        "v_perm_b32 v165, v162, v162, v169\n\t"
        "s_waitcnt lgkmcnt(8)\n\t"
        "v_mfma_f32_16x16x32_bf16 v[48:51], a[48:51], v[104:107], v[48:51]\n\t"
        "ds_read_b64_tr_b16 v[70:71], v158 offset:64\n\t"
        "ds_read_b64_tr_b16 v[72:73], v159 offset:64\n\t"
        "v_perm_b32 v166, v162, v162, v170\n\t"
        "v_mfma_f32_16x16x32_bf16 v[52:55], a[52:55], v[104:107], v[52:55]\n\t"
        "ds_read_b64_tr_b16 v[74:75], v152 offset:64\n\t"
        "v_perm_b32 v167, v162, v162, v171\n\t"
        "v_mfma_f32_16x16x32_bf16 v[56:59], a[56:59], v[104:107], v[56:59]\n\t"
        "ds_read_b128 v[96:99], v151 offset:64\n\t"
        "ds_read_b64_tr_b16 v[76:77], v157 offset:128\n\t"
        "v_lshlrev_b32_sdwa v157, 5, v161 dst_sel:DWORD dst_unused:UNUSED_PAD src0_sel:DWORD src1_sel:WORD_0\n\t"
        "v_mfma_f32_16x16x32_bf16 v[48:51], a[84:87], v[108:111], v[48:51]\n\t"
        "ds_read_b64_tr_b16 v[78:79], v158 offset:128\n\t"
        "v_lshlrev_b32_sdwa v158, 5, v161 dst_sel:DWORD dst_unused:UNUSED_PAD src0_sel:DWORD src1_sel:WORD_1\n\t"
        "v_mfma_f32_16x16x32_bf16 v[52:55], a[88:91], v[108:111], v[52:55]\n\t"
        "ds_read_b64_tr_b16 v[80:81], v159 offset:128\n\t"
        "ds_read_b64_tr_b16 v[82:83], v152 offset:128\n\t"
        "v_or_b32 v157, v184, v157\n\t"
        "s_waitcnt lgkmcnt(11)\n\t"
        "v_mfma_f32_16x16x32_bf16 v[0:3], v[60:63], v[164:167], 0\n\t"
        "ds_read_b64_tr_b16 v[60:61], v157 offset:0\n\t"
        "v_or_b32 v158, v184, v158\n\t"
        "v_xor_b32 v159, 32, v157\n\t"
        "v_mfma_f32_16x16x32_bf16 v[56:59], a[92:95], v[108:111], v[56:59]\n\t"
        "ds_read_b128 v[100:103], v151 offset:128\n\t"
        "v_add_u32 v151, s9, v176\n\t"
        "v_mfma_f32_16x16x32_bf16 v[48:51], a[120:123], v[112:115], v[48:51]\n\t"
        "ds_read_b64_tr_b16 v[62:63], v158 offset:0\n\t"
        "v_max_u32 v151, v151, v136\n\t"
        "v_xor_b32 v137, v181, v137\n\t"
        "v_mfma_f32_16x16x32_bf16 v[52:55], a[124:127], v[112:115], v[52:55]\n\t"
        "ds_read_b128 v[104:107], v183 offset:0\n\t"
        "v_xor_b32 v152, 32, v158\n\t"
        "s_waitcnt lgkmcnt(10)\n\t"
        "v_mfma_f32_16x16x32_bf16 v[4:7], v[64:67], v[164:167], 0\n\t"
        "ds_read_b64_tr_b16 v[64:65], v159 offset:0\n\t"
        "ds_read_b64_tr_b16 v[66:67], v152 offset:0\n\t"
        "s_add_u32 s55, s41, 86016\n\t"
        "v_mfma_f32_16x16x32_bf16 v[56:59], a[128:131], v[112:115], v[56:59]\n\t"
        "ds_read_b128 v[108:111], v183 offset:64\n\t"
        "v_cvt_pk_bf16_f32 v116, v24, v25\n\t"
        "v_cvt_pk_bf16_f32 v117, v26, v27\n\t"
        "v_mfma_f32_16x16x32_bf16 v[8:11], v[68:71], v[164:167], 0\n\t"
        "ds_read_b64_tr_b16 v[68:69], v157 offset:64\n\t"
        "v_cvt_pk_bf16_f32 v118, v28, v29\n\t"
        "v_mfma_f32_16x16x32_bf16 v[48:51], a[156:159], v[84:87], v[48:51]\n\t"
        "ds_read_b64_tr_b16 v[70:71], v158 offset:64\n\t"
        "v_cvt_pk_bf16_f32 v119, v30, v31\n\t"
        "v_cvt_pk_bf16_f32 v120, v32, v33\n\t"
        "s_waitcnt lgkmcnt(9)\n\t"
        "v_mfma_f32_16x16x32_bf16 v[12:15], v[72:75], v[164:167], 0\n\t"
        "ds_read_b64_tr_b16 v[72:73], v159 offset:64\n\t"
        "v_cvt_pk_bf16_f32 v121, v34, v35\n\t"
        "v_mfma_f32_16x16x32_bf16 v[52:55], a[160:163], v[84:87], v[52:55]\n\t"
        "v_permlane16_swap_b32 v116, v118\n\t"
        "v_mfma_f32_16x16x32_bf16 v[16:19], v[76:79], v[164:167], 0\n\t"
        "v_permlane16_swap_b32 v117, v119\n\t"
        "v_mfma_f32_16x16x32_bf16 v[56:59], a[164:167], v[84:87], v[56:59]\n\t"
        "buffer_store_dwordx4 v[116:119], v144, s[28:31], s55 offen sc1\n\t"
        "v_mfma_f32_16x16x32_bf16 v[20:23], v[80:83], v[164:167], 0\n\t"
        "ds_write_b64 v146, v[120:121] offset:4096\n\t"
        "v_perm_b32 v164, v163, v163, v168\n\t"
        "v_perm_b32 v165, v163, v163, v169\n\t"
        "v_mfma_f32_16x16x32_bf16 v[48:51], a[192:195], v[88:91], v[48:51]\n\t"
        "ds_read_b64_tr_b16 v[74:75], v152 offset:64\n\t"
        "v_perm_b32 v166, v163, v163, v170\n\t"
        "v_mfma_f32_16x16x32_bf16 v[52:55], a[196:199], v[88:91], v[52:55]\n\t"
        "ds_read_b64_tr_b16 v[76:77], v157 offset:128\n\t"
        "ds_read_b64_tr_b16 v[78:79], v158 offset:128\n\t"
        "v_perm_b32 v167, v163, v163, v171\n\t"
        "v_mfma_f32_16x16x32_bf16 v[56:59], a[200:203], v[88:91], v[56:59]\n\t"
        "ds_read_b128 v[112:115], v183 offset:128\n\t"
        "v_add_u32 v183, s9, v177\n\t"
        "s_waitcnt lgkmcnt(6)\n\t"
        "v_mfma_f32_16x16x32_bf16 v[0:3], v[60:63], v[164:167], v[0:3]\n\t"
        "ds_read_b64_tr_b16 v[80:81], v159 offset:128\n\t"
        "ds_read_b64_tr_b16 v[82:83], v152 offset:128\n\t"
        "v_max_u32 v183, v183, v137\n\t"
        "v_mfma_f32_16x16x32_bf16 v[48:51], a[228:231], v[92:95], v[48:51]\n\t"
        "ds_read_b128 v[160:163], v185 offset:768\n\t"
        "v_xor_b32 v138, v181, v138\n\t"
        "v_mfma_f32_16x16x32_bf16 v[4:7], v[64:67], v[164:167], v[4:7]\n\t"
        "ds_read_b128 v[84:87], v151 offset:0\n\t"
        "s_add_u32 s55, s41, 129024\n\t"
        "v_cvt_pk_bf16_f32 v116, v36, v37\n\t"
        "v_mfma_f32_16x16x32_bf16 v[52:55], a[232:235], v[92:95], v[52:55]\n\t"
        "ds_read_b128 v[88:91], v151 offset:64\n\t"
        "v_cvt_pk_bf16_f32 v117, v38, v39\n\t"
        "v_cvt_pk_bf16_f32 v118, v40, v41\n\t"
        "v_mfma_f32_16x16x32_bf16 v[8:11], v[68:71], v[164:167], v[8:11]\n\t"
        "v_cvt_pk_bf16_f32 v0, v0, v1\n\t"
        "v_cvt_pk_bf16_f32 v1, v2, v3\n\t"
        "v_mfma_f32_16x16x32_bf16 v[56:59], a[236:239], v[92:95], v[56:59]\n\t"
        "ds_write_b64 v186, v[0:1] offset:24576\n\t"
        "v_mfma_f32_16x16x32_bf16 v[48:51], v[196:199], v[96:99], v[48:51]\n\t"
        "ds_read_b128 v[92:95], v151 offset:128\n\t"
        "v_add_u32 v151, s9, v178\n\t"
        "v_max_u32 v151, v151, v138\n\t"
        "s_waitcnt lgkmcnt(8)\n\t"
        "v_mfma_f32_16x16x32_bf16 v[12:15], v[72:75], v[164:167], v[12:15]\n\t"
        "v_xor_b32 v139, v181, v139\n\t"
        "v_cvt_pk_bf16_f32 v4, v4, v5\n\t"
        "v_cvt_pk_bf16_f32 v5, v6, v7\n\t"
        "v_mfma_f32_16x16x32_bf16 v[52:55], v[200:203], v[96:99], v[52:55]\n\t"
        "ds_write_b64 v154, v[4:5] offset:24576\n\t"
        "v_mfma_f32_16x16x32_bf16 v[16:19], v[76:79], v[164:167], v[16:19]\n\t"
        "v_cvt_pk_bf16_f32 v8, v8, v9\n\t"
        "v_cvt_pk_bf16_f32 v9, v10, v11\n\t"
        "v_cvt_pk_bf16_f32 v119, v42, v43\n\t"
        "v_mfma_f32_16x16x32_bf16 v[56:59], v[204:207], v[96:99], v[56:59]\n\t"
        "ds_write_b64 v186, v[8:9] offset:24640\n\t"
        "s_waitcnt lgkmcnt(5)\n\t"
        "v_mfma_f32_16x16x32_bf16 v[20:23], v[80:83], v[164:167], v[20:23]\n\t"
        "v_lshlrev_b32_sdwa v157, 5, v160 dst_sel:DWORD dst_unused:UNUSED_PAD src0_sel:DWORD src1_sel:WORD_0\n\t"
        "v_lshlrev_b32_sdwa v158, 5, v160 dst_sel:DWORD dst_unused:UNUSED_PAD src0_sel:DWORD src1_sel:WORD_1\n\t"
        "v_or_b32 v157, v184, v157\n\t"
        "v_or_b32 v158, v184, v158\n\t"
        "v_mfma_f32_16x16x32_bf16 v[48:51], v[232:235], v[100:103], v[48:51]\n\t"
        "ds_read_b64_tr_b16 v[60:61], v157 offset:0\n\t"
        "v_xor_b32 v159, 32, v157\n\t"
        "v_mfma_f32_16x16x32_bf16 v[52:55], v[236:239], v[100:103], v[52:55]\n\t"
        "ds_read_b64_tr_b16 v[62:63], v158 offset:0\n\t"
        "ds_read_b64_tr_b16 v[64:65], v159 offset:0\n\t"
        "v_xor_b32 v152, 32, v158\n\t"
        "v_mfma_f32_16x16x32_bf16 v[56:59], v[240:243], v[100:103], v[56:59]\n\t"
        "ds_read_b64_tr_b16 v[66:67], v152 offset:0\n\t"
        "v_cvt_pk_bf16_f32 v12, v12, v13\n\t"
        "v_mfma_f32_16x16x32_bf16 v[48:51], a[0:3], v[104:107], v[48:51]\n\t"
        "ds_read_b64_tr_b16 v[68:69], v157 offset:64\n\t"
        "ds_read_b64_tr_b16 v[70:71], v158 offset:64\n\t"
        "v_cvt_pk_bf16_f32 v13, v14, v15\n\t"
        "v_mfma_f32_16x16x32_bf16 v[52:55], a[4:7], v[104:107], v[52:55]\n\t"
        "ds_write_b64 v154, v[12:13] offset:24640\n\t"
        "v_mfma_f32_16x16x32_bf16 v[56:59], a[8:11], v[104:107], v[56:59]\n\t"
        "ds_read_b128 v[96:99], v183 offset:0\n\t"
        "ds_read_b64_tr_b16 v[72:73], v159 offset:64\n\t"
        "v_cvt_pk_bf16_f32 v16, v16, v17\n\t"
        "v_mfma_f32_16x16x32_bf16 v[48:51], a[36:39], v[108:111], v[48:51]\n\t"
        "ds_read_b64_tr_b16 v[74:75], v152 offset:64\n\t"
        "ds_read_b64_tr_b16 v[76:77], v157 offset:128\n\t"
        "v_lshlrev_b32_sdwa v157, 5, v161 dst_sel:DWORD dst_unused:UNUSED_PAD src0_sel:DWORD src1_sel:WORD_0\n\t"
        "v_mfma_f32_16x16x32_bf16 v[52:55], a[40:43], v[108:111], v[52:55]\n\t"
        "ds_read_b64_tr_b16 v[78:79], v158 offset:128\n\t"
        "v_lshlrev_b32_sdwa v158, 5, v161 dst_sel:DWORD dst_unused:UNUSED_PAD src0_sel:DWORD src1_sel:WORD_1\n\t"
        "v_mfma_f32_16x16x32_bf16 v[56:59], a[44:47], v[108:111], v[56:59]\n\t"
        "ds_read_b128 v[100:103], v183 offset:64\n\t"
        "ds_read_b64_tr_b16 v[80:81], v159 offset:128\n\t"
        "v_or_b32 v157, v184, v157\n\t"
        "v_mfma_f32_16x16x32_bf16 v[48:51], a[72:75], v[112:115], v[48:51]\n\t"
        "ds_read_b64_tr_b16 v[82:83], v152 offset:128\n\t"
        "v_or_b32 v158, v184, v158\n\t"
        "v_mfma_f32_16x16x32_bf16 v[52:55], a[76:79], v[112:115], v[52:55]\n\t"
        "ds_read_b128 v[104:107], v183 offset:128\n\t"
        "v_add_u32 v183, s9, v179\n\t"
        "v_max_u32 v183, v183, v139\n\t"
        "v_mfma_f32_16x16x32_bf16 v[56:59], a[80:83], v[112:115], v[56:59]\n\t"
        "ds_read_b128 v[136:139], v187 offset:4112\n\t"
        "v_xor_b32 v140, v181, v140\n\t"
        "v_xor_b32 v159, 32, v157\n\t"
        "v_mfma_f32_16x16x32_bf16 v[48:51], a[108:111], v[84:87], v[48:51]\n\t"
        "ds_read_b128 v[108:111], v151 offset:0\n\t"
        "v_xor_b32 v152, 32, v158\n\t"
        "v_mfma_f32_16x16x32_bf16 v[52:55], a[112:115], v[84:87], v[52:55]\n\t"
        "ds_read_b128 v[112:115], v151 offset:64\n\t"
        "v_cvt_pk_bf16_f32 v17, v18, v19\n\t"
        "v_cvt_pk_bf16_f32 v20, v20, v21\n\t"
        "v_mfma_f32_16x16x32_bf16 v[56:59], a[116:119], v[84:87], v[56:59]\n\t"
        "ds_write_b64 v186, v[16:17] offset:24704\n\t"
        "s_waitcnt lgkmcnt(9)\n\t"
        "v_mfma_f32_16x16x32_bf16 v[48:51], a[144:147], v[88:91], v[48:51]\n\t"
        "ds_read_b128 v[84:87], v151 offset:128\n\t"
        "v_add_u32 v151, s9, v180\n\t"
        "v_max_u32 v151, v151, v140\n\t"
        "v_mfma_f32_16x16x32_bf16 v[52:55], a[148:151], v[88:91], v[52:55]\n\t"
        "ds_read_b32 v140, v187 offset:4128\n\t"
        "v_cvt_pk_bf16_f32 v21, v22, v23\n\t"
        "v_perm_b32 v164, v162, v162, v168\n\t"
        "v_mfma_f32_16x16x32_bf16 v[56:59], a[152:155], v[88:91], v[56:59]\n\t"
        "ds_write_b64 v154, v[20:21] offset:24704\n\t"
        "v_mfma_f32_16x16x32_bf16 v[48:51], a[180:183], v[92:95], v[48:51]\n\t"
        "ds_read_b128 v[88:91], v183 offset:0\n\t"
        "v_perm_b32 v165, v162, v162, v169\n\t"
        "v_perm_b32 v166, v162, v162, v170\n\t"
        "v_mfma_f32_16x16x32_bf16 v[52:55], a[184:187], v[92:95], v[52:55]\n\t"
        "v_perm_b32 v167, v162, v162, v171\n\t"
        "v_cvt_pk_bf16_f32 v120, v44, v45\n\t"
        "v_mfma_f32_16x16x32_bf16 v[56:59], a[188:191], v[92:95], v[56:59]\n\t"
        "v_cvt_pk_bf16_f32 v121, v46, v47\n\t"
        "v_permlane16_swap_b32 v116, v118\n\t"
        "v_mfma_f32_16x16x32_bf16 v[0:3], v[60:63], v[164:167], 0\n\t"
        "v_permlane16_swap_b32 v117, v119\n\t"
        "v_mfma_f32_16x16x32_bf16 v[48:51], a[216:219], v[96:99], v[48:51]\n\t"
        "buffer_store_dwordx4 v[116:119], v144, s[28:31], s55 offen sc1\n\t"
        "v_mfma_f32_16x16x32_bf16 v[4:7], v[64:67], v[164:167], 0\n\t"
        "ds_write_b64 v146, v[120:121] offset:6144\n\t"
        "v_mfma_f32_16x16x32_bf16 v[52:55], a[220:223], v[96:99], v[52:55]\n\t"
        "ds_read_b64_tr_b16 v[60:61], v157 offset:0\n\t"
        "ds_read_b64_tr_b16 v[62:63], v158 offset:0\n\t"
        "v_mfma_f32_16x16x32_bf16 v[8:11], v[68:71], v[164:167], 0\n\t"
        "ds_read_b64_tr_b16 v[64:65], v159 offset:0\n\t"
        "ds_read_b64_tr_b16 v[66:67], v152 offset:0\n\t"
        "v_mfma_f32_16x16x32_bf16 v[56:59], a[224:227], v[96:99], v[56:59]\n\t"
        "ds_read_b64_tr_b16 v[68:69], v157 offset:64\n\t"
        "ds_read_b64_tr_b16 v[70:71], v158 offset:64\n\t"
        "v_mfma_f32_16x16x32_bf16 v[12:15], v[72:75], v[164:167], 0\n\t"
        "ds_read_b64_tr_b16 v[72:73], v159 offset:64\n\t"
        "ds_read_b64_tr_b16 v[74:75], v152 offset:64\n\t"
        "s_waitcnt lgkmcnt(9)\n\t"
        "v_mfma_f32_16x16x32_bf16 v[48:51], a[252:255], v[100:103], v[48:51]\n\t"
        "ds_read_b128 v[92:95], v183 offset:64\n\t"
        "v_mfma_f32_16x16x32_bf16 v[16:19], v[76:79], v[164:167], 0\n\t"
        "ds_read_b64_tr_b16 v[76:77], v157 offset:128\n\t"
        "ds_read_b64_tr_b16 v[78:79], v158 offset:128\n\t"
        "v_mfma_f32_16x16x32_bf16 v[52:55], v[188:191], v[100:103], v[52:55]\n\t"
        "ds_read_b128 v[96:99], v183 offset:128\n\t"
        "v_mfma_f32_16x16x32_bf16 v[20:23], v[80:83], v[164:167], 0\n\t"
        "ds_read_b64_tr_b16 v[80:81], v159 offset:128\n\t"
        "ds_read_b64_tr_b16 v[82:83], v152 offset:128\n\t"
        "v_perm_b32 v164, v163, v163, v168\n\t"
        "v_perm_b32 v165, v163, v163, v169\n\t"
        "v_mfma_f32_16x16x32_bf16 v[56:59], v[192:195], v[100:103], v[56:59]\n\t"
        "v_perm_b32 v166, v163, v163, v170\n\t"
        "v_perm_b32 v167, v163, v163, v171\n\t"
        "v_mfma_f32_16x16x32_bf16 v[48:51], v[220:223], v[104:107], v[48:51]\n\t"
        "ds_read_b128 v[100:103], v151 offset:0\n\t"
        "s_waitcnt lgkmcnt(12)\n\t"
        "v_mfma_f32_16x16x32_bf16 v[0:3], v[60:63], v[164:167], v[0:3]\n\t"
        "v_mfma_f32_16x16x32_bf16 v[52:55], v[224:227], v[104:107], v[52:55]\n\t"
        "s_waitcnt lgkmcnt(9)\n\t"
        "v_mfma_f32_16x16x32_bf16 v[4:7], v[64:67], v[164:167], v[4:7]\n\t"
        "v_mfma_f32_16x16x32_bf16 v[56:59], v[228:231], v[104:107], v[56:59]\n\t"
        "v_mfma_f32_16x16x32_bf16 v[8:11], v[68:71], v[164:167], v[8:11]\n\t"
        "s_nop 2\n\t"
        "v_cvt_pk_bf16_f32 v0, v0, v1\n\t"
        "v_cvt_pk_bf16_f32 v1, v2, v3\n\t"
        "v_mfma_f32_16x16x32_bf16 v[48:51], a[24:27], v[108:111], v[48:51]\n\t"
        "ds_write_b64 v186, v[0:1] offset:36864\n\t"
        "s_waitcnt lgkmcnt(3)\n\t"
        "v_mfma_f32_16x16x32_bf16 v[12:15], v[72:75], v[164:167], v[12:15]\n\t"
        "ds_read_b128 v[104:107], v151 offset:64\n\t"
        "v_cvt_pk_bf16_f32 v4, v4, v5\n\t"
        "v_cvt_pk_bf16_f32 v5, v6, v7\n\t"
        "v_mfma_f32_16x16x32_bf16 v[52:55], a[28:31], v[108:111], v[52:55]\n\t"
        "ds_write_b64 v154, v[4:5] offset:36864\n\t"
        "v_mfma_f32_16x16x32_bf16 v[16:19], v[76:79], v[164:167], v[16:19]\n\t"
        "v_cvt_pk_bf16_f32 v8, v8, v9\n\t"
        "v_cvt_pk_bf16_f32 v9, v10, v11\n\t"
        "v_mfma_f32_16x16x32_bf16 v[56:59], a[32:35], v[108:111], v[56:59]\n\t"
        "ds_write_b64 v186, v[8:9] offset:36928\n\t"
        "s_waitcnt lgkmcnt(4)\n\t"
        "v_mfma_f32_16x16x32_bf16 v[20:23], v[80:83], v[164:167], v[20:23]\n\t"
        "v_cvt_pk_bf16_f32 v12, v12, v13\n\t"
        "v_cvt_pk_bf16_f32 v13, v14, v15\n\t"
        "v_mfma_f32_16x16x32_bf16 v[48:51], a[60:63], v[112:115], v[48:51]\n\t"
        "ds_write_b64 v154, v[12:13] offset:36928\n\t"
        "v_mfma_f32_16x16x32_bf16 v[52:55], a[64:67], v[112:115], v[52:55]\n\t"
        "ds_read_b128 v[108:111], v151 offset:128\n\t"
        "v_cvt_pk_bf16_f32 v16, v16, v17\n\t"
        "v_cvt_pk_bf16_f32 v17, v18, v19\n\t"
        "v_mfma_f32_16x16x32_bf16 v[56:59], a[68:71], v[112:115], v[56:59]\n\t"
        "ds_write_b64 v186, v[16:17] offset:36992\n\t"
        "v_mfma_f32_16x16x32_bf16 v[48:51], a[96:99], v[84:87], v[48:51]\n\t"
        "v_cvt_pk_bf16_f32 v20, v20, v21\n\t"
        "v_cvt_pk_bf16_f32 v21, v22, v23\n\t"
        "v_mfma_f32_16x16x32_bf16 v[52:55], a[100:103], v[84:87], v[52:55]\n\t"
        "ds_write_b64 v154, v[20:21] offset:36992\n\t"
        "s_cmp_gt_u32 s61, 64\n\t"
        "s_cbranch_scc1 .Lx70%=\n\t"
        ".Lx71%=:\n\t"
        "v_mfma_f32_16x16x32_bf16 v[56:59], a[104:107], v[84:87], v[56:59]\n\t"
        "v_mfma_f32_16x16x32_bf16 v[48:51], a[132:135], v[88:91], v[48:51]\n\t"
        "v_mfma_f32_16x16x32_bf16 v[52:55], a[136:139], v[88:91], v[52:55]\n\t"
        "v_mfma_f32_16x16x32_bf16 v[56:59], a[140:143], v[88:91], v[56:59]\n\t"
        "v_mfma_f32_16x16x32_bf16 v[48:51], a[168:171], v[92:95], v[48:51]\n\t"
        "v_mfma_f32_16x16x32_bf16 v[52:55], a[172:175], v[92:95], v[52:55]\n\t"
        "s_waitcnt lgkmcnt(0)\n\t"
        "s_barrier\n\t"
        "s_bitcmp1_b32 s6, 2\n\t"
        "s_cbranch_scc0 .Lx72%=\n\t"
        "ds_read_b128 v[132:135], v187 offset:4096\n\t"
        "ds_read_b128 v[136:139], v187 offset:4112\n\t"
        "ds_read_b32 v140, v187 offset:4128\n\t"
        "s_waitcnt lgkmcnt(0)\n\t"
        ".Lx72%=:\n\t"
        "v_xor_b32 v132, v181, v132\n\t"
        "v_mfma_f32_16x16x32_bf16 v[56:59], a[176:179], v[92:95], v[56:59]\n\t"
        "ds_read_b128 v[124:127], v182 offset:16\n\t"
        "v_readlane_b32 s9, v155, 48\n\t"
        "v_mfma_f32_16x16x32_bf16 v[48:51], a[204:207], v[96:99], v[48:51]\n\t"
        "s_nop 0\n\t"
        "v_add_u32 v183, s9, v172\n\t"
        "v_max_u32 v183, v183, v132\n\t"
        "v_xor_b32 v133, v181, v133\n\t"
        "v_add_u32 v151, s9, v173\n\t"
        "v_mfma_f32_16x16x32_bf16 v[52:55], a[208:211], v[96:99], v[52:55]\n\t"
        "ds_read_b128 v[112:115], v183 offset:0\n\t"
        "v_max_u32 v151, v151, v133\n\t"
        "v_mfma_f32_16x16x32_bf16 v[56:59], a[212:215], v[96:99], v[56:59]\n\t"
        "ds_read_b128 v[84:87], v183 offset:64\n\t"
        "v_xor_b32 v134, v181, v134\n\t"
        "v_mfma_f32_16x16x32_bf16 v[48:51], a[240:243], v[100:103], v[48:51]\n\t"
        "ds_read_b128 v[88:91], v183 offset:128\n\t"
        "v_add_u32 v183, s9, v174\n\t"
        "v_mfma_f32_16x16x32_bf16 v[52:55], a[244:247], v[100:103], v[52:55]\n\t"
        "ds_read_b128 v[92:95], v151 offset:0\n\t"
        "v_max_u32 v183, v183, v134\n\t"
        "v_mfma_f32_16x16x32_bf16 v[56:59], a[248:251], v[100:103], v[56:59]\n\t"
        "ds_read_b128 v[96:99], v151 offset:64\n\t"
        "v_xor_b32 v135, v181, v135\n\t"
        "v_mfma_f32_16x16x32_bf16 v[48:51], v[208:211], v[104:107], v[48:51]\n\t"
        "ds_read_b128 v[100:103], v151 offset:128\n\t"
        "v_add_u32 v151, s9, v175\n\t"
        "v_mfma_f32_16x16x32_bf16 v[52:55], v[212:215], v[104:107], v[52:55]\n\t"
        "v_max_u32 v151, v151, v135\n\t"
        "v_xor_b32 v136, v181, v136\n\t"
        "v_mfma_f32_16x16x32_bf16 v[56:59], v[216:219], v[104:107], v[56:59]\n\t"
        "s_waitcnt lgkmcnt(6)\n\t"
        "v_or3_b32 v124, v124, v125, v126\n\t"
        "v_or_b32 v124, v124, v127\n\t"
        "v_mfma_f32_16x16x32_bf16 v[48:51], v[244:247], v[108:111], v[48:51]\n\t"
        "ds_read_b128 v[104:107], v183 offset:0\n\t"
        "v_readfirstlane_b32 s8, v124\n\t"
        "v_mfma_f32_16x16x32_bf16 v[52:55], v[248:251], v[108:111], v[52:55]\n\t"
        "ds_read_b128 v[124:127], v147 offset:0\n\t"
        "s_or_b32 s6, s6, s8\n\t"
        "v_mfma_f32_16x16x32_bf16 v[56:59], v[252:255], v[108:111], v[56:59]\n\t"
        "s_waitcnt lgkmcnt(7)\n\t"
        "v_mfma_f32_16x16x32_bf16 v[72:75], a[12:15], v[112:115], 0\n\t"
        "ds_read_b128 v[108:111], v183 offset:64\n\t"
        "v_mfma_f32_16x16x32_bf16 v[76:79], a[16:19], v[112:115], 0\n\t"
        "v_mfma_f32_16x16x32_bf16 v[80:83], a[20:23], v[112:115], 0\n\t"
        "s_waitcnt lgkmcnt(5)\n\t"
        "v_mfma_f32_16x16x32_bf16 v[72:75], a[48:51], v[84:87], v[72:75]\n\t"
        "ds_read_b128 v[112:115], v183 offset:128\n\t"
        "v_add_u32 v183, s9, v176\n\t"
        "v_mfma_f32_16x16x32_bf16 v[76:79], a[52:55], v[84:87], v[76:79]\n\t"
        "v_max_u32 v183, v183, v136\n\t"
        "v_xor_b32 v137, v181, v137\n\t"
        "v_mfma_f32_16x16x32_bf16 v[80:83], a[56:59], v[84:87], v[80:83]\n\t"
        "v_mfma_f32_16x16x32_bf16 v[72:75], a[84:87], v[88:91], v[72:75]\n\t"
        "ds_read_b128 v[84:87], v151 offset:0\n\t"
        "v_mfma_f32_16x16x32_bf16 v[76:79], a[88:91], v[88:91], v[76:79]\n\t"
        "s_waitcnt lgkmcnt(3)\n\t"
        "buffer_store_dwordx4 v[124:127], v145, s[28:31], s50 offen sc1\n\t"
        "v_mfma_f32_16x16x32_bf16 v[80:83], a[92:95], v[88:91], v[80:83]\n\t"
        "ds_read_b128 v[124:127], v147 offset:4096\n\t"
        "s_add_u32 s50, s50, 86016\n\t"
        "v_mfma_f32_16x16x32_bf16 v[72:75], a[120:123], v[92:95], v[72:75]\n\t"
        "ds_read_b128 v[88:91], v151 offset:64\n\t"
        "v_mfma_f32_16x16x32_bf16 v[76:79], a[124:127], v[92:95], v[76:79]\n\t"
        "v_mfma_f32_16x16x32_bf16 v[80:83], a[128:131], v[92:95], v[80:83]\n\t"
        "v_mfma_f32_16x16x32_bf16 v[72:75], a[156:159], v[96:99], v[72:75]\n\t"
        "ds_read_b128 v[92:95], v151 offset:128\n\t"
        "v_add_u32 v151, s9, v177\n\t"
        "v_mfma_f32_16x16x32_bf16 v[76:79], a[160:163], v[96:99], v[76:79]\n\t"
        "v_max_u32 v151, v151, v137\n\t"
        "v_xor_b32 v138, v181, v138\n\t"
        "v_mfma_f32_16x16x32_bf16 v[80:83], a[164:167], v[96:99], v[80:83]\n\t"
        "v_mfma_f32_16x16x32_bf16 v[72:75], a[192:195], v[100:103], v[72:75]\n\t"
        "ds_read_b128 v[96:99], v183 offset:0\n\t"
        "v_mfma_f32_16x16x32_bf16 v[76:79], a[196:199], v[100:103], v[76:79]\n\t"
        "v_mfma_f32_16x16x32_bf16 v[80:83], a[200:203], v[100:103], v[80:83]\n\t"
        "v_mfma_f32_16x16x32_bf16 v[72:75], a[228:231], v[104:107], v[72:75]\n\t"
        "ds_read_b128 v[100:103], v183 offset:64\n\t"
        "v_mfma_f32_16x16x32_bf16 v[76:79], a[232:235], v[104:107], v[76:79]\n\t"
        "v_mfma_f32_16x16x32_bf16 v[80:83], a[236:239], v[104:107], v[80:83]\n\t"
        "s_waitcnt lgkmcnt(3)\n\t"
        "v_mfma_f32_16x16x32_bf16 v[72:75], v[196:199], v[108:111], v[72:75]\n\t"
        "buffer_store_dwordx4 v[124:127], v145, s[28:31], s50 offen sc1\n\t"
        "v_mfma_f32_16x16x32_bf16 v[76:79], v[200:203], v[108:111], v[76:79]\n\t"
        "ds_read_b128 v[104:107], v183 offset:128\n\t"
        "v_add_u32 v183, s9, v178\n\t"
        "v_mfma_f32_16x16x32_bf16 v[80:83], v[204:207], v[108:111], v[80:83]\n\t"
        "v_max_u32 v183, v183, v138\n\t"
        "v_xor_b32 v139, v181, v139\n\t"
        "v_mfma_f32_16x16x32_bf16 v[72:75], v[232:235], v[112:115], v[72:75]\n\t"
        "ds_read_b128 v[108:111], v151 offset:0\n\t"
        "v_readlane_b32 s55, v155, 49\n\t"
        "v_mfma_f32_16x16x32_bf16 v[76:79], v[236:239], v[112:115], v[76:79]\n\t"
        "v_cvt_pk_bf16_f32 v116, v48, v49\n\t"
        "v_cvt_pk_bf16_f32 v117, v50, v51\n\t"
        "v_mfma_f32_16x16x32_bf16 v[80:83], v[240:243], v[112:115], v[80:83]\n\t"
        "v_cvt_pk_bf16_f32 v118, v52, v53\n\t"
        "v_cvt_pk_bf16_f32 v119, v54, v55\n\t"
        "v_mfma_f32_16x16x32_bf16 v[72:75], a[0:3], v[84:87], v[72:75]\n\t"
        "ds_read_b128 v[112:115], v151 offset:64\n\t"
        "v_cvt_pk_bf16_f32 v120, v56, v57\n\t"
        ".LpX2%=:\n\t"
        "s_cbranch_execz .LrX%=\n\t"
        "v_mfma_f32_16x16x32_bf16 v[76:79], a[4:7], v[84:87], v[76:79]\n\t"
        "v_cvt_pk_bf16_f32 v121, v58, v59\n\t"
        "v_mfma_f32_16x16x32_bf16 v[80:83], a[8:11], v[84:87], v[80:83]\n\t"
        "v_permlane16_swap_b32 v116, v118\n\t"
        "v_mfma_f32_16x16x32_bf16 v[72:75], a[36:39], v[88:91], v[72:75]\n\t"
        "v_permlane16_swap_b32 v117, v119\n\t"
        "v_mfma_f32_16x16x32_bf16 v[76:79], a[40:43], v[88:91], v[76:79]\n\t"
        "ds_read_b128 v[84:87], v151 offset:128\n\t"
        "v_add_u32 v151, s9, v179\n\t"
        "v_mfma_f32_16x16x32_bf16 v[80:83], a[44:47], v[88:91], v[80:83]\n\t"
        "v_max_u32 v151, v151, v139\n\t"
        "v_xor_b32 v140, v181, v140\n\t"
        "s_waitcnt lgkmcnt(3)\n\t"
        "v_mfma_f32_16x16x32_bf16 v[72:75], a[72:75], v[92:95], v[72:75]\n\t"
        "ds_read_b128 v[88:91], v183 offset:0\n\t"
        "v_mfma_f32_16x16x32_bf16 v[76:79], a[76:79], v[92:95], v[76:79]\n\t"
        "v_mfma_f32_16x16x32_bf16 v[80:83], a[80:83], v[92:95], v[80:83]\n\t"
        "buffer_store_dwordx4 v[116:119], v148, s[28:31], s55 offen sc1\n\t"
        "v_mfma_f32_16x16x32_bf16 v[72:75], a[108:111], v[96:99], v[72:75]\n\t"
        "ds_write_b64 v146, v[120:121] offset:8192\n\t"
        "v_mfma_f32_16x16x32_bf16 v[76:79], a[112:115], v[96:99], v[76:79]\n\t"
        "ds_read_b128 v[92:95], v183 offset:64\n\t"
        "v_mfma_f32_16x16x32_bf16 v[80:83], a[116:119], v[96:99], v[80:83]\n\t"
        "ds_read_b128 v[96:99], v183 offset:128\n\t"
        "v_add_u32 v183, s9, v180\n\t"
        "v_mfma_f32_16x16x32_bf16 v[72:75], a[144:147], v[100:103], v[72:75]\n\t"
        "v_max_u32 v183, v183, v140\n\t"
        "v_mfma_f32_16x16x32_bf16 v[76:79], a[148:151], v[100:103], v[76:79]\n\t"
        "v_mfma_f32_16x16x32_bf16 v[80:83], a[152:155], v[100:103], v[80:83]\n\t"
        "ds_read_b128 v[100:103], v151 offset:0\n\t"
        "v_mfma_f32_16x16x32_bf16 v[72:75], a[180:183], v[104:107], v[72:75]\n\t"
        "v_mfma_f32_16x16x32_bf16 v[76:79], a[184:187], v[104:107], v[76:79]\n\t"
        "v_mfma_f32_16x16x32_bf16 v[80:83], a[188:191], v[104:107], v[80:83]\n\t"
        "ds_read_b128 v[104:107], v151 offset:64\n\t"
        "s_waitcnt lgkmcnt(5)\n\t"
        "v_mfma_f32_16x16x32_bf16 v[72:75], a[216:219], v[108:111], v[72:75]\n\t"
        "v_mfma_f32_16x16x32_bf16 v[76:79], a[220:223], v[108:111], v[76:79]\n\t"
        "v_mfma_f32_16x16x32_bf16 v[80:83], a[224:227], v[108:111], v[80:83]\n\t"
        "ds_read_b128 v[108:111], v151 offset:128\n\t"
        "v_mfma_f32_16x16x32_bf16 v[72:75], a[252:255], v[112:115], v[72:75]\n\t"
        "v_mfma_f32_16x16x32_bf16 v[76:79], v[188:191], v[112:115], v[76:79]\n\t"
        "v_mfma_f32_16x16x32_bf16 v[80:83], v[192:195], v[112:115], v[80:83]\n\t"
        "ds_read_b128 v[112:115], v183 offset:0\n\t"
        "v_mfma_f32_16x16x32_bf16 v[72:75], v[220:223], v[84:87], v[72:75]\n\t"
        "v_mfma_f32_16x16x32_bf16 v[76:79], v[224:227], v[84:87], v[76:79]\n\t"
        "v_mfma_f32_16x16x32_bf16 v[80:83], v[228:231], v[84:87], v[80:83]\n\t"
        "ds_read_b128 v[84:87], v183 offset:64\n\t"
        "v_mfma_f32_16x16x32_bf16 v[72:75], a[24:27], v[88:91], v[72:75]\n\t"
        "v_mfma_f32_16x16x32_bf16 v[76:79], a[28:31], v[88:91], v[76:79]\n\t"
        "v_mfma_f32_16x16x32_bf16 v[80:83], a[32:35], v[88:91], v[80:83]\n\t"
        "ds_read_b128 v[88:91], v183 offset:128\n\t"
        "s_waitcnt lgkmcnt(4)\n\t"
        "v_mfma_f32_16x16x32_bf16 v[80:83], a[68:71], v[92:95], v[80:83]\n\t"
        "v_mfma_f32_16x16x32_bf16 v[80:83], a[104:107], v[96:99], v[80:83]\n\t"
        "v_mfma_f32_16x16x32_bf16 v[80:83], a[140:143], v[100:103], v[80:83]\n\t"
        "v_mfma_f32_16x16x32_bf16 v[80:83], a[176:179], v[104:107], v[80:83]\n\t"
        "s_waitcnt lgkmcnt(2)\n\t"
        "v_mfma_f32_16x16x32_bf16 v[80:83], a[212:215], v[108:111], v[80:83]\n\t"
        "v_mfma_f32_16x16x32_bf16 v[80:83], a[248:251], v[112:115], v[80:83]\n\t"
        "s_waitcnt lgkmcnt(1)\n\t"
        "v_mfma_f32_16x16x32_bf16 v[80:83], v[216:219], v[84:87], v[80:83]\n\t"
        "s_waitcnt lgkmcnt(0)\n\t"
        "v_mfma_f32_16x16x32_bf16 v[80:83], v[252:255], v[88:91], v[80:83]\n\t"
        "s_nop 2\n\t"
        "v_mfma_f32_16x16x32_bf16 v[72:75], a[60:63], v[92:95], v[72:75]\n\t"
        "s_nop 2\n\t"
        "v_mfma_f32_16x16x32_bf16 v[72:75], a[96:99], v[96:99], v[72:75]\n\t"
        "v_cvt_pk_bf16_f32 v120, v80, v81\n\t"
        "v_cvt_pk_bf16_f32 v121, v82, v83\n\t"
        "v_mfma_f32_16x16x32_bf16 v[72:75], a[132:135], v[100:103], v[72:75]\n\t"
        "ds_write_b64 v146, v[120:121] offset:10240\n\t"
        "v_mfma_f32_16x16x32_bf16 v[72:75], a[168:171], v[104:107], v[72:75]\n\t"
        "v_mfma_f32_16x16x32_bf16 v[72:75], a[204:207], v[108:111], v[72:75]\n\t"
        "v_mfma_f32_16x16x32_bf16 v[72:75], a[240:243], v[112:115], v[72:75]\n\t"
        "s_waitcnt lgkmcnt(0)\n\t"
        "s_barrier\n\t"
        "v_mfma_f32_16x16x32_bf16 v[72:75], v[208:211], v[84:87], v[72:75]\n\t"
        "ds_read_b128 v[124:127], v147 offset:8192\n\t"
        "v_readlane_b32 s55, v155, 51\n\t"
        "v_mfma_f32_16x16x32_bf16 v[72:75], v[244:247], v[88:91], v[72:75]\n\t"
        "s_nop 1\n\t"
        "v_mfma_f32_16x16x32_bf16 v[76:79], a[64:67], v[92:95], v[76:79]\n\t"
        "s_nop 1\n\t"
        "v_mfma_f32_16x16x32_bf16 v[76:79], a[100:103], v[96:99], v[76:79]\n\t"
        "v_mfma_f32_16x16x32_bf16 v[76:79], a[136:139], v[100:103], v[76:79]\n\t"
        "v_mfma_f32_16x16x32_bf16 v[76:79], a[172:175], v[104:107], v[76:79]\n\t"
        "v_mfma_f32_16x16x32_bf16 v[76:79], a[208:211], v[108:111], v[76:79]\n\t"
        "v_cvt_pk_bf16_f32 v116, v72, v73\n\t"
        "v_cvt_pk_bf16_f32 v117, v74, v75\n\t"
        "v_mfma_f32_16x16x32_bf16 v[76:79], a[244:247], v[112:115], v[76:79]\n\t"
        "s_waitcnt lgkmcnt(0)\n\t"
        "buffer_store_dwordx4 v[124:127], v150, s[28:31], s52 offen sc1\n\t"
        "v_mfma_f32_16x16x32_bf16 v[76:79], v[212:215], v[84:87], v[76:79]\n\t"
        "v_mfma_f32_16x16x32_bf16 v[76:79], v[248:251], v[88:91], v[76:79]\n\t"
        "s_nop 7\n\t"
        "v_cvt_pk_bf16_f32 v118, v76, v77\n\t"
        "v_cvt_pk_bf16_f32 v119, v78, v79\n\t"
        "s_nop 0\n\t"
        "v_permlane16_swap_b32 v116, v118\n\t"
        "v_permlane16_swap_b32 v117, v119\n\t"
        "buffer_store_dwordx4 v[116:119], v148, s[28:31], s55 offen sc1\n\t"
        "s_andn2_b32 s6, s6, 6\n\t"
        "s_cbranch_scc1 99f\n\t"
        "s_endpgm\n\t"
        "22:\n\t"
        "ds_bpermute_b32 v64, v65, v155 offset:172\n\t"
        "s_add_u32 m0, s37, -24576\n\t"
        "s_add_u32 s36, s36, 6144\n\t"
        "buffer_load_dwordx4 v140, s[24:27], s36 offen offset:0 lds\n\t"
        "ds_bpermute_b32 v78, v81, v155 offset:148\n\t"
        "ds_bpermute_b32 v79, v81, v155 offset:160\n\t"
        "v_readlane_b32 s71, v155, 26\n\t"
        "v_readlane_b32 s10, v155, 29\n\t"
        "s_nop 0\n\t"
        "v_mul_u32_u24 v73, s71, v66\n\t"
        "v_lshrrev_b32 v73, 11, v73\n\t"
        "v_lshlrev_b32 v66, 4, v66\n\t"
        "v_mad_u32_u24 v73, v73, s10, v66\n\t"
        "s_waitcnt lgkmcnt(2)\n\t"
        "v_add_u32 v64, v64, v73\n\t"
        "buffer_load_dwordx4 v141, s[24:27], s36 offen offset:1024 lds\n\t"
        "v_readlane_b32 s69, v155, 34\n\t"
        "v_readlane_b32 s70, v155, 35\n\t"
        "s_nop 0\n\t"
        "v_mad_u32_u24 v70, v52, s69, v70\n\t"
        "v_mad_u32_u24 v70, v158, s70, v70\n\t"
        "s_mul_i32 s71, s69, s68\n\t"
        "s_add_u32 s71, s71, 157728\n\t"
        "v_mad_u32_u24 v67, v82, s70, v67\n\t"
        "v_add_u32 v67, s71, v67\n\t"
        "v_readlane_b32 s11, v155, 30\n\t"
        "buffer_load_dwordx4 v142, s[24:27], s36 offen offset:2048 lds\n\t"
        "s_movk_i32 s10, 223\n\t"
        "v_mov_b32 v75, 0x7ff00000\n\t"
        "v_readlane_b32 s12, v155, 24\n\t"
        "v_readlane_b32 s13, v155, 25\n\t"
        "s_nop 0\n\t"
        "s_bfe_u32 s70, s12, 524288\n\t"
        "s_bfe_u32 s71, s13, 524288\n\t"
        "v_cmp_gt_u32 vcc, s71, v71\n\t"
        "v_add3_u32 v73, v71, s70, -2\n\t"
        "v_med3_i32 v73, v73, 0, s10\n\t"
        "s_add_u32 m0, s35, 4096\n\t"
        "s_add_u32 s43, s34, 43008\n\t"
        "buffer_load_dwordx4 v143, s[16:19], s43 offen  lds\n\t"
        "v_cndmask_b32 v74, v75, v72, vcc\n\t"
        "v_mad_u32_u24 v134, v73, s11, v74\n\t"
        "s_bfe_u32 s70, s12, 524296\n\t"
        "s_bfe_u32 s71, s13, 524296\n\t"
        "v_cmp_gt_u32 vcc, s71, v71\n\t"
        "v_add3_u32 v73, v71, s70, -2\n\t"
        "v_med3_i32 v73, v73, 0, s10\n\t"
        "v_cndmask_b32 v74, v75, v72, vcc\n\t"
        "v_mad_u32_u24 v135, v73, s11, v74\n\t"
        "s_add_u32 m0, s35, 20480\n\t"
        "s_add_u32 s43, s34, 215040\n\t"
        "buffer_load_dwordx4 v143, s[16:19], s43 offen  lds\n\t"
        "s_bfe_u32 s70, s12, 524304\n\t"
        "s_bfe_u32 s71, s13, 524304\n\t"
        "v_cmp_gt_u32 vcc, s71, v71\n\t"
        "v_add3_u32 v73, v71, s70, -2\n\t"
        "v_med3_i32 v73, v73, 0, s10\n\t"
        "v_cndmask_b32 v74, v75, v72, vcc\n\t"
        "v_mad_u32_u24 v136, v73, s11, v74\n\t"
        "s_bfe_u32 s70, s12, 524312\n\t"
        "s_bfe_u32 s71, s13, 524312\n\t"
        "v_cmp_gt_u32 vcc, s71, v71\n\t"
        "v_add3_u32 v73, v71, s70, -2\n\t"
        "v_med3_i32 v73, v73, 0, s10\n\t"
        "v_cndmask_b32 v74, v75, v72, vcc\n\t"
        "v_mad_u32_u24 v137, v73, s11, v74\n\t"
        "s_mov_b64 exec, 0\n\t"
        "s_cmp_eq_u32 s4, 0\n\t"
        "s_cbranch_scc1 .LpY0%=\n\t"
        "s_cmp_eq_u32 s4, 1\n\t"
        "s_cbranch_scc1 .LpY1%=\n\t"
        "s_cmp_eq_u32 s4, 2\n\t"
        "s_cbranch_scc1 .LpY2%=\n\t"
        "s_nop 0\n\t"
        ".LrY%=:\n\t"
        "s_mov_b64 exec, -1\n\t"
        "s_waitcnt vmcnt(9)\n\t"
        "s_barrier\n\t"
        "ds_read_b64_tr_b16 a[0:1], v138 offset:6144\n\t"
        "ds_read_b64_tr_b16 a[2:3], v138 offset:6912\n\t"
        "ds_read_b64_tr_b16 a[4:5], v139 offset:6144\n\t"
        "ds_read_b64_tr_b16 a[6:7], v139 offset:6912\n\t"
        "ds_read_b64_tr_b16 a[8:9], v149 offset:6144\n\t"
        "ds_read_b64_tr_b16 a[10:11], v149 offset:6912\n\t"
        "ds_read_b128 v[84:87], v128 offset:8192\n\t"
        "ds_read_b64_tr_b16 a[12:13], v138 offset:12288\n\t"
        "ds_read_b64_tr_b16 a[14:15], v138 offset:13056\n\t"
        "ds_read_b64_tr_b16 a[16:17], v139 offset:12288\n\t"
        "ds_read_b64_tr_b16 a[18:19], v139 offset:13056\n\t"
        "ds_read_b64_tr_b16 a[20:21], v149 offset:12288\n\t"
        "ds_read_b64_tr_b16 a[22:23], v149 offset:13056\n\t"
        "ds_read_b128 v[88:91], v128 offset:8192\n\t"
        "s_add_u32 m0, s37, -12288\n\t"
        "s_add_u32 s36, s36, s39\n\t"
        "s_waitcnt lgkmcnt(7)\n\t"
        "v_mfma_f32_16x16x32_bf16 v[12:15], a[0:3], v[84:87], 0\n\t"
        "buffer_load_dwordx4 v140, s[24:27], s36 offen offset:0 lds\n\t"
        "buffer_load_dwordx4 v141, s[24:27], s36 offen offset:1024 lds\n\t"
        "buffer_load_dwordx4 v142, s[24:27], s36 offen offset:2048 lds\n\t"
        "s_waitcnt vmcnt(8)\n\t"
        "s_barrier\n\t"
        "ds_read_b128 v[92:95], v128 offset:12288\n\t"
        "ds_read_b64_tr_b16 a[24:25], v138 offset:0\n\t"
        "v_mfma_f32_16x16x32_bf16 v[16:19], a[4:7], v[84:87], 0\n\t"
        "ds_read_b64_tr_b16 a[26:27], v138 offset:768\n\t"
        "s_add_u32 m0, s35, 157696\n\t"
        "s_nop 0\n\t"
        "buffer_load_dwordx4 v64, s[20:23], 0 offen lds\n\t"
        "s_add_u32 m0, s35, 16384\n\t"
        "s_add_u32 s43, s34, 172032\n\t"
        "v_mfma_f32_16x16x32_bf16 v[20:23], a[8:11], v[84:87], 0\n\t"
        "buffer_load_dwordx4 v143, s[16:19], s43 offen  lds\n\t"
        "s_add_u32 m0, s35, 32768\n\t"
        "ds_read_b64_tr_b16 a[28:29], v139 offset:0\n\t"
        "s_add_u32 s43, s34, 344064\n\t"
        "s_min_u32 s43, s43, 9590784\n\t"
        "s_waitcnt lgkmcnt(4)\n\t"
        "v_mfma_f32_16x16x32_bf16 v[24:27], a[12:15], v[88:91], 0\n\t"
        "buffer_load_dwordx4 v143, s[16:19], s43 offen  lds\n\t"
        "s_add_u32 m0, s37, 0\n\t"
        "ds_read_b64_tr_b16 a[30:31], v139 offset:768\n\t"
        "s_add_u32 s36, s36, s38\n\t"
        "ds_read_b64_tr_b16 a[32:33], v149 offset:0\n\t"
        "v_mfma_f32_16x16x32_bf16 v[28:31], a[16:19], v[88:91], 0\n\t"
        "buffer_load_dwordx4 v140, s[24:27], s36 offen offset:0 lds\n\t"
        "ds_read_b64_tr_b16 a[34:35], v149 offset:768\n\t"
        "ds_read_b128 v[96:99], v128 offset:8192\n\t"
        "v_readlane_b32 s44, v155, 32\n\t"
        "v_mfma_f32_16x16x32_bf16 v[32:35], a[20:23], v[88:91], 0\n\t"
        "buffer_load_dwordx4 v141, s[24:27], s36 offen offset:1024 lds\n\t"
        "v_readlane_b32 s45, v155, 33\n\t"
        "s_waitcnt lgkmcnt(7)\n\t"
        "v_mfma_f32_16x16x32_bf16 v[24:27], a[0:3], v[92:95], v[24:27]\n\t"
        "ds_read_b128 v[100:103], v128 offset:12288\n\t"
        "v_readlane_b32 s62, v155, 36\n\t"
        "v_mfma_f32_16x16x32_bf16 v[28:31], a[4:7], v[92:95], v[28:31]\n\t"
        "s_nop 0\n\t"
        "s_lshl_b32 s70, s62, 5\n\t"
        "s_sub_u32 s71, s70, 16384\n\t"
        "v_mfma_f32_16x16x32_bf16 v[32:35], a[8:11], v[92:95], v[32:35]\n\t"
        "buffer_load_dwordx4 v142, s[24:27], s36 offen offset:2048 lds\n\t"
        "s_add_u32 m0, s35, 0\n\t"
        "s_add_u32 s43, s34, 0\n\t"
        "v_mfma_f32_16x16x32_bf16 v[36:39], a[12:15], v[92:95], 0\n\t"
        "s_mov_b32 exec_lo, 0\n\t"
        "s_mov_b32 exec_hi, 0xfff00000\n\t"
        "ds_write_b32 v119, v155\n\t"
        "s_mov_b64 exec, -1\n\t"
        "v_mfma_f32_16x16x32_bf16 v[40:43], a[16:19], v[92:95], 0\n\t"
        "s_mul_i32 s71, s71, s68\n\t"
        "v_add_u32 v172, s71, v128\n\t"
        "v_mfma_f32_16x16x32_bf16 v[44:47], a[20:23], v[92:95], 0\n\t"
        "buffer_load_dwordx4 v143, s[16:19], s43 offen  lds\n\t"
        "s_add_u32 m0, s35, 36864\n\t"
        "s_add_u32 s43, s34, 387072\n\t"
        "s_min_u32 s43, s43, 9590784\n\t"
        "buffer_load_dwordx4 v143, s[16:19], s43 offen  lds\n\t"
        "s_waitcnt vmcnt(8)\n\t"
        "s_barrier\n\t"
        "ds_read_b128 v[104:107], v128 offset:4096\n\t"
        "s_add_u32 m0, s37, 12288\n\t"
        "ds_read_b64_tr_b16 a[36:37], v138 offset:18432\n\t"
        "s_add_u32 s36, s36, s5\n\t"
        "s_waitcnt lgkmcnt(4)\n\t"
        "v_mfma_f32_16x16x32_bf16 v[0:3], a[24:27], v[96:99], 0\n\t"
        "buffer_load_dwordx4 v140, s[24:27], s36 offen offset:0 lds\n\t"
        "ds_read_b64_tr_b16 a[38:39], v138 offset:19200\n\t"
        "v_add_u32 v173, s71, v129\n\t"
        "v_mfma_f32_16x16x32_bf16 v[4:7], a[28:31], v[96:99], 0\n\t"
        "buffer_load_dwordx4 v141, s[24:27], s36 offen offset:1024 lds\n\t"
        "ds_read_b64_tr_b16 a[40:41], v139 offset:18432\n\t"
        "v_add_u32 v174, s71, v130\n\t"
        "v_mfma_f32_16x16x32_bf16 v[8:11], a[32:35], v[96:99], 0\n\t"
        "ds_read_b64_tr_b16 a[42:43], v139 offset:19200\n\t"
        "ds_read_b64_tr_b16 a[44:45], v149 offset:18432\n\t"
        "s_add_u32 s71, s71, s70\n\t"
        "v_add_u32 v175, s71, v128\n\t"
        "s_waitcnt lgkmcnt(7)\n\t"
        "v_mfma_f32_16x16x32_bf16 v[12:15], a[24:27], v[100:103], v[12:15]\n\t"
        "buffer_load_dwordx4 v142, s[24:27], s36 offen offset:2048 lds\n\t"
        "s_add_u32 m0, s37, 24576\n\t"
        "ds_read_b64_tr_b16 a[46:47], v149 offset:19200\n\t"
        "s_add_u32 s36, s36, s39\n\t"
        "ds_read_b64_tr_b16 a[48:49], v138 offset:24576\n\t"
        "v_mfma_f32_16x16x32_bf16 v[16:19], a[28:31], v[100:103], v[16:19]\n\t"
        "ds_read_b64_tr_b16 a[50:51], v138 offset:25344\n\t"
        "ds_read_b64_tr_b16 a[52:53], v139 offset:24576\n\t"
        "ds_read_b64_tr_b16 a[54:55], v139 offset:25344\n\t"
        "v_add_u32 v176, s71, v129\n\t"
        "v_add_u32 v177, s71, v130\n\t"
        "v_mfma_f32_16x16x32_bf16 v[20:23], a[32:35], v[100:103], v[20:23]\n\t"
        "ds_read_b64_tr_b16 a[56:57], v149 offset:24576\n\t"
        "ds_read_b64_tr_b16 a[58:59], v149 offset:25344\n\t"
        "ds_read_b128 v[108:111], v128 offset:4160\n\t"
        "s_add_u32 s71, s71, s70\n\t"
        "s_waitcnt lgkmcnt(11)\n\t"
        "v_mfma_f32_16x16x32_bf16 v[0:3], a[0:3], v[104:107], v[0:3]\n\t"
        "ds_read_b64_tr_b16 a[60:61], v138 offset:30720\n\t"
        "ds_read_b64_tr_b16 a[62:63], v138 offset:31488\n\t"
        "v_add_u32 v178, s71, v128\n\t"
        "v_add_u32 v179, s71, v129\n\t"
        "v_mfma_f32_16x16x32_bf16 v[4:7], a[4:7], v[104:107], v[4:7]\n\t"
        "ds_read_b64_tr_b16 a[64:65], v139 offset:30720\n\t"
        "ds_read_b64_tr_b16 a[66:67], v139 offset:31488\n\t"
        "v_add_u32 v180, s71, v130\n\t"
        "v_mfma_f32_16x16x32_bf16 v[8:11], a[8:11], v[104:107], v[8:11]\n\t"
        "ds_read_b64_tr_b16 a[68:69], v149 offset:30720\n\t"
        "ds_read_b64_tr_b16 a[70:71], v149 offset:31488\n\t"
        "v_add_f32 v78, v78, v56\n\t"
        "v_add_f32 v79, v79, v57\n\t"
        "v_mfma_f32_16x16x32_bf16 v[12:15], a[12:15], v[104:107], v[12:15]\n\t"
        "ds_read_b128 v[112:115], v128 offset:8256\n\t"
        "v_mfma_f32_16x16x32_bf16 v[16:19], a[16:19], v[104:107], v[16:19]\n\t"
        "s_cmp_eq_u32 s69, 36\n\t"
        "s_cselect_b64 exec, -1, 0\n\t"
        "v_swap_b32 v68, v69\n\t"
        "v_swap_b32 v173, v175\n\t"
        "v_swap_b32 v174, v178\n\t"
        "v_swap_b32 v177, v179\n\t"
        "s_mov_b64 exec, -1\n\t"
        "v_mfma_f32_16x16x32_bf16 v[20:23], a[20:23], v[104:107], v[20:23]\n\t"
        "ds_read_b128 v[84:87], v128 offset:12352\n\t"
        "v_add3_u32 v68, v68, v52, -1\n\t"
        "v_add3_u32 v69, v69, v158, -1\n\t"
        "s_waitcnt lgkmcnt(8)\n\t"
        "v_mfma_f32_16x16x32_bf16 v[0:3], a[36:39], v[108:111], v[0:3]\n\t"
        "ds_read_b64_tr_b16 a[72:73], v138 offset:36864\n\t"
        "ds_read_b64_tr_b16 a[74:75], v138 offset:37632\n\t"
        "v_cvt_f32_i32 v68, v68\n\t"
        "v_cvt_f32_i32 v69, v69\n\t"
        "v_mfma_f32_16x16x32_bf16 v[4:7], a[40:43], v[108:111], v[4:7]\n\t"
        "ds_read_b64_tr_b16 a[76:77], v139 offset:36864\n\t"
        "ds_read_b64_tr_b16 a[78:79], v139 offset:37632\n\t"
        "v_readlane_b32 s69, v155, 31\n\t"
        "v_mfma_f32_16x16x32_bf16 v[8:11], a[44:47], v[108:111], v[8:11]\n\t"
        "ds_read_b64_tr_b16 a[80:81], v149 offset:36864\n\t"
        "ds_read_b64_tr_b16 a[82:83], v149 offset:37632\n\t"
        "v_mov_b32 v154, s69\n\t"
        "v_mov_b32 v119, s62\n\t"
        "v_mfma_f32_16x16x32_bf16 v[12:15], a[48:51], v[108:111], v[12:15]\n\t"
        "v_readlane_b32 s53, v155, 28\n\t"
        "v_readlane_b32 s9, v155, 46\n\t"
        "v_mfma_f32_16x16x32_bf16 v[16:19], a[52:55], v[108:111], v[16:19]\n\t"
        "buffer_load_dwordx4 v140, s[24:27], s36 offen offset:0 lds\n\t"
        "s_sub_u32 s10, 43200, s11\n\t"
        "s_mul_i32 s10, s10, s68\n\t"
        "v_mfma_f32_16x16x32_bf16 v[20:23], a[56:59], v[108:111], v[20:23]\n\t"
        "s_sub_u32 s11, s11, 192\n\t"
        "v_mad_u32_u24 v148, v158, s11, v144\n\t"
        "s_waitcnt lgkmcnt(7)\n\t"
        "v_mfma_f32_16x16x32_bf16 v[0:3], a[60:63], v[112:115], v[0:3]\n\t"
        "v_add_u32 v148, s10, v148\n\t"
        "v_mad_u32_u24 v150, v83, s11, v145\n\t"
        "s_lshl_b32 s70, s49, 1\n\t"
        "v_mfma_f32_16x16x32_bf16 v[4:7], a[64:67], v[112:115], v[4:7]\n\t"
        "s_add_u32 s70, s70, 49\n\t"
        "v_readlane_b32 s52, v155, s70\n\t"
        "v_mfma_f32_16x16x32_bf16 v[8:11], a[68:71], v[112:115], v[8:11]\n\t"
        "buffer_load_dwordx4 v141, s[24:27], s36 offen offset:1024 lds\n\t"
        "s_add_u32 s52, s52, s10\n\t"
        "v_mfma_f32_16x16x32_bf16 v[12:15], a[36:39], v[112:115], v[12:15]\n\t"
        "v_mfma_f32_16x16x32_bf16 v[16:19], a[40:43], v[112:115], v[16:19]\n\t"
        "v_mfma_f32_16x16x32_bf16 v[20:23], a[44:47], v[112:115], v[20:23]\n\t"
        "v_mfma_f32_16x16x32_bf16 v[24:27], a[48:51], v[112:115], v[24:27]\n\t"
        "buffer_load_dwordx4 v142, s[24:27], s36 offen offset:2048 lds\n\t"
        "v_mfma_f32_16x16x32_bf16 v[28:31], a[52:55], v[112:115], v[28:31]\n\t"
        "v_mfma_f32_16x16x32_bf16 v[32:35], a[56:59], v[112:115], v[32:35]\n\t"
        "s_waitcnt lgkmcnt(0)\n\t"
        "v_mfma_f32_16x16x32_bf16 v[12:15], a[60:63], v[84:87], v[12:15]\n\t"
        "v_mfma_f32_16x16x32_bf16 v[16:19], a[64:67], v[84:87], v[16:19]\n\t"
        "buffer_load_dwordx4 v[48:51], v55, s[20:23], s48 offen\n\t"
        "v_mfma_f32_16x16x32_bf16 v[20:23], a[68:71], v[84:87], v[20:23]\n\t"
        "v_mfma_f32_16x16x32_bf16 v[24:27], a[36:39], v[84:87], v[24:27]\n\t"
        "ds_read_b128 v[88:91], v128 offset:4224\n\t"
        "v_mfma_f32_16x16x32_bf16 v[28:31], a[40:43], v[84:87], v[28:31]\n\t"
        "v_mfma_f32_16x16x32_bf16 v[32:35], a[44:47], v[84:87], v[32:35]\n\t"
        "buffer_load_dword v61, v61, s[20:23], s48 offen offset:512\n\t"
        "v_readlane_b32 s40, v155, 12\n\t"
        "v_readlane_b32 s43, v155, 0\n\t"
        "v_mfma_f32_16x16x32_bf16 v[36:39], a[48:51], v[84:87], v[36:39]\n\t"
        "ds_read_b128 v[92:95], v128 offset:8320\n\t"
        "s_add_u32 m0, s40, s35\n\t"
        "v_mfma_f32_16x16x32_bf16 v[40:43], a[52:55], v[84:87], v[40:43]\n\t"
        "v_mfma_f32_16x16x32_bf16 v[44:47], a[56:59], v[84:87], v[44:47]\n\t"
        "s_waitcnt lgkmcnt(1)\n\t"
        "v_mfma_f32_16x16x32_bf16 v[0:3], a[72:75], v[88:91], v[0:3]\n\t"
        "s_waitcnt vmcnt(10)\n\t"
        "s_barrier\n\t"
        "buffer_load_dwordx4 v134, s[16:19], s43 offen  lds\n\t"
        "ds_read_b128 v[96:99], v128 offset:16384\n\t"
        "v_mfma_f32_16x16x32_bf16 v[4:7], a[76:79], v[88:91], v[4:7]\n\t"
        "ds_read_b128 v[100:103], v128 offset:16448\n\t"
        "ds_read_b64_tr_b16 a[84:85], v131 offset:0\n\t"
        "v_readlane_b32 s40, v155, 13\n\t"
        "v_readlane_b32 s43, v155, 1\n\t"
        "v_mfma_f32_16x16x32_bf16 v[8:11], a[80:83], v[88:91], v[8:11]\n\t"
        "ds_read_b64_tr_b16 a[86:87], v131 offset:768\n\t"
        "s_add_u32 m0, s40, s35\n\t"
        "ds_read_b64_tr_b16 a[88:89], v132 offset:0\n\t"
        "s_waitcnt lgkmcnt(5)\n\t"
        "v_mfma_f32_16x16x32_bf16 v[12:15], a[72:75], v[92:95], v[12:15]\n\t"
        "ds_read_b64_tr_b16 a[90:91], v132 offset:768\n\t"
        "ds_read_b64_tr_b16 a[92:93], v133 offset:0\n\t"
        "v_readlane_b32 s46, v155, 37\n\t"
        "v_readlane_b32 s47, v155, 40\n\t"
        "v_mfma_f32_16x16x32_bf16 v[16:19], a[76:79], v[92:95], v[16:19]\n\t"
        "buffer_load_dwordx4 v135, s[16:19], s43 offen  lds\n\t"
        "ds_read_b64_tr_b16 a[94:95], v133 offset:768\n\t"
        "v_readlane_b32 s40, v155, 14\n\t"
        "v_readlane_b32 s43, v155, 2\n\t"
        "v_mfma_f32_16x16x32_bf16 v[20:23], a[80:83], v[92:95], v[20:23]\n\t"
        "ds_read_b128 v[104:107], v128 offset:4224\n\t"
        "s_add_u32 m0, s40, s35\n\t"
        "ds_read_b64_tr_b16 a[96:97], v131 offset:6144\n\t"
        "s_waitcnt lgkmcnt(9)\n\t"
        "v_mfma_f32_16x16x32_bf16 v[24:27], a[24:27], v[96:99], v[24:27]\n\t"
        "ds_read_b64_tr_b16 a[98:99], v131 offset:6912\n\t"
        "ds_read_b64_tr_b16 a[100:101], v132 offset:6144\n\t"
        "v_add_f32 v58, s46, v68\n\t"
        "v_add_f32 v59, s47, v69\n\t"
        "v_mfma_f32_16x16x32_bf16 v[28:31], a[28:31], v[96:99], v[28:31]\n\t"
        "ds_read_b64_tr_b16 a[102:103], v132 offset:6912\n\t"
        "ds_read_b64_tr_b16 a[104:105], v133 offset:6144\n\t"
        "v_readlane_b32 s47, v155, 42\n\t"
        "v_mfma_f32_16x16x32_bf16 v[32:35], a[32:35], v[96:99], v[32:35]\n\t"
        "buffer_load_dwordx4 v134, s[16:19], s43 offen  lds\n\t"
        "ds_read_b64_tr_b16 a[106:107], v133 offset:6912\n\t"
        "v_readlane_b32 s40, v155, 15\n\t"
        "v_readlane_b32 s43, v155, 3\n\t"
        "v_mfma_f32_16x16x32_bf16 v[36:39], a[0:3], v[96:99], v[36:39]\n\t"
        "ds_read_b128 v[108:111], v128 offset:8320\n\t"
        "s_add_u32 m0, s40, s35\n\t"
        "v_mfma_f32_16x16x32_bf16 v[40:43], a[4:7], v[96:99], v[40:43]\n\t"
        "ds_read_b32 v64, v70 offset:0\n\t"
        "v_mfma_f32_16x16x32_bf16 v[44:47], a[8:11], v[96:99], v[44:47]\n\t"
        "ds_read_b128 v[112:115], v128 offset:12416\n\t"
        "s_waitcnt lgkmcnt(10)\n\t"
        "v_mfma_f32_16x16x32_bf16 v[24:27], a[60:63], v[100:103], v[24:27]\n\t"
        "ds_read_b32 v67, v67 offset:0\n\t"
        "v_mfma_f32_16x16x32_bf16 v[28:31], a[64:67], v[100:103], v[28:31]\n\t"
        "ds_read_b32 v66, v70 offset:2560\n\t"
        "v_mfma_f32_16x16x32_bf16 v[32:35], a[68:71], v[100:103], v[32:35]\n\t"
        "buffer_load_dwordx4 v135, s[16:19], s43 offen  lds\n\t"
        "s_add_u32 m0, s37, 36864\n\t"
        "s_add_u32 s36, s36, s38\n\t"
        "v_mfma_f32_16x16x32_bf16 v[36:39], a[36:39], v[100:103], v[36:39]\n\t"
        "ds_read_b128 v[84:87], v128 offset:16512\n\t"
        "v_mfma_f32_16x16x32_bf16 v[40:43], a[40:43], v[100:103], v[40:43]\n\t"
        "v_mfma_f32_16x16x32_bf16 v[44:47], a[44:47], v[100:103], v[44:47]\n\t"
        "s_waitcnt lgkmcnt(7)\n\t"
        "v_mfma_f32_16x16x32_bf16 v[12:15], a[84:87], v[104:107], v[12:15]\n\t"
        "buffer_load_dwordx4 v140, s[24:27], s36 offen offset:0 lds\n\t"
        "v_mfma_f32_16x16x32_bf16 v[16:19], a[88:91], v[104:107], v[16:19]\n\t"
        "s_waitcnt lgkmcnt(4)\n\t"
        "v_perm_b32 v56, v64, v64, s44\n\t"
        "v_perm_b32 v57, v64, v64, s45\n\t"
        "v_add_f32 v56, v56, v58\n\t"
        "v_mfma_f32_16x16x32_bf16 v[20:23], a[92:95], v[104:107], v[20:23]\n\t"
        "v_add_f32 v57, v57, v59\n\t"
        "v_cvt_pk_bf16_f32 v52, v56, v57\n\t"
        "v_cvt_pk_bf16_f32 v58, v58, v59\n\t"
        "v_mfma_f32_16x16x32_bf16 v[0:3], a[96:99], v[108:111], v[0:3]\n\t"
        "v_cmp_ne_u32 s[64:65], v52, v58\n\t"
        "s_waitcnt lgkmcnt(2)\n\t"
        "v_perm_b32 v57, v67, v67, s45\n\t"
        "v_add_f32 v57, v57, v79\n\t"
        "v_mfma_f32_16x16x32_bf16 v[4:7], a[100:103], v[108:111], v[4:7]\n\t"
        "buffer_load_dwordx4 v141, s[24:27], s36 offen offset:1024 lds\n\t"
        "v_mbcnt_lo_u32_b32 v56, s64, 1\n\t"
        "v_mfma_f32_16x16x32_bf16 v[8:11], a[104:107], v[108:111], v[8:11]\n\t"
        "v_mbcnt_hi_u32_b32 v56, s65, v56\n\t"
        "s_bcnt1_i32_b64 s61, s[64:65]\n\t"
        "v_mfma_f32_16x16x32_bf16 v[24:27], a[84:87], v[108:111], v[24:27]\n\t"
        "v_add_u32 v54, s61, 1\n\t"
        "v_lshl_add_u32 v56, v56, 3, s60\n\t"
        "v_cvt_pk_bf16_f32 v58, v78, v79\n\t"
        "v_mfma_f32_16x16x32_bf16 v[28:31], a[88:91], v[108:111], v[28:31]\n\t"
        "s_mov_b64 exec, s[64:65]\n\t"
        "ds_write_b64 v56, v[52:53]\n\t"
        "s_mov_b64 exec, -1\n\t"
        "v_mfma_f32_16x16x32_bf16 v[32:35], a[92:95], v[108:111], v[32:35]\n\t"
        "s_waitcnt vmcnt(8) lgkmcnt(1)\n\t"
        "s_barrier\n\t"
        "buffer_load_dwordx4 v142, s[24:27], s36 offen offset:2048 lds\n\t"
        "s_add_u32 m0, s37, 49152\n\t"
        "ds_read_b128 v[88:91], v128 offset:0\n\t"
        "v_mfma_f32_16x16x32_bf16 v[12:15], a[96:99], v[112:115], v[12:15]\n\t"
        "ds_read_b128 v[92:95], v128 offset:64\n\t"
        "s_add_u32 s36, s36, s5\n\t"
        "ds_read_b128 v[96:99], v128 offset:128\n\t"
        "v_mfma_f32_16x16x32_bf16 v[16:19], a[100:103], v[112:115], v[16:19]\n\t"
        "ds_read_b64_tr_b16 a[108:109], v131 offset:12288\n\t"
        "ds_read_b64_tr_b16 a[110:111], v131 offset:13056\n\t"
        "v_swap_b32 v53, v80\n\t"
        "v_perm_b32 v56, v67, v67, s44\n\t"
        "v_mfma_f32_16x16x32_bf16 v[20:23], a[104:107], v[112:115], v[20:23]\n\t"
        "ds_read_b64_tr_b16 a[112:113], v132 offset:12288\n\t"
        "ds_read_b64_tr_b16 a[114:115], v132 offset:13056\n\t"
        "v_add_f32 v56, v56, v78\n\t"
        "v_cvt_pk_bf16_f32 v52, v56, v57\n\t"
        "v_mfma_f32_16x16x32_bf16 v[24:27], a[72:75], v[112:115], v[24:27]\n\t"
        "buffer_load_dwordx4 v140, s[24:27], s36 offen offset:0 lds\n\t"
        "ds_read_b128 v[100:103], v128 offset:20480\n\t"
        "v_cmp_ne_u32 s[64:65], v52, v58\n\t"
        "v_mfma_f32_16x16x32_bf16 v[28:31], a[76:79], v[112:115], v[28:31]\n\t"
        "ds_read_b64_tr_b16 a[116:117], v133 offset:12288\n\t"
        "ds_read_b64_tr_b16 a[118:119], v133 offset:13056\n\t"
        "s_and_b64 s[64:65], s[64:65], 0xffffff\n\t"
        "v_mbcnt_lo_u32_b32 v56, s64, v54\n\t"
        "v_mfma_f32_16x16x32_bf16 v[32:35], a[80:83], v[112:115], v[32:35]\n\t"
        "ds_read_b64_tr_b16 a[120:121], v131 offset:18432\n\t"
        "ds_read_b64_tr_b16 a[122:123], v131 offset:19200\n\t"
        "s_bcnt1_i32_b64 s46, s[64:65]\n\t"
        "v_mfma_f32_16x16x32_bf16 v[36:39], a[84:87], v[112:115], v[36:39]\n\t"
        "ds_read_b128 v[104:107], v128 offset:20544\n\t"
        "ds_read_b64_tr_b16 a[124:125], v132 offset:18432\n\t"
        "s_add_u32 s61, s61, s46\n\t"
        "v_add_u32 v54, s46, v54\n\t"
        "v_mfma_f32_16x16x32_bf16 v[40:43], a[88:91], v[112:115], v[40:43]\n\t"
        "buffer_load_dwordx4 v141, s[24:27], s36 offen offset:1024 lds\n\t"
        "ds_read_b64_tr_b16 a[126:127], v132 offset:19200\n\t"
        "s_mov_b32 s3, s61\n\t"
        "v_mfma_f32_16x16x32_bf16 v[44:47], a[92:95], v[112:115], v[44:47]\n\t"
        "ds_read_b64_tr_b16 a[128:129], v133 offset:18432\n\t"
        "ds_read_b64_tr_b16 a[130:131], v133 offset:19200\n\t"
        "v_lshl_add_u32 v56, v56, 3, s60\n\t"
        "v_readlane_b32 s46, v155, 39\n\t"
        "v_mfma_f32_16x16x32_bf16 v[24:27], a[96:99], v[84:87], v[24:27]\n\t"
        "s_mov_b64 exec, s[64:65]\n\t"
        "ds_write_b64 v56, v[52:53]\n\t"
        "s_mov_b64 exec, -1\n\t"
        "ds_read_b128 v[108:111], v129 offset:0\n\t"
        "v_mfma_f32_16x16x32_bf16 v[28:31], a[100:103], v[84:87], v[28:31]\n\t"
        "ds_read_b64_tr_b16 a[132:133], v131 offset:24576\n\t"
        "ds_read_b64_tr_b16 a[134:135], v131 offset:25344\n\t"
        "v_add_u32 v53, 4096, v80\n\t"
        "v_perm_b32 v56, v66, v66, s44\n\t"
        "v_mfma_f32_16x16x32_bf16 v[32:35], a[104:107], v[84:87], v[32:35]\n\t"
        "buffer_load_dwordx4 v142, s[24:27], s36 offen offset:2048 lds\n\t"
        "ds_read_b64_tr_b16 a[136:137], v132 offset:24576\n\t"
        "v_readlane_b32 s40, v155, 16\n\t"
        "v_mfma_f32_16x16x32_bf16 v[36:39], a[72:75], v[84:87], v[36:39]\n\t"
        "ds_read_b128 v[112:115], v129 offset:4096\n\t"
        "ds_read_b64_tr_b16 a[138:139], v132 offset:25344\n\t"
        "v_readlane_b32 s43, v155, 4\n\t"
        "s_add_u32 m0, s40, s35\n\t"
        "v_mfma_f32_16x16x32_bf16 v[40:43], a[76:79], v[84:87], v[40:43]\n\t"
        "ds_read_b64_tr_b16 a[140:141], v133 offset:24576\n\t"
        "ds_read_b64_tr_b16 a[142:143], v133 offset:25344\n\t"
        "v_perm_b32 v57, v66, v66, s45\n\t"
        "v_mfma_f32_16x16x32_bf16 v[44:47], a[80:83], v[84:87], v[44:47]\n\t"
        "ds_read_b64_tr_b16 a[144:145], v131 offset:30720\n\t"
        "ds_read_b64_tr_b16 a[146:147], v131 offset:31488\n\t"
        "v_add_f32 v58, s46, v68\n\t"
        "s_waitcnt lgkmcnt(11)\n\t"
        "v_mfma_f32_16x16x32_bf16 v[0:3], a[12:15], v[88:91], v[0:3]\n\t"
        "buffer_load_dwordx4 v134, s[16:19], s43 offen  lds\n\t"
        "ds_read_b64_tr_b16 a[148:149], v132 offset:30720\n\t"
        "v_readlane_b32 s40, v155, 17\n\t"
        "v_readlane_b32 s43, v155, 5\n\t"
        "v_mfma_f32_16x16x32_bf16 v[4:7], a[16:19], v[88:91], v[4:7]\n\t"
        "ds_read_b64_tr_b16 a[150:151], v132 offset:31488\n\t"
        "s_add_u32 m0, s40, s35\n\t"
        "ds_read_b64_tr_b16 a[152:153], v133 offset:30720\n\t"
        "v_mfma_f32_16x16x32_bf16 v[8:11], a[20:23], v[88:91], v[8:11]\n\t"
        "ds_read_b64_tr_b16 a[154:155], v133 offset:31488\n\t"
        "v_add_f32 v59, s47, v69\n\t"
        "v_add_f32 v56, v56, v58\n\t"
        "v_mfma_f32_16x16x32_bf16 v[0:3], a[48:51], v[92:95], v[0:3]\n\t"
        "ds_read_b128 v[84:87], v129 offset:8192\n\t"
        "v_add_f32 v57, v57, v59\n\t"
        "v_mfma_f32_16x16x32_bf16 v[4:7], a[52:55], v[92:95], v[4:7]\n\t"
        "buffer_load_dwordx4 v135, s[16:19], s43 offen  lds\n\t"
        "v_readlane_b32 s40, v155, 18\n\t"
        "v_readlane_b32 s43, v155, 6\n\t"
        "v_mfma_f32_16x16x32_bf16 v[8:11], a[56:59], v[92:95], v[8:11]\n\t"
        "s_add_u32 m0, s40, s35\n\t"
        "v_max_f32 v56, 0, v56\n\t"
        "v_mfma_f32_16x16x32_bf16 v[0:3], a[84:87], v[96:99], v[0:3]\n\t"
        "v_max_f32 v57, 0, v57\n\t"
        "v_max_f32 v58, 0, v58\n\t"
        "v_mfma_f32_16x16x32_bf16 v[4:7], a[88:91], v[96:99], v[4:7]\n\t"
        "v_max_f32 v59, 0, v59\n\t"
        "v_cvt_pk_bf16_f32 v52, v56, v57\n\t"
        "v_cvt_pk_bf16_f32 v58, v58, v59\n\t"
        "v_mfma_f32_16x16x32_bf16 v[8:11], a[92:95], v[96:99], v[8:11]\n\t"
        "buffer_load_dwordx4 v136, s[16:19], s43 offen  lds\n\t"
        ".LpY0%=:\n\t"
        "s_cbranch_execz .LrY%=\n\t"
        "v_readlane_b32 s40, v155, 19\n\t"
        "v_mfma_f32_16x16x32_bf16 v[36:39], a[24:27], v[100:103], v[36:39]\n\t"
        "v_readlane_b32 s43, v155, 7\n\t"
        "s_add_u32 m0, s40, s35\n\t"
        "v_cmp_ne_u32 s[64:65], v52, v58\n\t"
        "v_mfma_f32_16x16x32_bf16 v[40:43], a[28:31], v[100:103], v[40:43]\n\t"
        "s_nop 0\n\t"
        "v_mbcnt_lo_u32_b32 v56, s64, v54\n\t"
        "v_mbcnt_hi_u32_b32 v56, s65, v56\n\t"
        "v_mfma_f32_16x16x32_bf16 v[44:47], a[32:35], v[100:103], v[44:47]\n\t"
        "s_bcnt1_i32_b64 s46, s[64:65]\n\t"
        "s_add_u32 s61, s61, s46\n\t"
        "v_mfma_f32_16x16x32_bf16 v[36:39], a[60:63], v[104:107], v[36:39]\n\t"
        "buffer_load_dwordx4 v137, s[16:19], s43 offen  lds\n\t"
        "s_add_u32 m0, s37, 0\n\t"
        "ds_read_b128 v[88:91], v129 offset:12288\n\t"
        "v_mfma_f32_16x16x32_bf16 v[40:43], a[64:67], v[104:107], v[40:43]\n\t"
        "s_add_u32 s36, s36, s39\n\t"
        "s_waitcnt vmcnt(15)\n\t"
        "v_and_b32 v48, s54, v48\n\t"
        "v_pk_max_u16 v60, v154, v48\n\t"
        "v_mfma_f32_16x16x32_bf16 v[44:47], a[68:71], v[104:107], v[44:47]\n\t"
        "v_and_b32 v49, s54, v49\n\t"
        "v_pk_max_u16 v60, v60, v49\n\t"
        "v_and_b32 v50, s54, v50\n\t"
        "s_waitcnt lgkmcnt(1)\n\t"
        "v_mfma_f32_16x16x32_bf16 v[0:3], a[120:123], v[108:111], v[0:3]\n\t"
        "v_pk_max_u16 v60, v60, v50\n\t"
        "v_and_b32 v51, s54, v51\n\t"
        "v_mfma_f32_16x16x32_bf16 v[4:7], a[124:127], v[108:111], v[4:7]\n\t"
        "buffer_load_dwordx4 v140, s[24:27], s36 offen offset:0 lds\n\t"
        "v_pk_max_u16 v60, v60, v51\n\t"
        "v_mfma_f32_16x16x32_bf16 v[8:11], a[128:131], v[108:111], v[8:11]\n\t"
        "s_waitcnt vmcnt(15)\n\t"
        "v_and_b32 v61, s54, v61\n\t"
        "v_pk_max_u16 v60, v60, v61\n\t"
        "v_cmp_ne_u32 s[12:13], v60, v154\n\t"
        "v_mfma_f32_16x16x32_bf16 v[0:3], a[108:111], v[112:115], v[0:3]\n\t"
        "s_lshl_b32 s6, 256, s4\n\t"
        "v_min_u32 v56, 97, v56\n\t"
        "v_mfma_f32_16x16x32_bf16 v[4:7], a[112:115], v[112:115], v[4:7]\n\t"
        "s_cmp_lg_u64 s[12:13], 0\n\t"
        "s_cselect_b32 s6, s6, 0\n\t"
        "v_mfma_f32_16x16x32_bf16 v[8:11], a[116:119], v[112:115], v[8:11]\n\t"
        "buffer_load_dwordx4 v141, s[24:27], s36 offen offset:1024 lds\n\t"
        "v_lshl_add_u32 v56, v56, 3, s60\n\t"
        "v_mfma_f32_16x16x32_bf16 v[12:15], a[120:123], v[112:115], v[12:15]\n\t"
        "s_mov_b64 exec, s[64:65]\n\t"
        "ds_write_b64 v56, v[52:53]\n\t"
        "s_mov_b64 exec, -1\n\t"
        "v_mfma_f32_16x16x32_bf16 v[16:19], a[124:127], v[112:115], v[16:19]\n\t"
        "s_cmp_gt_u32 s61, s53\n\t"
        "s_cselect_b32 s8, 1, 0\n\t"
        "s_or_b32 s56, s56, s8\n\t"
        "v_mfma_f32_16x16x32_bf16 v[20:23], a[128:131], v[112:115], v[20:23]\n\t"
        "s_cmp_gt_u32 s3, 64\n\t"
        "s_cselect_b32 s8, 1, 0\n\t"
        "s_or_b32 s56, s56, s8\n\t"
        "v_mfma_f32_16x16x32_bf16 v[0:3], a[132:135], v[84:87], v[0:3]\n\t"
        "s_waitcnt vmcnt(9)\n\t"
        "s_barrier\n\t"
        "buffer_load_dwordx4 v142, s[24:27], s36 offen offset:2048 lds\n\t"
        "s_add_u32 m0, s37, 12288\n\t"
        "ds_read_b128 v[92:95], v129 offset:16384\n\t"
        "v_mfma_f32_16x16x32_bf16 v[4:7], a[136:139], v[84:87], v[4:7]\n\t"
        "ds_read_b64_tr_b16 a[156:157], v131 offset:36864\n\t"
        "s_add_u32 s36, s36, s38\n\t"
        "ds_read_b64_tr_b16 a[158:159], v131 offset:37632\n\t"
        "v_mfma_f32_16x16x32_bf16 v[8:11], a[140:143], v[84:87], v[8:11]\n\t"
        "ds_read_b64_tr_b16 a[160:161], v132 offset:36864\n\t"
        "ds_read_b64_tr_b16 a[162:163], v132 offset:37632\n\t"
        "s_min_u32 s61, s61, 80\n\t"
        "v_min_u32 v78, s61, v116\n\t"
        "v_mfma_f32_16x16x32_bf16 v[12:15], a[108:111], v[84:87], v[12:15]\n\t"
        "ds_read_b128 v[96:99], v128 offset:20608\n\t"
        "ds_read_b64_tr_b16 a[164:165], v133 offset:36864\n\t"
        "v_lshl_add_u32 v78, v78, 3, s60\n\t"
        "v_mfma_f32_16x16x32_bf16 v[16:19], a[112:115], v[84:87], v[16:19]\n\t"
        "ds_read_b64 v[62:63], v78\n\t"
        "buffer_load_dwordx4 v140, s[24:27], s36 offen offset:0 lds\n\t"
        "v_cmp_ge_u32 s[66:67], s61, v116\n\t"
        "v_mfma_f32_16x16x32_bf16 v[20:23], a[116:119], v[84:87], v[20:23]\n\t"
        "ds_read_b64_tr_b16 a[166:167], v133 offset:37632\n\t"
        "ds_read_b64_tr_b16 a[168:169], v131 offset:43008\n\t"
        "s_mov_b32 s46, 0x07060302\n\t"
        "v_mfma_f32_16x16x32_bf16 v[24:27], a[120:123], v[84:87], v[24:27]\n\t"
        "ds_read_b128 v[100:103], v129 offset:64\n\t"
        "ds_read_b64_tr_b16 a[170:171], v131 offset:43776\n\t"
        "v_mfma_f32_16x16x32_bf16 v[28:31], a[124:127], v[84:87], v[28:31]\n\t"
        "ds_read_b64_tr_b16 a[172:173], v132 offset:43008\n\t"
        "ds_read_b64_tr_b16 a[174:175], v132 offset:43776\n\t"
        "v_mfma_f32_16x16x32_bf16 v[32:35], a[128:131], v[84:87], v[32:35]\n\t"
        "s_waitcnt vmcnt(8)\n\t"
        "s_barrier\n\t"
        "buffer_load_dwordx4 v141, s[24:27], s36 offen offset:1024 lds\n\t"
        "ds_read_b64_tr_b16 a[176:177], v133 offset:43008\n\t"
        "s_waitcnt lgkmcnt(11)\n\t"
        "v_mfma_f32_16x16x32_bf16 v[12:15], a[132:135], v[88:91], v[12:15]\n\t"
        "ds_read_b128 v[104:107], v129 offset:4160\n\t"
        "ds_read_b64_tr_b16 a[178:179], v133 offset:43776\n\t"
        "v_mfma_f32_16x16x32_bf16 v[16:19], a[136:139], v[88:91], v[16:19]\n\t"
        "ds_read_b64_tr_b16 a[180:181], v131 offset:49152\n\t"
        "ds_read_b64_tr_b16 a[182:183], v131 offset:49920\n\t"
        "v_mfma_f32_16x16x32_bf16 v[20:23], a[140:143], v[88:91], v[20:23]\n\t"
        "ds_read_b64_tr_b16 a[184:185], v132 offset:49152\n\t"
        "ds_read_b64_tr_b16 a[186:187], v132 offset:49920\n\t"
        "v_mfma_f32_16x16x32_bf16 v[24:27], a[108:111], v[88:91], v[24:27]\n\t"
        "buffer_load_dwordx4 v142, s[24:27], s36 offen offset:2048 lds\n\t"
        "s_add_u32 m0, s37, 24576\n\t"
        "ds_read_b64_tr_b16 a[188:189], v133 offset:49152\n\t"
        "v_mfma_f32_16x16x32_bf16 v[28:31], a[112:115], v[88:91], v[28:31]\n\t"
        "s_mov_b64 exec, s[66:67]\n\t"
        "s_waitcnt lgkmcnt(12)\n\t"
        "ds_write_b32 v63, v117\n\t"
        "s_mov_b64 exec, -1\n\t"
        "s_add_u32 s36, s36, s5\n\t"
        "ds_read_b64_tr_b16 a[190:191], v133 offset:49920\n\t"
        "v_mfma_f32_16x16x32_bf16 v[32:35], a[116:119], v[88:91], v[32:35]\n\t"
        "ds_read_b64_tr_b16 a[192:193], v131 offset:55296\n\t"
        "ds_read_b64_tr_b16 a[194:195], v131 offset:56064\n\t"
        "v_and_b32 v78, 0xfffff800, v63\n\t"
        "v_lshlrev_b32 v72, 16, v62\n\t"
        "v_mfma_f32_16x16x32_bf16 v[36:39], a[120:123], v[88:91], v[36:39]\n\t"
        "ds_read_b128 v[64:67], v78 offset:1536\n\t"
        "ds_read_b128 v[108:111], v129 offset:8256\n\t"
        "v_and_b32 v73, 0xffff0000, v62\n\t"
        "v_med3_f32 v72, v72, 0, s63\n\t"
        "v_mfma_f32_16x16x32_bf16 v[40:43], a[124:127], v[88:91], v[40:43]\n\t"
        "buffer_load_dwordx4 v140, s[24:27], s36 offen offset:0 lds\n\t"
        "ds_read_b64_tr_b16 a[196:197], v132 offset:55296\n\t"
        "v_med3_f32 v73, v73, 0, s63\n\t"
        "v_mfma_f32_16x16x32_bf16 v[44:47], a[128:131], v[88:91], v[44:47]\n\t"
        "ds_read_b64_tr_b16 a[198:199], v132 offset:56064\n\t"
        "ds_read_b64_tr_b16 a[200:201], v133 offset:55296\n\t"
        "v_perm_b32 v62, v73, v72, s46\n\t"
        "v_mfma_f32_16x16x32_bf16 v[24:27], a[132:135], v[92:95], v[24:27]\n\t"
        "ds_read_b64_tr_b16 a[202:203], v133 offset:56064\n\t"
        "v_fract_f32 v74, v72\n\t"
        "v_fract_f32 v75, v73\n\t"
        "v_mfma_f32_16x16x32_bf16 v[28:31], a[136:139], v[92:95], v[28:31]\n\t"
        "v_cvt_u32_f32 v76, v72\n\t"
        "v_cvt_u32_f32 v77, v73\n\t"
        "v_mfma_f32_16x16x32_bf16 v[32:35], a[140:143], v[92:95], v[32:35]\n\t"
        "v_cmp_neq_f32 s[64:65], 0, v74\n\t"
        "v_cmp_neq_f32 s[44:45], 0, v75\n\t"
        "v_mfma_f32_16x16x32_bf16 v[36:39], a[108:111], v[92:95], v[36:39]\n\t"
        "v_sub_f32 v78, 1.0, v74\n\t"
        "v_sub_f32 v79, 1.0, v75\n\t"
        "v_cvt_pk_bf16_f32 v79, v78, v79\n\t"
        "v_mfma_f32_16x16x32_bf16 v[40:43], a[112:115], v[92:95], v[40:43]\n\t"
        "v_lshlrev_b32 v78, 16, v79\n\t"
        "v_and_b32 v79, 0xffff0000, v79\n\t"
        "v_mfma_f32_16x16x32_bf16 v[44:47], a[116:119], v[92:95], v[44:47]\n\t"
        "buffer_load_dwordx4 v141, s[24:27], s36 offen offset:1024 lds\n\t"
        "v_mul_f32 v80, v78, v79\n\t"
        "v_mfma_f32_16x16x32_bf16 v[36:39], a[96:99], v[96:99], v[36:39]\n\t"
        "ds_read_b128 v[112:115], v129 offset:128\n\t"
        "v_mul_f32 v78, v78, v75\n\t"
        "v_mul_f32 v79, v74, v79\n\t"
        "v_mfma_f32_16x16x32_bf16 v[40:43], a[100:103], v[96:99], v[40:43]\n\t"
        "v_mul_f32 v81, v74, v75\n\t"
        "v_cndmask_b32 v78, v79, v78, s[44:45]\n\t"
        "v_mfma_f32_16x16x32_bf16 v[44:47], a[104:107], v[96:99], v[44:47]\n\t"
        "v_cndmask_b32 v79, 0, v79, s[44:45]\n\t"
        "v_cvt_pk_bf16_f32 v70, v80, v78\n\t"
        "s_waitcnt lgkmcnt(2)\n\t"
        "v_mfma_f32_16x16x32_bf16 v[0:3], a[156:159], v[100:103], v[0:3]\n\t"
        "buffer_load_dwordx4 v142, s[24:27], s36 offen offset:2048 lds\n\t"
        "s_add_u32 m0, s37, 36864\n\t"
        "ds_read_b128 v[84:87], v129 offset:4224\n\t"
        "v_mfma_f32_16x16x32_bf16 v[4:7], a[160:163], v[100:103], v[4:7]\n\t"
        "s_add_u32 s36, s36, s39\n\t"
        "v_cvt_pk_bf16_f32 v71, v79, v81\n\t"
        "v_pk_max_u16 v78, v62, v64\n\t"
        "v_mfma_f32_16x16x32_bf16 v[8:11], a[164:167], v[100:103], v[8:11]\n\t"
        "v_pk_min_u16 v78, v78, v65\n\t"
        "v_cmp_ne_u32 vcc, v78, v62\n\t"
        "v_mfma_f32_16x16x32_bf16 v[0:3], a[144:147], v[104:107], v[0:3]\n\t"
        "v_sub_u32 v78, v77, v67\n\t"
        "v_mad_u32_u24 v79, v77, 6, v66\n\t"
        "v_mfma_f32_16x16x32_bf16 v[4:7], a[148:151], v[104:107], v[4:7]\n\t"
        "v_mad_u32_u24 v79, v76, s62, v79\n\t"
        "s_and_b64 vcc, vcc, s[66:67]\n\t"
        "v_mfma_f32_16x16x32_bf16 v[8:11], a[152:155], v[104:107], v[8:11]\n\t"
        "s_waitcnt vmcnt(6) lgkmcnt(2)\n\t"
        "s_barrier\n\t"
        "buffer_load_dwordx4 v140, s[24:27], s36 offen offset:0 lds\n\t"
        "ds_read_b64_tr_b16 a[204:205], v131 offset:0\n\t"
        "v_mfma_f32_16x16x32_bf16 v[12:15], a[156:159], v[104:107], v[12:15]\n\t"
        "ds_read_b128 v[88:91], v129 offset:12352\n\t"
        "ds_read_b64_tr_b16 a[206:207], v131 offset:768\n\t"
        "s_or_b64 s[56:57], s[56:57], vcc\n\t"
        "v_bfe_u32 v80, v78, 2, 1\n\t"
        "v_mfma_f32_16x16x32_bf16 v[16:19], a[160:163], v[104:107], v[16:19]\n\t"
        "ds_read_b64_tr_b16 a[208:209], v132 offset:0\n\t"
        "ds_read_b64_tr_b16 a[210:211], v132 offset:768\n\t"
        "v_or_b32 v80, v80, v79\n\t"
        "v_mfma_f32_16x16x32_bf16 v[20:23], a[164:167], v[104:107], v[20:23]\n\t"
        "ds_read_b64_tr_b16 a[212:213], v133 offset:0\n\t"
        "ds_read_b64_tr_b16 a[214:215], v133 offset:768\n\t"
        "v_add_u32 v78, 1, v78\n\t"
        "v_mfma_f32_16x16x32_bf16 v[0:3], a[168:171], v[108:111], v[0:3]\n\t"
        "buffer_load_dwordx4 v141, s[24:27], s36 offen offset:1024 lds\n\t"
        "ds_read_b64_tr_b16 a[216:217], v131 offset:6144\n\t"
        "v_bfe_u32 v78, v78, 2, 1\n\t"
        "v_mfma_f32_16x16x32_bf16 v[4:7], a[172:175], v[108:111], v[4:7]\n\t"
        "ds_read_b64_tr_b16 a[218:219], v131 offset:6912\n\t"
        "ds_read_b64_tr_b16 a[220:221], v132 offset:6144\n\t"
        "v_add3_u32 v78, v79, v78, 6\n\t"
        "v_cndmask_b32 v78, v80, v78, s[44:45]\n\t"
        "v_mfma_f32_16x16x32_bf16 v[8:11], a[176:179], v[108:111], v[8:11]\n\t"
        "ds_read_b64_tr_b16 a[222:223], v132 offset:6912\n\t"
        "ds_read_b64_tr_b16 a[224:225], v133 offset:6144\n\t"
        "v_cndmask_b32 v79, 0, v119, s[64:65]\n\t"
        "v_mfma_f32_16x16x32_bf16 v[12:15], a[144:147], v[108:111], v[12:15]\n\t"
        "ds_read_b64_tr_b16 a[226:227], v133 offset:6912\n\t"
        "v_add_u32 v81, v80, v79\n\t"
        "v_mfma_f32_16x16x32_bf16 v[16:19], a[148:151], v[108:111], v[16:19]\n\t"
        "buffer_load_dwordx4 v142, s[24:27], s36 offen offset:2048 lds\n\t"
        "s_add_u32 m0, s37, 49152\n\t"
        "v_mfma_f32_16x16x32_bf16 v[20:23], a[152:155], v[108:111], v[20:23]\n\t"
        "s_waitcnt vmcnt(6)\n\t"
        "s_barrier\n\t"
        "ds_read_b64_tr_b16 a[228:229], v131 offset:12288\n\t"
        "s_add_u32 s36, s36, s38\n\t"
        "ds_read_b64_tr_b16 a[230:231], v131 offset:13056\n\t"
        "v_mfma_f32_16x16x32_bf16 v[24:27], a[156:159], v[108:111], v[24:27]\n\t"
        "ds_read_b128 v[92:95], v129 offset:8320\n\t"
        "ds_read_b64_tr_b16 a[232:233], v132 offset:12288\n\t"
        "v_add_u32 v82, v78, v79\n\t"
        "v_cndmask_b32 v78, v81, v78, s[44:45]\n\t"
        "v_mfma_f32_16x16x32_bf16 v[28:31], a[160:163], v[108:111], v[28:31]\n\t"
        "ds_read_b64_tr_b16 a[234:235], v132 offset:13056\n\t"
        "ds_read_b64_tr_b16 a[236:237], v133 offset:12288\n\t"
        "v_lshl_or_b32 v68, v78, 16, v80\n\t"
        "v_mfma_f32_16x16x32_bf16 v[32:35], a[164:167], v[108:111], v[32:35]\n\t"
        "buffer_load_dwordx4 v140, s[24:27], s36 offen offset:0 lds\n\t"
        "ds_read_b64_tr_b16 a[238:239], v133 offset:13056\n\t"
        "v_lshl_or_b32 v69, v82, 16, v81\n\t"
        "s_waitcnt lgkmcnt(10)\n\t"
        "v_mfma_f32_16x16x32_bf16 v[0:3], a[192:195], v[112:115], v[0:3]\n\t"
        "ds_write_b128 v118, v[68:71]\n\t"
        "v_mfma_f32_16x16x32_bf16 v[4:7], a[196:199], v[112:115], v[4:7]\n\t"
        "ds_read_b128 v[160:163], v185 offset:0\n\t"
        "v_xor_b32 v154, 32, v186\n\t"
        "v_mfma_f32_16x16x32_bf16 v[8:11], a[200:203], v[112:115], v[8:11]\n\t"
        "ds_read_b64_tr_b16 a[240:241], v131 offset:18432\n\t"
        "v_mfma_f32_16x16x32_bf16 v[0:3], a[180:183], v[84:87], v[0:3]\n\t"
        "buffer_load_dwordx4 v141, s[24:27], s36 offen offset:1024 lds\n\t"
        "v_mfma_f32_16x16x32_bf16 v[4:7], a[184:187], v[84:87], v[4:7]\n\t"
        "ds_read_b64_tr_b16 a[242:243], v131 offset:19200\n\t"
        "v_mfma_f32_16x16x32_bf16 v[8:11], a[188:191], v[84:87], v[8:11]\n\t"
        "ds_read_b64_tr_b16 a[244:245], v132 offset:18432\n\t"
        "v_mfma_f32_16x16x32_bf16 v[12:15], a[192:195], v[84:87], v[12:15]\n\t"
        "ds_read_b128 v[96:99], v130 offset:0\n\t"
        "ds_read_b64_tr_b16 a[246:247], v132 offset:19200\n\t"
        "v_mfma_f32_16x16x32_bf16 v[16:19], a[196:199], v[84:87], v[16:19]\n\t"
        "buffer_load_dwordx4 v142, s[24:27], s36 offen offset:2048 lds\n\t"
        "ds_read_b64_tr_b16 a[248:249], v133 offset:18432\n\t"
        "v_mfma_f32_16x16x32_bf16 v[20:23], a[200:203], v[84:87], v[20:23]\n\t"
        "ds_read_b64_tr_b16 a[250:251], v133 offset:19200\n\t"
        "v_mfma_f32_16x16x32_bf16 v[12:15], a[168:171], v[88:91], v[12:15]\n\t"
        "ds_read_b128 v[100:103], v130 offset:4096\n\t"
        "v_mfma_f32_16x16x32_bf16 v[16:19], a[172:175], v[88:91], v[16:19]\n\t"
        "s_waitcnt lgkmcnt(8)\n\t"
        "v_lshlrev_b32_sdwa v157, 5, v160 dst_sel:DWORD dst_unused:UNUSED_PAD src0_sel:DWORD src1_sel:WORD_0\n\t"
        "v_lshlrev_b32_sdwa v158, 5, v160 dst_sel:DWORD dst_unused:UNUSED_PAD src0_sel:DWORD src1_sel:WORD_1\n\t"
        "v_or_b32 v157, v184, v157\n\t"
        "v_mfma_f32_16x16x32_bf16 v[20:23], a[176:179], v[88:91], v[20:23]\n\t"
        "ds_read_b64_tr_b16 v[60:61], v157 offset:0\n\t"
        "v_or_b32 v158, v184, v158\n\t"
        "v_xor_b32 v159, 32, v157\n\t"
        "v_mfma_f32_16x16x32_bf16 v[24:27], a[144:147], v[88:91], v[24:27]\n\t"
        "ds_read_b64_tr_b16 v[62:63], v158 offset:0\n\t"
        "v_xor_b32 v152, 32, v158\n\t"
        "v_mfma_f32_16x16x32_bf16 v[28:31], a[148:151], v[88:91], v[28:31]\n\t"
        "ds_read_b64_tr_b16 v[64:65], v159 offset:0\n\t"
        "v_perm_b32 v164, v162, v162, v168\n\t"
        "v_mfma_f32_16x16x32_bf16 v[32:35], a[152:155], v[88:91], v[32:35]\n\t"
        "ds_read_b64_tr_b16 v[66:67], v152 offset:0\n\t"
        "v_perm_b32 v165, v162, v162, v169\n\t"
        "v_mfma_f32_16x16x32_bf16 v[36:39], a[156:159], v[88:91], v[36:39]\n\t"
        "ds_read_b64_tr_b16 v[68:69], v157 offset:64\n\t"
        "v_perm_b32 v166, v162, v162, v170\n\t"
        "v_perm_b32 v167, v162, v162, v171\n\t"
        "v_mfma_f32_16x16x32_bf16 v[40:43], a[160:163], v[88:91], v[40:43]\n\t"
        "ds_read_b64_tr_b16 v[70:71], v158 offset:64\n\t"
        "v_mfma_f32_16x16x32_bf16 v[44:47], a[164:167], v[88:91], v[44:47]\n\t"
        "s_waitcnt vmcnt(3) lgkmcnt(7)\n\t"
        "s_barrier\n\t"
        "v_mfma_f32_16x16x32_bf16 v[0:3], a[204:207], v[92:95], v[0:3]\n\t"
        "ds_read_b64_tr_b16 v[72:73], v159 offset:64\n\t"
        "ds_read_b128 v[104:107], v130 offset:8192\n\t"
        "v_mfma_f32_16x16x32_bf16 v[4:7], a[208:211], v[92:95], v[4:7]\n\t"
        "ds_read_b64_tr_b16 v[74:75], v152 offset:64\n\t"
        "ds_read_b64_tr_b16 a[252:253], v131 offset:24576\n\t"
        "v_mfma_f32_16x16x32_bf16 v[8:11], a[212:215], v[92:95], v[8:11]\n\t"
        "ds_read_b64_tr_b16 v[76:77], v157 offset:128\n\t"
        "ds_read_b64_tr_b16 a[254:255], v131 offset:25344\n\t"
        "v_lshlrev_b32_sdwa v157, 5, v161 dst_sel:DWORD dst_unused:UNUSED_PAD src0_sel:DWORD src1_sel:WORD_0\n\t"
        "v_or_b32 v157, v184, v157\n\t"
        "v_mfma_f32_16x16x32_bf16 v[12:15], a[180:183], v[92:95], v[12:15]\n\t"
        "ds_read_b64_tr_b16 v[78:79], v158 offset:128\n\t"
        "ds_read_b64_tr_b16 v[188:189], v132 offset:24576\n\t"
        "v_lshlrev_b32_sdwa v158, 5, v161 dst_sel:DWORD dst_unused:UNUSED_PAD src0_sel:DWORD src1_sel:WORD_1\n\t"
        "s_waitcnt lgkmcnt(11)\n\t"
        "v_mfma_f32_16x16x32_bf16 v[48:51], v[60:63], v[164:167], 0\n\t"
        "ds_read_b64_tr_b16 v[80:81], v159 offset:128\n\t"
        "ds_read_b64_tr_b16 v[190:191], v132 offset:25344\n\t"
        "v_or_b32 v158, v184, v158\n\t"
        "v_mfma_f32_16x16x32_bf16 v[16:19], a[184:187], v[92:95], v[16:19]\n\t"
        "ds_read_b64_tr_b16 v[82:83], v152 offset:128\n\t"
        "ds_read_b64_tr_b16 v[192:193], v133 offset:24576\n\t"
        "v_xor_b32 v159, 32, v157\n\t"
        "s_waitcnt lgkmcnt(12)\n\t"
        "v_mfma_f32_16x16x32_bf16 v[52:55], v[64:67], v[164:167], 0\n\t"
        "ds_read_b64_tr_b16 v[60:61], v157 offset:0\n\t"
        "ds_read_b64_tr_b16 v[194:195], v133 offset:25344\n\t"
        "v_xor_b32 v152, 32, v158\n\t"
        "v_mfma_f32_16x16x32_bf16 v[20:23], a[188:191], v[92:95], v[20:23]\n\t"
        "ds_read_b64_tr_b16 v[62:63], v158 offset:0\n\t"
        "v_mfma_f32_16x16x32_bf16 v[56:59], v[68:71], v[164:167], 0\n\t"
        "ds_read_b64_tr_b16 v[64:65], v159 offset:0\n\t"
        "v_mfma_f32_16x16x32_bf16 v[24:27], a[192:195], v[92:95], v[24:27]\n\t"
        "ds_read_b64_tr_b16 v[66:67], v152 offset:0\n\t"
        "v_mfma_f32_16x16x32_bf16 v[28:31], a[196:199], v[92:95], v[28:31]\n\t"
        "ds_read_b64_tr_b16 v[68:69], v157 offset:64\n\t"
        "s_waitcnt lgkmcnt(12)\n\t"
        "v_mfma_f32_16x16x32_bf16 v[116:119], v[72:75], v[164:167], 0\n\t"
        "ds_read_b64_tr_b16 v[70:71], v158 offset:64\n\t"
        "v_mfma_f32_16x16x32_bf16 v[32:35], a[200:203], v[92:95], v[32:35]\n\t"
        "ds_read_b64_tr_b16 v[72:73], v159 offset:64\n\t"
        "s_waitcnt lgkmcnt(11)\n\t"
        "v_mfma_f32_16x16x32_bf16 v[120:123], v[76:79], v[164:167], 0\n\t"
        "ds_read_b64_tr_b16 v[74:75], v152 offset:64\n\t"
        "v_mfma_f32_16x16x32_bf16 v[0:3], a[228:231], v[96:99], v[0:3]\n\t"
        "ds_read_b64_tr_b16 v[76:77], v157 offset:128\n\t"
        "s_waitcnt lgkmcnt(10)\n\t"
        "v_mfma_f32_16x16x32_bf16 v[124:127], v[80:83], v[164:167], 0\n\t"
        "ds_read_b64_tr_b16 v[78:79], v158 offset:128\n\t"
        "v_perm_b32 v164, v163, v163, v168\n\t"
        "v_perm_b32 v165, v163, v163, v169\n\t"
        "v_mfma_f32_16x16x32_bf16 v[4:7], a[232:235], v[96:99], v[4:7]\n\t"
        "ds_read_b64_tr_b16 v[80:81], v159 offset:128\n\t"
        "v_perm_b32 v166, v163, v163, v170\n\t"
        "v_perm_b32 v167, v163, v163, v171\n\t"
        "v_mfma_f32_16x16x32_bf16 v[8:11], a[236:239], v[96:99], v[8:11]\n\t"
        "ds_read_b128 v[108:111], v129 offset:12416\n\t"
        "s_waitcnt lgkmcnt(9)\n\t"
        "v_mfma_f32_16x16x32_bf16 v[48:51], v[60:63], v[164:167], v[48:51]\n\t"
        "ds_read_b64_tr_b16 v[82:83], v152 offset:128\n\t"
        "v_mfma_f32_16x16x32_bf16 v[0:3], a[216:219], v[100:103], v[0:3]\n\t"
        "ds_read_b128 v[160:163], v185 offset:256\n\t"
        "ds_read_b64_tr_b16 v[196:197], v131 offset:30720\n\t"
        "s_waitcnt lgkmcnt(10)\n\t"
        "v_mfma_f32_16x16x32_bf16 v[52:55], v[64:67], v[164:167], v[52:55]\n\t"
        "ds_read_b64_tr_b16 v[198:199], v131 offset:31488\n\t"
        "ds_read_b64_tr_b16 v[200:201], v132 offset:30720\n\t"
        "v_mfma_f32_16x16x32_bf16 v[4:7], a[220:223], v[100:103], v[4:7]\n\t"
        "ds_read_b64_tr_b16 v[202:203], v132 offset:31488\n\t"
        "ds_read_b64_tr_b16 v[204:205], v133 offset:30720\n\t"
        "s_waitcnt lgkmcnt(11)\n\t"
        "v_mfma_f32_16x16x32_bf16 v[56:59], v[68:71], v[164:167], v[56:59]\n\t"
        "ds_read_b64_tr_b16 v[206:207], v133 offset:31488\n\t"
        "v_cvt_pk_bf16_f32 v48, v48, v49\n\t"
        "v_cvt_pk_bf16_f32 v49, v50, v51\n\t"
        "v_mfma_f32_16x16x32_bf16 v[8:11], a[224:227], v[100:103], v[8:11]\n\t"
        "ds_write_b64 v186, v[48:49] offset:0\n\t"
        "v_mfma_f32_16x16x32_bf16 v[116:119], v[72:75], v[164:167], v[116:119]\n\t"
        "ds_read_b64_tr_b16 v[208:209], v131 offset:36864\n\t"
        "v_cvt_pk_bf16_f32 v52, v52, v53\n\t"
        "v_cvt_pk_bf16_f32 v53, v54, v55\n\t"
        "v_mfma_f32_16x16x32_bf16 v[12:15], a[228:231], v[100:103], v[12:15]\n\t"
        "s_waitcnt vmcnt(0)\n\t"
        "s_barrier\n\t"
        "s_waitcnt lgkmcnt(10)\n\t"
        "v_mfma_f32_16x16x32_bf16 v[120:123], v[76:79], v[164:167], v[120:123]\n\t"
        "ds_write_b64 v154, v[52:53] offset:0\n\t"
        "v_mfma_f32_16x16x32_bf16 v[16:19], a[232:235], v[100:103], v[16:19]\n\t"
        "ds_read_b128 v[112:115], v130 offset:64\n\t"
        "v_cvt_pk_bf16_f32 v56, v56, v57\n\t"
        "v_cvt_pk_bf16_f32 v57, v58, v59\n\t"
        "s_waitcnt lgkmcnt(9)\n\t"
        "v_mfma_f32_16x16x32_bf16 v[124:127], v[80:83], v[164:167], v[124:127]\n\t"
        "ds_write_b64 v186, v[56:57] offset:64\n\t"
        "v_mfma_f32_16x16x32_bf16 v[20:23], a[236:239], v[100:103], v[20:23]\n\t"
        "ds_read_b64_tr_b16 v[210:211], v131 offset:37632\n\t"
        "v_lshlrev_b32_sdwa v157, 5, v160 dst_sel:DWORD dst_unused:UNUSED_PAD src0_sel:DWORD src1_sel:WORD_0\n\t"
        "v_mfma_f32_16x16x32_bf16 v[0:3], a[240:243], v[104:107], v[0:3]\n\t"
        "ds_read_b128 v[84:87], v130 offset:4160\n\t"
        "v_lshlrev_b32_sdwa v158, 5, v160 dst_sel:DWORD dst_unused:UNUSED_PAD src0_sel:DWORD src1_sel:WORD_1\n\t"
        "v_mfma_f32_16x16x32_bf16 v[4:7], a[244:247], v[104:107], v[4:7]\n\t"
        "ds_read_b64_tr_b16 v[212:213], v132 offset:36864\n\t"
        "v_or_b32 v157, v184, v157\n\t"
        "v_or_b32 v158, v184, v158\n\t"
        "v_mfma_f32_16x16x32_bf16 v[8:11], a[248:251], v[104:107], v[8:11]\n\t"
        "ds_read_b64_tr_b16 v[60:61], v157 offset:0\n\t"
        "v_xor_b32 v159, 32, v157\n\t"
        "v_mfma_f32_16x16x32_bf16 v[12:15], a[216:219], v[104:107], v[12:15]\n\t"
        "ds_read_b64_tr_b16 v[62:63], v158 offset:0\n\t"
        "v_xor_b32 v152, 32, v158\n\t"
        "v_mfma_f32_16x16x32_bf16 v[16:19], a[220:223], v[104:107], v[16:19]\n\t"
        "ds_read_b64_tr_b16 v[64:65], v159 offset:0\n\t"
        "v_cvt_pk_bf16_f32 v116, v116, v117\n\t"
        "v_mfma_f32_16x16x32_bf16 v[20:23], a[224:227], v[104:107], v[20:23]\n\t"
        "ds_read_b64_tr_b16 v[66:67], v152 offset:0\n\t"
        "v_cvt_pk_bf16_f32 v117, v118, v119\n\t"
        "v_cvt_pk_bf16_f32 v120, v120, v121\n\t"
        "v_mfma_f32_16x16x32_bf16 v[24:27], a[228:231], v[104:107], v[24:27]\n\t"
        "ds_write_b64 v154, v[116:117] offset:64\n\t"
        "ds_read_b128 v[88:91], v130 offset:12288\n\t"
        "v_mfma_f32_16x16x32_bf16 v[28:31], a[232:235], v[104:107], v[28:31]\n\t"
        "ds_read_b64_tr_b16 v[68:69], v157 offset:64\n\t"
        "ds_read_b64_tr_b16 v[214:215], v132 offset:37632\n\t"
        "v_cvt_pk_bf16_f32 v121, v122, v123\n\t"
        "v_mfma_f32_16x16x32_bf16 v[32:35], a[236:239], v[104:107], v[32:35]\n\t"
        "ds_write_b64 v186, v[120:121] offset:128\n\t"
        "ds_read_b64_tr_b16 v[216:217], v133 offset:36864\n\t"
        "v_mfma_f32_16x16x32_bf16 v[12:15], a[204:207], v[108:111], v[12:15]\n\t"
        "ds_read_b64_tr_b16 v[70:71], v158 offset:64\n\t"
        "ds_read_b64_tr_b16 v[218:219], v133 offset:37632\n\t"
        "v_cvt_pk_bf16_f32 v124, v124, v125\n\t"
        "v_cvt_pk_bf16_f32 v125, v126, v127\n\t"
        "v_mfma_f32_16x16x32_bf16 v[16:19], a[208:211], v[108:111], v[16:19]\n\t"
        "ds_write_b64 v154, v[124:125] offset:128\n\t"
        "v_mfma_f32_16x16x32_bf16 v[20:23], a[212:215], v[108:111], v[20:23]\n\t"
        "ds_read_b64_tr_b16 v[72:73], v159 offset:64\n\t"
        "v_perm_b32 v164, v162, v162, v168\n\t"
        "v_mfma_f32_16x16x32_bf16 v[24:27], a[180:183], v[108:111], v[24:27]\n\t"
        "ds_read_b64_tr_b16 v[74:75], v152 offset:64\n\t"
        "v_perm_b32 v165, v162, v162, v169\n\t"
        "v_mfma_f32_16x16x32_bf16 v[28:31], a[184:187], v[108:111], v[28:31]\n\t"
        "ds_read_b64_tr_b16 v[76:77], v157 offset:128\n\t"
        "v_lshlrev_b32_sdwa v157, 5, v161 dst_sel:DWORD dst_unused:UNUSED_PAD src0_sel:DWORD src1_sel:WORD_0\n\t"
        "v_or_b32 v157, v184, v157\n\t"
        "v_mfma_f32_16x16x32_bf16 v[32:35], a[188:191], v[108:111], v[32:35]\n\t"
        "ds_read_b64_tr_b16 v[78:79], v158 offset:128\n\t"
        "v_lshlrev_b32_sdwa v158, 5, v161 dst_sel:DWORD dst_unused:UNUSED_PAD src0_sel:DWORD src1_sel:WORD_1\n\t"
        "v_mfma_f32_16x16x32_bf16 v[36:39], a[192:195], v[108:111], v[36:39]\n\t"
        "ds_read_b64_tr_b16 v[80:81], v159 offset:128\n\t"
        "v_or_b32 v158, v184, v158\n\t"
        "v_mfma_f32_16x16x32_bf16 v[40:43], a[196:199], v[108:111], v[40:43]\n\t"
        "ds_read_b64_tr_b16 v[82:83], v152 offset:128\n\t"
        "v_xor_b32 v159, 32, v157\n\t"
        "v_mfma_f32_16x16x32_bf16 v[44:47], a[200:203], v[108:111], v[44:47]\n\t"
        "ds_read_b128 v[92:95], v130 offset:8256\n\t"
        "v_xor_b32 v152, 32, v158\n\t"
        "s_waitcnt lgkmcnt(9)\n\t"
        "v_mfma_f32_16x16x32_bf16 v[0:3], v[196:199], v[112:115], v[0:3]\n\t"
        "ds_read_b64_tr_b16 v[220:221], v131 offset:43008\n\t"
        "ds_read_b64_tr_b16 v[222:223], v131 offset:43776\n\t"
        "v_perm_b32 v166, v162, v162, v170\n\t"
        "v_perm_b32 v167, v162, v162, v171\n\t"
        "v_mfma_f32_16x16x32_bf16 v[4:7], v[200:203], v[112:115], v[4:7]\n\t"
        "ds_read_b64_tr_b16 v[224:225], v132 offset:43008\n\t"
        "ds_read_b64_tr_b16 v[226:227], v132 offset:43776\n\t"
        "v_mfma_f32_16x16x32_bf16 v[48:51], v[60:63], v[164:167], 0\n\t"
        "ds_read_b64_tr_b16 v[60:61], v157 offset:0\n\t"
        "ds_read_b64_tr_b16 v[228:229], v133 offset:43008\n\t"
        "v_mfma_f32_16x16x32_bf16 v[8:11], v[204:207], v[112:115], v[8:11]\n\t"
        "ds_read_b64_tr_b16 v[62:63], v158 offset:0\n\t"
        "ds_read_b64_tr_b16 v[230:231], v133 offset:43776\n\t"
        "v_mfma_f32_16x16x32_bf16 v[52:55], v[64:67], v[164:167], 0\n\t"
        "ds_read_b64_tr_b16 v[64:65], v159 offset:0\n\t"
        "v_mfma_f32_16x16x32_bf16 v[0:3], a[252:255], v[84:87], v[0:3]\n\t"
        "ds_read_b64_tr_b16 v[66:67], v152 offset:0\n\t"
        "v_mfma_f32_16x16x32_bf16 v[56:59], v[68:71], v[164:167], 0\n\t"
        "ds_read_b64_tr_b16 v[68:69], v157 offset:64\n\t"
        "v_mfma_f32_16x16x32_bf16 v[4:7], v[188:191], v[84:87], v[4:7]\n\t"
        "ds_read_b64_tr_b16 v[70:71], v158 offset:64\n\t"
        "s_waitcnt lgkmcnt(12)\n\t"
        "v_mfma_f32_16x16x32_bf16 v[116:119], v[72:75], v[164:167], 0\n\t"
        "ds_read_b64_tr_b16 v[72:73], v159 offset:64\n\t"
        "v_mfma_f32_16x16x32_bf16 v[8:11], v[192:195], v[84:87], v[8:11]\n\t"
        "ds_read_b64_tr_b16 v[74:75], v152 offset:64\n\t"
        "v_mfma_f32_16x16x32_bf16 v[120:123], v[76:79], v[164:167], 0\n\t"
        "ds_read_b64_tr_b16 v[76:77], v157 offset:128\n\t"
        "v_mfma_f32_16x16x32_bf16 v[12:15], v[196:199], v[84:87], v[12:15]\n\t"
        "s_waitcnt lgkmcnt(7)\n\t"
        "s_barrier\n\t"
        "v_mfma_f32_16x16x32_bf16 v[124:127], v[80:83], v[164:167], 0\n\t"
        "ds_read_b64_tr_b16 v[78:79], v158 offset:128\n\t"
        "v_perm_b32 v164, v163, v163, v168\n\t"
        "v_perm_b32 v165, v163, v163, v169\n\t"
        "v_mfma_f32_16x16x32_bf16 v[16:19], v[200:203], v[84:87], v[16:19]\n\t"
        "ds_read_b64_tr_b16 v[80:81], v159 offset:128\n\t"
        "v_perm_b32 v166, v163, v163, v170\n\t"
        "v_perm_b32 v167, v163, v163, v171\n\t"
        "v_mfma_f32_16x16x32_bf16 v[20:23], v[204:207], v[84:87], v[20:23]\n\t"
        "ds_read_b64_tr_b16 v[82:83], v152 offset:128\n\t"
        "v_mfma_f32_16x16x32_bf16 v[48:51], v[60:63], v[164:167], v[48:51]\n\t"
        "ds_read_b128 v[160:163], v185 offset:512\n\t"
        "v_mfma_f32_16x16x32_bf16 v[12:15], a[240:243], v[88:91], v[12:15]\n\t"
        "ds_read_b128 v[96:99], v130 offset:12352\n\t"
        "s_waitcnt lgkmcnt(8)\n\t"
        "v_mfma_f32_16x16x32_bf16 v[52:55], v[64:67], v[164:167], v[52:55]\n\t"
        "ds_read_b64_tr_b16 v[232:233], v131 offset:49152\n\t"
        "v_mfma_f32_16x16x32_bf16 v[16:19], a[244:247], v[88:91], v[16:19]\n\t"
        "ds_read_b64_tr_b16 v[234:235], v131 offset:49920\n\t"
        "v_mfma_f32_16x16x32_bf16 v[56:59], v[68:71], v[164:167], v[56:59]\n\t"
        "ds_read_b64_tr_b16 v[236:237], v132 offset:49152\n\t"
        "v_cvt_pk_bf16_f32 v48, v48, v49\n\t"
        "v_cvt_pk_bf16_f32 v49, v50, v51\n\t"
        "v_mfma_f32_16x16x32_bf16 v[20:23], a[248:251], v[88:91], v[20:23]\n\t"
        "ds_write_b64 v186, v[48:49] offset:12288\n\t"
        "s_waitcnt lgkmcnt(9)\n\t"
        "v_mfma_f32_16x16x32_bf16 v[116:119], v[72:75], v[164:167], v[116:119]\n\t"
        "ds_read_b64_tr_b16 v[238:239], v132 offset:49920\n\t"
        "v_cvt_pk_bf16_f32 v52, v52, v53\n\t"
        "v_cvt_pk_bf16_f32 v53, v54, v55\n\t"
        "v_mfma_f32_16x16x32_bf16 v[24:27], a[216:219], v[88:91], v[24:27]\n\t"
        "ds_write_b64 v154, v[52:53] offset:12288\n\t"
        "ds_read_b64_tr_b16 v[240:241], v133 offset:49152\n\t"
        "s_waitcnt lgkmcnt(9)\n\t"
        "v_mfma_f32_16x16x32_bf16 v[120:123], v[76:79], v[164:167], v[120:123]\n\t"
        "ds_read_b64_tr_b16 v[242:243], v133 offset:49920\n\t"
        "v_cvt_pk_bf16_f32 v56, v56, v57\n\t"
        "v_cvt_pk_bf16_f32 v57, v58, v59\n\t"
        "v_mfma_f32_16x16x32_bf16 v[28:31], a[220:223], v[88:91], v[28:31]\n\t"
        "ds_write_b64 v186, v[56:57] offset:12352\n\t"
        "v_mfma_f32_16x16x32_bf16 v[124:127], v[80:83], v[164:167], v[124:127]\n\t"
        "ds_read_b64_tr_b16 v[244:245], v131 offset:55296\n\t"
        "s_waitcnt lgkmcnt(9)\n\t"
        "v_lshlrev_b32_sdwa v157, 5, v160 dst_sel:DWORD dst_unused:UNUSED_PAD src0_sel:DWORD src1_sel:WORD_0\n\t"
        "v_mfma_f32_16x16x32_bf16 v[32:35], a[224:227], v[88:91], v[32:35]\n\t"
        "ds_read_b64_tr_b16 v[246:247], v131 offset:56064\n\t"
        "v_lshlrev_b32_sdwa v158, 5, v160 dst_sel:DWORD dst_unused:UNUSED_PAD src0_sel:DWORD src1_sel:WORD_1\n\t"
        "v_or_b32 v157, v184, v157\n\t"
        "v_mfma_f32_16x16x32_bf16 v[36:39], a[228:231], v[88:91], v[36:39]\n\t"
        "ds_read_b64_tr_b16 v[60:61], v157 offset:0\n\t"
        "v_or_b32 v158, v184, v158\n\t"
        "v_mfma_f32_16x16x32_bf16 v[40:43], a[232:235], v[88:91], v[40:43]\n\t"
        "ds_read_b64_tr_b16 v[62:63], v158 offset:0\n\t"
        "v_xor_b32 v159, 32, v157\n\t"
        "v_mfma_f32_16x16x32_bf16 v[44:47], a[236:239], v[88:91], v[44:47]\n\t"
        "ds_read_b64_tr_b16 v[64:65], v159 offset:0\n\t"
        "v_xor_b32 v152, 32, v158\n\t"
        "v_cvt_pk_bf16_f32 v116, v116, v117\n\t"
        "v_mfma_f32_16x16x32_bf16 v[0:3], v[208:211], v[92:95], v[0:3]\n\t"
        "ds_read_b64_tr_b16 v[66:67], v152 offset:0\n\t"
        "v_cvt_pk_bf16_f32 v117, v118, v119\n\t"
        "v_mfma_f32_16x16x32_bf16 v[4:7], v[212:215], v[92:95], v[4:7]\n\t"
        "ds_write_b64 v154, v[116:117] offset:12352\n\t"
        "v_mfma_f32_16x16x32_bf16 v[8:11], v[216:219], v[92:95], v[8:11]\n\t"
        "ds_read_b128 v[100:103], v130 offset:128\n\t"
        "v_cvt_pk_bf16_f32 v120, v120, v121\n\t"
        "v_mfma_f32_16x16x32_bf16 v[12:15], a[252:255], v[92:95], v[12:15]\n\t"
        "ds_read_b64_tr_b16 v[68:69], v157 offset:64\n\t"
        "v_cvt_pk_bf16_f32 v121, v122, v123\n\t"
        "v_cvt_pk_bf16_f32 v124, v124, v125\n\t"
        "v_mfma_f32_16x16x32_bf16 v[16:19], v[188:191], v[92:95], v[16:19]\n\t"
        "ds_write_b64 v186, v[120:121] offset:12416\n\t"
        "v_mfma_f32_16x16x32_bf16 v[20:23], v[192:195], v[92:95], v[20:23]\n\t"
        "ds_read_b128 v[104:107], v130 offset:4224\n\t"
        "v_cvt_pk_bf16_f32 v125, v126, v127\n\t"
        "v_mfma_f32_16x16x32_bf16 v[24:27], v[196:199], v[92:95], v[24:27]\n\t"
        "ds_write_b64 v154, v[124:125] offset:12416\n\t"
        "ds_read_b128 v[108:111], v129 offset:16448\n\t"
        "v_mfma_f32_16x16x32_bf16 v[28:31], v[200:203], v[92:95], v[28:31]\n\t"
        "ds_read_b64_tr_b16 v[70:71], v158 offset:64\n\t"
        "ds_read_b64_tr_b16 v[248:249], v132 offset:55296\n\t"
        "v_perm_b32 v164, v162, v162, v168\n\t"
        "v_perm_b32 v165, v162, v162, v169\n\t"
        "v_mfma_f32_16x16x32_bf16 v[32:35], v[204:207], v[92:95], v[32:35]\n\t"
        "ds_read_b64_tr_b16 v[72:73], v159 offset:64\n\t"
        ".LpY1%=:\n\t"
        "s_cbranch_execz .LrY%=\n\t"
        "ds_read_b64_tr_b16 v[250:251], v132 offset:56064\n\t"
        "v_perm_b32 v166, v162, v162, v170\n\t"
        "v_mfma_f32_16x16x32_bf16 v[12:15], v[208:211], v[96:99], v[12:15]\n\t"
        "ds_read_b64_tr_b16 v[74:75], v152 offset:64\n\t"
        "ds_read_b64_tr_b16 v[252:253], v133 offset:55296\n\t"
        "v_perm_b32 v167, v162, v162, v171\n\t"
        "v_mfma_f32_16x16x32_bf16 v[16:19], v[212:215], v[96:99], v[16:19]\n\t"
        "ds_read_b64_tr_b16 v[76:77], v157 offset:128\n\t"
        "ds_read_b64_tr_b16 v[254:255], v133 offset:56064\n\t"
        "v_lshlrev_b32_sdwa v157, 5, v161 dst_sel:DWORD dst_unused:UNUSED_PAD src0_sel:DWORD src1_sel:WORD_0\n\t"
        "v_or_b32 v157, v184, v157\n\t"
        "s_waitcnt lgkmcnt(12)\n\t"
        "v_mfma_f32_16x16x32_bf16 v[48:51], v[60:63], v[164:167], 0\n\t"
        "ds_read_b64_tr_b16 v[78:79], v158 offset:128\n\t"
        "v_lshlrev_b32_sdwa v158, 5, v161 dst_sel:DWORD dst_unused:UNUSED_PAD src0_sel:DWORD src1_sel:WORD_1\n\t"
        "v_mfma_f32_16x16x32_bf16 v[20:23], v[216:219], v[96:99], v[20:23]\n\t"
        "ds_read_b64_tr_b16 v[80:81], v159 offset:128\n\t"
        "v_or_b32 v158, v184, v158\n\t"
        "v_mfma_f32_16x16x32_bf16 v[52:55], v[64:67], v[164:167], 0\n\t"
        "ds_read_b64_tr_b16 v[82:83], v152 offset:128\n\t"
        "v_xor_b32 v159, 32, v157\n\t"
        "v_mfma_f32_16x16x32_bf16 v[24:27], a[252:255], v[96:99], v[24:27]\n\t"
        "ds_read_b64_tr_b16 v[60:61], v157 offset:0\n\t"
        "v_xor_b32 v152, 32, v158\n\t"
        "v_mfma_f32_16x16x32_bf16 v[28:31], v[188:191], v[96:99], v[28:31]\n\t"
        "ds_read_b64_tr_b16 v[62:63], v158 offset:0\n\t"
        "v_mfma_f32_16x16x32_bf16 v[32:35], v[192:195], v[96:99], v[32:35]\n\t"
        "ds_read_b64_tr_b16 v[64:65], v159 offset:0\n\t"
        "s_waitcnt lgkmcnt(11)\n\t"
        "v_mfma_f32_16x16x32_bf16 v[56:59], v[68:71], v[164:167], 0\n\t"
        "ds_read_b64_tr_b16 v[66:67], v152 offset:0\n\t"
        "v_mfma_f32_16x16x32_bf16 v[36:39], v[196:199], v[96:99], v[36:39]\n\t"
        "ds_read_b64_tr_b16 v[68:69], v157 offset:64\n\t"
        "s_waitcnt lgkmcnt(10)\n\t"
        "v_mfma_f32_16x16x32_bf16 v[116:119], v[72:75], v[164:167], 0\n\t"
        "ds_read_b64_tr_b16 v[70:71], v158 offset:64\n\t"
        "v_mfma_f32_16x16x32_bf16 v[40:43], v[200:203], v[96:99], v[40:43]\n\t"
        "ds_read_b64_tr_b16 v[72:73], v159 offset:64\n\t"
        "s_waitcnt lgkmcnt(9)\n\t"
        "v_mfma_f32_16x16x32_bf16 v[120:123], v[76:79], v[164:167], 0\n\t"
        "ds_read_b64_tr_b16 v[74:75], v152 offset:64\n\t"
        "v_mfma_f32_16x16x32_bf16 v[44:47], v[204:207], v[96:99], v[44:47]\n\t"
        "ds_read_b128 v[112:115], v130 offset:8320\n\t"
        "s_waitcnt lgkmcnt(9)\n\t"
        "v_mfma_f32_16x16x32_bf16 v[124:127], v[80:83], v[164:167], 0\n\t"
        "ds_read_b64_tr_b16 v[76:77], v157 offset:128\n\t"
        "v_perm_b32 v164, v163, v163, v168\n\t"
        "v_perm_b32 v165, v163, v163, v169\n\t"
        "v_mfma_f32_16x16x32_bf16 v[0:3], v[232:235], v[100:103], v[0:3]\n\t"
        "ds_read_b64_tr_b16 v[78:79], v158 offset:128\n\t"
        "v_perm_b32 v166, v163, v163, v170\n\t"
        "v_perm_b32 v167, v163, v163, v171\n\t"
        "v_mfma_f32_16x16x32_bf16 v[4:7], v[236:239], v[100:103], v[4:7]\n\t"
        "ds_read_b64_tr_b16 v[80:81], v159 offset:128\n\t"
        "s_waitcnt lgkmcnt(9)\n\t"
        "v_mfma_f32_16x16x32_bf16 v[48:51], v[60:63], v[164:167], v[48:51]\n\t"
        "ds_read_b64_tr_b16 v[82:83], v152 offset:128\n\t"
        "v_mfma_f32_16x16x32_bf16 v[8:11], v[240:243], v[100:103], v[8:11]\n\t"
        "ds_read_b128 v[160:163], v185 offset:768\n\t"
        "s_waitcnt lgkmcnt(9)\n\t"
        "v_mfma_f32_16x16x32_bf16 v[52:55], v[64:67], v[164:167], v[52:55]\n\t"
        "v_mfma_f32_16x16x32_bf16 v[0:3], v[220:223], v[104:107], v[0:3]\n\t"
        "ds_read_b128 v[84:87], v130 offset:12416\n\t"
        "s_waitcnt lgkmcnt(8)\n\t"
        "v_mfma_f32_16x16x32_bf16 v[56:59], v[68:71], v[164:167], v[56:59]\n\t"
        "v_cvt_pk_bf16_f32 v48, v48, v49\n\t"
        "v_cvt_pk_bf16_f32 v49, v50, v51\n\t"
        "v_mfma_f32_16x16x32_bf16 v[4:7], v[224:227], v[104:107], v[4:7]\n\t"
        "ds_write_b64 v186, v[48:49] offset:24576\n\t"
        "s_waitcnt lgkmcnt(7)\n\t"
        "v_mfma_f32_16x16x32_bf16 v[116:119], v[72:75], v[164:167], v[116:119]\n\t"
        "v_cvt_pk_bf16_f32 v52, v52, v53\n\t"
        "v_cvt_pk_bf16_f32 v53, v54, v55\n\t"
        "v_mfma_f32_16x16x32_bf16 v[8:11], v[228:231], v[104:107], v[8:11]\n\t"
        "ds_write_b64 v154, v[52:53] offset:24576\n\t"
        "s_waitcnt lgkmcnt(6)\n\t"
        "v_mfma_f32_16x16x32_bf16 v[120:123], v[76:79], v[164:167], v[120:123]\n\t"
        "v_cvt_pk_bf16_f32 v56, v56, v57\n\t"
        "v_cvt_pk_bf16_f32 v57, v58, v59\n\t"
        "v_mfma_f32_16x16x32_bf16 v[12:15], v[232:235], v[104:107], v[12:15]\n\t"
        "ds_write_b64 v186, v[56:57] offset:24640\n\t"
        "s_waitcnt lgkmcnt(5)\n\t"
        "v_mfma_f32_16x16x32_bf16 v[124:127], v[80:83], v[164:167], v[124:127]\n\t"
        "s_waitcnt lgkmcnt(4)\n\t"
        "v_lshlrev_b32_sdwa v157, 5, v160 dst_sel:DWORD dst_unused:UNUSED_PAD src0_sel:DWORD src1_sel:WORD_0\n\t"
        "v_lshlrev_b32_sdwa v158, 5, v160 dst_sel:DWORD dst_unused:UNUSED_PAD src0_sel:DWORD src1_sel:WORD_1\n\t"
        "v_or_b32 v157, v184, v157\n\t"
        "v_mfma_f32_16x16x32_bf16 v[16:19], v[236:239], v[104:107], v[16:19]\n\t"
        "ds_read_b64_tr_b16 v[60:61], v157 offset:0\n\t"
        "v_or_b32 v158, v184, v158\n\t"
        "v_xor_b32 v159, 32, v157\n\t"
        "v_mfma_f32_16x16x32_bf16 v[20:23], v[240:243], v[104:107], v[20:23]\n\t"
        "ds_read_b64_tr_b16 v[62:63], v158 offset:0\n\t"
        "v_xor_b32 v152, 32, v158\n\t"
        "v_mfma_f32_16x16x32_bf16 v[24:27], a[168:171], v[108:111], v[24:27]\n\t"
        "ds_read_b64_tr_b16 v[64:65], v159 offset:0\n\t"
        "v_cvt_pk_bf16_f32 v116, v116, v117\n\t"
        "v_mfma_f32_16x16x32_bf16 v[28:31], a[172:175], v[108:111], v[28:31]\n\t"
        "ds_read_b64_tr_b16 v[66:67], v152 offset:0\n\t"
        "v_cvt_pk_bf16_f32 v117, v118, v119\n\t"
        "v_cvt_pk_bf16_f32 v120, v120, v121\n\t"
        "v_mfma_f32_16x16x32_bf16 v[32:35], a[176:179], v[108:111], v[32:35]\n\t"
        "ds_write_b64 v154, v[116:117] offset:24640\n\t"
        "v_mfma_f32_16x16x32_bf16 v[36:39], a[144:147], v[108:111], v[36:39]\n\t"
        "ds_read_b64_tr_b16 v[68:69], v157 offset:64\n\t"
        "v_cvt_pk_bf16_f32 v121, v122, v123\n\t"
        "v_mfma_f32_16x16x32_bf16 v[40:43], a[148:151], v[108:111], v[40:43]\n\t"
        "ds_write_b64 v186, v[120:121] offset:24704\n\t"
        "v_mfma_f32_16x16x32_bf16 v[44:47], a[152:155], v[108:111], v[44:47]\n\t"
        "ds_read_b64_tr_b16 v[70:71], v158 offset:64\n\t"
        "v_cvt_pk_bf16_f32 v124, v124, v125\n\t"
        "v_cvt_pk_bf16_f32 v125, v126, v127\n\t"
        "v_mfma_f32_16x16x32_bf16 v[0:3], v[244:247], v[112:115], v[0:3]\n\t"
        "ds_write_b64 v154, v[124:125] offset:24704\n\t"
        "v_mfma_f32_16x16x32_bf16 v[4:7], v[248:251], v[112:115], v[4:7]\n\t"
        "ds_read_b64_tr_b16 v[72:73], v159 offset:64\n\t"
        "v_perm_b32 v164, v162, v162, v168\n\t"
        "v_mfma_f32_16x16x32_bf16 v[8:11], v[252:255], v[112:115], v[8:11]\n\t"
        "ds_read_b128 v[88:91], v129 offset:16512\n\t"
        "v_perm_b32 v165, v162, v162, v169\n\t"
        "v_mfma_f32_16x16x32_bf16 v[12:15], v[220:223], v[112:115], v[12:15]\n\t"
        "ds_read_b64_tr_b16 v[74:75], v152 offset:64\n\t"
        "v_perm_b32 v166, v162, v162, v170\n\t"
        "v_perm_b32 v167, v162, v162, v171\n\t"
        "v_mfma_f32_16x16x32_bf16 v[16:19], v[224:227], v[112:115], v[16:19]\n\t"
        "ds_read_b64_tr_b16 v[76:77], v157 offset:128\n\t"
        "v_lshlrev_b32_sdwa v157, 5, v161 dst_sel:DWORD dst_unused:UNUSED_PAD src0_sel:DWORD src1_sel:WORD_0\n\t"
        "s_waitcnt lgkmcnt(9)\n\t"
        "v_mfma_f32_16x16x32_bf16 v[48:51], v[60:63], v[164:167], 0\n\t"
        "ds_read_b64_tr_b16 v[78:79], v158 offset:128\n\t"
        "v_lshlrev_b32_sdwa v158, 5, v161 dst_sel:DWORD dst_unused:UNUSED_PAD src0_sel:DWORD src1_sel:WORD_1\n\t"
        "v_mfma_f32_16x16x32_bf16 v[20:23], v[228:231], v[112:115], v[20:23]\n\t"
        "ds_read_b64_tr_b16 v[80:81], v159 offset:128\n\t"
        "v_or_b32 v157, v184, v157\n\t"
        "v_mfma_f32_16x16x32_bf16 v[52:55], v[64:67], v[164:167], 0\n\t"
        "ds_read_b64_tr_b16 v[82:83], v152 offset:128\n\t"
        "v_or_b32 v158, v184, v158\n\t"
        "v_mfma_f32_16x16x32_bf16 v[24:27], v[232:235], v[112:115], v[24:27]\n\t"
        "ds_read_b64_tr_b16 v[60:61], v157 offset:0\n\t"
        "v_xor_b32 v159, 32, v157\n\t"
        "s_waitcnt lgkmcnt(9)\n\t"
        "v_mfma_f32_16x16x32_bf16 v[56:59], v[68:71], v[164:167], 0\n\t"
        "ds_read_b64_tr_b16 v[62:63], v158 offset:0\n\t"
        "v_xor_b32 v152, 32, v158\n\t"
        "v_mfma_f32_16x16x32_bf16 v[28:31], v[236:239], v[112:115], v[28:31]\n\t"
        "ds_read_b64_tr_b16 v[64:65], v159 offset:0\n\t"
        "v_mfma_f32_16x16x32_bf16 v[32:35], v[240:243], v[112:115], v[32:35]\n\t"
        "ds_read_b128 v[92:95], v130 offset:16384\n\t"
        "v_mfma_f32_16x16x32_bf16 v[12:15], v[244:247], v[84:87], v[12:15]\n\t"
        "ds_read_b64_tr_b16 v[66:67], v152 offset:0\n\t"
        "s_waitcnt lgkmcnt(9)\n\t"
        "v_mfma_f32_16x16x32_bf16 v[116:119], v[72:75], v[164:167], 0\n\t"
        "ds_read_b64_tr_b16 v[68:69], v157 offset:64\n\t"
        "v_mfma_f32_16x16x32_bf16 v[16:19], v[248:251], v[84:87], v[16:19]\n\t"
        "ds_read_b64_tr_b16 v[70:71], v158 offset:64\n\t"
        "s_waitcnt lgkmcnt(9)\n\t"
        "v_mfma_f32_16x16x32_bf16 v[120:123], v[76:79], v[164:167], 0\n\t"
        "ds_read_b64_tr_b16 v[72:73], v159 offset:64\n\t"
        "v_mfma_f32_16x16x32_bf16 v[20:23], v[252:255], v[84:87], v[20:23]\n\t"
        "ds_read_b64_tr_b16 v[74:75], v152 offset:64\n\t"
        "s_waitcnt lgkmcnt(9)\n\t"
        "v_mfma_f32_16x16x32_bf16 v[124:127], v[80:83], v[164:167], 0\n\t"
        "ds_read_b64_tr_b16 v[76:77], v157 offset:128\n\t"
        "v_perm_b32 v164, v163, v163, v168\n\t"
        "v_perm_b32 v165, v163, v163, v169\n\t"
        "v_mfma_f32_16x16x32_bf16 v[24:27], v[220:223], v[84:87], v[24:27]\n\t"
        "ds_read_b64_tr_b16 v[78:79], v158 offset:128\n\t"
        "v_perm_b32 v166, v163, v163, v170\n\t"
        "v_perm_b32 v167, v163, v163, v171\n\t"
        "v_mfma_f32_16x16x32_bf16 v[28:31], v[224:227], v[84:87], v[28:31]\n\t"
        "ds_read_b64_tr_b16 v[80:81], v159 offset:128\n\t"
        "s_waitcnt lgkmcnt(9)\n\t"
        "v_mfma_f32_16x16x32_bf16 v[48:51], v[60:63], v[164:167], v[48:51]\n\t"
        "ds_read_b64_tr_b16 v[82:83], v152 offset:128\n\t"
        "v_mfma_f32_16x16x32_bf16 v[32:35], v[228:231], v[84:87], v[32:35]\n\t"
        "ds_read_b128 v[96:99], v130 offset:16448\n\t"
        "s_waitcnt lgkmcnt(9)\n\t"
        "v_mfma_f32_16x16x32_bf16 v[52:55], v[64:67], v[164:167], v[52:55]\n\t"
        "v_mfma_f32_16x16x32_bf16 v[36:39], v[232:235], v[84:87], v[36:39]\n\t"
        "ds_read_b128 v[100:103], v130 offset:16512\n\t"
        "s_waitcnt lgkmcnt(8)\n\t"
        "v_mfma_f32_16x16x32_bf16 v[56:59], v[68:71], v[164:167], v[56:59]\n\t"
        "v_cvt_pk_bf16_f32 v48, v48, v49\n\t"
        "v_cvt_pk_bf16_f32 v49, v50, v51\n\t"
        "v_mfma_f32_16x16x32_bf16 v[40:43], v[236:239], v[84:87], v[40:43]\n\t"
        "ds_write_b64 v186, v[48:49] offset:36864\n\t"
        "s_waitcnt lgkmcnt(7)\n\t"
        "v_mfma_f32_16x16x32_bf16 v[116:119], v[72:75], v[164:167], v[116:119]\n\t"
        "v_cvt_pk_bf16_f32 v52, v52, v53\n\t"
        "v_cvt_pk_bf16_f32 v53, v54, v55\n\t"
        "v_mfma_f32_16x16x32_bf16 v[44:47], v[240:243], v[84:87], v[44:47]\n\t"
        "ds_write_b64 v154, v[52:53] offset:36864\n\t"
        "s_waitcnt lgkmcnt(6)\n\t"
        "v_mfma_f32_16x16x32_bf16 v[120:123], v[76:79], v[164:167], v[120:123]\n\t"
        "v_cvt_pk_bf16_f32 v56, v56, v57\n\t"
        "v_cvt_pk_bf16_f32 v57, v58, v59\n\t"
        "v_mfma_f32_16x16x32_bf16 v[24:27], a[204:207], v[88:91], v[24:27]\n\t"
        "ds_write_b64 v186, v[56:57] offset:36928\n\t"
        "s_waitcnt lgkmcnt(5)\n\t"
        "v_mfma_f32_16x16x32_bf16 v[124:127], v[80:83], v[164:167], v[124:127]\n\t"
        "v_cvt_pk_bf16_f32 v116, v116, v117\n\t"
        "v_cvt_pk_bf16_f32 v117, v118, v119\n\t"
        "v_mfma_f32_16x16x32_bf16 v[28:31], a[208:211], v[88:91], v[28:31]\n\t"
        "ds_write_b64 v154, v[116:117] offset:36928\n\t"
        "v_mfma_f32_16x16x32_bf16 v[32:35], a[212:215], v[88:91], v[32:35]\n\t"
        "v_cvt_pk_bf16_f32 v120, v120, v121\n\t"
        "v_cvt_pk_bf16_f32 v121, v122, v123\n\t"
        "v_mfma_f32_16x16x32_bf16 v[36:39], a[180:183], v[88:91], v[36:39]\n\t"
        "ds_write_b64 v186, v[120:121] offset:36992\n\t"
        "v_mfma_f32_16x16x32_bf16 v[40:43], a[184:187], v[88:91], v[40:43]\n\t"
        "ds_read_b128 v[104:107], v129 offset:20480\n\t"
        "v_cvt_pk_bf16_f32 v124, v124, v125\n\t"
        "v_cvt_pk_bf16_f32 v125, v126, v127\n\t"
        "v_mfma_f32_16x16x32_bf16 v[44:47], a[188:191], v[88:91], v[44:47]\n\t"
        "ds_write_b64 v154, v[124:125] offset:36992\n\t"
        "v_mfma_f32_16x16x32_bf16 v[24:27], a[240:243], v[92:95], v[24:27]\n\t"
        "s_cmp_gt_u32 s61, 64\n\t"
        "s_cbranch_scc0 .Lx74%=\n\t"
        "v_bfe_u32 v52, v185, 4, 4\n\t"
        "v_add_u32 v48, 65, v52\n\t"
        "s_sub_u32 s8, s37, 12288\n\t"
        "v_mov_b32 v51, s62\n\t"
        "v_lshlrev_b32 v53, 3, v52\n\t"
        "v_mul_u32_u24 v52, 192, v52\n\t"
        "v_and_or_b32 v52, v53, 48, v52\n\t"
        "v_add_u32 v49, s8, v52\n\t"
        "v_subrev_u32 v186, 12288, v186\n\t"
        "v_xor_b32 v154, 32, v186\n\t"
        "v_min_u32 v76, s61, v48\n\t"
        "v_lshl_add_u32 v76, v76, 3, s60\n\t"
        "ds_read_b64 v[60:61], v76\n\t"
        "v_cmp_ge_u32 s[66:67], s61, v48\n\t"
        "s_mov_b32 s46, 0x07060302\n\t"
        "s_waitcnt lgkmcnt(0)\n\t"
        "v_and_b32 v76, 0xfffff800, v61\n\t"
        "ds_read_b128 v[62:65], v76 offset:1536\n\t"
        "v_lshlrev_b32 v70, 16, v60\n\t"
        "v_and_b32 v71, 0xffff0000, v60\n\t"
        "v_med3_f32 v70, v70, 0, s63\n\t"
        "v_med3_f32 v71, v71, 0, s63\n\t"
        "s_mov_b64 exec, s[66:67]\n\t"
        "ds_write_b32 v61, v49\n\t"
        "s_mov_b64 exec, -1\n\t"
        "v_perm_b32 v60, v71, v70, s46\n\t"
        "v_fract_f32 v72, v70\n\t"
        "v_fract_f32 v73, v71\n\t"
        "v_cvt_u32_f32 v74, v70\n\t"
        "v_cvt_u32_f32 v75, v71\n\t"
        "v_cmp_neq_f32 s[64:65], 0, v72\n\t"
        "v_cmp_neq_f32 s[44:45], 0, v73\n\t"
        "v_sub_f32 v76, 1.0, v72\n\t"
        "v_sub_f32 v77, 1.0, v73\n\t"
        "v_cvt_pk_bf16_f32 v77, v76, v77\n\t"
        "v_lshlrev_b32 v76, 16, v77\n\t"
        "v_and_b32 v77, 0xffff0000, v77\n\t"
        "v_mul_f32 v78, v76, v77\n\t"
        "v_mul_f32 v76, v76, v73\n\t"
        "v_mul_f32 v77, v72, v77\n\t"
        "v_mul_f32 v79, v72, v73\n\t"
        "v_cndmask_b32 v76, v77, v76, s[44:45]\n\t"
        "v_cndmask_b32 v77, 0, v77, s[44:45]\n\t"
        "v_cvt_pk_bf16_f32 v162, v78, v76\n\t"
        "v_cvt_pk_bf16_f32 v163, v77, v79\n\t"
        "s_waitcnt lgkmcnt(1)\n\t"
        "v_pk_max_u16 v76, v60, v62\n\t"
        "v_pk_min_u16 v76, v76, v63\n\t"
        "v_cmp_ne_u32 vcc, v76, v60\n\t"
        "v_sub_u32 v76, v75, v65\n\t"
        "v_mad_u32_u24 v77, v75, 6, v64\n\t"
        "v_mad_u32_u24 v77, v74, s62, v77\n\t"
        "s_and_b64 vcc, vcc, s[66:67]\n\t"
        "s_or_b64 s[56:57], s[56:57], vcc\n\t"
        "v_bfe_u32 v78, v76, 2, 1\n\t"
        "v_or_b32 v78, v78, v77\n\t"
        "v_add_u32 v76, 1, v76\n\t"
        "v_bfe_u32 v76, v76, 2, 1\n\t"
        "v_add3_u32 v76, v77, v76, 6\n\t"
        "v_cndmask_b32 v76, v78, v76, s[44:45]\n\t"
        "v_cndmask_b32 v77, 0, v51, s[64:65]\n\t"
        "v_add_u32 v79, v78, v77\n\t"
        "v_add_u32 v80, v76, v77\n\t"
        "v_cndmask_b32 v76, v79, v76, s[44:45]\n\t"
        "v_lshl_or_b32 v160, v76, 16, v78\n\t"
        "v_lshl_or_b32 v161, v80, 16, v79\n\t"
        "v_lshlrev_b32_sdwa v157, 5, v160 dst_sel:DWORD dst_unused:UNUSED_PAD src0_sel:DWORD src1_sel:WORD_0\n\t"
        "v_lshlrev_b32_sdwa v158, 5, v160 dst_sel:DWORD dst_unused:UNUSED_PAD src0_sel:DWORD src1_sel:WORD_1\n\t"
        "v_or_b32 v157, v184, v157\n\t"
        "v_or_b32 v158, v184, v158\n\t"
        "v_xor_b32 v159, 32, v157\n\t"
        "v_xor_b32 v152, 32, v158\n\t"
        "ds_read_b64_tr_b16 v[60:61], v157 offset:0\n\t"
        "ds_read_b64_tr_b16 v[62:63], v158 offset:0\n\t"
        "ds_read_b64_tr_b16 v[64:65], v159 offset:0\n\t"
        "ds_read_b64_tr_b16 v[66:67], v152 offset:0\n\t"
        "ds_read_b64_tr_b16 v[68:69], v157 offset:64\n\t"
        "ds_read_b64_tr_b16 v[70:71], v158 offset:64\n\t"
        "ds_read_b64_tr_b16 v[72:73], v159 offset:64\n\t"
        "ds_read_b64_tr_b16 v[74:75], v152 offset:64\n\t"
        "ds_read_b64_tr_b16 v[76:77], v157 offset:128\n\t"
        "ds_read_b64_tr_b16 v[78:79], v158 offset:128\n\t"
        "ds_read_b64_tr_b16 v[80:81], v159 offset:128\n\t"
        "ds_read_b64_tr_b16 v[82:83], v152 offset:128\n\t"
        "v_perm_b32 v164, v162, v162, v168\n\t"
        "v_perm_b32 v165, v162, v162, v169\n\t"
        "v_perm_b32 v166, v162, v162, v170\n\t"
        "v_perm_b32 v167, v162, v162, v171\n\t"
        "s_waitcnt lgkmcnt(10)\n\t"
        "s_nop 0\n\t"
        "v_mfma_f32_16x16x32_bf16 v[48:51], v[60:63], v[164:167], 0\n\t"
        "s_waitcnt lgkmcnt(8)\n\t"
        "v_mfma_f32_16x16x32_bf16 v[52:55], v[64:67], v[164:167], 0\n\t"
        "s_waitcnt lgkmcnt(6)\n\t"
        "v_mfma_f32_16x16x32_bf16 v[56:59], v[68:71], v[164:167], 0\n\t"
        "s_waitcnt lgkmcnt(4)\n\t"
        "v_mfma_f32_16x16x32_bf16 v[116:119], v[72:75], v[164:167], 0\n\t"
        "s_waitcnt lgkmcnt(2)\n\t"
        "v_mfma_f32_16x16x32_bf16 v[120:123], v[76:79], v[164:167], 0\n\t"
        "s_waitcnt lgkmcnt(0)\n\t"
        "v_mfma_f32_16x16x32_bf16 v[124:127], v[80:83], v[164:167], 0\n\t"
        "v_lshlrev_b32_sdwa v157, 5, v161 dst_sel:DWORD dst_unused:UNUSED_PAD src0_sel:DWORD src1_sel:WORD_0\n\t"
        "v_lshlrev_b32_sdwa v158, 5, v161 dst_sel:DWORD dst_unused:UNUSED_PAD src0_sel:DWORD src1_sel:WORD_1\n\t"
        "v_or_b32 v157, v184, v157\n\t"
        "v_or_b32 v158, v184, v158\n\t"
        "v_xor_b32 v159, 32, v157\n\t"
        "v_xor_b32 v152, 32, v158\n\t"
        "ds_read_b64_tr_b16 v[60:61], v157 offset:0\n\t"
        "ds_read_b64_tr_b16 v[62:63], v158 offset:0\n\t"
        "ds_read_b64_tr_b16 v[64:65], v159 offset:0\n\t"
        "ds_read_b64_tr_b16 v[66:67], v152 offset:0\n\t"
        "ds_read_b64_tr_b16 v[68:69], v157 offset:64\n\t"
        "ds_read_b64_tr_b16 v[70:71], v158 offset:64\n\t"
        "ds_read_b64_tr_b16 v[72:73], v159 offset:64\n\t"
        "ds_read_b64_tr_b16 v[74:75], v152 offset:64\n\t"
        "ds_read_b64_tr_b16 v[76:77], v157 offset:128\n\t"
        "ds_read_b64_tr_b16 v[78:79], v158 offset:128\n\t"
        "ds_read_b64_tr_b16 v[80:81], v159 offset:128\n\t"
        "ds_read_b64_tr_b16 v[82:83], v152 offset:128\n\t"
        "v_perm_b32 v164, v163, v163, v168\n\t"
        "v_perm_b32 v165, v163, v163, v169\n\t"
        "v_perm_b32 v166, v163, v163, v170\n\t"
        "v_perm_b32 v167, v163, v163, v171\n\t"
        "s_waitcnt lgkmcnt(10)\n\t"
        "s_nop 0\n\t"
        "v_mfma_f32_16x16x32_bf16 v[48:51], v[60:63], v[164:167], v[48:51]\n\t"
        "s_waitcnt lgkmcnt(8)\n\t"
        "v_mfma_f32_16x16x32_bf16 v[52:55], v[64:67], v[164:167], v[52:55]\n\t"
        "s_waitcnt lgkmcnt(6)\n\t"
        "v_mfma_f32_16x16x32_bf16 v[56:59], v[68:71], v[164:167], v[56:59]\n\t"
        "s_waitcnt lgkmcnt(4)\n\t"
        "v_mfma_f32_16x16x32_bf16 v[116:119], v[72:75], v[164:167], v[116:119]\n\t"
        "s_waitcnt lgkmcnt(2)\n\t"
        "v_mfma_f32_16x16x32_bf16 v[120:123], v[76:79], v[164:167], v[120:123]\n\t"
        "s_waitcnt lgkmcnt(0)\n\t"
        "v_mfma_f32_16x16x32_bf16 v[124:127], v[80:83], v[164:167], v[124:127]\n\t"
        "v_cvt_pk_bf16_f32 v48, v48, v49\n\t"
        "v_cvt_pk_bf16_f32 v49, v50, v51\n\t"
        "v_cvt_pk_bf16_f32 v52, v52, v53\n\t"
        "v_cvt_pk_bf16_f32 v53, v54, v55\n\t"
        "v_cvt_pk_bf16_f32 v56, v56, v57\n\t"
        "v_cvt_pk_bf16_f32 v57, v58, v59\n\t"
        "v_cvt_pk_bf16_f32 v116, v116, v117\n\t"
        "v_cvt_pk_bf16_f32 v117, v118, v119\n\t"
        "v_cvt_pk_bf16_f32 v120, v120, v121\n\t"
        "v_cvt_pk_bf16_f32 v121, v122, v123\n\t"
        "v_cvt_pk_bf16_f32 v124, v124, v125\n\t"
        "v_cvt_pk_bf16_f32 v125, v126, v127\n\t"
        "ds_write_b64 v186, v[48:49] offset:0\n\t"
        "ds_write_b64 v154, v[52:53] offset:0\n\t"
        "ds_write_b64 v186, v[56:57] offset:64\n\t"
        "ds_write_b64 v154, v[116:117] offset:64\n\t"
        "ds_write_b64 v186, v[120:121] offset:128\n\t"
        "ds_write_b64 v154, v[124:125] offset:128\n\t"
        "v_add_u32 v186, 12288, v186\n\t"
        "v_xor_b32 v154, 32, v186\n\t"
        ".Lx74%=:\n\t"
        "v_mfma_f32_16x16x32_bf16 v[28:31], a[244:247], v[92:95], v[28:31]\n\t"
        "s_cmp_gt_u32 s3, 64\n\t"
        "s_cselect_b32 s8, 2, 0\n\t"
        "v_mfma_f32_16x16x32_bf16 v[32:35], a[248:251], v[92:95], v[32:35]\n\t"
        "s_cmp_lg_u64 s[56:57], 0\n\t"
        "s_addc_u32 s8, s8, s6\n\t"
        "v_mov_b32 v151, s8\n\t"
        "v_mfma_f32_16x16x32_bf16 v[36:39], a[216:219], v[92:95], v[36:39]\n\t"
        "s_mov_b64 exec, 1\n\t"
        "ds_write_b32 v153, v151\n\t"
        "s_mov_b64 exec, -1\n\t"
        "v_mfma_f32_16x16x32_bf16 v[40:43], a[220:223], v[92:95], v[40:43]\n\t"
        "ds_read_b128 v[108:111], v129 offset:20544\n\t"
        "ds_read_b128 v[112:115], v129 offset:20608\n\t"
        "v_mfma_f32_16x16x32_bf16 v[44:47], a[224:227], v[92:95], v[44:47]\n\t"
        "s_waitcnt lgkmcnt(10)\n\t"
        "v_mfma_f32_16x16x32_bf16 v[24:27], v[208:211], v[96:99], v[24:27]\n\t"
        "ds_read_b128 v[84:87], v130 offset:20480\n\t"
        "v_mfma_f32_16x16x32_bf16 v[28:31], v[212:215], v[96:99], v[28:31]\n\t"
        "v_mfma_f32_16x16x32_bf16 v[32:35], v[216:219], v[96:99], v[32:35]\n\t"
        "v_mfma_f32_16x16x32_bf16 v[36:39], a[252:255], v[96:99], v[36:39]\n\t"
        "s_waitcnt lgkmcnt(3)\n\t"
        "s_barrier\n\t"
        "v_mfma_f32_16x16x32_bf16 v[40:43], v[188:191], v[96:99], v[40:43]\n\t"
        "ds_read_b128 v[124:127], v182 offset:0\n\t"
        "v_mfma_f32_16x16x32_bf16 v[44:47], v[192:195], v[96:99], v[44:47]\n\t"
        "ds_read_b128 v[132:135], v187 offset:4096\n\t"
        "v_mfma_f32_16x16x32_bf16 v[24:27], v[244:247], v[100:103], v[24:27]\n\t"
        "ds_read_b128 v[136:139], v187 offset:4112\n\t"
        "v_mfma_f32_16x16x32_bf16 v[28:31], v[248:251], v[100:103], v[28:31]\n\t"
        "ds_read_b32 v140, v187 offset:4128\n\t"
        "v_mfma_f32_16x16x32_bf16 v[32:35], v[252:255], v[100:103], v[32:35]\n\t"
        "ds_read_b128 v[88:91], v130 offset:20544\n\t"
        "v_mfma_f32_16x16x32_bf16 v[36:39], v[220:223], v[100:103], v[36:39]\n\t"
        "ds_read_b128 v[92:95], v130 offset:20608\n\t"
        "v_mfma_f32_16x16x32_bf16 v[40:43], v[224:227], v[100:103], v[40:43]\n\t"
        "v_mfma_f32_16x16x32_bf16 v[44:47], v[228:231], v[100:103], v[44:47]\n\t"
        "v_mfma_f32_16x16x32_bf16 v[36:39], a[132:135], v[104:107], v[36:39]\n\t"
        "v_mfma_f32_16x16x32_bf16 v[40:43], a[136:139], v[104:107], v[40:43]\n\t"
        "s_waitcnt lgkmcnt(5)\n\t"
        "v_or3_b32 v124, v124, v125, v126\n\t"
        "v_or_b32 v124, v124, v127\n\t"
        "v_mfma_f32_16x16x32_bf16 v[44:47], a[140:143], v[104:107], v[44:47]\n\t"
        "v_readfirstlane_b32 s6, v124\n\t"
        "v_mfma_f32_16x16x32_bf16 v[36:39], a[168:171], v[108:111], v[36:39]\n\t"
        "s_nop 0\n\t"
        "s_bitcmp1_b32 s6, 1\n\t"
        "s_cbranch_scc1 .Lx76%=\n\t"
        ".Lx77%=:\n\t"
        "v_mfma_f32_16x16x32_bf16 v[40:43], a[172:175], v[108:111], v[40:43]\n\t"
        "s_waitcnt lgkmcnt(3)\n\t"
        "v_xor_b32 v132, v181, v132\n\t"
        "v_readlane_b32 s9, v155, 48\n\t"
        "v_mfma_f32_16x16x32_bf16 v[44:47], a[176:179], v[108:111], v[44:47]\n\t"
        "s_nop 0\n\t"
        "v_add_u32 v151, s9, v172\n\t"
        "v_max_u32 v151, v151, v132\n\t"
        "v_xor_b32 v133, v181, v133\n\t"
        "v_add_u32 v183, s9, v173\n\t"
        "v_mfma_f32_16x16x32_bf16 v[36:39], a[204:207], v[112:115], v[36:39]\n\t"
        "ds_read_b128 v[96:99], v151 offset:0\n\t"
        "v_max_u32 v183, v183, v133\n\t"
        "v_mfma_f32_16x16x32_bf16 v[40:43], a[208:211], v[112:115], v[40:43]\n\t"
        "ds_read_b128 v[100:103], v151 offset:64\n\t"
        "v_xor_b32 v134, v181, v134\n\t"
        "v_mfma_f32_16x16x32_bf16 v[44:47], a[212:215], v[112:115], v[44:47]\n\t"
        "ds_read_b128 v[104:107], v151 offset:128\n\t"
        "v_add_u32 v151, s9, v174\n\t"
        "v_max_u32 v151, v151, v134\n\t"
        "v_mfma_f32_16x16x32_bf16 v[36:39], a[240:243], v[84:87], v[36:39]\n\t"
        "ds_read_b128 v[108:111], v183 offset:0\n\t"
        "v_xor_b32 v135, v181, v135\n\t"
        "v_mfma_f32_16x16x32_bf16 v[40:43], a[244:247], v[84:87], v[40:43]\n\t"
        "ds_read_b128 v[112:115], v183 offset:64\n\t"
        "s_add_u32 s55, s41, 0\n\t"
        "v_mfma_f32_16x16x32_bf16 v[44:47], a[248:251], v[84:87], v[44:47]\n\t"
        "v_cvt_pk_bf16_f32 v116, v0, v1\n\t"
        "v_cvt_pk_bf16_f32 v117, v2, v3\n\t"
        "s_waitcnt lgkmcnt(5)\n\t"
        "v_mfma_f32_16x16x32_bf16 v[36:39], v[208:211], v[88:91], v[36:39]\n\t"
        "ds_read_b128 v[84:87], v183 offset:128\n\t"
        "v_add_u32 v183, s9, v175\n\t"
        "v_mfma_f32_16x16x32_bf16 v[40:43], v[212:215], v[88:91], v[40:43]\n\t"
        "v_max_u32 v183, v183, v135\n\t"
        "v_xor_b32 v136, v181, v136\n\t"
        "v_cvt_pk_bf16_f32 v118, v4, v5\n\t"
        "v_mfma_f32_16x16x32_bf16 v[44:47], v[216:219], v[88:91], v[44:47]\n\t"
        "v_cvt_pk_bf16_f32 v119, v6, v7\n\t"
        "v_cvt_pk_bf16_f32 v120, v8, v9\n\t"
        "v_mfma_f32_16x16x32_bf16 v[36:39], v[244:247], v[92:95], v[36:39]\n\t"
        "ds_read_b128 v[88:91], v151 offset:0\n\t"
        "v_cvt_pk_bf16_f32 v121, v10, v11\n\t"
        "v_mfma_f32_16x16x32_bf16 v[40:43], v[248:251], v[92:95], v[40:43]\n\t"
        "v_permlane16_swap_b32 v116, v118\n\t"
        "v_mfma_f32_16x16x32_bf16 v[44:47], v[252:255], v[92:95], v[44:47]\n\t"
        "v_permlane16_swap_b32 v117, v119\n\t"
        "s_waitcnt lgkmcnt(4)\n\t"
        "v_mfma_f32_16x16x32_bf16 v[72:75], a[12:15], v[96:99], 0\n\t"
        "buffer_store_dwordx4 v[116:119], v144, s[28:31], s55 offen sc1\n\t"
        "v_mfma_f32_16x16x32_bf16 v[76:79], a[16:19], v[96:99], 0\n\t"
        "ds_write_b64 v146, v[120:121] offset:0\n\t"
        "v_mfma_f32_16x16x32_bf16 v[80:83], a[20:23], v[96:99], 0\n\t"
        "ds_read_b128 v[92:95], v151 offset:64\n\t"
        "s_add_u32 s55, s41, 43008\n\t"
        "v_cvt_pk_bf16_f32 v116, v12, v13\n\t"
        "v_mfma_f32_16x16x32_bf16 v[72:75], a[48:51], v[100:103], v[72:75]\n\t"
        "ds_read_b128 v[96:99], v151 offset:128\n\t"
        "v_add_u32 v151, s9, v176\n\t"
        "v_mfma_f32_16x16x32_bf16 v[76:79], a[52:55], v[100:103], v[76:79]\n\t"
        "v_max_u32 v151, v151, v136\n\t"
        "v_xor_b32 v137, v181, v137\n\t"
        "v_mfma_f32_16x16x32_bf16 v[80:83], a[56:59], v[100:103], v[80:83]\n\t"
        "v_cvt_pk_bf16_f32 v117, v14, v15\n\t"
        "v_cvt_pk_bf16_f32 v118, v16, v17\n\t"
        "v_mfma_f32_16x16x32_bf16 v[72:75], a[84:87], v[104:107], v[72:75]\n\t"
        "ds_read_b128 v[100:103], v183 offset:0\n\t"
        "v_cvt_pk_bf16_f32 v119, v18, v19\n\t"
        "v_cvt_pk_bf16_f32 v120, v20, v21\n\t"
        "v_mfma_f32_16x16x32_bf16 v[76:79], a[88:91], v[104:107], v[76:79]\n\t"
        "v_cvt_pk_bf16_f32 v121, v22, v23\n\t"
        "v_mfma_f32_16x16x32_bf16 v[80:83], a[92:95], v[104:107], v[80:83]\n\t"
        "v_permlane16_swap_b32 v116, v118\n\t"
        "s_waitcnt lgkmcnt(4)\n\t"
        "v_mfma_f32_16x16x32_bf16 v[72:75], a[120:123], v[108:111], v[72:75]\n\t"
        "v_permlane16_swap_b32 v117, v119\n\t"
        "v_mfma_f32_16x16x32_bf16 v[76:79], a[124:127], v[108:111], v[76:79]\n\t"
        "buffer_store_dwordx4 v[116:119], v144, s[28:31], s55 offen sc1\n\t"
        "v_mfma_f32_16x16x32_bf16 v[80:83], a[128:131], v[108:111], v[80:83]\n\t"
        "ds_write_b64 v146, v[120:121] offset:2048\n\t"
        "v_mfma_f32_16x16x32_bf16 v[72:75], a[156:159], v[112:115], v[72:75]\n\t"
        "ds_read_b128 v[104:107], v183 offset:64\n\t"
        "s_add_u32 s55, s41, 86016\n\t"
        "v_cvt_pk_bf16_f32 v116, v24, v25\n\t"
        "v_mfma_f32_16x16x32_bf16 v[76:79], a[160:163], v[112:115], v[76:79]\n\t"
        "ds_read_b128 v[108:111], v183 offset:128\n\t"
        "v_add_u32 v183, s9, v177\n\t"
        "v_mfma_f32_16x16x32_bf16 v[80:83], a[164:167], v[112:115], v[80:83]\n\t"
        "v_max_u32 v183, v183, v137\n\t"
        "v_xor_b32 v138, v181, v138\n\t"
        "v_mfma_f32_16x16x32_bf16 v[72:75], a[192:195], v[84:87], v[72:75]\n\t"
        "ds_read_b128 v[112:115], v151 offset:0\n\t"
        "v_cvt_pk_bf16_f32 v117, v26, v27\n\t"
        "v_mfma_f32_16x16x32_bf16 v[76:79], a[196:199], v[84:87], v[76:79]\n\t"
        "v_cvt_pk_bf16_f32 v118, v28, v29\n\t"
        "v_cvt_pk_bf16_f32 v119, v30, v31\n\t"
        "v_cvt_pk_bf16_f32 v120, v32, v33\n\t"
        "v_mfma_f32_16x16x32_bf16 v[80:83], a[200:203], v[84:87], v[80:83]\n\t"
        "v_cvt_pk_bf16_f32 v121, v34, v35\n\t"
        "v_mfma_f32_16x16x32_bf16 v[72:75], a[228:231], v[88:91], v[72:75]\n\t"
        "v_permlane16_swap_b32 v116, v118\n\t"
        "v_mfma_f32_16x16x32_bf16 v[76:79], a[232:235], v[88:91], v[76:79]\n\t"
        "v_permlane16_swap_b32 v117, v119\n\t"
        "v_mfma_f32_16x16x32_bf16 v[80:83], a[236:239], v[88:91], v[80:83]\n\t"
        "buffer_store_dwordx4 v[116:119], v144, s[28:31], s55 offen sc1\n\t"
        "s_waitcnt lgkmcnt(2)\n\t"
        "v_mfma_f32_16x16x32_bf16 v[72:75], v[196:199], v[92:95], v[72:75]\n\t"
        "ds_write_b64 v146, v[120:121] offset:4096\n\t"
        "v_mfma_f32_16x16x32_bf16 v[76:79], v[200:203], v[92:95], v[76:79]\n\t"
        "ds_read_b128 v[84:87], v151 offset:64\n\t"
        "s_add_u32 s55, s41, 129024\n\t"
        "v_cvt_pk_bf16_f32 v116, v36, v37\n\t"
        "v_mfma_f32_16x16x32_bf16 v[80:83], v[204:207], v[92:95], v[80:83]\n\t"
        "ds_read_b128 v[88:91], v151 offset:128\n\t"
        "v_add_u32 v151, s9, v178\n\t"
        "v_mfma_f32_16x16x32_bf16 v[72:75], v[232:235], v[96:99], v[72:75]\n\t"
        "ds_read_b128 v[92:95], v183 offset:0\n\t"
        "v_max_u32 v151, v151, v138\n\t"
        "v_mfma_f32_16x16x32_bf16 v[76:79], v[236:239], v[96:99], v[76:79]\n\t"
        "v_xor_b32 v139, v181, v139\n\t"
        "v_cvt_pk_bf16_f32 v117, v38, v39\n\t"
        "v_mfma_f32_16x16x32_bf16 v[80:83], v[240:243], v[96:99], v[80:83]\n\t"
        "v_cvt_pk_bf16_f32 v118, v40, v41\n\t"
        "v_cvt_pk_bf16_f32 v119, v42, v43\n\t"
        "v_cvt_pk_bf16_f32 v120, v44, v45\n\t"
        "v_mfma_f32_16x16x32_bf16 v[72:75], a[0:3], v[100:103], v[72:75]\n\t"
        "ds_read_b128 v[96:99], v183 offset:64\n\t"
        "v_cvt_pk_bf16_f32 v121, v46, v47\n\t"
        "v_mfma_f32_16x16x32_bf16 v[76:79], a[4:7], v[100:103], v[76:79]\n\t"
        "v_permlane16_swap_b32 v116, v118\n\t"
        "v_mfma_f32_16x16x32_bf16 v[80:83], a[8:11], v[100:103], v[80:83]\n\t"
        "v_permlane16_swap_b32 v117, v119\n\t"
        "v_mfma_f32_16x16x32_bf16 v[72:75], a[36:39], v[104:107], v[72:75]\n\t"
        "buffer_store_dwordx4 v[116:119], v144, s[28:31], s55 offen sc1\n\t"
        "v_mfma_f32_16x16x32_bf16 v[76:79], a[40:43], v[104:107], v[76:79]\n\t"
        "ds_write_b64 v146, v[120:121] offset:6144\n\t"
        "v_mfma_f32_16x16x32_bf16 v[80:83], a[44:47], v[104:107], v[80:83]\n\t"
        "ds_read_b128 v[100:103], v183 offset:128\n\t"
        "v_add_u32 v183, s9, v179\n\t"
        "v_max_u32 v183, v183, v139\n\t"
        "s_waitcnt lgkmcnt(3)\n\t"
        "v_mfma_f32_16x16x32_bf16 v[72:75], a[72:75], v[108:111], v[72:75]\n\t"
        "ds_read_b128 v[104:107], v151 offset:0\n\t"
        "v_xor_b32 v140, v181, v140\n\t"
        "v_mfma_f32_16x16x32_bf16 v[76:79], a[76:79], v[108:111], v[76:79]\n\t"
        "v_mfma_f32_16x16x32_bf16 v[80:83], a[80:83], v[108:111], v[80:83]\n\t"
        "v_mfma_f32_16x16x32_bf16 v[72:75], a[108:111], v[112:115], v[72:75]\n\t"
        "ds_read_b128 v[108:111], v151 offset:64\n\t"
        "v_mfma_f32_16x16x32_bf16 v[76:79], a[112:115], v[112:115], v[76:79]\n\t"
        "s_waitcnt lgkmcnt(3)\n\t"
        "s_barrier\n\t"
        "v_mfma_f32_16x16x32_bf16 v[80:83], a[116:119], v[112:115], v[80:83]\n\t"
        "ds_read_b128 v[124:127], v147 offset:0\n\t"
        "v_mfma_f32_16x16x32_bf16 v[72:75], a[144:147], v[84:87], v[72:75]\n\t"
        "ds_read_b128 v[112:115], v151 offset:128\n\t"
        "v_add_u32 v151, s9, v180\n\t"
        "v_max_u32 v151, v151, v140\n\t"
        "v_mfma_f32_16x16x32_bf16 v[76:79], a[148:151], v[84:87], v[76:79]\n\t"
        "v_mfma_f32_16x16x32_bf16 v[80:83], a[152:155], v[84:87], v[80:83]\n\t"
        "ds_read_b128 v[84:87], v183 offset:0\n\t"
        "v_mfma_f32_16x16x32_bf16 v[72:75], a[180:183], v[88:91], v[72:75]\n\t"
        "v_mfma_f32_16x16x32_bf16 v[76:79], a[184:187], v[88:91], v[76:79]\n\t"
        "v_mfma_f32_16x16x32_bf16 v[80:83], a[188:191], v[88:91], v[80:83]\n\t"
        "ds_read_b128 v[88:91], v183 offset:64\n\t"
        "v_mfma_f32_16x16x32_bf16 v[72:75], a[216:219], v[92:95], v[72:75]\n\t"
        ".LpY2%=:\n\t"
        "s_cbranch_execz .LrY%=\n\t"
        "v_mfma_f32_16x16x32_bf16 v[76:79], a[220:223], v[92:95], v[76:79]\n\t"
        "v_mfma_f32_16x16x32_bf16 v[80:83], a[224:227], v[92:95], v[80:83]\n\t"
        "ds_read_b128 v[92:95], v183 offset:128\n\t"
        "v_mfma_f32_16x16x32_bf16 v[72:75], a[252:255], v[96:99], v[72:75]\n\t"
        "s_waitcnt lgkmcnt(4)\n\t"
        "buffer_store_dwordx4 v[124:127], v145, s[28:31], s50 offen sc1\n\t"
        "v_mfma_f32_16x16x32_bf16 v[76:79], v[188:191], v[96:99], v[76:79]\n\t"
        "ds_read_b128 v[124:127], v147 offset:4096\n\t"
        "s_add_u32 s50, s50, 86016\n\t"
        "v_mfma_f32_16x16x32_bf16 v[80:83], v[192:195], v[96:99], v[80:83]\n\t"
        "ds_read_b128 v[96:99], v151 offset:0\n\t"
        "v_mfma_f32_16x16x32_bf16 v[72:75], v[220:223], v[100:103], v[72:75]\n\t"
        "v_mfma_f32_16x16x32_bf16 v[76:79], v[224:227], v[100:103], v[76:79]\n\t"
        "v_mfma_f32_16x16x32_bf16 v[80:83], v[228:231], v[100:103], v[80:83]\n\t"
        "ds_read_b128 v[100:103], v151 offset:64\n\t"
        "v_mfma_f32_16x16x32_bf16 v[72:75], a[24:27], v[104:107], v[72:75]\n\t"
        "v_mfma_f32_16x16x32_bf16 v[76:79], a[28:31], v[104:107], v[76:79]\n\t"
        "v_mfma_f32_16x16x32_bf16 v[80:83], a[32:35], v[104:107], v[80:83]\n\t"
        "ds_read_b128 v[104:107], v151 offset:128\n\t"
        "v_mfma_f32_16x16x32_bf16 v[80:83], a[68:71], v[108:111], v[80:83]\n\t"
        "s_waitcnt lgkmcnt(4)\n\t"
        "v_mfma_f32_16x16x32_bf16 v[80:83], a[104:107], v[112:115], v[80:83]\n\t"
        "s_waitcnt lgkmcnt(3)\n\t"
        "buffer_store_dwordx4 v[124:127], v145, s[28:31], s50 offen sc1\n\t"
        "v_mfma_f32_16x16x32_bf16 v[80:83], a[140:143], v[84:87], v[80:83]\n\t"
        "v_mfma_f32_16x16x32_bf16 v[80:83], a[176:179], v[88:91], v[80:83]\n\t"
        "v_mfma_f32_16x16x32_bf16 v[80:83], a[212:215], v[92:95], v[80:83]\n\t"
        "s_waitcnt lgkmcnt(2)\n\t"
        "v_mfma_f32_16x16x32_bf16 v[80:83], a[248:251], v[96:99], v[80:83]\n\t"
        "s_waitcnt lgkmcnt(1)\n\t"
        "v_mfma_f32_16x16x32_bf16 v[80:83], v[216:219], v[100:103], v[80:83]\n\t"
        "s_waitcnt lgkmcnt(0)\n\t"
        "v_mfma_f32_16x16x32_bf16 v[80:83], v[252:255], v[104:107], v[80:83]\n\t"
        "s_nop 2\n\t"
        "v_mfma_f32_16x16x32_bf16 v[72:75], a[60:63], v[108:111], v[72:75]\n\t"
        "s_nop 2\n\t"
        "v_mfma_f32_16x16x32_bf16 v[72:75], a[96:99], v[112:115], v[72:75]\n\t"
        "v_cvt_pk_bf16_f32 v120, v80, v81\n\t"
        "v_cvt_pk_bf16_f32 v121, v82, v83\n\t"
        "v_mfma_f32_16x16x32_bf16 v[72:75], a[132:135], v[84:87], v[72:75]\n\t"
        "ds_write_b64 v146, v[120:121] offset:10240\n\t"
        "v_mfma_f32_16x16x32_bf16 v[72:75], a[168:171], v[88:91], v[72:75]\n\t"
        "v_mfma_f32_16x16x32_bf16 v[72:75], a[204:207], v[92:95], v[72:75]\n\t"
        "v_mfma_f32_16x16x32_bf16 v[72:75], a[240:243], v[96:99], v[72:75]\n\t"
        "s_waitcnt lgkmcnt(0)\n\t"
        "s_barrier\n\t"
        "v_readlane_b32 s55, v155, 51\n\t"
        "v_mfma_f32_16x16x32_bf16 v[72:75], v[208:211], v[100:103], v[72:75]\n\t"
        "ds_read_b128 v[124:127], v147 offset:8192\n\t"
        "v_mfma_f32_16x16x32_bf16 v[72:75], v[244:247], v[104:107], v[72:75]\n\t"
        "s_nop 1\n\t"
        "v_mfma_f32_16x16x32_bf16 v[76:79], a[64:67], v[108:111], v[76:79]\n\t"
        "s_nop 1\n\t"
        "v_mfma_f32_16x16x32_bf16 v[76:79], a[100:103], v[112:115], v[76:79]\n\t"
        "v_mfma_f32_16x16x32_bf16 v[76:79], a[136:139], v[84:87], v[76:79]\n\t"
        "v_mfma_f32_16x16x32_bf16 v[76:79], a[172:175], v[88:91], v[76:79]\n\t"
        "v_mfma_f32_16x16x32_bf16 v[76:79], a[208:211], v[92:95], v[76:79]\n\t"
        "v_cvt_pk_bf16_f32 v116, v72, v73\n\t"
        "v_cvt_pk_bf16_f32 v117, v74, v75\n\t"
        "v_mfma_f32_16x16x32_bf16 v[76:79], a[244:247], v[96:99], v[76:79]\n\t"
        "s_waitcnt lgkmcnt(0)\n\t"
        "buffer_store_dwordx4 v[124:127], v150, s[28:31], s52 offen sc1\n\t"
        "v_mfma_f32_16x16x32_bf16 v[76:79], v[212:215], v[100:103], v[76:79]\n\t"
        "v_mfma_f32_16x16x32_bf16 v[76:79], v[248:251], v[104:107], v[76:79]\n\t"
        "s_nop 7\n\t"
        "v_cvt_pk_bf16_f32 v118, v76, v77\n\t"
        "v_cvt_pk_bf16_f32 v119, v78, v79\n\t"
        "s_nop 0\n\t"
        "v_permlane16_swap_b32 v116, v118\n\t"
        "v_permlane16_swap_b32 v117, v119\n\t"
        "buffer_store_dwordx4 v[116:119], v148, s[28:31], s55 offen sc1\n\t"
        "s_andn2_b32 s6, s6, 6\n\t"
        "s_cbranch_scc1 99f\n\t"
        "s_endpgm\n\t"
        ".Lx50%=:\n\t"
        "v_mov_b32 v151, v185\n\t"
        "v_mov_b32 v183, v186\n\t"
        "s_min_u32 s8, s3, 64\n\t"
        "s_waitcnt lgkmcnt(0)\n\t"
        "30:\n\t"
        "ds_read_b128 v[160:163], v185\n\t"
        "s_waitcnt lgkmcnt(0)\n\t"
        "v_lshlrev_b32_sdwa v157, 5, v160 dst_sel:DWORD dst_unused:UNUSED_PAD src0_sel:DWORD src1_sel:WORD_0\n\t"
        "v_lshlrev_b32_sdwa v158, 5, v160 dst_sel:DWORD dst_unused:UNUSED_PAD src0_sel:DWORD src1_sel:WORD_1\n\t"
        "v_or_b32 v157, v184, v157\n\t"
        "v_or_b32 v158, v184, v158\n\t"
        "v_xor_b32 v159, 32, v157\n\t"
        "v_xor_b32 v152, 32, v158\n\t"
        "ds_read_b64_tr_b16 v[60:61], v157 offset:0\n\t"
        "ds_read_b64_tr_b16 v[62:63], v158 offset:0\n\t"
        "ds_read_b64_tr_b16 v[64:65], v159 offset:0\n\t"
        "ds_read_b64_tr_b16 v[66:67], v152 offset:0\n\t"
        "ds_read_b64_tr_b16 v[68:69], v157 offset:64\n\t"
        "ds_read_b64_tr_b16 v[70:71], v158 offset:64\n\t"
        "ds_read_b64_tr_b16 v[72:73], v159 offset:64\n\t"
        "ds_read_b64_tr_b16 v[74:75], v152 offset:64\n\t"
        "ds_read_b64_tr_b16 v[76:77], v157 offset:128\n\t"
        "ds_read_b64_tr_b16 v[78:79], v158 offset:128\n\t"
        "ds_read_b64_tr_b16 v[80:81], v159 offset:128\n\t"
        "ds_read_b64_tr_b16 v[82:83], v152 offset:128\n\t"
        "v_perm_b32 v164, v162, v162, v168\n\t"
        "v_perm_b32 v165, v162, v162, v169\n\t"
        "v_perm_b32 v166, v162, v162, v170\n\t"
        "v_perm_b32 v167, v162, v162, v171\n\t"
        "v_cmp_ne_u32 vcc, 0, v163\n\t"
        "s_waitcnt lgkmcnt(10)\n\t"
        "v_mfma_f32_16x16x32_bf16 v[48:51], v[60:63], v[164:167], 0\n\t"
        "s_waitcnt lgkmcnt(8)\n\t"
        "v_mfma_f32_16x16x32_bf16 v[52:55], v[64:67], v[164:167], 0\n\t"
        "s_waitcnt lgkmcnt(6)\n\t"
        "v_mfma_f32_16x16x32_bf16 v[56:59], v[68:71], v[164:167], 0\n\t"
        "s_waitcnt lgkmcnt(4)\n\t"
        "v_mfma_f32_16x16x32_bf16 v[116:119], v[72:75], v[164:167], 0\n\t"
        "s_waitcnt lgkmcnt(2)\n\t"
        "v_mfma_f32_16x16x32_bf16 v[120:123], v[76:79], v[164:167], 0\n\t"
        "s_waitcnt lgkmcnt(0)\n\t"
        "v_mfma_f32_16x16x32_bf16 v[124:127], v[80:83], v[164:167], 0\n\t"
        "s_cbranch_vccz 31f\n\t"
        "v_lshlrev_b32_sdwa v157, 5, v161 dst_sel:DWORD dst_unused:UNUSED_PAD src0_sel:DWORD src1_sel:WORD_0\n\t"
        "v_lshlrev_b32_sdwa v158, 5, v161 dst_sel:DWORD dst_unused:UNUSED_PAD src0_sel:DWORD src1_sel:WORD_1\n\t"
        "v_or_b32 v157, v184, v157\n\t"
        "v_or_b32 v158, v184, v158\n\t"
        "v_xor_b32 v159, 32, v157\n\t"
        "v_xor_b32 v152, 32, v158\n\t"
        "ds_read_b64_tr_b16 v[60:61], v157 offset:0\n\t"
        "ds_read_b64_tr_b16 v[62:63], v158 offset:0\n\t"
        "ds_read_b64_tr_b16 v[64:65], v159 offset:0\n\t"
        "ds_read_b64_tr_b16 v[66:67], v152 offset:0\n\t"
        "ds_read_b64_tr_b16 v[68:69], v157 offset:64\n\t"
        "ds_read_b64_tr_b16 v[70:71], v158 offset:64\n\t"
        "ds_read_b64_tr_b16 v[72:73], v159 offset:64\n\t"
        "ds_read_b64_tr_b16 v[74:75], v152 offset:64\n\t"
        "ds_read_b64_tr_b16 v[76:77], v157 offset:128\n\t"
        "ds_read_b64_tr_b16 v[78:79], v158 offset:128\n\t"
        "ds_read_b64_tr_b16 v[80:81], v159 offset:128\n\t"
        "ds_read_b64_tr_b16 v[82:83], v152 offset:128\n\t"
        "v_perm_b32 v164, v163, v163, v168\n\t"
        "v_perm_b32 v165, v163, v163, v169\n\t"
        "v_perm_b32 v166, v163, v163, v170\n\t"
        "v_perm_b32 v167, v163, v163, v171\n\t"
        "s_waitcnt lgkmcnt(10)\n\t"
        "s_nop 0\n\t"
        "v_mfma_f32_16x16x32_bf16 v[48:51], v[60:63], v[164:167], v[48:51]\n\t"
        "s_waitcnt lgkmcnt(8)\n\t"
        "v_mfma_f32_16x16x32_bf16 v[52:55], v[64:67], v[164:167], v[52:55]\n\t"
        "s_waitcnt lgkmcnt(6)\n\t"
        "v_mfma_f32_16x16x32_bf16 v[56:59], v[68:71], v[164:167], v[56:59]\n\t"
        "s_waitcnt lgkmcnt(4)\n\t"
        "v_mfma_f32_16x16x32_bf16 v[116:119], v[72:75], v[164:167], v[116:119]\n\t"
        "s_waitcnt lgkmcnt(2)\n\t"
        "v_mfma_f32_16x16x32_bf16 v[120:123], v[76:79], v[164:167], v[120:123]\n\t"
        "s_waitcnt lgkmcnt(0)\n\t"
        "v_mfma_f32_16x16x32_bf16 v[124:127], v[80:83], v[164:167], v[124:127]\n\t"
        "31:\n\t"
        "v_cvt_pk_bf16_f32 v48, v48, v49\n\t"
        "v_cvt_pk_bf16_f32 v49, v50, v51\n\t"
        "v_cvt_pk_bf16_f32 v52, v52, v53\n\t"
        "v_cvt_pk_bf16_f32 v53, v54, v55\n\t"
        "v_cvt_pk_bf16_f32 v56, v56, v57\n\t"
        "v_cvt_pk_bf16_f32 v57, v58, v59\n\t"
        "v_cvt_pk_bf16_f32 v116, v116, v117\n\t"
        "v_cvt_pk_bf16_f32 v117, v118, v119\n\t"
        "v_cvt_pk_bf16_f32 v120, v120, v121\n\t"
        "v_cvt_pk_bf16_f32 v121, v122, v123\n\t"
        "v_cvt_pk_bf16_f32 v124, v124, v125\n\t"
        "v_cvt_pk_bf16_f32 v125, v126, v127\n\t"
        "ds_write_b64 v186, v[48:49] offset:0\n\t"
        "ds_write_b64 v154, v[52:53] offset:0\n\t"
        "ds_write_b64 v186, v[56:57] offset:64\n\t"
        "ds_write_b64 v154, v[116:117] offset:64\n\t"
        "ds_write_b64 v186, v[120:121] offset:128\n\t"
        "ds_write_b64 v154, v[124:125] offset:128\n\t"
        "v_add_u32 v185, 256, v185\n\t"
        "v_add_u32 v186, 12288, v186\n\t"
        "v_xor_b32 v154, 32, v186\n\t"
        "s_sub_i32 s8, s8, 16\n\t"
        "s_cmp_gt_i32 s8, 0\n\t"
        "s_cbranch_scc1 30b\n\t"
        "v_mov_b32 v185, v151\n\t"
        "v_mov_b32 v186, v183\n\t"
        "v_xor_b32 v154, 32, v186\n\t"
        "s_waitcnt lgkmcnt(0)\n\t"
        "s_barrier\n\t"
        "s_branch .Lx51%=\n\t"
        ".LwM%=:\n\t"
        "s_branch .LrM%=\n\t"
        ".Lx54%=:\n\t"
        "v_add_u32 v185, 768, v185\n\t"
        "v_add_u32 v186, 36864, v186\n\t"
        "v_xor_b32 v154, 32, v186\n\t"
        "s_min_u32 s47, s61, 64\n\t"
        "s_sub_u32 s47, s47, 48\n\t"
        "s_waitcnt lgkmcnt(0)\n\t"
        "40:\n\t"
        "ds_read_b128 v[160:163], v185\n\t"
        "s_waitcnt lgkmcnt(0)\n\t"
        "v_lshlrev_b32_sdwa v157, 5, v160 dst_sel:DWORD dst_unused:UNUSED_PAD src0_sel:DWORD src1_sel:WORD_0\n\t"
        "v_lshlrev_b32_sdwa v158, 5, v160 dst_sel:DWORD dst_unused:UNUSED_PAD src0_sel:DWORD src1_sel:WORD_1\n\t"
        "v_or_b32 v157, v184, v157\n\t"
        "v_or_b32 v158, v184, v158\n\t"
        "v_xor_b32 v159, 32, v157\n\t"
        "v_xor_b32 v152, 32, v158\n\t"
        "ds_read_b64_tr_b16 v[60:61], v157 offset:0\n\t"
        "ds_read_b64_tr_b16 v[62:63], v158 offset:0\n\t"
        "ds_read_b64_tr_b16 v[64:65], v159 offset:0\n\t"
        "ds_read_b64_tr_b16 v[66:67], v152 offset:0\n\t"
        "ds_read_b64_tr_b16 v[68:69], v157 offset:64\n\t"
        "ds_read_b64_tr_b16 v[70:71], v158 offset:64\n\t"
        "ds_read_b64_tr_b16 v[72:73], v159 offset:64\n\t"
        "ds_read_b64_tr_b16 v[74:75], v152 offset:64\n\t"
        "ds_read_b64_tr_b16 v[76:77], v157 offset:128\n\t"
        "ds_read_b64_tr_b16 v[78:79], v158 offset:128\n\t"
        "ds_read_b64_tr_b16 v[80:81], v159 offset:128\n\t"
        "ds_read_b64_tr_b16 v[82:83], v152 offset:128\n\t"
        "v_perm_b32 v164, v162, v162, v168\n\t"
        "v_perm_b32 v165, v162, v162, v169\n\t"
        "v_perm_b32 v166, v162, v162, v170\n\t"
        "v_perm_b32 v167, v162, v162, v171\n\t"
        "v_cmp_ne_u32 vcc, 0, v163\n\t"
        "s_waitcnt lgkmcnt(10)\n\t"
        "v_mfma_f32_16x16x32_bf16 v[0:3], v[60:63], v[164:167], 0\n\t"
        "s_waitcnt lgkmcnt(8)\n\t"
        "v_mfma_f32_16x16x32_bf16 v[4:7], v[64:67], v[164:167], 0\n\t"
        "s_waitcnt lgkmcnt(6)\n\t"
        "v_mfma_f32_16x16x32_bf16 v[8:11], v[68:71], v[164:167], 0\n\t"
        "s_waitcnt lgkmcnt(4)\n\t"
        "v_mfma_f32_16x16x32_bf16 v[12:15], v[72:75], v[164:167], 0\n\t"
        "s_waitcnt lgkmcnt(2)\n\t"
        "v_mfma_f32_16x16x32_bf16 v[16:19], v[76:79], v[164:167], 0\n\t"
        "s_waitcnt lgkmcnt(0)\n\t"
        "v_mfma_f32_16x16x32_bf16 v[20:23], v[80:83], v[164:167], 0\n\t"
        "s_cbranch_vccz 41f\n\t"
        "v_lshlrev_b32_sdwa v157, 5, v161 dst_sel:DWORD dst_unused:UNUSED_PAD src0_sel:DWORD src1_sel:WORD_0\n\t"
        "v_lshlrev_b32_sdwa v158, 5, v161 dst_sel:DWORD dst_unused:UNUSED_PAD src0_sel:DWORD src1_sel:WORD_1\n\t"
        "v_or_b32 v157, v184, v157\n\t"
        "v_or_b32 v158, v184, v158\n\t"
        "v_xor_b32 v159, 32, v157\n\t"
        "v_xor_b32 v152, 32, v158\n\t"
        "ds_read_b64_tr_b16 v[60:61], v157 offset:0\n\t"
        "ds_read_b64_tr_b16 v[62:63], v158 offset:0\n\t"
        "ds_read_b64_tr_b16 v[64:65], v159 offset:0\n\t"
        "ds_read_b64_tr_b16 v[66:67], v152 offset:0\n\t"
        "ds_read_b64_tr_b16 v[68:69], v157 offset:64\n\t"
        "ds_read_b64_tr_b16 v[70:71], v158 offset:64\n\t"
        "ds_read_b64_tr_b16 v[72:73], v159 offset:64\n\t"
        "ds_read_b64_tr_b16 v[74:75], v152 offset:64\n\t"
        "ds_read_b64_tr_b16 v[76:77], v157 offset:128\n\t"
        "ds_read_b64_tr_b16 v[78:79], v158 offset:128\n\t"
        "ds_read_b64_tr_b16 v[80:81], v159 offset:128\n\t"
        "ds_read_b64_tr_b16 v[82:83], v152 offset:128\n\t"
        "v_perm_b32 v164, v163, v163, v168\n\t"
        "v_perm_b32 v165, v163, v163, v169\n\t"
        "v_perm_b32 v166, v163, v163, v170\n\t"
        "v_perm_b32 v167, v163, v163, v171\n\t"
        "s_waitcnt lgkmcnt(10)\n\t"
        "s_nop 0\n\t"
        "v_mfma_f32_16x16x32_bf16 v[0:3], v[60:63], v[164:167], v[0:3]\n\t"
        "s_waitcnt lgkmcnt(8)\n\t"
        "v_mfma_f32_16x16x32_bf16 v[4:7], v[64:67], v[164:167], v[4:7]\n\t"
        "s_waitcnt lgkmcnt(6)\n\t"
        "v_mfma_f32_16x16x32_bf16 v[8:11], v[68:71], v[164:167], v[8:11]\n\t"
        "s_waitcnt lgkmcnt(4)\n\t"
        "v_mfma_f32_16x16x32_bf16 v[12:15], v[72:75], v[164:167], v[12:15]\n\t"
        "s_waitcnt lgkmcnt(2)\n\t"
        "v_mfma_f32_16x16x32_bf16 v[16:19], v[76:79], v[164:167], v[16:19]\n\t"
        "s_waitcnt lgkmcnt(0)\n\t"
        "v_mfma_f32_16x16x32_bf16 v[20:23], v[80:83], v[164:167], v[20:23]\n\t"
        "41:\n\t"
        "v_cvt_pk_bf16_f32 v0, v0, v1\n\t"
        "v_cvt_pk_bf16_f32 v1, v2, v3\n\t"
        "v_cvt_pk_bf16_f32 v4, v4, v5\n\t"
        "v_cvt_pk_bf16_f32 v5, v6, v7\n\t"
        "v_cvt_pk_bf16_f32 v8, v8, v9\n\t"
        "v_cvt_pk_bf16_f32 v9, v10, v11\n\t"
        "v_cvt_pk_bf16_f32 v12, v12, v13\n\t"
        "v_cvt_pk_bf16_f32 v13, v14, v15\n\t"
        "v_cvt_pk_bf16_f32 v16, v16, v17\n\t"
        "v_cvt_pk_bf16_f32 v17, v18, v19\n\t"
        "v_cvt_pk_bf16_f32 v20, v20, v21\n\t"
        "v_cvt_pk_bf16_f32 v21, v22, v23\n\t"
        "ds_write_b64 v186, v[0:1] offset:0\n\t"
        "ds_write_b64 v154, v[4:5] offset:0\n\t"
        "ds_write_b64 v186, v[8:9] offset:64\n\t"
        "ds_write_b64 v154, v[12:13] offset:64\n\t"
        "ds_write_b64 v186, v[16:17] offset:128\n\t"
        "ds_write_b64 v154, v[20:21] offset:128\n\t"
        "v_add_u32 v185, 256, v185\n\t"
        "v_add_u32 v186, 12288, v186\n\t"
        "v_xor_b32 v154, 32, v186\n\t"
        "s_sub_i32 s47, s47, 16\n\t"
        "s_cmp_gt_i32 s47, 0\n\t"
        "s_cbranch_scc1 40b\n\t"
        "s_cmp_le_u32 s61, 64\n\t"
        "s_cbranch_scc1 52f\n\t"
        "v_bfe_u32 v64, v185, 4, 4\n\t"
        "v_add_u32 v60, 65, v64\n\t"
        "s_sub_u32 s8, s37, 12288\n\t"
        "v_mov_b32 v63, s62\n\t"
        "v_lshlrev_b32 v65, 3, v64\n\t"
        "v_mul_u32_u24 v64, 192, v64\n\t"
        "v_and_or_b32 v64, v65, 48, v64\n\t"
        "v_add_u32 v61, s8, v64\n\t"
        "v_subrev_u32 v186, 61440, v186\n\t"
        "v_xor_b32 v154, 32, v186\n\t"
        "v_min_u32 v16, s61, v60\n\t"
        "v_lshl_add_u32 v16, v16, 3, s60\n\t"
        "ds_read_b64 v[0:1], v16\n\t"
        "v_cmp_ge_u32 s[66:67], s61, v60\n\t"
        "s_mov_b32 s46, 0x07060302\n\t"
        "s_waitcnt lgkmcnt(0)\n\t"
        "v_and_b32 v16, 0xfffff800, v1\n\t"
        "ds_read_b128 v[2:5], v16 offset:1536\n\t"
        "v_lshlrev_b32 v10, 16, v0\n\t"
        "v_and_b32 v11, 0xffff0000, v0\n\t"
        "v_med3_f32 v10, v10, 0, s63\n\t"
        "v_med3_f32 v11, v11, 0, s63\n\t"
        "s_mov_b64 exec, s[66:67]\n\t"
        "ds_write_b32 v1, v61\n\t"
        "s_mov_b64 exec, -1\n\t"
        "v_perm_b32 v0, v11, v10, s46\n\t"
        "v_fract_f32 v12, v10\n\t"
        "v_fract_f32 v13, v11\n\t"
        "v_cvt_u32_f32 v14, v10\n\t"
        "v_cvt_u32_f32 v15, v11\n\t"
        "v_cmp_neq_f32 s[64:65], 0, v12\n\t"
        "v_cmp_neq_f32 s[44:45], 0, v13\n\t"
        "v_sub_f32 v16, 1.0, v12\n\t"
        "v_sub_f32 v17, 1.0, v13\n\t"
        "v_cvt_pk_bf16_f32 v17, v16, v17\n\t"
        "v_lshlrev_b32 v16, 16, v17\n\t"
        "v_and_b32 v17, 0xffff0000, v17\n\t"
        "v_mul_f32 v18, v16, v17\n\t"
        "v_mul_f32 v16, v16, v13\n\t"
        "v_mul_f32 v17, v12, v17\n\t"
        "v_mul_f32 v19, v12, v13\n\t"
        "v_cndmask_b32 v16, v17, v16, s[44:45]\n\t"
        "v_cndmask_b32 v17, 0, v17, s[44:45]\n\t"
        "v_cvt_pk_bf16_f32 v162, v18, v16\n\t"
        "v_cvt_pk_bf16_f32 v163, v17, v19\n\t"
        "s_waitcnt lgkmcnt(1)\n\t"
        "v_pk_max_u16 v16, v0, v2\n\t"
        "v_pk_min_u16 v16, v16, v3\n\t"
        "v_cmp_ne_u32 vcc, v16, v0\n\t"
        "v_sub_u32 v16, v15, v5\n\t"
        "v_mad_u32_u24 v17, v15, 6, v4\n\t"
        "v_mad_u32_u24 v17, v14, s62, v17\n\t"
        "s_and_b64 vcc, vcc, s[66:67]\n\t"
        "s_or_b64 s[56:57], s[56:57], vcc\n\t"
        "v_bfe_u32 v18, v16, 2, 1\n\t"
        "v_or_b32 v18, v18, v17\n\t"
        "v_add_u32 v16, 1, v16\n\t"
        "v_bfe_u32 v16, v16, 2, 1\n\t"
        "v_add3_u32 v16, v17, v16, 6\n\t"
        "v_cndmask_b32 v16, v18, v16, s[44:45]\n\t"
        "v_cndmask_b32 v17, 0, v63, s[64:65]\n\t"
        "v_add_u32 v19, v18, v17\n\t"
        "v_add_u32 v20, v16, v17\n\t"
        "v_cndmask_b32 v16, v19, v16, s[44:45]\n\t"
        "v_lshl_or_b32 v160, v16, 16, v18\n\t"
        "v_lshl_or_b32 v161, v20, 16, v19\n\t"
        "v_lshlrev_b32_sdwa v157, 5, v160 dst_sel:DWORD dst_unused:UNUSED_PAD src0_sel:DWORD src1_sel:WORD_0\n\t"
        "v_lshlrev_b32_sdwa v158, 5, v160 dst_sel:DWORD dst_unused:UNUSED_PAD src0_sel:DWORD src1_sel:WORD_1\n\t"
        "v_or_b32 v157, v184, v157\n\t"
        "v_or_b32 v158, v184, v158\n\t"
        "v_xor_b32 v159, 32, v157\n\t"
        "v_xor_b32 v152, 32, v158\n\t"
        "ds_read_b64_tr_b16 v[60:61], v157 offset:0\n\t"
        "ds_read_b64_tr_b16 v[62:63], v158 offset:0\n\t"
        "ds_read_b64_tr_b16 v[64:65], v159 offset:0\n\t"
        "ds_read_b64_tr_b16 v[66:67], v152 offset:0\n\t"
        "ds_read_b64_tr_b16 v[68:69], v157 offset:64\n\t"
        "ds_read_b64_tr_b16 v[70:71], v158 offset:64\n\t"
        "ds_read_b64_tr_b16 v[72:73], v159 offset:64\n\t"
        "ds_read_b64_tr_b16 v[74:75], v152 offset:64\n\t"
        "ds_read_b64_tr_b16 v[76:77], v157 offset:128\n\t"
        "ds_read_b64_tr_b16 v[78:79], v158 offset:128\n\t"
        "ds_read_b64_tr_b16 v[80:81], v159 offset:128\n\t"
        "ds_read_b64_tr_b16 v[82:83], v152 offset:128\n\t"
        "v_lshlrev_b32_sdwa v157, 5, v161 dst_sel:DWORD dst_unused:UNUSED_PAD src0_sel:DWORD src1_sel:WORD_0\n\t"
        "v_lshlrev_b32_sdwa v158, 5, v161 dst_sel:DWORD dst_unused:UNUSED_PAD src0_sel:DWORD src1_sel:WORD_1\n\t"
        "v_or_b32 v157, v184, v157\n\t"
        "v_or_b32 v158, v184, v158\n\t"
        "v_xor_b32 v159, 32, v157\n\t"
        "v_xor_b32 v152, 32, v158\n\t"
        "ds_read_b64_tr_b16 v[24:25], v157 offset:0\n\t"
        "ds_read_b64_tr_b16 v[26:27], v158 offset:0\n\t"
        "ds_read_b64_tr_b16 v[28:29], v159 offset:0\n\t"
        "ds_read_b64_tr_b16 v[30:31], v152 offset:0\n\t"
        "ds_read_b64_tr_b16 v[32:33], v157 offset:64\n\t"
        "ds_read_b64_tr_b16 v[34:35], v158 offset:64\n\t"
        "ds_read_b64_tr_b16 v[36:37], v159 offset:64\n\t"
        "ds_read_b64_tr_b16 v[38:39], v152 offset:64\n\t"
        "ds_read_b64_tr_b16 v[40:41], v157 offset:128\n\t"
        "ds_read_b64_tr_b16 v[42:43], v158 offset:128\n\t"
        "ds_read_b64_tr_b16 v[44:45], v159 offset:128\n\t"
        "ds_read_b64_tr_b16 v[46:47], v152 offset:128\n\t"
        "s_cmp_lg_u64 s[56:57], 0\n\t"
        "s_cselect_b32 s8, 1, 0\n\t"
        "v_mov_b32 v157, s8\n\t"
        "v_perm_b32 v164, v162, v162, v168\n\t"
        "v_perm_b32 v165, v162, v162, v169\n\t"
        "v_perm_b32 v166, v162, v162, v170\n\t"
        "v_perm_b32 v167, v162, v162, v171\n\t"
        "s_waitcnt lgkmcnt(12)\n\t"
        "s_nop 0\n\t"
        "v_mfma_f32_16x16x32_bf16 v[0:3], v[60:63], v[164:167], 0\n\t"
        "v_mfma_f32_16x16x32_bf16 v[4:7], v[64:67], v[164:167], 0\n\t"
        "v_mfma_f32_16x16x32_bf16 v[8:11], v[68:71], v[164:167], 0\n\t"
        "v_mfma_f32_16x16x32_bf16 v[12:15], v[72:75], v[164:167], 0\n\t"
        "v_mfma_f32_16x16x32_bf16 v[16:19], v[76:79], v[164:167], 0\n\t"
        "v_mfma_f32_16x16x32_bf16 v[20:23], v[80:83], v[164:167], 0\n\t"
        "v_perm_b32 v164, v163, v163, v168\n\t"
        "v_perm_b32 v165, v163, v163, v169\n\t"
        "v_perm_b32 v166, v163, v163, v170\n\t"
        "v_perm_b32 v167, v163, v163, v171\n\t"
        "s_waitcnt lgkmcnt(3)\n\t"
        "s_nop 0\n\t"
        "v_mfma_f32_16x16x32_bf16 v[0:3], v[24:27], v[164:167], v[0:3]\n\t"
        "v_mfma_f32_16x16x32_bf16 v[4:7], v[28:31], v[164:167], v[4:7]\n\t"
        "v_mfma_f32_16x16x32_bf16 v[8:11], v[32:35], v[164:167], v[8:11]\n\t"
        "v_mfma_f32_16x16x32_bf16 v[12:15], v[36:39], v[164:167], v[12:15]\n\t"
        "s_waitcnt lgkmcnt(0)\n\t"
        "v_mfma_f32_16x16x32_bf16 v[16:19], v[40:43], v[164:167], v[16:19]\n\t"
        "v_mfma_f32_16x16x32_bf16 v[20:23], v[44:47], v[164:167], v[20:23]\n\t"
        "ds_write_b32 v153, v157 offset:16\n\t"
        "s_nop 0\n\t"
        "v_cvt_pk_bf16_f32 v0, v0, v1\n\t"
        "v_cvt_pk_bf16_f32 v1, v2, v3\n\t"
        "v_cvt_pk_bf16_f32 v4, v4, v5\n\t"
        "v_cvt_pk_bf16_f32 v5, v6, v7\n\t"
        "v_cvt_pk_bf16_f32 v8, v8, v9\n\t"
        "v_cvt_pk_bf16_f32 v9, v10, v11\n\t"
        "v_cvt_pk_bf16_f32 v12, v12, v13\n\t"
        "v_cvt_pk_bf16_f32 v13, v14, v15\n\t"
        "v_cvt_pk_bf16_f32 v16, v16, v17\n\t"
        "v_cvt_pk_bf16_f32 v17, v18, v19\n\t"
        "v_cvt_pk_bf16_f32 v20, v20, v21\n\t"
        "v_cvt_pk_bf16_f32 v21, v22, v23\n\t"
        "ds_write_b64 v186, v[0:1] offset:0\n\t"
        "ds_write_b64 v154, v[4:5] offset:0\n\t"
        "ds_write_b64 v186, v[8:9] offset:64\n\t"
        "ds_write_b64 v154, v[12:13] offset:64\n\t"
        "ds_write_b64 v186, v[16:17] offset:128\n\t"
        "ds_write_b64 v154, v[20:21] offset:128\n\t"
        "52:\n\t"
        "s_branch .Lx55%=\n\t"
        ".Lx60%=:\n\t"
        "v_mov_b32 v151, v185\n\t"
        "v_mov_b32 v183, v186\n\t"
        "s_min_u32 s8, s3, 64\n\t"
        "s_waitcnt lgkmcnt(0)\n\t"
        "30:\n\t"
        "ds_read_b128 v[160:163], v185\n\t"
        "s_waitcnt lgkmcnt(0)\n\t"
        "v_lshlrev_b32_sdwa v157, 5, v160 dst_sel:DWORD dst_unused:UNUSED_PAD src0_sel:DWORD src1_sel:WORD_0\n\t"
        "v_lshlrev_b32_sdwa v158, 5, v160 dst_sel:DWORD dst_unused:UNUSED_PAD src0_sel:DWORD src1_sel:WORD_1\n\t"
        "v_or_b32 v157, v184, v157\n\t"
        "v_or_b32 v158, v184, v158\n\t"
        "v_xor_b32 v159, 32, v157\n\t"
        "v_xor_b32 v152, 32, v158\n\t"
        "ds_read_b64_tr_b16 v[60:61], v157 offset:0\n\t"
        "ds_read_b64_tr_b16 v[62:63], v158 offset:0\n\t"
        "ds_read_b64_tr_b16 v[64:65], v159 offset:0\n\t"
        "ds_read_b64_tr_b16 v[66:67], v152 offset:0\n\t"
        "ds_read_b64_tr_b16 v[68:69], v157 offset:64\n\t"
        "ds_read_b64_tr_b16 v[70:71], v158 offset:64\n\t"
        "ds_read_b64_tr_b16 v[72:73], v159 offset:64\n\t"
        "ds_read_b64_tr_b16 v[74:75], v152 offset:64\n\t"
        "ds_read_b64_tr_b16 v[76:77], v157 offset:128\n\t"
        "ds_read_b64_tr_b16 v[78:79], v158 offset:128\n\t"
        "ds_read_b64_tr_b16 v[80:81], v159 offset:128\n\t"
        "ds_read_b64_tr_b16 v[82:83], v152 offset:128\n\t"
        "v_perm_b32 v164, v162, v162, v168\n\t"
        "v_perm_b32 v165, v162, v162, v169\n\t"
        "v_perm_b32 v166, v162, v162, v170\n\t"
        "v_perm_b32 v167, v162, v162, v171\n\t"
        "v_cmp_ne_u32 vcc, 0, v163\n\t"
        "s_waitcnt lgkmcnt(10)\n\t"
        "v_mfma_f32_16x16x32_bf16 v[48:51], v[60:63], v[164:167], 0\n\t"
        "s_waitcnt lgkmcnt(8)\n\t"
        "v_mfma_f32_16x16x32_bf16 v[52:55], v[64:67], v[164:167], 0\n\t"
        "s_waitcnt lgkmcnt(6)\n\t"
        "v_mfma_f32_16x16x32_bf16 v[56:59], v[68:71], v[164:167], 0\n\t"
        "s_waitcnt lgkmcnt(4)\n\t"
        "v_mfma_f32_16x16x32_bf16 v[116:119], v[72:75], v[164:167], 0\n\t"
        "s_waitcnt lgkmcnt(2)\n\t"
        "v_mfma_f32_16x16x32_bf16 v[120:123], v[76:79], v[164:167], 0\n\t"
        "s_waitcnt lgkmcnt(0)\n\t"
        "v_mfma_f32_16x16x32_bf16 v[124:127], v[80:83], v[164:167], 0\n\t"
        "s_cbranch_vccz 31f\n\t"
        "v_lshlrev_b32_sdwa v157, 5, v161 dst_sel:DWORD dst_unused:UNUSED_PAD src0_sel:DWORD src1_sel:WORD_0\n\t"
        "v_lshlrev_b32_sdwa v158, 5, v161 dst_sel:DWORD dst_unused:UNUSED_PAD src0_sel:DWORD src1_sel:WORD_1\n\t"
        "v_or_b32 v157, v184, v157\n\t"
        "v_or_b32 v158, v184, v158\n\t"
        "v_xor_b32 v159, 32, v157\n\t"
        "v_xor_b32 v152, 32, v158\n\t"
        "ds_read_b64_tr_b16 v[60:61], v157 offset:0\n\t"
        "ds_read_b64_tr_b16 v[62:63], v158 offset:0\n\t"
        "ds_read_b64_tr_b16 v[64:65], v159 offset:0\n\t"
        "ds_read_b64_tr_b16 v[66:67], v152 offset:0\n\t"
        "ds_read_b64_tr_b16 v[68:69], v157 offset:64\n\t"
        "ds_read_b64_tr_b16 v[70:71], v158 offset:64\n\t"
        "ds_read_b64_tr_b16 v[72:73], v159 offset:64\n\t"
        "ds_read_b64_tr_b16 v[74:75], v152 offset:64\n\t"
        "ds_read_b64_tr_b16 v[76:77], v157 offset:128\n\t"
        "ds_read_b64_tr_b16 v[78:79], v158 offset:128\n\t"
        "ds_read_b64_tr_b16 v[80:81], v159 offset:128\n\t"
        "ds_read_b64_tr_b16 v[82:83], v152 offset:128\n\t"
        "v_perm_b32 v164, v163, v163, v168\n\t"
        "v_perm_b32 v165, v163, v163, v169\n\t"
        "v_perm_b32 v166, v163, v163, v170\n\t"
        "v_perm_b32 v167, v163, v163, v171\n\t"
        "s_waitcnt lgkmcnt(10)\n\t"
        "s_nop 0\n\t"
        "v_mfma_f32_16x16x32_bf16 v[48:51], v[60:63], v[164:167], v[48:51]\n\t"
        "s_waitcnt lgkmcnt(8)\n\t"
        "v_mfma_f32_16x16x32_bf16 v[52:55], v[64:67], v[164:167], v[52:55]\n\t"
        "s_waitcnt lgkmcnt(6)\n\t"
        "v_mfma_f32_16x16x32_bf16 v[56:59], v[68:71], v[164:167], v[56:59]\n\t"
        "s_waitcnt lgkmcnt(4)\n\t"
        "v_mfma_f32_16x16x32_bf16 v[116:119], v[72:75], v[164:167], v[116:119]\n\t"
        "s_waitcnt lgkmcnt(2)\n\t"
        "v_mfma_f32_16x16x32_bf16 v[120:123], v[76:79], v[164:167], v[120:123]\n\t"
        "s_waitcnt lgkmcnt(0)\n\t"
        "v_mfma_f32_16x16x32_bf16 v[124:127], v[80:83], v[164:167], v[124:127]\n\t"
        "31:\n\t"
        "v_cvt_pk_bf16_f32 v48, v48, v49\n\t"
        "v_cvt_pk_bf16_f32 v49, v50, v51\n\t"
        "v_cvt_pk_bf16_f32 v52, v52, v53\n\t"
        "v_cvt_pk_bf16_f32 v53, v54, v55\n\t"
        "v_cvt_pk_bf16_f32 v56, v56, v57\n\t"
        "v_cvt_pk_bf16_f32 v57, v58, v59\n\t"
        "v_cvt_pk_bf16_f32 v116, v116, v117\n\t"
        "v_cvt_pk_bf16_f32 v117, v118, v119\n\t"
        "v_cvt_pk_bf16_f32 v120, v120, v121\n\t"
        "v_cvt_pk_bf16_f32 v121, v122, v123\n\t"
        "v_cvt_pk_bf16_f32 v124, v124, v125\n\t"
        "v_cvt_pk_bf16_f32 v125, v126, v127\n\t"
        "ds_write_b64 v186, v[48:49] offset:0\n\t"
        "ds_write_b64 v154, v[52:53] offset:0\n\t"
        "ds_write_b64 v186, v[56:57] offset:64\n\t"
        "ds_write_b64 v154, v[116:117] offset:64\n\t"
        "ds_write_b64 v186, v[120:121] offset:128\n\t"
        "ds_write_b64 v154, v[124:125] offset:128\n\t"
        "v_add_u32 v185, 256, v185\n\t"
        "v_add_u32 v186, 12288, v186\n\t"
        "v_xor_b32 v154, 32, v186\n\t"
        "s_sub_i32 s8, s8, 16\n\t"
        "s_cmp_gt_i32 s8, 0\n\t"
        "s_cbranch_scc1 30b\n\t"
        "v_mov_b32 v185, v151\n\t"
        "v_mov_b32 v186, v183\n\t"
        "v_xor_b32 v154, 32, v186\n\t"
        "s_waitcnt lgkmcnt(0)\n\t"
        "s_barrier\n\t"
        "s_branch .Lx61%=\n\t"
        ".Lx68%=:\n\t"
        "v_mov_b32 v151, v185\n\t"
        "v_mov_b32 v183, v186\n\t"
        "s_min_u32 s8, s3, 64\n\t"
        "s_waitcnt lgkmcnt(0)\n\t"
        "30:\n\t"
        "ds_read_b128 v[160:163], v185\n\t"
        "s_waitcnt lgkmcnt(0)\n\t"
        "v_lshlrev_b32_sdwa v157, 5, v160 dst_sel:DWORD dst_unused:UNUSED_PAD src0_sel:DWORD src1_sel:WORD_0\n\t"
        "v_lshlrev_b32_sdwa v158, 5, v160 dst_sel:DWORD dst_unused:UNUSED_PAD src0_sel:DWORD src1_sel:WORD_1\n\t"
        "v_or_b32 v157, v184, v157\n\t"
        "v_or_b32 v158, v184, v158\n\t"
        "v_xor_b32 v159, 32, v157\n\t"
        "v_xor_b32 v152, 32, v158\n\t"
        "ds_read_b64_tr_b16 v[60:61], v157 offset:0\n\t"
        "ds_read_b64_tr_b16 v[62:63], v158 offset:0\n\t"
        "ds_read_b64_tr_b16 v[64:65], v159 offset:0\n\t"
        "ds_read_b64_tr_b16 v[66:67], v152 offset:0\n\t"
        "ds_read_b64_tr_b16 v[68:69], v157 offset:64\n\t"
        "ds_read_b64_tr_b16 v[70:71], v158 offset:64\n\t"
        "ds_read_b64_tr_b16 v[72:73], v159 offset:64\n\t"
        "ds_read_b64_tr_b16 v[74:75], v152 offset:64\n\t"
        "ds_read_b64_tr_b16 v[76:77], v157 offset:128\n\t"
        "ds_read_b64_tr_b16 v[78:79], v158 offset:128\n\t"
        "ds_read_b64_tr_b16 v[80:81], v159 offset:128\n\t"
        "ds_read_b64_tr_b16 v[82:83], v152 offset:128\n\t"
        "v_perm_b32 v164, v162, v162, v168\n\t"
        "v_perm_b32 v165, v162, v162, v169\n\t"
        "v_perm_b32 v166, v162, v162, v170\n\t"
        "v_perm_b32 v167, v162, v162, v171\n\t"
        "v_cmp_ne_u32 vcc, 0, v163\n\t"
        "s_waitcnt lgkmcnt(10)\n\t"
        "v_mfma_f32_16x16x32_bf16 v[48:51], v[60:63], v[164:167], 0\n\t"
        "s_waitcnt lgkmcnt(8)\n\t"
        "v_mfma_f32_16x16x32_bf16 v[52:55], v[64:67], v[164:167], 0\n\t"
        "s_waitcnt lgkmcnt(6)\n\t"
        "v_mfma_f32_16x16x32_bf16 v[56:59], v[68:71], v[164:167], 0\n\t"
        "s_waitcnt lgkmcnt(4)\n\t"
        "v_mfma_f32_16x16x32_bf16 v[116:119], v[72:75], v[164:167], 0\n\t"
        "s_waitcnt lgkmcnt(2)\n\t"
        "v_mfma_f32_16x16x32_bf16 v[120:123], v[76:79], v[164:167], 0\n\t"
        "s_waitcnt lgkmcnt(0)\n\t"
        "v_mfma_f32_16x16x32_bf16 v[124:127], v[80:83], v[164:167], 0\n\t"
        "s_cbranch_vccz 31f\n\t"
        "v_lshlrev_b32_sdwa v157, 5, v161 dst_sel:DWORD dst_unused:UNUSED_PAD src0_sel:DWORD src1_sel:WORD_0\n\t"
        "v_lshlrev_b32_sdwa v158, 5, v161 dst_sel:DWORD dst_unused:UNUSED_PAD src0_sel:DWORD src1_sel:WORD_1\n\t"
        "v_or_b32 v157, v184, v157\n\t"
        "v_or_b32 v158, v184, v158\n\t"
        "v_xor_b32 v159, 32, v157\n\t"
        "v_xor_b32 v152, 32, v158\n\t"
        "ds_read_b64_tr_b16 v[60:61], v157 offset:0\n\t"
        "ds_read_b64_tr_b16 v[62:63], v158 offset:0\n\t"
        "ds_read_b64_tr_b16 v[64:65], v159 offset:0\n\t"
        "ds_read_b64_tr_b16 v[66:67], v152 offset:0\n\t"
        "ds_read_b64_tr_b16 v[68:69], v157 offset:64\n\t"
        "ds_read_b64_tr_b16 v[70:71], v158 offset:64\n\t"
        "ds_read_b64_tr_b16 v[72:73], v159 offset:64\n\t"
        "ds_read_b64_tr_b16 v[74:75], v152 offset:64\n\t"
        "ds_read_b64_tr_b16 v[76:77], v157 offset:128\n\t"
        "ds_read_b64_tr_b16 v[78:79], v158 offset:128\n\t"
        "ds_read_b64_tr_b16 v[80:81], v159 offset:128\n\t"
        "ds_read_b64_tr_b16 v[82:83], v152 offset:128\n\t"
        "v_perm_b32 v164, v163, v163, v168\n\t"
        "v_perm_b32 v165, v163, v163, v169\n\t"
        "v_perm_b32 v166, v163, v163, v170\n\t"
        "v_perm_b32 v167, v163, v163, v171\n\t"
        "s_waitcnt lgkmcnt(10)\n\t"
        "s_nop 0\n\t"
        "v_mfma_f32_16x16x32_bf16 v[48:51], v[60:63], v[164:167], v[48:51]\n\t"
        "s_waitcnt lgkmcnt(8)\n\t"
        "v_mfma_f32_16x16x32_bf16 v[52:55], v[64:67], v[164:167], v[52:55]\n\t"
        "s_waitcnt lgkmcnt(6)\n\t"
        "v_mfma_f32_16x16x32_bf16 v[56:59], v[68:71], v[164:167], v[56:59]\n\t"
        "s_waitcnt lgkmcnt(4)\n\t"
        "v_mfma_f32_16x16x32_bf16 v[116:119], v[72:75], v[164:167], v[116:119]\n\t"
        "s_waitcnt lgkmcnt(2)\n\t"
        "v_mfma_f32_16x16x32_bf16 v[120:123], v[76:79], v[164:167], v[120:123]\n\t"
        "s_waitcnt lgkmcnt(0)\n\t"
        "v_mfma_f32_16x16x32_bf16 v[124:127], v[80:83], v[164:167], v[124:127]\n\t"
        "31:\n\t"
        "v_cvt_pk_bf16_f32 v48, v48, v49\n\t"
        "v_cvt_pk_bf16_f32 v49, v50, v51\n\t"
        "v_cvt_pk_bf16_f32 v52, v52, v53\n\t"
        "v_cvt_pk_bf16_f32 v53, v54, v55\n\t"
        "v_cvt_pk_bf16_f32 v56, v56, v57\n\t"
        "v_cvt_pk_bf16_f32 v57, v58, v59\n\t"
        "v_cvt_pk_bf16_f32 v116, v116, v117\n\t"
        "v_cvt_pk_bf16_f32 v117, v118, v119\n\t"
        "v_cvt_pk_bf16_f32 v120, v120, v121\n\t"
        "v_cvt_pk_bf16_f32 v121, v122, v123\n\t"
        "v_cvt_pk_bf16_f32 v124, v124, v125\n\t"
        "v_cvt_pk_bf16_f32 v125, v126, v127\n\t"
        "ds_write_b64 v186, v[48:49] offset:0\n\t"
        "ds_write_b64 v154, v[52:53] offset:0\n\t"
        "ds_write_b64 v186, v[56:57] offset:64\n\t"
        "ds_write_b64 v154, v[116:117] offset:64\n\t"
        "ds_write_b64 v186, v[120:121] offset:128\n\t"
        "ds_write_b64 v154, v[124:125] offset:128\n\t"
        "v_add_u32 v185, 256, v185\n\t"
        "v_add_u32 v186, 12288, v186\n\t"
        "v_xor_b32 v154, 32, v186\n\t"
        "s_sub_i32 s8, s8, 16\n\t"
        "s_cmp_gt_i32 s8, 0\n\t"
        "s_cbranch_scc1 30b\n\t"
        "v_mov_b32 v185, v151\n\t"
        "v_mov_b32 v186, v183\n\t"
        "v_xor_b32 v154, 32, v186\n\t"
        "s_waitcnt lgkmcnt(0)\n\t"
        "s_barrier\n\t"
        "s_branch .Lx69%=\n\t"
        ".LwX%=:\n\t"
        "s_branch .LrX%=\n\t"
        ".Lx70%=:\n\t"
        "v_add_u32 v185, 1024, v185\n\t"
        "v_add_u32 v186, 49152, v186\n\t"
        "v_xor_b32 v154, 32, v186\n\t"
        "v_bfe_u32 v64, v185, 4, 4\n\t"
        "v_add_u32 v60, 65, v64\n\t"
        "s_sub_u32 s8, s37, 12288\n\t"
        "v_mov_b32 v63, s62\n\t"
        "v_lshlrev_b32 v65, 3, v64\n\t"
        "v_mul_u32_u24 v64, 192, v64\n\t"
        "v_and_or_b32 v64, v65, 48, v64\n\t"
        "v_add_u32 v61, s8, v64\n\t"
        "v_subrev_u32 v186, 61440, v186\n\t"
        "v_xor_b32 v154, 32, v186\n\t"
        "v_min_u32 v16, s61, v60\n\t"
        "v_lshl_add_u32 v16, v16, 3, s60\n\t"
        "ds_read_b64 v[0:1], v16\n\t"
        "v_cmp_ge_u32 s[66:67], s61, v60\n\t"
        "s_mov_b32 s46, 0x07060302\n\t"
        "s_waitcnt lgkmcnt(0)\n\t"
        "v_and_b32 v16, 0xfffff800, v1\n\t"
        "ds_read_b128 v[2:5], v16 offset:1536\n\t"
        "v_lshlrev_b32 v10, 16, v0\n\t"
        "v_and_b32 v11, 0xffff0000, v0\n\t"
        "v_med3_f32 v10, v10, 0, s63\n\t"
        "v_med3_f32 v11, v11, 0, s63\n\t"
        "s_mov_b64 exec, s[66:67]\n\t"
        "ds_write_b32 v1, v61\n\t"
        "s_mov_b64 exec, -1\n\t"
        "v_perm_b32 v0, v11, v10, s46\n\t"
        "v_fract_f32 v12, v10\n\t"
        "v_fract_f32 v13, v11\n\t"
        "v_cvt_u32_f32 v14, v10\n\t"
        "v_cvt_u32_f32 v15, v11\n\t"
        "v_cmp_neq_f32 s[64:65], 0, v12\n\t"
        "v_cmp_neq_f32 s[44:45], 0, v13\n\t"
        "v_sub_f32 v16, 1.0, v12\n\t"
        "v_sub_f32 v17, 1.0, v13\n\t"
        "v_cvt_pk_bf16_f32 v17, v16, v17\n\t"
        "v_lshlrev_b32 v16, 16, v17\n\t"
        "v_and_b32 v17, 0xffff0000, v17\n\t"
        "v_mul_f32 v18, v16, v17\n\t"
        "v_mul_f32 v16, v16, v13\n\t"
        "v_mul_f32 v17, v12, v17\n\t"
        "v_mul_f32 v19, v12, v13\n\t"
        "v_cndmask_b32 v16, v17, v16, s[44:45]\n\t"
        "v_cndmask_b32 v17, 0, v17, s[44:45]\n\t"
        "v_cvt_pk_bf16_f32 v162, v18, v16\n\t"
        "v_cvt_pk_bf16_f32 v163, v17, v19\n\t"
        "s_waitcnt lgkmcnt(1)\n\t"
        "v_pk_max_u16 v16, v0, v2\n\t"
        "v_pk_min_u16 v16, v16, v3\n\t"
        "v_cmp_ne_u32 vcc, v16, v0\n\t"
        "v_sub_u32 v16, v15, v5\n\t"
        "v_mad_u32_u24 v17, v15, 6, v4\n\t"
        "v_mad_u32_u24 v17, v14, s62, v17\n\t"
        "s_and_b64 vcc, vcc, s[66:67]\n\t"
        "s_or_b64 s[56:57], s[56:57], vcc\n\t"
        "v_bfe_u32 v18, v16, 2, 1\n\t"
        "v_or_b32 v18, v18, v17\n\t"
        "v_add_u32 v16, 1, v16\n\t"
        "v_bfe_u32 v16, v16, 2, 1\n\t"
        "v_add3_u32 v16, v17, v16, 6\n\t"
        "v_cndmask_b32 v16, v18, v16, s[44:45]\n\t"
        "v_cndmask_b32 v17, 0, v63, s[64:65]\n\t"
        "v_add_u32 v19, v18, v17\n\t"
        "v_add_u32 v20, v16, v17\n\t"
        "v_cndmask_b32 v16, v19, v16, s[44:45]\n\t"
        "v_lshl_or_b32 v160, v16, 16, v18\n\t"
        "v_lshl_or_b32 v161, v20, 16, v19\n\t"
        "v_lshlrev_b32_sdwa v157, 5, v160 dst_sel:DWORD dst_unused:UNUSED_PAD src0_sel:DWORD src1_sel:WORD_0\n\t"
        "v_lshlrev_b32_sdwa v158, 5, v160 dst_sel:DWORD dst_unused:UNUSED_PAD src0_sel:DWORD src1_sel:WORD_1\n\t"
        "v_or_b32 v157, v184, v157\n\t"
        "v_or_b32 v158, v184, v158\n\t"
        "v_xor_b32 v159, 32, v157\n\t"
        "v_xor_b32 v152, 32, v158\n\t"
        "ds_read_b64_tr_b16 v[60:61], v157 offset:0\n\t"
        "ds_read_b64_tr_b16 v[62:63], v158 offset:0\n\t"
        "ds_read_b64_tr_b16 v[64:65], v159 offset:0\n\t"
        "ds_read_b64_tr_b16 v[66:67], v152 offset:0\n\t"
        "ds_read_b64_tr_b16 v[68:69], v157 offset:64\n\t"
        "ds_read_b64_tr_b16 v[70:71], v158 offset:64\n\t"
        "ds_read_b64_tr_b16 v[72:73], v159 offset:64\n\t"
        "ds_read_b64_tr_b16 v[74:75], v152 offset:64\n\t"
        "ds_read_b64_tr_b16 v[76:77], v157 offset:128\n\t"
        "ds_read_b64_tr_b16 v[78:79], v158 offset:128\n\t"
        "ds_read_b64_tr_b16 v[80:81], v159 offset:128\n\t"
        "ds_read_b64_tr_b16 v[82:83], v152 offset:128\n\t"
        "v_lshlrev_b32_sdwa v157, 5, v161 dst_sel:DWORD dst_unused:UNUSED_PAD src0_sel:DWORD src1_sel:WORD_0\n\t"
        "v_lshlrev_b32_sdwa v158, 5, v161 dst_sel:DWORD dst_unused:UNUSED_PAD src0_sel:DWORD src1_sel:WORD_1\n\t"
        "v_or_b32 v157, v184, v157\n\t"
        "v_or_b32 v158, v184, v158\n\t"
        "v_xor_b32 v159, 32, v157\n\t"
        "v_xor_b32 v152, 32, v158\n\t"
        "ds_read_b64_tr_b16 v[24:25], v157 offset:0\n\t"
        "ds_read_b64_tr_b16 v[26:27], v158 offset:0\n\t"
        "ds_read_b64_tr_b16 v[28:29], v159 offset:0\n\t"
        "ds_read_b64_tr_b16 v[30:31], v152 offset:0\n\t"
        "ds_read_b64_tr_b16 v[32:33], v157 offset:64\n\t"
        "ds_read_b64_tr_b16 v[34:35], v158 offset:64\n\t"
        "ds_read_b64_tr_b16 v[36:37], v159 offset:64\n\t"
        "ds_read_b64_tr_b16 v[38:39], v152 offset:64\n\t"
        "ds_read_b64_tr_b16 v[40:41], v157 offset:128\n\t"
        "ds_read_b64_tr_b16 v[42:43], v158 offset:128\n\t"
        "ds_read_b64_tr_b16 v[44:45], v159 offset:128\n\t"
        "ds_read_b64_tr_b16 v[46:47], v152 offset:128\n\t"
        "s_cmp_lg_u64 s[56:57], 0\n\t"
        "s_cselect_b32 s8, 1, 0\n\t"
        "v_mov_b32 v157, s8\n\t"
        "v_perm_b32 v164, v162, v162, v168\n\t"
        "v_perm_b32 v165, v162, v162, v169\n\t"
        "v_perm_b32 v166, v162, v162, v170\n\t"
        "v_perm_b32 v167, v162, v162, v171\n\t"
        "s_waitcnt lgkmcnt(12)\n\t"
        "s_nop 0\n\t"
        "v_mfma_f32_16x16x32_bf16 v[0:3], v[60:63], v[164:167], 0\n\t"
        "v_mfma_f32_16x16x32_bf16 v[4:7], v[64:67], v[164:167], 0\n\t"
        "v_mfma_f32_16x16x32_bf16 v[8:11], v[68:71], v[164:167], 0\n\t"
        "v_mfma_f32_16x16x32_bf16 v[12:15], v[72:75], v[164:167], 0\n\t"
        "v_mfma_f32_16x16x32_bf16 v[16:19], v[76:79], v[164:167], 0\n\t"
        "v_mfma_f32_16x16x32_bf16 v[20:23], v[80:83], v[164:167], 0\n\t"
        "v_perm_b32 v164, v163, v163, v168\n\t"
        "v_perm_b32 v165, v163, v163, v169\n\t"
        "v_perm_b32 v166, v163, v163, v170\n\t"
        "v_perm_b32 v167, v163, v163, v171\n\t"
        "s_waitcnt lgkmcnt(3)\n\t"
        "s_nop 0\n\t"
        "v_mfma_f32_16x16x32_bf16 v[0:3], v[24:27], v[164:167], v[0:3]\n\t"
        "v_mfma_f32_16x16x32_bf16 v[4:7], v[28:31], v[164:167], v[4:7]\n\t"
        "v_mfma_f32_16x16x32_bf16 v[8:11], v[32:35], v[164:167], v[8:11]\n\t"
        "v_mfma_f32_16x16x32_bf16 v[12:15], v[36:39], v[164:167], v[12:15]\n\t"
        "s_waitcnt lgkmcnt(0)\n\t"
        "v_mfma_f32_16x16x32_bf16 v[16:19], v[40:43], v[164:167], v[16:19]\n\t"
        "v_mfma_f32_16x16x32_bf16 v[20:23], v[44:47], v[164:167], v[20:23]\n\t"
        "ds_write_b32 v153, v157 offset:16\n\t"
        "s_nop 0\n\t"
        "v_cvt_pk_bf16_f32 v0, v0, v1\n\t"
        "v_cvt_pk_bf16_f32 v1, v2, v3\n\t"
        "v_cvt_pk_bf16_f32 v4, v4, v5\n\t"
        "v_cvt_pk_bf16_f32 v5, v6, v7\n\t"
        "v_cvt_pk_bf16_f32 v8, v8, v9\n\t"
        "v_cvt_pk_bf16_f32 v9, v10, v11\n\t"
        "v_cvt_pk_bf16_f32 v12, v12, v13\n\t"
        "v_cvt_pk_bf16_f32 v13, v14, v15\n\t"
        "v_cvt_pk_bf16_f32 v16, v16, v17\n\t"
        "v_cvt_pk_bf16_f32 v17, v18, v19\n\t"
        "v_cvt_pk_bf16_f32 v20, v20, v21\n\t"
        "v_cvt_pk_bf16_f32 v21, v22, v23\n\t"
        "ds_write_b64 v186, v[0:1] offset:0\n\t"
        "ds_write_b64 v154, v[4:5] offset:0\n\t"
        "ds_write_b64 v186, v[8:9] offset:64\n\t"
        "ds_write_b64 v154, v[12:13] offset:64\n\t"
        "ds_write_b64 v186, v[16:17] offset:128\n\t"
        "ds_write_b64 v154, v[20:21] offset:128\n\t"
        "s_branch .Lx71%=\n\t"
        ".Lx76%=:\n\t"
        "v_mov_b32 v151, v185\n\t"
        "v_mov_b32 v183, v186\n\t"
        "s_min_u32 s8, s3, 64\n\t"
        "s_waitcnt lgkmcnt(0)\n\t"
        "30:\n\t"
        "ds_read_b128 v[160:163], v185\n\t"
        "s_waitcnt lgkmcnt(0)\n\t"
        "v_lshlrev_b32_sdwa v157, 5, v160 dst_sel:DWORD dst_unused:UNUSED_PAD src0_sel:DWORD src1_sel:WORD_0\n\t"
        "v_lshlrev_b32_sdwa v158, 5, v160 dst_sel:DWORD dst_unused:UNUSED_PAD src0_sel:DWORD src1_sel:WORD_1\n\t"
        "v_or_b32 v157, v184, v157\n\t"
        "v_or_b32 v158, v184, v158\n\t"
        "v_xor_b32 v159, 32, v157\n\t"
        "v_xor_b32 v152, 32, v158\n\t"
        "ds_read_b64_tr_b16 v[60:61], v157 offset:0\n\t"
        "ds_read_b64_tr_b16 v[62:63], v158 offset:0\n\t"
        "ds_read_b64_tr_b16 v[64:65], v159 offset:0\n\t"
        "ds_read_b64_tr_b16 v[66:67], v152 offset:0\n\t"
        "ds_read_b64_tr_b16 v[68:69], v157 offset:64\n\t"
        "ds_read_b64_tr_b16 v[70:71], v158 offset:64\n\t"
        "ds_read_b64_tr_b16 v[72:73], v159 offset:64\n\t"
        "ds_read_b64_tr_b16 v[74:75], v152 offset:64\n\t"
        "ds_read_b64_tr_b16 v[76:77], v157 offset:128\n\t"
        "ds_read_b64_tr_b16 v[78:79], v158 offset:128\n\t"
        "ds_read_b64_tr_b16 v[80:81], v159 offset:128\n\t"
        "ds_read_b64_tr_b16 v[82:83], v152 offset:128\n\t"
        "v_perm_b32 v164, v162, v162, v168\n\t"
        "v_perm_b32 v165, v162, v162, v169\n\t"
        "v_perm_b32 v166, v162, v162, v170\n\t"
        "v_perm_b32 v167, v162, v162, v171\n\t"
        "v_cmp_ne_u32 vcc, 0, v163\n\t"
        "s_waitcnt lgkmcnt(10)\n\t"
        "v_mfma_f32_16x16x32_bf16 v[48:51], v[60:63], v[164:167], 0\n\t"
        "s_waitcnt lgkmcnt(8)\n\t"
        "v_mfma_f32_16x16x32_bf16 v[52:55], v[64:67], v[164:167], 0\n\t"
        "s_waitcnt lgkmcnt(6)\n\t"
        "v_mfma_f32_16x16x32_bf16 v[56:59], v[68:71], v[164:167], 0\n\t"
        "s_waitcnt lgkmcnt(4)\n\t"
        "v_mfma_f32_16x16x32_bf16 v[116:119], v[72:75], v[164:167], 0\n\t"
        "s_waitcnt lgkmcnt(2)\n\t"
        "v_mfma_f32_16x16x32_bf16 v[120:123], v[76:79], v[164:167], 0\n\t"
        "s_waitcnt lgkmcnt(0)\n\t"
        "v_mfma_f32_16x16x32_bf16 v[124:127], v[80:83], v[164:167], 0\n\t"
        "s_cbranch_vccz 31f\n\t"
        "v_lshlrev_b32_sdwa v157, 5, v161 dst_sel:DWORD dst_unused:UNUSED_PAD src0_sel:DWORD src1_sel:WORD_0\n\t"
        "v_lshlrev_b32_sdwa v158, 5, v161 dst_sel:DWORD dst_unused:UNUSED_PAD src0_sel:DWORD src1_sel:WORD_1\n\t"
        "v_or_b32 v157, v184, v157\n\t"
        "v_or_b32 v158, v184, v158\n\t"
        "v_xor_b32 v159, 32, v157\n\t"
        "v_xor_b32 v152, 32, v158\n\t"
        "ds_read_b64_tr_b16 v[60:61], v157 offset:0\n\t"
        "ds_read_b64_tr_b16 v[62:63], v158 offset:0\n\t"
        "ds_read_b64_tr_b16 v[64:65], v159 offset:0\n\t"
        "ds_read_b64_tr_b16 v[66:67], v152 offset:0\n\t"
        "ds_read_b64_tr_b16 v[68:69], v157 offset:64\n\t"
        "ds_read_b64_tr_b16 v[70:71], v158 offset:64\n\t"
        "ds_read_b64_tr_b16 v[72:73], v159 offset:64\n\t"
        "ds_read_b64_tr_b16 v[74:75], v152 offset:64\n\t"
        "ds_read_b64_tr_b16 v[76:77], v157 offset:128\n\t"
        "ds_read_b64_tr_b16 v[78:79], v158 offset:128\n\t"
        "ds_read_b64_tr_b16 v[80:81], v159 offset:128\n\t"
        "ds_read_b64_tr_b16 v[82:83], v152 offset:128\n\t"
        "v_perm_b32 v164, v163, v163, v168\n\t"
        "v_perm_b32 v165, v163, v163, v169\n\t"
        "v_perm_b32 v166, v163, v163, v170\n\t"
        "v_perm_b32 v167, v163, v163, v171\n\t"
        "s_waitcnt lgkmcnt(10)\n\t"
        "s_nop 0\n\t"
        "v_mfma_f32_16x16x32_bf16 v[48:51], v[60:63], v[164:167], v[48:51]\n\t"
        "s_waitcnt lgkmcnt(8)\n\t"
        "v_mfma_f32_16x16x32_bf16 v[52:55], v[64:67], v[164:167], v[52:55]\n\t"
        "s_waitcnt lgkmcnt(6)\n\t"
        "v_mfma_f32_16x16x32_bf16 v[56:59], v[68:71], v[164:167], v[56:59]\n\t"
        "s_waitcnt lgkmcnt(4)\n\t"
        "v_mfma_f32_16x16x32_bf16 v[116:119], v[72:75], v[164:167], v[116:119]\n\t"
        "s_waitcnt lgkmcnt(2)\n\t"
        "v_mfma_f32_16x16x32_bf16 v[120:123], v[76:79], v[164:167], v[120:123]\n\t"
        "s_waitcnt lgkmcnt(0)\n\t"
        "v_mfma_f32_16x16x32_bf16 v[124:127], v[80:83], v[164:167], v[124:127]\n\t"
        "31:\n\t"
        "v_cvt_pk_bf16_f32 v48, v48, v49\n\t"
        "v_cvt_pk_bf16_f32 v49, v50, v51\n\t"
        "v_cvt_pk_bf16_f32 v52, v52, v53\n\t"
        "v_cvt_pk_bf16_f32 v53, v54, v55\n\t"
        "v_cvt_pk_bf16_f32 v56, v56, v57\n\t"
        "v_cvt_pk_bf16_f32 v57, v58, v59\n\t"
        "v_cvt_pk_bf16_f32 v116, v116, v117\n\t"
        "v_cvt_pk_bf16_f32 v117, v118, v119\n\t"
        "v_cvt_pk_bf16_f32 v120, v120, v121\n\t"
        "v_cvt_pk_bf16_f32 v121, v122, v123\n\t"
        "v_cvt_pk_bf16_f32 v124, v124, v125\n\t"
        "v_cvt_pk_bf16_f32 v125, v126, v127\n\t"
        "ds_write_b64 v186, v[48:49] offset:0\n\t"
        "ds_write_b64 v154, v[52:53] offset:0\n\t"
        "ds_write_b64 v186, v[56:57] offset:64\n\t"
        "ds_write_b64 v154, v[116:117] offset:64\n\t"
        "ds_write_b64 v186, v[120:121] offset:128\n\t"
        "ds_write_b64 v154, v[124:125] offset:128\n\t"
        "v_add_u32 v185, 256, v185\n\t"
        "v_add_u32 v186, 12288, v186\n\t"
        "v_xor_b32 v154, 32, v186\n\t"
        "s_sub_i32 s8, s8, 16\n\t"
        "s_cmp_gt_i32 s8, 0\n\t"
        "s_cbranch_scc1 30b\n\t"
        "v_mov_b32 v185, v151\n\t"
        "v_mov_b32 v186, v183\n\t"
        "v_xor_b32 v154, 32, v186\n\t"
        "s_waitcnt lgkmcnt(0)\n\t"
        "s_barrier\n\t"
        "s_branch .Lx77%=\n\t"
        "99:\n\t"
        "s_nop 7\n\t"
        "s_waitcnt vmcnt(0) lgkmcnt(0)\n\t"
        "s_barrier\n\t"
        "s_lshl_b32 s6, s6, 16\n\t"
        "v_or_b32 %0, s6, v156\n\t"
        : "+v"(tid) : "s"(ka), "s"(bid), "s"(recs)
        : "memory", "scc", "vcc", "s3", "s4", "s5", "s6", "s7", "s8", "s9", "s10", "s11", "s12", "s13", "s16", "s17", "s18", "s19", "s20", "s21", "s22", "s23", "s24", "s25", "s26", "s27", "s28", "s29", "s30", "s31", "s34", "s35", "s36", "s37", "s38", "s39", "s40", "s41", "s42", "s43", "s44", "s45", "s46", "s47", "s48", "s49", "s50", "s51", "s52", "s53", "s54", "s55", "s56", "s57", "s60", "s61", "s62", "s63", "s64", "s65", "s66", "s67", "s68", "s69", "s70", "s71", "s72", "s73", "s74", "s75", "s76", "s77", "s78", "s79", "s80", "s81", "s82", "s83", "s84", "s85", "s86", "s87", "s88", "s89", "s90", "s91", "s92", "s93", "s94", "s95", "s96", "s97", "s98", "s99", "v1", "v2", "v3", "v4", "v5", "v6", "v7", "v8", "v9", "v10", "v11", "v12", "v13", "v14", "v15", "v16", "v17", "v18", "v19", "v20", "v21", "v22", "v23", "v24", "v25", "v26", "v27", "v28", "v29", "v30", "v31", "v32", "v33", "v34", "v35", "v36", "v37", "v38", "v39", "v40", "v41", "v42", "v43", "v44", "v45", "v46", "v47", "v48", "v49", "v50", "v51", "v52", "v53", "v54", "v55", "v56", "v57", "v58", "v59", "v60", "v61", "v62", "v63", "v64", "v65", "v66", "v67", "v68", "v69", "v70", "v71", "v72", "v73", "v74", "v75", "v76", "v77", "v78", "v79", "v80", "v81", "v82", "v83", "v84", "v85", "v86", "v87", "v88", "v89", "v90", "v91", "v92", "v93", "v94", "v95", "v96", "v97", "v98", "v99", "v100", "v101", "v102", "v103", "v104", "v105", "v106", "v107", "v108", "v109", "v110", "v111", "v112", "v113", "v114", "v115", "v116", "v117", "v118", "v119", "v120", "v121", "v122", "v123", "v124", "v125", "v126", "v127", "v128", "v129", "v130", "v131", "v132", "v133", "v134", "v135", "v136", "v137", "v138", "v139", "v140", "v141", "v142", "v143", "v144", "v145", "v146", "v147", "v148", "v149", "v150", "v151", "v152", "v153", "v154", "v155", "v156", "v157", "v158", "v159", "v160", "v161", "v162", "v163", "v164", "v165", "v166", "v167", "v168", "v169", "v170", "v171", "v172", "v173", "v174", "v175", "v176", "v177", "v178", "v179", "v180", "v181", "v182", "v183", "v184", "v185", "v186", "v187", "v188", "v189", "v190", "v191", "v192", "v193", "v194", "v195", "v196", "v197", "v198", "v199", "v200", "v201", "v202", "v203", "v204", "v205", "v206", "v207", "v208", "v209", "v210", "v211", "v212", "v213", "v214", "v215", "v216", "v217", "v218", "v219", "v220", "v221", "v222", "v223", "v224", "v225", "v226", "v227", "v228", "v229", "v230", "v231", "v232", "v233", "v234", "v235", "v236", "v237", "v238", "v239", "v240", "v241", "v242", "v243", "v244", "v245", "v246", "v247", "v248", "v249", "v250", "v251", "v252", "v253", "v254", "v255", "a0", "a1", "a2", "a3", "a4", "a5", "a6", "a7", "a8", "a9", "a10", "a11", "a12", "a13", "a14", "a15", "a16", "a17", "a18", "a19", "a20", "a21", "a22", "a23", "a24", "a25", "a26", "a27", "a28", "a29", "a30", "a31", "a32", "a33", "a34", "a35", "a36", "a37", "a38", "a39", "a40", "a41", "a42", "a43", "a44", "a45", "a46", "a47", "a48", "a49", "a50", "a51", "a52", "a53", "a54", "a55", "a56", "a57", "a58", "a59", "a60", "a61", "a62", "a63", "a64", "a65", "a66", "a67", "a68", "a69", "a70", "a71", "a72", "a73", "a74", "a75", "a76", "a77", "a78", "a79", "a80", "a81", "a82", "a83", "a84", "a85", "a86", "a87", "a88", "a89", "a90", "a91", "a92", "a93", "a94", "a95", "a96", "a97", "a98", "a99", "a100", "a101", "a102", "a103", "a104", "a105", "a106", "a107", "a108", "a109", "a110", "a111", "a112", "a113", "a114", "a115", "a116", "a117", "a118", "a119", "a120", "a121", "a122", "a123", "a124", "a125", "a126", "a127", "a128", "a129", "a130", "a131", "a132", "a133", "a134", "a135", "a136", "a137", "a138", "a139", "a140", "a141", "a142", "a143", "a144", "a145", "a146", "a147", "a148", "a149", "a150", "a151", "a152", "a153", "a154", "a155", "a156", "a157", "a158", "a159", "a160", "a161", "a162", "a163", "a164", "a165", "a166", "a167", "a168", "a169", "a170", "a171", "a172", "a173", "a174", "a175", "a176", "a177", "a178", "a179", "a180", "a181", "a182", "a183", "a184", "a185", "a186", "a187", "a188", "a189", "a190", "a191", "a192", "a193", "a194", "a195", "a196", "a197", "a198", "a199", "a200", "a201", "a202", "a203", "a204", "a205", "a206", "a207", "a208", "a209", "a210", "a211", "a212", "a213", "a214", "a215", "a216", "a217", "a218", "a219", "a220", "a221", "a222", "a223", "a224", "a225", "a226", "a227", "a228", "a229", "a230", "a231", "a232", "a233", "a234", "a235", "a236", "a237", "a238", "a239", "a240", "a241", "a242", "a243", "a244", "a245", "a246", "a247", "a248", "a249", "a250", "a251", "a252", "a253", "a254", "a255");
    asm volatile("" : "+s"(ka), "+s"(bid) :: "memory");
    {
        typedef short v4s __attribute__((ext_vector_type(4)));
        const unsigned short *x = ka->x, *w = ka->w;
        const unsigned* off = (const unsigned*)ka->off;
        unsigned short* out = ka->out;
        const unsigned tw = recs[bid * 64 + 27] >> 16, redo = __builtin_amdgcn_readfirstlane(tid >> 16);
        const unsigned* rc = recs + bid * 64;
        tid &= 255;
        const int lane = tid & 63, li = lane & 15, q = lane >> 4, wv = __builtin_amdgcn_readfirstlane(tid >> 6);
        int pix[4] = {-1, -1, -1, -1}, n = 0;
#pragma unroll 1
        for (int s = 0; s < 14; ++s) {
            const unsigned o = s < 8 ? (((tw & 255) + s) * 224 + ((tw >> 8) & 255)) * 192 : rc[49 + ((s - 8) >> 1)] + (s & 1) * (43200 - rc[30]);
            if (!((redo >> (s < 8 ? 8 + (s >> 1) : 0)) & 1) || o >= 224 * 224 * 192) continue;
            const int p = (o + li * (s < 8 ? 192 : rc[30])) / 192;
#pragma unroll
            for (int i = 0; i < 4; ++i) pix[i] = n == wv + 4 * i ? p : pix[i];
            ++n;
        }
        f32x4 acc[4][6];
        unsigned od[4];
        u32x4 wn[5];
#pragma unroll
        for (int i = 0; i < 4; ++i) {
            od[i] = off[max(pix[i], 0) * 9];
#pragma unroll
            for (int k = 0; k < 6; ++k) acc[i][k] = (f32x4){0.f, 0.f, 0.f, 0.f};
        }
#pragma unroll
        for (int k = 0; k < 5; ++k) wn[k] = *(const u32x4*)(w + min(tid + 256 * k, 1151u) * 8);
        const __amdgpu_buffer_rsrc_t xr = __builtin_amdgcn_make_buffer_rsrc((void*)x, 0, 224 * 224 * 192, 0x00020000);
        const unsigned wb = (8 * q + (li >> 2)) * 192 + (li & 3) * 8;
#pragma unroll 1
        for (int t = 0; t < 9; ++t) {
            unsigned char* slab = lds + (t & 1) * 18432;
#pragma unroll
            for (int k = 0; k < 5; ++k) {
                const unsigned c = min(tid + 256 * k, 1151u);
                *(u32x4*)(slab + ((c * 16) ^ (((c / 96) & 1) << 5))) = wn[k];
            }
            __syncthreads();
            u32x4 wf[3][6];
#pragma unroll
            for (int k = 0; k < 18; ++k) {
                __attribute__((address_space(3))) unsigned char* a = (__attribute__((address_space(3))) unsigned char*)slab + (k / 6) * 6144 + wb + (((k % 6) * 32) ^ ((q & 1) << 5));
                const u32x2 lo = __builtin_bit_cast(u32x2, __builtin_amdgcn_ds_read_tr16_b64_v4i16((__attribute__((address_space(3))) v4s*)a));
                const u32x2 hi = __builtin_bit_cast(u32x2, __builtin_amdgcn_ds_read_tr16_b64_v4i16((__attribute__((address_space(3))) v4s*)(a + 768)));
                wf[k / 6][k % 6] = (u32x4){lo[0], lo[1], hi[0], hi[1]};
            }
            const int tn = min(t + 1, 8);
#pragma unroll
            for (int k = 0; k < 5; ++k) wn[k] = *(const u32x4*)(w + tn * 9216 + min(tid + 256 * k, 1151u) * 8);
#pragma unroll
            for (int i0 = 0; i0 < 4; i0 += 2) {
                if (wv + 4 * i0 >= n) continue;
                u32x4 g[2][3][4];
                float wt[2][4];
#pragma unroll
                for (int j = 0; j < 2; ++j) {
                    const int i = i0 + j, p = max(pix[i], 0), h = p / 224, c = p - h * 224;
                    float py = rb((float)(h + t / 3 - 1) + bf_lo(od[i])), px = rb((float)(c + t % 3 - 1) + bf_hi(od[i]));
                    od[i] = off[p * 9 + tn];
                    py = fminf(fmaxf(py, 0.f), 223.f); px = fminf(fmaxf(px, 0.f), 223.f);
                    const float y0 = floorf(py), x0 = floorf(px), y1 = fminf(y0 + 1.f, 223.f), x1 = fminf(x0 + 1.f, 223.f);
                    const float wy = py - y0, wx = px - x0, a = rb(1.f - wy), b = rb(1.f - wx);
                    wt[j][0] = rb(a * b); wt[j][1] = rb(a * wx); wt[j][2] = rb(wy * b); wt[j][3] = rb(wy * wx);
                    int ad[4] = {(int)y0 * 224 + (int)x0, (int)y0 * 224 + (int)x1, (int)y1 * 224 + (int)x0, (int)y1 * 224 + (int)x1};
#pragma unroll
                    for (int k = 0; k < 4; ++k) ad[k] = wt[j][k] != 0.f && pix[i] >= 0 ? ad[k] * 192 + q * 16 : 0x40000000;
#pragma unroll
                    for (int k = 0; k < 4; ++k) {
#pragma unroll
                        for (int ks = 0; ks < 3; ++ks) g[j][ks][k] = (u32x4){0u, 0u, 0u, 0u};
                        if (__builtin_amdgcn_ballot_w64(ad[k] != 0x40000000) == 0) continue;
#pragma unroll
                        for (int ks = 0; ks < 3; ++ks) g[j][ks][k] = __builtin_amdgcn_raw_buffer_load_b128(xr, ad[k] + ks * 64, 0, 0);
                    }
                }
                asm volatile("" ::: "memory");
#pragma unroll
                for (int j = 0; j < 2; ++j) {
                    if (wv + 4 * (i0 + j) >= n) continue;
#pragma unroll
                    for (int ks = 0; ks < 3; ++ks) {
                        u32x4 bfr;
#pragma unroll
                        for (int d = 0; d < 4; ++d) {
                            float lo = 0.f, hi = 0.f;
#pragma unroll
                            for (int cn = 0; cn < 4; ++cn) {
                                const unsigned u = g[j][ks][cn][d];
                                const bf16x2 v = __builtin_bit_cast(bf16x2, u);
                                lo = __builtin_amdgcn_fdot2_f32_bf16(v, __builtin_bit_cast(bf16x2, __float_as_uint(wt[j][cn]) >> 16), lo, false);
                                hi = __builtin_amdgcn_fdot2_f32_bf16(v, __builtin_bit_cast(bf16x2, __float_as_uint(wt[j][cn])), hi, false);
                            }
                            bfr[d] = cvt_pk(lo, hi);
                        }
#pragma unroll
                        for (int k = 0; k < 6; ++k)
                            acc[i0 + j][k] = __builtin_amdgcn_mfma_f32_16x16x32_bf16(__builtin_bit_cast(bf16x8, wf[ks][k]), __builtin_bit_cast(bf16x8, bfr), acc[i0 + j][k], 0, 0, 0);
                    }
                }
            }
        }
#pragma unroll
        for (int i = 0; i < 4; ++i) {
            if (wv + 4 * i >= n) continue;
#pragma unroll
            for (int k = 0; k < 6; ++k) {
                u32x2 o; o[0] = cvt_pk(acc[i][k][0], acc[i][k][1]); o[1] = cvt_pk(acc[i][k][2], acc[i][k][3]);
                *(u32x2*)(out + pix[i] * 96 + k * 16 + q * 4) = o;
            }
        }
    }
}
extern "C" void kernel_launch(void* const* d_in, const int* in_sizes, int n_in, void* d_out, int out_size, void* d_ws, size_t ws_size, hipStream_t stream) {
    dcn_kernel<<<256, 256, 0, stream>>>((const unsigned short*)d_in[0], (const unsigned short*)d_in[1], (const unsigned short*)d_in[2], (unsigned short*)d_out, d_ws);
}
```
